```python
import torch

def _softmax(x, axis=-1):
    return torch.softmax(x, dim=axis)

def _gelu(x):
    return torch.nn.functional.gelu(x, approximate='tanh')

def _silu(x):
    return torch.nn.functional.silu(x)

def _sigmoid(x):
    return torch.sigmoid(x)

def _layernorm(x, axis=-1, eps=1e-05):
    m = x.mean(dim=axis, keepdim=True)
    v = x.var(dim=axis, keepdim=True, unbiased=False)
    return (x - m) / torch.sqrt(v + eps)

def _scatter_add(tgt, idx, src, axis=0):
    return tgt.clone().index_add_(axis, idx, src)

def _segment_sum(src, seg, n):
    out = torch.zeros((n,) + src.shape[1:], dtype=src.dtype)
    return out.index_add_(0, seg, src)

def _pad(x, pw):
    flat = tuple((v for pair in reversed(pw) for v in pair))
    return torch.nn.functional.pad(x, flat)
kernel_name = 'interleave_layers__sliding_local_dilated__memory_aug_recurrent_mem_tr__prompt'

def setup_inputs():

    _w = lambda a: (lambda t: t.to(torch.bfloat16) if t.is_floating_point() else t)(torch.as_tensor(a))

    def _A_setup():
        B, S, D = (1, 2048, 576)
        return {'x': _w((lambda *s: torch.randn(*s, dtype=torch.bfloat16) * 0.05)(B, S, D)), 'wqkv': _w((lambda *s: torch.randn(*s, dtype=torch.bfloat16) * 0.05)(D, 3 * D)), 'wo': _w((lambda *s: torch.randn(*s, dtype=torch.bfloat16) * 0.05)(D, D))}
    _A_in = _A_setup()

    def _B_setup():
        B, S, Dm = (1, 2048, 576)
        H, Dh = (32, 128)
        M, seg = (8, 32)
        seg = min(seg, S)
        S = S // seg * seg
        return {'x': _w((lambda *s: torch.randn(*s, dtype=torch.bfloat16) * 0.05)(B, S, Dm)), 'mem0': _w((lambda *s: torch.randn(*s, dtype=torch.bfloat16) * 0.8)(B, M, Dm)), 'Wq': _w((lambda *s: torch.randn(*s, dtype=torch.bfloat16) * 1.6)(Dm, H, Dh) * Dm ** (-0.5)), 'Wk': _w((lambda *s: torch.randn(*s, dtype=torch.bfloat16) * 1.6)(Dm, H, Dh) * Dm ** (-0.5)), 'Wv': _w((lambda *s: torch.randn(*s, dtype=torch.bfloat16) * 0.8)(Dm, H, Dh) * Dm ** (-0.5)), 'Wo': _w((lambda *s: torch.randn(*s, dtype=torch.bfloat16) * 0.8)(H, Dh, Dm) * (H * Dh) ** (-0.5)), 'seg_len': seg}
    _B_in = _B_setup()
    return {'A_x': _A_in['x'], 'A_wqkv': _A_in['wqkv'], 'A_wo': _A_in['wo'], 'B_x': _B_in['x'], 'B_mem0': _B_in['mem0'], 'B_Wq': _B_in['Wq'], 'B_Wk': _B_in['Wk'], 'B_Wv': _B_in['Wv'], 'B_Wo': _B_in['Wo'], 'B_seg_len': _B_in['seg_len']}

def reference(A_x, A_wqkv, A_wo, B_x, B_mem0, B_Wq, B_Wk, B_Wv, B_Wo, B_seg_len):

    def _A_ref(x, wqkv, wo):
        B, S, D = x.shape
        H = 32
        Dh = D // H
        W, dil = (8, 2)
        qkv = (lambda x, *d: x.permute(*d))((x @ wqkv).reshape(B, S, 3, H, Dh), 2, 0, 3, 1, 4)
        q, k, v = (qkv[0], qkv[1], qkv[2])
        scale = Dh ** (-0.5)
        pos = torch.arange(S)
        off = torch.arange(W) * dil
        idx = pos[:, None] - off[None, :]
        mask = idx >= 0
        idx_c = torch.where(mask, idx, 0).reshape(-1)
        kg = k[:, :, idx_c, :].reshape(B, H, S, W, Dh)
        vg = v[:, :, idx_c, :].reshape(B, H, S, W, Dh)
        logits = torch.einsum('bhsd,bhswd->bhsw', q, kg) * scale
        logits = torch.where(mask[None, None, :, :], logits, -1000000000.0)
        attn = _softmax(logits, axis=-1)
        o = torch.einsum('bhsw,bhswd->bhsd', attn, vg)
        return (lambda x, *d: x.permute(*d))(o, 0, 2, 1, 3).reshape(B, S, D) @ wo

    def _B_ref(x, mem0, Wq, Wk, Wv, Wo, seg_len):
        B, S, Dm = x.shape
        H, Dh = (Wq.shape[1], Wq.shape[2])
        M = mem0.shape[1]
        scale = Dh ** (-0.5)
        n_seg = S // seg_len
        L = M + seg_len
        idx = torch.arange(L)
        mask = (idx[:, None] < M) | (idx[:, None] >= idx[None, :])
        mem = mem0
        outs = []
        for s in range(n_seg):
            xs = x[:, s * seg_len:(s + 1) * seg_len, :].clone()
            h = torch.concatenate([mem, xs], axis=1)
            q = torch.einsum('btd,dhe->bhte', h, Wq)
            k = torch.einsum('btd,dhe->bhte', h, Wk)
            v = torch.einsum('btd,dhe->bhte', h, Wv)
            scores = torch.einsum('bhqe,bhke->bhqk', q, k) * scale
            scores = torch.where(mask[None, None], scores, -1000000000.0)
            p = _softmax(scores, axis=-1)
            o = torch.einsum('bhqk,bhke->bhqe', p, v)
            o = torch.einsum('bhte,hed->btd', o, Wo)
            mem = o[:, :M, :].clone()
            outs.append(o[:, M:, :])
        return torch.concatenate(outs, axis=1)
    x = A_x
    x = x + _A_ref(_layernorm(x, axis=-1), A_wqkv, A_wo)
    x = x + _B_ref(_layernorm(x, axis=-1), B_mem0, B_Wq, B_Wk, B_Wv, B_Wo, B_seg_len)
    x = x + _A_ref(_layernorm(x, axis=-1), A_wqkv, A_wo)
    x = x + _B_ref(_layernorm(x, axis=-1), B_mem0, B_Wq, B_Wk, B_Wv, B_Wo, B_seg_len)
    return x
```

```cpp
#include <hip/hip_runtime.h>
#include <hip/hip_bf16.h>
#include <stdint.h>

typedef unsigned short u16;
typedef unsigned u32;
typedef unsigned long long u64;
using bf16x8 = __attribute__((ext_vector_type(8))) short;
using bf16x4 = __attribute__((ext_vector_type(4))) short;
using f32x4 = __attribute__((ext_vector_type(4))) float;
using v4u = __attribute__((ext_vector_type(4))) unsigned;
using v2u = __attribute__((ext_vector_type(2))) unsigned;
typedef __bf16 bfv2 __attribute__((ext_vector_type(2)));
#define GAS __attribute__((address_space(1)))
typedef GAS unsigned gu32;
#define RLX_AGENT __ATOMIC_RELAXED, __HIP_MEMORY_SCOPE_AGENT
typedef __amdgpu_buffer_rsrc_t rsrc_t;

constexpr int S_ = 2048, D_ = 576, NSEG = 64, SEG = 32, NB = 4096, NQA = 1728;

constexpr u32 OFF_ABORT = 0;
constexpr u32 OFF_POL = 64;
constexpr u32 OFF_RDY = 128;
constexpr u32 OFF_ROLE = 256;
constexpr u32 OFF_CXNA = 1024;
constexpr u32 OFF_CPA = OFF_CXNA + 8192;
constexpr u32 OFF_CXNB = OFF_CPA + 8192;
constexpr u32 OFF_CPB = OFF_CXNB + 8192;
constexpr u32 OFF_COA = OFF_CPB + 8192;
constexpr u32 OFF_COB = OFF_COA + 8192;
constexpr u32 OFF_FKV = OFF_COB + 32768;
constexpr u32 SYNC_BYTES = OFF_FKV + 16384;
constexpr u32 MB2 = 2u << 20;
constexpr u32 OFF_XA = 0;
constexpr u32 OFF_XB = MB2;
constexpr u32 XB = S_ * D_ * 2;
constexpr u32 OFF_XNA = 2 * MB2;
constexpr u32 OFF_XNB = OFF_XNA + 2 * XB;
constexpr u32 OFF_X = OFF_XNB + 2 * XB;
constexpr u32 OFF_OA = OFF_X + 3 * XB;
constexpr u32 OFF_AO = OFF_OA + 2 * XB;
constexpr u32 OFF_PB = OFF_AO + 2 * XB;
constexpr u32 OFF_OB = OFF_PB + 64 * 4 * 32 * 576 * 4;
constexpr u32 HB_ = 2 * 64 * 32 * 8192;
constexpr u32 OFF_QX = (OFF_OB + S_ * NB * 2 + 2 * MB2 - 1) & ~(2 * MB2 - 1);
constexpr u32 OFF_KX = OFF_QX + HB_;
constexpr u32 OFF_VX = OFF_KX + HB_;
constexpr u32 WS_BYTES = OFF_VX + HB_;
__device__ __forceinline__ u32 hslot(int lp, int s, int h) { const u32 i = s * 32 + h; return (i * 2 + ((lp ^ (i >> 7)) & 1)) * 8192; }

#define STAMP(i, k) do {} while (0)
#define PH(k) do {} while (0)
__device__ __forceinline__ float bf2f(u16 v) { return __uint_as_float(((u32)v) << 16); }
__device__ __forceinline__ float bflo(u32 v) { return __uint_as_float(v << 16); }
__device__ __forceinline__ float bfhi(u32 v) { return __uint_as_float(v & 0xffff0000u); }
__device__ __forceinline__ u32 pack2(float a, float b) { bfv2 v = {(__bf16)a, (__bf16)b}; return __builtin_bit_cast(u32, v); }
__device__ __forceinline__ float rbf(float f) { return (float)(__bf16)f; }
__device__ __forceinline__ v4u ld16(rsrc_t r, u32 off) { return __builtin_amdgcn_raw_buffer_load_b128(r, off, 0, 16); }
__device__ __forceinline__ v2u ld8(rsrc_t r, u32 off) { return __builtin_amdgcn_raw_buffer_load_b64(r, off, 0, 16); }
__device__ __forceinline__ void st16(rsrc_t r, u32 off, v4u v) { __builtin_amdgcn_raw_buffer_store_b128(v, r, off, 0, 16); }
__device__ __forceinline__ void st8(rsrc_t r, u32 off, v2u v) { __builtin_amdgcn_raw_buffer_store_b64(v, r, off, 0, 16); }
__device__ __forceinline__ void st4(rsrc_t r, u32 off, u32 v) { __builtin_amdgcn_raw_buffer_store_b32(v, r, off, 0, 16); }
#define DRAIN() asm volatile("s_waitcnt vmcnt(0)" ::: "memory")
#define CBAR() asm volatile("" ::: "memory")
__device__ __forceinline__ u32 ldw(char* ws, u32 off) { return __hip_atomic_load((gu32*)(ws + off), RLX_AGENT); }
__device__ __forceinline__ void addw(char* ws, u32 off) { __hip_atomic_fetch_add((gu32*)(ws + off), 1u, RLX_AGENT); }
constexpr u32 SPIN_CAP = 1u << 20;
#define SLP_W 8
#define SLP_C 8
__device__ __forceinline__ bool spin_fail(char* ws, u32& spins) {
    ++spins;
    if ((spins & 63u) == 0u) {
        if (ldw(ws, OFF_ABORT)) return true;
        if (spins > SPIN_CAP) { __hip_atomic_store((gu32*)(ws + OFF_ABORT), 1u, RLX_AGENT); return true; }
    }
    return false;
}
__device__ __forceinline__ void wait_ge(char* ws, u32 off, u32 target) {
    u32 spins = 0;
    while (ldw(ws, off) < target) {
        if (spin_fail(ws, spins)) break;
        __builtin_amdgcn_s_sleep(SLP_W);
    }
    CBAR();
}
__device__ __forceinline__ f32x4 mfma32(bf16x8 a, bf16x8 b, f32x4 c) { return __builtin_amdgcn_mfma_f32_16x16x32_bf16(a, b, c, 0, 0, 0); }
__device__ __forceinline__ f32x4 mfma16(bf16x4 a, bf16x4 b, f32x4 c) { return __builtin_amdgcn_mfma_f32_16x16x16bf16_1k(a, b, c, 0, 0, 0); }
template <bool AG, bool SW, bool FIRST>
__device__ __forceinline__ void mmaw(f32x4& acc, const bf16x8& w, const bf16x8& x) {
    if constexpr (FIRST) {
        if constexpr (AG && !SW) asm volatile("v_mfma_f32_16x16x32_bf16 %0, %1, %2, 0" : "=&v"(acc) : "a"(w), "v"(x));
        else if constexpr (AG && SW) asm volatile("v_mfma_f32_16x16x32_bf16 %0, %2, %1, 0" : "=&v"(acc) : "a"(w), "v"(x));
        else if constexpr (!SW) asm volatile("v_mfma_f32_16x16x32_bf16 %0, %1, %2, 0" : "=&v"(acc) : "v"(w), "v"(x));
        else asm volatile("v_mfma_f32_16x16x32_bf16 %0, %2, %1, 0" : "=&v"(acc) : "v"(w), "v"(x));
    } else {
        if constexpr (AG && !SW) asm volatile("v_mfma_f32_16x16x32_bf16 %0, %1, %2, %0" : "+v"(acc) : "a"(w), "v"(x));
        else if constexpr (AG && SW) asm volatile("v_mfma_f32_16x16x32_bf16 %0, %2, %1, %0" : "+v"(acc) : "a"(w), "v"(x));
        else if constexpr (!SW) asm volatile("v_mfma_f32_16x16x32_bf16 %0, %1, %2, %0" : "+v"(acc) : "v"(w), "v"(x));
        else asm volatile("v_mfma_f32_16x16x32_bf16 %0, %2, %1, %0" : "+v"(acc) : "v"(w), "v"(x));
    }
}
#define MFMA_PAD() asm volatile("s_nop 7\n\ts_nop 7")
#define PIN(x) asm volatile("" : "+v"(x))
#define PINA(x) asm volatile("" : "+a"(x))
template <bool PERM = false>
__device__ __forceinline__ void load_pair(const u16* W, int ld, int krow, int ncol, bf16x8& f0, bf16x8& f1) {
    u32 d[8];
#pragma unroll
    for (int j = 0; j < 8; ++j) d[j] = *(const u32*)(W + (size_t)(PERM ? krow + 8 * (j >> 2) + 2 * (j & 3) : krow + j) * ld + ncol);
    v4u a, b;
#pragma unroll
    for (int q = 0; q < 4; ++q) {
        a[q] = __builtin_amdgcn_perm(d[2 * q + 1], d[2 * q], 0x05040100u);
        b[q] = __builtin_amdgcn_perm(d[2 * q + 1], d[2 * q], 0x07060302u);
    }
    f0 = __builtin_bit_cast(bf16x8, a);
    f1 = __builtin_bit_cast(bf16x8, b);
}
template <bool PERM = false>
__device__ __forceinline__ void load_quad(const u16* W, int ld, int krow, int ncol, bf16x8& f0, bf16x8& f1, bf16x8& f2, bf16x8& f3) {
    v2u d[8];
#pragma unroll
    for (int j = 0; j < 8; ++j) d[j] = *(const v2u*)(W + (size_t)(PERM ? krow + 8 * (j >> 2) + 2 * (j & 3) : krow + j) * ld + ncol);
    v4u a, b, c, e;
#pragma unroll
    for (int q = 0; q < 4; ++q) {
        a[q] = __builtin_amdgcn_perm(d[2 * q + 1][0], d[2 * q][0], 0x05040100u);
        b[q] = __builtin_amdgcn_perm(d[2 * q + 1][0], d[2 * q][0], 0x07060302u);
        c[q] = __builtin_amdgcn_perm(d[2 * q + 1][1], d[2 * q][1], 0x05040100u);
        e[q] = __builtin_amdgcn_perm(d[2 * q + 1][1], d[2 * q][1], 0x07060302u);
    }
    f0 = __builtin_bit_cast(bf16x8, a); f1 = __builtin_bit_cast(bf16x8, b);
    f2 = __builtin_bit_cast(bf16x8, c); f3 = __builtin_bit_cast(bf16x8, e);
}
__device__ __forceinline__ v4u pack_quad(f32x4 t0, f32x4 t1, f32x4 t2, f32x4 t3, bool hf) {
    return v4u{pack2(hf ? t0[2] : t0[0], hf ? t1[2] : t1[0]), pack2(hf ? t2[2] : t2[0], hf ? t3[2] : t3[0]),
               pack2(hf ? t0[3] : t0[1], hf ? t1[3] : t1[1]), pack2(hf ? t2[3] : t2[1], hf ? t3[3] : t3[1])};
}
__device__ __forceinline__ v4u pack_pair(f32x4 t0, f32x4 t1) {
    v4u o;
#pragma unroll
    for (int r = 0; r < 4; ++r) o[r] = pack2(t0[r], t1[r]);
    return o;
}
__device__ __forceinline__ float vmax(float a, float b) { float r; asm("v_max_f32 %0, %1, %2" : "=v"(r) : "v"(a), "v"(b)); return r; }
__device__ __forceinline__ float xmax(float v) {
    auto a = __builtin_amdgcn_permlane16_swap(__float_as_uint(v), __float_as_uint(v), false, false);
    v = vmax(__uint_as_float(a[0]), __uint_as_float(a[1]));
    auto b = __builtin_amdgcn_permlane32_swap(__float_as_uint(v), __float_as_uint(v), false, false);
    return vmax(__uint_as_float(b[0]), __uint_as_float(b[1]));
}
__device__ __forceinline__ float xsum(float v) {
    auto a = __builtin_amdgcn_permlane16_swap(__float_as_uint(v), __float_as_uint(v), false, false);
    v = __uint_as_float(a[0]) + __uint_as_float(a[1]);
    auto b = __builtin_amdgcn_permlane32_swap(__float_as_uint(v), __float_as_uint(v), false, false);
    return __uint_as_float(b[0]) + __uint_as_float(b[1]);
}
template <int CTRL> __device__ __forceinline__ float dpp(float v) {
    return __uint_as_float(__builtin_amdgcn_update_dpp(0u, __float_as_uint(v), CTRL, 0xf, 0xf, false));
}
__device__ __forceinline__ float wave_sum(float v) {
    v += dpp<0xB1>(v);
    v += dpp<0x4E>(v);
    v += dpp<0x141>(v);
    v += dpp<0x140>(v);
    return xsum(v);
}
__device__ __forceinline__ void ln_row(const float (&v)[12], bool act, v2u (&y)[3]) {
    float s = 0.f;
#pragma unroll
    for (int i = 0; i < 12; ++i) s += v[i];
    s = wave_sum(act ? s : 0.f);
    const float mean = s * (1.f / D_);
    float q = 0.f;
#pragma unroll
    for (int i = 0; i < 12; ++i) { const float d = v[i] - mean; q += d * d; }
    q = wave_sum(act ? q : 0.f);
    const float mb = rbf(mean), vb = rbf(q * (1.f / D_));
    const float inv = 1.f / rbf(sqrtf(rbf(vb + 1e-5f)));
#pragma unroll
    for (int j = 0; j < 3; ++j) {
        y[j][0] = pack2(rbf(v[4 * j] - mb) * inv, rbf(v[4 * j + 1] - mb) * inv);
        y[j][1] = pack2(rbf(v[4 * j + 2] - mb) * inv, rbf(v[4 * j + 3] - mb) * inv);
    }
}

__device__ __forceinline__ void role_t0(int idx, const u16* x0, char* ws, rsrc_t rs) {
    const int lane = threadIdx.x & 63, wv = idx * 4 + (threadIdx.x >> 6), row = wv & 31, grp = wv >> 5;
    const bool act = lane < 48;
    const int lc = act ? lane : 0;
    for (int s0 = grp; s0 < NSEG; s0 += (s0 < 3 ? 3 : 12)) {
        const int nu = s0 < 3 ? 1 : 4;
        v2u in[4][3];
#pragma unroll
        for (int u = 0; u < 4; ++u) {
            const int sg = (u < nu && s0 + 3 * u < NSEG) ? s0 + 3 * u : s0;
#pragma unroll
            for (int j = 0; j < 3; ++j) in[u][j] = *(const v2u*)(x0 + (size_t)(sg * SEG + row) * D_ + (j * 48 + lc) * 4);
        }
#pragma unroll
        for (int u = 0; u < 4; ++u) {
            if (u < nu && s0 + 3 * u < NSEG) {
                float v[12];
#pragma unroll
                for (int j = 0; j < 3; ++j) { v[4 * j] = bflo(in[u][j][0]); v[4 * j + 1] = bfhi(in[u][j][0]); v[4 * j + 2] = bflo(in[u][j][1]); v[4 * j + 3] = bfhi(in[u][j][1]); }
                v2u y[3];
                ln_row(v, act, y);
                if (act) {
#pragma unroll
                    for (int j = 0; j < 3; ++j) st8(rs, OFF_XNA + ((s0 + 3 * u) * SEG + row) * (D_ * 2) + (j * 48 + lc) * 8, y[j]);
                }
            }
        }
        DRAIN();
        if (lane < nu && s0 + 3 * lane < NSEG) addw(ws, OFF_CXNA + (s0 + 3 * lane) * 64);
    }
}

template <int NP> struct RIn { v2u xr[2][3]; v2u ar[2][3]; v4u pr[2][NP][3]; };
template <int NP>
__device__ __forceinline__ void role_r_stream(int rid, int idx, int layer, const u16* x0, u16* out, char* ws, rsrc_t rs) {
    const int lane = threadIdx.x & 63;
    const bool act = lane < 48;
    const int lc = act ? lane : 0;
    const u32 cnt = (NP == 1 ? OFF_CPA : OFF_CPB) + layer * 4096;
    const u32 target = NP == 1 ? 8 : 24;
    const bool fin = NP == 4 && layer == 1;
    const u32 sig = NP == 1 ? OFF_CXNB + layer * 4096 : OFF_CXNA + 4096;
    constexpr int NT = 2 * NSEG;
    auto issue = [&](int t, RIn<NP>& in) {
#pragma unroll
        for (int u = 0; u < 2; ++u) {
            const int rl = idx * 4 + (t & 1) * 2 + u, rowg = (t >> 1) * SEG + rl;
#pragma unroll
            for (int j = 0; j < 3; ++j) {
                if (NP == 1 && layer == 0) in.xr[u][j] = *(const v2u*)(x0 + (size_t)rowg * D_ + (j * 48 + lc) * 4);
                else in.xr[u][j] = ld8(rs, OFF_X + (NP == 1 ? 1 : 2 * layer) * XB + rowg * (D_ * 2) + (j * 48 + lc) * 8);
                if (NP == 1) in.ar[u][j] = ld8(rs, OFF_AO + layer * XB + rowg * (D_ * 2) + (j * 48 + lc) * 8);
            }
            if (NP > 1) {
#pragma unroll
                for (int p = 0; p < NP; ++p)
#pragma unroll
                    for (int j = 0; j < 3; ++j) in.pr[u][p][j] = ld16(rs, OFF_PB + ((((t >> 1) * NP + p) * SEG + rl) * D_ + (j * 48 + lc) * 4) * 4);
            }
        }
    };
    RIn<NP> A, B;
    wait_ge(ws, cnt, target);
    issue(0, A);
    bool r1 = true;
    u32 gq = ldw(ws, cnt + 64);
    int pend = -1;
    auto body = [&](int t, RIn<NP>& cur, RIn<NP>& nxt) {
        STAMP(t >> 1, (t & 1) * 2);
        issue(t + 1 < NT ? t + 1 : t, nxt);
        v2u xo[2][3], y[2][3];
#pragma unroll
        for (int u = 0; u < 2; ++u) {
            float v[12];
#pragma unroll
            for (int j = 0; j < 3; ++j)
#pragma unroll
                for (int e = 0; e < 4; ++e) {
                    float a;
                    if (NP == 1) a = (e & 1) ? bfhi(cur.ar[u][j][e >> 1]) : bflo(cur.ar[u][j][e >> 1]);
                    else {
                        a = __uint_as_float(cur.pr[u][0][j][e]);
#pragma unroll
                        for (int p = 1; p < NP; ++p) a += __uint_as_float(cur.pr[u][p][j][e]);
                        a = rbf(a);
                    }
                    const u32 xw = cur.xr[u][j][e >> 1];
                    v[4 * j + e] = rbf(((e & 1) ? bfhi(xw) : bflo(xw)) + a);
                }
#pragma unroll
            for (int j = 0; j < 3; ++j) { xo[u][j][0] = pack2(v[4 * j], v[4 * j + 1]); xo[u][j][1] = pack2(v[4 * j + 2], v[4 * j + 3]); }
            if (!fin) ln_row(v, act, y[u]);
        }
        const bool r2 = t + 2 < NT && (u32)__builtin_amdgcn_readfirstlane(gq) >= target;
        CBAR();
        if (!fin && pend >= 0 && lane == 0) addw(ws, sig + (pend >> 1) * 64);
        if (act) {
#pragma unroll
            for (int u = 0; u < 2; ++u) {
                const int rowg = (t >> 1) * SEG + idx * 4 + (t & 1) * 2 + u;
#pragma unroll
                for (int j = 0; j < 3; ++j) {
                    if (fin) *(v2u*)(out + (size_t)rowg * D_ + (j * 48 + lc) * 4) = xo[u][j];
                    else {
                        st8(rs, (NP == 1 ? OFF_XNB + layer * XB : OFF_XNA + XB) + rowg * (D_ * 2) + (j * 48 + lc) * 8, y[u][j]);
                        st8(rs, OFF_X + (NP == 1 ? 2 * layer : 1) * XB + rowg * (D_ * 2) + (j * 48 + lc) * 8, xo[u][j]);
                    }
                }
            }
        }
        CBAR();
        if (r1) pend = t;
        else {
            DRAIN();
            if (!fin && lane == 0) addw(ws, sig + (t >> 1) * 64);
            pend = -1;
            if (t + 1 < NT) { wait_ge(ws, cnt + ((t + 1) >> 1) * 64, target); issue(t + 1, nxt); }
        }
        gq = ldw(ws, cnt + (t + 3 < NT ? (t + 3) >> 1 : 0) * 64);
        r1 = r2;
        STAMP(t >> 1, (t & 1) * 2 + 1);
    };
    for (int t = 0; t < NT; t += 2) { body(t, A, B); body(t + 1, B, A); }
}

__device__ __forceinline__ void stage_ld(rsrc_t rs, u32 src, v4u (&t)[9]) {
#pragma unroll
    for (int i = 0; i < 9; ++i) t[i] = __builtin_amdgcn_raw_buffer_load_b128(rs, threadIdx.x * 16, src + 4096 * i, 16);
}
__device__ __forceinline__ void stage_st(const v4u (&t)[9], u16* sX) {
#pragma unroll
    for (int i = 0; i < 9; ++i) { const int c = threadIdx.x + 256 * i; *(v4u*)(sX + (c / 72) * 584 + (c % 72) * 8) = t[i]; }
}
__device__ __forceinline__ void pick2(char* ws, bool en1, u32 f1, u32 t1, bool en0, u32 f0, u32 t0, int* sAct) {
    if (threadIdx.x == 0) {
        int act;
        u32 spins = 0;
        for (;;) {
            const u32 a = en1 ? ldw(ws, f1) : 0u, b = en0 ? ldw(ws, f0) : 0u;
            if (en1 && a >= t1) { act = 1; break; }
            if (en0 && b >= t0) { act = 0; break; }
            if (spin_fail(ws, spins)) { act = en0 ? 0 : 1; break; }
            __builtin_amdgcn_s_sleep(SLP_W);
        }
        *sAct = act;
    }
}

__device__ __forceinline__ void role_a1(int rid, int layer, int hg, const u16* wqkv, char* ws, rsrc_t rs, char* smem) {
    const int tid = threadIdx.x, lane = tid & 63, l15 = lane & 15, g = lane >> 4;
    const int wave = __builtin_amdgcn_readfirstlane(tid >> 6);
    u16* sX = (u16*)smem;
    u16* sQ = sX + 32 * 584;
    u16* sKV = sQ + 32 * 80;
    int* sAct = (int*)(sKV + 64 * 152);
    bf16x8 wq[2][18][2];
    {
        int lcx = wave * 64 + 4 * l15;
        if (lcx >= 216) lcx = 212;
        const int col = (lcx / 72) * D_ + 72 * hg + lcx % 72;
#pragma unroll
        for (int ks = 0; ks < 18; ++ks) {
            load_quad(wqkv, NQA, ks * 32 + 8 * g, col, wq[0][ks][0], wq[0][ks][1], wq[1][ks][0], wq[1][ks][1]);
#pragma unroll
            for (int i = 0; i < 2; ++i) { if (ks < 15) { PINA(wq[i][ks][0]); PINA(wq[i][ks][1]); } else { PIN(wq[i][ks][0]); PIN(wq[i][ks][1]); } }
            if (ks % 6 == 5) __builtin_amdgcn_sched_barrier(0);
        }
    }
    const u32 xtarget = layer == 0 ? 32 : 16;
    const u32 fx = OFF_CXNA + layer * 4096, fo = OFF_COA + layer * 4096;
    v4u t9[9];
    STAMP(64, 0);
    wait_ge(ws, fx, xtarget);
    stage_ld(rs, OFF_XNA + layer * XB, t9);
    u32 fa = ldw(ws, fx + 64);
    int pend = -1;
    for (int s = 0; s < NSEG; ++s) {
        STAMP(s, 0);
        stage_st(t9, sX);
        if (tid == 0) *sAct = s > 0 && s + 1 < NSEG && fa >= xtarget;
        __syncthreads();
        const int nxt = *sAct;
        if (nxt) stage_ld(rs, OFF_XNA + layer * XB + (s + 1) * (SEG * D_ * 2), t9);
        fa = ldw(ws, fx + ((s + 2) & 63) * 64);
            {
                f32x4 acc[2][2][2];
                bf16x8 xb[4][2];
                const u16* xr = sX + l15 * 584 + 8 * g;
#pragma unroll
                for (int k = 0; k < 2; ++k)
#pragma unroll
                    for (int m = 0; m < 2; ++m) xb[k][m] = *(const bf16x8*)(xr + m * 16 * 584 + k * 32);
#pragma unroll
                for (int ks = 0; ks < 18; ++ks) {
                    if (ks + 2 < 18) {
#pragma unroll
                        for (int m = 0; m < 2; ++m) xb[(ks + 2) % 4][m] = *(const bf16x8*)(xr + m * 16 * 584 + (ks + 2) * 32);
                    }
#pragma unroll
                    for (int i = 0; i < 2; ++i)
#pragma unroll
                        for (int t = 0; t < 2; ++t)
#pragma unroll
                            for (int m = 0; m < 2; ++m) {
                                if (ks == 0) mmaw<true, false, true>(acc[i][t][m], wq[i][ks][t], xb[ks % 4][m]);
                                else if (ks < 15) mmaw<true, false, false>(acc[i][t][m], wq[i][ks][t], xb[ks % 4][m]);
                                else mmaw<false, false, false>(acc[i][t][m], wq[i][ks][t], xb[ks % 4][m]);
                            }
                }
                MFMA_PAD();
#pragma unroll
                for (int m = 0; m < 2; ++m) {
                    PIN(acc[0][0][m]); PIN(acc[0][1][m]); PIN(acc[1][0][m]); PIN(acc[1][1][m]);
                    const int tok = m * 16 + l15;
#pragma unroll
                    for (int i = 0; i < 2; ++i) {
                        const int lcx = wave * 64 + 16 * g + 8 * i;
                        u16* dst = lcx < 72 ? sQ + tok * 80 + lcx : sKV + ((s * SEG + tok) & 63) * 152 + (lcx - 72);
                        if (lcx < 216) *(v4u*)dst = pack_quad(acc[0][0][m], acc[0][1][m], acc[1][0][m], acc[1][1][m], i);
                    }
                }
            }
            DRAIN();
            __syncthreads();
            if (tid == 0 && pend >= 0) addw(ws, fo + pend * 64);
            {
                const int half = tid & 1, hd = (tid >> 1) & 3, tok = tid >> 3;
                const int pos = s * SEG + tok;
                const float scale = 0.23570226039551584f;
                float q[18];
#pragma unroll
                for (int d = 0; d < 9; ++d) { const u32 w = *(const u32*)(sQ + tok * 80 + hd * 18 + 2 * d); q[2 * d] = bflo(w); q[2 * d + 1] = bfhi(w); }
                float lg[4];
                float mx = -3.0e38f;
#pragma unroll
                for (int w = 0; w < 4; ++w) {
                    const int kp = pos - 2 * (half * 4 + w);
                    const u16* kr = sKV + (kp & 63) * 152 + hd * 18;
                    float a = 0.f;
#pragma unroll
                    for (int d = 0; d < 9; ++d) { const u32 x = *(const u32*)(kr + 2 * d); a += q[2 * d] * bflo(x); a += q[2 * d + 1] * bfhi(x); }
                    lg[w] = kp >= 0 ? rbf(rbf(a) * scale) : -1.0e9f;
                    mx = fmaxf(mx, lg[w]);
                }
                mx = fmaxf(mx, dpp<0xB1>(mx));
                float sum = 0.f;
#pragma unroll
                for (int w = 0; w < 4; ++w) { lg[w] = __expf(lg[w] - mx); sum += lg[w]; }
                sum += dpp<0xB1>(sum);
                const float inv = 1.f / sum;
                float o[18];
#pragma unroll
                for (int d = 0; d < 18; ++d) o[d] = 0.f;
#pragma unroll
                for (int w = 0; w < 4; ++w) {
                    const int kp = pos - 2 * (half * 4 + w);
                    const float p = rbf(lg[w] * inv);
                    const u16* vr = sKV + (kp & 63) * 152 + 72 + hd * 18;
                    if (kp >= 0) {
#pragma unroll
                        for (int d = 0; d < 9; ++d) { const u32 x = *(const u32*)(vr + 2 * d); o[2 * d] += p * bflo(x); o[2 * d + 1] += p * bfhi(x); }
                    }
                }
#pragma unroll
                for (int d = 0; d < 18; ++d) o[d] += dpp<0xB1>(o[d]);
                const u32 dst = OFF_OA + layer * XB + (pos * D_ + 72 * hg + hd * 18) * 2;
#pragma unroll
                for (int d = 0; d < 5; ++d) {
                    const u32 lo = pack2(o[2 * d], o[2 * d + 1]);
                    const u32 hi = d < 4 ? pack2(o[10 + 2 * d], o[11 + 2 * d]) : 0u;
                    if (half == 0 || d < 4) st4(rs, dst + (half ? 20 : 0) + 4 * d, half ? hi : lo);
                }
            }
        if (nxt) pend = s;
        else {
            DRAIN();
            __syncthreads();
            if (tid == 0) addw(ws, fo + s * 64);
            pend = -1;
            if (s + 1 < NSEG) {
                if (tid == 0) wait_ge(ws, fx + (s + 1) * 64, xtarget);
                __syncthreads();
                stage_ld(rs, OFF_XNA + layer * XB + (s + 1) * (SEG * D_ * 2), t9);
            }
        }
        STAMP(s, 1);
    }
}

__device__ __forceinline__ void role_a2(int rid, int hg, const u16* woA, char* ws, rsrc_t rs, char* smem) {
    const int tid = threadIdx.x, lane = tid & 63, l15 = lane & 15, g = lane >> 4;
    const int wave = __builtin_amdgcn_readfirstlane(tid >> 6);
    u16* sX = (u16*)smem;
    int* sAct = (int*)(sX + 32 * 584);
    bf16x8 w[18][2];
#pragma unroll
    for (int ks = 0; ks < 18; ++ks) {
        load_pair(woA, D_, ks * 32 + 8 * g, 72 * hg + (wave < 2 ? wave * 32 + 2 * l15 : 64 + (l15 & 6)), w[ks][0], w[ks][1]);
        if (wave >= 2 && (l15 & 1)) w[ks][0] = w[ks][1];
        PINA(w[ks][0]); PINA(w[ks][1]);
    }
    const u32 f0 = OFF_COA, f1 = OFF_COA + 4096;
    int nx0 = 1, nx1 = 0, layer = 0, s = 0;
    u32 pend = 0;
    v4u t9[9];
    wait_ge(ws, f0, 8);
    stage_ld(rs, OFF_OA, t9);
    u32 fa = ldw(ws, f1), fb = ldw(ws, f0 + 64);
    for (;;) {
        STAMP(layer * 64 + s, 2);
        stage_st(t9, sX);
        if (tid == 0) *sAct = (nx1 < NSEG && fa >= 8) ? 1 : ((nx0 < NSEG && nx0 > 1 && fb >= 8) ? 0 : -1);
        __syncthreads();
        const int nxt = *sAct;
        const int cl = layer, cs = s;
        if (nxt >= 0) {
            layer = nxt;
            s = nxt ? nx1++ : nx0++;
            stage_ld(rs, OFF_OA + layer * XB + s * (SEG * D_ * 2), t9);
            fa = ldw(ws, f1 + (nx1 & 63) * 64); fb = ldw(ws, f0 + (nx0 & 63) * 64);
        }
        f32x4 acc[2][2];
        const u16* xr = sX + l15 * 584 + 8 * g;
        if (wave < 2) {
            bf16x8 xb[4][2];
#pragma unroll
            for (int k = 0; k < 2; ++k)
#pragma unroll
                for (int m = 0; m < 2; ++m) xb[k][m] = *(const bf16x8*)(xr + m * 16 * 584 + k * 32);
#pragma unroll
            for (int ks = 0; ks < 18; ++ks) {
                if (ks + 2 < 18) {
#pragma unroll
                    for (int m = 0; m < 2; ++m) xb[(ks + 2) % 4][m] = *(const bf16x8*)(xr + m * 16 * 584 + (ks + 2) * 32);
                }
#pragma unroll
                for (int t = 0; t < 2; ++t)
#pragma unroll
                    for (int m = 0; m < 2; ++m) { if (ks == 0) mmaw<true, false, true>(acc[t][m], w[ks][t], xb[ks % 4][m]); else mmaw<true, false, false>(acc[t][m], w[ks][t], xb[ks % 4][m]); }
            }
            MFMA_PAD();
#pragma unroll
            for (int t = 0; t < 2; ++t)
#pragma unroll
                for (int m = 0; m < 2; ++m) PIN(acc[t][m]);
        } else {
            bf16x8 xb[18];
#pragma unroll
            for (int ks = 0; ks < 18; ++ks) xb[ks] = *(const bf16x8*)(xr + (wave - 2) * 16 * 584 + ks * 32);
#pragma unroll
            for (int ks = 0; ks < 18; ++ks) { if (ks == 0) mmaw<true, false, true>(acc[0][0], w[ks][0], xb[ks]); else mmaw<true, false, false>(acc[0][0], w[ks][0], xb[ks]); }
            MFMA_PAD();
            PIN(acc[0][0]);
            acc[0][1] = acc[0][0]; acc[1][0] = acc[0][0]; acc[1][1] = acc[0][0];
        }
        DRAIN();
        __syncthreads();
        if (tid == 0 && pend) addw(ws, pend);
        const u32 dst = OFF_AO + cl * XB + (cs * SEG * D_ + 72 * hg) * 2;
        if (wave < 2) {
#pragma unroll
            for (int m = 0; m < 2; ++m) st16(rs, dst + ((m * 16 + l15) * D_ + wave * 32 + 8 * g) * 2, pack_pair(acc[0][m], acc[1][m]));
        } else if (g < 2) st8(rs, dst + (((wave - 2) * 16 + l15) * D_ + 64 + 4 * g) * 2, v2u{pack2(acc[0][0][0], acc[0][0][1]), pack2(acc[0][0][2], acc[0][0][3])});
        const u32 sig = OFF_CPA + cl * 4096 + cs * 64;
        if (nxt >= 0) pend = sig;
        else {
            DRAIN();
            __syncthreads();
            if (tid == 0) addw(ws, sig);
            pend = 0;
            if (nx0 >= NSEG && nx1 >= NSEG) break;
            pick2(ws, nx1 < NSEG, f1 + (nx1 & 63) * 64, 8, nx0 < NSEG, f0 + (nx0 & 63) * 64, 8, sAct + 1);
            __syncthreads();
            layer = sAct[1];
            s = layer ? nx1++ : nx0++;
            stage_ld(rs, OFF_OA + layer * XB + s * (SEG * D_ * 2), t9);
            fa = ldw(ws, f1 + (nx1 & 63) * 64); fb = ldw(ws, f0 + (nx0 & 63) * 64);
        }
        STAMP(cl * 64 + cs, 3);
    }
}

__device__ __forceinline__ void role_w(int rid, int kb, int nb, const u16* Wo, char* ws, rsrc_t rs, char* smem) {
    const int tid = threadIdx.x, lane = tid & 63, l15 = lane & 15, g = lane >> 4;
    const int wave = __builtin_amdgcn_readfirstlane(tid >> 6);
    float* sR = (float*)smem;
    int* sAct = (int*)(smem + 4 * 32 * 100 * 4);
    u16* sO = (u16*)(smem + 4 * 32 * 100 * 4 + 64) + wave * (32 * 264);
    bf16x8 w[3][8][2];
#pragma unroll
    for (int i = 0; i < 3; ++i)
#pragma unroll
        for (int ks = 0; ks < 8; ++ks) {
            load_pair(Wo, D_, kb * 1024 + wave * 256 + ks * 32 + 8 * g, nb * 96 + i * 32 + 2 * l15, w[i][ks][0], w[i][ks][1]);
            PINA(w[i][ks][0]); PINA(w[i][ks][1]);
        }
    const u32 f0 = OFF_COB + kb * 64, f1 = OFF_COB + 64 * 256 + kb * 64;
    int nx0 = 1, nx1 = 0, layer = 0, s = 0, par = 0;
    u32 pend = 0;
    v4u tb[16];
    auto issue = [&](int sg) {
#pragma unroll
        for (int i = 0; i < 16; ++i)
            tb[i] = __builtin_amdgcn_raw_buffer_load_b128(rs, (lane >> 5) * (NB * 2) + (lane & 31) * 16, OFF_OB + ((sg * SEG + 2 * i) * NB + kb * 1024 + wave * 256) * 2, 16);
    };
    wait_ge(ws, f0, 16);
    issue(0);
    u32 fa = ldw(ws, f0 + 256), fb = ldw(ws, f1);
    for (;;) {
        STAMP(layer * 64 + s, 0);
        float* sRp = sR;
        {
#pragma unroll
            for (int i = 0; i < 16; ++i) *(v4u*)(sO + (2 * i + (lane >> 5)) * 264 + (lane & 31) * 8) = tb[i];
            bf16x8 b[2][8];
#pragma unroll
            for (int m = 0; m < 2; ++m)
#pragma unroll
                for (int ks = 0; ks < 8; ++ks) b[m][ks] = *(const bf16x8*)(sO + (m * 16 + l15) * 264 + ks * 32 + 8 * g);
            f32x4 acc[3][2][2];
#pragma unroll
            for (int m = 0; m < 2; ++m)
#pragma unroll
                for (int ks = 0; ks < 8; ++ks)
#pragma unroll
                    for (int i = 0; i < 3; ++i)
#pragma unroll
                        for (int t = 0; t < 2; ++t) {
                            if (ks == 0) mmaw<true, false, true>(acc[i][t][m], w[i][ks][t], b[m][ks]);
                            else mmaw<true, false, false>(acc[i][t][m], w[i][ks][t], b[m][ks]);
                        }
            MFMA_PAD();
            __syncthreads();
#pragma unroll
            for (int i = 0; i < 3; ++i)
#pragma unroll
                for (int m = 0; m < 2; ++m) {
                    PIN(acc[i][0][m]); PIN(acc[i][1][m]);
                    const f32x4 a0 = acc[i][0][m], a1 = acc[i][1][m];
                    float* dst = sRp + (wave * 32 + m * 16 + l15) * 100 + i * 32 + 8 * g;
                    *(f32x4*)dst = f32x4{a0[0], a1[0], a0[1], a1[1]};
                    *(f32x4*)(dst + 4) = f32x4{a0[2], a1[2], a0[3], a1[3]};
                }
        }
        DRAIN();
        if (tid == 0) sAct[par] = (nx0 < NSEG && fa >= 16) ? 0 : ((nx1 < NSEG && fb >= 16) ? 1 : -1);
        __syncthreads();
        if (tid == 0 && pend) addw(ws, pend);
        const int nxt = sAct[par];
        const int cs = s;
        const u32 sig = OFF_CPB + layer * 4096 + s * 64;
        if (nxt >= 0) {
            layer = nxt;
            s = nxt ? nx1++ : nx0++;
            issue(s);
            fa = ldw(ws, f0 + (nx0 & 63) * 256); fb = ldw(ws, f1 + (nx1 & 63) * 256);
        }
#pragma unroll
        for (int i = 0; i < 3; ++i) {
            const int c = tid + 256 * i, tok = c / 24, c4 = (c % 24) * 4;
            f32x4 a = *(const f32x4*)(sRp + tok * 100 + c4);
#pragma unroll
            for (int wv = 1; wv < 4; ++wv) a += *(const f32x4*)(sRp + (wv * 32 + tok) * 100 + c4);
            st16(rs, OFF_PB + (((cs * 4 + kb) * SEG + tok) * D_ + nb * 96 + c4) * 4, __builtin_bit_cast(v4u, a));
        }
        if (nxt >= 0) pend = sig;
        else {
            DRAIN();
            __syncthreads();
            if (tid == 0) addw(ws, sig);
            pend = 0;
            if (nx0 >= NSEG && nx1 >= NSEG) break;
            pick2(ws, nx0 < NSEG, f0 + (nx0 & 63) * 256, 16, nx1 < NSEG, f1 + (nx1 & 63) * 256, 16, sAct + 2);
            __syncthreads();
            layer = 1 - sAct[2];
            s = layer ? nx1++ : nx0++;
            issue(s);
            fa = ldw(ws, f0 + (nx0 & 63) * 256); fb = ldw(ws, f1 + (nx1 & 63) * 256);
        }
        par ^= 1;
    }
}

#define PDV 6
struct NoFill { __device__ __forceinline__ void operator()(int) const {} };
template <int NR, int NA, int MT, int TY0 = 0, int TY1 = 3, class F = NoFill>
__device__ __forceinline__ void qkv_gemm(const bf16x8 (&w)[3][NR][2], const char* sW, const u16* xr, f32x4 (&acc)[3][2][MT], F fill = F()) {
    static_assert(NR == 14 || NR == 13, "k-step orders below");
    constexpr int KS14[18] = {0, 1, 2, 14, 3, 4, 5, 15, 6, 7, 8, 16, 9, 10, 11, 17, 12, 13};
    constexpr int KS13[18] = {0, 1, 13, 2, 3, 4, 14, 5, 6, 15, 7, 8, 9, 16, 10, 11, 17, 12};
#define KS (NR == 14 ? KS14 : KS13)
    constexpr int PD = (TY1 - TY0) * MT >= 3 ? 2 : ((TY1 - TY0) == 2 ? 4 : PDV), RG = PD + 2;
    bf16x8 xb[RG][MT], lw[6];
#pragma unroll
    for (int k = 0; k < PD; ++k)
#pragma unroll
        for (int m = 0; m < MT; ++m) xb[k][m] = *(const bf16x8*)(xr + m * 16 * 584 + KS[k] * 32);
#pragma unroll
    for (int j = 2 * TY0; j < 2 * TY1; ++j) lw[j] = *(const bf16x8*)(sW + j * 1024);
#pragma unroll
    for (int ps = 0; ps < 18; ++ps) {
        const int ks = KS[ps];
        if (ps + PD < 18) {
#pragma unroll
            for (int m = 0; m < MT; ++m) xb[(ps + PD) % RG][m] = *(const bf16x8*)(xr + m * 16 * 584 + KS[ps + PD < 18 ? ps + PD : 0] * 32);
        }
#pragma unroll
        for (int ty = TY0; ty < TY1; ++ty)
#pragma unroll
            for (int t = 0; t < 2; ++t)
#pragma unroll
                for (int m = 0; m < MT; ++m) {
                    const bf16x8& f = ks < NR ? w[ty][ks < NR ? ks : 0][t] : lw[ty * 2 + t];
                    if (ty < 2) {
                        if (ps == 0) mmaw<true, false, true>(acc[ty][t][m], f, xb[ps % RG][m]);
                        else if (ks < NA) mmaw<true, false, false>(acc[ty][t][m], f, xb[ps % RG][m]);
                        else mmaw<false, false, false>(acc[ty][t][m], f, xb[ps % RG][m]);
                    } else {
                        if (ps == 0) mmaw<true, true, true>(acc[ty][t][m], f, xb[ps % RG][m]);
                        else if (ks < NA) mmaw<true, true, false>(acc[ty][t][m], f, xb[ps % RG][m]);
                        else mmaw<false, true, false>(acc[ty][t][m], f, xb[ps % RG][m]);
                    }
                }
        if (ps > 0 && KS[ps > 0 ? ps - 1 : 0] >= NR && KS[ps > 0 ? ps - 1 : 0] < 17) {
            CBAR();
#pragma unroll
            for (int j = 2 * TY0; j < 2 * TY1; ++j) lw[j] = *(const bf16x8*)(sW + ((KS[ps > 0 ? ps - 1 : 0] + 1 - NR) * 6 + j) * 1024);
        }
        fill(ps);
    }
    MFMA_PAD();
#pragma unroll
    for (int ty = TY0; ty < TY1; ++ty)
#pragma unroll
        for (int t = 0; t < 2; ++t)
#pragma unroll
            for (int m = 0; m < MT; ++m) PIN(acc[ty][t][m]);
}
template <int NR, int NA, bool PERM>
__device__ __forceinline__ void load_qkv_w(const u16* Wq, const u16* Wk, const u16* Wv, int h, int wave, int l15, int g, bf16x8 (&w)[3][NR][2], char* sW) {
    const u16* Wa = wave < 2 ? Wq : Wk;
    const int ca = h * 128 + (wave & 1) * 64 + 4 * l15, cv = h * 128 + wave * 32 + 2 * l15;
#pragma unroll
    for (int ks = 17; ks >= 0; --ks) {
        bf16x8 f[6];
        if (PERM && (ks == 8 || ks == 17)) {
            load_quad<true>(Wa, NB, ks * 32 + 16 * (g & 1) + (g >> 1), ca, f[0], f[1], f[2], f[3]);
            load_pair<true>(Wv, NB, ks * 32 + 16 * (g & 1) + (g >> 1), cv, f[4], f[5]);
        } else {
            load_quad(Wa, NB, ks * 32 + 8 * g, ca, f[0], f[1], f[2], f[3]);
            load_pair(Wv, NB, ks * 32 + 8 * g, cv, f[4], f[5]);
        }
#pragma unroll
        for (int j = 0; j < 6; ++j) {
            if (ks < NA) PINA(f[j]); else PIN(f[j]);
            if (ks < NR) w[j >> 1][ks < NR ? ks : 0][j & 1] = f[j];
            else *(bf16x8*)(sW + ((ks - NR) * 6 + j) * 1024) = f[j];
        }
        if (ks % 3 == 0) __builtin_amdgcn_sched_barrier(0);
    }
}

__device__ __forceinline__ void softmax_t(const float (&l)[3][4], bf16x4 (&p)[3]) {
    float mx = -3.0e38f;
#pragma unroll
    for (int t = 0; t < 3; ++t)
#pragma unroll
        for (int r = 0; r < 4; ++r) mx = fmaxf(mx, l[t][r]);
    mx = xmax(mx);
    float e[3][4], sum = 0.f;
#pragma unroll
    for (int t = 0; t < 3; ++t)
#pragma unroll
        for (int r = 0; r < 4; ++r) { e[t][r] = __expf(l[t][r] - mx); sum += e[t][r]; }
    sum = xsum(sum);
    const float inv = 1.f / sum;
#pragma unroll
    for (int t = 0; t < 3; ++t) {
        v2u w = {pack2(e[t][0] * inv, e[t][1] * inv), pack2(e[t][2] * inv, e[t][3] * inv)};
        p[t] = __builtin_bit_cast(bf16x4, w);
    }
}

constexpr int H_NR = 13;
__device__ __forceinline__ void role_h(int rid, int layer, int h, const u16* Wq, const u16* Wk, const u16* Wv, char* ws, rsrc_t rs, char* smem) {
    const int tid = threadIdx.x, lane = tid & 63, l15 = lane & 15, g = lane >> 4;
    const int wave = __builtin_amdgcn_readfirstlane(tid >> 6);
    u16* sX = (u16*)smem;
    int* sAct = (int*)(smem + 32 * 584 * 2);
    char* sW = smem + 32 * 584 * 2 + 64 + wave * (6 * (18 - H_NR) * 1024) + lane * 16;
    bf16x8 w[3][H_NR][2];
    load_qkv_w<H_NR, 10, false>(Wq, Wk, Wv, h, wave, l15, g, w, sW);
    const u32 fx = OFF_CXNB + layer * 4096, fo = OFF_FKV + (layer * 32 + h) * 256;
    v4u t9[9];
    wait_ge(ws, OFF_POL, 4);
    const int lp = layer ^ __builtin_amdgcn_readfirstlane(ldw(ws, OFF_POL));
    wait_ge(ws, fx, 16);
    stage_ld(rs, OFF_XNB + layer * XB, t9);
    u32 fa = ldw(ws, fx + 64);
    int pend = -1;
    for (int s = 0; s < NSEG; ++s) {
        STAMP(s, 0);
        stage_st(t9, sX);
        if (tid == 0) *sAct = s + 1 < NSEG && fa >= 16;
        __syncthreads();
        const int nxt = *sAct;
        if (nxt) stage_ld(rs, OFF_XNB + layer * XB + (s + 1) * (SEG * D_ * 2), t9);
        fa = ldw(ws, fx + ((s + 2) & 63) * 64);
        f32x4 acc[3][2][2];
        qkv_gemm<H_NR, 10, 2>(w, sW, sX + l15 * 584 + 8 * g, acc);
        DRAIN();
        __syncthreads();
        if (tid == 0 && pend >= 0) __hip_atomic_store((gu32*)(ws + fo + pend * 4), 1u, RLX_AGENT);
        const u32 slot = hslot(lp, s, h);
#pragma unroll
        for (int m = 0; m < 2; ++m) {
            const u32 o = (wave < 2 ? OFF_QX : OFF_KX) + slot + m * 4096 + (2 * (wave & 1) + (g >> 1)) * 1024 + (2 * (g & 1) * 16 + l15) * 16;
            st16(rs, o, pack_quad(acc[0][0][m], acc[0][1][m], acc[1][0][m], acc[1][1][m], false));
            st16(rs, o + 256, pack_quad(acc[0][0][m], acc[0][1][m], acc[1][0][m], acc[1][1][m], true));
        }
#pragma unroll
        for (int t = 0; t < 2; ++t)
            st16(rs, OFF_VX + slot + (t * 4 + wave) * 1024 + lane * 16,
                 v4u{pack2(acc[2][t][0][0], acc[2][t][0][1]), pack2(acc[2][t][0][2], acc[2][t][0][3]),
                     pack2(acc[2][t][1][0], acc[2][t][1][1]), pack2(acc[2][t][1][2], acc[2][t][1][3])});
        if (nxt) pend = s;
        else {
            DRAIN();
            __syncthreads();
            if (tid == 0) __hip_atomic_store((gu32*)(ws + fo + s * 4), 1u, RLX_AGENT);
            pend = -1;
            if (s + 1 < NSEG) {
                if (tid == 0) wait_ge(ws, fx + (s + 1) * 64, 16);
                __syncthreads();
                stage_ld(rs, OFF_XNB + layer * XB + (s + 1) * (SEG * D_ * 2), t9);
            }
        }
        STAMP(s, 1);
    }
}

#define SB() __builtin_amdgcn_sched_barrier(0)
__device__ __forceinline__ u32 now32() { return (u32)__builtin_amdgcn_s_memtime(); }
constexpr int L_M = 20480, L_C = 31872;
__device__ __forceinline__ void role_chain(int rid, int layer, int h, int p, const u16* mem0, const u16* Wq, const u16* Wk, const u16* Wv,
                                           const u16* Wo, char* ws, char* wd, rsrc_t rs, char* smem) {
    const int tid = threadIdx.x;
    const int wave = __builtin_amdgcn_readfirstlane(tid >> 6);
    {
        const v4u ff = {~0u, ~0u, ~0u, ~0u};
#pragma unroll
        for (int i = 0; i < 6; ++i) {
            const int pg = (layer * 64 + 2 * h + p) * 6 + i;
            st16(rs, (pg < 384 ? OFF_XA + pg * 4096 : OFF_XB + (pg - 384) * 4096) + tid * 16, ff);
        }
    }
    for (int i = tid; i < 8 * 576; i += 256) {
        const int gid = i >> 6, r = (i >> 3) & 7, a = i & 7;
        ((u16*)(smem + L_M))[i] = mem0[r * 576 + 32 * (gid >> 2) + 16 * (gid & 1) + 2 * a + ((gid >> 1) & 1)];
    }
    if (tid < 12) ((u32*)(smem + L_C))[tid] = tid < 4 ? 0u : tid < 8 ? 0x3f803f80u : 0x3f003f00u;
    asm volatile(
    "s_mov_b64 s[40:41], %0\n"
    "s_and_b32 s41, s41, 0xffff\n"
    "s_mov_b32 s42, 0xa000000\n"
    "s_mov_b32 s43, 0x20000\n"
    "v_mbcnt_lo_u32_b32 v112, -1, 0\n"
    "v_mbcnt_hi_u32_b32 v112, -1, v112\n"
    "v_and_b32 v113, 15, v112\n"
    "v_lshrrev_b32 v114, 4, v112\n"
    "v_lshlrev_b32 v0, 4, v112\n"
    "v_mov_b32 v11, 0\n"
    "s_mov_b32 s57, 131072\n"
    "v_and_b32 v115, 1, v114\n"
    "v_lshrrev_b32 v116, 1, v114\n"
    "v_lshlrev_b32 v117, 3, v115\n"
    "v_add_u32 v117, v117, v116\n"
    "v_lshlrev_b32 v117, 13, v117\n"
    "v_lshl_add_u32 v117, v113, 2, v117\n"
    "v_lshlrev_b32 v118, 2, v116\n"
    "v_add_u32 v118, v118, v115\n"
    "v_mul_u32_u24 v118, 4608, v118\n"
    "v_lshl_add_u32 v118, v113, 2, v118\n"
    "s_mul_i32 s73, %6, 15\n"
    "s_cmp_eq_u32 %6, 3\n"
    "s_cselect_b32 s51, 2, 0\n"
    "s_add_u32 s73, s73, s51\n"
    "s_lshl_b32 s73, s73, 11\n"
    "s_add_u32 s73, s73, 31936\n"
    "v_add_u32 v2, s73, v0\n"
    "s_lshl_b32 s60, %7, 8\n"
    "s_lshl_b32 s73, %6, 6\n"
    "s_add_u32 s60, s60, s73\n"
    "s_mov_b32 s61, 0x05040100\n"
    "s_mov_b32 s62, 0x07060302\n"
    "s_bitcmp1_b32 %6, 0\n"
    "s_cselect_b32 s63, s62, s61\n"
    "s_mul_i32 s51, %7, 147456\n"
    "s_mul_i32 s64, %8, 576\n"
    "s_add_u32 s51, s51, s64\n"
    "s_add_u32 s68, s51, 512\n"
    "s_add_u32 s66, s51, s73\n"
    "s_add_u32 s67, s66, 256\n"
    "s_mov_b64 s[44:45], %5\n"
    "s_and_b32 s45, s45, 0xffff\n"
    "s_mov_b32 s46, 0x480000\n"
    "s_mov_b32 s47, 0x20000\n"
    "s_add_u32 s64, s66, 0\n"
    "buffer_load_dword v32, v118, s[44:47], s64 offen\n"
    "s_add_u32 s65, s66, 0\n"
    "buffer_load_dword v33, v118, s[44:47], s65 offen offset:1152\n"
    "s_add_u32 s64, s66, 9216\n"
    "buffer_load_dword v34, v118, s[44:47], s64 offen\n"
    "s_add_u32 s65, s66, 9216\n"
    "buffer_load_dword v35, v118, s[44:47], s65 offen offset:1152\n"
    "s_add_u32 s64, s66, 0\n"
    "buffer_load_dword v36, v118, s[44:47], s64 offen offset:2304\n"
    "s_add_u32 s65, s66, 0\n"
    "buffer_load_dword v37, v118, s[44:47], s65 offen offset:3456\n"
    "s_add_u32 s64, s66, 9216\n"
    "buffer_load_dword v38, v118, s[44:47], s64 offen offset:2304\n"
    "s_add_u32 s65, s66, 9216\n"
    "buffer_load_dword v39, v118, s[44:47], s65 offen offset:3456\n"
    "s_add_u32 s64, s66, 36864\n"
    "buffer_load_dword v40, v118, s[44:47], s64 offen\n"
    "s_add_u32 s65, s66, 36864\n"
    "buffer_load_dword v41, v118, s[44:47], s65 offen offset:1152\n"
    "s_add_u32 s64, s66, 46080\n"
    "buffer_load_dword v42, v118, s[44:47], s64 offen\n"
    "s_add_u32 s65, s66, 46080\n"
    "buffer_load_dword v43, v118, s[44:47], s65 offen offset:1152\n"
    "s_add_u32 s64, s66, 36864\n"
    "buffer_load_dword v44, v118, s[44:47], s64 offen offset:2304\n"
    "s_add_u32 s65, s66, 36864\n"
    "buffer_load_dword v45, v118, s[44:47], s65 offen offset:3456\n"
    "s_add_u32 s64, s66, 46080\n"
    "buffer_load_dword v46, v118, s[44:47], s64 offen offset:2304\n"
    "s_add_u32 s65, s66, 46080\n"
    "buffer_load_dword v47, v118, s[44:47], s65 offen offset:3456\n"
    "s_add_u32 s64, s66, 73728\n"
    "buffer_load_dword v48, v118, s[44:47], s64 offen\n"
    "s_add_u32 s65, s66, 73728\n"
    "buffer_load_dword v49, v118, s[44:47], s65 offen offset:1152\n"
    "s_add_u32 s64, s66, 82944\n"
    "buffer_load_dword v50, v118, s[44:47], s64 offen\n"
    "s_add_u32 s65, s66, 82944\n"
    "buffer_load_dword v51, v118, s[44:47], s65 offen offset:1152\n"
    "s_add_u32 s64, s66, 73728\n"
    "buffer_load_dword v52, v118, s[44:47], s64 offen offset:2304\n"
    "s_add_u32 s65, s66, 73728\n"
    "buffer_load_dword v53, v118, s[44:47], s65 offen offset:3456\n"
    "s_add_u32 s64, s66, 82944\n"
    "buffer_load_dword v54, v118, s[44:47], s64 offen offset:2304\n"
    "s_add_u32 s65, s66, 82944\n"
    "buffer_load_dword v55, v118, s[44:47], s65 offen offset:3456\n"
    "s_add_u32 s64, s66, 110592\n"
    "buffer_load_dword v56, v118, s[44:47], s64 offen\n"
    "s_add_u32 s65, s66, 110592\n"
    "buffer_load_dword v57, v118, s[44:47], s65 offen offset:1152\n"
    "s_add_u32 s64, s66, 119808\n"
    "buffer_load_dword v58, v118, s[44:47], s64 offen\n"
    "s_add_u32 s65, s66, 119808\n"
    "buffer_load_dword v59, v118, s[44:47], s65 offen offset:1152\n"
    "s_add_u32 s64, s66, 110592\n"
    "buffer_load_dword v60, v118, s[44:47], s64 offen offset:2304\n"
    "s_add_u32 s65, s66, 110592\n"
    "buffer_load_dword v61, v118, s[44:47], s65 offen offset:3456\n"
    "s_add_u32 s64, s66, 119808\n"
    "buffer_load_dword v62, v118, s[44:47], s64 offen offset:2304\n"
    "s_add_u32 s65, s66, 119808\n"
    "buffer_load_dword v63, v118, s[44:47], s65 offen offset:3456\n"
    "s_add_u32 s64, s67, 0\n"
    "buffer_load_dword v64, v118, s[44:47], s64 offen\n"
    "s_add_u32 s65, s67, 0\n"
    "buffer_load_dword v65, v118, s[44:47], s65 offen offset:1152\n"
    "s_add_u32 s64, s67, 9216\n"
    "buffer_load_dword v66, v118, s[44:47], s64 offen\n"
    "s_add_u32 s65, s67, 9216\n"
    "buffer_load_dword v67, v118, s[44:47], s65 offen offset:1152\n"
    "s_add_u32 s64, s67, 0\n"
    "buffer_load_dword v68, v118, s[44:47], s64 offen offset:2304\n"
    "s_add_u32 s65, s67, 0\n"
    "buffer_load_dword v69, v118, s[44:47], s65 offen offset:3456\n"
    "s_add_u32 s64, s67, 9216\n"
    "buffer_load_dword v70, v118, s[44:47], s64 offen offset:2304\n"
    "s_add_u32 s65, s67, 9216\n"
    "buffer_load_dword v71, v118, s[44:47], s65 offen offset:3456\n"
    "s_add_u32 s64, s67, 36864\n"
    "buffer_load_dword v72, v118, s[44:47], s64 offen\n"
    "s_add_u32 s65, s67, 36864\n"
    "buffer_load_dword v73, v118, s[44:47], s65 offen offset:1152\n"
    "s_add_u32 s64, s67, 46080\n"
    "buffer_load_dword v74, v118, s[44:47], s64 offen\n"
    "s_add_u32 s65, s67, 46080\n"
    "buffer_load_dword v75, v118, s[44:47], s65 offen offset:1152\n"
    "s_add_u32 s64, s67, 36864\n"
    "buffer_load_dword v76, v118, s[44:47], s64 offen offset:2304\n"
    "s_add_u32 s65, s67, 36864\n"
    "buffer_load_dword v77, v118, s[44:47], s65 offen offset:3456\n"
    "s_add_u32 s64, s67, 46080\n"
    "buffer_load_dword v78, v118, s[44:47], s64 offen offset:2304\n"
    "s_add_u32 s65, s67, 46080\n"
    "buffer_load_dword v79, v118, s[44:47], s65 offen offset:3456\n"
    "s_add_u32 s64, s67, 73728\n"
    "buffer_load_dword v80, v118, s[44:47], s64 offen\n"
    "s_add_u32 s65, s67, 73728\n"
    "buffer_load_dword v81, v118, s[44:47], s65 offen offset:1152\n"
    "s_add_u32 s64, s67, 82944\n"
    "buffer_load_dword v82, v118, s[44:47], s64 offen\n"
    "s_add_u32 s65, s67, 82944\n"
    "buffer_load_dword v83, v118, s[44:47], s65 offen offset:1152\n"
    "s_add_u32 s64, s67, 73728\n"
    "buffer_load_dword v84, v118, s[44:47], s64 offen offset:2304\n"
    "s_add_u32 s65, s67, 73728\n"
    "buffer_load_dword v85, v118, s[44:47], s65 offen offset:3456\n"
    "s_add_u32 s64, s67, 82944\n"
    "buffer_load_dword v86, v118, s[44:47], s64 offen offset:2304\n"
    "s_add_u32 s65, s67, 82944\n"
    "buffer_load_dword v87, v118, s[44:47], s65 offen offset:3456\n"
    "s_add_u32 s64, s67, 110592\n"
    "buffer_load_dword v88, v118, s[44:47], s64 offen\n"
    "s_add_u32 s65, s67, 110592\n"
    "buffer_load_dword v89, v118, s[44:47], s65 offen offset:1152\n"
    "s_add_u32 s64, s67, 119808\n"
    "buffer_load_dword v90, v118, s[44:47], s64 offen\n"
    "s_add_u32 s65, s67, 119808\n"
    "buffer_load_dword v91, v118, s[44:47], s65 offen offset:1152\n"
    "s_add_u32 s64, s67, 110592\n"
    "buffer_load_dword v92, v118, s[44:47], s64 offen offset:2304\n"
    "s_add_u32 s65, s67, 110592\n"
    "buffer_load_dword v93, v118, s[44:47], s65 offen offset:3456\n"
    "s_add_u32 s64, s67, 119808\n"
    "buffer_load_dword v94, v118, s[44:47], s64 offen offset:2304\n"
    "s_add_u32 s65, s67, 119808\n"
    "buffer_load_dword v95, v118, s[44:47], s65 offen offset:3456\n"
    "s_waitcnt vmcnt(62)\n"
    "v_perm_b32 v96, v33, v32, s61\n"
    "s_waitcnt vmcnt(60)\n"
    "v_perm_b32 v97, v35, v34, s61\n"
    "s_waitcnt vmcnt(58)\n"
    "v_perm_b32 v98, v37, v36, s61\n"
    "s_waitcnt vmcnt(56)\n"
    "v_perm_b32 v99, v39, v38, s61\n"
    "s_waitcnt vmcnt(54)\n"
    "v_perm_b32 v100, v41, v40, s61\n"
    "s_waitcnt vmcnt(52)\n"
    "v_perm_b32 v101, v43, v42, s61\n"
    "s_waitcnt vmcnt(50)\n"
    "v_perm_b32 v102, v45, v44, s61\n"
    "s_waitcnt vmcnt(48)\n"
    "v_perm_b32 v103, v47, v46, s61\n"
    "ds_write_b128 v2, v[96:99] offset:14336\n"
    "ds_write_b128 v2, v[100:103] offset:15360\n"
    "v_perm_b32 v104, v33, v32, s62\n"
    "v_perm_b32 v105, v35, v34, s62\n"
    "v_perm_b32 v106, v37, v36, s62\n"
    "v_perm_b32 v107, v39, v38, s62\n"
    "v_perm_b32 v108, v41, v40, s62\n"
    "v_perm_b32 v109, v43, v42, s62\n"
    "v_perm_b32 v110, v45, v44, s62\n"
    "v_perm_b32 v111, v47, v46, s62\n"
    "ds_write_b128 v2, v[104:107] offset:16384\n"
    "ds_write_b128 v2, v[108:111] offset:17408\n"
    "s_add_u32 s64, s68, 0\n"
    "buffer_load_dword v32, v118, s[44:47], s64 offen\n"
    "s_add_u32 s65, s68, 0\n"
    "buffer_load_dword v33, v118, s[44:47], s65 offen offset:1152\n"
    "s_add_u32 s64, s68, 9216\n"
    "buffer_load_dword v34, v118, s[44:47], s64 offen\n"
    "s_add_u32 s65, s68, 9216\n"
    "buffer_load_dword v35, v118, s[44:47], s65 offen offset:1152\n"
    "s_add_u32 s64, s68, 0\n"
    "buffer_load_dword v36, v118, s[44:47], s64 offen offset:2304\n"
    "s_add_u32 s65, s68, 0\n"
    "buffer_load_dword v37, v118, s[44:47], s65 offen offset:3456\n"
    "s_add_u32 s64, s68, 9216\n"
    "buffer_load_dword v38, v118, s[44:47], s64 offen offset:2304\n"
    "s_add_u32 s65, s68, 9216\n"
    "buffer_load_dword v39, v118, s[44:47], s65 offen offset:3456\n"
    "s_add_u32 s64, s68, 36864\n"
    "buffer_load_dword v40, v118, s[44:47], s64 offen\n"
    "s_add_u32 s65, s68, 36864\n"
    "buffer_load_dword v41, v118, s[44:47], s65 offen offset:1152\n"
    "s_add_u32 s64, s68, 46080\n"
    "buffer_load_dword v42, v118, s[44:47], s64 offen\n"
    "s_add_u32 s65, s68, 46080\n"
    "buffer_load_dword v43, v118, s[44:47], s65 offen offset:1152\n"
    "s_add_u32 s64, s68, 36864\n"
    "buffer_load_dword v44, v118, s[44:47], s64 offen offset:2304\n"
    "s_add_u32 s65, s68, 36864\n"
    "buffer_load_dword v45, v118, s[44:47], s65 offen offset:3456\n"
    "s_add_u32 s64, s68, 46080\n"
    "buffer_load_dword v46, v118, s[44:47], s64 offen offset:2304\n"
    "s_add_u32 s65, s68, 46080\n"
    "buffer_load_dword v47, v118, s[44:47], s65 offen offset:3456\n"
    "s_waitcnt vmcnt(62)\n"
    "v_perm_b32 v96, v49, v48, s61\n"
    "s_waitcnt vmcnt(60)\n"
    "v_perm_b32 v97, v51, v50, s61\n"
    "s_waitcnt vmcnt(58)\n"
    "v_perm_b32 v98, v53, v52, s61\n"
    "s_waitcnt vmcnt(56)\n"
    "v_perm_b32 v99, v55, v54, s61\n"
    "s_waitcnt vmcnt(54)\n"
    "v_perm_b32 v100, v57, v56, s61\n"
    "s_waitcnt vmcnt(52)\n"
    "v_perm_b32 v101, v59, v58, s61\n"
    "s_waitcnt vmcnt(50)\n"
    "v_perm_b32 v102, v61, v60, s61\n"
    "s_waitcnt vmcnt(48)\n"
    "v_perm_b32 v103, v63, v62, s61\n"
    "ds_write_b128 v2, v[96:99] offset:18432\n"
    "ds_write_b128 v2, v[100:103] offset:19456\n"
    "v_perm_b32 v104, v49, v48, s62\n"
    "v_perm_b32 v105, v51, v50, s62\n"
    "v_perm_b32 v106, v53, v52, s62\n"
    "v_perm_b32 v107, v55, v54, s62\n"
    "v_perm_b32 v108, v57, v56, s62\n"
    "v_perm_b32 v109, v59, v58, s62\n"
    "v_perm_b32 v110, v61, v60, s62\n"
    "v_perm_b32 v111, v63, v62, s62\n"
    "ds_write_b128 v2, v[104:107] offset:20480\n"
    "ds_write_b128 v2, v[108:111] offset:21504\n"
    "s_add_u32 s64, s68, 73728\n"
    "buffer_load_dword v48, v118, s[44:47], s64 offen\n"
    "s_add_u32 s65, s68, 73728\n"
    "buffer_load_dword v49, v118, s[44:47], s65 offen offset:1152\n"
    "s_add_u32 s64, s68, 82944\n"
    "buffer_load_dword v50, v118, s[44:47], s64 offen\n"
    "s_add_u32 s65, s68, 82944\n"
    "buffer_load_dword v51, v118, s[44:47], s65 offen offset:1152\n"
    "s_add_u32 s64, s68, 73728\n"
    "buffer_load_dword v52, v118, s[44:47], s64 offen offset:2304\n"
    "s_add_u32 s65, s68, 73728\n"
    "buffer_load_dword v53, v118, s[44:47], s65 offen offset:3456\n"
    "s_add_u32 s64, s68, 82944\n"
    "buffer_load_dword v54, v118, s[44:47], s64 offen offset:2304\n"
    "s_add_u32 s65, s68, 82944\n"
    "buffer_load_dword v55, v118, s[44:47], s65 offen offset:3456\n"
    "s_add_u32 s64, s68, 110592\n"
    "buffer_load_dword v56, v118, s[44:47], s64 offen\n"
    "s_add_u32 s65, s68, 110592\n"
    "buffer_load_dword v57, v118, s[44:47], s65 offen offset:1152\n"
    "s_add_u32 s64, s68, 119808\n"
    "buffer_load_dword v58, v118, s[44:47], s64 offen\n"
    "s_add_u32 s65, s68, 119808\n"
    "buffer_load_dword v59, v118, s[44:47], s65 offen offset:1152\n"
    "s_add_u32 s64, s68, 110592\n"
    "buffer_load_dword v60, v118, s[44:47], s64 offen offset:2304\n"
    "s_add_u32 s65, s68, 110592\n"
    "buffer_load_dword v61, v118, s[44:47], s65 offen offset:3456\n"
    "s_add_u32 s64, s68, 119808\n"
    "buffer_load_dword v62, v118, s[44:47], s64 offen offset:2304\n"
    "s_add_u32 s65, s68, 119808\n"
    "buffer_load_dword v63, v118, s[44:47], s65 offen offset:3456\n"
    "s_waitcnt vmcnt(62)\n"
    "v_perm_b32 v96, v65, v64, s61\n"
    "s_waitcnt vmcnt(60)\n"
    "v_perm_b32 v97, v67, v66, s61\n"
    "s_waitcnt vmcnt(58)\n"
    "v_perm_b32 v98, v69, v68, s61\n"
    "s_waitcnt vmcnt(56)\n"
    "v_perm_b32 v99, v71, v70, s61\n"
    "s_waitcnt vmcnt(54)\n"
    "v_perm_b32 v100, v73, v72, s61\n"
    "s_waitcnt vmcnt(52)\n"
    "v_perm_b32 v101, v75, v74, s61\n"
    "s_waitcnt vmcnt(50)\n"
    "v_perm_b32 v102, v77, v76, s61\n"
    "s_waitcnt vmcnt(48)\n"
    "v_perm_b32 v103, v79, v78, s61\n"
    "ds_write_b128 v2, v[96:99] offset:22528\n"
    "ds_write_b128 v2, v[100:103] offset:23552\n"
    "v_perm_b32 v104, v65, v64, s62\n"
    "v_perm_b32 v105, v67, v66, s62\n"
    "v_perm_b32 v106, v69, v68, s62\n"
    "v_perm_b32 v107, v71, v70, s62\n"
    "v_perm_b32 v108, v73, v72, s62\n"
    "v_perm_b32 v109, v75, v74, s62\n"
    "v_perm_b32 v110, v77, v76, s62\n"
    "v_perm_b32 v111, v79, v78, s62\n"
    "ds_write_b128 v2, v[104:107] offset:24576\n"
    "ds_write_b128 v2, v[108:111] offset:25600\n"
    "s_mov_b64 s[44:45], %2\n"
    "s_and_b32 s45, s45, 0xffff\n"
    "s_mov_b32 s46, 0x480000\n"
    "s_mov_b32 s47, 0x20000\n"
    "s_add_u32 s64, s60, 0\n"
    "buffer_load_dword v64, v117, s[44:47], s64 offen\n"
    "s_add_u32 s65, s60, 16384\n"
    "buffer_load_dword v65, v117, s[44:47], s65 offen\n"
    "s_add_u32 s64, s60, 131072\n"
    "buffer_load_dword v66, v117, s[44:47], s64 offen\n"
    "s_add_u32 s65, s60, 147456\n"
    "buffer_load_dword v67, v117, s[44:47], s65 offen\n"
    "s_add_u32 s64, s60, 32768\n"
    "buffer_load_dword v68, v117, s[44:47], s64 offen\n"
    "s_add_u32 s65, s60, 49152\n"
    "buffer_load_dword v69, v117, s[44:47], s65 offen\n"
    "s_add_u32 s64, s60, 163840\n"
    "buffer_load_dword v70, v117, s[44:47], s64 offen\n"
    "s_add_u32 s65, s60, 180224\n"
    "buffer_load_dword v71, v117, s[44:47], s65 offen\n"
    "s_add_u32 s64, s60, 262144\n"
    "buffer_load_dword v72, v117, s[44:47], s64 offen\n"
    "s_add_u32 s65, s60, 278528\n"
    "buffer_load_dword v73, v117, s[44:47], s65 offen\n"
    "s_add_u32 s64, s60, 393216\n"
    "buffer_load_dword v74, v117, s[44:47], s64 offen\n"
    "s_add_u32 s65, s60, 409600\n"
    "buffer_load_dword v75, v117, s[44:47], s65 offen\n"
    "s_add_u32 s64, s60, 294912\n"
    "buffer_load_dword v76, v117, s[44:47], s64 offen\n"
    "s_add_u32 s65, s60, 311296\n"
    "buffer_load_dword v77, v117, s[44:47], s65 offen\n"
    "s_add_u32 s64, s60, 425984\n"
    "buffer_load_dword v78, v117, s[44:47], s64 offen\n"
    "s_add_u32 s65, s60, 442368\n"
    "buffer_load_dword v79, v117, s[44:47], s65 offen\n"
    "s_waitcnt vmcnt(62)\n"
    "v_perm_b32 v96, v81, v80, s61\n"
    "s_waitcnt vmcnt(60)\n"
    "v_perm_b32 v97, v83, v82, s61\n"
    "s_waitcnt vmcnt(58)\n"
    "v_perm_b32 v98, v85, v84, s61\n"
    "s_waitcnt vmcnt(56)\n"
    "v_perm_b32 v99, v87, v86, s61\n"
    "s_waitcnt vmcnt(54)\n"
    "v_perm_b32 v100, v89, v88, s61\n"
    "s_waitcnt vmcnt(52)\n"
    "v_perm_b32 v101, v91, v90, s61\n"
    "s_waitcnt vmcnt(50)\n"
    "v_perm_b32 v102, v93, v92, s61\n"
    "s_waitcnt vmcnt(48)\n"
    "v_perm_b32 v103, v95, v94, s61\n"
    "ds_write_b128 v2, v[96:99] offset:26624\n"
    "ds_write_b128 v2, v[100:103] offset:27648\n"
    "v_perm_b32 v104, v81, v80, s62\n"
    "v_perm_b32 v105, v83, v82, s62\n"
    "v_perm_b32 v106, v85, v84, s62\n"
    "v_perm_b32 v107, v87, v86, s62\n"
    "v_perm_b32 v108, v89, v88, s62\n"
    "v_perm_b32 v109, v91, v90, s62\n"
    "v_perm_b32 v110, v93, v92, s62\n"
    "v_perm_b32 v111, v95, v94, s62\n"
    "ds_write_b128 v2, v[104:107] offset:28672\n"
    "ds_write_b128 v2, v[108:111] offset:29696\n"
    "s_add_u32 s64, s60, 524288\n"
    "buffer_load_dword v80, v117, s[44:47], s64 offen\n"
    "s_add_u32 s65, s60, 540672\n"
    "buffer_load_dword v81, v117, s[44:47], s65 offen\n"
    "s_add_u32 s64, s60, 655360\n"
    "buffer_load_dword v82, v117, s[44:47], s64 offen\n"
    "s_add_u32 s65, s60, 671744\n"
    "buffer_load_dword v83, v117, s[44:47], s65 offen\n"
    "s_add_u32 s64, s60, 557056\n"
    "buffer_load_dword v84, v117, s[44:47], s64 offen\n"
    "s_add_u32 s65, s60, 573440\n"
    "buffer_load_dword v85, v117, s[44:47], s65 offen\n"
    "s_add_u32 s64, s60, 688128\n"
    "buffer_load_dword v86, v117, s[44:47], s64 offen\n"
    "s_add_u32 s65, s60, 704512\n"
    "buffer_load_dword v87, v117, s[44:47], s65 offen\n"
    "s_add_u32 s64, s60, 786432\n"
    "buffer_load_dword v88, v117, s[44:47], s64 offen\n"
    "s_add_u32 s65, s60, 802816\n"
    "buffer_load_dword v89, v117, s[44:47], s65 offen\n"
    "s_add_u32 s64, s60, 917504\n"
    "buffer_load_dword v90, v117, s[44:47], s64 offen\n"
    "s_add_u32 s65, s60, 933888\n"
    "buffer_load_dword v91, v117, s[44:47], s65 offen\n"
    "s_add_u32 s64, s60, 819200\n"
    "buffer_load_dword v92, v117, s[44:47], s64 offen\n"
    "s_add_u32 s65, s60, 835584\n"
    "buffer_load_dword v93, v117, s[44:47], s65 offen\n"
    "s_add_u32 s64, s60, 950272\n"
    "buffer_load_dword v94, v117, s[44:47], s64 offen\n"
    "s_add_u32 s65, s60, 966656\n"
    "buffer_load_dword v95, v117, s[44:47], s65 offen\n"
    "s_waitcnt vmcnt(62)\n"
    "v_perm_b32 v96, v33, v32, s63\n"
    "s_waitcnt vmcnt(60)\n"
    "v_perm_b32 v97, v35, v34, s63\n"
    "s_waitcnt vmcnt(58)\n"
    "v_perm_b32 v98, v37, v36, s63\n"
    "s_waitcnt vmcnt(56)\n"
    "v_perm_b32 v99, v39, v38, s63\n"
    "s_waitcnt vmcnt(54)\n"
    "v_perm_b32 v100, v41, v40, s63\n"
    "s_waitcnt vmcnt(52)\n"
    "v_perm_b32 v101, v43, v42, s63\n"
    "s_waitcnt vmcnt(50)\n"
    "v_perm_b32 v102, v45, v44, s63\n"
    "s_waitcnt vmcnt(48)\n"
    "v_perm_b32 v103, v47, v46, s63\n"
    "s_cmp_lt_u32 %6, 2\n"
    "s_cbranch_scc1 .Lsk4_%=\n"
    "ds_write_b128 v2, v[96:99] offset:30720\n"
    "ds_write_b128 v2, v[100:103] offset:31744\n"
    ".Lsk4_%=:\n"
    "s_add_u32 s64, s60, 1048576\n"
    "buffer_load_dword v32, v117, s[44:47], s64 offen\n"
    "s_add_u32 s65, s60, 1064960\n"
    "buffer_load_dword v33, v117, s[44:47], s65 offen\n"
    "s_add_u32 s64, s60, 1179648\n"
    "buffer_load_dword v34, v117, s[44:47], s64 offen\n"
    "s_add_u32 s65, s60, 1196032\n"
    "buffer_load_dword v35, v117, s[44:47], s65 offen\n"
    "s_add_u32 s64, s60, 1081344\n"
    "buffer_load_dword v36, v117, s[44:47], s64 offen\n"
    "s_add_u32 s65, s60, 1097728\n"
    "buffer_load_dword v37, v117, s[44:47], s65 offen\n"
    "s_add_u32 s64, s60, 1212416\n"
    "buffer_load_dword v38, v117, s[44:47], s64 offen\n"
    "s_add_u32 s65, s60, 1228800\n"
    "buffer_load_dword v39, v117, s[44:47], s65 offen\n"
    "s_add_u32 s64, s60, 1310720\n"
    "buffer_load_dword v40, v117, s[44:47], s64 offen\n"
    "s_add_u32 s65, s60, 1327104\n"
    "buffer_load_dword v41, v117, s[44:47], s65 offen\n"
    "s_add_u32 s64, s60, 1441792\n"
    "buffer_load_dword v42, v117, s[44:47], s64 offen\n"
    "s_add_u32 s65, s60, 1458176\n"
    "buffer_load_dword v43, v117, s[44:47], s65 offen\n"
    "s_add_u32 s64, s60, 1343488\n"
    "buffer_load_dword v44, v117, s[44:47], s64 offen\n"
    "s_add_u32 s65, s60, 1359872\n"
    "buffer_load_dword v45, v117, s[44:47], s65 offen\n"
    "s_add_u32 s64, s60, 1474560\n"
    "buffer_load_dword v46, v117, s[44:47], s64 offen\n"
    "s_add_u32 s65, s60, 1490944\n"
    "buffer_load_dword v47, v117, s[44:47], s65 offen\n"
    "s_waitcnt vmcnt(62)\n"
    "v_perm_b32 v96, v49, v48, s63\n"
    "s_waitcnt vmcnt(60)\n"
    "v_perm_b32 v97, v51, v50, s63\n"
    "s_waitcnt vmcnt(58)\n"
    "v_perm_b32 v98, v53, v52, s63\n"
    "s_waitcnt vmcnt(56)\n"
    "v_perm_b32 v99, v55, v54, s63\n"
    "s_waitcnt vmcnt(54)\n"
    "v_perm_b32 v100, v57, v56, s63\n"
    "s_waitcnt vmcnt(52)\n"
    "v_perm_b32 v101, v59, v58, s63\n"
    "s_waitcnt vmcnt(50)\n"
    "v_perm_b32 v102, v61, v60, s63\n"
    "s_waitcnt vmcnt(48)\n"
    "v_perm_b32 v103, v63, v62, s63\n"
    "s_cmp_lt_u32 %6, 2\n"
    "s_cbranch_scc1 .Lsk5_%=\n"
    "ds_write_b128 v2, v[96:99] offset:32768\n"
    "ds_write_b128 v2, v[100:103] offset:33792\n"
    ".Lsk5_%=:\n"
    "s_add_u32 s64, s60, 1572864\n"
    "buffer_load_dword v48, v117, s[44:47], s64 offen\n"
    "s_add_u32 s65, s60, 1589248\n"
    "buffer_load_dword v49, v117, s[44:47], s65 offen\n"
    "s_add_u32 s64, s60, 1703936\n"
    "buffer_load_dword v50, v117, s[44:47], s64 offen\n"
    "s_add_u32 s65, s60, 1720320\n"
    "buffer_load_dword v51, v117, s[44:47], s65 offen\n"
    "s_add_u32 s64, s60, 1605632\n"
    "buffer_load_dword v52, v117, s[44:47], s64 offen\n"
    "s_add_u32 s65, s60, 1622016\n"
    "buffer_load_dword v53, v117, s[44:47], s65 offen\n"
    "s_add_u32 s64, s60, 1736704\n"
    "buffer_load_dword v54, v117, s[44:47], s64 offen\n"
    "s_add_u32 s65, s60, 1753088\n"
    "buffer_load_dword v55, v117, s[44:47], s65 offen\n"
    "s_add_u32 s64, s60, 1835008\n"
    "buffer_load_dword v56, v117, s[44:47], s64 offen\n"
    "s_add_u32 s65, s60, 1851392\n"
    "buffer_load_dword v57, v117, s[44:47], s65 offen\n"
    "s_add_u32 s64, s60, 1966080\n"
    "buffer_load_dword v58, v117, s[44:47], s64 offen\n"
    "s_add_u32 s65, s60, 1982464\n"
    "buffer_load_dword v59, v117, s[44:47], s65 offen\n"
    "s_add_u32 s64, s60, 1867776\n"
    "buffer_load_dword v60, v117, s[44:47], s64 offen\n"
    "s_add_u32 s65, s60, 1884160\n"
    "buffer_load_dword v61, v117, s[44:47], s65 offen\n"
    "s_add_u32 s64, s60, 1998848\n"
    "buffer_load_dword v62, v117, s[44:47], s64 offen\n"
    "s_add_u32 s65, s60, 2015232\n"
    "buffer_load_dword v63, v117, s[44:47], s65 offen\n"
    "s_waitcnt vmcnt(62)\n"
    "v_perm_b32 v96, v65, v64, s61\n"
    "s_waitcnt vmcnt(60)\n"
    "v_perm_b32 v97, v67, v66, s61\n"
    "s_waitcnt vmcnt(58)\n"
    "v_perm_b32 v98, v69, v68, s61\n"
    "s_waitcnt vmcnt(56)\n"
    "v_perm_b32 v99, v71, v70, s61\n"
    "s_waitcnt vmcnt(54)\n"
    "v_perm_b32 v100, v73, v72, s61\n"
    "s_waitcnt vmcnt(52)\n"
    "v_perm_b32 v101, v75, v74, s61\n"
    "s_waitcnt vmcnt(50)\n"
    "v_perm_b32 v102, v77, v76, s61\n"
    "s_waitcnt vmcnt(48)\n"
    "v_perm_b32 v103, v79, v78, s61\n"
    "ds_write_b128 v2, v[96:99]\n"
    "ds_write_b128 v2, v[100:103] offset:1024\n"
    "v_perm_b32 v104, v65, v64, s62\n"
    "v_perm_b32 v105, v67, v66, s62\n"
    "v_perm_b32 v106, v69, v68, s62\n"
    "v_perm_b32 v107, v71, v70, s62\n"
    "v_perm_b32 v108, v73, v72, s62\n"
    "v_perm_b32 v109, v75, v74, s62\n"
    "v_perm_b32 v110, v77, v76, s62\n"
    "v_perm_b32 v111, v79, v78, s62\n"
    "ds_write_b128 v2, v[104:107] offset:2048\n"
    "ds_write_b128 v2, v[108:111] offset:3072\n"
    "s_add_u32 s64, s60, 2097152\n"
    "buffer_load_dword v64, v117, s[44:47], s64 offen\n"
    "s_add_u32 s65, s60, 2113536\n"
    "buffer_load_dword v65, v117, s[44:47], s65 offen\n"
    "s_add_u32 s64, s60, 2228224\n"
    "buffer_load_dword v66, v117, s[44:47], s64 offen\n"
    "s_add_u32 s65, s60, 2244608\n"
    "buffer_load_dword v67, v117, s[44:47], s65 offen\n"
    "s_add_u32 s64, s60, 2129920\n"
    "buffer_load_dword v68, v117, s[44:47], s64 offen\n"
    "s_add_u32 s65, s60, 2146304\n"
    "buffer_load_dword v69, v117, s[44:47], s65 offen\n"
    "s_add_u32 s64, s60, 2260992\n"
    "buffer_load_dword v70, v117, s[44:47], s64 offen\n"
    "s_add_u32 s65, s60, 2277376\n"
    "buffer_load_dword v71, v117, s[44:47], s65 offen\n"
    "s_add_u32 s64, s60, 2359296\n"
    "buffer_load_dword v72, v117, s[44:47], s64 offen\n"
    "s_add_u32 s65, s60, 2375680\n"
    "buffer_load_dword v73, v117, s[44:47], s65 offen\n"
    "s_add_u32 s64, s60, 2490368\n"
    "buffer_load_dword v74, v117, s[44:47], s64 offen\n"
    "s_add_u32 s65, s60, 2506752\n"
    "buffer_load_dword v75, v117, s[44:47], s65 offen\n"
    "s_add_u32 s64, s60, 2392064\n"
    "buffer_load_dword v76, v117, s[44:47], s64 offen\n"
    "s_add_u32 s65, s60, 2408448\n"
    "buffer_load_dword v77, v117, s[44:47], s65 offen\n"
    "s_add_u32 s64, s60, 2523136\n"
    "buffer_load_dword v78, v117, s[44:47], s64 offen\n"
    "s_add_u32 s65, s60, 2539520\n"
    "buffer_load_dword v79, v117, s[44:47], s65 offen\n"
    "s_waitcnt vmcnt(62)\n"
    "v_perm_b32 v96, v81, v80, s61\n"
    "s_waitcnt vmcnt(60)\n"
    "v_perm_b32 v97, v83, v82, s61\n"
    "s_waitcnt vmcnt(58)\n"
    "v_perm_b32 v98, v85, v84, s61\n"
    "s_waitcnt vmcnt(56)\n"
    "v_perm_b32 v99, v87, v86, s61\n"
    "s_waitcnt vmcnt(54)\n"
    "v_perm_b32 v100, v89, v88, s61\n"
    "s_waitcnt vmcnt(52)\n"
    "v_perm_b32 v101, v91, v90, s61\n"
    "s_waitcnt vmcnt(50)\n"
    "v_perm_b32 v102, v93, v92, s61\n"
    "s_waitcnt vmcnt(48)\n"
    "v_perm_b32 v103, v95, v94, s61\n"
    "ds_write_b128 v2, v[96:99] offset:4096\n"
    "ds_write_b128 v2, v[100:103] offset:5120\n"
    "v_perm_b32 v104, v81, v80, s62\n"
    "v_perm_b32 v105, v83, v82, s62\n"
    "v_perm_b32 v106, v85, v84, s62\n"
    "v_perm_b32 v107, v87, v86, s62\n"
    "v_perm_b32 v108, v89, v88, s62\n"
    "v_perm_b32 v109, v91, v90, s62\n"
    "v_perm_b32 v110, v93, v92, s62\n"
    "v_perm_b32 v111, v95, v94, s62\n"
    "ds_write_b128 v2, v[104:107] offset:6144\n"
    "ds_write_b128 v2, v[108:111] offset:7168\n"
    "s_add_u32 s64, s60, 2621440\n"
    "buffer_load_dword v80, v117, s[44:47], s64 offen\n"
    "s_add_u32 s65, s60, 2637824\n"
    "buffer_load_dword v81, v117, s[44:47], s65 offen\n"
    "s_add_u32 s64, s60, 2752512\n"
    "buffer_load_dword v82, v117, s[44:47], s64 offen\n"
    "s_add_u32 s65, s60, 2768896\n"
    "buffer_load_dword v83, v117, s[44:47], s65 offen\n"
    "s_add_u32 s64, s60, 2654208\n"
    "buffer_load_dword v84, v117, s[44:47], s64 offen\n"
    "s_add_u32 s65, s60, 2670592\n"
    "buffer_load_dword v85, v117, s[44:47], s65 offen\n"
    "s_add_u32 s64, s60, 2785280\n"
    "buffer_load_dword v86, v117, s[44:47], s64 offen\n"
    "s_add_u32 s65, s60, 2801664\n"
    "buffer_load_dword v87, v117, s[44:47], s65 offen\n"
    "s_add_u32 s64, s60, 2883584\n"
    "buffer_load_dword v88, v117, s[44:47], s64 offen\n"
    "s_add_u32 s65, s60, 2899968\n"
    "buffer_load_dword v89, v117, s[44:47], s65 offen\n"
    "s_add_u32 s64, s60, 3014656\n"
    "buffer_load_dword v90, v117, s[44:47], s64 offen\n"
    "s_add_u32 s65, s60, 3031040\n"
    "buffer_load_dword v91, v117, s[44:47], s65 offen\n"
    "s_add_u32 s64, s60, 2916352\n"
    "buffer_load_dword v92, v117, s[44:47], s64 offen\n"
    "s_add_u32 s65, s60, 2932736\n"
    "buffer_load_dword v93, v117, s[44:47], s65 offen\n"
    "s_add_u32 s64, s60, 3047424\n"
    "buffer_load_dword v94, v117, s[44:47], s64 offen\n"
    "s_add_u32 s65, s60, 3063808\n"
    "buffer_load_dword v95, v117, s[44:47], s65 offen\n"
    "s_waitcnt vmcnt(62)\n"
    "v_perm_b32 v96, v33, v32, s61\n"
    "s_waitcnt vmcnt(60)\n"
    "v_perm_b32 v97, v35, v34, s61\n"
    "s_waitcnt vmcnt(58)\n"
    "v_perm_b32 v98, v37, v36, s61\n"
    "s_waitcnt vmcnt(56)\n"
    "v_perm_b32 v99, v39, v38, s61\n"
    "s_waitcnt vmcnt(54)\n"
    "v_perm_b32 v100, v41, v40, s61\n"
    "s_waitcnt vmcnt(52)\n"
    "v_perm_b32 v101, v43, v42, s61\n"
    "s_waitcnt vmcnt(50)\n"
    "v_perm_b32 v102, v45, v44, s61\n"
    "s_waitcnt vmcnt(48)\n"
    "v_perm_b32 v103, v47, v46, s61\n"
    "ds_write_b128 v2, v[96:99] offset:8192\n"
    "ds_write_b128 v2, v[100:103] offset:9216\n"
    "v_perm_b32 v104, v33, v32, s62\n"
    "v_perm_b32 v105, v35, v34, s62\n"
    "v_perm_b32 v106, v37, v36, s62\n"
    "v_perm_b32 v107, v39, v38, s62\n"
    "v_perm_b32 v108, v41, v40, s62\n"
    "v_perm_b32 v109, v43, v42, s62\n"
    "v_perm_b32 v110, v45, v44, s62\n"
    "v_perm_b32 v111, v47, v46, s62\n"
    "ds_write_b128 v2, v[104:107] offset:10240\n"
    "ds_write_b128 v2, v[108:111] offset:11264\n"
    "s_add_u32 s64, s60, 3145728\n"
    "buffer_load_dword v32, v117, s[44:47], s64 offen\n"
    "s_add_u32 s65, s60, 3162112\n"
    "buffer_load_dword v33, v117, s[44:47], s65 offen\n"
    "s_add_u32 s64, s60, 3276800\n"
    "buffer_load_dword v34, v117, s[44:47], s64 offen\n"
    "s_add_u32 s65, s60, 3293184\n"
    "buffer_load_dword v35, v117, s[44:47], s65 offen\n"
    "s_add_u32 s64, s60, 3178496\n"
    "buffer_load_dword v36, v117, s[44:47], s64 offen\n"
    "s_add_u32 s65, s60, 3194880\n"
    "buffer_load_dword v37, v117, s[44:47], s65 offen\n"
    "s_add_u32 s64, s60, 3309568\n"
    "buffer_load_dword v38, v117, s[44:47], s64 offen\n"
    "s_add_u32 s65, s60, 3325952\n"
    "buffer_load_dword v39, v117, s[44:47], s65 offen\n"
    "s_add_u32 s64, s60, 3407872\n"
    "buffer_load_dword v40, v117, s[44:47], s64 offen\n"
    "s_add_u32 s65, s60, 3424256\n"
    "buffer_load_dword v41, v117, s[44:47], s65 offen\n"
    "s_add_u32 s64, s60, 3538944\n"
    "buffer_load_dword v42, v117, s[44:47], s64 offen\n"
    "s_add_u32 s65, s60, 3555328\n"
    "buffer_load_dword v43, v117, s[44:47], s65 offen\n"
    "s_add_u32 s64, s60, 3440640\n"
    "buffer_load_dword v44, v117, s[44:47], s64 offen\n"
    "s_add_u32 s65, s60, 3457024\n"
    "buffer_load_dword v45, v117, s[44:47], s65 offen\n"
    "s_add_u32 s64, s60, 3571712\n"
    "buffer_load_dword v46, v117, s[44:47], s64 offen\n"
    "s_add_u32 s65, s60, 3588096\n"
    "buffer_load_dword v47, v117, s[44:47], s65 offen\n"
    "s_waitcnt vmcnt(62)\n"
    "v_perm_b32 v96, v49, v48, s61\n"
    "s_waitcnt vmcnt(60)\n"
    "v_perm_b32 v97, v51, v50, s61\n"
    "s_waitcnt vmcnt(58)\n"
    "v_perm_b32 v98, v53, v52, s61\n"
    "s_waitcnt vmcnt(56)\n"
    "v_perm_b32 v99, v55, v54, s61\n"
    "s_waitcnt vmcnt(54)\n"
    "v_perm_b32 v100, v57, v56, s61\n"
    "s_waitcnt vmcnt(52)\n"
    "v_perm_b32 v101, v59, v58, s61\n"
    "s_waitcnt vmcnt(50)\n"
    "v_perm_b32 v102, v61, v60, s61\n"
    "s_waitcnt vmcnt(48)\n"
    "v_perm_b32 v103, v63, v62, s61\n"
    "ds_write_b128 v2, v[96:99] offset:12288\n"
    "ds_write_b128 v2, v[100:103] offset:13312\n"
    "v_perm_b32 v136, v49, v48, s62\n"
    "v_perm_b32 v137, v51, v50, s62\n"
    "v_perm_b32 v138, v53, v52, s62\n"
    "v_perm_b32 v139, v55, v54, s62\n"
    "v_perm_b32 v140, v57, v56, s62\n"
    "v_perm_b32 v141, v59, v58, s62\n"
    "v_perm_b32 v142, v61, v60, s62\n"
    "v_perm_b32 v143, v63, v62, s62\n"
    "s_add_u32 s64, s60, 3670016\n"
    "buffer_load_dword v48, v117, s[44:47], s64 offen\n"
    "s_add_u32 s65, s60, 3686400\n"
    "buffer_load_dword v49, v117, s[44:47], s65 offen\n"
    "s_add_u32 s64, s60, 3801088\n"
    "buffer_load_dword v50, v117, s[44:47], s64 offen\n"
    "s_add_u32 s65, s60, 3817472\n"
    "buffer_load_dword v51, v117, s[44:47], s65 offen\n"
    "s_add_u32 s64, s60, 3702784\n"
    "buffer_load_dword v52, v117, s[44:47], s64 offen\n"
    "s_add_u32 s65, s60, 3719168\n"
    "buffer_load_dword v53, v117, s[44:47], s65 offen\n"
    "s_add_u32 s64, s60, 3833856\n"
    "buffer_load_dword v54, v117, s[44:47], s64 offen\n"
    "s_add_u32 s65, s60, 3850240\n"
    "buffer_load_dword v55, v117, s[44:47], s65 offen\n"
    "s_add_u32 s64, s60, 3932160\n"
    "buffer_load_dword v56, v117, s[44:47], s64 offen\n"
    "s_add_u32 s65, s60, 3948544\n"
    "buffer_load_dword v57, v117, s[44:47], s65 offen\n"
    "s_add_u32 s64, s60, 4063232\n"
    "buffer_load_dword v58, v117, s[44:47], s64 offen\n"
    "s_add_u32 s65, s60, 4079616\n"
    "buffer_load_dword v59, v117, s[44:47], s65 offen\n"
    "s_add_u32 s64, s60, 3964928\n"
    "buffer_load_dword v60, v117, s[44:47], s64 offen\n"
    "s_add_u32 s65, s60, 3981312\n"
    "buffer_load_dword v61, v117, s[44:47], s65 offen\n"
    "s_add_u32 s64, s60, 4096000\n"
    "buffer_load_dword v62, v117, s[44:47], s64 offen\n"
    "s_add_u32 s65, s60, 4112384\n"
    "buffer_load_dword v63, v117, s[44:47], s65 offen\n"
    "s_waitcnt vmcnt(62)\n"
    "v_perm_b32 v144, v65, v64, s61\n"
    "s_waitcnt vmcnt(60)\n"
    "v_perm_b32 v145, v67, v66, s61\n"
    "s_waitcnt vmcnt(58)\n"
    "v_perm_b32 v146, v69, v68, s61\n"
    "s_waitcnt vmcnt(56)\n"
    "v_perm_b32 v147, v71, v70, s61\n"
    "s_waitcnt vmcnt(54)\n"
    "v_perm_b32 v148, v73, v72, s61\n"
    "s_waitcnt vmcnt(52)\n"
    "v_perm_b32 v149, v75, v74, s61\n"
    "s_waitcnt vmcnt(50)\n"
    "v_perm_b32 v150, v77, v76, s61\n"
    "s_waitcnt vmcnt(48)\n"
    "v_perm_b32 v151, v79, v78, s61\n"
    "v_perm_b32 v152, v65, v64, s62\n"
    "v_perm_b32 v153, v67, v66, s62\n"
    "v_perm_b32 v154, v69, v68, s62\n"
    "v_perm_b32 v155, v71, v70, s62\n"
    "v_perm_b32 v156, v73, v72, s62\n"
    "v_perm_b32 v157, v75, v74, s62\n"
    "v_perm_b32 v158, v77, v76, s62\n"
    "v_perm_b32 v159, v79, v78, s62\n"
    "s_add_u32 s64, s60, 4194304\n"
    "buffer_load_dword v64, v117, s[44:47], s64 offen\n"
    "s_add_u32 s65, s60, 4210688\n"
    "buffer_load_dword v65, v117, s[44:47], s65 offen\n"
    "s_add_u32 s64, s60, 4325376\n"
    "buffer_load_dword v66, v117, s[44:47], s64 offen\n"
    "s_add_u32 s65, s60, 4341760\n"
    "buffer_load_dword v67, v117, s[44:47], s65 offen\n"
    "s_add_u32 s64, s60, 4227072\n"
    "buffer_load_dword v68, v117, s[44:47], s64 offen\n"
    "s_add_u32 s65, s60, 4243456\n"
    "buffer_load_dword v69, v117, s[44:47], s65 offen\n"
    "s_add_u32 s64, s60, 4358144\n"
    "buffer_load_dword v70, v117, s[44:47], s64 offen\n"
    "s_add_u32 s65, s60, 4374528\n"
    "buffer_load_dword v71, v117, s[44:47], s65 offen\n"
    "s_add_u32 s64, s60, 4456448\n"
    "buffer_load_dword v72, v117, s[44:47], s64 offen\n"
    "s_add_u32 s65, s60, 4472832\n"
    "buffer_load_dword v73, v117, s[44:47], s65 offen\n"
    "s_add_u32 s64, s60, 4587520\n"
    "buffer_load_dword v74, v117, s[44:47], s64 offen\n"
    "s_add_u32 s65, s60, 4603904\n"
    "buffer_load_dword v75, v117, s[44:47], s65 offen\n"
    "s_add_u32 s64, s60, 4489216\n"
    "buffer_load_dword v76, v117, s[44:47], s64 offen\n"
    "s_add_u32 s65, s60, 4505600\n"
    "buffer_load_dword v77, v117, s[44:47], s65 offen\n"
    "s_add_u32 s64, s60, 4620288\n"
    "buffer_load_dword v78, v117, s[44:47], s64 offen\n"
    "s_add_u32 s65, s60, 4636672\n"
    "buffer_load_dword v79, v117, s[44:47], s65 offen\n"
    "s_waitcnt vmcnt(62)\n"
    "v_perm_b32 v160, v81, v80, s61\n"
    "s_waitcnt vmcnt(60)\n"
    "v_perm_b32 v161, v83, v82, s61\n"
    "s_waitcnt vmcnt(58)\n"
    "v_perm_b32 v162, v85, v84, s61\n"
    "s_waitcnt vmcnt(56)\n"
    "v_perm_b32 v163, v87, v86, s61\n"
    "s_waitcnt vmcnt(54)\n"
    "v_perm_b32 v164, v89, v88, s61\n"
    "s_waitcnt vmcnt(52)\n"
    "v_perm_b32 v165, v91, v90, s61\n"
    "s_waitcnt vmcnt(50)\n"
    "v_perm_b32 v166, v93, v92, s61\n"
    "s_waitcnt vmcnt(48)\n"
    "v_perm_b32 v167, v95, v94, s61\n"
    "v_perm_b32 v168, v81, v80, s62\n"
    "v_perm_b32 v169, v83, v82, s62\n"
    "v_perm_b32 v170, v85, v84, s62\n"
    "v_perm_b32 v171, v87, v86, s62\n"
    "v_perm_b32 v172, v89, v88, s62\n"
    "v_perm_b32 v173, v91, v90, s62\n"
    "v_perm_b32 v174, v93, v92, s62\n"
    "v_perm_b32 v175, v95, v94, s62\n"
    "s_mov_b64 s[44:45], %3\n"
    "s_and_b32 s45, s45, 0xffff\n"
    "s_mov_b32 s46, 0x480000\n"
    "s_mov_b32 s47, 0x20000\n"
    "s_add_u32 s64, s60, 0\n"
    "buffer_load_dword v80, v117, s[44:47], s64 offen\n"
    "s_add_u32 s65, s60, 16384\n"
    "buffer_load_dword v81, v117, s[44:47], s65 offen\n"
    "s_add_u32 s64, s60, 131072\n"
    "buffer_load_dword v82, v117, s[44:47], s64 offen\n"
    "s_add_u32 s65, s60, 147456\n"
    "buffer_load_dword v83, v117, s[44:47], s65 offen\n"
    "s_add_u32 s64, s60, 32768\n"
    "buffer_load_dword v84, v117, s[44:47], s64 offen\n"
    "s_add_u32 s65, s60, 49152\n"
    "buffer_load_dword v85, v117, s[44:47], s65 offen\n"
    "s_add_u32 s64, s60, 163840\n"
    "buffer_load_dword v86, v117, s[44:47], s64 offen\n"
    "s_add_u32 s65, s60, 180224\n"
    "buffer_load_dword v87, v117, s[44:47], s65 offen\n"
    "s_add_u32 s64, s60, 262144\n"
    "buffer_load_dword v88, v117, s[44:47], s64 offen\n"
    "s_add_u32 s65, s60, 278528\n"
    "buffer_load_dword v89, v117, s[44:47], s65 offen\n"
    "s_add_u32 s64, s60, 393216\n"
    "buffer_load_dword v90, v117, s[44:47], s64 offen\n"
    "s_add_u32 s65, s60, 409600\n"
    "buffer_load_dword v91, v117, s[44:47], s65 offen\n"
    "s_add_u32 s64, s60, 294912\n"
    "buffer_load_dword v92, v117, s[44:47], s64 offen\n"
    "s_add_u32 s65, s60, 311296\n"
    "buffer_load_dword v93, v117, s[44:47], s65 offen\n"
    "s_add_u32 s64, s60, 425984\n"
    "buffer_load_dword v94, v117, s[44:47], s64 offen\n"
    "s_add_u32 s65, s60, 442368\n"
    "buffer_load_dword v95, v117, s[44:47], s65 offen\n"
    "s_waitcnt vmcnt(62)\n"
    "v_perm_b32 v176, v33, v32, s61\n"
    "s_waitcnt vmcnt(60)\n"
    "v_perm_b32 v177, v35, v34, s61\n"
    "s_waitcnt vmcnt(58)\n"
    "v_perm_b32 v178, v37, v36, s61\n"
    "s_waitcnt vmcnt(56)\n"
    "v_perm_b32 v179, v39, v38, s61\n"
    "s_waitcnt vmcnt(54)\n"
    "v_perm_b32 v180, v41, v40, s61\n"
    "s_waitcnt vmcnt(52)\n"
    "v_perm_b32 v181, v43, v42, s61\n"
    "s_waitcnt vmcnt(50)\n"
    "v_perm_b32 v182, v45, v44, s61\n"
    "s_waitcnt vmcnt(48)\n"
    "v_perm_b32 v183, v47, v46, s61\n"
    "v_perm_b32 v184, v33, v32, s62\n"
    "v_perm_b32 v185, v35, v34, s62\n"
    "v_perm_b32 v186, v37, v36, s62\n"
    "v_perm_b32 v187, v39, v38, s62\n"
    "v_perm_b32 v188, v41, v40, s62\n"
    "v_perm_b32 v189, v43, v42, s62\n"
    "v_perm_b32 v190, v45, v44, s62\n"
    "v_perm_b32 v191, v47, v46, s62\n"
    "s_add_u32 s64, s60, 524288\n"
    "buffer_load_dword v32, v117, s[44:47], s64 offen\n"
    "s_add_u32 s65, s60, 540672\n"
    "buffer_load_dword v33, v117, s[44:47], s65 offen\n"
    "s_add_u32 s64, s60, 655360\n"
    "buffer_load_dword v34, v117, s[44:47], s64 offen\n"
    "s_add_u32 s65, s60, 671744\n"
    "buffer_load_dword v35, v117, s[44:47], s65 offen\n"
    "s_add_u32 s64, s60, 557056\n"
    "buffer_load_dword v36, v117, s[44:47], s64 offen\n"
    "s_add_u32 s65, s60, 573440\n"
    "buffer_load_dword v37, v117, s[44:47], s65 offen\n"
    "s_add_u32 s64, s60, 688128\n"
    "buffer_load_dword v38, v117, s[44:47], s64 offen\n"
    "s_add_u32 s65, s60, 704512\n"
    "buffer_load_dword v39, v117, s[44:47], s65 offen\n"
    "s_add_u32 s64, s60, 786432\n"
    "buffer_load_dword v40, v117, s[44:47], s64 offen\n"
    "s_add_u32 s65, s60, 802816\n"
    "buffer_load_dword v41, v117, s[44:47], s65 offen\n"
    "s_add_u32 s64, s60, 917504\n"
    "buffer_load_dword v42, v117, s[44:47], s64 offen\n"
    "s_add_u32 s65, s60, 933888\n"
    "buffer_load_dword v43, v117, s[44:47], s65 offen\n"
    "s_add_u32 s64, s60, 819200\n"
    "buffer_load_dword v44, v117, s[44:47], s64 offen\n"
    "s_add_u32 s65, s60, 835584\n"
    "buffer_load_dword v45, v117, s[44:47], s65 offen\n"
    "s_add_u32 s64, s60, 950272\n"
    "buffer_load_dword v46, v117, s[44:47], s64 offen\n"
    "s_add_u32 s65, s60, 966656\n"
    "buffer_load_dword v47, v117, s[44:47], s65 offen\n"
    "s_waitcnt vmcnt(62)\n"
    "v_perm_b32 v192, v49, v48, s61\n"
    "s_waitcnt vmcnt(60)\n"
    "v_perm_b32 v193, v51, v50, s61\n"
    "s_waitcnt vmcnt(58)\n"
    "v_perm_b32 v194, v53, v52, s61\n"
    "s_waitcnt vmcnt(56)\n"
    "v_perm_b32 v195, v55, v54, s61\n"
    "s_waitcnt vmcnt(54)\n"
    "v_perm_b32 v196, v57, v56, s61\n"
    "s_waitcnt vmcnt(52)\n"
    "v_perm_b32 v197, v59, v58, s61\n"
    "s_waitcnt vmcnt(50)\n"
    "v_perm_b32 v198, v61, v60, s61\n"
    "s_waitcnt vmcnt(48)\n"
    "v_perm_b32 v199, v63, v62, s61\n"
    "v_perm_b32 v200, v49, v48, s62\n"
    "v_perm_b32 v201, v51, v50, s62\n"
    "v_perm_b32 v202, v53, v52, s62\n"
    "v_perm_b32 v203, v55, v54, s62\n"
    "v_perm_b32 v204, v57, v56, s62\n"
    "v_perm_b32 v205, v59, v58, s62\n"
    "v_perm_b32 v206, v61, v60, s62\n"
    "v_perm_b32 v207, v63, v62, s62\n"
    "s_add_u32 s64, s60, 1048576\n"
    "buffer_load_dword v48, v117, s[44:47], s64 offen\n"
    "s_add_u32 s65, s60, 1064960\n"
    "buffer_load_dword v49, v117, s[44:47], s65 offen\n"
    "s_add_u32 s64, s60, 1179648\n"
    "buffer_load_dword v50, v117, s[44:47], s64 offen\n"
    "s_add_u32 s65, s60, 1196032\n"
    "buffer_load_dword v51, v117, s[44:47], s65 offen\n"
    "s_add_u32 s64, s60, 1081344\n"
    "buffer_load_dword v52, v117, s[44:47], s64 offen\n"
    "s_add_u32 s65, s60, 1097728\n"
    "buffer_load_dword v53, v117, s[44:47], s65 offen\n"
    "s_add_u32 s64, s60, 1212416\n"
    "buffer_load_dword v54, v117, s[44:47], s64 offen\n"
    "s_add_u32 s65, s60, 1228800\n"
    "buffer_load_dword v55, v117, s[44:47], s65 offen\n"
    "s_add_u32 s64, s60, 1310720\n"
    "buffer_load_dword v56, v117, s[44:47], s64 offen\n"
    "s_add_u32 s65, s60, 1327104\n"
    "buffer_load_dword v57, v117, s[44:47], s65 offen\n"
    "s_add_u32 s64, s60, 1441792\n"
    "buffer_load_dword v58, v117, s[44:47], s64 offen\n"
    "s_add_u32 s65, s60, 1458176\n"
    "buffer_load_dword v59, v117, s[44:47], s65 offen\n"
    "s_add_u32 s64, s60, 1343488\n"
    "buffer_load_dword v60, v117, s[44:47], s64 offen\n"
    "s_add_u32 s65, s60, 1359872\n"
    "buffer_load_dword v61, v117, s[44:47], s65 offen\n"
    "s_add_u32 s64, s60, 1474560\n"
    "buffer_load_dword v62, v117, s[44:47], s64 offen\n"
    "s_add_u32 s65, s60, 1490944\n"
    "buffer_load_dword v63, v117, s[44:47], s65 offen\n"
    "s_waitcnt vmcnt(62)\n"
    "v_perm_b32 v208, v65, v64, s61\n"
    "s_waitcnt vmcnt(60)\n"
    "v_perm_b32 v209, v67, v66, s61\n"
    "s_waitcnt vmcnt(58)\n"
    "v_perm_b32 v210, v69, v68, s61\n"
    "s_waitcnt vmcnt(56)\n"
    "v_perm_b32 v211, v71, v70, s61\n"
    "s_waitcnt vmcnt(54)\n"
    "v_perm_b32 v212, v73, v72, s61\n"
    "s_waitcnt vmcnt(52)\n"
    "v_perm_b32 v213, v75, v74, s61\n"
    "s_waitcnt vmcnt(50)\n"
    "v_perm_b32 v214, v77, v76, s61\n"
    "s_waitcnt vmcnt(48)\n"
    "v_perm_b32 v215, v79, v78, s61\n"
    "v_perm_b32 v216, v65, v64, s62\n"
    "v_perm_b32 v217, v67, v66, s62\n"
    "v_perm_b32 v218, v69, v68, s62\n"
    "v_perm_b32 v219, v71, v70, s62\n"
    "v_perm_b32 v220, v73, v72, s62\n"
    "v_perm_b32 v221, v75, v74, s62\n"
    "v_perm_b32 v222, v77, v76, s62\n"
    "v_perm_b32 v223, v79, v78, s62\n"
    "s_add_u32 s64, s60, 1572864\n"
    "buffer_load_dword v64, v117, s[44:47], s64 offen\n"
    "s_add_u32 s65, s60, 1589248\n"
    "buffer_load_dword v65, v117, s[44:47], s65 offen\n"
    "s_add_u32 s64, s60, 1703936\n"
    "buffer_load_dword v66, v117, s[44:47], s64 offen\n"
    "s_add_u32 s65, s60, 1720320\n"
    "buffer_load_dword v67, v117, s[44:47], s65 offen\n"
    "s_add_u32 s64, s60, 1605632\n"
    "buffer_load_dword v68, v117, s[44:47], s64 offen\n"
    "s_add_u32 s65, s60, 1622016\n"
    "buffer_load_dword v69, v117, s[44:47], s65 offen\n"
    "s_add_u32 s64, s60, 1736704\n"
    "buffer_load_dword v70, v117, s[44:47], s64 offen\n"
    "s_add_u32 s65, s60, 1753088\n"
    "buffer_load_dword v71, v117, s[44:47], s65 offen\n"
    "s_add_u32 s64, s60, 1835008\n"
    "buffer_load_dword v72, v117, s[44:47], s64 offen\n"
    "s_add_u32 s65, s60, 1851392\n"
    "buffer_load_dword v73, v117, s[44:47], s65 offen\n"
    "s_add_u32 s64, s60, 1966080\n"
    "buffer_load_dword v74, v117, s[44:47], s64 offen\n"
    "s_add_u32 s65, s60, 1982464\n"
    "buffer_load_dword v75, v117, s[44:47], s65 offen\n"
    "s_add_u32 s64, s60, 1867776\n"
    "buffer_load_dword v76, v117, s[44:47], s64 offen\n"
    "s_add_u32 s65, s60, 1884160\n"
    "buffer_load_dword v77, v117, s[44:47], s65 offen\n"
    "s_add_u32 s64, s60, 1998848\n"
    "buffer_load_dword v78, v117, s[44:47], s64 offen\n"
    "s_add_u32 s65, s60, 2015232\n"
    "buffer_load_dword v79, v117, s[44:47], s65 offen\n"
    "s_waitcnt vmcnt(62)\n"
    "v_perm_b32 v224, v81, v80, s61\n"
    "s_waitcnt vmcnt(60)\n"
    "v_perm_b32 v225, v83, v82, s61\n"
    "s_waitcnt vmcnt(58)\n"
    "v_perm_b32 v226, v85, v84, s61\n"
    "s_waitcnt vmcnt(56)\n"
    "v_perm_b32 v227, v87, v86, s61\n"
    "s_waitcnt vmcnt(54)\n"
    "v_perm_b32 v228, v89, v88, s61\n"
    "s_waitcnt vmcnt(52)\n"
    "v_perm_b32 v229, v91, v90, s61\n"
    "s_waitcnt vmcnt(50)\n"
    "v_perm_b32 v230, v93, v92, s61\n"
    "s_waitcnt vmcnt(48)\n"
    "v_perm_b32 v231, v95, v94, s61\n"
    "v_perm_b32 v232, v81, v80, s62\n"
    "v_perm_b32 v233, v83, v82, s62\n"
    "v_perm_b32 v234, v85, v84, s62\n"
    "v_perm_b32 v235, v87, v86, s62\n"
    "v_perm_b32 v236, v89, v88, s62\n"
    "v_perm_b32 v237, v91, v90, s62\n"
    "v_perm_b32 v238, v93, v92, s62\n"
    "v_perm_b32 v239, v95, v94, s62\n"
    "s_add_u32 s64, s60, 2097152\n"
    "buffer_load_dword v80, v117, s[44:47], s64 offen\n"
    "s_add_u32 s65, s60, 2113536\n"
    "buffer_load_dword v81, v117, s[44:47], s65 offen\n"
    "s_add_u32 s64, s60, 2228224\n"
    "buffer_load_dword v82, v117, s[44:47], s64 offen\n"
    "s_add_u32 s65, s60, 2244608\n"
    "buffer_load_dword v83, v117, s[44:47], s65 offen\n"
    "s_add_u32 s64, s60, 2129920\n"
    "buffer_load_dword v84, v117, s[44:47], s64 offen\n"
    "s_add_u32 s65, s60, 2146304\n"
    "buffer_load_dword v85, v117, s[44:47], s65 offen\n"
    "s_add_u32 s64, s60, 2260992\n"
    "buffer_load_dword v86, v117, s[44:47], s64 offen\n"
    "s_add_u32 s65, s60, 2277376\n"
    "buffer_load_dword v87, v117, s[44:47], s65 offen\n"
    "s_add_u32 s64, s60, 2359296\n"
    "buffer_load_dword v88, v117, s[44:47], s64 offen\n"
    "s_add_u32 s65, s60, 2375680\n"
    "buffer_load_dword v89, v117, s[44:47], s65 offen\n"
    "s_add_u32 s64, s60, 2490368\n"
    "buffer_load_dword v90, v117, s[44:47], s64 offen\n"
    "s_add_u32 s65, s60, 2506752\n"
    "buffer_load_dword v91, v117, s[44:47], s65 offen\n"
    "s_add_u32 s64, s60, 2392064\n"
    "buffer_load_dword v92, v117, s[44:47], s64 offen\n"
    "s_add_u32 s65, s60, 2408448\n"
    "buffer_load_dword v93, v117, s[44:47], s65 offen\n"
    "s_add_u32 s64, s60, 2523136\n"
    "buffer_load_dword v94, v117, s[44:47], s64 offen\n"
    "s_add_u32 s65, s60, 2539520\n"
    "buffer_load_dword v95, v117, s[44:47], s65 offen\n"
    "s_waitcnt vmcnt(62)\n"
    "v_perm_b32 v240, v33, v32, s61\n"
    "s_waitcnt vmcnt(60)\n"
    "v_perm_b32 v241, v35, v34, s61\n"
    "s_waitcnt vmcnt(58)\n"
    "v_perm_b32 v242, v37, v36, s61\n"
    "s_waitcnt vmcnt(56)\n"
    "v_perm_b32 v243, v39, v38, s61\n"
    "s_waitcnt vmcnt(54)\n"
    "v_perm_b32 v244, v41, v40, s61\n"
    "s_waitcnt vmcnt(52)\n"
    "v_perm_b32 v245, v43, v42, s61\n"
    "s_waitcnt vmcnt(50)\n"
    "v_perm_b32 v246, v45, v44, s61\n"
    "s_waitcnt vmcnt(48)\n"
    "v_perm_b32 v247, v47, v46, s61\n"
    "v_perm_b32 v248, v33, v32, s62\n"
    "v_perm_b32 v249, v35, v34, s62\n"
    "v_perm_b32 v250, v37, v36, s62\n"
    "v_perm_b32 v251, v39, v38, s62\n"
    "v_perm_b32 v252, v41, v40, s62\n"
    "v_perm_b32 v253, v43, v42, s62\n"
    "v_perm_b32 v254, v45, v44, s62\n"
    "v_perm_b32 v255, v47, v46, s62\n"
    "s_add_u32 s64, s60, 2621440\n"
    "buffer_load_dword v32, v117, s[44:47], s64 offen\n"
    "s_add_u32 s65, s60, 2637824\n"
    "buffer_load_dword v33, v117, s[44:47], s65 offen\n"
    "s_add_u32 s64, s60, 2752512\n"
    "buffer_load_dword v34, v117, s[44:47], s64 offen\n"
    "s_add_u32 s65, s60, 2768896\n"
    "buffer_load_dword v35, v117, s[44:47], s65 offen\n"
    "s_add_u32 s64, s60, 2654208\n"
    "buffer_load_dword v36, v117, s[44:47], s64 offen\n"
    "s_add_u32 s65, s60, 2670592\n"
    "buffer_load_dword v37, v117, s[44:47], s65 offen\n"
    "s_add_u32 s64, s60, 2785280\n"
    "buffer_load_dword v38, v117, s[44:47], s64 offen\n"
    "s_add_u32 s65, s60, 2801664\n"
    "buffer_load_dword v39, v117, s[44:47], s65 offen\n"
    "s_add_u32 s64, s60, 2883584\n"
    "buffer_load_dword v40, v117, s[44:47], s64 offen\n"
    "s_add_u32 s65, s60, 2899968\n"
    "buffer_load_dword v41, v117, s[44:47], s65 offen\n"
    "s_add_u32 s64, s60, 3014656\n"
    "buffer_load_dword v42, v117, s[44:47], s64 offen\n"
    "s_add_u32 s65, s60, 3031040\n"
    "buffer_load_dword v43, v117, s[44:47], s65 offen\n"
    "s_add_u32 s64, s60, 2916352\n"
    "buffer_load_dword v44, v117, s[44:47], s64 offen\n"
    "s_add_u32 s65, s60, 2932736\n"
    "buffer_load_dword v45, v117, s[44:47], s65 offen\n"
    "s_add_u32 s64, s60, 3047424\n"
    "buffer_load_dword v46, v117, s[44:47], s64 offen\n"
    "s_add_u32 s65, s60, 3063808\n"
    "buffer_load_dword v47, v117, s[44:47], s65 offen\n"
    "s_waitcnt vmcnt(62)\n"
    "v_perm_b32 v96, v49, v48, s61\n"
    "s_waitcnt vmcnt(60)\n"
    "v_perm_b32 v97, v51, v50, s61\n"
    "s_waitcnt vmcnt(58)\n"
    "v_perm_b32 v98, v53, v52, s61\n"
    "s_waitcnt vmcnt(56)\n"
    "v_perm_b32 v99, v55, v54, s61\n"
    "s_waitcnt vmcnt(54)\n"
    "v_perm_b32 v100, v57, v56, s61\n"
    "s_waitcnt vmcnt(52)\n"
    "v_perm_b32 v101, v59, v58, s61\n"
    "s_waitcnt vmcnt(50)\n"
    "v_perm_b32 v102, v61, v60, s61\n"
    "s_waitcnt vmcnt(48)\n"
    "v_perm_b32 v103, v63, v62, s61\n"
    "v_accvgpr_write_b32 a0, v96\n"
    "v_accvgpr_write_b32 a1, v97\n"
    "v_accvgpr_write_b32 a2, v98\n"
    "v_accvgpr_write_b32 a3, v99\n"
    "v_accvgpr_write_b32 a4, v100\n"
    "v_accvgpr_write_b32 a5, v101\n"
    "v_accvgpr_write_b32 a6, v102\n"
    "v_accvgpr_write_b32 a7, v103\n"
    "v_perm_b32 v104, v49, v48, s62\n"
    "v_perm_b32 v105, v51, v50, s62\n"
    "v_perm_b32 v106, v53, v52, s62\n"
    "v_perm_b32 v107, v55, v54, s62\n"
    "v_perm_b32 v108, v57, v56, s62\n"
    "v_perm_b32 v109, v59, v58, s62\n"
    "v_perm_b32 v110, v61, v60, s62\n"
    "v_perm_b32 v111, v63, v62, s62\n"
    "v_accvgpr_write_b32 a8, v104\n"
    "v_accvgpr_write_b32 a9, v105\n"
    "v_accvgpr_write_b32 a10, v106\n"
    "v_accvgpr_write_b32 a11, v107\n"
    "v_accvgpr_write_b32 a12, v108\n"
    "v_accvgpr_write_b32 a13, v109\n"
    "v_accvgpr_write_b32 a14, v110\n"
    "v_accvgpr_write_b32 a15, v111\n"
    "s_add_u32 s64, s60, 3145728\n"
    "buffer_load_dword v48, v117, s[44:47], s64 offen\n"
    "s_add_u32 s65, s60, 3162112\n"
    "buffer_load_dword v49, v117, s[44:47], s65 offen\n"
    "s_add_u32 s64, s60, 3276800\n"
    "buffer_load_dword v50, v117, s[44:47], s64 offen\n"
    "s_add_u32 s65, s60, 3293184\n"
    "buffer_load_dword v51, v117, s[44:47], s65 offen\n"
    "s_add_u32 s64, s60, 3178496\n"
    "buffer_load_dword v52, v117, s[44:47], s64 offen\n"
    "s_add_u32 s65, s60, 3194880\n"
    "buffer_load_dword v53, v117, s[44:47], s65 offen\n"
    "s_add_u32 s64, s60, 3309568\n"
    "buffer_load_dword v54, v117, s[44:47], s64 offen\n"
    "s_add_u32 s65, s60, 3325952\n"
    "buffer_load_dword v55, v117, s[44:47], s65 offen\n"
    "s_add_u32 s64, s60, 3407872\n"
    "buffer_load_dword v56, v117, s[44:47], s64 offen\n"
    "s_add_u32 s65, s60, 3424256\n"
    "buffer_load_dword v57, v117, s[44:47], s65 offen\n"
    "s_add_u32 s64, s60, 3538944\n"
    "buffer_load_dword v58, v117, s[44:47], s64 offen\n"
    "s_add_u32 s65, s60, 3555328\n"
    "buffer_load_dword v59, v117, s[44:47], s65 offen\n"
    "s_add_u32 s64, s60, 3440640\n"
    "buffer_load_dword v60, v117, s[44:47], s64 offen\n"
    "s_add_u32 s65, s60, 3457024\n"
    "buffer_load_dword v61, v117, s[44:47], s65 offen\n"
    "s_add_u32 s64, s60, 3571712\n"
    "buffer_load_dword v62, v117, s[44:47], s64 offen\n"
    "s_add_u32 s65, s60, 3588096\n"
    "buffer_load_dword v63, v117, s[44:47], s65 offen\n"
    "s_waitcnt vmcnt(62)\n"
    "v_perm_b32 v96, v65, v64, s61\n"
    "s_waitcnt vmcnt(60)\n"
    "v_perm_b32 v97, v67, v66, s61\n"
    "s_waitcnt vmcnt(58)\n"
    "v_perm_b32 v98, v69, v68, s61\n"
    "s_waitcnt vmcnt(56)\n"
    "v_perm_b32 v99, v71, v70, s61\n"
    "s_waitcnt vmcnt(54)\n"
    "v_perm_b32 v100, v73, v72, s61\n"
    "s_waitcnt vmcnt(52)\n"
    "v_perm_b32 v101, v75, v74, s61\n"
    "s_waitcnt vmcnt(50)\n"
    "v_perm_b32 v102, v77, v76, s61\n"
    "s_waitcnt vmcnt(48)\n"
    "v_perm_b32 v103, v79, v78, s61\n"
    "v_accvgpr_write_b32 a16, v96\n"
    "v_accvgpr_write_b32 a17, v97\n"
    "v_accvgpr_write_b32 a18, v98\n"
    "v_accvgpr_write_b32 a19, v99\n"
    "v_accvgpr_write_b32 a20, v100\n"
    "v_accvgpr_write_b32 a21, v101\n"
    "v_accvgpr_write_b32 a22, v102\n"
    "v_accvgpr_write_b32 a23, v103\n"
    "v_perm_b32 v104, v65, v64, s62\n"
    "v_perm_b32 v105, v67, v66, s62\n"
    "v_perm_b32 v106, v69, v68, s62\n"
    "v_perm_b32 v107, v71, v70, s62\n"
    "v_perm_b32 v108, v73, v72, s62\n"
    "v_perm_b32 v109, v75, v74, s62\n"
    "v_perm_b32 v110, v77, v76, s62\n"
    "v_perm_b32 v111, v79, v78, s62\n"
    "v_accvgpr_write_b32 a24, v104\n"
    "v_accvgpr_write_b32 a25, v105\n"
    "v_accvgpr_write_b32 a26, v106\n"
    "v_accvgpr_write_b32 a27, v107\n"
    "v_accvgpr_write_b32 a28, v108\n"
    "v_accvgpr_write_b32 a29, v109\n"
    "v_accvgpr_write_b32 a30, v110\n"
    "v_accvgpr_write_b32 a31, v111\n"
    "s_add_u32 s64, s60, 3670016\n"
    "buffer_load_dword v64, v117, s[44:47], s64 offen\n"
    "s_add_u32 s65, s60, 3686400\n"
    "buffer_load_dword v65, v117, s[44:47], s65 offen\n"
    "s_add_u32 s64, s60, 3801088\n"
    "buffer_load_dword v66, v117, s[44:47], s64 offen\n"
    "s_add_u32 s65, s60, 3817472\n"
    "buffer_load_dword v67, v117, s[44:47], s65 offen\n"
    "s_add_u32 s64, s60, 3702784\n"
    "buffer_load_dword v68, v117, s[44:47], s64 offen\n"
    "s_add_u32 s65, s60, 3719168\n"
    "buffer_load_dword v69, v117, s[44:47], s65 offen\n"
    "s_add_u32 s64, s60, 3833856\n"
    "buffer_load_dword v70, v117, s[44:47], s64 offen\n"
    "s_add_u32 s65, s60, 3850240\n"
    "buffer_load_dword v71, v117, s[44:47], s65 offen\n"
    "s_add_u32 s64, s60, 3932160\n"
    "buffer_load_dword v72, v117, s[44:47], s64 offen\n"
    "s_add_u32 s65, s60, 3948544\n"
    "buffer_load_dword v73, v117, s[44:47], s65 offen\n"
    "s_add_u32 s64, s60, 4063232\n"
    "buffer_load_dword v74, v117, s[44:47], s64 offen\n"
    "s_add_u32 s65, s60, 4079616\n"
    "buffer_load_dword v75, v117, s[44:47], s65 offen\n"
    "s_add_u32 s64, s60, 3964928\n"
    "buffer_load_dword v76, v117, s[44:47], s64 offen\n"
    "s_add_u32 s65, s60, 3981312\n"
    "buffer_load_dword v77, v117, s[44:47], s65 offen\n"
    "s_add_u32 s64, s60, 4096000\n"
    "buffer_load_dword v78, v117, s[44:47], s64 offen\n"
    "s_add_u32 s65, s60, 4112384\n"
    "buffer_load_dword v79, v117, s[44:47], s65 offen\n"
    "s_waitcnt vmcnt(62)\n"
    "v_perm_b32 v96, v81, v80, s61\n"
    "s_waitcnt vmcnt(60)\n"
    "v_perm_b32 v97, v83, v82, s61\n"
    "s_waitcnt vmcnt(58)\n"
    "v_perm_b32 v98, v85, v84, s61\n"
    "s_waitcnt vmcnt(56)\n"
    "v_perm_b32 v99, v87, v86, s61\n"
    "s_waitcnt vmcnt(54)\n"
    "v_perm_b32 v100, v89, v88, s61\n"
    "s_waitcnt vmcnt(52)\n"
    "v_perm_b32 v101, v91, v90, s61\n"
    "s_waitcnt vmcnt(50)\n"
    "v_perm_b32 v102, v93, v92, s61\n"
    "s_waitcnt vmcnt(48)\n"
    "v_perm_b32 v103, v95, v94, s61\n"
    "v_accvgpr_write_b32 a32, v96\n"
    "v_accvgpr_write_b32 a33, v97\n"
    "v_accvgpr_write_b32 a34, v98\n"
    "v_accvgpr_write_b32 a35, v99\n"
    "v_accvgpr_write_b32 a36, v100\n"
    "v_accvgpr_write_b32 a37, v101\n"
    "v_accvgpr_write_b32 a38, v102\n"
    "v_accvgpr_write_b32 a39, v103\n"
    "v_perm_b32 v104, v81, v80, s62\n"
    "v_perm_b32 v105, v83, v82, s62\n"
    "v_perm_b32 v106, v85, v84, s62\n"
    "v_perm_b32 v107, v87, v86, s62\n"
    "v_perm_b32 v108, v89, v88, s62\n"
    "v_perm_b32 v109, v91, v90, s62\n"
    "v_perm_b32 v110, v93, v92, s62\n"
    "v_perm_b32 v111, v95, v94, s62\n"
    "v_accvgpr_write_b32 a40, v104\n"
    "v_accvgpr_write_b32 a41, v105\n"
    "v_accvgpr_write_b32 a42, v106\n"
    "v_accvgpr_write_b32 a43, v107\n"
    "v_accvgpr_write_b32 a44, v108\n"
    "v_accvgpr_write_b32 a45, v109\n"
    "v_accvgpr_write_b32 a46, v110\n"
    "v_accvgpr_write_b32 a47, v111\n"
    "s_add_u32 s64, s60, 4194304\n"
    "buffer_load_dword v80, v117, s[44:47], s64 offen\n"
    "s_add_u32 s65, s60, 4210688\n"
    "buffer_load_dword v81, v117, s[44:47], s65 offen\n"
    "s_add_u32 s64, s60, 4325376\n"
    "buffer_load_dword v82, v117, s[44:47], s64 offen\n"
    "s_add_u32 s65, s60, 4341760\n"
    "buffer_load_dword v83, v117, s[44:47], s65 offen\n"
    "s_add_u32 s64, s60, 4227072\n"
    "buffer_load_dword v84, v117, s[44:47], s64 offen\n"
    "s_add_u32 s65, s60, 4243456\n"
    "buffer_load_dword v85, v117, s[44:47], s65 offen\n"
    "s_add_u32 s64, s60, 4358144\n"
    "buffer_load_dword v86, v117, s[44:47], s64 offen\n"
    "s_add_u32 s65, s60, 4374528\n"
    "buffer_load_dword v87, v117, s[44:47], s65 offen\n"
    "s_add_u32 s64, s60, 4456448\n"
    "buffer_load_dword v88, v117, s[44:47], s64 offen\n"
    "s_add_u32 s65, s60, 4472832\n"
    "buffer_load_dword v89, v117, s[44:47], s65 offen\n"
    "s_add_u32 s64, s60, 4587520\n"
    "buffer_load_dword v90, v117, s[44:47], s64 offen\n"
    "s_add_u32 s65, s60, 4603904\n"
    "buffer_load_dword v91, v117, s[44:47], s65 offen\n"
    "s_add_u32 s64, s60, 4489216\n"
    "buffer_load_dword v92, v117, s[44:47], s64 offen\n"
    "s_add_u32 s65, s60, 4505600\n"
    "buffer_load_dword v93, v117, s[44:47], s65 offen\n"
    "s_add_u32 s64, s60, 4620288\n"
    "buffer_load_dword v94, v117, s[44:47], s64 offen\n"
    "s_add_u32 s65, s60, 4636672\n"
    "buffer_load_dword v95, v117, s[44:47], s65 offen\n"
    "s_waitcnt vmcnt(62)\n"
    "v_perm_b32 v96, v33, v32, s61\n"
    "s_waitcnt vmcnt(60)\n"
    "v_perm_b32 v97, v35, v34, s61\n"
    "s_waitcnt vmcnt(58)\n"
    "v_perm_b32 v98, v37, v36, s61\n"
    "s_waitcnt vmcnt(56)\n"
    "v_perm_b32 v99, v39, v38, s61\n"
    "s_waitcnt vmcnt(54)\n"
    "v_perm_b32 v100, v41, v40, s61\n"
    "s_waitcnt vmcnt(52)\n"
    "v_perm_b32 v101, v43, v42, s61\n"
    "s_waitcnt vmcnt(50)\n"
    "v_perm_b32 v102, v45, v44, s61\n"
    "s_waitcnt vmcnt(48)\n"
    "v_perm_b32 v103, v47, v46, s61\n"
    "v_accvgpr_write_b32 a48, v96\n"
    "v_accvgpr_write_b32 a49, v97\n"
    "v_accvgpr_write_b32 a50, v98\n"
    "v_accvgpr_write_b32 a51, v99\n"
    "v_accvgpr_write_b32 a52, v100\n"
    "v_accvgpr_write_b32 a53, v101\n"
    "v_accvgpr_write_b32 a54, v102\n"
    "v_accvgpr_write_b32 a55, v103\n"
    "v_perm_b32 v104, v33, v32, s62\n"
    "v_perm_b32 v105, v35, v34, s62\n"
    "v_perm_b32 v106, v37, v36, s62\n"
    "v_perm_b32 v107, v39, v38, s62\n"
    "v_perm_b32 v108, v41, v40, s62\n"
    "v_perm_b32 v109, v43, v42, s62\n"
    "v_perm_b32 v110, v45, v44, s62\n"
    "v_perm_b32 v111, v47, v46, s62\n"
    "v_accvgpr_write_b32 a56, v104\n"
    "v_accvgpr_write_b32 a57, v105\n"
    "v_accvgpr_write_b32 a58, v106\n"
    "v_accvgpr_write_b32 a59, v107\n"
    "v_accvgpr_write_b32 a60, v108\n"
    "v_accvgpr_write_b32 a61, v109\n"
    "v_accvgpr_write_b32 a62, v110\n"
    "v_accvgpr_write_b32 a63, v111\n"
    "s_mov_b64 s[44:45], %4\n"
    "s_and_b32 s45, s45, 0xffff\n"
    "s_mov_b32 s46, 0x480000\n"
    "s_mov_b32 s47, 0x20000\n"
    "s_add_u32 s64, s60, 0\n"
    "buffer_load_dword v32, v117, s[44:47], s64 offen\n"
    "s_add_u32 s65, s60, 16384\n"
    "buffer_load_dword v33, v117, s[44:47], s65 offen\n"
    "s_add_u32 s64, s60, 131072\n"
    "buffer_load_dword v34, v117, s[44:47], s64 offen\n"
    "s_add_u32 s65, s60, 147456\n"
    "buffer_load_dword v35, v117, s[44:47], s65 offen\n"
    "s_add_u32 s64, s60, 32768\n"
    "buffer_load_dword v36, v117, s[44:47], s64 offen\n"
    "s_add_u32 s65, s60, 49152\n"
    "buffer_load_dword v37, v117, s[44:47], s65 offen\n"
    "s_add_u32 s64, s60, 163840\n"
    "buffer_load_dword v38, v117, s[44:47], s64 offen\n"
    "s_add_u32 s65, s60, 180224\n"
    "buffer_load_dword v39, v117, s[44:47], s65 offen\n"
    "s_add_u32 s64, s60, 262144\n"
    "buffer_load_dword v40, v117, s[44:47], s64 offen\n"
    "s_add_u32 s65, s60, 278528\n"
    "buffer_load_dword v41, v117, s[44:47], s65 offen\n"
    "s_add_u32 s64, s60, 393216\n"
    "buffer_load_dword v42, v117, s[44:47], s64 offen\n"
    "s_add_u32 s65, s60, 409600\n"
    "buffer_load_dword v43, v117, s[44:47], s65 offen\n"
    "s_add_u32 s64, s60, 294912\n"
    "buffer_load_dword v44, v117, s[44:47], s64 offen\n"
    "s_add_u32 s65, s60, 311296\n"
    "buffer_load_dword v45, v117, s[44:47], s65 offen\n"
    "s_add_u32 s64, s60, 425984\n"
    "buffer_load_dword v46, v117, s[44:47], s64 offen\n"
    "s_add_u32 s65, s60, 442368\n"
    "buffer_load_dword v47, v117, s[44:47], s65 offen\n"
    "s_waitcnt vmcnt(62)\n"
    "v_perm_b32 v96, v49, v48, s61\n"
    "s_waitcnt vmcnt(60)\n"
    "v_perm_b32 v97, v51, v50, s61\n"
    "s_waitcnt vmcnt(58)\n"
    "v_perm_b32 v98, v53, v52, s61\n"
    "s_waitcnt vmcnt(56)\n"
    "v_perm_b32 v99, v55, v54, s61\n"
    "s_waitcnt vmcnt(54)\n"
    "v_perm_b32 v100, v57, v56, s61\n"
    "s_waitcnt vmcnt(52)\n"
    "v_perm_b32 v101, v59, v58, s61\n"
    "s_waitcnt vmcnt(50)\n"
    "v_perm_b32 v102, v61, v60, s61\n"
    "s_waitcnt vmcnt(48)\n"
    "v_perm_b32 v103, v63, v62, s61\n"
    "v_accvgpr_write_b32 a64, v96\n"
    "v_accvgpr_write_b32 a65, v97\n"
    "v_accvgpr_write_b32 a66, v98\n"
    "v_accvgpr_write_b32 a67, v99\n"
    "v_accvgpr_write_b32 a68, v100\n"
    "v_accvgpr_write_b32 a69, v101\n"
    "v_accvgpr_write_b32 a70, v102\n"
    "v_accvgpr_write_b32 a71, v103\n"
    "v_perm_b32 v104, v49, v48, s62\n"
    "v_perm_b32 v105, v51, v50, s62\n"
    "v_perm_b32 v106, v53, v52, s62\n"
    "v_perm_b32 v107, v55, v54, s62\n"
    "v_perm_b32 v108, v57, v56, s62\n"
    "v_perm_b32 v109, v59, v58, s62\n"
    "v_perm_b32 v110, v61, v60, s62\n"
    "v_perm_b32 v111, v63, v62, s62\n"
    "v_accvgpr_write_b32 a72, v104\n"
    "v_accvgpr_write_b32 a73, v105\n"
    "v_accvgpr_write_b32 a74, v106\n"
    "v_accvgpr_write_b32 a75, v107\n"
    "v_accvgpr_write_b32 a76, v108\n"
    "v_accvgpr_write_b32 a77, v109\n"
    "v_accvgpr_write_b32 a78, v110\n"
    "v_accvgpr_write_b32 a79, v111\n"
    "s_add_u32 s64, s60, 524288\n"
    "buffer_load_dword v48, v117, s[44:47], s64 offen\n"
    "s_add_u32 s65, s60, 540672\n"
    "buffer_load_dword v49, v117, s[44:47], s65 offen\n"
    "s_add_u32 s64, s60, 655360\n"
    "buffer_load_dword v50, v117, s[44:47], s64 offen\n"
    "s_add_u32 s65, s60, 671744\n"
    "buffer_load_dword v51, v117, s[44:47], s65 offen\n"
    "s_add_u32 s64, s60, 557056\n"
    "buffer_load_dword v52, v117, s[44:47], s64 offen\n"
    "s_add_u32 s65, s60, 573440\n"
    "buffer_load_dword v53, v117, s[44:47], s65 offen\n"
    "s_add_u32 s64, s60, 688128\n"
    "buffer_load_dword v54, v117, s[44:47], s64 offen\n"
    "s_add_u32 s65, s60, 704512\n"
    "buffer_load_dword v55, v117, s[44:47], s65 offen\n"
    "s_add_u32 s64, s60, 786432\n"
    "buffer_load_dword v56, v117, s[44:47], s64 offen\n"
    "s_add_u32 s65, s60, 802816\n"
    "buffer_load_dword v57, v117, s[44:47], s65 offen\n"
    "s_add_u32 s64, s60, 917504\n"
    "buffer_load_dword v58, v117, s[44:47], s64 offen\n"
    "s_add_u32 s65, s60, 933888\n"
    "buffer_load_dword v59, v117, s[44:47], s65 offen\n"
    "s_add_u32 s64, s60, 819200\n"
    "buffer_load_dword v60, v117, s[44:47], s64 offen\n"
    "s_add_u32 s65, s60, 835584\n"
    "buffer_load_dword v61, v117, s[44:47], s65 offen\n"
    "s_add_u32 s64, s60, 950272\n"
    "buffer_load_dword v62, v117, s[44:47], s64 offen\n"
    "s_add_u32 s65, s60, 966656\n"
    "buffer_load_dword v63, v117, s[44:47], s65 offen\n"
    "s_waitcnt vmcnt(62)\n"
    "v_perm_b32 v96, v65, v64, s61\n"
    "s_waitcnt vmcnt(60)\n"
    "v_perm_b32 v97, v67, v66, s61\n"
    "s_waitcnt vmcnt(58)\n"
    "v_perm_b32 v98, v69, v68, s61\n"
    "s_waitcnt vmcnt(56)\n"
    "v_perm_b32 v99, v71, v70, s61\n"
    "s_waitcnt vmcnt(54)\n"
    "v_perm_b32 v100, v73, v72, s61\n"
    "s_waitcnt vmcnt(52)\n"
    "v_perm_b32 v101, v75, v74, s61\n"
    "s_waitcnt vmcnt(50)\n"
    "v_perm_b32 v102, v77, v76, s61\n"
    "s_waitcnt vmcnt(48)\n"
    "v_perm_b32 v103, v79, v78, s61\n"
    "v_accvgpr_write_b32 a80, v96\n"
    "v_accvgpr_write_b32 a81, v97\n"
    "v_accvgpr_write_b32 a82, v98\n"
    "v_accvgpr_write_b32 a83, v99\n"
    "v_accvgpr_write_b32 a84, v100\n"
    "v_accvgpr_write_b32 a85, v101\n"
    "v_accvgpr_write_b32 a86, v102\n"
    "v_accvgpr_write_b32 a87, v103\n"
    "v_perm_b32 v104, v65, v64, s62\n"
    "v_perm_b32 v105, v67, v66, s62\n"
    "v_perm_b32 v106, v69, v68, s62\n"
    "v_perm_b32 v107, v71, v70, s62\n"
    "v_perm_b32 v108, v73, v72, s62\n"
    "v_perm_b32 v109, v75, v74, s62\n"
    "v_perm_b32 v110, v77, v76, s62\n"
    "v_perm_b32 v111, v79, v78, s62\n"
    "v_accvgpr_write_b32 a88, v104\n"
    "v_accvgpr_write_b32 a89, v105\n"
    "v_accvgpr_write_b32 a90, v106\n"
    "v_accvgpr_write_b32 a91, v107\n"
    "v_accvgpr_write_b32 a92, v108\n"
    "v_accvgpr_write_b32 a93, v109\n"
    "v_accvgpr_write_b32 a94, v110\n"
    "v_accvgpr_write_b32 a95, v111\n"
    "s_add_u32 s64, s60, 1048576\n"
    "buffer_load_dword v64, v117, s[44:47], s64 offen\n"
    "s_add_u32 s65, s60, 1064960\n"
    "buffer_load_dword v65, v117, s[44:47], s65 offen\n"
    "s_add_u32 s64, s60, 1179648\n"
    "buffer_load_dword v66, v117, s[44:47], s64 offen\n"
    "s_add_u32 s65, s60, 1196032\n"
    "buffer_load_dword v67, v117, s[44:47], s65 offen\n"
    "s_add_u32 s64, s60, 1081344\n"
    "buffer_load_dword v68, v117, s[44:47], s64 offen\n"
    "s_add_u32 s65, s60, 1097728\n"
    "buffer_load_dword v69, v117, s[44:47], s65 offen\n"
    "s_add_u32 s64, s60, 1212416\n"
    "buffer_load_dword v70, v117, s[44:47], s64 offen\n"
    "s_add_u32 s65, s60, 1228800\n"
    "buffer_load_dword v71, v117, s[44:47], s65 offen\n"
    "s_add_u32 s64, s60, 1310720\n"
    "buffer_load_dword v72, v117, s[44:47], s64 offen\n"
    "s_add_u32 s65, s60, 1327104\n"
    "buffer_load_dword v73, v117, s[44:47], s65 offen\n"
    "s_add_u32 s64, s60, 1441792\n"
    "buffer_load_dword v74, v117, s[44:47], s64 offen\n"
    "s_add_u32 s65, s60, 1458176\n"
    "buffer_load_dword v75, v117, s[44:47], s65 offen\n"
    "s_add_u32 s64, s60, 1343488\n"
    "buffer_load_dword v76, v117, s[44:47], s64 offen\n"
    "s_add_u32 s65, s60, 1359872\n"
    "buffer_load_dword v77, v117, s[44:47], s65 offen\n"
    "s_add_u32 s64, s60, 1474560\n"
    "buffer_load_dword v78, v117, s[44:47], s64 offen\n"
    "s_add_u32 s65, s60, 1490944\n"
    "buffer_load_dword v79, v117, s[44:47], s65 offen\n"
    "s_waitcnt vmcnt(62)\n"
    "v_perm_b32 v96, v81, v80, s61\n"
    "s_waitcnt vmcnt(60)\n"
    "v_perm_b32 v97, v83, v82, s61\n"
    "s_waitcnt vmcnt(58)\n"
    "v_perm_b32 v98, v85, v84, s61\n"
    "s_waitcnt vmcnt(56)\n"
    "v_perm_b32 v99, v87, v86, s61\n"
    "s_waitcnt vmcnt(54)\n"
    "v_perm_b32 v100, v89, v88, s61\n"
    "s_waitcnt vmcnt(52)\n"
    "v_perm_b32 v101, v91, v90, s61\n"
    "s_waitcnt vmcnt(50)\n"
    "v_perm_b32 v102, v93, v92, s61\n"
    "s_waitcnt vmcnt(48)\n"
    "v_perm_b32 v103, v95, v94, s61\n"
    "v_accvgpr_write_b32 a96, v96\n"
    "v_accvgpr_write_b32 a97, v97\n"
    "v_accvgpr_write_b32 a98, v98\n"
    "v_accvgpr_write_b32 a99, v99\n"
    "v_accvgpr_write_b32 a100, v100\n"
    "v_accvgpr_write_b32 a101, v101\n"
    "v_accvgpr_write_b32 a102, v102\n"
    "v_accvgpr_write_b32 a103, v103\n"
    "v_perm_b32 v104, v81, v80, s62\n"
    "v_perm_b32 v105, v83, v82, s62\n"
    "v_perm_b32 v106, v85, v84, s62\n"
    "v_perm_b32 v107, v87, v86, s62\n"
    "v_perm_b32 v108, v89, v88, s62\n"
    "v_perm_b32 v109, v91, v90, s62\n"
    "v_perm_b32 v110, v93, v92, s62\n"
    "v_perm_b32 v111, v95, v94, s62\n"
    "v_accvgpr_write_b32 a104, v104\n"
    "v_accvgpr_write_b32 a105, v105\n"
    "v_accvgpr_write_b32 a106, v106\n"
    "v_accvgpr_write_b32 a107, v107\n"
    "v_accvgpr_write_b32 a108, v108\n"
    "v_accvgpr_write_b32 a109, v109\n"
    "v_accvgpr_write_b32 a110, v110\n"
    "v_accvgpr_write_b32 a111, v111\n"
    "s_add_u32 s64, s60, 1572864\n"
    "buffer_load_dword v80, v117, s[44:47], s64 offen\n"
    "s_add_u32 s65, s60, 1589248\n"
    "buffer_load_dword v81, v117, s[44:47], s65 offen\n"
    "s_add_u32 s64, s60, 1703936\n"
    "buffer_load_dword v82, v117, s[44:47], s64 offen\n"
    "s_add_u32 s65, s60, 1720320\n"
    "buffer_load_dword v83, v117, s[44:47], s65 offen\n"
    "s_add_u32 s64, s60, 1605632\n"
    "buffer_load_dword v84, v117, s[44:47], s64 offen\n"
    "s_add_u32 s65, s60, 1622016\n"
    "buffer_load_dword v85, v117, s[44:47], s65 offen\n"
    "s_add_u32 s64, s60, 1736704\n"
    "buffer_load_dword v86, v117, s[44:47], s64 offen\n"
    "s_add_u32 s65, s60, 1753088\n"
    "buffer_load_dword v87, v117, s[44:47], s65 offen\n"
    "s_add_u32 s64, s60, 1835008\n"
    "buffer_load_dword v88, v117, s[44:47], s64 offen\n"
    "s_add_u32 s65, s60, 1851392\n"
    "buffer_load_dword v89, v117, s[44:47], s65 offen\n"
    "s_add_u32 s64, s60, 1966080\n"
    "buffer_load_dword v90, v117, s[44:47], s64 offen\n"
    "s_add_u32 s65, s60, 1982464\n"
    "buffer_load_dword v91, v117, s[44:47], s65 offen\n"
    "s_add_u32 s64, s60, 1867776\n"
    "buffer_load_dword v92, v117, s[44:47], s64 offen\n"
    "s_add_u32 s65, s60, 1884160\n"
    "buffer_load_dword v93, v117, s[44:47], s65 offen\n"
    "s_add_u32 s64, s60, 1998848\n"
    "buffer_load_dword v94, v117, s[44:47], s64 offen\n"
    "s_add_u32 s65, s60, 2015232\n"
    "buffer_load_dword v95, v117, s[44:47], s65 offen\n"
    "s_waitcnt vmcnt(62)\n"
    "v_perm_b32 v96, v33, v32, s61\n"
    "s_waitcnt vmcnt(60)\n"
    "v_perm_b32 v97, v35, v34, s61\n"
    "s_waitcnt vmcnt(58)\n"
    "v_perm_b32 v98, v37, v36, s61\n"
    "s_waitcnt vmcnt(56)\n"
    "v_perm_b32 v99, v39, v38, s61\n"
    "s_waitcnt vmcnt(54)\n"
    "v_perm_b32 v100, v41, v40, s61\n"
    "s_waitcnt vmcnt(52)\n"
    "v_perm_b32 v101, v43, v42, s61\n"
    "s_waitcnt vmcnt(50)\n"
    "v_perm_b32 v102, v45, v44, s61\n"
    "s_waitcnt vmcnt(48)\n"
    "v_perm_b32 v103, v47, v46, s61\n"
    "v_accvgpr_write_b32 a112, v96\n"
    "v_accvgpr_write_b32 a113, v97\n"
    "v_accvgpr_write_b32 a114, v98\n"
    "v_accvgpr_write_b32 a115, v99\n"
    "v_accvgpr_write_b32 a116, v100\n"
    "v_accvgpr_write_b32 a117, v101\n"
    "v_accvgpr_write_b32 a118, v102\n"
    "v_accvgpr_write_b32 a119, v103\n"
    "v_perm_b32 v104, v33, v32, s62\n"
    "v_perm_b32 v105, v35, v34, s62\n"
    "v_perm_b32 v106, v37, v36, s62\n"
    "v_perm_b32 v107, v39, v38, s62\n"
    "v_perm_b32 v108, v41, v40, s62\n"
    "v_perm_b32 v109, v43, v42, s62\n"
    "v_perm_b32 v110, v45, v44, s62\n"
    "v_perm_b32 v111, v47, v46, s62\n"
    "v_accvgpr_write_b32 a120, v104\n"
    "v_accvgpr_write_b32 a121, v105\n"
    "v_accvgpr_write_b32 a122, v106\n"
    "v_accvgpr_write_b32 a123, v107\n"
    "v_accvgpr_write_b32 a124, v108\n"
    "v_accvgpr_write_b32 a125, v109\n"
    "v_accvgpr_write_b32 a126, v110\n"
    "v_accvgpr_write_b32 a127, v111\n"
    "s_add_u32 s64, s60, 2097152\n"
    "buffer_load_dword v32, v117, s[44:47], s64 offen\n"
    "s_add_u32 s65, s60, 2113536\n"
    "buffer_load_dword v33, v117, s[44:47], s65 offen\n"
    "s_add_u32 s64, s60, 2228224\n"
    "buffer_load_dword v34, v117, s[44:47], s64 offen\n"
    "s_add_u32 s65, s60, 2244608\n"
    "buffer_load_dword v35, v117, s[44:47], s65 offen\n"
    "s_add_u32 s64, s60, 2129920\n"
    "buffer_load_dword v36, v117, s[44:47], s64 offen\n"
    "s_add_u32 s65, s60, 2146304\n"
    "buffer_load_dword v37, v117, s[44:47], s65 offen\n"
    "s_add_u32 s64, s60, 2260992\n"
    "buffer_load_dword v38, v117, s[44:47], s64 offen\n"
    "s_add_u32 s65, s60, 2277376\n"
    "buffer_load_dword v39, v117, s[44:47], s65 offen\n"
    "s_add_u32 s64, s60, 2359296\n"
    "buffer_load_dword v40, v117, s[44:47], s64 offen\n"
    "s_add_u32 s65, s60, 2375680\n"
    "buffer_load_dword v41, v117, s[44:47], s65 offen\n"
    "s_add_u32 s64, s60, 2490368\n"
    "buffer_load_dword v42, v117, s[44:47], s64 offen\n"
    "s_add_u32 s65, s60, 2506752\n"
    "buffer_load_dword v43, v117, s[44:47], s65 offen\n"
    "s_add_u32 s64, s60, 2392064\n"
    "buffer_load_dword v44, v117, s[44:47], s64 offen\n"
    "s_add_u32 s65, s60, 2408448\n"
    "buffer_load_dword v45, v117, s[44:47], s65 offen\n"
    "s_add_u32 s64, s60, 2523136\n"
    "buffer_load_dword v46, v117, s[44:47], s64 offen\n"
    "s_add_u32 s65, s60, 2539520\n"
    "buffer_load_dword v47, v117, s[44:47], s65 offen\n"
    "s_waitcnt vmcnt(62)\n"
    "v_perm_b32 v96, v49, v48, s61\n"
    "s_waitcnt vmcnt(60)\n"
    "v_perm_b32 v97, v51, v50, s61\n"
    "s_waitcnt vmcnt(58)\n"
    "v_perm_b32 v98, v53, v52, s61\n"
    "s_waitcnt vmcnt(56)\n"
    "v_perm_b32 v99, v55, v54, s61\n"
    "s_waitcnt vmcnt(54)\n"
    "v_perm_b32 v100, v57, v56, s61\n"
    "s_waitcnt vmcnt(52)\n"
    "v_perm_b32 v101, v59, v58, s61\n"
    "s_waitcnt vmcnt(50)\n"
    "v_perm_b32 v102, v61, v60, s61\n"
    "s_waitcnt vmcnt(48)\n"
    "v_perm_b32 v103, v63, v62, s61\n"
    "v_accvgpr_write_b32 a128, v96\n"
    "v_accvgpr_write_b32 a129, v97\n"
    "v_accvgpr_write_b32 a130, v98\n"
    "v_accvgpr_write_b32 a131, v99\n"
    "v_accvgpr_write_b32 a132, v100\n"
    "v_accvgpr_write_b32 a133, v101\n"
    "v_accvgpr_write_b32 a134, v102\n"
    "v_accvgpr_write_b32 a135, v103\n"
    "v_perm_b32 v104, v49, v48, s62\n"
    "v_perm_b32 v105, v51, v50, s62\n"
    "v_perm_b32 v106, v53, v52, s62\n"
    "v_perm_b32 v107, v55, v54, s62\n"
    "v_perm_b32 v108, v57, v56, s62\n"
    "v_perm_b32 v109, v59, v58, s62\n"
    "v_perm_b32 v110, v61, v60, s62\n"
    "v_perm_b32 v111, v63, v62, s62\n"
    "v_accvgpr_write_b32 a136, v104\n"
    "v_accvgpr_write_b32 a137, v105\n"
    "v_accvgpr_write_b32 a138, v106\n"
    "v_accvgpr_write_b32 a139, v107\n"
    "v_accvgpr_write_b32 a140, v108\n"
    "v_accvgpr_write_b32 a141, v109\n"
    "v_accvgpr_write_b32 a142, v110\n"
    "v_accvgpr_write_b32 a143, v111\n"
    "s_add_u32 s64, s60, 2621440\n"
    "buffer_load_dword v48, v117, s[44:47], s64 offen\n"
    "s_add_u32 s65, s60, 2637824\n"
    "buffer_load_dword v49, v117, s[44:47], s65 offen\n"
    "s_add_u32 s64, s60, 2752512\n"
    "buffer_load_dword v50, v117, s[44:47], s64 offen\n"
    "s_add_u32 s65, s60, 2768896\n"
    "buffer_load_dword v51, v117, s[44:47], s65 offen\n"
    "s_add_u32 s64, s60, 2654208\n"
    "buffer_load_dword v52, v117, s[44:47], s64 offen\n"
    "s_add_u32 s65, s60, 2670592\n"
    "buffer_load_dword v53, v117, s[44:47], s65 offen\n"
    "s_add_u32 s64, s60, 2785280\n"
    "buffer_load_dword v54, v117, s[44:47], s64 offen\n"
    "s_add_u32 s65, s60, 2801664\n"
    "buffer_load_dword v55, v117, s[44:47], s65 offen\n"
    "s_add_u32 s64, s60, 2883584\n"
    "buffer_load_dword v56, v117, s[44:47], s64 offen\n"
    "s_add_u32 s65, s60, 2899968\n"
    "buffer_load_dword v57, v117, s[44:47], s65 offen\n"
    "s_add_u32 s64, s60, 3014656\n"
    "buffer_load_dword v58, v117, s[44:47], s64 offen\n"
    "s_add_u32 s65, s60, 3031040\n"
    "buffer_load_dword v59, v117, s[44:47], s65 offen\n"
    "s_add_u32 s64, s60, 2916352\n"
    "buffer_load_dword v60, v117, s[44:47], s64 offen\n"
    "s_add_u32 s65, s60, 2932736\n"
    "buffer_load_dword v61, v117, s[44:47], s65 offen\n"
    "s_add_u32 s64, s60, 3047424\n"
    "buffer_load_dword v62, v117, s[44:47], s64 offen\n"
    "s_add_u32 s65, s60, 3063808\n"
    "buffer_load_dword v63, v117, s[44:47], s65 offen\n"
    "s_waitcnt vmcnt(62)\n"
    "v_perm_b32 v96, v65, v64, s61\n"
    "s_waitcnt vmcnt(60)\n"
    "v_perm_b32 v97, v67, v66, s61\n"
    "s_waitcnt vmcnt(58)\n"
    "v_perm_b32 v98, v69, v68, s61\n"
    "s_waitcnt vmcnt(56)\n"
    "v_perm_b32 v99, v71, v70, s61\n"
    "s_waitcnt vmcnt(54)\n"
    "v_perm_b32 v100, v73, v72, s61\n"
    "s_waitcnt vmcnt(52)\n"
    "v_perm_b32 v101, v75, v74, s61\n"
    "s_waitcnt vmcnt(50)\n"
    "v_perm_b32 v102, v77, v76, s61\n"
    "s_waitcnt vmcnt(48)\n"
    "v_perm_b32 v103, v79, v78, s61\n"
    "v_accvgpr_write_b32 a144, v96\n"
    "v_accvgpr_write_b32 a145, v97\n"
    "v_accvgpr_write_b32 a146, v98\n"
    "v_accvgpr_write_b32 a147, v99\n"
    "v_accvgpr_write_b32 a148, v100\n"
    "v_accvgpr_write_b32 a149, v101\n"
    "v_accvgpr_write_b32 a150, v102\n"
    "v_accvgpr_write_b32 a151, v103\n"
    "v_perm_b32 v104, v65, v64, s62\n"
    "v_perm_b32 v105, v67, v66, s62\n"
    "v_perm_b32 v106, v69, v68, s62\n"
    "v_perm_b32 v107, v71, v70, s62\n"
    "v_perm_b32 v108, v73, v72, s62\n"
    "v_perm_b32 v109, v75, v74, s62\n"
    "v_perm_b32 v110, v77, v76, s62\n"
    "v_perm_b32 v111, v79, v78, s62\n"
    "v_accvgpr_write_b32 a152, v104\n"
    "v_accvgpr_write_b32 a153, v105\n"
    "v_accvgpr_write_b32 a154, v106\n"
    "v_accvgpr_write_b32 a155, v107\n"
    "v_accvgpr_write_b32 a156, v108\n"
    "v_accvgpr_write_b32 a157, v109\n"
    "v_accvgpr_write_b32 a158, v110\n"
    "v_accvgpr_write_b32 a159, v111\n"
    "s_add_u32 s64, s60, 3145728\n"
    "buffer_load_dword v64, v117, s[44:47], s64 offen\n"
    "s_add_u32 s65, s60, 3162112\n"
    "buffer_load_dword v65, v117, s[44:47], s65 offen\n"
    "s_add_u32 s64, s60, 3276800\n"
    "buffer_load_dword v66, v117, s[44:47], s64 offen\n"
    "s_add_u32 s65, s60, 3293184\n"
    "buffer_load_dword v67, v117, s[44:47], s65 offen\n"
    "s_add_u32 s64, s60, 3178496\n"
    "buffer_load_dword v68, v117, s[44:47], s64 offen\n"
    "s_add_u32 s65, s60, 3194880\n"
    "buffer_load_dword v69, v117, s[44:47], s65 offen\n"
    "s_add_u32 s64, s60, 3309568\n"
    "buffer_load_dword v70, v117, s[44:47], s64 offen\n"
    "s_add_u32 s65, s60, 3325952\n"
    "buffer_load_dword v71, v117, s[44:47], s65 offen\n"
    "s_add_u32 s64, s60, 3407872\n"
    "buffer_load_dword v72, v117, s[44:47], s64 offen\n"
    "s_add_u32 s65, s60, 3424256\n"
    "buffer_load_dword v73, v117, s[44:47], s65 offen\n"
    "s_add_u32 s64, s60, 3538944\n"
    "buffer_load_dword v74, v117, s[44:47], s64 offen\n"
    "s_add_u32 s65, s60, 3555328\n"
    "buffer_load_dword v75, v117, s[44:47], s65 offen\n"
    "s_add_u32 s64, s60, 3440640\n"
    "buffer_load_dword v76, v117, s[44:47], s64 offen\n"
    "s_add_u32 s65, s60, 3457024\n"
    "buffer_load_dword v77, v117, s[44:47], s65 offen\n"
    "s_add_u32 s64, s60, 3571712\n"
    "buffer_load_dword v78, v117, s[44:47], s64 offen\n"
    "s_add_u32 s65, s60, 3588096\n"
    "buffer_load_dword v79, v117, s[44:47], s65 offen\n"
    "s_waitcnt vmcnt(62)\n"
    "v_perm_b32 v96, v81, v80, s61\n"
    "s_waitcnt vmcnt(60)\n"
    "v_perm_b32 v97, v83, v82, s61\n"
    "s_waitcnt vmcnt(58)\n"
    "v_perm_b32 v98, v85, v84, s61\n"
    "s_waitcnt vmcnt(56)\n"
    "v_perm_b32 v99, v87, v86, s61\n"
    "s_waitcnt vmcnt(54)\n"
    "v_perm_b32 v100, v89, v88, s61\n"
    "s_waitcnt vmcnt(52)\n"
    "v_perm_b32 v101, v91, v90, s61\n"
    "s_waitcnt vmcnt(50)\n"
    "v_perm_b32 v102, v93, v92, s61\n"
    "s_waitcnt vmcnt(48)\n"
    "v_perm_b32 v103, v95, v94, s61\n"
    "v_accvgpr_write_b32 a160, v96\n"
    "v_accvgpr_write_b32 a161, v97\n"
    "v_accvgpr_write_b32 a162, v98\n"
    "v_accvgpr_write_b32 a163, v99\n"
    "v_accvgpr_write_b32 a164, v100\n"
    "v_accvgpr_write_b32 a165, v101\n"
    "v_accvgpr_write_b32 a166, v102\n"
    "v_accvgpr_write_b32 a167, v103\n"
    "v_perm_b32 v104, v81, v80, s62\n"
    "v_perm_b32 v105, v83, v82, s62\n"
    "v_perm_b32 v106, v85, v84, s62\n"
    "v_perm_b32 v107, v87, v86, s62\n"
    "v_perm_b32 v108, v89, v88, s62\n"
    "v_perm_b32 v109, v91, v90, s62\n"
    "v_perm_b32 v110, v93, v92, s62\n"
    "v_perm_b32 v111, v95, v94, s62\n"
    "v_accvgpr_write_b32 a168, v104\n"
    "v_accvgpr_write_b32 a169, v105\n"
    "v_accvgpr_write_b32 a170, v106\n"
    "v_accvgpr_write_b32 a171, v107\n"
    "v_accvgpr_write_b32 a172, v108\n"
    "v_accvgpr_write_b32 a173, v109\n"
    "v_accvgpr_write_b32 a174, v110\n"
    "v_accvgpr_write_b32 a175, v111\n"
    "s_add_u32 s64, s60, 3670016\n"
    "buffer_load_dword v80, v117, s[44:47], s64 offen\n"
    "s_add_u32 s65, s60, 3686400\n"
    "buffer_load_dword v81, v117, s[44:47], s65 offen\n"
    "s_add_u32 s64, s60, 3801088\n"
    "buffer_load_dword v82, v117, s[44:47], s64 offen\n"
    "s_add_u32 s65, s60, 3817472\n"
    "buffer_load_dword v83, v117, s[44:47], s65 offen\n"
    "s_add_u32 s64, s60, 3702784\n"
    "buffer_load_dword v84, v117, s[44:47], s64 offen\n"
    "s_add_u32 s65, s60, 3719168\n"
    "buffer_load_dword v85, v117, s[44:47], s65 offen\n"
    "s_add_u32 s64, s60, 3833856\n"
    "buffer_load_dword v86, v117, s[44:47], s64 offen\n"
    "s_add_u32 s65, s60, 3850240\n"
    "buffer_load_dword v87, v117, s[44:47], s65 offen\n"
    "s_add_u32 s64, s60, 3932160\n"
    "buffer_load_dword v88, v117, s[44:47], s64 offen\n"
    "s_add_u32 s65, s60, 3948544\n"
    "buffer_load_dword v89, v117, s[44:47], s65 offen\n"
    "s_add_u32 s64, s60, 4063232\n"
    "buffer_load_dword v90, v117, s[44:47], s64 offen\n"
    "s_add_u32 s65, s60, 4079616\n"
    "buffer_load_dword v91, v117, s[44:47], s65 offen\n"
    "s_add_u32 s64, s60, 3964928\n"
    "buffer_load_dword v92, v117, s[44:47], s64 offen\n"
    "s_add_u32 s65, s60, 3981312\n"
    "buffer_load_dword v93, v117, s[44:47], s65 offen\n"
    "s_add_u32 s64, s60, 4096000\n"
    "buffer_load_dword v94, v117, s[44:47], s64 offen\n"
    "s_add_u32 s65, s60, 4112384\n"
    "buffer_load_dword v95, v117, s[44:47], s65 offen\n"
    "s_waitcnt vmcnt(62)\n"
    "v_perm_b32 v96, v33, v32, s61\n"
    "s_waitcnt vmcnt(60)\n"
    "v_perm_b32 v97, v35, v34, s61\n"
    "s_waitcnt vmcnt(58)\n"
    "v_perm_b32 v98, v37, v36, s61\n"
    "s_waitcnt vmcnt(56)\n"
    "v_perm_b32 v99, v39, v38, s61\n"
    "s_waitcnt vmcnt(54)\n"
    "v_perm_b32 v100, v41, v40, s61\n"
    "s_waitcnt vmcnt(52)\n"
    "v_perm_b32 v101, v43, v42, s61\n"
    "s_waitcnt vmcnt(50)\n"
    "v_perm_b32 v102, v45, v44, s61\n"
    "s_waitcnt vmcnt(48)\n"
    "v_perm_b32 v103, v47, v46, s61\n"
    "v_accvgpr_write_b32 a176, v96\n"
    "v_accvgpr_write_b32 a177, v97\n"
    "v_accvgpr_write_b32 a178, v98\n"
    "v_accvgpr_write_b32 a179, v99\n"
    "v_accvgpr_write_b32 a180, v100\n"
    "v_accvgpr_write_b32 a181, v101\n"
    "v_accvgpr_write_b32 a182, v102\n"
    "v_accvgpr_write_b32 a183, v103\n"
    "v_perm_b32 v104, v33, v32, s62\n"
    "v_perm_b32 v105, v35, v34, s62\n"
    "v_perm_b32 v106, v37, v36, s62\n"
    "v_perm_b32 v107, v39, v38, s62\n"
    "v_perm_b32 v108, v41, v40, s62\n"
    "v_perm_b32 v109, v43, v42, s62\n"
    "v_perm_b32 v110, v45, v44, s62\n"
    "v_perm_b32 v111, v47, v46, s62\n"
    "v_accvgpr_write_b32 a184, v104\n"
    "v_accvgpr_write_b32 a185, v105\n"
    "v_accvgpr_write_b32 a186, v106\n"
    "v_accvgpr_write_b32 a187, v107\n"
    "v_accvgpr_write_b32 a188, v108\n"
    "v_accvgpr_write_b32 a189, v109\n"
    "v_accvgpr_write_b32 a190, v110\n"
    "v_accvgpr_write_b32 a191, v111\n"
    "s_add_u32 s64, s60, 4194304\n"
    "buffer_load_dword v32, v117, s[44:47], s64 offen\n"
    "s_add_u32 s65, s60, 4210688\n"
    "buffer_load_dword v33, v117, s[44:47], s65 offen\n"
    "s_add_u32 s64, s60, 4325376\n"
    "buffer_load_dword v34, v117, s[44:47], s64 offen\n"
    "s_add_u32 s65, s60, 4341760\n"
    "buffer_load_dword v35, v117, s[44:47], s65 offen\n"
    "s_add_u32 s64, s60, 4227072\n"
    "buffer_load_dword v36, v117, s[44:47], s64 offen\n"
    "s_add_u32 s65, s60, 4243456\n"
    "buffer_load_dword v37, v117, s[44:47], s65 offen\n"
    "s_add_u32 s64, s60, 4358144\n"
    "buffer_load_dword v38, v117, s[44:47], s64 offen\n"
    "s_add_u32 s65, s60, 4374528\n"
    "buffer_load_dword v39, v117, s[44:47], s65 offen\n"
    "s_add_u32 s64, s60, 4456448\n"
    "buffer_load_dword v40, v117, s[44:47], s64 offen\n"
    "s_add_u32 s65, s60, 4472832\n"
    "buffer_load_dword v41, v117, s[44:47], s65 offen\n"
    "s_add_u32 s64, s60, 4587520\n"
    "buffer_load_dword v42, v117, s[44:47], s64 offen\n"
    "s_add_u32 s65, s60, 4603904\n"
    "buffer_load_dword v43, v117, s[44:47], s65 offen\n"
    "s_add_u32 s64, s60, 4489216\n"
    "buffer_load_dword v44, v117, s[44:47], s64 offen\n"
    "s_add_u32 s65, s60, 4505600\n"
    "buffer_load_dword v45, v117, s[44:47], s65 offen\n"
    "s_add_u32 s64, s60, 4620288\n"
    "buffer_load_dword v46, v117, s[44:47], s64 offen\n"
    "s_add_u32 s65, s60, 4636672\n"
    "buffer_load_dword v47, v117, s[44:47], s65 offen\n"
    "s_waitcnt vmcnt(62)\n"
    "v_perm_b32 v96, v49, v48, s61\n"
    "s_waitcnt vmcnt(60)\n"
    "v_perm_b32 v97, v51, v50, s61\n"
    "s_waitcnt vmcnt(58)\n"
    "v_perm_b32 v98, v53, v52, s61\n"
    "s_waitcnt vmcnt(56)\n"
    "v_perm_b32 v99, v55, v54, s61\n"
    "s_waitcnt vmcnt(54)\n"
    "v_perm_b32 v100, v57, v56, s61\n"
    "s_waitcnt vmcnt(52)\n"
    "v_perm_b32 v101, v59, v58, s61\n"
    "s_waitcnt vmcnt(50)\n"
    "v_perm_b32 v102, v61, v60, s61\n"
    "s_waitcnt vmcnt(48)\n"
    "v_perm_b32 v103, v63, v62, s61\n"
    "v_accvgpr_write_b32 a192, v96\n"
    "v_accvgpr_write_b32 a193, v97\n"
    "v_accvgpr_write_b32 a194, v98\n"
    "v_accvgpr_write_b32 a195, v99\n"
    "v_accvgpr_write_b32 a196, v100\n"
    "v_accvgpr_write_b32 a197, v101\n"
    "v_accvgpr_write_b32 a198, v102\n"
    "v_accvgpr_write_b32 a199, v103\n"
    "v_perm_b32 v104, v49, v48, s62\n"
    "v_perm_b32 v105, v51, v50, s62\n"
    "v_perm_b32 v106, v53, v52, s62\n"
    "v_perm_b32 v107, v55, v54, s62\n"
    "v_perm_b32 v108, v57, v56, s62\n"
    "v_perm_b32 v109, v59, v58, s62\n"
    "v_perm_b32 v110, v61, v60, s62\n"
    "v_perm_b32 v111, v63, v62, s62\n"
    "v_accvgpr_write_b32 a200, v104\n"
    "v_accvgpr_write_b32 a201, v105\n"
    "v_accvgpr_write_b32 a202, v106\n"
    "v_accvgpr_write_b32 a203, v107\n"
    "v_accvgpr_write_b32 a204, v108\n"
    "v_accvgpr_write_b32 a205, v109\n"
    "v_accvgpr_write_b32 a206, v110\n"
    "v_accvgpr_write_b32 a207, v111\n"
    "s_waitcnt vmcnt(46)\n"
    "v_perm_b32 v96, v65, v64, s61\n"
    "s_waitcnt vmcnt(44)\n"
    "v_perm_b32 v97, v67, v66, s61\n"
    "s_waitcnt vmcnt(42)\n"
    "v_perm_b32 v98, v69, v68, s61\n"
    "s_waitcnt vmcnt(40)\n"
    "v_perm_b32 v99, v71, v70, s61\n"
    "s_waitcnt vmcnt(38)\n"
    "v_perm_b32 v100, v73, v72, s61\n"
    "s_waitcnt vmcnt(36)\n"
    "v_perm_b32 v101, v75, v74, s61\n"
    "s_waitcnt vmcnt(34)\n"
    "v_perm_b32 v102, v77, v76, s61\n"
    "s_waitcnt vmcnt(32)\n"
    "v_perm_b32 v103, v79, v78, s61\n"
    "v_accvgpr_write_b32 a208, v96\n"
    "v_accvgpr_write_b32 a209, v97\n"
    "v_accvgpr_write_b32 a210, v98\n"
    "v_accvgpr_write_b32 a211, v99\n"
    "v_accvgpr_write_b32 a212, v100\n"
    "v_accvgpr_write_b32 a213, v101\n"
    "v_accvgpr_write_b32 a214, v102\n"
    "v_accvgpr_write_b32 a215, v103\n"
    "v_perm_b32 v104, v65, v64, s62\n"
    "v_perm_b32 v105, v67, v66, s62\n"
    "v_perm_b32 v106, v69, v68, s62\n"
    "v_perm_b32 v107, v71, v70, s62\n"
    "v_perm_b32 v108, v73, v72, s62\n"
    "v_perm_b32 v109, v75, v74, s62\n"
    "v_perm_b32 v110, v77, v76, s62\n"
    "v_perm_b32 v111, v79, v78, s62\n"
    "v_accvgpr_write_b32 a216, v104\n"
    "v_accvgpr_write_b32 a217, v105\n"
    "v_accvgpr_write_b32 a218, v106\n"
    "v_accvgpr_write_b32 a219, v107\n"
    "v_accvgpr_write_b32 a220, v108\n"
    "v_accvgpr_write_b32 a221, v109\n"
    "v_accvgpr_write_b32 a222, v110\n"
    "v_accvgpr_write_b32 a223, v111\n"
    "s_waitcnt vmcnt(30)\n"
    "v_perm_b32 v96, v81, v80, s61\n"
    "s_waitcnt vmcnt(28)\n"
    "v_perm_b32 v97, v83, v82, s61\n"
    "s_waitcnt vmcnt(26)\n"
    "v_perm_b32 v98, v85, v84, s61\n"
    "s_waitcnt vmcnt(24)\n"
    "v_perm_b32 v99, v87, v86, s61\n"
    "s_waitcnt vmcnt(22)\n"
    "v_perm_b32 v100, v89, v88, s61\n"
    "s_waitcnt vmcnt(20)\n"
    "v_perm_b32 v101, v91, v90, s61\n"
    "s_waitcnt vmcnt(18)\n"
    "v_perm_b32 v102, v93, v92, s61\n"
    "s_waitcnt vmcnt(16)\n"
    "v_perm_b32 v103, v95, v94, s61\n"
    "v_accvgpr_write_b32 a224, v96\n"
    "v_accvgpr_write_b32 a225, v97\n"
    "v_accvgpr_write_b32 a226, v98\n"
    "v_accvgpr_write_b32 a227, v99\n"
    "v_accvgpr_write_b32 a228, v100\n"
    "v_accvgpr_write_b32 a229, v101\n"
    "v_accvgpr_write_b32 a230, v102\n"
    "v_accvgpr_write_b32 a231, v103\n"
    "v_perm_b32 v104, v81, v80, s62\n"
    "v_perm_b32 v105, v83, v82, s62\n"
    "v_perm_b32 v106, v85, v84, s62\n"
    "v_perm_b32 v107, v87, v86, s62\n"
    "v_perm_b32 v108, v89, v88, s62\n"
    "v_perm_b32 v109, v91, v90, s62\n"
    "v_perm_b32 v110, v93, v92, s62\n"
    "v_perm_b32 v111, v95, v94, s62\n"
    "v_accvgpr_write_b32 a232, v104\n"
    "v_accvgpr_write_b32 a233, v105\n"
    "v_accvgpr_write_b32 a234, v106\n"
    "v_accvgpr_write_b32 a235, v107\n"
    "v_accvgpr_write_b32 a236, v108\n"
    "v_accvgpr_write_b32 a237, v109\n"
    "v_accvgpr_write_b32 a238, v110\n"
    "v_accvgpr_write_b32 a239, v111\n"
    "s_waitcnt vmcnt(14)\n"
    "v_perm_b32 v96, v33, v32, s61\n"
    "s_waitcnt vmcnt(12)\n"
    "v_perm_b32 v97, v35, v34, s61\n"
    "s_waitcnt vmcnt(10)\n"
    "v_perm_b32 v98, v37, v36, s61\n"
    "s_waitcnt vmcnt(8)\n"
    "v_perm_b32 v99, v39, v38, s61\n"
    "s_waitcnt vmcnt(6)\n"
    "v_perm_b32 v100, v41, v40, s61\n"
    "s_waitcnt vmcnt(4)\n"
    "v_perm_b32 v101, v43, v42, s61\n"
    "s_waitcnt vmcnt(2)\n"
    "v_perm_b32 v102, v45, v44, s61\n"
    "s_waitcnt vmcnt(0)\n"
    "v_perm_b32 v103, v47, v46, s61\n"
    "v_accvgpr_write_b32 a240, v96\n"
    "v_accvgpr_write_b32 a241, v97\n"
    "v_accvgpr_write_b32 a242, v98\n"
    "v_accvgpr_write_b32 a243, v99\n"
    "v_accvgpr_write_b32 a244, v100\n"
    "v_accvgpr_write_b32 a245, v101\n"
    "v_accvgpr_write_b32 a246, v102\n"
    "v_accvgpr_write_b32 a247, v103\n"
    "v_perm_b32 v104, v33, v32, s62\n"
    "v_perm_b32 v105, v35, v34, s62\n"
    "v_perm_b32 v106, v37, v36, s62\n"
    "v_perm_b32 v107, v39, v38, s62\n"
    "v_perm_b32 v108, v41, v40, s62\n"
    "v_perm_b32 v109, v43, v42, s62\n"
    "v_perm_b32 v110, v45, v44, s62\n"
    "v_perm_b32 v111, v47, v46, s62\n"
    "v_accvgpr_write_b32 a248, v104\n"
    "v_accvgpr_write_b32 a249, v105\n"
    "v_accvgpr_write_b32 a250, v106\n"
    "v_accvgpr_write_b32 a251, v107\n"
    "v_accvgpr_write_b32 a252, v108\n"
    "v_accvgpr_write_b32 a253, v109\n"
    "v_accvgpr_write_b32 a254, v110\n"
    "v_accvgpr_write_b32 a255, v111\n"
    "s_waitcnt vmcnt(0) lgkmcnt(0)\n"
    "s_barrier\n"
    "s_cmp_eq_u32 %6, 0\n"
    "s_cbranch_scc0 .Lnr_%=\n"
    "v_mov_b32 v134, 128\n"
    "v_mov_b32 v135, 1\n"
    "s_mov_b64 exec, 1\n"
    "global_atomic_add v134, v135, %1\n"
    "s_mov_b64 exec, -1\n"
    ".Lnr_%=:\n"
    "v_mov_b32 v1, 0x4444\n"
    "v_mov_b32 v118, 0xeeee\n"
    "v_cmp_gt_u32 vcc, 8, v113\n"
    "s_nop 1\n"
    "v_cndmask_b32 v1, v118, v1, vcc\n"
    "s_lshl_b32 s73, %6, 10\n"
    "v_add_u32 v5, s73, v0\n"
    "v_and_b32 v119, 7, v113\n"
    "v_lshrrev_b32 v120, 3, v113\n"
    "v_lshl_add_u32 v3, v115, 1, v116\n"
    "v_mul_u32_u24 v3, 544, v3\n"
    "v_lshl_add_u32 v3, v113, 2, v3\n"
    "s_lshl_b32 s73, %6, 6\n"
    "s_add_u32 s73, s73, 20480\n"
    "v_add_u32 v3, s73, v3\n"
    "v_mul_u32_u24 v4, 272, v119\n"
    "v_lshl_add_u32 v4, v114, 4, v4\n"
    "v_lshlrev_b32 v6, 7, v114\n"
    "v_lshl_add_u32 v6, v119, 4, v6\n"
    "s_lshl_b32 s73, %6, 9\n"
    "v_add_u32 v6, s73, v6\n"
    "s_mov_b32 s56, 0\n"
    "v_mov_b32 v134, 64\n"
    ".Lpoll1_%=:\n"
    "global_load_dword v135, v134, %1 sc1\n"
    "s_waitcnt vmcnt(0)\n"
    "v_readfirstlane_b32 s73, v135\n"
    "s_cmp_ge_u32 s73, 4\n"
    "s_cbranch_scc1 .Lpoll1_x%=\n"
    "s_add_u32 s56, s56, 1\n"
    "s_cmp_lt_u32 s56, 3\n"
    "s_cbranch_scc1 .Lpoll1_%=\n"
    "s_sleep 8\n"
    "s_cmp_lt_u32 s56, s57\n"
    "s_cbranch_scc1 .Lpoll1_%=\n"
    "s_mov_b32 s57, 0\n"
    "v_mov_b32 v135, 1\n"
    "global_store_dword v11, v135, %1 sc1\n"
    ".Lpoll1_x%=:\n"
    "s_xor_b32 s69, s73, %9\n"
    "s_lshr_b32 s51, s73, 1\n"
    "s_xor_b32 s51, s51, %9\n"
    "s_and_b32 s51, s51, 1\n"
    "s_lshl_b32 s51, s51, 13\n"
    "s_add_u32 s68, s51, 0x200000\n"
    "s_and_b32 s60, s69, 1\n"
    "s_lshl_b32 s60, s60, 13\n"
    "v_lshl_add_u32 v8, v120, 2, v115\n"
    "v_lshlrev_b32 v8, 12, v8\n"
    "v_lshl_add_u32 v8, v119, 4, v8\n"
    "v_lshl_add_u32 v7, v116, 15, v8\n"
    "v_lshl_add_u32 v8, v116, 3, v8\n"
    "s_mul_i32 s73, %8, 589824\n"
    "s_lshl_b32 s51, %7, 7\n"
    "s_add_u32 s73, s73, s51\n"
    "s_add_u32 s73, s73, s60\n"
    "s_lshl_b32 s51, %6, 16\n"
    "s_add_u32 s51, s51, s73\n"
    "v_add_u32 v7, s51, v7\n"
    "s_and_b32 s51, %6, 1\n"
    "s_lshl_b32 s51, s51, 15\n"
    "s_add_u32 s51, s51, 524288\n"
    "s_add_u32 s51, s51, s73\n"
    "v_add_u32 v8, s51, v8\n"
    "s_mov_b32 s74, 262144\n"
    "s_lshl_b32 s61, %7, 1\n"
    "s_add_u32 s61, s61, %8\n"
    "s_lshr_b32 s73, s61, 2\n"
    "s_add_u32 s73, s73, 128\n"
    "s_and_b32 s51, s61, 3\n"
    "s_cmp_eq_u32 s51, 0\n"
    "s_cselect_b32 s71, s73, -1\n"
    "s_lshl_b32 s73, s61, 1\n"
    "s_add_u32 s73, s73, %6\n"
    "s_cmp_lt_u32 %6, 2\n"
    "s_cselect_b32 s71, s73, s71\n"
    "s_max_i32 s61, s71, 0\n"
    "s_lshr_b32 s73, s61, 1\n"
    "s_lshl_b32 s73, s73, 14\n"
    "s_and_b32 s51, s61, 1\n"
    "s_lshl_b32 s51, s51, 12\n"
    "s_add_u32 s65, s73, s51\n"
    "s_add_u32 s65, s65, s60\n"
    "s_cmp_ge_u32 s61, 48\n"
    "s_cselect_b32 s94, 1, 0\n"
    "s_cmp_ge_u32 s61, 96\n"
    "s_cselect_b32 s94, 2, s94\n"
    "s_mul_i32 s73, s94, 48\n"
    "s_sub_u32 s73, s61, s73\n"
    "s_lshl_b32 s73, s73, 6\n"
    "v_lshlrev_b32 v9, 4, v114\n"
    "v_add_u32 v9, s73, v9\n"
    "v_bfrev_b32 v118, 1\n"
    "v_cmp_eq_u32 vcc, 0, v113\n"
    "s_nop 1\n"
    "v_cndmask_b32 v9, v118, v9, vcc\n"
    "s_mul_i32 s73, %6, 3072\n"
    "v_add_u32 v10, s73, v0\n"
    "s_cmp_eq_u32 %6, 3\n"
    "s_cbranch_scc0 .Lnw3_%=\n"
    "v_lshlrev_b32 v9, 13, v113\n"
    "v_lshl_add_u32 v9, v114, 4, v9\n"
    "s_lshl_b32 s73, %8, 4\n"
    "v_add_u32 v10, s73, v113\n"
    "v_lshlrev_b32 v118, 2, v114\n"
    "v_sub_u32 v10, v10, v118\n"
    "v_cmp_le_i32 s[76:77], 0, v10\n"
    "v_cmp_le_i32 s[78:79], 1, v10\n"
    "v_cmp_le_i32 s[80:81], 2, v10\n"
    "v_cmp_le_i32 s[82:83], 3, v10\n"
    "v_cmp_le_i32 s[84:85], 16, v10\n"
    "v_cmp_le_i32 s[86:87], 17, v10\n"
    "v_cmp_le_i32 s[88:89], 18, v10\n"
    "v_cmp_le_i32 s[90:91], 19, v10\n"
    ".Lnw3_%=:\n"
    "s_lshl_b32 s73, %9, 5\n"
    "s_add_u32 s73, s73, %7\n"
    "s_lshl_b32 s73, s73, 8\n"
    "s_add_u32 s92, s73, 74752\n"
    "s_lshl_b32 s73, %9, 8\n"
    "s_lshr_b32 s51, %7, 3\n"
    "s_add_u32 s73, s73, s51\n"
    "s_lshl_b32 s73, s73, 6\n"
    "s_add_u32 s93, s73, 41984\n"
    "s_lshl_b32 s73, %8, 17\n"
    "s_lshl_b32 s51, %7, 8\n"
    "s_add_u32 s73, s73, s51\n"
    "s_add_u32 s75, s73, 0x2ec0000\n"
    "s_mov_b32 s48, 0\n"
    "s_mov_b32 s54, 600\n"
    "s_mov_b32 s55, 2200\n"
    "s_mov_b32 s56, 0\n"
    "v_mov_b32 v134, s92\n"
    ".Lpoll2_%=:\n"
    "global_load_dword v135, v134, %1 sc1\n"
    "s_waitcnt vmcnt(0)\n"
    "v_readfirstlane_b32 s73, v135\n"
    "s_cmp_ge_u32 s73, 1\n"
    "s_cbranch_scc1 .Lpoll2_x%=\n"
    "s_add_u32 s56, s56, 1\n"
    "s_cmp_lt_u32 s56, 3\n"
    "s_cbranch_scc1 .Lpoll2_%=\n"
    "s_sleep 8\n"
    "s_cmp_lt_u32 s56, s57\n"
    "s_cbranch_scc1 .Lpoll2_%=\n"
    "s_mov_b32 s57, 0\n"
    "v_mov_b32 v135, 1\n"
    "global_store_dword v11, v135, %1 sc1\n"
    ".Lpoll2_x%=:\n"
    "s_add_u32 s73, s48, 0\n"
    "s_and_b32 s73, s73, 63\n"
    "s_lshr_b32 s51, s73, 2\n"
    "s_xor_b32 s51, s51, s69\n"
    "s_and_b32 s51, s51, 1\n"
    "s_lshl_b32 s51, s51, 13\n"
    "s_lshl_b32 s73, s73, 5\n"
    "s_add_u32 s73, s73, %7\n"
    "s_lshl_b32 s73, s73, 14\n"
    "s_add_u32 s73, s73, s51\n"
    "s_add_u32 s60, s73, 0x6000000\n"
    "s_add_u32 s61, s73, 0x6001000\n"
    "s_add_u32 s62, s73, 0x8000000\n"
    "s_add_u32 s63, s73, 0x8001000\n"
    "s_lshl_b32 s51, %8, 12\n"
    "s_add_u32 s73, s73, s51\n"
    "s_add_u32 s64, s73, 0x4000000\n"
    "buffer_load_dwordx4 v[12:15], v5, s[40:43], s60 offen sc1\n"
    "buffer_load_dwordx4 v[16:19], v5, s[40:43], s61 offen sc1\n"
    "buffer_load_dwordx4 v[20:23], v5, s[40:43], s62 offen sc1\n"
    "buffer_load_dwordx4 v[24:27], v5, s[40:43], s63 offen sc1\n"
    "buffer_load_dwordx4 v[28:31], v5, s[40:43], s64 offen sc1\n"
    "s_waitcnt vmcnt(4)\n"
    "ds_write_b128 v5, v[12:15]\n"
    "s_waitcnt vmcnt(3)\n"
    "ds_write_b128 v5, v[16:19] offset:4096\n"
    "s_waitcnt vmcnt(2)\n"
    "ds_write_b128 v5, v[20:23] offset:8192\n"
    "s_waitcnt vmcnt(1)\n"
    "ds_write_b128 v5, v[24:27] offset:12288\n"
    "s_waitcnt vmcnt(0)\n"
    "ds_write_b128 v5, v[28:31] offset:16384\n"
    "s_add_u32 s73, s48, 1\n"
    "s_and_b32 s73, s73, 63\n"
    "s_lshl_b32 s73, s73, 2\n"
    "s_add_u32 s73, s73, s92\n"
    "v_mov_b32 v134, s73\n"
    "global_load_dword v133, v134, %1 sc1\n"
    "s_waitcnt vmcnt(0)\n"
    "v_readfirstlane_b32 s59, v133\n"
    "s_mov_b32 s56, 0\n"
    "v_mov_b32 v134, 128\n"
    ".Lpoll3_%=:\n"
    "global_load_dword v135, v134, %1 sc1\n"
    "s_waitcnt vmcnt(0)\n"
    "v_readfirstlane_b32 s73, v135\n"
    "s_cmp_ge_u32 s73, 128\n"
    "s_cbranch_scc1 .Lpoll3_x%=\n"
    "s_add_u32 s56, s56, 1\n"
    "s_cmp_lt_u32 s56, 3\n"
    "s_cbranch_scc1 .Lpoll3_%=\n"
    "s_sleep 8\n"
    "s_cmp_lt_u32 s56, s57\n"
    "s_cbranch_scc1 .Lpoll3_%=\n"
    "s_mov_b32 s57, 0\n"
    "v_mov_b32 v135, 1\n"
    "global_store_dword v11, v135, %1 sc1\n"
    ".Lpoll3_x%=:\n"
    "s_add_u32 s73, s48, 1\n"
    "s_and_b32 s73, s73, 63\n"
    "s_lshr_b32 s51, s73, 2\n"
    "s_xor_b32 s51, s51, s69\n"
    "s_and_b32 s51, s51, 1\n"
    "s_lshl_b32 s51, s51, 13\n"
    "s_lshl_b32 s73, s73, 5\n"
    "s_add_u32 s73, s73, %7\n"
    "s_lshl_b32 s73, s73, 14\n"
    "s_add_u32 s73, s73, s51\n"
    "s_add_u32 s60, s73, 0x6000000\n"
    "s_add_u32 s61, s73, 0x6001000\n"
    "s_add_u32 s62, s73, 0x8000000\n"
    "s_add_u32 s63, s73, 0x8001000\n"
    "s_lshl_b32 s51, %8, 12\n"
    "s_add_u32 s73, s73, s51\n"
    "s_add_u32 s64, s73, 0x4000000\n"
    "ds_read_b128 v[32:35], v2\n"
    "ds_read_b128 v[36:39], v2 offset:1024\n"
    "ds_read_b128 v[40:43], v2 offset:2048\n"
    "ds_read_b128 v[44:47], v2 offset:3072\n"
    "ds_read_b128 v[48:51], v2 offset:4096\n"
    "ds_read_b128 v[52:55], v2 offset:5120\n"
    "ds_read_b128 v[56:59], v2 offset:6144\n"
    "ds_read_b128 v[60:63], v2 offset:7168\n"
    "ds_read_b128 v[64:67], v2 offset:8192\n"
    "ds_read_b128 v[68:71], v2 offset:9216\n"
    "ds_read_b128 v[72:75], v2 offset:10240\n"
    "ds_read_b128 v[76:79], v2 offset:11264\n"
    "ds_read_b128 v[80:83], v2 offset:12288\n"
    "ds_read_b128 v[84:87], v2 offset:13312\n"
    "v_mov_b64 v[124:125], 0\n"
    "v_mov_b64 v[126:127], 0\n"
    "v_mov_b64 v[128:129], 0\n"
    "v_mov_b64 v[130:131], 0\n"
    "s_waitcnt lgkmcnt(0)\n"
    "s_barrier\n"
    ".Lstep_%=:\n"
    "ds_read_b128 v[88:91], v0 offset:20480\n"
    "ds_read_b128 v[92:95], v0 offset:21504\n"
    "ds_read_b128 v[96:99], v0 offset:22528\n"
    "ds_read_b128 v[100:103], v0 offset:23552\n"
    "ds_read_b128 v[104:107], v0 offset:24576\n"
    "ds_read_b128 v[108:111], v0 offset:25600\n"
    "ds_read_b128 v[112:115], v0 offset:26624\n"
    "ds_read_b128 v[116:119], v0 offset:27648\n"
    "ds_read_b128 v[120:123], v0 offset:28672\n"
    "buffer_load_dwordx4 v[12:15], v5, s[40:43], s60 offen sc1\n"
    "buffer_load_dwordx4 v[16:19], v5, s[40:43], s61 offen sc1\n"
    "buffer_load_dwordx4 v[20:23], v5, s[40:43], s62 offen sc1\n"
    "buffer_load_dwordx4 v[24:27], v5, s[40:43], s63 offen sc1\n"
    "buffer_load_dwordx4 v[28:31], v5, s[40:43], s64 offen sc1\n"
    "s_waitcnt lgkmcnt(8)\n"
    "v_smfmac_f32_16x16x64_bf16 v[124:127], v[88:91], v[32:39], v1\n"
    "v_smfmac_f32_16x16x64_bf16 v[128:131], v[88:91], v[40:47], v1\n"
    "s_waitcnt lgkmcnt(7)\n"
    "v_smfmac_f32_16x16x64_bf16 v[124:127], v[92:95], v[48:55], v1\n"
    "v_mov_b64 v[32:33], 0\n"
    "v_mov_b64 v[34:35], 0\n"
    "v_smfmac_f32_16x16x64_bf16 v[128:131], v[92:95], v[56:63], v1\n"
    "v_mov_b64 v[36:37], 0\n"
    "v_mov_b64 v[38:39], 0\n"
    "s_waitcnt lgkmcnt(6)\n"
    "v_smfmac_f32_16x16x64_bf16 v[124:127], v[96:99], v[64:71], v1\n"
    "v_smfmac_f32_16x16x64_bf16 v[128:131], v[96:99], v[72:79], v1\n"
    "s_waitcnt lgkmcnt(5)\n"
    "v_smfmac_f32_16x16x64_bf16 v[124:127], v[100:103], v[80:87], v1\n"
    "v_smfmac_f32_16x16x64_bf16 v[128:131], v[100:103], v[136:143], v1\n"
    "s_waitcnt lgkmcnt(4)\n"
    "v_smfmac_f32_16x16x64_bf16 v[124:127], v[104:107], v[144:151], v1\n"
    "ds_read_b128 v[40:43], v0\n"
    "v_smfmac_f32_16x16x64_bf16 v[128:131], v[104:107], v[152:159], v1\n"
    "ds_read_b128 v[44:47], v0 offset:4096\n"
    "s_waitcnt lgkmcnt(5)\n"
    "v_smfmac_f32_16x16x64_bf16 v[124:127], v[108:111], v[160:167], v1\n"
    "ds_read_b128 v[48:51], v0 offset:1024\n"
    "v_smfmac_f32_16x16x64_bf16 v[128:131], v[108:111], v[168:175], v1\n"
    "ds_read_b128 v[52:55], v0 offset:5120\n"
    "s_waitcnt lgkmcnt(6)\n"
    "v_smfmac_f32_16x16x64_bf16 v[124:127], v[112:115], v[176:183], v1\n"
    "ds_read_b128 v[56:59], v0 offset:2048\n"
    "v_smfmac_f32_16x16x64_bf16 v[128:131], v[112:115], v[184:191], v1\n"
    "ds_read_b128 v[60:63], v0 offset:6144\n"
    "s_waitcnt lgkmcnt(7)\n"
    "v_smfmac_f32_16x16x64_bf16 v[124:127], v[116:119], v[192:199], v1\n"
    "ds_read_b128 v[64:67], v0 offset:3072\n"
    "v_smfmac_f32_16x16x64_bf16 v[128:131], v[116:119], v[200:207], v1\n"
    "ds_read_b128 v[68:71], v0 offset:7168\n"
    "s_waitcnt lgkmcnt(8)\n"
    "v_smfmac_f32_16x16x64_bf16 v[124:127], v[120:123], v[208:215], v1\n"
    "v_smfmac_f32_16x16x64_bf16 v[128:131], v[120:123], v[216:223], v1\n"
    "v_smfmac_f32_16x16x64_bf16 v[32:35], v[88:91], v[224:231], v1\n"
    "v_smfmac_f32_16x16x64_bf16 v[36:39], v[88:91], v[232:239], v1\n"
    "v_smfmac_f32_16x16x64_bf16 v[32:35], v[92:95], v[240:247], v1\n"
    "v_permlane32_swap_b32 v124, v126\n"
    "v_permlane32_swap_b32 v128, v130\n"
    "v_smfmac_f32_16x16x64_bf16 v[36:39], v[92:95], v[248:255], v1\n"
    "v_permlane32_swap_b32 v125, v127\n"
    "v_permlane32_swap_b32 v129, v131\n"
    "v_smfmac_f32_16x16x64_bf16 v[32:35], v[96:99], a[0:7], v1\n"
    "v_add_f32 v124, v124, v126\n"
    "v_add_f32 v128, v128, v130\n"
    "v_smfmac_f32_16x16x64_bf16 v[36:39], v[96:99], a[8:15], v1\n"
    "v_add_f32 v125, v125, v127\n"
    "v_add_f32 v129, v129, v131\n"
    "v_smfmac_f32_16x16x64_bf16 v[32:35], v[100:103], a[16:23], v1\n"
    "v_cvt_pk_bf16_f32 v76, v124, v128\n"
    "v_cvt_pk_bf16_f32 v77, v125, v129\n"
    "ds_write_b32 v3, v76 offset:9216\n"
    "ds_write_b32 v3, v77 offset:9488\n"
    "v_smfmac_f32_16x16x64_bf16 v[36:39], v[100:103], a[24:31], v1\n"
    "v_smfmac_f32_16x16x64_bf16 v[32:35], v[104:107], a[32:39], v1\n"
    "v_smfmac_f32_16x16x64_bf16 v[36:39], v[104:107], a[40:47], v1\n"
    "v_smfmac_f32_16x16x64_bf16 v[32:35], v[108:111], a[48:55], v1\n"
    "s_waitcnt lgkmcnt(0)\n"
    "s_barrier\n"
    "ds_read_b128 v[124:127], v4 offset:29696\n"
    "v_smfmac_f32_16x16x64_bf16 v[36:39], v[108:111], a[56:63], v1\n"
    "ds_read_b128 v[128:131], v4 offset:29760\n"
    "v_smfmac_f32_16x16x64_bf16 v[32:35], v[112:115], a[64:71], v1\n"
    "ds_read_b128 v[132:135], v4 offset:29824\n"
    "v_smfmac_f32_16x16x64_bf16 v[36:39], v[112:115], a[72:79], v1\n"
    "ds_read_b128 v[72:75], v4 offset:29888\n"
    "v_smfmac_f32_16x16x64_bf16 v[32:35], v[116:119], a[80:87], v1\n"
    "v_smfmac_f32_16x16x64_bf16 v[36:39], v[116:119], a[88:95], v1\n"
    "v_smfmac_f32_16x16x64_bf16 v[32:35], v[120:123], a[96:103], v1\n"
    "v_smfmac_f32_16x16x64_bf16 v[36:39], v[120:123], a[104:111], v1\n"
    "s_waitcnt lgkmcnt(3)\n"
    "v_mfma_f32_16x16x32_bf16 v[80:83], v[40:43], v[124:127], 0\n"
    "v_mfma_f32_16x16x32_bf16 v[84:87], v[44:47], v[124:127], 0\n"
    "ds_read_b128 v[40:43], v11 offset:31872\n"
    "ds_read_b128 v[44:47], v11 offset:31872\n"
    "s_waitcnt lgkmcnt(4)\n"
    "v_mfma_f32_16x16x32_bf16 v[80:83], v[48:51], v[128:131], v[80:83]\n"
    "v_permlane32_swap_b32 v32, v34\n"
    "v_permlane32_swap_b32 v36, v38\n"
    "v_mfma_f32_16x16x32_bf16 v[84:87], v[52:55], v[128:131], v[84:87]\n"
    "v_permlane32_swap_b32 v33, v35\n"
    "v_permlane32_swap_b32 v37, v39\n"
    "s_waitcnt lgkmcnt(3)\n"
    "v_mfma_f32_16x16x32_bf16 v[80:83], v[56:59], v[132:135], v[80:83]\n"
    "v_add_f32 v32, v32, v34\n"
    "v_add_f32 v36, v36, v38\n"
    "v_mfma_f32_16x16x32_bf16 v[84:87], v[60:63], v[132:135], v[84:87]\n"
    "v_add_f32 v33, v33, v35\n"
    "v_add_f32 v37, v37, v39\n"
    "s_waitcnt lgkmcnt(2)\n"
    "v_mfma_f32_16x16x32_bf16 v[80:83], v[64:67], v[72:75], v[80:83]\n"
    "v_cvt_pk_bf16_f32 v76, v32, v36\n"
    "v_cvt_pk_bf16_f32 v77, v33, v37\n"
    "ds_write2_b32 v3, v76, v77 offset1:68\n"
    "v_mfma_f32_16x16x32_bf16 v[84:87], v[68:71], v[72:75], v[84:87]\n"
    "s_waitcnt lgkmcnt(2)\n"
    "v_smfmac_f32_16x16x64_bf16 v[40:43], v[88:91], a[112:119], v1\n"
    "s_waitcnt lgkmcnt(1)\n"
    "v_smfmac_f32_16x16x64_bf16 v[44:47], v[88:91], a[120:127], v1\n"
    "s_waitcnt lgkmcnt(0)\n"
    "s_barrier\n"
    "ds_read_b128 v[48:51], v4 offset:20480\n"
    "ds_read_b128 v[52:55], v4 offset:20544\n"
    "ds_read_b128 v[56:59], v4 offset:20608\n"
    "ds_read_b128 v[60:63], v4 offset:20672\n"
    "v_smfmac_f32_16x16x64_bf16 v[40:43], v[92:95], a[128:135], v1\n"
    "v_maximum3_f32 v78, v80, v81, v82\n"
    "v_maximum3_f32 v78, v78, v83, v84\n"
    "v_smfmac_f32_16x16x64_bf16 v[44:47], v[92:95], a[136:143], v1\n"
    "v_maximum3_f32 v78, v78, v85, v86\n"
    "v_maximum3_f32 v78, v78, v87, v87\n"
    "ds_read_b128 v[32:35], v5 offset:8192\n"
    "ds_read_b128 v[36:39], v5 offset:12288\n"
    "v_smfmac_f32_16x16x64_bf16 v[40:43], v[96:99], a[144:151], v1\n"
    "v_mov_b32 v79, v78\n"
    "ds_read_b128 v[68:71], v11 offset:31888\n"
    "ds_read_b64 v[76:77], v11 offset:31904\n"
    "v_smfmac_f32_16x16x64_bf16 v[44:47], v[96:99], a[152:159], v1\n"
    "v_permlane32_swap_b32 v79, v78\n"
    "v_maximum3_f32 v78, v78, v79, v79\n"
    "s_waitcnt lgkmcnt(7)\n"
    "v_mfma_f32_16x16x32_bf16 v[64:67], v[48:51], v[124:127], 0\n"
    "ds_read_b128 v[88:91], v2 offset:14336\n"
    "ds_read_b128 v[92:95], v2 offset:15360\n"
    "s_waitcnt lgkmcnt(8)\n"
    "v_mfma_f32_16x16x32_bf16 v[64:67], v[52:55], v[128:131], v[64:67]\n"
    "s_waitcnt lgkmcnt(7)\n"
    "v_mfma_f32_16x16x32_bf16 v[64:67], v[56:59], v[132:135], v[64:67]\n"
    "s_waitcnt lgkmcnt(6)\n"
    "v_mfma_f32_16x16x32_bf16 v[64:67], v[60:63], v[72:75], v[64:67]\n"
    "v_smfmac_f32_16x16x64_bf16 v[40:43], v[100:103], a[160:167], v1\n"
    "ds_read_b128 v[48:51], v2 offset:18432\n"
    "ds_read_b128 v[52:55], v2 offset:19456\n"
    "v_smfmac_f32_16x16x64_bf16 v[44:47], v[100:103], a[168:175], v1\n"
    "ds_read_b128 v[56:59], v2 offset:20480\n"
    "ds_read_b128 v[60:63], v2 offset:21504\n"
    "v_maximum3_f32 v79, v64, v65, v66\n"
    "v_maximum3_f32 v78, v79, v67, v78\n"
    "v_mov_b32 v79, v78\n"
    "v_smfmac_f32_16x16x64_bf16 v[40:43], v[104:107], a[176:183], v1\n"
    "ds_read_b128 v[96:99], v2 offset:16384\n"
    "ds_read_b128 v[100:103], v2 offset:17408\n"
    "v_permlane16_swap_b32 v79, v78\n"
    "v_maximum3_f32 v78, v78, v79, v79\n"
    "v_mul_f32 v78, 0xbe0293ee, v78\n"
    "v_smfmac_f32_16x16x64_bf16 v[44:47], v[104:107], a[184:191], v1\n"
    "v_fmamk_f32 v80, v80, 0x3e0293ee, v78\n"
    "v_fmamk_f32 v81, v81, 0x3e0293ee, v78\n"
    "v_exp_f32 v80, v80\n"
    "v_exp_f32 v81, v81\n"
    "ds_read_b128 v[124:127], v2 offset:22528\n"
    "ds_read_b128 v[128:131], v2 offset:23552\n"
    "v_smfmac_f32_16x16x64_bf16 v[40:43], v[108:111], a[192:199], v1\n"
    "v_fmamk_f32 v82, v82, 0x3e0293ee, v78\n"
    "v_fmamk_f32 v83, v83, 0x3e0293ee, v78\n"
    "v_exp_f32 v82, v82\n"
    "v_exp_f32 v83, v83\n"
    "v_cvt_pk_bf16_f32 v80, v80, v81\n"
    "v_smfmac_f32_16x16x64_bf16 v[44:47], v[108:111], a[200:207], v1\n"
    "v_fmamk_f32 v84, v84, 0x3e0293ee, v78\n"
    "v_fmamk_f32 v85, v85, 0x3e0293ee, v78\n"
    "v_exp_f32 v84, v84\n"
    "v_exp_f32 v85, v85\n"
    "v_cvt_pk_bf16_f32 v81, v82, v83\n"
    "v_smfmac_f32_16x16x64_bf16 v[40:43], v[112:115], a[208:215], v1\n"
    "v_fmamk_f32 v86, v86, 0x3e0293ee, v78\n"
    "v_fmamk_f32 v87, v87, 0x3e0293ee, v78\n"
    "v_exp_f32 v86, v86\n"
    "v_exp_f32 v87, v87\n"
    "v_cvt_pk_bf16_f32 v82, v84, v85\n"
    "ds_read_b128 v[104:107], v2 offset:24576\n"
    "ds_read_b128 v[108:111], v2 offset:25600\n"
    "v_smfmac_f32_16x16x64_bf16 v[44:47], v[112:115], a[216:223], v1\n"
    "v_fmamk_f32 v64, v64, 0x3e0293ee, v78\n"
    "v_fmamk_f32 v65, v65, 0x3e0293ee, v78\n"
    "v_exp_f32 v64, v64\n"
    "v_exp_f32 v65, v65\n"
    "v_cvt_pk_bf16_f32 v83, v86, v87\n"
    "v_smfmac_f32_16x16x64_bf16 v[40:43], v[116:119], a[224:231], v1\n"
    "v_fmamk_f32 v66, v66, 0x3e0293ee, v78\n"
    "v_fmamk_f32 v67, v67, 0x3e0293ee, v78\n"
    "v_exp_f32 v66, v66\n"
    "v_exp_f32 v67, v67\n"
    "v_cvt_pk_bf16_f32 v64, v64, v65\n"
    "v_smfmac_f32_16x16x64_bf16 v[44:47], v[116:119], a[232:239], v1\n"
    "v_cvt_pk_bf16_f32 v65, v66, v67\n"
    "v_smfmac_f32_16x16x64_bf16 v[40:43], v[120:123], a[240:247], v1\n"
    "ds_read_b128 v[112:115], v2 offset:26624\n"
    "ds_read_b128 v[116:119], v2 offset:27648\n"
    "v_smfmac_f32_16x16x64_bf16 v[44:47], v[120:123], a[248:255], v1\n"
    "s_waitcnt lgkmcnt(14)\n"
    "v_mfma_f32_16x16x32_bf16 v[72:75], v[68:71], v[80:83], 0\n"
    "v_mfma_f32_16x16x32_bf16 v[84:87], v[32:35], v[80:83], 0\n"
    "ds_read_b128 v[120:123], v11 offset:31872\n"
    "ds_read_b128 v[68:71], v11 offset:31872\n"
    "v_mfma_f32_16x16x32_bf16 v[132:135], v[36:39], v[80:83], 0\n"
    "ds_read_b128 v[32:35], v2 offset:28672\n"
    "ds_read_b128 v[36:39], v2 offset:29696\n"
    "v_cvt_pk_bf16_f32 v40, v40, v41\n"
    "v_cvt_pk_bf16_f32 v41, v42, v43\n"
    "v_mfma_f32_16x16x16_bf16 v[72:75], v[76:77], v[64:65], v[72:75]\n"
    "v_cvt_pk_bf16_f32 v42, v44, v45\n"
    "v_cvt_pk_bf16_f32 v43, v46, v47\n"
    "v_mfma_f32_16x16x16_bf16 v[84:87], v[40:41], v[64:65], v[84:87]\n"
    "ds_read_b128 v[80:83], v11 offset:31872\n"
    "ds_read_b128 v[44:47], v11 offset:31872\n"
    "v_mfma_f32_16x16x16_bf16 v[132:135], v[42:43], v[64:65], v[132:135]\n"
    "ds_write_b128 v5, v[40:43] offset:22784\n"
    "ds_read_b128 v[64:67], v11 offset:31872\n"
    "s_and_b32 s49, s48, 1\n"
    "s_lshl_b32 s70, s49, 3\n"
    "v_rcp_f32 v78, v72\n"
    "s_nop 0\n"
    "v_pk_mul_f32 v[84:85], v[84:85], v[78:79] op_sel_hi:[1,0]\n"
    "v_pk_mul_f32 v[86:87], v[86:87], v[78:79] op_sel_hi:[1,0]\n"
    "s_nop 0\n"
    "v_pk_mul_f32 v[132:133], v[132:133], v[78:79] op_sel_hi:[1,0]\n"
    "v_pk_mul_f32 v[134:135], v[134:135], v[78:79] op_sel_hi:[1,0]\n"
    "v_cvt_pk_bf16_f32 v84, v84, v132\n"
    "v_cvt_pk_bf16_f32 v85, v85, v133\n"
    "v_cvt_pk_bf16_f32 v86, v86, v134\n"
    "v_cvt_pk_bf16_f32 v87, v87, v135\n"
    "ds_write_b128 v6, v[84:87] offset:26880\n"
    "s_cmp_eq_u32 s48, 63\n"
    "s_waitcnt lgkmcnt(0)\n"
    "s_barrier\n"
    "s_cbranch_scc1 .Lend_%=\n"
    "ds_read_b128 v[132:135], v0 offset:26880\n"
    "ds_read_b128 v[72:75], v0 offset:27904\n"
    "s_cmp_lt_u32 %6, 2\n"
    "s_waitcnt lgkmcnt(1)\n"
    "v_smfmac_f32_16x16x64_bf16 v[68:71], v[132:135], v[88:95], v1\n"
    "v_smfmac_f32_16x16x64_bf16 v[80:83], v[132:135], v[96:103], v1\n"
    "ds_read_b128 v[88:91], v2 offset:30720\n"
    "ds_read_b128 v[92:95], v2 offset:31744\n"
    "s_waitcnt lgkmcnt(2)\n"
    "v_smfmac_f32_16x16x64_bf16 v[68:71], v[72:75], v[48:55], v1\n"
    "ds_read_b128 v[96:99], v2 offset:32768\n"
    "ds_read_b128 v[100:103], v2 offset:33792\n"
    "v_smfmac_f32_16x16x64_bf16 v[80:83], v[72:75], v[56:63], v1\n"
    "v_smfmac_f32_16x16x64_bf16 v[120:123], v[132:135], v[124:131], v1\n"
    "v_smfmac_f32_16x16x64_bf16 v[44:47], v[132:135], v[104:111], v1\n"
    "v_smfmac_f32_16x16x64_bf16 v[120:123], v[72:75], v[112:119], v1\n"
    "v_permlane32_swap_b32 v68, v80\n"
    "v_permlane32_swap_b32 v69, v81\n"
    "v_smfmac_f32_16x16x64_bf16 v[44:47], v[72:75], v[32:39], v1\n"
    "s_waitcnt lgkmcnt(0)\n"
    "s_cbranch_scc1 .Lx1_%=\n"
    "v_smfmac_f32_16x16x64_bf16 v[64:67], v[132:135], v[88:95], v1\n"
    "v_smfmac_f32_16x16x64_bf16 v[64:67], v[72:75], v[96:103], v1\n"
    ".Lx1_%=:\n"
    "v_permlane32_swap_b32 v70, v82\n"
    "v_permlane32_swap_b32 v71, v83\n"
    "v_pk_add_f32 v[68:69], v[68:69], v[80:81]\n"
    "v_pk_add_f32 v[70:71], v[70:71], v[82:83]\n"
    "v_bfi_b32 v68, 15, s49, v68\n"
    "v_bfi_b32 v70, 15, s49, v70\n"
    "buffer_store_dwordx4 v[68:71], v7, s[40:43], 0 offen sc1\n"
    "v_permlane32_swap_b32 v120, v44\n"
    "v_permlane32_swap_b32 v121, v45\n"
    "v_permlane32_swap_b32 v122, v46\n"
    "v_permlane32_swap_b32 v123, v47\n"
    "v_pk_add_f32 v[120:121], v[120:121], v[44:45]\n"
    "v_pk_add_f32 v[122:123], v[122:123], v[46:47]\n"
    "v_bfi_b32 v120, 15, s49, v120\n"
    "v_bfi_b32 v122, 15, s49, v122\n"
    "buffer_store_dwordx4 v[120:123], v7, s[40:43], s74 offen sc1\n"
    "s_cbranch_scc1 .Lx2_%=\n"
    "v_permlane32_swap_b32 v64, v66\n"
    "v_permlane32_swap_b32 v65, v67\n"
    "v_pk_add_f32 v[64:65], v[64:65], v[66:67]\n"
    "v_bfi_b32 v64, 15, s49, v64\n"
    "buffer_store_dwordx2 v[64:65], v8, s[40:43], 0 offen sc1\n"
    ".Lx2_%=:\n"
    "s_memtime s[50:51]\n"
    "s_waitcnt lgkmcnt(0)\n"
    "s_mov_b32 s52, s50\n"
    "s_and_b32 s73, s48, 3\n"
    "s_cmp_eq_u32 s73, 3\n"
    "s_cselect_b32 s58, 96, 0\n"
    "s_cmp_eq_u32 %6, 3\n"
    "s_cbranch_scc1 .Lw3_%=\n"
    "ds_read_b128 v[32:35], v2\n"
    "ds_read_b128 v[36:39], v2 offset:1024\n"
    "ds_read_b128 v[40:43], v2 offset:2048\n"
    "ds_read_b128 v[44:47], v2 offset:3072\n"
    "ds_read_b128 v[48:51], v2 offset:4096\n"
    "ds_read_b128 v[52:55], v2 offset:5120\n"
    "ds_read_b128 v[56:59], v2 offset:6144\n"
    "ds_read_b128 v[60:63], v2 offset:7168\n"
    "ds_read_b128 v[64:67], v2 offset:8192\n"
    "ds_read_b128 v[68:71], v2 offset:9216\n"
    "ds_read_b128 v[72:75], v2 offset:10240\n"
    "ds_read_b128 v[76:79], v2 offset:11264\n"
    "ds_read_b128 v[80:83], v2 offset:12288\n"
    "ds_read_b128 v[84:87], v2 offset:13312\n"
    "v_mov_b64 v[124:125], 0\n"
    "v_mov_b64 v[126:127], 0\n"
    "v_mov_b64 v[128:129], 0\n"
    "v_mov_b64 v[130:131], 0\n"
    "s_mul_i32 s73, s48, 3\n"
    "s_add_u32 s73, s73, s94\n"
    "s_and_b32 s51, s73, 1\n"
    "s_lshl_b32 s51, s51, 12\n"
    "s_lshr_b32 s73, s73, 1\n"
    "s_lshl_b32 s73, s73, 14\n"
    "s_add_u32 s73, s73, s51\n"
    "s_add_u32 s66, s73, s68\n"
    "s_mul_i32 s73, s48, 3\n"
    "s_add_u32 s73, s73, %6\n"
    "s_and_b32 s51, s73, 1\n"
    "s_lshl_b32 s51, s51, 12\n"
    "s_lshr_b32 s73, s73, 1\n"
    "s_lshl_b32 s73, s73, 14\n"
    "s_add_u32 s73, s73, s51\n"
    "s_add_u32 s67, s73, s68\n"
    "s_waitcnt lgkmcnt(0)\n"
    "s_cmp_lt_i32 s71, 0\n"
    "s_cbranch_scc1 .Lnored_%=\n"
    "s_mov_b32 s56, 0\n"
    "s_sub_u32 s53, s54, s58\n"
    "s_waitcnt lgkmcnt(0)\n"
    ".Ltl4_%=:\n"
    "s_memtime s[50:51]\n"
    "s_waitcnt lgkmcnt(0)\n"
    "s_sub_u32 s50, s50, s52\n"
    "s_cmp_lt_u32 s50, s53\n"
    "s_cbranch_scc1 .Ltl4_%=\n"
    ".Lred_%=:\n"
    "buffer_load_dwordx4 v[88:91], v0, s[40:43], s65 offen sc1\n"
    "buffer_load_dwordx4 v[92:95], v0, s[40:43], s65 offen offset:1024 sc1\n"
    "buffer_load_dwordx4 v[96:99], v0, s[40:43], s65 offen offset:2048 sc1\n"
    "buffer_load_dwordx4 v[100:103], v0, s[40:43], s65 offen offset:3072 sc1\n"
    "s_waitcnt vmcnt(2)\n"
    "v_add3_u32 v104, v88, v90, v92\n"
    "v_pk_add_f32 v[108:109], v[88:89], v[92:93]\n"
    "v_pk_add_f32 v[110:111], v[90:91], v[94:95]\n"
    "s_waitcnt vmcnt(1)\n"
    "v_add3_u32 v104, v104, v94, v96\n"
    "v_pk_add_f32 v[108:109], v[108:109], v[96:97]\n"
    "v_pk_add_f32 v[110:111], v[110:111], v[98:99]\n"
    "s_waitcnt vmcnt(0)\n"
    "v_add3_u32 v104, v104, v98, v100\n"
    "v_pk_add_f32 v[108:109], v[108:109], v[100:101]\n"
    "v_pk_add_f32 v[110:111], v[110:111], v[102:103]\n"
    "v_add_u32 v104, v104, v102\n"
    "v_and_b32 v104, 15, v104\n"
    "v_permlane32_swap_b32 v108, v110\n"
    "v_permlane32_swap_b32 v109, v111\n"
    "v_cmp_ne_u32 vcc, s70, v104\n"
    "v_pk_add_f32 v[108:109], v[108:109], v[110:111]\n"
    "s_cbranch_vccz .Lredok_%=\n"
    "s_add_u32 s56, s56, 1\n"
    "s_cmp_lt_u32 s56, 3\n"
    "s_cbranch_scc1 .Lred_%=\n"
    "s_sleep 8\n"
    "s_cmp_lt_u32 s56, s57\n"
    "s_cbranch_scc1 .Lred_%=\n"
    "s_mov_b32 s57, 0\n"
    "v_mov_b32 v135, 1\n"
    "global_store_dword v11, v135, %1 sc1\n"
    ".Lredok_%=:\n"
    "s_nop 0\n"
    "v_permlane16_swap_b32 v108, v109\n"
    "v_add_f32 v108, v108, v109\n"
    "s_nop 1\n"
    "v_add_f32_dpp v108, v108, v108 row_ror:8 row_mask:0xf bank_mask:0xf\n"
    "s_nop 1\n"
    "v_mov_b32_dpp v109, v108 quad_perm:[1,0,3,2] row_mask:0xf bank_mask:0xf\n"
    "v_cvt_pk_bf16_f32 v108, v108, v109\n"
    "v_min_u32 v108, -2, v108\n"
    "s_nop 1\n"
    "v_mov_b32_dpp v109, v108 row_shl:2 row_mask:0xf bank_mask:0xf\n"
    "v_mov_b32_dpp v110, v108 row_shl:4 row_mask:0xf bank_mask:0xf\n"
    "v_mov_b32_dpp v111, v108 row_shl:6 row_mask:0xf bank_mask:0xf\n"
    "buffer_store_dwordx4 v[108:111], v9, s[40:43], s66 offen sc1\n"
    "s_cmp_eq_u32 s56, 0\n"
    "s_cbranch_scc0 .Lad5_a%=\n"
    "s_cmp_gt_u32 s54, 300\n"
    "s_cselect_b32 s73, s58, 0\n"
    "s_sub_u32 s54, s54, s73\n"
    "s_branch .Lad5_x%=\n"
    ".Lad5_a%=:\n"
    "s_add_u32 s73, s56, s58\n"
    "s_cmp_le_u32 s73, 2\n"
    "s_cselect_b32 s73, 96, 0\n"
    "s_add_u32 s54, s54, s73\n"
    "s_min_u32 s54, s54, 6000\n"
    ".Lad5_x%=:\n"
    ".Lnored_%=:\n"
    "s_waitcnt vmcnt(1)\n"
    "s_cmp_lg_u32 s59, 0\n"
    "s_cbranch_scc1 .Lkn6_%=\n"
    "s_waitcnt lgkmcnt(0)\n"
    "s_add_u32 s73, s48, 1\n"
    "s_and_b32 s73, s73, 63\n"
    "s_lshl_b32 s73, s73, 2\n"
    "s_add_u32 s73, s73, s92\n"
    "s_mov_b32 s53, s73\n"
    "s_mov_b32 s56, 0\n"
    "v_mov_b32 v134, s53\n"
    ".Lpoll7_%=:\n"
    "global_load_dword v135, v134, %1 sc1\n"
    "s_waitcnt vmcnt(0)\n"
    "v_readfirstlane_b32 s73, v135\n"
    "s_cmp_ge_u32 s73, 1\n"
    "s_cbranch_scc1 .Lpoll7_x%=\n"
    "s_add_u32 s56, s56, 1\n"
    "s_cmp_lt_u32 s56, 3\n"
    "s_cbranch_scc1 .Lpoll7_%=\n"
    "s_sleep 8\n"
    "s_cmp_lt_u32 s56, s57\n"
    "s_cbranch_scc1 .Lpoll7_%=\n"
    "s_mov_b32 s57, 0\n"
    "v_mov_b32 v135, 1\n"
    "global_store_dword v11, v135, %1 sc1\n"
    ".Lpoll7_x%=:\n"
    "buffer_load_dwordx4 v[12:15], v5, s[40:43], s60 offen sc1\n"
    "buffer_load_dwordx4 v[16:19], v5, s[40:43], s61 offen sc1\n"
    "buffer_load_dwordx4 v[20:23], v5, s[40:43], s62 offen sc1\n"
    "buffer_load_dwordx4 v[24:27], v5, s[40:43], s63 offen sc1\n"
    "buffer_load_dwordx4 v[28:31], v5, s[40:43], s64 offen sc1\n"
    "s_waitcnt vmcnt(0)\n"
    ".Lkn6_%=:\n"
    "s_add_u32 s73, s48, 2\n"
    "s_and_b32 s73, s73, 63\n"
    "s_lshl_b32 s73, s73, 2\n"
    "s_add_u32 s73, s73, s92\n"
    "v_mov_b32 v134, s73\n"
    "global_load_dword v133, v134, %1 sc1\n"
    "s_add_u32 s73, s48, 2\n"
    "s_and_b32 s73, s73, 63\n"
    "s_lshr_b32 s51, s73, 2\n"
    "s_xor_b32 s51, s51, s69\n"
    "s_and_b32 s51, s51, 1\n"
    "s_lshl_b32 s51, s51, 13\n"
    "s_lshl_b32 s73, s73, 5\n"
    "s_add_u32 s73, s73, %7\n"
    "s_lshl_b32 s73, s73, 14\n"
    "s_add_u32 s73, s73, s51\n"
    "s_add_u32 s60, s73, 0x6000000\n"
    "s_add_u32 s61, s73, 0x6001000\n"
    "s_add_u32 s62, s73, 0x8000000\n"
    "s_add_u32 s63, s73, 0x8001000\n"
    "s_lshl_b32 s51, %8, 12\n"
    "s_add_u32 s73, s73, s51\n"
    "s_add_u32 s64, s73, 0x4000000\n"
    "s_mov_b32 s56, 0\n"
    "s_sub_u32 s53, s55, s58\n"
    "s_waitcnt lgkmcnt(0)\n"
    ".Ltl8_%=:\n"
    "s_memtime s[50:51]\n"
    "s_waitcnt lgkmcnt(0)\n"
    "s_sub_u32 s50, s50, s52\n"
    "s_cmp_lt_u32 s50, s53\n"
    "s_cbranch_scc1 .Ltl8_%=\n"
    "buffer_load_dwordx4 v[88:91], v0, s[40:43], s67 offen sc1\n"
    "buffer_load_dwordx4 v[92:95], v0, s[40:43], s67 offen offset:1024 sc1\n"
    "buffer_load_dwordx4 v[96:99], v0, s[40:43], s67 offen offset:2048 sc1\n"
    "s_waitcnt lgkmcnt(0)\n"
    "s_barrier\n"
    "ds_write_b128 v5, v[12:15]\n"
    "ds_write_b128 v5, v[16:19] offset:4096\n"
    "ds_write_b128 v5, v[20:23] offset:8192\n"
    "ds_write_b128 v5, v[24:27] offset:12288\n"
    "ds_write_b128 v5, v[28:31] offset:16384\n"
    "s_waitcnt vmcnt(3)\n"
    "v_readfirstlane_b32 s59, v133\n"
    ".Lgchk_%=:\n"
    "s_waitcnt vmcnt(1)\n"
    "v_max3_u32 v100, v88, v90, v92\n"
    "ds_write_b128 v10, v[88:91] offset:20480\n"
    "ds_write_b128 v10, v[92:95] offset:21504\n"
    "s_waitcnt vmcnt(0)\n"
    "v_max3_u32 v100, v100, v94, v96\n"
    "ds_write_b128 v10, v[96:99] offset:22528\n"
    "v_max_u32 v100, v100, v98\n"
    "v_cmp_eq_u32 vcc, -1, v100\n"
    "s_cbranch_vccz .Lgok_%=\n"
    "buffer_load_dwordx4 v[88:91], v0, s[40:43], s67 offen sc1\n"
    "buffer_load_dwordx4 v[92:95], v0, s[40:43], s67 offen offset:1024 sc1\n"
    "buffer_load_dwordx4 v[96:99], v0, s[40:43], s67 offen offset:2048 sc1\n"
    "s_add_u32 s56, s56, 1\n"
    "s_cmp_lt_u32 s56, 3\n"
    "s_cbranch_scc1 .Lgchk_%=\n"
    "s_sleep 8\n"
    "s_cmp_lt_u32 s56, s57\n"
    "s_cbranch_scc1 .Lgchk_%=\n"
    "s_mov_b32 s57, 0\n"
    "v_mov_b32 v135, 1\n"
    "global_store_dword v11, v135, %1 sc1\n"
    "s_waitcnt vmcnt(0)\n"
    ".Lgok_%=:\n"
    "s_cmp_eq_u32 s56, 0\n"
    "s_cbranch_scc0 .Lad9_a%=\n"
    "s_cmp_gt_u32 s55, 900\n"
    "s_cselect_b32 s73, s58, 0\n"
    "s_sub_u32 s55, s55, s73\n"
    "s_branch .Lad9_x%=\n"
    ".Lad9_a%=:\n"
    "s_add_u32 s73, s56, s58\n"
    "s_cmp_le_u32 s73, 2\n"
    "s_cselect_b32 s73, 96, 0\n"
    "s_add_u32 s55, s55, s73\n"
    "s_min_u32 s55, s55, 6000\n"
    ".Lad9_x%=:\n"
    "s_add_u32 s48, s48, 1\n"
    "s_waitcnt lgkmcnt(0)\n"
    "s_barrier\n"
    "s_branch .Lstep_%=\n"
    ".Lw3_%=:\n"
    "ds_read_b128 v[32:35], v0 offset:16384\n"
    "ds_read_b128 v[48:51], v0\n"
    "ds_read_b128 v[64:67], v0 offset:4096\n"
    "ds_read_b128 v[80:83], v4 offset:20480\n"
    "ds_read_b128 v[36:39], v0 offset:17408\n"
    "ds_read_b128 v[52:55], v0 offset:1024\n"
    "ds_read_b128 v[68:71], v0 offset:5120\n"
    "ds_read_b128 v[84:87], v4 offset:20544\n"
    "ds_read_b128 v[40:43], v0 offset:18432\n"
    "ds_read_b128 v[56:59], v0 offset:2048\n"
    "ds_read_b128 v[72:75], v0 offset:6144\n"
    "ds_read_b128 v[88:91], v4 offset:20608\n"
    "ds_read_b128 v[44:47], v0 offset:19456\n"
    "ds_read_b128 v[60:63], v0 offset:3072\n"
    "ds_read_b128 v[76:79], v0 offset:7168\n"
    "ds_read_b128 v[92:95], v4 offset:20672\n"
    "s_waitcnt lgkmcnt(14)\n"
    "v_mfma_f32_16x16x32_bf16 v[96:99], v[48:51], v[32:35], 0\n"
    "s_waitcnt lgkmcnt(13)\n"
    "v_mfma_f32_16x16x32_bf16 v[100:103], v[64:67], v[32:35], 0\n"
    "s_waitcnt lgkmcnt(12)\n"
    "v_mfma_f32_16x16x32_bf16 v[104:107], v[80:83], v[32:35], 0\n"
    "s_waitcnt lgkmcnt(10)\n"
    "v_mfma_f32_16x16x32_bf16 v[96:99], v[52:55], v[36:39], v[96:99]\n"
    "s_waitcnt lgkmcnt(9)\n"
    "v_mfma_f32_16x16x32_bf16 v[100:103], v[68:71], v[36:39], v[100:103]\n"
    "s_waitcnt lgkmcnt(8)\n"
    "v_mfma_f32_16x16x32_bf16 v[104:107], v[84:87], v[36:39], v[104:107]\n"
    "s_waitcnt lgkmcnt(6)\n"
    "v_mfma_f32_16x16x32_bf16 v[96:99], v[56:59], v[40:43], v[96:99]\n"
    "s_waitcnt lgkmcnt(5)\n"
    "v_mfma_f32_16x16x32_bf16 v[100:103], v[72:75], v[40:43], v[100:103]\n"
    "s_waitcnt lgkmcnt(4)\n"
    "v_mfma_f32_16x16x32_bf16 v[104:107], v[88:91], v[40:43], v[104:107]\n"
    "s_waitcnt lgkmcnt(2)\n"
    "v_mfma_f32_16x16x32_bf16 v[96:99], v[60:63], v[44:47], v[96:99]\n"
    "s_waitcnt lgkmcnt(1)\n"
    "v_mfma_f32_16x16x32_bf16 v[100:103], v[76:79], v[44:47], v[100:103]\n"
    "s_waitcnt lgkmcnt(0)\n"
    "v_mfma_f32_16x16x32_bf16 v[104:107], v[92:95], v[44:47], v[104:107]\n"
    "ds_read_b128 v[32:35], v0 offset:8192\n"
    "ds_read_b128 v[36:39], v0 offset:12288\n"
    "ds_read_b128 v[40:43], v0 offset:9216\n"
    "ds_read_b128 v[44:47], v0 offset:13312\n"
    "ds_read_b128 v[48:51], v0 offset:10240\n"
    "ds_read_b128 v[52:55], v0 offset:14336\n"
    "ds_read_b128 v[56:59], v0 offset:11264\n"
    "ds_read_b128 v[60:63], v0 offset:15360\n"
    "ds_read_b128 v[64:67], v0 offset:22784\n"
    "ds_read_b128 v[68:71], v0 offset:23808\n"
    "ds_read_b128 v[72:75], v0 offset:24832\n"
    "ds_read_b128 v[76:79], v0 offset:25856\n"
    "ds_read_b128 v[80:83], v11 offset:31888\n"
    "ds_read_b64 v[84:85], v11 offset:31904\n"
    "v_mov_b32 v86, 0xd01502f9\n"
    "v_cndmask_b32 v96, v86, v96, s[76:77]\n"
    "v_cndmask_b32 v97, v86, v97, s[78:79]\n"
    "v_cndmask_b32 v98, v86, v98, s[80:81]\n"
    "v_cndmask_b32 v99, v86, v99, s[82:83]\n"
    "v_cndmask_b32 v100, v86, v100, s[84:85]\n"
    "v_cndmask_b32 v101, v86, v101, s[86:87]\n"
    "v_cndmask_b32 v102, v86, v102, s[88:89]\n"
    "v_cndmask_b32 v103, v86, v103, s[90:91]\n"
    "v_maximum3_f32 v87, v104, v105, v106\n"
    "v_maximum3_f32 v87, v87, v107, v96\n"
    "v_maximum3_f32 v87, v87, v97, v98\n"
    "v_maximum3_f32 v87, v87, v99, v100\n"
    "v_maximum3_f32 v87, v87, v101, v102\n"
    "v_maximum3_f32 v87, v87, v103, v103\n"
    "v_mov_b32 v86, v87\n"
    "s_nop 1\n"
    "v_permlane16_swap_b32 v86, v87\n"
    "v_maximum3_f32 v87, v87, v86, v86\n"
    "v_mov_b32 v86, v87\n"
    "s_nop 1\n"
    "v_permlane32_swap_b32 v86, v87\n"
    "v_maximum3_f32 v87, v87, v86, v86\n"
    "v_mul_f32 v87, 0xbe0293ee, v87\n"
    "v_fmamk_f32 v96, v96, 0x3e0293ee, v87\n"
    "v_fmamk_f32 v97, v97, 0x3e0293ee, v87\n"
    "v_fmamk_f32 v98, v98, 0x3e0293ee, v87\n"
    "v_fmamk_f32 v99, v99, 0x3e0293ee, v87\n"
    "v_fmamk_f32 v100, v100, 0x3e0293ee, v87\n"
    "v_fmamk_f32 v101, v101, 0x3e0293ee, v87\n"
    "v_fmamk_f32 v102, v102, 0x3e0293ee, v87\n"
    "v_fmamk_f32 v103, v103, 0x3e0293ee, v87\n"
    "v_fmamk_f32 v104, v104, 0x3e0293ee, v87\n"
    "v_fmamk_f32 v105, v105, 0x3e0293ee, v87\n"
    "v_fmamk_f32 v106, v106, 0x3e0293ee, v87\n"
    "v_fmamk_f32 v107, v107, 0x3e0293ee, v87\n"
    "v_exp_f32 v96, v96\n"
    "v_exp_f32 v97, v97\n"
    "v_exp_f32 v98, v98\n"
    "v_exp_f32 v99, v99\n"
    "v_exp_f32 v100, v100\n"
    "v_exp_f32 v101, v101\n"
    "v_exp_f32 v102, v102\n"
    "v_exp_f32 v103, v103\n"
    "v_exp_f32 v104, v104\n"
    "v_exp_f32 v105, v105\n"
    "v_exp_f32 v106, v106\n"
    "v_exp_f32 v107, v107\n"
    "v_cvt_pk_bf16_f32 v96, v96, v97\n"
    "v_cvt_pk_bf16_f32 v97, v98, v99\n"
    "v_cvt_pk_bf16_f32 v98, v100, v101\n"
    "v_cvt_pk_bf16_f32 v99, v102, v103\n"
    "v_cvt_pk_bf16_f32 v104, v104, v105\n"
    "v_cvt_pk_bf16_f32 v105, v106, v107\n"
    "s_waitcnt lgkmcnt(1)\n"
    "v_mfma_f32_16x16x32_bf16 v[108:111], v[80:83], v[96:99], 0\n"
    "v_mfma_f32_16x16x32_bf16 v[88:91], v[32:35], v[96:99], 0\n"
    "v_mfma_f32_16x16x32_bf16 v[120:123], v[36:39], v[96:99], 0\n"
    "v_mfma_f32_16x16x32_bf16 v[92:95], v[40:43], v[96:99], 0\n"
    "s_waitcnt lgkmcnt(0)\n"
    "v_mfma_f32_16x16x16_bf16 v[108:111], v[84:85], v[104:105], v[108:111]\n"
    "v_mfma_f32_16x16x32_bf16 v[124:127], v[44:47], v[96:99], 0\n"
    "v_mfma_f32_16x16x32_bf16 v[112:115], v[48:51], v[96:99], 0\n"
    "v_mfma_f32_16x16x32_bf16 v[128:131], v[52:55], v[96:99], 0\n"
    "v_mfma_f32_16x16x32_bf16 v[116:119], v[56:59], v[96:99], 0\n"
    "v_mfma_f32_16x16x32_bf16 v[132:135], v[60:63], v[96:99], 0\n"
    "v_rcp_f32 v86, v108\n"
    "v_mfma_f32_16x16x16_bf16 v[88:91], v[64:65], v[104:105], v[88:91]\n"
    "v_mfma_f32_16x16x16_bf16 v[120:123], v[66:67], v[104:105], v[120:123]\n"
    "v_mfma_f32_16x16x16_bf16 v[92:95], v[68:69], v[104:105], v[92:95]\n"
    "v_mfma_f32_16x16x16_bf16 v[124:127], v[70:71], v[104:105], v[124:127]\n"
    "v_mfma_f32_16x16x16_bf16 v[112:115], v[72:73], v[104:105], v[112:115]\n"
    "v_mfma_f32_16x16x16_bf16 v[128:131], v[74:75], v[104:105], v[128:131]\n"
    "v_mfma_f32_16x16x16_bf16 v[116:119], v[76:77], v[104:105], v[116:119]\n"
    "v_mfma_f32_16x16x16_bf16 v[132:135], v[78:79], v[104:105], v[132:135]\n"
    "v_pk_mul_f32 v[88:89], v[88:89], v[86:87] op_sel_hi:[1,0]\n"
    "v_pk_mul_f32 v[90:91], v[90:91], v[86:87] op_sel_hi:[1,0]\n"
    "v_pk_mul_f32 v[120:121], v[120:121], v[86:87] op_sel_hi:[1,0]\n"
    "v_pk_mul_f32 v[122:123], v[122:123], v[86:87] op_sel_hi:[1,0]\n"
    "v_cvt_pk_bf16_f32 v88, v88, v120\n"
    "v_cvt_pk_bf16_f32 v89, v89, v121\n"
    "v_cvt_pk_bf16_f32 v90, v90, v122\n"
    "v_cvt_pk_bf16_f32 v91, v91, v123\n"
    "v_pk_mul_f32 v[92:93], v[92:93], v[86:87] op_sel_hi:[1,0]\n"
    "v_pk_mul_f32 v[94:95], v[94:95], v[86:87] op_sel_hi:[1,0]\n"
    "v_pk_mul_f32 v[124:125], v[124:125], v[86:87] op_sel_hi:[1,0]\n"
    "v_pk_mul_f32 v[126:127], v[126:127], v[86:87] op_sel_hi:[1,0]\n"
    "v_cvt_pk_bf16_f32 v92, v92, v124\n"
    "v_cvt_pk_bf16_f32 v93, v93, v125\n"
    "v_cvt_pk_bf16_f32 v94, v94, v126\n"
    "v_cvt_pk_bf16_f32 v95, v95, v127\n"
    "v_pk_mul_f32 v[112:113], v[112:113], v[86:87] op_sel_hi:[1,0]\n"
    "v_pk_mul_f32 v[114:115], v[114:115], v[86:87] op_sel_hi:[1,0]\n"
    "v_pk_mul_f32 v[128:129], v[128:129], v[86:87] op_sel_hi:[1,0]\n"
    "v_pk_mul_f32 v[130:131], v[130:131], v[86:87] op_sel_hi:[1,0]\n"
    "v_cvt_pk_bf16_f32 v112, v112, v128\n"
    "v_cvt_pk_bf16_f32 v113, v113, v129\n"
    "v_cvt_pk_bf16_f32 v114, v114, v130\n"
    "v_cvt_pk_bf16_f32 v115, v115, v131\n"
    "v_pk_mul_f32 v[116:117], v[116:117], v[86:87] op_sel_hi:[1,0]\n"
    "v_pk_mul_f32 v[118:119], v[118:119], v[86:87] op_sel_hi:[1,0]\n"
    "v_pk_mul_f32 v[132:133], v[132:133], v[86:87] op_sel_hi:[1,0]\n"
    "v_pk_mul_f32 v[134:135], v[134:135], v[86:87] op_sel_hi:[1,0]\n"
    "v_cvt_pk_bf16_f32 v116, v116, v132\n"
    "v_cvt_pk_bf16_f32 v117, v117, v133\n"
    "v_cvt_pk_bf16_f32 v118, v118, v134\n"
    "v_cvt_pk_bf16_f32 v119, v119, v135\n"
    "ds_read_b128 v[32:35], v2\n"
    "ds_read_b128 v[36:39], v2 offset:1024\n"
    "ds_read_b128 v[40:43], v2 offset:2048\n"
    "ds_read_b128 v[44:47], v2 offset:3072\n"
    "ds_read_b128 v[48:51], v2 offset:4096\n"
    "ds_read_b128 v[52:55], v2 offset:5120\n"
    "ds_read_b128 v[56:59], v2 offset:6144\n"
    "ds_read_b128 v[60:63], v2 offset:7168\n"
    "ds_read_b128 v[64:67], v2 offset:8192\n"
    "ds_read_b128 v[68:71], v2 offset:9216\n"
    "ds_read_b128 v[72:75], v2 offset:10240\n"
    "ds_read_b128 v[76:79], v2 offset:11264\n"
    "ds_read_b128 v[80:83], v2 offset:12288\n"
    "ds_read_b128 v[84:87], v2 offset:13312\n"
    "v_mov_b64 v[124:125], 0\n"
    "v_mov_b64 v[126:127], 0\n"
    "v_mov_b64 v[128:129], 0\n"
    "v_mov_b64 v[130:131], 0\n"
    "s_waitcnt vmcnt(0)\n"
    "s_cmp_lg_u32 s59, 0\n"
    "s_cbranch_scc1 .Lkn10_%=\n"
    "s_waitcnt lgkmcnt(0)\n"
    "s_add_u32 s73, s48, 1\n"
    "s_and_b32 s73, s73, 63\n"
    "s_lshl_b32 s73, s73, 2\n"
    "s_add_u32 s73, s73, s92\n"
    "s_mov_b32 s53, s73\n"
    "s_mov_b32 s56, 0\n"
    "v_mov_b32 v134, s53\n"
    ".Lpoll11_%=:\n"
    "global_load_dword v135, v134, %1 sc1\n"
    "s_waitcnt vmcnt(0)\n"
    "v_readfirstlane_b32 s73, v135\n"
    "s_cmp_ge_u32 s73, 1\n"
    "s_cbranch_scc1 .Lpoll11_x%=\n"
    "s_add_u32 s56, s56, 1\n"
    "s_cmp_lt_u32 s56, 3\n"
    "s_cbranch_scc1 .Lpoll11_%=\n"
    "s_sleep 8\n"
    "s_cmp_lt_u32 s56, s57\n"
    "s_cbranch_scc1 .Lpoll11_%=\n"
    "s_mov_b32 s57, 0\n"
    "v_mov_b32 v135, 1\n"
    "global_store_dword v11, v135, %1 sc1\n"
    ".Lpoll11_x%=:\n"
    "buffer_load_dwordx4 v[12:15], v5, s[40:43], s60 offen sc1\n"
    "buffer_load_dwordx4 v[16:19], v5, s[40:43], s61 offen sc1\n"
    "buffer_load_dwordx4 v[20:23], v5, s[40:43], s62 offen sc1\n"
    "buffer_load_dwordx4 v[24:27], v5, s[40:43], s63 offen sc1\n"
    "buffer_load_dwordx4 v[28:31], v5, s[40:43], s64 offen sc1\n"
    "s_waitcnt vmcnt(0)\n"
    ".Lkn10_%=:\n"
    "s_add_u32 s73, s48, 2\n"
    "s_and_b32 s73, s73, 63\n"
    "s_lshl_b32 s73, s73, 2\n"
    "s_add_u32 s73, s73, s92\n"
    "v_mov_b32 v134, s73\n"
    "global_load_dword v133, v134, %1 sc1\n"
    "s_add_u32 s73, s48, 2\n"
    "s_and_b32 s73, s73, 63\n"
    "s_lshr_b32 s51, s73, 2\n"
    "s_xor_b32 s51, s51, s69\n"
    "s_and_b32 s51, s51, 1\n"
    "s_lshl_b32 s51, s51, 13\n"
    "s_lshl_b32 s73, s73, 5\n"
    "s_add_u32 s73, s73, %7\n"
    "s_lshl_b32 s73, s73, 14\n"
    "s_add_u32 s73, s73, s51\n"
    "s_add_u32 s60, s73, 0x6000000\n"
    "s_add_u32 s61, s73, 0x6001000\n"
    "s_add_u32 s62, s73, 0x8000000\n"
    "s_add_u32 s63, s73, 0x8001000\n"
    "s_lshl_b32 s51, %8, 12\n"
    "s_add_u32 s73, s73, s51\n"
    "s_add_u32 s64, s73, 0x4000000\n"
    "s_mov_b32 s73, 1700\n"
    "s_sub_u32 s53, s73, s58\n"
    "s_waitcnt lgkmcnt(0)\n"
    ".Ltl12_%=:\n"
    "s_memtime s[50:51]\n"
    "s_waitcnt lgkmcnt(0)\n"
    "s_sub_u32 s50, s50, s52\n"
    "s_cmp_lt_u32 s50, s53\n"
    "s_cbranch_scc1 .Ltl12_%=\n"
    "s_lshl_b32 s73, s48, 18\n"
    "s_add_u32 s73, s73, s75\n"
    "buffer_store_dwordx4 v[88:91], v9, s[40:43], s73 offen sc1\n"
    "buffer_store_dwordx4 v[92:95], v9, s[40:43], s73 offen offset:64 sc1\n"
    "buffer_store_dwordx4 v[112:115], v9, s[40:43], s73 offen offset:128 sc1\n"
    "buffer_store_dwordx4 v[116:119], v9, s[40:43], s73 offen offset:192 sc1\n"
    "s_waitcnt lgkmcnt(0)\n"
    "s_barrier\n"
    "ds_write_b128 v5, v[12:15]\n"
    "ds_write_b128 v5, v[16:19] offset:4096\n"
    "ds_write_b128 v5, v[20:23] offset:8192\n"
    "ds_write_b128 v5, v[24:27] offset:12288\n"
    "ds_write_b128 v5, v[28:31] offset:16384\n"
    "s_waitcnt vmcnt(0)\n"
    "v_readfirstlane_b32 s59, v133\n"
    "s_lshl_b32 s73, s48, 8\n"
    "s_add_u32 s73, s73, s93\n"
    "v_mov_b32 v134, s73\n"
    "v_mov_b32 v135, 1\n"
    "s_mov_b64 exec, 1\n"
    "global_atomic_add v134, v135, %1\n"
    "s_mov_b64 exec, -1\n"
    "s_add_u32 s48, s48, 1\n"
    "s_waitcnt lgkmcnt(0)\n"
    "s_barrier\n"
    "s_branch .Lstep_%=\n"
    ".Lend_%=:\n"
    "s_cmp_eq_u32 %6, 3\n"
    "s_cbranch_scc0 .Lout_%=\n"
    "ds_read_b128 v[32:35], v0 offset:16384\n"
    "ds_read_b128 v[48:51], v0\n"
    "ds_read_b128 v[64:67], v0 offset:4096\n"
    "ds_read_b128 v[80:83], v4 offset:20480\n"
    "ds_read_b128 v[36:39], v0 offset:17408\n"
    "ds_read_b128 v[52:55], v0 offset:1024\n"
    "ds_read_b128 v[68:71], v0 offset:5120\n"
    "ds_read_b128 v[84:87], v4 offset:20544\n"
    "ds_read_b128 v[40:43], v0 offset:18432\n"
    "ds_read_b128 v[56:59], v0 offset:2048\n"
    "ds_read_b128 v[72:75], v0 offset:6144\n"
    "ds_read_b128 v[88:91], v4 offset:20608\n"
    "ds_read_b128 v[44:47], v0 offset:19456\n"
    "ds_read_b128 v[60:63], v0 offset:3072\n"
    "ds_read_b128 v[76:79], v0 offset:7168\n"
    "ds_read_b128 v[92:95], v4 offset:20672\n"
    "s_waitcnt lgkmcnt(14)\n"
    "v_mfma_f32_16x16x32_bf16 v[96:99], v[48:51], v[32:35], 0\n"
    "s_waitcnt lgkmcnt(13)\n"
    "v_mfma_f32_16x16x32_bf16 v[100:103], v[64:67], v[32:35], 0\n"
    "s_waitcnt lgkmcnt(12)\n"
    "v_mfma_f32_16x16x32_bf16 v[104:107], v[80:83], v[32:35], 0\n"
    "s_waitcnt lgkmcnt(10)\n"
    "v_mfma_f32_16x16x32_bf16 v[96:99], v[52:55], v[36:39], v[96:99]\n"
    "s_waitcnt lgkmcnt(9)\n"
    "v_mfma_f32_16x16x32_bf16 v[100:103], v[68:71], v[36:39], v[100:103]\n"
    "s_waitcnt lgkmcnt(8)\n"
    "v_mfma_f32_16x16x32_bf16 v[104:107], v[84:87], v[36:39], v[104:107]\n"
    "s_waitcnt lgkmcnt(6)\n"
    "v_mfma_f32_16x16x32_bf16 v[96:99], v[56:59], v[40:43], v[96:99]\n"
    "s_waitcnt lgkmcnt(5)\n"
    "v_mfma_f32_16x16x32_bf16 v[100:103], v[72:75], v[40:43], v[100:103]\n"
    "s_waitcnt lgkmcnt(4)\n"
    "v_mfma_f32_16x16x32_bf16 v[104:107], v[88:91], v[40:43], v[104:107]\n"
    "s_waitcnt lgkmcnt(2)\n"
    "v_mfma_f32_16x16x32_bf16 v[96:99], v[60:63], v[44:47], v[96:99]\n"
    "s_waitcnt lgkmcnt(1)\n"
    "v_mfma_f32_16x16x32_bf16 v[100:103], v[76:79], v[44:47], v[100:103]\n"
    "s_waitcnt lgkmcnt(0)\n"
    "v_mfma_f32_16x16x32_bf16 v[104:107], v[92:95], v[44:47], v[104:107]\n"
    "ds_read_b128 v[32:35], v0 offset:8192\n"
    "ds_read_b128 v[36:39], v0 offset:12288\n"
    "ds_read_b128 v[40:43], v0 offset:9216\n"
    "ds_read_b128 v[44:47], v0 offset:13312\n"
    "ds_read_b128 v[48:51], v0 offset:10240\n"
    "ds_read_b128 v[52:55], v0 offset:14336\n"
    "ds_read_b128 v[56:59], v0 offset:11264\n"
    "ds_read_b128 v[60:63], v0 offset:15360\n"
    "ds_read_b128 v[64:67], v0 offset:22784\n"
    "ds_read_b128 v[68:71], v0 offset:23808\n"
    "ds_read_b128 v[72:75], v0 offset:24832\n"
    "ds_read_b128 v[76:79], v0 offset:25856\n"
    "ds_read_b128 v[80:83], v11 offset:31888\n"
    "ds_read_b64 v[84:85], v11 offset:31904\n"
    "v_mov_b32 v86, 0xd01502f9\n"
    "v_cndmask_b32 v96, v86, v96, s[76:77]\n"
    "v_cndmask_b32 v97, v86, v97, s[78:79]\n"
    "v_cndmask_b32 v98, v86, v98, s[80:81]\n"
    "v_cndmask_b32 v99, v86, v99, s[82:83]\n"
    "v_cndmask_b32 v100, v86, v100, s[84:85]\n"
    "v_cndmask_b32 v101, v86, v101, s[86:87]\n"
    "v_cndmask_b32 v102, v86, v102, s[88:89]\n"
    "v_cndmask_b32 v103, v86, v103, s[90:91]\n"
    "v_maximum3_f32 v87, v104, v105, v106\n"
    "v_maximum3_f32 v87, v87, v107, v96\n"
    "v_maximum3_f32 v87, v87, v97, v98\n"
    "v_maximum3_f32 v87, v87, v99, v100\n"
    "v_maximum3_f32 v87, v87, v101, v102\n"
    "v_maximum3_f32 v87, v87, v103, v103\n"
    "v_mov_b32 v86, v87\n"
    "s_nop 1\n"
    "v_permlane16_swap_b32 v86, v87\n"
    "v_maximum3_f32 v87, v87, v86, v86\n"
    "v_mov_b32 v86, v87\n"
    "s_nop 1\n"
    "v_permlane32_swap_b32 v86, v87\n"
    "v_maximum3_f32 v87, v87, v86, v86\n"
    "v_mul_f32 v87, 0xbe0293ee, v87\n"
    "v_fmamk_f32 v96, v96, 0x3e0293ee, v87\n"
    "v_fmamk_f32 v97, v97, 0x3e0293ee, v87\n"
    "v_fmamk_f32 v98, v98, 0x3e0293ee, v87\n"
    "v_fmamk_f32 v99, v99, 0x3e0293ee, v87\n"
    "v_fmamk_f32 v100, v100, 0x3e0293ee, v87\n"
    "v_fmamk_f32 v101, v101, 0x3e0293ee, v87\n"
    "v_fmamk_f32 v102, v102, 0x3e0293ee, v87\n"
    "v_fmamk_f32 v103, v103, 0x3e0293ee, v87\n"
    "v_fmamk_f32 v104, v104, 0x3e0293ee, v87\n"
    "v_fmamk_f32 v105, v105, 0x3e0293ee, v87\n"
    "v_fmamk_f32 v106, v106, 0x3e0293ee, v87\n"
    "v_fmamk_f32 v107, v107, 0x3e0293ee, v87\n"
    "v_exp_f32 v96, v96\n"
    "v_exp_f32 v97, v97\n"
    "v_exp_f32 v98, v98\n"
    "v_exp_f32 v99, v99\n"
    "v_exp_f32 v100, v100\n"
    "v_exp_f32 v101, v101\n"
    "v_exp_f32 v102, v102\n"
    "v_exp_f32 v103, v103\n"
    "v_exp_f32 v104, v104\n"
    "v_exp_f32 v105, v105\n"
    "v_exp_f32 v106, v106\n"
    "v_exp_f32 v107, v107\n"
    "v_cvt_pk_bf16_f32 v96, v96, v97\n"
    "v_cvt_pk_bf16_f32 v97, v98, v99\n"
    "v_cvt_pk_bf16_f32 v98, v100, v101\n"
    "v_cvt_pk_bf16_f32 v99, v102, v103\n"
    "v_cvt_pk_bf16_f32 v104, v104, v105\n"
    "v_cvt_pk_bf16_f32 v105, v106, v107\n"
    "s_waitcnt lgkmcnt(1)\n"
    "v_mfma_f32_16x16x32_bf16 v[108:111], v[80:83], v[96:99], 0\n"
    "v_mfma_f32_16x16x32_bf16 v[88:91], v[32:35], v[96:99], 0\n"
    "v_mfma_f32_16x16x32_bf16 v[120:123], v[36:39], v[96:99], 0\n"
    "v_mfma_f32_16x16x32_bf16 v[92:95], v[40:43], v[96:99], 0\n"
    "s_waitcnt lgkmcnt(0)\n"
    "v_mfma_f32_16x16x16_bf16 v[108:111], v[84:85], v[104:105], v[108:111]\n"
    "v_mfma_f32_16x16x32_bf16 v[124:127], v[44:47], v[96:99], 0\n"
    "v_mfma_f32_16x16x32_bf16 v[112:115], v[48:51], v[96:99], 0\n"
    "v_mfma_f32_16x16x32_bf16 v[128:131], v[52:55], v[96:99], 0\n"
    "v_mfma_f32_16x16x32_bf16 v[116:119], v[56:59], v[96:99], 0\n"
    "v_mfma_f32_16x16x32_bf16 v[132:135], v[60:63], v[96:99], 0\n"
    "v_rcp_f32 v86, v108\n"
    "v_mfma_f32_16x16x16_bf16 v[88:91], v[64:65], v[104:105], v[88:91]\n"
    "v_mfma_f32_16x16x16_bf16 v[120:123], v[66:67], v[104:105], v[120:123]\n"
    "v_mfma_f32_16x16x16_bf16 v[92:95], v[68:69], v[104:105], v[92:95]\n"
    "v_mfma_f32_16x16x16_bf16 v[124:127], v[70:71], v[104:105], v[124:127]\n"
    "v_mfma_f32_16x16x16_bf16 v[112:115], v[72:73], v[104:105], v[112:115]\n"
    "v_mfma_f32_16x16x16_bf16 v[128:131], v[74:75], v[104:105], v[128:131]\n"
    "v_mfma_f32_16x16x16_bf16 v[116:119], v[76:77], v[104:105], v[116:119]\n"
    "v_mfma_f32_16x16x16_bf16 v[132:135], v[78:79], v[104:105], v[132:135]\n"
    "v_pk_mul_f32 v[88:89], v[88:89], v[86:87] op_sel_hi:[1,0]\n"
    "v_pk_mul_f32 v[90:91], v[90:91], v[86:87] op_sel_hi:[1,0]\n"
    "v_pk_mul_f32 v[120:121], v[120:121], v[86:87] op_sel_hi:[1,0]\n"
    "v_pk_mul_f32 v[122:123], v[122:123], v[86:87] op_sel_hi:[1,0]\n"
    "v_cvt_pk_bf16_f32 v88, v88, v120\n"
    "v_cvt_pk_bf16_f32 v89, v89, v121\n"
    "v_cvt_pk_bf16_f32 v90, v90, v122\n"
    "v_cvt_pk_bf16_f32 v91, v91, v123\n"
    "v_pk_mul_f32 v[92:93], v[92:93], v[86:87] op_sel_hi:[1,0]\n"
    "v_pk_mul_f32 v[94:95], v[94:95], v[86:87] op_sel_hi:[1,0]\n"
    "v_pk_mul_f32 v[124:125], v[124:125], v[86:87] op_sel_hi:[1,0]\n"
    "v_pk_mul_f32 v[126:127], v[126:127], v[86:87] op_sel_hi:[1,0]\n"
    "v_cvt_pk_bf16_f32 v92, v92, v124\n"
    "v_cvt_pk_bf16_f32 v93, v93, v125\n"
    "v_cvt_pk_bf16_f32 v94, v94, v126\n"
    "v_cvt_pk_bf16_f32 v95, v95, v127\n"
    "v_pk_mul_f32 v[112:113], v[112:113], v[86:87] op_sel_hi:[1,0]\n"
    "v_pk_mul_f32 v[114:115], v[114:115], v[86:87] op_sel_hi:[1,0]\n"
    "v_pk_mul_f32 v[128:129], v[128:129], v[86:87] op_sel_hi:[1,0]\n"
    "v_pk_mul_f32 v[130:131], v[130:131], v[86:87] op_sel_hi:[1,0]\n"
    "v_cvt_pk_bf16_f32 v112, v112, v128\n"
    "v_cvt_pk_bf16_f32 v113, v113, v129\n"
    "v_cvt_pk_bf16_f32 v114, v114, v130\n"
    "v_cvt_pk_bf16_f32 v115, v115, v131\n"
    "v_pk_mul_f32 v[116:117], v[116:117], v[86:87] op_sel_hi:[1,0]\n"
    "v_pk_mul_f32 v[118:119], v[118:119], v[86:87] op_sel_hi:[1,0]\n"
    "v_pk_mul_f32 v[132:133], v[132:133], v[86:87] op_sel_hi:[1,0]\n"
    "v_pk_mul_f32 v[134:135], v[134:135], v[86:87] op_sel_hi:[1,0]\n"
    "v_cvt_pk_bf16_f32 v116, v116, v132\n"
    "v_cvt_pk_bf16_f32 v117, v117, v133\n"
    "v_cvt_pk_bf16_f32 v118, v118, v134\n"
    "v_cvt_pk_bf16_f32 v119, v119, v135\n"
    "s_lshl_b32 s73, s48, 18\n"
    "s_add_u32 s73, s73, s75\n"
    "buffer_store_dwordx4 v[88:91], v9, s[40:43], s73 offen sc1\n"
    "buffer_store_dwordx4 v[92:95], v9, s[40:43], s73 offen offset:64 sc1\n"
    "buffer_store_dwordx4 v[112:115], v9, s[40:43], s73 offen offset:128 sc1\n"
    "buffer_store_dwordx4 v[116:119], v9, s[40:43], s73 offen offset:192 sc1\n"
    "s_waitcnt vmcnt(0)\n"
    "s_lshl_b32 s73, s48, 8\n"
    "s_add_u32 s73, s73, s93\n"
    "v_mov_b32 v134, s73\n"
    "v_mov_b32 v135, 1\n"
    "s_mov_b64 exec, 1\n"
    "global_atomic_add v134, v135, %1\n"
    "s_mov_b64 exec, -1\n"
    ".Lout_%=:\n"
    "s_waitcnt vmcnt(0)\n"
        :: "s"(wd), "s"(ws), "s"(Wq), "s"(Wk), "s"(Wv), "s"(Wo), "s"(wave), "s"(h), "s"(p), "s"(layer), "s"(rid)
        : "memory", "vcc", "scc",
        "v0", "v1", "v2", "v3", "v4", "v5", "v6", "v7", "v8", "v9", "v10", "v11", "v12", "v13", "v14", "v15", "v16", "v17", "v18", "v19", "v20", "v21", "v22", "v23",
        "v24", "v25", "v26", "v27", "v28", "v29", "v30", "v31", "v32", "v33", "v34", "v35", "v36", "v37", "v38", "v39", "v40", "v41", "v42", "v43", "v44", "v45", "v46", "v47",
        "v48", "v49", "v50", "v51", "v52", "v53", "v54", "v55", "v56", "v57", "v58", "v59", "v60", "v61", "v62", "v63", "v64", "v65", "v66", "v67", "v68", "v69", "v70", "v71",
        "v72", "v73", "v74", "v75", "v76", "v77", "v78", "v79", "v80", "v81", "v82", "v83", "v84", "v85", "v86", "v87", "v88", "v89", "v90", "v91", "v92", "v93", "v94", "v95",
        "v96", "v97", "v98", "v99", "v100", "v101", "v102", "v103", "v104", "v105", "v106", "v107", "v108", "v109", "v110", "v111", "v112", "v113", "v114", "v115", "v116", "v117", "v118", "v119",
        "v120", "v121", "v122", "v123", "v124", "v125", "v126", "v127", "v128", "v129", "v130", "v131", "v132", "v133", "v134", "v135", "v136", "v137", "v138", "v139", "v140", "v141", "v142", "v143",
        "v144", "v145", "v146", "v147", "v148", "v149", "v150", "v151", "v152", "v153", "v154", "v155", "v156", "v157", "v158", "v159", "v160", "v161", "v162", "v163", "v164", "v165", "v166", "v167",
        "v168", "v169", "v170", "v171", "v172", "v173", "v174", "v175", "v176", "v177", "v178", "v179", "v180", "v181", "v182", "v183", "v184", "v185", "v186", "v187", "v188", "v189", "v190", "v191",
        "v192", "v193", "v194", "v195", "v196", "v197", "v198", "v199", "v200", "v201", "v202", "v203", "v204", "v205", "v206", "v207", "v208", "v209", "v210", "v211", "v212", "v213", "v214", "v215",
        "v216", "v217", "v218", "v219", "v220", "v221", "v222", "v223", "v224", "v225", "v226", "v227", "v228", "v229", "v230", "v231", "v232", "v233", "v234", "v235", "v236", "v237", "v238", "v239",
        "v240", "v241", "v242", "v243", "v244", "v245", "v246", "v247", "v248", "v249", "v250", "v251", "v252", "v253", "v254", "v255", "a0", "a1", "a2", "a3", "a4", "a5", "a6", "a7",
        "a8", "a9", "a10", "a11", "a12", "a13", "a14", "a15", "a16", "a17", "a18", "a19", "a20", "a21", "a22", "a23", "a24", "a25", "a26", "a27", "a28", "a29", "a30", "a31",
        "a32", "a33", "a34", "a35", "a36", "a37", "a38", "a39", "a40", "a41", "a42", "a43", "a44", "a45", "a46", "a47", "a48", "a49", "a50", "a51", "a52", "a53", "a54", "a55",
        "a56", "a57", "a58", "a59", "a60", "a61", "a62", "a63", "a64", "a65", "a66", "a67", "a68", "a69", "a70", "a71", "a72", "a73", "a74", "a75", "a76", "a77", "a78", "a79",
        "a80", "a81", "a82", "a83", "a84", "a85", "a86", "a87", "a88", "a89", "a90", "a91", "a92", "a93", "a94", "a95", "a96", "a97", "a98", "a99", "a100", "a101", "a102", "a103",
        "a104", "a105", "a106", "a107", "a108", "a109", "a110", "a111", "a112", "a113", "a114", "a115", "a116", "a117", "a118", "a119", "a120", "a121", "a122", "a123", "a124", "a125", "a126", "a127",
        "a128", "a129", "a130", "a131", "a132", "a133", "a134", "a135", "a136", "a137", "a138", "a139", "a140", "a141", "a142", "a143", "a144", "a145", "a146", "a147", "a148", "a149", "a150", "a151",
        "a152", "a153", "a154", "a155", "a156", "a157", "a158", "a159", "a160", "a161", "a162", "a163", "a164", "a165", "a166", "a167", "a168", "a169", "a170", "a171", "a172", "a173", "a174", "a175",
        "a176", "a177", "a178", "a179", "a180", "a181", "a182", "a183", "a184", "a185", "a186", "a187", "a188", "a189", "a190", "a191", "a192", "a193", "a194", "a195", "a196", "a197", "a198", "a199",
        "a200", "a201", "a202", "a203", "a204", "a205", "a206", "a207", "a208", "a209", "a210", "a211", "a212", "a213", "a214", "a215", "a216", "a217", "a218", "a219", "a220", "a221", "a222", "a223",
        "a224", "a225", "a226", "a227", "a228", "a229", "a230", "a231", "a232", "a233", "a234", "a235", "a236", "a237", "a238", "a239", "a240", "a241", "a242", "a243", "a244", "a245", "a246", "a247",
        "a248", "a249", "a250", "a251", "a252", "a253", "a254", "a255", "s40", "s41", "s42", "s43", "s44", "s45", "s46", "s47", "s48", "s49", "s50", "s51", "s52", "s53", "s54", "s55",
        "s56", "s57", "s58", "s59", "s60", "s61", "s62", "s63", "s64", "s65", "s66", "s67", "s68", "s69", "s70", "s71", "s72", "s73", "s74", "s75", "s76", "s77", "s78", "s79",
        "s80", "s81", "s82", "s83", "s84", "s85", "s86", "s87", "s88", "s89", "s90", "s91", "s92", "s93", "s94", "s95", "s96", "s97", "s98", "s99", "s100", "s101"
    );
}

static_assert(OFF_POL == 64u);
static_assert(OFF_RDY == 128u);
static_assert(OFF_COB == 41984u);
static_assert(OFF_FKV == 74752u);
static_assert(OFF_XA == 0u);
static_assert(OFF_XB == 2097152u);
static_assert(OFF_OB == 49020928u);
static_assert(OFF_QX == 67108864u);
static_assert(OFF_KX == 100663296u);
static_assert(OFF_VX == 134217728u);
static_assert(WS_BYTES == 167772160u);
static_assert(L_M == 20480u);
static_assert(L_C == 31872u);
__device__ __forceinline__ void measure_pol(int side, char* ws, rsrc_t rs, char* smem) {
    const int wave = __builtin_amdgcn_readfirstlane(threadIdx.x >> 6);
    u32 best[4] = {~0u, ~0u, ~0u, ~0u};
    if (wave < 2) {
        for (int n = 0; n < 12; ++n)
#pragma unroll
            for (int i = 0; i < 4; ++i) {
                SB();
                const u32 t0 = now32();
                SB();
                st4(rs, wave * MB2 + (508 + i) * 4096 + n * 128, t0);
                DRAIN();
                const u32 t = now32() - t0;
                best[i] = t < best[i] ? t : best[i];
            }
        if ((threadIdx.x & 63) == 0) ((u32*)smem)[wave] = (best[0] + best[1] < best[2] + best[3] ? 0u : 1u) ^ (u32)side;
    }
    __syncthreads();
    if (threadIdx.x == 0) __hip_atomic_store((gu32*)(ws + OFF_POL), 4u | ((u32*)smem)[0] | (((u32*)smem)[1] << 1), RLX_AGENT);
}

__global__ __launch_bounds__(256, 1) void mega_kernel(const u16* __restrict__ x0, const u16* __restrict__ wqkv, const u16* __restrict__ woA,
                                                     const u16* __restrict__ mem0, const u16* __restrict__ Wq, const u16* __restrict__ Wk,
                                                     const u16* __restrict__ Wv, const u16* __restrict__ Wo, u16* out, char* ws) {
    __shared__ __attribute__((aligned(16))) char smem[163840];
    char* wd = (char*)(((uintptr_t)ws + SYNC_BYTES + MB2 - 1) & ~(uintptr_t)(MB2 - 1));
    const rsrc_t rs = __builtin_amdgcn_make_buffer_rsrc(wd, 0, WS_BYTES, 0x00020000);
    u32 xcc;
    asm volatile("s_getreg_b32 %0, hwreg(HW_REG_XCC_ID)" : "=s"(xcc));
    xcc &= 7;
    if (threadIdx.x == 0) {
        u32 x = xcc, t;
        for (;;) {
            t = __hip_atomic_fetch_add((gu32*)(ws + OFF_ROLE + x * 64), 1u, RLX_AGENT);
            if (t < 32) break;
            x = (x + 1) & 7;
        }
        *(u32*)smem = x * 32 + t;
    }
    {
        const int sh = blockIdx.x;
        v4u a[23];
        const int t = threadIdx.x;
        a[20] = ((const v4u*)wqkv)[sh * 486 + t]; a[21] = ((const v4u*)wqkv)[sh * 486 + (t + 256 < 486 ? t + 256 : t)];
        a[22] = ((const v4u*)woA)[sh * 162 + (t < 162 ? t : 0)];
#pragma unroll
        for (int i = 0; i < 5; ++i) {
            const int c = sh * 1152 + (t + 256 * i < 1152 ? t + 256 * i : t);
            a[i] = ((const v4u*)Wq)[c]; a[5 + i] = ((const v4u*)Wk)[c]; a[10 + i] = ((const v4u*)Wv)[c]; a[15 + i] = ((const v4u*)Wo)[c];
        }
#pragma unroll
        for (int i = 0; i < 23; ++i) asm volatile("" :: "v"(a[i]));
    }
    __syncthreads();
    const int rid = __builtin_amdgcn_readfirstlane(*(const u32*)smem);
    __syncthreads();
    STAMP(159, 0);
    const int x = rid >> 5, t = rid & 31, hi = x >> 2;
    if (!(x & 1)) role_chain(rid, hi, (x & 2) * 8 + (t >> 1), t & 1, mem0, Wq, Wk, Wv, Wo, ws, wd, rs, smem);
    else if (rid == 3 * 32 + 31) measure_pol(xcc >> 2, ws, rs, smem);
    else if (!(x & 2)) role_h(rid, hi, t, Wq, Wk, Wv, ws, rs, smem);
    else if (t < 12) {
        role_t0(hi * 12 + t, x0, ws, rs);
        role_w(rid, hi * 2 + t / 6, t % 6, Wo, ws, rs, smem);
    } else if (t < 20) role_a1(rid, hi, t - 12, wqkv, ws, rs, smem);
    else if (t < 24) role_a2(rid, hi * 4 + t - 20, woA, ws, rs, smem);
    else if (t < 28) {
        const int wave = __builtin_amdgcn_readfirstlane(threadIdx.x >> 6);
        if (wave & 1) role_r_stream<4>(rid, hi * 4 + t - 24, wave >> 1, x0, out, ws, rs);
        else role_r_stream<1>(rid, hi * 4 + t - 24, wave >> 1, x0, out, ws, rs);
    }
}

extern "C" void kernel_launch(void* const* d_in, const int* in_sizes, int n_in, void* d_out, int out_size, void* d_ws,
                              size_t ws_size, hipStream_t stream) {
    hipMemsetAsync(d_ws, 0, SYNC_BYTES, stream);
    mega_kernel<<<256, 256, 0, stream>>>((const u16*)d_in[0], (const u16*)d_in[1], (const u16*)d_in[2], (const u16*)d_in[4],
                                         (const u16*)d_in[5], (const u16*)d_in[6], (const u16*)d_in[7], (const u16*)d_in[8],
                                         (u16*)d_out, (char*)d_ws);
}
```

```cpp
#include <hip/hip_runtime.h>
#include <hip/hip_bf16.h>
#include <stdint.h>

typedef unsigned short u16;
typedef unsigned u32;
typedef unsigned long long u64;
using bf16x8 = __attribute__((ext_vector_type(8))) short;
using bf16x4 = __attribute__((ext_vector_type(4))) short;
using f32x4 = __attribute__((ext_vector_type(4))) float;
using v4u = __attribute__((ext_vector_type(4))) unsigned;
using v2u = __attribute__((ext_vector_type(2))) unsigned;
typedef __bf16 bfv2 __attribute__((ext_vector_type(2)));
#define GAS __attribute__((address_space(1)))
typedef GAS unsigned gu32;
#define RLX_AGENT __ATOMIC_RELAXED, __HIP_MEMORY_SCOPE_AGENT
typedef __amdgpu_buffer_rsrc_t rsrc_t;

constexpr int S_ = 2048, D_ = 576, NSEG = 64, SEG = 32, NB = 4096, NQA = 1728;

constexpr u32 OFF_ABORT = 0;
constexpr u32 OFF_POL = 64;
constexpr u32 OFF_RDY = 128;
constexpr u32 OFF_ROLE = 256;
constexpr u32 OFF_CXNA = 1024;
constexpr u32 OFF_CPA = OFF_CXNA + 8192;
constexpr u32 OFF_CXNB = OFF_CPA + 8192;
constexpr u32 OFF_CPB = OFF_CXNB + 8192;
constexpr u32 OFF_COA = OFF_CPB + 8192;
constexpr u32 OFF_COB = OFF_COA + 8192;
constexpr u32 OFF_FKV = OFF_COB + 32768;
constexpr u32 SYNC_BYTES = OFF_FKV + 16384;
constexpr u32 MB2 = 2u << 20;
constexpr u32 OFF_XA = 0;
constexpr u32 OFF_XB = MB2;
constexpr u32 XB = S_ * D_ * 2;
constexpr u32 OFF_XNA = 2 * MB2;
constexpr u32 OFF_XNB = OFF_XNA + 2 * XB;
constexpr u32 OFF_X = OFF_XNB + 2 * XB;
constexpr u32 OFF_OA = OFF_X + 3 * XB;
constexpr u32 OFF_AO = OFF_OA + 2 * XB;
constexpr u32 OFF_PB = OFF_AO + 4 * XB;
constexpr u32 HB_ = 2 * 64 * 32 * 8192;
constexpr u32 OFF_QX = (OFF_PB + 16 * XB + 2 * MB2 - 1) & ~(2 * MB2 - 1);
constexpr u32 OFF_KX = OFF_QX + HB_;
constexpr u32 OFF_VX = OFF_KX + HB_;
constexpr u32 OFF_OX = OFF_VX + HB_;
constexpr u32 WS_BYTES = OFF_OX + HB_;
constexpr u32 L_TAB = 147456, L_EX = 139264, L_W = 31936;

#define STAMP(i, k) do {} while (0)
#define PH(k) do {} while (0)
__device__ __forceinline__ float bf2f(u16 v) { return __uint_as_float(((u32)v) << 16); }
__device__ __forceinline__ float bflo(u32 v) { return __uint_as_float(v << 16); }
__device__ __forceinline__ float bfhi(u32 v) { return __uint_as_float(v & 0xffff0000u); }
__device__ __forceinline__ u32 pack2(float a, float b) { bfv2 v = {(__bf16)a, (__bf16)b}; return __builtin_bit_cast(u32, v); }
__device__ __forceinline__ float rbf(float f) { return (float)(__bf16)f; }
__device__ __forceinline__ v4u ld16(rsrc_t r, u32 off) { return __builtin_amdgcn_raw_buffer_load_b128(r, off, 0, 16); }
__device__ __forceinline__ v2u ld8(rsrc_t r, u32 off) { return __builtin_amdgcn_raw_buffer_load_b64(r, off, 0, 16); }
__device__ __forceinline__ void st16(rsrc_t r, u32 off, v4u v) { __builtin_amdgcn_raw_buffer_store_b128(v, r, off, 0, 16); }
__device__ __forceinline__ void st8(rsrc_t r, u32 off, v2u v) { __builtin_amdgcn_raw_buffer_store_b64(v, r, off, 0, 16); }
__device__ __forceinline__ void st4(rsrc_t r, u32 off, u32 v) { __builtin_amdgcn_raw_buffer_store_b32(v, r, off, 0, 16); }
#define DRAIN() asm volatile("s_waitcnt vmcnt(0)" ::: "memory")
#define CBAR() asm volatile("" ::: "memory")
__device__ __forceinline__ u32 ldw(char* ws, u32 off) { return __hip_atomic_load((gu32*)(ws + off), RLX_AGENT); }
__device__ __forceinline__ void addw(char* ws, u32 off) { __hip_atomic_fetch_add((gu32*)(ws + off), 1u, RLX_AGENT); }
constexpr u32 SPIN_CAP = 1u << 20;
#define SLP_W 8
#define SLP_C 8
__device__ __forceinline__ bool spin_fail(char* ws, u32& spins) {
    ++spins;
    if ((spins & 63u) == 0u) {
        if (ldw(ws, OFF_ABORT)) return true;
        if (spins > SPIN_CAP) { __hip_atomic_store((gu32*)(ws + OFF_ABORT), 1u, RLX_AGENT); return true; }
    }
    return false;
}
__device__ __forceinline__ void wait_ge(char* ws, u32 off, u32 target) {
    u32 spins = 0;
    while (ldw(ws, off) < target) {
        if (spin_fail(ws, spins)) break;
        __builtin_amdgcn_s_sleep(SLP_W);
    }
    CBAR();
}
template <int CTRL> __device__ __forceinline__ float dpp(float v) {
    return __uint_as_float(__builtin_amdgcn_update_dpp(0u, __float_as_uint(v), CTRL, 0xf, 0xf, false));
}
__device__ __forceinline__ float half_sum(float v) {
    v += dpp<0xB1>(v);
    v += dpp<0x4E>(v);
    v += dpp<0x141>(v);
    v += dpp<0x140>(v);
    auto a = __builtin_amdgcn_permlane16_swap(__float_as_uint(v), __float_as_uint(v), false, false);
    return __uint_as_float(a[0]) + __uint_as_float(a[1]);
}
__device__ __forceinline__ void ln_row(const v4u (&x)[3], bool act, v4u (&y)[3]) {
    float v[24], s = 0.f;
#pragma unroll
    for (int i = 0; i < 12; ++i) { v[2 * i] = bflo(x[i >> 2][i & 3]); v[2 * i + 1] = bfhi(x[i >> 2][i & 3]); s += v[2 * i] + v[2 * i + 1]; }
    const float mean = half_sum(s) * (1.f / D_);
    float q = 0.f;
#pragma unroll
    for (int i = 0; i < 24; ++i) { v[i] -= mean; q += v[i] * v[i]; }
    q = half_sum(act ? q : 0.f);
    const float inv = __builtin_amdgcn_rsqf(q * (1.f / D_) + 1e-5f);
#pragma unroll
    for (int i = 0; i < 12; ++i) y[i >> 2][i & 3] = pack2(v[2 * i] * inv, v[2 * i + 1] * inv);
}
constexpr u32 OOB = 0x80000000u;

__device__ __forceinline__ void role_t0(int idx, const u16* x0, char* ws, rsrc_t rs) {
    const int tid = threadIdx.x, l = tid & 31;
    const bool act = l < 24;
    const rsrc_t rx = __builtin_amdgcn_make_buffer_rsrc((void*)x0, 0, XB, 0x00020000);
    const u32 vo = act ? ((idx & 3) * 8 + (tid >> 5)) * 1152 + l * 16 : OOB;
    for (int b = 0; b < 4; ++b) {
        const int nu = b ? 5 : 1, s0 = (idx >> 2) + (b ? 20 * b - 16 : 0);
        v4u x[5][3];
#pragma unroll
        for (int u = 0; u < 5; ++u)
#pragma unroll
            for (int i = 0; i < 3; ++i) x[u][i] = __builtin_amdgcn_raw_buffer_load_b128(rx, vo + i * 384, (s0 + (u < nu ? 4 * u : 0)) * 36864, 0);
#pragma unroll
        for (int u = 0; u < 5; ++u)
            if (u < nu) {
                v4u y[3];
                ln_row(x[u], act, y);
#pragma unroll
                for (int i = 0; i < 3; ++i) __builtin_amdgcn_raw_buffer_store_b128(y[i], rs, vo + i * 384, OFF_XNA + (s0 + 4 * u) * 36864, 16);
            }
        DRAIN();
        __syncthreads();
        if (tid < nu) addw(ws, OFF_CXNA + (s0 + 4 * tid) * 64);
    }
}

template <int NP, int PR>
__device__ __forceinline__ void role_r(int rid, int idx, int layer, const u16* x0, u16* out, char* ws, char* wd, rsrc_t rs) {
    const int tid = threadIdx.x, l = tid & 31;
    const bool act = l < 24;
    const u32 cnt = (NP == 1 ? OFF_CPA : OFF_CPB) + layer * 4096, target = NP == 1 ? 6 : 12;
    const bool fin = NP == 4 && layer == 1;
    const u32 sig = NP == 1 ? OFF_CXNB + layer * 4096 : OFF_CXNA + 4096;
    const int xi = 2 * layer + (NP > 1);
    const rsrc_t rx = __builtin_amdgcn_make_buffer_rsrc(xi ? (void*)(wd + OFF_X + (xi - 1) * XB) : (void*)x0, 0, XB, 0x00020000);
    const rsrc_t ro = __builtin_amdgcn_make_buffer_rsrc(fin ? (void*)out : (void*)(wd + OFF_X + xi * XB), 0, XB, 0x00020000);
    const int row = idx * 8 * PR + (tid >> 5);
    const u32 vx = act ? row * 1152 + l * 16 : OOB, vp = act ? row * 2304 + l * 16 : OOB;
    const u32 pb = NP == 1 ? OFF_AO + layer * 2 * XB : OFF_PB + layer * 8 * XB, yb = NP == 1 ? OFF_XNB + layer * XB : OFF_XNA + XB;
    for (int s = 0; s < NSEG; ++s) {
        if (tid < 64) wait_ge(ws, cnt + s * 64, target);
        __syncthreads();
        STAMP(s, 0);
        v4u x[PR][3], pr[PR][NP][6];
#pragma unroll
        for (int u = 0; u < PR; ++u) {
#pragma unroll
            for (int p = 0; p < NP; ++p)
#pragma unroll
                for (int i = 0; i < 6; ++i) pr[u][p][i] = __builtin_amdgcn_raw_buffer_load_b128(rs, vp + i * 384, pb + ((s * NP + p) * 32 + u * 8) * 2304, 16);
#pragma unroll
            for (int i = 0; i < 3; ++i) x[u][i] = __builtin_amdgcn_raw_buffer_load_b128(rx, vx + i * 384, (s * 32 + u * 8) * 1152, 16);
        }
#pragma unroll
        for (int u = 0; u < PR; ++u) {
#pragma unroll
            for (int i = 0; i < 6; ++i) {
                f32x4 a = __builtin_bit_cast(f32x4, pr[u][0][i]);
                if (NP == 4) a = (a + __builtin_bit_cast(f32x4, pr[u][NP > 1][i])) + (__builtin_bit_cast(f32x4, pr[u][NP / 2][i]) + __builtin_bit_cast(f32x4, pr[u][NP - 1][i]));
#pragma unroll
                for (int e = 0; e < 2; ++e) {
                    const u32 w = x[u][i >> 1][(i & 1) * 2 + e], t = pack2(a[2 * e], a[2 * e + 1]);
                    x[u][i >> 1][(i & 1) * 2 + e] = pack2(bflo(w) + bflo(t), bfhi(w) + bfhi(t));
                }
            }
            if (!fin) {
                v4u y[3];
                ln_row(x[u], act, y);
#pragma unroll
                for (int i = 0; i < 3; ++i) __builtin_amdgcn_raw_buffer_store_b128(y[i], rs, vx + i * 384, yb + (s * 32 + u * 8) * 1152, 16);
            }
#pragma unroll
            for (int i = 0; i < 3; ++i) __builtin_amdgcn_raw_buffer_store_b128(x[u][i], ro, vx + i * 384, (s * 32 + u * 8) * 1152, 16);
        }
        STAMP(s, 1);
        if (!fin) {
            DRAIN();
            __syncthreads();
            if (tid == 0) addw(ws, sig + s * 64);
        }
    }
}

#define CLOBBERS "memory", "vcc", "scc", \
    "v0", "v1", "v2", "v3", "v4", "v5", "v6", "v7", "v8", "v9", "v10", "v11", "v12", "v13", "v14", "v15", "v16", "v17", "v18", "v19", "v20", "v21", "v22", "v23", \
    "v24", "v25", "v26", "v27", "v28", "v29", "v30", "v31", "v32", "v33", "v34", "v35", "v36", "v37", "v38", "v39", "v40", "v41", "v42", "v43", "v44", "v45", "v46", "v47", \
    "v48", "v49", "v50", "v51", "v52", "v53", "v54", "v55", "v56", "v57", "v58", "v59", "v60", "v61", "v62", "v63", "v64", "v65", "v66", "v67", "v68", "v69", "v70", "v71", \
    "v72", "v73", "v74", "v75", "v76", "v77", "v78", "v79", "v80", "v81", "v82", "v83", "v84", "v85", "v86", "v87", "v88", "v89", "v90", "v91", "v92", "v93", "v94", "v95", \
    "v96", "v97", "v98", "v99", "v100", "v101", "v102", "v103", "v104", "v105", "v106", "v107", "v108", "v109", "v110", "v111", "v112", "v113", "v114", "v115", "v116", "v117", "v118", "v119", \
    "v120", "v121", "v122", "v123", "v124", "v125", "v126", "v127", "v128", "v129", "v130", "v131", "v132", "v133", "v134", "v135", "v136", "v137", "v138", "v139", "v140", "v141", "v142", "v143", \
    "v144", "v145", "v146", "v147", "v148", "v149", "v150", "v151", "v152", "v153", "v154", "v155", "v156", "v157", "v158", "v159", "v160", "v161", "v162", "v163", "v164", "v165", "v166", "v167", \
    "v168", "v169", "v170", "v171", "v172", "v173", "v174", "v175", "v176", "v177", "v178", "v179", "v180", "v181", "v182", "v183", "v184", "v185", "v186", "v187", "v188", "v189", "v190", "v191", \
    "v192", "v193", "v194", "v195", "v196", "v197", "v198", "v199", "v200", "v201", "v202", "v203", "v204", "v205", "v206", "v207", "v208", "v209", "v210", "v211", "v212", "v213", "v214", "v215", \
    "v216", "v217", "v218", "v219", "v220", "v221", "v222", "v223", "v224", "v225", "v226", "v227", "v228", "v229", "v230", "v231", "v232", "v233", "v234", "v235", "v236", "v237", "v238", "v239", \
    "v240", "v241", "v242", "v243", "v244", "v245", "v246", "v247", "v248", "v249", "v250", "v251", "v252", "v253", "v254", "v255", "a0", "a1", "a2", "a3", "a4", "a5", "a6", "a7", \
    "a8", "a9", "a10", "a11", "a12", "a13", "a14", "a15", "a16", "a17", "a18", "a19", "a20", "a21", "a22", "a23", "a24", "a25", "a26", "a27", "a28", "a29", "a30", "a31", \
    "a32", "a33", "a34", "a35", "a36", "a37", "a38", "a39", "a40", "a41", "a42", "a43", "a44", "a45", "a46", "a47", "a48", "a49", "a50", "a51", "a52", "a53", "a54", "a55", \
    "a56", "a57", "a58", "a59", "a60", "a61", "a62", "a63", "a64", "a65", "a66", "a67", "a68", "a69", "a70", "a71", "a72", "a73", "a74", "a75", "a76", "a77", "a78", "a79", \
    "a80", "a81", "a82", "a83", "a84", "a85", "a86", "a87", "a88", "a89", "a90", "a91", "a92", "a93", "a94", "a95", "a96", "a97", "a98", "a99", "a100", "a101", "a102", "a103", \
    "a104", "a105", "a106", "a107", "a108", "a109", "a110", "a111", "a112", "a113", "a114", "a115", "a116", "a117", "a118", "a119", "a120", "a121", "a122", "a123", "a124", "a125", "a126", "a127", \
    "a128", "a129", "a130", "a131", "a132", "a133", "a134", "a135", "a136", "a137", "a138", "a139", "a140", "a141", "a142", "a143", "a144", "a145", "a146", "a147", "a148", "a149", "a150", "a151", \
    "a152", "a153", "a154", "a155", "a156", "a157", "a158", "a159", "a160", "a161", "a162", "a163", "a164", "a165", "a166", "a167", "a168", "a169", "a170", "a171", "a172", "a173", "a174", "a175", \
    "a176", "a177", "a178", "a179", "a180", "a181", "a182", "a183", "a184", "a185", "a186", "a187", "a188", "a189", "a190", "a191", "a192", "a193", "a194", "a195", "a196", "a197", "a198", "a199", \
    "a200", "a201", "a202", "a203", "a204", "a205", "a206", "a207", "a208", "a209", "a210", "a211", "a212", "a213", "a214", "a215", "a216", "a217", "a218", "a219", "a220", "a221", "a222", "a223", \
    "a224", "a225", "a226", "a227", "a228", "a229", "a230", "a231", "a232", "a233", "a234", "a235", "a236", "a237", "a238", "a239", "a240", "a241", "a242", "a243", "a244", "a245", "a246", "a247", \
    "a248", "a249", "a250", "a251", "a252", "a253", "a254", "a255", "s38", "s39", "s40", "s41", "s42", "s43", "s44", "s45", "s46", "s47", "s48", "s49", "s50", "s51", "s52", "s53", \
    "s54", "s55", "s56", "s57", "s58", "s59", "s60", "s61", "s62", "s63", "s64", "s65", "s66", "s67", "s68", "s69", "s70", "s71", "s72", "s73", "s74", "s75", "s76", "s77", \
    "s78", "s79", "s80", "s81", "s82", "s83", "s84", "s85", "s86", "s87", "s88", "s89", "s90", "s91", "s92", "s93", "s94", "s95", "s96", "s97", "s98", "s99", "s100", "s101"
#define LANES() const int tid = threadIdx.x, lane = tid & 63, c = lane & 15, g = lane >> 4, q = c >> 2, p4 = c & 3; \
    const int wave = __builtin_amdgcn_readfirstlane(tid >> 6); u32* tab = (u32*)(smem + L_TAB) + tid; (void)q; (void)p4; (void)g
struct Par { u32 role, fin, fout, src, dst, woff; };
__device__ __forceinline__ void x_lanes(u32* tab, int lane, int r, int rm, int rc, u32 rd) {
    const int c = lane & 15, g = lane >> 4, b = lane & 7;
    tab[0] = rd + (g * 16 + (c ^ g)) * 16;
    tab[256] = rd + (g * 16 + (c ^ (4 + g))) * 16;
    tab[512] = (rm * 18 + (b >> 2)) * 1024 + ((b & 3) * 16 + (rc ^ b)) * 16;
    tab[768] = r * 1152 + b * 16;
}

__device__ __forceinline__ Par role_h(u32 layer, u32 h, char* smem) {
    LANES();
    const int r = 8 * wave + (lane >> 3);
    x_lanes(tab, lane, r, r >> 4, r & 15, 0x10000);
    tab[4 * 256] = tid * 16;
    tab[5 * 256] = (4 * wave + g) * 8192 + (c ^ ((g | (wave >> 1) << 2) << 1)) * 16;
    tab[6 * 256] = (8 * g + q) * 256 + ((2 * wave) ^ (q | (g & 1) << 2)) * 32 + p4 * 8;
    tab[7 * 256] = tab[6 * 256] ^ 32;
    return Par{1, OFF_CXNB + layer * 4096, OFF_FKV + (layer * 32 + h) * 256, OFF_XNB + layer * XB, OFF_QX + h * 16384, h * 256};
}

__device__ __forceinline__ Par role_w(u32 layer, u32 kb, u32 nb, char* smem) {
    LANES();
    tab[0] = 0x10000 + lane * 16;
    tab[256] = wave * 16384 + lane * 16;
    tab[512] = wave * 32768 + lane * 16;
    tab[768] = c * 2304 + g * 16 + wave * 192;
    tab[4 * 256] = (8 * wave + (lane >> 3)) * 1152 + ((lane & 7) ^ ((lane >> 5) << 2)) * 16;
#pragma unroll
    for (int t = 0; t < 3; ++t) {
        const int T = 3 * (wave & 1) + t;
        tab[(5 + t) * 256] = (T >> 1) * 4096 + (16 * (q & 1) + 4 * g + (q >> 1)) * 128 + ((T ^ g) & 1) * 64 + p4 * 16 + (wave >> 1) * 8;
    }
    return Par{2, OFF_COB + layer * 16384 + kb * 64, OFF_CPB + layer * 4096, OFF_OX + kb * 8 * 16384, OFF_PB + layer * 8 * XB + kb * 32 * 2304 + nb * 768,
                 kb * 1024 * 1152 + nb * 384};
}

__device__ __forceinline__ Par role_a2(u32 layer, u32 i, char* smem) {
    LANES();
    const int hd = 8 * wave + (lane >> 3);
    tab[0] = (wave & 1) * 18432 + lane * 16;
    tab[256] = wave * 4096 + lane * 16;
    tab[512] = wave * 8192 + lane * 16;
    tab[768] = (2 * c + (wave & 1)) * 2304 + g * 16 + (wave >> 1) * 384;
    tab[4 * 256] = (16 + (hd >> 4)) * 1024 + (((hd >> 2) & 3) * 16 + 2 * (lane & 7)) * 16 + (hd & 3) * 4;
    tab[5 * 256] = 32768 + tid * 16;
    tab[6 * 256] = (16 * wave + (lane >> 2)) * 1152 + (lane & 3) * 16;
    tab[7 * 256] = (4 * g + q) * 64 + p4 * 16 + (wave >> 1) * 8;
    tab[8 * 256] = (72 * g + 16 + q) * 64 + p4 * 16 + (wave >> 1) * 8;
    return Par{4, OFF_COA + layer * 4096, OFF_CPA + layer * 4096, OFF_OA + layer * XB, OFF_AO + layer * 2 * XB + (i >> 1) * 768 + (i & 1) * 192, i * 192};
}

__device__ __forceinline__ Par role_a1(u32 layer, u32 hg, char* smem) {
    LANES();
    const int r = 8 * wave + (lane >> 3);
    x_lanes(tab, lane, r, r & 1, r >> 1, 0);
    tab[4 * 256] = (4 * hg + wave) * 1024 + lane * 16;
    tab[5 * 256] = g ? OOB : 32768 + (4 * hg + wave) * 128 + c * 8;
    tab[6 * 256] = L_EX + tid * 8;
    tab[7 * 256] = L_EX + ((wave >> 1) * 16 + c) * 8 + (wave & 1) * 4;
    tab[8 * 256] = g ? L_EX + 2048 : tab[7 * 256] + 256;
    tab[9 * 256] = (c & 2) ? L_EX + 2056 : L_EX + 1024 + (g * 16 + 2 * wave + (c & 1)) * 8;
    tab[10 * 256] = 4 * g - c;
    tab[11 * 256] = (8 * g + q) * 144 + 36 * wave + 4 * (wave & 1) + p4 * 8;
    tab[12 * 256] = (wave < 2 ? p4 >> 1 : 2) * 9216 + (8 * g + q) * 144 + 72 * (p4 & 1) + 32;
    if (tid < 2) *(v4u*)(smem + L_EX + 2048 + tid * 512) = v4u{0u, 0u, 0x3f803f80u, 0x3f803f80u};
    return Par{3, OFF_CXNA + layer * 4096, OFF_COA + layer * 4096, OFF_XNA + layer * XB, OFF_OA + layer * XB, hg * 144};
}

#define SB() __builtin_amdgcn_sched_barrier(0)
__device__ __forceinline__ u32 now32() { return (u32)__builtin_amdgcn_s_memtime(); }
__device__ __forceinline__ Par role_chain(u32 layer, u32 h, u32 p, rsrc_t rs, char* smem) {
    LANES();
    {
        const v4u ff = {~0u, ~0u, ~0u, ~0u};
#pragma unroll
        for (int i = 0; i < 6; ++i) {
            const int pg = (layer * 64 + 2 * h + p) * 6 + i;
            st16(rs, (pg < 384 ? OFF_XA + pg * 4096 : OFF_XB + (pg - 384) * 4096) + tid * 16, ff);
        }
    }
    const int ro = 16 * (q & 1) + 4 * (q >> 1) + 8 * (g >> 1) + 2 * (g & 1);
    tab[0] = lane * 16;
    tab[256] = L_W + (wave * 15 + (wave == 3 ? 2 : 0)) * 2048 + lane * 16;
    tab[2 * 256] = (4 * wave + g) * 8192 + (c ^ (((g & 1) | wave << 1) << 1)) * 16;
    tab[3 * 256] = (16 * (g >> 1) + 4 * (g & 1) + 8 * (q >> 1) + (q & 1)) * 256 + ((2 * wave) ^ ((q & 1) | (g & 1) << 1 | (q >> 1) << 2)) * 32 + p4 * 8;
    tab[4 * 256] = tab[3 * 256] ^ 32;
    tab[5 * 256] = (2 * wave + (lane >> 5)) * 1152 + ((lane & 31) ^ (wave << 1)) * 16;
    tab[6 * 256] = ro * 512 + ((2 * wave) ^ ((g & 1) | (q >> 1) << 1)) * 32 + p4 * 8;
    tab[7 * 256] = tab[6 * 256] ^ 32;
    tab[8 * 256] = (32 * wave + (lane >> 2)) * 1152 + (lane & 3) * 16;
    tab[9 * 256] = ro * 64 + (wave & 1) * 32 + p4 * 8;
    return Par{0, h, p, 0, 0, 0};
}

static_assert(OFF_POL == 64u);
static_assert(OFF_RDY == 128u);
static_assert(OFF_COB == 41984u);
static_assert(OFF_FKV == 74752u);
static_assert(OFF_XA == 0u);
static_assert(OFF_XB == 2097152u);
static_assert(OFF_QX == 75497472u);
static_assert(OFF_KX == 109051904u);
static_assert(OFF_VX == 142606336u);
static_assert(OFF_OX == 176160768u);
static_assert(WS_BYTES == 209715200u);
static_assert(L_TAB == 147456u);
static_assert(L_EX == 139264u);
static_assert(L_W == 31936);
__device__ __forceinline__ void measure_pol(int side, char* ws, rsrc_t rs, char* smem) {
    const int wave = __builtin_amdgcn_readfirstlane(threadIdx.x >> 6);
    u32 best[4] = {~0u, ~0u, ~0u, ~0u};
    if (wave < 2) {
        for (int n = 0; n < 12; ++n)
#pragma unroll
            for (int i = 0; i < 4; ++i) {
                SB();
                const u32 t0 = now32();
                SB();
                st4(rs, wave * MB2 + (508 + i) * 4096 + n * 128, t0);
                DRAIN();
                const u32 t = now32() - t0;
                best[i] = t < best[i] ? t : best[i];
            }
        if ((threadIdx.x & 63) == 0) ((u32*)smem)[wave] = (best[0] + best[1] < best[2] + best[3] ? 0u : 1u) ^ (u32)side;
    }
    __syncthreads();
    if (threadIdx.x == 0) __hip_atomic_store((gu32*)(ws + OFF_POL), 4u | ((u32*)smem)[0] | (((u32*)smem)[1] << 1), RLX_AGENT);
}

__global__ __launch_bounds__(256, 1) void mega_kernel(const u16* __restrict__ x0, const u16* __restrict__ wqkv, const u16* __restrict__ woA,
                                                     const u16* __restrict__ mem0, const u16* __restrict__ Wq, const u16* __restrict__ Wk,
                                                     const u16* __restrict__ Wv, const u16* __restrict__ Wo, u16* out, char* ws) {
    __shared__ __attribute__((aligned(16))) char smem[163840];
    char* wd = (char*)(((uintptr_t)ws + SYNC_BYTES + MB2 - 1) & ~(uintptr_t)(MB2 - 1));
    const rsrc_t rs = __builtin_amdgcn_make_buffer_rsrc(wd, 0, WS_BYTES, 0x00020000);
    u32 xcc;
    asm volatile("s_getreg_b32 %0, hwreg(HW_REG_XCC_ID)" : "=s"(xcc));
    xcc &= 7;
    if (threadIdx.x == 0) {
        u32 x = xcc, t;
        for (;;) {
            t = __hip_atomic_fetch_add((gu32*)(ws + OFF_ROLE + x * 64), 1u, RLX_AGENT);
            if (t < 32) break;
            x = (x + 1) & 7;
        }
        *(u32*)smem = x * 32 + t;
    }
    {
        const int sh = blockIdx.x;
        v4u a[23];
        const int t = threadIdx.x;
        a[20] = ((const v4u*)wqkv)[sh * 486 + t]; a[21] = ((const v4u*)wqkv)[sh * 486 + (t + 256 < 486 ? t + 256 : t)];
        a[22] = ((const v4u*)woA)[sh * 162 + (t < 162 ? t : 0)];
#pragma unroll
        for (int i = 0; i < 5; ++i) {
            const int c = sh * 1152 + (t + 256 * i < 1152 ? t + 256 * i : t);
            a[i] = ((const v4u*)Wq)[c]; a[5 + i] = ((const v4u*)Wk)[c]; a[10 + i] = ((const v4u*)Wv)[c]; a[15 + i] = ((const v4u*)Wo)[c];
        }
#pragma unroll
        for (int i = 0; i < 23; ++i) asm volatile("" :: "v"(a[i]));
    }
    __syncthreads();
    const int rid = __builtin_amdgcn_readfirstlane(*(const u32*)smem);
    __syncthreads();
    STAMP(159, 0);
    const int xq = (rid >> 5) & 3, t = rid & 31, layer = rid >> 7;
    if (rid == 29) measure_pol(xcc >> 2, ws, rs, smem);
    Par P{5, 0, 0, 0, 0, 0};
    const u16* w0 = Wq;
    if (layer && (t == 29 || (t >= 24 && t < 27))) role_t0((t == 29 ? 3 : t - 24) * 4 + xq, x0, ws, rs);
    if (t < 16) P = role_chain(layer, xq * 8 + (t >> 1), t & 1, rs, smem);
    else if (t < 24) P = role_h(layer, xq * 8 + t - 16, smem);
    else if (t < 27) { P = role_w(layer, xq, t - 24, smem); w0 = Wo; }
    else if (t < 29) { P = role_a1(layer, 2 * xq + t - 27, smem); w0 = wqkv; }
    else if (t == 29) role_r<4, 1>(rid, xq, layer, x0, out, ws, wd, rs);
    else if (t == 31 && xq >= 2) role_r<1, 2>(rid, xq - 2, layer, x0, out, ws, wd, rs);
    else { P = role_a2(layer, (t - 30) * 4 + xq, smem); w0 = woA; }
    const int wave = __builtin_amdgcn_readfirstlane(threadIdx.x >> 6);
    asm volatile(
    "s_cmp_eq_u32 %15, 1\n"
    "s_cbranch_scc1 .Lrole1_%=\n"
    "s_cmp_eq_u32 %15, 2\n"
    "s_cbranch_scc1 .Lrole2_%=\n"
    "s_cmp_eq_u32 %15, 3\n"
    "s_cbranch_scc1 .Lrole3_%=\n"
    "s_cmp_eq_u32 %15, 4\n"
    "s_cbranch_scc1 .Lrole4_%=\n"
    "s_cmp_eq_u32 %15, 5\n"
    "s_cbranch_scc1 .Lrole5_%=\n"
    "s_mov_b64 s[40:41], %0\n"
    "s_and_b32 s41, s41, 0xffff\n"
    "s_mov_b32 s42, 0xc800000\n"
    "s_mov_b32 s43, 0x20000\n"
    "s_mov_b32 s57, 131072\n"
    "v_mbcnt_lo_u32_b32 v10, -1, 0\n"
    "v_mbcnt_hi_u32_b32 v10, -1, v10\n"
    "s_lshl_b32 s73, %2, 8\n"
    "s_add_u32 s73, s73, 147456\n"
    "v_lshl_add_u32 v10, v10, 2, s73\n"
    "ds_read_b32 v0, v10\n"
    "ds_read_b32 v2, v10 offset:1024\n"
    "ds_read_b32 v1, v10 offset:2048\n"
    "ds_read_b32 v3, v10 offset:3072\n"
    "ds_read_b32 v4, v10 offset:4096\n"
    "ds_read_b32 v5, v10 offset:5120\n"
    "ds_read_b32 v6, v10 offset:6144\n"
    "ds_read_b32 v7, v10 offset:7168\n"
    "ds_read_b32 v8, v10 offset:8192\n"
    "ds_read_b32 v9, v10 offset:9216\n"
    "s_waitcnt lgkmcnt(0)\n"
    "s_barrier\n"
    "s_lshl_b32 s60, %4, 8\n"
    "s_lshl_b32 s61, %2, 10\n"
    "s_mul_i32 s63, %4, 147456\n"
    "s_mul_i32 s62, %5, 576\n"
    "s_add_u32 s63, s63, s62\n"
    "s_mov_b64 s[44:45], %13\n"
    "s_and_b32 s45, s45, 0xffff\n"
    "s_mov_b32 s46, 0x480000\n"
    "s_mov_b32 s47, 0x20000\n"
    "s_lshl_b32 s73, s61, 1\n"
    "s_add_u32 m0, s73, 0\n"
    "s_add_u32 s62, s63, 512\n"
    "buffer_load_dwordx4 v8, s[44:47], s62 offen lds\n"
    "s_lshl_b32 s73, s61, 1\n"
    "s_add_u32 m0, s73, 1024\n"
    "s_add_u32 s62, s63, 18944\n"
    "buffer_load_dwordx4 v8, s[44:47], s62 offen lds\n"
    "s_mov_b64 s[44:45], %10\n"
    "s_and_b32 s45, s45, 0xffff\n"
    "s_mov_b32 s46, 0x480000\n"
    "s_mov_b32 s47, 0x20000\n"
    "s_add_u32 m0, s61, 16384\n"
    "s_add_u32 s62, s60, 2097152\n"
    "buffer_load_dwordx4 v1, s[44:47], s62 offen lds\n"
    "s_add_u32 m0, s61, 20480\n"
    "s_add_u32 s62, s60, 2228224\n"
    "buffer_load_dwordx4 v1, s[44:47], s62 offen lds\n"
    "s_add_u32 m0, s61, 24576\n"
    "s_add_u32 s62, s60, 2359296\n"
    "buffer_load_dwordx4 v1, s[44:47], s62 offen lds\n"
    "s_add_u32 m0, s61, 28672\n"
    "s_add_u32 s62, s60, 2490368\n"
    "buffer_load_dwordx4 v1, s[44:47], s62 offen lds\n"
    "s_add_u32 m0, s61, 32768\n"
    "s_add_u32 s62, s60, 2621440\n"
    "buffer_load_dwordx4 v1, s[44:47], s62 offen lds\n"
    "s_add_u32 m0, s61, 36864\n"
    "s_add_u32 s62, s60, 2752512\n"
    "buffer_load_dwordx4 v1, s[44:47], s62 offen lds\n"
    "s_add_u32 m0, s61, 40960\n"
    "s_add_u32 s62, s60, 2883584\n"
    "buffer_load_dwordx4 v1, s[44:47], s62 offen lds\n"
    "s_add_u32 m0, s61, 45056\n"
    "s_add_u32 s62, s60, 3014656\n"
    "buffer_load_dwordx4 v1, s[44:47], s62 offen lds\n"
    "s_add_u32 m0, s61, 49152\n"
    "s_add_u32 s62, s60, 3145728\n"
    "buffer_load_dwordx4 v1, s[44:47], s62 offen lds\n"
    "s_add_u32 m0, s61, 53248\n"
    "s_add_u32 s62, s60, 3276800\n"
    "buffer_load_dwordx4 v1, s[44:47], s62 offen lds\n"
    "s_add_u32 m0, s61, 57344\n"
    "s_add_u32 s62, s60, 3407872\n"
    "buffer_load_dwordx4 v1, s[44:47], s62 offen lds\n"
    "s_add_u32 m0, s61, 61440\n"
    "s_add_u32 s62, s60, 3538944\n"
    "buffer_load_dwordx4 v1, s[44:47], s62 offen lds\n"
    "s_add_u32 m0, s61, 65536\n"
    "s_add_u32 s62, s60, 3670016\n"
    "buffer_load_dwordx4 v1, s[44:47], s62 offen lds\n"
    "s_add_u32 m0, s61, 69632\n"
    "s_add_u32 s62, s60, 3801088\n"
    "buffer_load_dwordx4 v1, s[44:47], s62 offen lds\n"
    "s_add_u32 m0, s61, 73728\n"
    "s_add_u32 s62, s60, 3932160\n"
    "buffer_load_dwordx4 v1, s[44:47], s62 offen lds\n"
    "s_add_u32 m0, s61, 77824\n"
    "s_add_u32 s62, s60, 4063232\n"
    "buffer_load_dwordx4 v1, s[44:47], s62 offen lds\n"
    "s_add_u32 m0, s61, 81920\n"
    "s_add_u32 s62, s60, 4194304\n"
    "buffer_load_dwordx4 v1, s[44:47], s62 offen lds\n"
    "s_add_u32 m0, s61, 86016\n"
    "s_add_u32 s62, s60, 4325376\n"
    "buffer_load_dwordx4 v1, s[44:47], s62 offen lds\n"
    "s_add_u32 m0, s61, 90112\n"
    "s_add_u32 s62, s60, 4456448\n"
    "buffer_load_dwordx4 v1, s[44:47], s62 offen lds\n"
    "s_add_u32 m0, s61, 94208\n"
    "s_add_u32 s62, s60, 4587520\n"
    "buffer_load_dwordx4 v1, s[44:47], s62 offen lds\n"
    "s_mov_b64 s[44:45], %11\n"
    "s_and_b32 s45, s45, 0xffff\n"
    "s_mov_b32 s46, 0x480000\n"
    "s_mov_b32 s47, 0x20000\n"
    "s_add_u32 m0, s61, 98304\n"
    "s_add_u32 s62, s60, 0\n"
    "buffer_load_dwordx4 v1, s[44:47], s62 offen lds\n"
    "s_add_u32 m0, s61, 102400\n"
    "s_add_u32 s62, s60, 131072\n"
    "buffer_load_dwordx4 v1, s[44:47], s62 offen lds\n"
    "s_add_u32 m0, s61, 106496\n"
    "s_add_u32 s62, s60, 262144\n"
    "buffer_load_dwordx4 v1, s[44:47], s62 offen lds\n"
    "s_add_u32 m0, s61, 110592\n"
    "s_add_u32 s62, s60, 393216\n"
    "buffer_load_dwordx4 v1, s[44:47], s62 offen lds\n"
    "s_add_u32 m0, s61, 114688\n"
    "s_add_u32 s62, s60, 524288\n"
    "buffer_load_dwordx4 v1, s[44:47], s62 offen lds\n"
    "s_add_u32 m0, s61, 118784\n"
    "s_add_u32 s62, s60, 655360\n"
    "buffer_load_dwordx4 v1, s[44:47], s62 offen lds\n"
    "s_add_u32 m0, s61, 122880\n"
    "s_add_u32 s62, s60, 786432\n"
    "buffer_load_dwordx4 v1, s[44:47], s62 offen lds\n"
    "s_add_u32 m0, s61, 126976\n"
    "s_add_u32 s62, s60, 917504\n"
    "buffer_load_dwordx4 v1, s[44:47], s62 offen lds\n"
    "s_add_u32 m0, s61, 131072\n"
    "s_add_u32 s62, s60, 1048576\n"
    "buffer_load_dwordx4 v1, s[44:47], s62 offen lds\n"
    "s_add_u32 m0, s61, 135168\n"
    "s_add_u32 s62, s60, 1179648\n"
    "buffer_load_dwordx4 v1, s[44:47], s62 offen lds\n"
    "s_add_u32 m0, s61, 139264\n"
    "s_add_u32 s62, s60, 1310720\n"
    "buffer_load_dwordx4 v1, s[44:47], s62 offen lds\n"
    "s_add_u32 m0, s61, 143360\n"
    "s_add_u32 s62, s60, 1441792\n"
    "buffer_load_dwordx4 v1, s[44:47], s62 offen lds\n"
    "s_add_u32 m0, s61, 147456\n"
    "s_add_u32 s62, s60, 1572864\n"
    "buffer_load_dwordx4 v1, s[44:47], s62 offen lds\n"
    "s_add_u32 m0, s61, 151552\n"
    "s_add_u32 s62, s60, 1703936\n"
    "buffer_load_dwordx4 v1, s[44:47], s62 offen lds\n"
    "s_add_u32 m0, s61, 155648\n"
    "s_add_u32 s62, s60, 1835008\n"
    "buffer_load_dwordx4 v1, s[44:47], s62 offen lds\n"
    "s_add_u32 m0, s61, 159744\n"
    "s_add_u32 s62, s60, 1966080\n"
    "buffer_load_dwordx4 v1, s[44:47], s62 offen lds\n"
    "s_waitcnt vmcnt(32) lgkmcnt(0)\n"
    "s_barrier\n"
    "ds_read_b64_tr_b16 v[144:145], v3 offset:16384\n"
    "ds_read_b64_tr_b16 v[146:147], v3 offset:16896\n"
    "ds_read_b64_tr_b16 v[148:149], v3 offset:24576\n"
    "ds_read_b64_tr_b16 v[150:151], v3 offset:25088\n"
    "ds_read_b64_tr_b16 v[152:153], v4 offset:16384\n"
    "ds_read_b64_tr_b16 v[154:155], v4 offset:16896\n"
    "ds_read_b64_tr_b16 v[156:157], v4 offset:24576\n"
    "ds_read_b64_tr_b16 v[158:159], v4 offset:25088\n"
    "s_waitcnt vmcnt(28) lgkmcnt(0)\n"
    "s_barrier\n"
    "s_add_u32 m0, s61, 16384\n"
    "s_add_u32 s62, s60, 2097152\n"
    "buffer_load_dwordx4 v1, s[44:47], s62 offen lds\n"
    "s_add_u32 m0, s61, 20480\n"
    "s_add_u32 s62, s60, 2228224\n"
    "buffer_load_dwordx4 v1, s[44:47], s62 offen lds\n"
    "s_add_u32 m0, s61, 24576\n"
    "s_add_u32 s62, s60, 2359296\n"
    "buffer_load_dwordx4 v1, s[44:47], s62 offen lds\n"
    "s_add_u32 m0, s61, 28672\n"
    "s_add_u32 s62, s60, 2490368\n"
    "buffer_load_dwordx4 v1, s[44:47], s62 offen lds\n"
    "ds_read_b64_tr_b16 v[160:161], v3 offset:32768\n"
    "ds_read_b64_tr_b16 v[162:163], v3 offset:33280\n"
    "ds_read_b64_tr_b16 v[164:165], v3 offset:40960\n"
    "ds_read_b64_tr_b16 v[166:167], v3 offset:41472\n"
    "ds_read_b64_tr_b16 v[168:169], v4 offset:32768\n"
    "ds_read_b64_tr_b16 v[170:171], v4 offset:33280\n"
    "ds_read_b64_tr_b16 v[172:173], v4 offset:40960\n"
    "ds_read_b64_tr_b16 v[174:175], v4 offset:41472\n"
    "s_waitcnt vmcnt(28) lgkmcnt(0)\n"
    "s_barrier\n"
    "s_add_u32 m0, s61, 32768\n"
    "s_add_u32 s62, s60, 2621440\n"
    "buffer_load_dwordx4 v1, s[44:47], s62 offen lds\n"
    "s_add_u32 m0, s61, 36864\n"
    "s_add_u32 s62, s60, 2752512\n"
    "buffer_load_dwordx4 v1, s[44:47], s62 offen lds\n"
    "s_add_u32 m0, s61, 40960\n"
    "s_add_u32 s62, s60, 2883584\n"
    "buffer_load_dwordx4 v1, s[44:47], s62 offen lds\n"
    "s_add_u32 m0, s61, 45056\n"
    "s_add_u32 s62, s60, 3014656\n"
    "buffer_load_dwordx4 v1, s[44:47], s62 offen lds\n"
    "ds_read_b64_tr_b16 v[176:177], v3 offset:49152\n"
    "ds_read_b64_tr_b16 v[178:179], v3 offset:49664\n"
    "ds_read_b64_tr_b16 v[180:181], v3 offset:57344\n"
    "ds_read_b64_tr_b16 v[182:183], v3 offset:57856\n"
    "ds_read_b64_tr_b16 v[184:185], v4 offset:49152\n"
    "ds_read_b64_tr_b16 v[186:187], v4 offset:49664\n"
    "ds_read_b64_tr_b16 v[188:189], v4 offset:57344\n"
    "ds_read_b64_tr_b16 v[190:191], v4 offset:57856\n"
    "s_waitcnt vmcnt(28) lgkmcnt(0)\n"
    "s_barrier\n"
    "s_add_u32 m0, s61, 49152\n"
    "s_add_u32 s62, s60, 3145728\n"
    "buffer_load_dwordx4 v1, s[44:47], s62 offen lds\n"
    "s_add_u32 m0, s61, 53248\n"
    "s_add_u32 s62, s60, 3276800\n"
    "buffer_load_dwordx4 v1, s[44:47], s62 offen lds\n"
    "s_add_u32 m0, s61, 57344\n"
    "s_add_u32 s62, s60, 3407872\n"
    "buffer_load_dwordx4 v1, s[44:47], s62 offen lds\n"
    "s_add_u32 m0, s61, 61440\n"
    "s_add_u32 s62, s60, 3538944\n"
    "buffer_load_dwordx4 v1, s[44:47], s62 offen lds\n"
    "v_add_u32 v31, 0x10000, v3\n"
    "ds_read_b64_tr_b16 v[192:193], v31\n"
    "ds_read_b64_tr_b16 v[194:195], v31 offset:512\n"
    "ds_read_b64_tr_b16 v[196:197], v31 offset:8192\n"
    "ds_read_b64_tr_b16 v[198:199], v31 offset:8704\n"
    "v_add_u32 v30, 0x10000, v4\n"
    "ds_read_b64_tr_b16 v[200:201], v30\n"
    "ds_read_b64_tr_b16 v[202:203], v30 offset:512\n"
    "ds_read_b64_tr_b16 v[204:205], v30 offset:8192\n"
    "ds_read_b64_tr_b16 v[206:207], v30 offset:8704\n"
    "s_waitcnt vmcnt(28) lgkmcnt(0)\n"
    "s_barrier\n"
    "s_add_u32 m0, s61, 65536\n"
    "s_add_u32 s62, s60, 3670016\n"
    "buffer_load_dwordx4 v1, s[44:47], s62 offen lds\n"
    "s_add_u32 m0, s61, 69632\n"
    "s_add_u32 s62, s60, 3801088\n"
    "buffer_load_dwordx4 v1, s[44:47], s62 offen lds\n"
    "s_add_u32 m0, s61, 73728\n"
    "s_add_u32 s62, s60, 3932160\n"
    "buffer_load_dwordx4 v1, s[44:47], s62 offen lds\n"
    "s_add_u32 m0, s61, 77824\n"
    "s_add_u32 s62, s60, 4063232\n"
    "buffer_load_dwordx4 v1, s[44:47], s62 offen lds\n"
    "ds_read_b64_tr_b16 v[208:209], v31 offset:16384\n"
    "ds_read_b64_tr_b16 v[210:211], v31 offset:16896\n"
    "ds_read_b64_tr_b16 v[212:213], v31 offset:24576\n"
    "ds_read_b64_tr_b16 v[214:215], v31 offset:25088\n"
    "ds_read_b64_tr_b16 v[216:217], v30 offset:16384\n"
    "ds_read_b64_tr_b16 v[218:219], v30 offset:16896\n"
    "ds_read_b64_tr_b16 v[220:221], v30 offset:24576\n"
    "ds_read_b64_tr_b16 v[222:223], v30 offset:25088\n"
    "s_waitcnt vmcnt(28) lgkmcnt(0)\n"
    "s_barrier\n"
    "s_add_u32 m0, s61, 81920\n"
    "s_add_u32 s62, s60, 4194304\n"
    "buffer_load_dwordx4 v1, s[44:47], s62 offen lds\n"
    "s_add_u32 m0, s61, 86016\n"
    "s_add_u32 s62, s60, 4325376\n"
    "buffer_load_dwordx4 v1, s[44:47], s62 offen lds\n"
    "s_add_u32 m0, s61, 90112\n"
    "s_add_u32 s62, s60, 4456448\n"
    "buffer_load_dwordx4 v1, s[44:47], s62 offen lds\n"
    "s_add_u32 m0, s61, 94208\n"
    "s_add_u32 s62, s60, 4587520\n"
    "buffer_load_dwordx4 v1, s[44:47], s62 offen lds\n"
    "ds_read_b64_tr_b16 v[224:225], v31 offset:32768\n"
    "ds_read_b64_tr_b16 v[226:227], v31 offset:33280\n"
    "ds_read_b64_tr_b16 v[228:229], v31 offset:40960\n"
    "ds_read_b64_tr_b16 v[230:231], v31 offset:41472\n"
    "ds_read_b64_tr_b16 v[232:233], v30 offset:32768\n"
    "ds_read_b64_tr_b16 v[234:235], v30 offset:33280\n"
    "ds_read_b64_tr_b16 v[236:237], v30 offset:40960\n"
    "ds_read_b64_tr_b16 v[238:239], v30 offset:41472\n"
    "s_waitcnt vmcnt(28) lgkmcnt(0)\n"
    "s_barrier\n"
    "s_mov_b64 s[44:45], %12\n"
    "s_and_b32 s45, s45, 0xffff\n"
    "s_mov_b32 s46, 0x480000\n"
    "s_mov_b32 s47, 0x20000\n"
    "s_add_u32 m0, s61, 98304\n"
    "s_add_u32 s62, s60, 0\n"
    "buffer_load_dwordx4 v1, s[44:47], s62 offen lds\n"
    "s_add_u32 m0, s61, 102400\n"
    "s_add_u32 s62, s60, 131072\n"
    "buffer_load_dwordx4 v1, s[44:47], s62 offen lds\n"
    "s_add_u32 m0, s61, 106496\n"
    "s_add_u32 s62, s60, 262144\n"
    "buffer_load_dwordx4 v1, s[44:47], s62 offen lds\n"
    "s_add_u32 m0, s61, 110592\n"
    "s_add_u32 s62, s60, 393216\n"
    "buffer_load_dwordx4 v1, s[44:47], s62 offen lds\n"
    "ds_read_b64_tr_b16 v[240:241], v31 offset:49152\n"
    "ds_read_b64_tr_b16 v[242:243], v31 offset:49664\n"
    "ds_read_b64_tr_b16 v[244:245], v31 offset:57344\n"
    "ds_read_b64_tr_b16 v[246:247], v31 offset:57856\n"
    "ds_read_b64_tr_b16 v[248:249], v30 offset:49152\n"
    "ds_read_b64_tr_b16 v[250:251], v30 offset:49664\n"
    "ds_read_b64_tr_b16 v[252:253], v30 offset:57344\n"
    "ds_read_b64_tr_b16 v[254:255], v30 offset:57856\n"
    "s_waitcnt vmcnt(28) lgkmcnt(0)\n"
    "s_barrier\n"
    "s_add_u32 m0, s61, 114688\n"
    "s_add_u32 s62, s60, 524288\n"
    "buffer_load_dwordx4 v1, s[44:47], s62 offen lds\n"
    "s_add_u32 m0, s61, 118784\n"
    "s_add_u32 s62, s60, 655360\n"
    "buffer_load_dwordx4 v1, s[44:47], s62 offen lds\n"
    "s_add_u32 m0, s61, 122880\n"
    "s_add_u32 s62, s60, 786432\n"
    "buffer_load_dwordx4 v1, s[44:47], s62 offen lds\n"
    "s_add_u32 m0, s61, 126976\n"
    "s_add_u32 s62, s60, 917504\n"
    "buffer_load_dwordx4 v1, s[44:47], s62 offen lds\n"
    "v_add_u32 v29, 0x20000, v3\n"
    "ds_read_b64_tr_b16 a[0:1], v29\n"
    "ds_read_b64_tr_b16 a[2:3], v29 offset:512\n"
    "ds_read_b64_tr_b16 a[4:5], v29 offset:8192\n"
    "ds_read_b64_tr_b16 a[6:7], v29 offset:8704\n"
    "v_add_u32 v28, 0x20000, v4\n"
    "ds_read_b64_tr_b16 a[8:9], v28\n"
    "ds_read_b64_tr_b16 a[10:11], v28 offset:512\n"
    "ds_read_b64_tr_b16 a[12:13], v28 offset:8192\n"
    "ds_read_b64_tr_b16 a[14:15], v28 offset:8704\n"
    "s_waitcnt vmcnt(28) lgkmcnt(0)\n"
    "s_barrier\n"
    "s_add_u32 m0, s61, 131072\n"
    "s_add_u32 s62, s60, 1048576\n"
    "buffer_load_dwordx4 v1, s[44:47], s62 offen lds\n"
    "s_add_u32 m0, s61, 135168\n"
    "s_add_u32 s62, s60, 1179648\n"
    "buffer_load_dwordx4 v1, s[44:47], s62 offen lds\n"
    "s_add_u32 m0, s61, 139264\n"
    "s_add_u32 s62, s60, 1310720\n"
    "buffer_load_dwordx4 v1, s[44:47], s62 offen lds\n"
    "s_add_u32 m0, s61, 143360\n"
    "s_add_u32 s62, s60, 1441792\n"
    "buffer_load_dwordx4 v1, s[44:47], s62 offen lds\n"
    "ds_read_b64_tr_b16 a[16:17], v29 offset:16384\n"
    "ds_read_b64_tr_b16 a[18:19], v29 offset:16896\n"
    "ds_read_b64_tr_b16 a[20:21], v29 offset:24576\n"
    "ds_read_b64_tr_b16 a[22:23], v29 offset:25088\n"
    "ds_read_b64_tr_b16 a[24:25], v28 offset:16384\n"
    "ds_read_b64_tr_b16 a[26:27], v28 offset:16896\n"
    "ds_read_b64_tr_b16 a[28:29], v28 offset:24576\n"
    "ds_read_b64_tr_b16 a[30:31], v28 offset:25088\n"
    "s_waitcnt vmcnt(28) lgkmcnt(0)\n"
    "s_barrier\n"
    "s_add_u32 m0, s61, 147456\n"
    "s_add_u32 s62, s60, 1572864\n"
    "buffer_load_dwordx4 v1, s[44:47], s62 offen lds\n"
    "s_add_u32 m0, s61, 151552\n"
    "s_add_u32 s62, s60, 1703936\n"
    "buffer_load_dwordx4 v1, s[44:47], s62 offen lds\n"
    "s_add_u32 m0, s61, 155648\n"
    "s_add_u32 s62, s60, 1835008\n"
    "buffer_load_dwordx4 v1, s[44:47], s62 offen lds\n"
    "s_add_u32 m0, s61, 159744\n"
    "s_add_u32 s62, s60, 1966080\n"
    "buffer_load_dwordx4 v1, s[44:47], s62 offen lds\n"
    "ds_read_b64_tr_b16 a[32:33], v3 offset:16384\n"
    "ds_read_b64_tr_b16 a[34:35], v3 offset:16896\n"
    "ds_read_b64_tr_b16 a[36:37], v3 offset:24576\n"
    "ds_read_b64_tr_b16 a[38:39], v3 offset:25088\n"
    "ds_read_b64_tr_b16 a[40:41], v4 offset:16384\n"
    "ds_read_b64_tr_b16 a[42:43], v4 offset:16896\n"
    "ds_read_b64_tr_b16 a[44:45], v4 offset:24576\n"
    "ds_read_b64_tr_b16 a[46:47], v4 offset:25088\n"
    "s_waitcnt vmcnt(28) lgkmcnt(0)\n"
    "s_barrier\n"
    "s_add_u32 m0, s61, 16384\n"
    "s_add_u32 s62, s60, 2097152\n"
    "buffer_load_dwordx4 v1, s[44:47], s62 offen lds\n"
    "s_add_u32 m0, s61, 20480\n"
    "s_add_u32 s62, s60, 2228224\n"
    "buffer_load_dwordx4 v1, s[44:47], s62 offen lds\n"
    "s_add_u32 m0, s61, 24576\n"
    "s_add_u32 s62, s60, 2359296\n"
    "buffer_load_dwordx4 v1, s[44:47], s62 offen lds\n"
    "s_add_u32 m0, s61, 28672\n"
    "s_add_u32 s62, s60, 2490368\n"
    "buffer_load_dwordx4 v1, s[44:47], s62 offen lds\n"
    "ds_read_b64_tr_b16 a[48:49], v3 offset:32768\n"
    "ds_read_b64_tr_b16 a[50:51], v3 offset:33280\n"
    "ds_read_b64_tr_b16 a[52:53], v3 offset:40960\n"
    "ds_read_b64_tr_b16 a[54:55], v3 offset:41472\n"
    "ds_read_b64_tr_b16 a[56:57], v4 offset:32768\n"
    "ds_read_b64_tr_b16 a[58:59], v4 offset:33280\n"
    "ds_read_b64_tr_b16 a[60:61], v4 offset:40960\n"
    "ds_read_b64_tr_b16 a[62:63], v4 offset:41472\n"
    "s_waitcnt vmcnt(28) lgkmcnt(0)\n"
    "s_barrier\n"
    "s_add_u32 m0, s61, 32768\n"
    "s_add_u32 s62, s60, 2621440\n"
    "buffer_load_dwordx4 v1, s[44:47], s62 offen lds\n"
    "s_add_u32 m0, s61, 36864\n"
    "s_add_u32 s62, s60, 2752512\n"
    "buffer_load_dwordx4 v1, s[44:47], s62 offen lds\n"
    "s_add_u32 m0, s61, 40960\n"
    "s_add_u32 s62, s60, 2883584\n"
    "buffer_load_dwordx4 v1, s[44:47], s62 offen lds\n"
    "s_add_u32 m0, s61, 45056\n"
    "s_add_u32 s62, s60, 3014656\n"
    "buffer_load_dwordx4 v1, s[44:47], s62 offen lds\n"
    "ds_read_b64_tr_b16 a[64:65], v3 offset:49152\n"
    "ds_read_b64_tr_b16 a[66:67], v3 offset:49664\n"
    "ds_read_b64_tr_b16 a[68:69], v3 offset:57344\n"
    "ds_read_b64_tr_b16 a[70:71], v3 offset:57856\n"
    "ds_read_b64_tr_b16 a[72:73], v4 offset:49152\n"
    "ds_read_b64_tr_b16 a[74:75], v4 offset:49664\n"
    "ds_read_b64_tr_b16 a[76:77], v4 offset:57344\n"
    "ds_read_b64_tr_b16 a[78:79], v4 offset:57856\n"
    "s_waitcnt vmcnt(28) lgkmcnt(0)\n"
    "s_barrier\n"
    "s_add_u32 m0, s61, 49152\n"
    "s_add_u32 s62, s60, 3145728\n"
    "buffer_load_dwordx4 v1, s[44:47], s62 offen lds\n"
    "s_add_u32 m0, s61, 53248\n"
    "s_add_u32 s62, s60, 3276800\n"
    "buffer_load_dwordx4 v1, s[44:47], s62 offen lds\n"
    "s_add_u32 m0, s61, 57344\n"
    "s_add_u32 s62, s60, 3407872\n"
    "buffer_load_dwordx4 v1, s[44:47], s62 offen lds\n"
    "s_add_u32 m0, s61, 61440\n"
    "s_add_u32 s62, s60, 3538944\n"
    "buffer_load_dwordx4 v1, s[44:47], s62 offen lds\n"
    "ds_read_b64_tr_b16 a[80:81], v31\n"
    "ds_read_b64_tr_b16 a[82:83], v31 offset:512\n"
    "ds_read_b64_tr_b16 a[84:85], v31 offset:8192\n"
    "ds_read_b64_tr_b16 a[86:87], v31 offset:8704\n"
    "ds_read_b64_tr_b16 a[88:89], v30\n"
    "ds_read_b64_tr_b16 a[90:91], v30 offset:512\n"
    "ds_read_b64_tr_b16 a[92:93], v30 offset:8192\n"
    "ds_read_b64_tr_b16 a[94:95], v30 offset:8704\n"
    "s_waitcnt vmcnt(28) lgkmcnt(0)\n"
    "s_barrier\n"
    "s_add_u32 m0, s61, 65536\n"
    "s_add_u32 s62, s60, 3670016\n"
    "buffer_load_dwordx4 v1, s[44:47], s62 offen lds\n"
    "s_add_u32 m0, s61, 69632\n"
    "s_add_u32 s62, s60, 3801088\n"
    "buffer_load_dwordx4 v1, s[44:47], s62 offen lds\n"
    "s_add_u32 m0, s61, 73728\n"
    "s_add_u32 s62, s60, 3932160\n"
    "buffer_load_dwordx4 v1, s[44:47], s62 offen lds\n"
    "s_add_u32 m0, s61, 77824\n"
    "s_add_u32 s62, s60, 4063232\n"
    "buffer_load_dwordx4 v1, s[44:47], s62 offen lds\n"
    "ds_read_b64_tr_b16 a[96:97], v31 offset:16384\n"
    "ds_read_b64_tr_b16 a[98:99], v31 offset:16896\n"
    "ds_read_b64_tr_b16 a[100:101], v31 offset:24576\n"
    "ds_read_b64_tr_b16 a[102:103], v31 offset:25088\n"
    "ds_read_b64_tr_b16 a[104:105], v30 offset:16384\n"
    "ds_read_b64_tr_b16 a[106:107], v30 offset:16896\n"
    "ds_read_b64_tr_b16 a[108:109], v30 offset:24576\n"
    "ds_read_b64_tr_b16 a[110:111], v30 offset:25088\n"
    "s_waitcnt vmcnt(28) lgkmcnt(0)\n"
    "s_barrier\n"
    "s_add_u32 m0, s61, 81920\n"
    "s_add_u32 s62, s60, 4194304\n"
    "buffer_load_dwordx4 v1, s[44:47], s62 offen lds\n"
    "s_add_u32 m0, s61, 86016\n"
    "s_add_u32 s62, s60, 4325376\n"
    "buffer_load_dwordx4 v1, s[44:47], s62 offen lds\n"
    "s_add_u32 m0, s61, 90112\n"
    "s_add_u32 s62, s60, 4456448\n"
    "buffer_load_dwordx4 v1, s[44:47], s62 offen lds\n"
    "s_add_u32 m0, s61, 94208\n"
    "s_add_u32 s62, s60, 4587520\n"
    "buffer_load_dwordx4 v1, s[44:47], s62 offen lds\n"
    "ds_read_b64_tr_b16 a[112:113], v31 offset:32768\n"
    "ds_read_b64_tr_b16 a[114:115], v31 offset:33280\n"
    "ds_read_b64_tr_b16 a[116:117], v31 offset:40960\n"
    "ds_read_b64_tr_b16 a[118:119], v31 offset:41472\n"
    "ds_read_b64_tr_b16 a[120:121], v30 offset:32768\n"
    "ds_read_b64_tr_b16 a[122:123], v30 offset:33280\n"
    "ds_read_b64_tr_b16 a[124:125], v30 offset:40960\n"
    "ds_read_b64_tr_b16 a[126:127], v30 offset:41472\n"
    "s_waitcnt vmcnt(28) lgkmcnt(0)\n"
    "s_barrier\n"
    "s_mov_b64 s[44:45], %10\n"
    "s_and_b32 s45, s45, 0xffff\n"
    "s_mov_b32 s46, 0x480000\n"
    "s_mov_b32 s47, 0x20000\n"
    "s_add_u32 m0, s61, 98304\n"
    "s_add_u32 s62, s60, 0\n"
    "buffer_load_dwordx4 v1, s[44:47], s62 offen lds\n"
    "s_add_u32 m0, s61, 102400\n"
    "s_add_u32 s62, s60, 131072\n"
    "buffer_load_dwordx4 v1, s[44:47], s62 offen lds\n"
    "s_add_u32 m0, s61, 106496\n"
    "s_add_u32 s62, s60, 262144\n"
    "buffer_load_dwordx4 v1, s[44:47], s62 offen lds\n"
    "s_add_u32 m0, s61, 110592\n"
    "s_add_u32 s62, s60, 393216\n"
    "buffer_load_dwordx4 v1, s[44:47], s62 offen lds\n"
    "ds_read_b64_tr_b16 a[128:129], v31 offset:49152\n"
    "ds_read_b64_tr_b16 a[130:131], v31 offset:49664\n"
    "ds_read_b64_tr_b16 a[132:133], v31 offset:57344\n"
    "ds_read_b64_tr_b16 a[134:135], v31 offset:57856\n"
    "ds_read_b64_tr_b16 a[136:137], v30 offset:49152\n"
    "ds_read_b64_tr_b16 a[138:139], v30 offset:49664\n"
    "ds_read_b64_tr_b16 a[140:141], v30 offset:57344\n"
    "ds_read_b64_tr_b16 a[142:143], v30 offset:57856\n"
    "s_waitcnt vmcnt(28) lgkmcnt(0)\n"
    "s_barrier\n"
    "s_add_u32 m0, s61, 114688\n"
    "s_add_u32 s62, s60, 524288\n"
    "buffer_load_dwordx4 v1, s[44:47], s62 offen lds\n"
    "s_add_u32 m0, s61, 118784\n"
    "s_add_u32 s62, s60, 655360\n"
    "buffer_load_dwordx4 v1, s[44:47], s62 offen lds\n"
    "s_add_u32 m0, s61, 122880\n"
    "s_add_u32 s62, s60, 786432\n"
    "buffer_load_dwordx4 v1, s[44:47], s62 offen lds\n"
    "s_add_u32 m0, s61, 126976\n"
    "s_add_u32 s62, s60, 917504\n"
    "buffer_load_dwordx4 v1, s[44:47], s62 offen lds\n"
    "ds_read_b64_tr_b16 a[144:145], v29\n"
    "ds_read_b64_tr_b16 a[146:147], v29 offset:512\n"
    "ds_read_b64_tr_b16 a[148:149], v29 offset:8192\n"
    "ds_read_b64_tr_b16 a[150:151], v29 offset:8704\n"
    "ds_read_b64_tr_b16 a[152:153], v28\n"
    "ds_read_b64_tr_b16 a[154:155], v28 offset:512\n"
    "ds_read_b64_tr_b16 a[156:157], v28 offset:8192\n"
    "ds_read_b64_tr_b16 a[158:159], v28 offset:8704\n"
    "s_waitcnt vmcnt(28) lgkmcnt(0)\n"
    "s_barrier\n"
    "s_add_u32 m0, s61, 131072\n"
    "s_add_u32 s62, s60, 1048576\n"
    "buffer_load_dwordx4 v1, s[44:47], s62 offen lds\n"
    "s_add_u32 m0, s61, 135168\n"
    "s_add_u32 s62, s60, 1179648\n"
    "buffer_load_dwordx4 v1, s[44:47], s62 offen lds\n"
    "s_add_u32 m0, s61, 139264\n"
    "s_add_u32 s62, s60, 1310720\n"
    "buffer_load_dwordx4 v1, s[44:47], s62 offen lds\n"
    "s_add_u32 m0, s61, 143360\n"
    "s_add_u32 s62, s60, 1441792\n"
    "buffer_load_dwordx4 v1, s[44:47], s62 offen lds\n"
    "ds_read_b64_tr_b16 a[160:161], v29 offset:16384\n"
    "ds_read_b64_tr_b16 a[162:163], v29 offset:16896\n"
    "ds_read_b64_tr_b16 a[164:165], v29 offset:24576\n"
    "ds_read_b64_tr_b16 a[166:167], v29 offset:25088\n"
    "ds_read_b64_tr_b16 a[168:169], v28 offset:16384\n"
    "ds_read_b64_tr_b16 a[170:171], v28 offset:16896\n"
    "ds_read_b64_tr_b16 a[172:173], v28 offset:24576\n"
    "ds_read_b64_tr_b16 a[174:175], v28 offset:25088\n"
    "s_waitcnt vmcnt(28) lgkmcnt(0)\n"
    "s_barrier\n"
    "s_add_u32 m0, s61, 147456\n"
    "s_add_u32 s62, s60, 1572864\n"
    "buffer_load_dwordx4 v1, s[44:47], s62 offen lds\n"
    "s_add_u32 m0, s61, 151552\n"
    "s_add_u32 s62, s60, 1703936\n"
    "buffer_load_dwordx4 v1, s[44:47], s62 offen lds\n"
    "s_add_u32 m0, s61, 155648\n"
    "s_add_u32 s62, s60, 1835008\n"
    "buffer_load_dwordx4 v1, s[44:47], s62 offen lds\n"
    "s_add_u32 m0, s61, 159744\n"
    "s_add_u32 s62, s60, 1966080\n"
    "buffer_load_dwordx4 v1, s[44:47], s62 offen lds\n"
    "ds_read_b64_tr_b16 a[176:177], v3 offset:16384\n"
    "ds_read_b64_tr_b16 a[178:179], v3 offset:16896\n"
    "ds_read_b64_tr_b16 a[180:181], v3 offset:24576\n"
    "ds_read_b64_tr_b16 a[182:183], v3 offset:25088\n"
    "ds_read_b64_tr_b16 a[184:185], v4 offset:16384\n"
    "ds_read_b64_tr_b16 a[186:187], v4 offset:16896\n"
    "ds_read_b64_tr_b16 a[188:189], v4 offset:24576\n"
    "ds_read_b64_tr_b16 a[190:191], v4 offset:25088\n"
    "s_waitcnt vmcnt(28) lgkmcnt(0)\n"
    "s_barrier\n"
    "s_mov_b64 s[44:45], %13\n"
    "s_and_b32 s45, s45, 0xffff\n"
    "s_mov_b32 s46, 0x480000\n"
    "s_mov_b32 s47, 0x20000\n"
    "s_add_u32 m0, s61, 16384\n"
    "s_add_u32 s62, s63, 0\n"
    "buffer_load_dwordx4 v5, s[44:47], s62 offen lds\n"
    "s_add_u32 m0, s61, 20480\n"
    "s_add_u32 s62, s63, 9216\n"
    "buffer_load_dwordx4 v5, s[44:47], s62 offen lds\n"
    "s_add_u32 m0, s61, 24576\n"
    "s_add_u32 s62, s63, 18432\n"
    "buffer_load_dwordx4 v5, s[44:47], s62 offen lds\n"
    "s_add_u32 m0, s61, 28672\n"
    "s_add_u32 s62, s63, 27648\n"
    "buffer_load_dwordx4 v5, s[44:47], s62 offen lds\n"
    "ds_read_b64_tr_b16 a[192:193], v3 offset:32768\n"
    "ds_read_b64_tr_b16 a[194:195], v3 offset:33280\n"
    "ds_read_b64_tr_b16 a[196:197], v3 offset:40960\n"
    "ds_read_b64_tr_b16 a[198:199], v3 offset:41472\n"
    "ds_read_b64_tr_b16 a[200:201], v4 offset:32768\n"
    "ds_read_b64_tr_b16 a[202:203], v4 offset:33280\n"
    "ds_read_b64_tr_b16 a[204:205], v4 offset:40960\n"
    "ds_read_b64_tr_b16 a[206:207], v4 offset:41472\n"
    "s_waitcnt vmcnt(28) lgkmcnt(0)\n"
    "s_barrier\n"
    "s_add_u32 m0, s61, 32768\n"
    "s_add_u32 s62, s63, 36864\n"
    "buffer_load_dwordx4 v5, s[44:47], s62 offen lds\n"
    "s_add_u32 m0, s61, 36864\n"
    "s_add_u32 s62, s63, 46080\n"
    "buffer_load_dwordx4 v5, s[44:47], s62 offen lds\n"
    "s_add_u32 m0, s61, 40960\n"
    "s_add_u32 s62, s63, 55296\n"
    "buffer_load_dwordx4 v5, s[44:47], s62 offen lds\n"
    "s_add_u32 m0, s61, 45056\n"
    "s_add_u32 s62, s63, 64512\n"
    "buffer_load_dwordx4 v5, s[44:47], s62 offen lds\n"
    "ds_read_b64_tr_b16 a[208:209], v3 offset:49152\n"
    "ds_read_b64_tr_b16 a[210:211], v3 offset:49664\n"
    "ds_read_b64_tr_b16 a[212:213], v3 offset:57344\n"
    "ds_read_b64_tr_b16 a[214:215], v3 offset:57856\n"
    "ds_read_b64_tr_b16 a[216:217], v4 offset:49152\n"
    "ds_read_b64_tr_b16 a[218:219], v4 offset:49664\n"
    "ds_read_b64_tr_b16 a[220:221], v4 offset:57344\n"
    "ds_read_b64_tr_b16 a[222:223], v4 offset:57856\n"
    "s_waitcnt vmcnt(28) lgkmcnt(0)\n"
    "s_barrier\n"
    "s_add_u32 m0, s61, 49152\n"
    "s_add_u32 s62, s63, 73728\n"
    "buffer_load_dwordx4 v5, s[44:47], s62 offen lds\n"
    "s_add_u32 m0, s61, 53248\n"
    "s_add_u32 s62, s63, 82944\n"
    "buffer_load_dwordx4 v5, s[44:47], s62 offen lds\n"
    "s_add_u32 m0, s61, 57344\n"
    "s_add_u32 s62, s63, 92160\n"
    "buffer_load_dwordx4 v5, s[44:47], s62 offen lds\n"
    "s_add_u32 m0, s61, 61440\n"
    "s_add_u32 s62, s63, 101376\n"
    "buffer_load_dwordx4 v5, s[44:47], s62 offen lds\n"
    "ds_read_b64_tr_b16 a[224:225], v31\n"
    "ds_read_b64_tr_b16 a[226:227], v31 offset:512\n"
    "ds_read_b64_tr_b16 a[228:229], v31 offset:8192\n"
    "ds_read_b64_tr_b16 a[230:231], v31 offset:8704\n"
    "ds_read_b64_tr_b16 a[232:233], v30\n"
    "ds_read_b64_tr_b16 a[234:235], v30 offset:512\n"
    "ds_read_b64_tr_b16 a[236:237], v30 offset:8192\n"
    "ds_read_b64_tr_b16 a[238:239], v30 offset:8704\n"
    "s_waitcnt vmcnt(28) lgkmcnt(0)\n"
    "s_barrier\n"
    "s_add_u32 m0, s61, 65536\n"
    "s_add_u32 s62, s63, 110592\n"
    "buffer_load_dwordx4 v5, s[44:47], s62 offen lds\n"
    "s_add_u32 m0, s61, 69632\n"
    "s_add_u32 s62, s63, 119808\n"
    "buffer_load_dwordx4 v5, s[44:47], s62 offen lds\n"
    "s_add_u32 m0, s61, 73728\n"
    "s_add_u32 s62, s63, 129024\n"
    "buffer_load_dwordx4 v5, s[44:47], s62 offen lds\n"
    "s_add_u32 m0, s61, 77824\n"
    "s_add_u32 s62, s63, 138240\n"
    "buffer_load_dwordx4 v5, s[44:47], s62 offen lds\n"
    "ds_read_b64_tr_b16 a[240:241], v31 offset:16384\n"
    "ds_read_b64_tr_b16 a[242:243], v31 offset:16896\n"
    "ds_read_b64_tr_b16 a[244:245], v31 offset:24576\n"
    "ds_read_b64_tr_b16 a[246:247], v31 offset:25088\n"
    "ds_read_b64_tr_b16 a[248:249], v30 offset:16384\n"
    "ds_read_b64_tr_b16 a[250:251], v30 offset:16896\n"
    "ds_read_b64_tr_b16 a[252:253], v30 offset:24576\n"
    "ds_read_b64_tr_b16 a[254:255], v30 offset:25088\n"
    "s_waitcnt vmcnt(28) lgkmcnt(0)\n"
    "s_barrier\n"
    "ds_read_b64_tr_b16 v[32:33], v31 offset:32768\n"
    "ds_read_b64_tr_b16 v[34:35], v31 offset:33280\n"
    "ds_read_b64_tr_b16 v[36:37], v31 offset:40960\n"
    "ds_read_b64_tr_b16 v[38:39], v31 offset:41472\n"
    "ds_read_b64_tr_b16 v[40:41], v30 offset:32768\n"
    "ds_read_b64_tr_b16 v[42:43], v30 offset:33280\n"
    "ds_read_b64_tr_b16 v[44:45], v30 offset:40960\n"
    "ds_read_b64_tr_b16 v[46:47], v30 offset:41472\n"
    "s_waitcnt vmcnt(24) lgkmcnt(0)\n"
    "s_barrier\n"
    "ds_read_b64_tr_b16 v[48:49], v31 offset:49152\n"
    "ds_read_b64_tr_b16 v[50:51], v31 offset:49664\n"
    "ds_read_b64_tr_b16 v[52:53], v31 offset:57344\n"
    "ds_read_b64_tr_b16 v[54:55], v31 offset:57856\n"
    "ds_read_b64_tr_b16 v[56:57], v30 offset:49152\n"
    "ds_read_b64_tr_b16 v[58:59], v30 offset:49664\n"
    "ds_read_b64_tr_b16 v[60:61], v30 offset:57344\n"
    "ds_read_b64_tr_b16 v[62:63], v30 offset:57856\n"
    "s_waitcnt vmcnt(20) lgkmcnt(0)\n"
    "s_barrier\n"
    "ds_read_b64_tr_b16 v[64:65], v29\n"
    "ds_read_b64_tr_b16 v[66:67], v29 offset:512\n"
    "ds_read_b64_tr_b16 v[68:69], v29 offset:8192\n"
    "ds_read_b64_tr_b16 v[70:71], v29 offset:8704\n"
    "ds_read_b64_tr_b16 v[72:73], v28\n"
    "ds_read_b64_tr_b16 v[74:75], v28 offset:512\n"
    "ds_read_b64_tr_b16 v[76:77], v28 offset:8192\n"
    "ds_read_b64_tr_b16 v[78:79], v28 offset:8704\n"
    "s_waitcnt vmcnt(16) lgkmcnt(0)\n"
    "s_barrier\n"
    "ds_read_b64_tr_b16 v[80:81], v29 offset:16384\n"
    "ds_read_b64_tr_b16 v[82:83], v29 offset:16896\n"
    "ds_read_b64_tr_b16 v[84:85], v29 offset:24576\n"
    "ds_read_b64_tr_b16 v[86:87], v29 offset:25088\n"
    "ds_read_b64_tr_b16 v[136:137], v28 offset:16384\n"
    "ds_read_b64_tr_b16 v[138:139], v28 offset:16896\n"
    "ds_read_b64_tr_b16 v[140:141], v28 offset:24576\n"
    "ds_read_b64_tr_b16 v[142:143], v28 offset:25088\n"
    "s_waitcnt vmcnt(12) lgkmcnt(0)\n"
    "s_barrier\n"
    "ds_read_b64_tr_b16 v[88:89], v6 offset:16384\n"
    "ds_read_b64_tr_b16 v[90:91], v6 offset:16896\n"
    "ds_read_b64_tr_b16 v[120:121], v6 offset:16640\n"
    "ds_read_b64_tr_b16 v[122:123], v6 offset:17152\n"
    "ds_read_b64_tr_b16 v[96:97], v7 offset:16384\n"
    "ds_read_b64_tr_b16 v[98:99], v7 offset:16896\n"
    "ds_read_b64_tr_b16 v[128:129], v7 offset:16640\n"
    "ds_read_b64_tr_b16 v[130:131], v7 offset:17152\n"
    "s_waitcnt vmcnt(8) lgkmcnt(0)\n"
    "s_barrier\n"
    "ds_read_b64_tr_b16 v[92:93], v6 offset:32768\n"
    "ds_read_b64_tr_b16 v[94:95], v6 offset:33280\n"
    "ds_read_b64_tr_b16 v[124:125], v6 offset:33024\n"
    "ds_read_b64_tr_b16 v[126:127], v6 offset:33536\n"
    "ds_read_b64_tr_b16 v[100:101], v7 offset:32768\n"
    "ds_read_b64_tr_b16 v[102:103], v7 offset:33280\n"
    "ds_read_b64_tr_b16 v[132:133], v7 offset:33024\n"
    "ds_read_b64_tr_b16 v[134:135], v7 offset:33536\n"
    "s_waitcnt vmcnt(4) lgkmcnt(0)\n"
    "s_barrier\n"
    "ds_read_b64_tr_b16 v[104:105], v6 offset:49152\n"
    "ds_read_b64_tr_b16 v[106:107], v6 offset:49664\n"
    "ds_read_b64_tr_b16 v[12:13], v6 offset:49408\n"
    "ds_read_b64_tr_b16 v[14:15], v6 offset:49920\n"
    "ds_read_b64_tr_b16 v[112:113], v7 offset:49152\n"
    "ds_read_b64_tr_b16 v[114:115], v7 offset:49664\n"
    "ds_read_b64_tr_b16 v[20:21], v7 offset:49408\n"
    "ds_read_b64_tr_b16 v[22:23], v7 offset:49920\n"
    "s_waitcnt vmcnt(0) lgkmcnt(0)\n"
    "s_barrier\n"
    "v_add_u32 v11, 0x10000, v6\n"
    "ds_read_b64_tr_b16 v[108:109], v11\n"
    "ds_read_b64_tr_b16 v[110:111], v11 offset:512\n"
    "ds_read_b64_tr_b16 v[16:17], v11 offset:256\n"
    "ds_read_b64_tr_b16 v[18:19], v11 offset:768\n"
    "v_add_u32 v10, 0x10000, v7\n"
    "ds_read_b64_tr_b16 v[116:117], v10\n"
    "ds_read_b64_tr_b16 v[118:119], v10 offset:512\n"
    "ds_read_b64_tr_b16 v[24:25], v10 offset:256\n"
    "ds_read_b64_tr_b16 v[26:27], v10 offset:768\n"
    "s_waitcnt lgkmcnt(0)\n"
    "s_barrier\n"
    "ds_write_b128 v2, v[88:91] offset:14336\n"
    "ds_write_b128 v2, v[92:95] offset:15360\n"
    "ds_write_b128 v2, v[96:99] offset:16384\n"
    "ds_write_b128 v2, v[100:103] offset:17408\n"
    "ds_write_b128 v2, v[104:107] offset:18432\n"
    "ds_write_b128 v2, v[108:111] offset:19456\n"
    "ds_write_b128 v2, v[112:115] offset:20480\n"
    "ds_write_b128 v2, v[116:119] offset:21504\n"
    "ds_write_b128 v2, v[120:123] offset:22528\n"
    "ds_write_b128 v2, v[124:127] offset:23552\n"
    "ds_write_b128 v2, v[128:131] offset:24576\n"
    "ds_write_b128 v2, v[132:135] offset:25600\n"
    "ds_write_b128 v2, v[12:15] offset:26624\n"
    "ds_write_b128 v2, v[16:19] offset:27648\n"
    "ds_write_b128 v2, v[20:23] offset:28672\n"
    "ds_write_b128 v2, v[24:27] offset:29696\n"
    "ds_read_b64_tr_b16 v[88:89], v9\n"
    "ds_read_b64_tr_b16 v[90:91], v9 offset:64\n"
    "ds_read_b64_tr_b16 v[92:93], v9 offset:2048\n"
    "ds_read_b64_tr_b16 v[94:95], v9 offset:2112\n"
    "ds_read_b64_tr_b16 v[96:97], v9 offset:4096\n"
    "ds_read_b64_tr_b16 v[98:99], v9 offset:4160\n"
    "ds_read_b64_tr_b16 v[100:101], v9 offset:6144\n"
    "ds_read_b64_tr_b16 v[102:103], v9 offset:6208\n"
    "ds_write_b128 v2, v[32:35]\n"
    "ds_write_b128 v2, v[36:39] offset:1024\n"
    "ds_write_b128 v2, v[40:43] offset:2048\n"
    "ds_write_b128 v2, v[44:47] offset:3072\n"
    "ds_write_b128 v2, v[48:51] offset:4096\n"
    "ds_write_b128 v2, v[52:55] offset:5120\n"
    "ds_write_b128 v2, v[56:59] offset:6144\n"
    "ds_write_b128 v2, v[60:63] offset:7168\n"
    "ds_write_b128 v2, v[64:67] offset:8192\n"
    "ds_write_b128 v2, v[68:71] offset:9216\n"
    "ds_write_b128 v2, v[72:75] offset:10240\n"
    "ds_write_b128 v2, v[76:79] offset:11264\n"
    "ds_write_b128 v2, v[80:83] offset:12288\n"
    "ds_write_b128 v2, v[84:87] offset:13312\n"
    "v_lshrrev_b32 v112, 4, v0\n"
    "v_and_b32 v113, 15, v112\n"
    "v_lshrrev_b32 v114, 4, v112\n"
    "v_mov_b32 v11, 0\n"
    "v_and_b32 v115, 7, v112\n"
    "v_mul_u32_u24 v115, 1152, v115\n"
    "v_lshrrev_b32 v116, 3, v112\n"
    "v_lshl_add_u32 v117, v116, 4, v115\n"
    "v_and_b32 v115, 1, v114\n"
    "v_lshrrev_b32 v116, 1, v114\n"
    "v_mov_b32 v120, 0\n"
    "v_mov_b32 v121, 0\n"
    "v_mov_b32 v122, 0\n"
    "v_mov_b32 v123, 0\n"
    "ds_write_b128 v11, v[120:123] offset:31872\n"
    "v_mov_b32 v124, 0x3f803f80\n"
    "v_mov_b32 v125, 0x3f803f80\n"
    "v_mov_b32 v126, 0x3f803f80\n"
    "v_mov_b32 v127, 0x3f803f80\n"
    "ds_write_b128 v11, v[124:127] offset:31888\n"
    "v_mov_b32 v128, 0x3f003f00\n"
    "v_mov_b32 v129, 0x3f003f00\n"
    "v_mov_b32 v130, 0x3f003f00\n"
    "v_mov_b32 v131, 0x3f003f00\n"
    "ds_write_b128 v11, v[128:131] offset:31904\n"
    "s_waitcnt lgkmcnt(0)\n"
    "s_cmp_lt_u32 %2, 2\n"
    "s_cbranch_scc1 .Lsk_%=\n"
    "ds_write_b128 v2, v[88:91] offset:30720\n"
    "ds_write_b128 v2, v[92:95] offset:31744\n"
    "ds_write_b128 v2, v[96:99] offset:32768\n"
    "ds_write_b128 v2, v[100:103] offset:33792\n"
    ".Lsk_%=:\n"
    "s_cmp_eq_u32 %2, 3\n"
    "s_cbranch_scc1 .Lsm_%=\n"
    "s_mov_b64 s[44:45], %14\n"
    "s_and_b32 s45, s45, 0xffff\n"
    "s_mov_b32 s46, 0x2400\n"
    "s_mov_b32 s47, 0x20000\n"
    "s_mul_i32 s62, %2, 384\n"
    "s_mul_i32 s61, %2, 3072\n"
    "s_add_u32 m0, s61, 20480\n"
    "s_add_u32 s73, s62, 0\n"
    "buffer_load_dwordx4 v117, s[44:47], s73 offen lds\n"
    "s_add_u32 m0, s61, 21504\n"
    "s_add_u32 s73, s62, 128\n"
    "buffer_load_dwordx4 v117, s[44:47], s73 offen lds\n"
    "s_add_u32 m0, s61, 22528\n"
    "s_add_u32 s73, s62, 256\n"
    "buffer_load_dwordx4 v117, s[44:47], s73 offen lds\n"
    ".Lsm_%=:\n"
    "s_waitcnt vmcnt(0) lgkmcnt(0)\n"
    "s_barrier\n"
    "s_cmp_eq_u32 %2, 0\n"
    "s_cbranch_scc0 .Lnr_%=\n"
    "v_mov_b32 v134, 128\n"
    "v_mov_b32 v135, 1\n"
    "s_mov_b64 exec, 1\n"
    "global_atomic_add v134, v135, %1\n"
    "s_mov_b64 exec, -1\n"
    ".Lnr_%=:\n"
    "v_mov_b32 v1, 0x4444\n"
    "v_mov_b32 v118, 0xeeee\n"
    "v_cmp_gt_u32 vcc, 8, v113\n"
    "s_nop 1\n"
    "v_cndmask_b32 v1, v118, v1, vcc\n"
    "s_lshl_b32 s73, %2, 10\n"
    "v_add_u32 v5, s73, v0\n"
    "v_and_b32 v119, 7, v113\n"
    "v_lshrrev_b32 v120, 3, v113\n"
    "v_lshl_add_u32 v3, v115, 1, v116\n"
    "v_mul_u32_u24 v3, 544, v3\n"
    "v_lshl_add_u32 v3, v113, 2, v3\n"
    "s_lshl_b32 s73, %2, 6\n"
    "s_add_u32 s73, s73, 20480\n"
    "v_add_u32 v3, s73, v3\n"
    "v_mul_u32_u24 v4, 272, v119\n"
    "v_lshl_add_u32 v4, v114, 4, v4\n"
    "v_lshlrev_b32 v6, 7, v114\n"
    "v_lshl_add_u32 v6, v119, 4, v6\n"
    "s_lshl_b32 s73, %2, 9\n"
    "v_add_u32 v6, s73, v6\n"
    "s_mov_b32 s56, 0\n"
    "v_mov_b32 v134, 64\n"
    ".Lpoll1_%=:\n"
    "global_load_dword v135, v134, %1 sc1\n"
    "s_waitcnt vmcnt(0)\n"
    "v_readfirstlane_b32 s73, v135\n"
    "s_cmp_ge_u32 s73, 4\n"
    "s_cbranch_scc1 .Lpoll1_x%=\n"
    "s_add_u32 s56, s56, 1\n"
    "s_cmp_lt_u32 s56, 3\n"
    "s_cbranch_scc1 .Lpoll1_%=\n"
    "s_sleep 8\n"
    "s_cmp_lt_u32 s56, s57\n"
    "s_cbranch_scc1 .Lpoll1_%=\n"
    "s_mov_b32 s57, 0\n"
    "v_mov_b32 v135, 1\n"
    "global_store_dword v11, v135, %1 sc1\n"
    ".Lpoll1_x%=:\n"
    "s_xor_b32 s69, s73, %9\n"
    "s_lshr_b32 s51, s73, 1\n"
    "s_xor_b32 s51, s51, %9\n"
    "s_and_b32 s51, s51, 1\n"
    "s_lshl_b32 s51, s51, 13\n"
    "s_add_u32 s68, s51, 0x200000\n"
    "s_and_b32 s60, s69, 1\n"
    "s_lshl_b32 s60, s60, 13\n"
    "v_lshl_add_u32 v8, v120, 2, v115\n"
    "v_lshlrev_b32 v8, 12, v8\n"
    "v_lshl_add_u32 v8, v119, 4, v8\n"
    "v_lshl_add_u32 v7, v116, 15, v8\n"
    "v_lshl_add_u32 v8, v116, 3, v8\n"
    "s_mul_i32 s73, %5, 589824\n"
    "s_lshl_b32 s51, %4, 7\n"
    "s_add_u32 s73, s73, s51\n"
    "s_add_u32 s73, s73, s60\n"
    "s_lshl_b32 s51, %2, 16\n"
    "s_add_u32 s51, s51, s73\n"
    "v_add_u32 v7, s51, v7\n"
    "s_and_b32 s51, %2, 1\n"
    "s_lshl_b32 s51, s51, 15\n"
    "s_add_u32 s51, s51, 524288\n"
    "s_add_u32 s51, s51, s73\n"
    "v_add_u32 v8, s51, v8\n"
    "s_mov_b32 s74, 262144\n"
    "s_lshl_b32 s61, %4, 1\n"
    "s_add_u32 s61, s61, %5\n"
    "s_lshr_b32 s73, s61, 2\n"
    "s_add_u32 s73, s73, 128\n"
    "s_and_b32 s51, s61, 3\n"
    "s_cmp_eq_u32 s51, 0\n"
    "s_cselect_b32 s71, s73, -1\n"
    "s_lshl_b32 s73, s61, 1\n"
    "s_add_u32 s73, s73, %2\n"
    "s_cmp_lt_u32 %2, 2\n"
    "s_cselect_b32 s71, s73, s71\n"
    "s_max_i32 s61, s71, 0\n"
    "s_lshr_b32 s73, s61, 1\n"
    "s_lshl_b32 s73, s73, 14\n"
    "s_and_b32 s51, s61, 1\n"
    "s_lshl_b32 s51, s51, 12\n"
    "s_add_u32 s65, s73, s51\n"
    "s_add_u32 s65, s65, s60\n"
    "s_cmp_ge_u32 s61, 48\n"
    "s_cselect_b32 s94, 1, 0\n"
    "s_cmp_ge_u32 s61, 96\n"
    "s_cselect_b32 s94, 2, s94\n"
    "s_mul_i32 s73, s94, 48\n"
    "s_sub_u32 s73, s61, s73\n"
    "s_lshl_b32 s73, s73, 6\n"
    "v_lshlrev_b32 v9, 4, v114\n"
    "v_add_u32 v9, s73, v9\n"
    "v_bfrev_b32 v118, 1\n"
    "v_cmp_eq_u32 vcc, 0, v113\n"
    "s_nop 1\n"
    "v_cndmask_b32 v9, v118, v9, vcc\n"
    "s_mul_i32 s73, %2, 3072\n"
    "v_add_u32 v10, s73, v0\n"
    "s_cmp_eq_u32 %2, 3\n"
    "s_cbranch_scc0 .Lnw3_%=\n"
    "s_lshl_b32 s73, %5, 4\n"
    "v_add_u32 v10, s73, v113\n"
    "v_lshlrev_b32 v118, 2, v114\n"
    "v_sub_u32 v10, v10, v118\n"
    "v_cmp_le_i32 s[76:77], 0, v10\n"
    "v_cmp_le_i32 s[78:79], 1, v10\n"
    "v_cmp_le_i32 s[80:81], 2, v10\n"
    "v_cmp_le_i32 s[82:83], 3, v10\n"
    "v_cmp_le_i32 s[84:85], 16, v10\n"
    "v_cmp_le_i32 s[86:87], 17, v10\n"
    "v_cmp_le_i32 s[88:89], 18, v10\n"
    "v_cmp_le_i32 s[90:91], 19, v10\n"
    ".Lnw3_%=:\n"
    "s_lshl_b32 s73, %9, 5\n"
    "s_add_u32 s73, s73, %4\n"
    "s_lshl_b32 s73, s73, 8\n"
    "s_add_u32 s92, s73, 74752\n"
    "s_lshl_b32 s73, %9, 8\n"
    "s_lshr_b32 s51, %4, 3\n"
    "s_add_u32 s73, s73, s51\n"
    "s_lshl_b32 s73, s73, 6\n"
    "s_add_u32 s93, s73, 41984\n"
    "s_lshl_b32 s73, %5, 12\n"
    "s_lshl_b32 s51, %4, 14\n"
    "s_add_u32 s73, s73, s51\n"
    "s_add_u32 s75, s73, 0xa800000\n"
    "s_mov_b32 s48, 0\n"
    "s_mov_b32 s54, 600\n"
    "s_mov_b32 s55, 2200\n"
    "s_mov_b32 s56, 0\n"
    "v_mov_b32 v134, s92\n"
    ".Lpoll2_%=:\n"
    "global_load_dword v135, v134, %1 sc1\n"
    "s_waitcnt vmcnt(0)\n"
    "v_readfirstlane_b32 s73, v135\n"
    "s_cmp_ge_u32 s73, 1\n"
    "s_cbranch_scc1 .Lpoll2_x%=\n"
    "s_add_u32 s56, s56, 1\n"
    "s_cmp_lt_u32 s56, 3\n"
    "s_cbranch_scc1 .Lpoll2_%=\n"
    "s_sleep 8\n"
    "s_cmp_lt_u32 s56, s57\n"
    "s_cbranch_scc1 .Lpoll2_%=\n"
    "s_mov_b32 s57, 0\n"
    "v_mov_b32 v135, 1\n"
    "global_store_dword v11, v135, %1 sc1\n"
    ".Lpoll2_x%=:\n"
    "s_add_u32 s73, s48, 0\n"
    "s_and_b32 s73, s73, 63\n"
    "s_lshr_b32 s51, s73, 2\n"
    "s_xor_b32 s51, s51, s69\n"
    "s_and_b32 s51, s51, 1\n"
    "s_lshl_b32 s51, s51, 13\n"
    "s_lshl_b32 s73, s73, 5\n"
    "s_add_u32 s73, s73, %4\n"
    "s_lshl_b32 s73, s73, 14\n"
    "s_add_u32 s73, s73, s51\n"
    "s_add_u32 s60, s73, 0x6800000\n"
    "s_add_u32 s61, s73, 0x6801000\n"
    "s_add_u32 s62, s73, 0x8800000\n"
    "s_add_u32 s63, s73, 0x8801000\n"
    "s_lshl_b32 s51, %5, 12\n"
    "s_add_u32 s73, s73, s51\n"
    "s_add_u32 s64, s73, 0x4800000\n"
    "buffer_load_dwordx4 v[12:15], v5, s[40:43], s60 offen sc1\n"
    "buffer_load_dwordx4 v[16:19], v5, s[40:43], s61 offen sc1\n"
    "buffer_load_dwordx4 v[20:23], v5, s[40:43], s62 offen sc1\n"
    "buffer_load_dwordx4 v[24:27], v5, s[40:43], s63 offen sc1\n"
    "buffer_load_dwordx4 v[28:31], v5, s[40:43], s64 offen sc1\n"
    "s_waitcnt vmcnt(4)\n"
    "ds_write_b128 v5, v[12:15]\n"
    "s_waitcnt vmcnt(3)\n"
    "ds_write_b128 v5, v[16:19] offset:4096\n"
    "s_waitcnt vmcnt(2)\n"
    "ds_write_b128 v5, v[20:23] offset:8192\n"
    "s_waitcnt vmcnt(1)\n"
    "ds_write_b128 v5, v[24:27] offset:12288\n"
    "s_waitcnt vmcnt(0)\n"
    "ds_write_b128 v5, v[28:31] offset:16384\n"
    "s_add_u32 s73, s48, 1\n"
    "s_and_b32 s73, s73, 63\n"
    "s_lshl_b32 s73, s73, 2\n"
    "s_add_u32 s73, s73, s92\n"
    "v_mov_b32 v134, s73\n"
    "global_load_dword v133, v134, %1 sc1\n"
    "s_waitcnt vmcnt(0)\n"
    "v_readfirstlane_b32 s59, v133\n"
    "s_mov_b32 s56, 0\n"
    "v_mov_b32 v134, 128\n"
    ".Lpoll3_%=:\n"
    "global_load_dword v135, v134, %1 sc1\n"
    "s_waitcnt vmcnt(0)\n"
    "v_readfirstlane_b32 s73, v135\n"
    "s_cmp_ge_u32 s73, 128\n"
    "s_cbranch_scc1 .Lpoll3_x%=\n"
    "s_add_u32 s56, s56, 1\n"
    "s_cmp_lt_u32 s56, 3\n"
    "s_cbranch_scc1 .Lpoll3_%=\n"
    "s_sleep 8\n"
    "s_cmp_lt_u32 s56, s57\n"
    "s_cbranch_scc1 .Lpoll3_%=\n"
    "s_mov_b32 s57, 0\n"
    "v_mov_b32 v135, 1\n"
    "global_store_dword v11, v135, %1 sc1\n"
    ".Lpoll3_x%=:\n"
    "s_add_u32 s73, s48, 1\n"
    "s_and_b32 s73, s73, 63\n"
    "s_lshr_b32 s51, s73, 2\n"
    "s_xor_b32 s51, s51, s69\n"
    "s_and_b32 s51, s51, 1\n"
    "s_lshl_b32 s51, s51, 13\n"
    "s_lshl_b32 s73, s73, 5\n"
    "s_add_u32 s73, s73, %4\n"
    "s_lshl_b32 s73, s73, 14\n"
    "s_add_u32 s73, s73, s51\n"
    "s_add_u32 s60, s73, 0x6800000\n"
    "s_add_u32 s61, s73, 0x6801000\n"
    "s_add_u32 s62, s73, 0x8800000\n"
    "s_add_u32 s63, s73, 0x8801000\n"
    "s_lshl_b32 s51, %5, 12\n"
    "s_add_u32 s73, s73, s51\n"
    "s_add_u32 s64, s73, 0x4800000\n"
    "ds_read_b128 v[32:35], v2\n"
    "ds_read_b128 v[36:39], v2 offset:1024\n"
    "ds_read_b128 v[40:43], v2 offset:2048\n"
    "ds_read_b128 v[44:47], v2 offset:3072\n"
    "ds_read_b128 v[48:51], v2 offset:4096\n"
    "ds_read_b128 v[52:55], v2 offset:5120\n"
    "ds_read_b128 v[56:59], v2 offset:6144\n"
    "ds_read_b128 v[60:63], v2 offset:7168\n"
    "ds_read_b128 v[64:67], v2 offset:8192\n"
    "ds_read_b128 v[68:71], v2 offset:9216\n"
    "ds_read_b128 v[72:75], v2 offset:10240\n"
    "ds_read_b128 v[76:79], v2 offset:11264\n"
    "ds_read_b128 v[80:83], v2 offset:12288\n"
    "ds_read_b128 v[84:87], v2 offset:13312\n"
    "v_mov_b64 v[124:125], 0\n"
    "v_mov_b64 v[126:127], 0\n"
    "v_mov_b64 v[128:129], 0\n"
    "v_mov_b64 v[130:131], 0\n"
    "s_waitcnt lgkmcnt(0)\n"
    "s_barrier\n"
    ".Lstep_%=:\n"
    "ds_read_b128 v[88:91], v0 offset:20480\n"
    "ds_read_b128 v[92:95], v0 offset:21504\n"
    "ds_read_b128 v[96:99], v0 offset:22528\n"
    "ds_read_b128 v[100:103], v0 offset:23552\n"
    "ds_read_b128 v[104:107], v0 offset:24576\n"
    "ds_read_b128 v[108:111], v0 offset:25600\n"
    "ds_read_b128 v[112:115], v0 offset:26624\n"
    "ds_read_b128 v[116:119], v0 offset:27648\n"
    "ds_read_b128 v[120:123], v0 offset:28672\n"
    "buffer_load_dwordx4 v[12:15], v5, s[40:43], s60 offen sc1\n"
    "buffer_load_dwordx4 v[16:19], v5, s[40:43], s61 offen sc1\n"
    "buffer_load_dwordx4 v[20:23], v5, s[40:43], s62 offen sc1\n"
    "buffer_load_dwordx4 v[24:27], v5, s[40:43], s63 offen sc1\n"
    "buffer_load_dwordx4 v[28:31], v5, s[40:43], s64 offen sc1\n"
    "s_waitcnt lgkmcnt(8)\n"
    "v_smfmac_f32_16x16x64_bf16 v[124:127], v[88:91], v[32:39], v1\n"
    "v_smfmac_f32_16x16x64_bf16 v[128:131], v[88:91], v[40:47], v1\n"
    "s_waitcnt lgkmcnt(7)\n"
    "v_smfmac_f32_16x16x64_bf16 v[124:127], v[92:95], v[48:55], v1\n"
    "v_mov_b64 v[32:33], 0\n"
    "v_mov_b64 v[34:35], 0\n"
    "v_smfmac_f32_16x16x64_bf16 v[128:131], v[92:95], v[56:63], v1\n"
    "v_mov_b64 v[36:37], 0\n"
    "v_mov_b64 v[38:39], 0\n"
    "s_waitcnt lgkmcnt(6)\n"
    "v_smfmac_f32_16x16x64_bf16 v[124:127], v[96:99], v[64:71], v1\n"
    "v_smfmac_f32_16x16x64_bf16 v[128:131], v[96:99], v[72:79], v1\n"
    "s_waitcnt lgkmcnt(5)\n"
    "v_smfmac_f32_16x16x64_bf16 v[124:127], v[100:103], v[80:87], v1\n"
    "v_smfmac_f32_16x16x64_bf16 v[128:131], v[100:103], v[136:143], v1\n"
    "s_waitcnt lgkmcnt(4)\n"
    "v_smfmac_f32_16x16x64_bf16 v[124:127], v[104:107], v[144:151], v1\n"
    "ds_read_b128 v[40:43], v0\n"
    "v_smfmac_f32_16x16x64_bf16 v[128:131], v[104:107], v[152:159], v1\n"
    "ds_read_b128 v[44:47], v0 offset:4096\n"
    "s_waitcnt lgkmcnt(5)\n"
    "v_smfmac_f32_16x16x64_bf16 v[124:127], v[108:111], v[160:167], v1\n"
    "ds_read_b128 v[48:51], v0 offset:1024\n"
    "v_smfmac_f32_16x16x64_bf16 v[128:131], v[108:111], v[168:175], v1\n"
    "ds_read_b128 v[52:55], v0 offset:5120\n"
    "s_waitcnt lgkmcnt(6)\n"
    "v_smfmac_f32_16x16x64_bf16 v[124:127], v[112:115], v[176:183], v1\n"
    "ds_read_b128 v[56:59], v0 offset:2048\n"
    "v_smfmac_f32_16x16x64_bf16 v[128:131], v[112:115], v[184:191], v1\n"
    "ds_read_b128 v[60:63], v0 offset:6144\n"
    "s_waitcnt lgkmcnt(7)\n"
    "v_smfmac_f32_16x16x64_bf16 v[124:127], v[116:119], v[192:199], v1\n"
    "ds_read_b128 v[64:67], v0 offset:3072\n"
    "v_smfmac_f32_16x16x64_bf16 v[128:131], v[116:119], v[200:207], v1\n"
    "ds_read_b128 v[68:71], v0 offset:7168\n"
    "s_waitcnt lgkmcnt(8)\n"
    "v_smfmac_f32_16x16x64_bf16 v[124:127], v[120:123], v[208:215], v1\n"
    "v_smfmac_f32_16x16x64_bf16 v[128:131], v[120:123], v[216:223], v1\n"
    "v_smfmac_f32_16x16x64_bf16 v[32:35], v[88:91], v[224:231], v1\n"
    "v_smfmac_f32_16x16x64_bf16 v[36:39], v[88:91], v[232:239], v1\n"
    "v_smfmac_f32_16x16x64_bf16 v[32:35], v[92:95], v[240:247], v1\n"
    "v_permlane32_swap_b32 v124, v126\n"
    "v_permlane32_swap_b32 v128, v130\n"
    "v_smfmac_f32_16x16x64_bf16 v[36:39], v[92:95], v[248:255], v1\n"
    "v_permlane32_swap_b32 v125, v127\n"
    "v_permlane32_swap_b32 v129, v131\n"
    "v_smfmac_f32_16x16x64_bf16 v[32:35], v[96:99], a[0:7], v1\n"
    "v_add_f32 v124, v124, v126\n"
    "v_add_f32 v128, v128, v130\n"
    "v_smfmac_f32_16x16x64_bf16 v[36:39], v[96:99], a[8:15], v1\n"
    "v_add_f32 v125, v125, v127\n"
    "v_add_f32 v129, v129, v131\n"
    "v_smfmac_f32_16x16x64_bf16 v[32:35], v[100:103], a[16:23], v1\n"
    "v_cvt_pk_bf16_f32 v76, v124, v128\n"
    "v_cvt_pk_bf16_f32 v77, v125, v129\n"
    "ds_write_b32 v3, v76 offset:9216\n"
    "ds_write_b32 v3, v77 offset:9488\n"
    "v_smfmac_f32_16x16x64_bf16 v[36:39], v[100:103], a[24:31], v1\n"
    "v_smfmac_f32_16x16x64_bf16 v[32:35], v[104:107], a[32:39], v1\n"
    "v_smfmac_f32_16x16x64_bf16 v[36:39], v[104:107], a[40:47], v1\n"
    "v_smfmac_f32_16x16x64_bf16 v[32:35], v[108:111], a[48:55], v1\n"
    "s_waitcnt lgkmcnt(0)\n"
    "s_barrier\n"
    "ds_read_b128 v[124:127], v4 offset:29696\n"
    "v_smfmac_f32_16x16x64_bf16 v[36:39], v[108:111], a[56:63], v1\n"
    "ds_read_b128 v[128:131], v4 offset:29760\n"
    "v_smfmac_f32_16x16x64_bf16 v[32:35], v[112:115], a[64:71], v1\n"
    "ds_read_b128 v[132:135], v4 offset:29824\n"
    "v_smfmac_f32_16x16x64_bf16 v[36:39], v[112:115], a[72:79], v1\n"
    "ds_read_b128 v[72:75], v4 offset:29888\n"
    "v_smfmac_f32_16x16x64_bf16 v[32:35], v[116:119], a[80:87], v1\n"
    "v_smfmac_f32_16x16x64_bf16 v[36:39], v[116:119], a[88:95], v1\n"
    "v_smfmac_f32_16x16x64_bf16 v[32:35], v[120:123], a[96:103], v1\n"
    "v_smfmac_f32_16x16x64_bf16 v[36:39], v[120:123], a[104:111], v1\n"
    "s_waitcnt lgkmcnt(3)\n"
    "v_mfma_f32_16x16x32_bf16 v[80:83], v[40:43], v[124:127], 0\n"
    "v_mfma_f32_16x16x32_bf16 v[84:87], v[44:47], v[124:127], 0\n"
    "ds_read_b128 v[40:43], v11 offset:31872\n"
    "ds_read_b128 v[44:47], v11 offset:31872\n"
    "s_waitcnt lgkmcnt(4)\n"
    "v_mfma_f32_16x16x32_bf16 v[80:83], v[48:51], v[128:131], v[80:83]\n"
    "v_permlane32_swap_b32 v32, v34\n"
    "v_permlane32_swap_b32 v36, v38\n"
    "v_mfma_f32_16x16x32_bf16 v[84:87], v[52:55], v[128:131], v[84:87]\n"
    "v_permlane32_swap_b32 v33, v35\n"
    "v_permlane32_swap_b32 v37, v39\n"
    "s_waitcnt lgkmcnt(3)\n"
    "v_mfma_f32_16x16x32_bf16 v[80:83], v[56:59], v[132:135], v[80:83]\n"
    "v_add_f32 v32, v32, v34\n"
    "v_add_f32 v36, v36, v38\n"
    "v_mfma_f32_16x16x32_bf16 v[84:87], v[60:63], v[132:135], v[84:87]\n"
    "v_add_f32 v33, v33, v35\n"
    "v_add_f32 v37, v37, v39\n"
    "s_waitcnt lgkmcnt(2)\n"
    "v_mfma_f32_16x16x32_bf16 v[80:83], v[64:67], v[72:75], v[80:83]\n"
    "v_cvt_pk_bf16_f32 v76, v32, v36\n"
    "v_cvt_pk_bf16_f32 v77, v33, v37\n"
    "ds_write2_b32 v3, v76, v77 offset1:68\n"
    "v_mfma_f32_16x16x32_bf16 v[84:87], v[68:71], v[72:75], v[84:87]\n"
    "s_waitcnt lgkmcnt(2)\n"
    "v_smfmac_f32_16x16x64_bf16 v[40:43], v[88:91], a[112:119], v1\n"
    "s_waitcnt lgkmcnt(1)\n"
    "v_smfmac_f32_16x16x64_bf16 v[44:47], v[88:91], a[120:127], v1\n"
    "s_waitcnt lgkmcnt(0)\n"
    "s_barrier\n"
    "ds_read_b128 v[48:51], v4 offset:20480\n"
    "ds_read_b128 v[52:55], v4 offset:20544\n"
    "ds_read_b128 v[56:59], v4 offset:20608\n"
    "ds_read_b128 v[60:63], v4 offset:20672\n"
    "v_smfmac_f32_16x16x64_bf16 v[40:43], v[92:95], a[128:135], v1\n"
    "v_maximum3_f32 v78, v80, v81, v82\n"
    "v_maximum3_f32 v78, v78, v83, v84\n"
    "v_smfmac_f32_16x16x64_bf16 v[44:47], v[92:95], a[136:143], v1\n"
    "v_maximum3_f32 v78, v78, v85, v86\n"
    "v_maximum3_f32 v78, v78, v87, v87\n"
    "ds_read_b128 v[32:35], v5 offset:8192\n"
    "ds_read_b128 v[36:39], v5 offset:12288\n"
    "v_smfmac_f32_16x16x64_bf16 v[40:43], v[96:99], a[144:151], v1\n"
    "v_mov_b32 v79, v78\n"
    "ds_read_b128 v[68:71], v11 offset:31888\n"
    "ds_read_b64 v[76:77], v11 offset:31904\n"
    "v_smfmac_f32_16x16x64_bf16 v[44:47], v[96:99], a[152:159], v1\n"
    "v_permlane32_swap_b32 v79, v78\n"
    "v_maximum3_f32 v78, v78, v79, v79\n"
    "s_waitcnt lgkmcnt(7)\n"
    "v_mfma_f32_16x16x32_bf16 v[64:67], v[48:51], v[124:127], 0\n"
    "ds_read_b128 v[88:91], v2 offset:14336\n"
    "ds_read_b128 v[92:95], v2 offset:15360\n"
    "s_waitcnt lgkmcnt(8)\n"
    "v_mfma_f32_16x16x32_bf16 v[64:67], v[52:55], v[128:131], v[64:67]\n"
    "s_waitcnt lgkmcnt(7)\n"
    "v_mfma_f32_16x16x32_bf16 v[64:67], v[56:59], v[132:135], v[64:67]\n"
    "s_waitcnt lgkmcnt(6)\n"
    "v_mfma_f32_16x16x32_bf16 v[64:67], v[60:63], v[72:75], v[64:67]\n"
    "v_smfmac_f32_16x16x64_bf16 v[40:43], v[100:103], a[160:167], v1\n"
    "ds_read_b128 v[48:51], v2 offset:18432\n"
    "ds_read_b128 v[52:55], v2 offset:19456\n"
    "v_smfmac_f32_16x16x64_bf16 v[44:47], v[100:103], a[168:175], v1\n"
    "ds_read_b128 v[56:59], v2 offset:20480\n"
    "ds_read_b128 v[60:63], v2 offset:21504\n"
    "v_maximum3_f32 v79, v64, v65, v66\n"
    "v_maximum3_f32 v78, v79, v67, v78\n"
    "v_mov_b32 v79, v78\n"
    "v_smfmac_f32_16x16x64_bf16 v[40:43], v[104:107], a[176:183], v1\n"
    "ds_read_b128 v[96:99], v2 offset:16384\n"
    "ds_read_b128 v[100:103], v2 offset:17408\n"
    "v_permlane16_swap_b32 v79, v78\n"
    "v_maximum3_f32 v78, v78, v79, v79\n"
    "v_mul_f32 v78, 0xbe0293ee, v78\n"
    "v_smfmac_f32_16x16x64_bf16 v[44:47], v[104:107], a[184:191], v1\n"
    "v_fmamk_f32 v80, v80, 0x3e0293ee, v78\n"
    "v_fmamk_f32 v81, v81, 0x3e0293ee, v78\n"
    "v_exp_f32 v80, v80\n"
    "v_exp_f32 v81, v81\n"
    "ds_read_b128 v[124:127], v2 offset:22528\n"
    "ds_read_b128 v[128:131], v2 offset:23552\n"
    "v_smfmac_f32_16x16x64_bf16 v[40:43], v[108:111], a[192:199], v1\n"
    "v_fmamk_f32 v82, v82, 0x3e0293ee, v78\n"
    "v_fmamk_f32 v83, v83, 0x3e0293ee, v78\n"
    "v_exp_f32 v82, v82\n"
    "v_exp_f32 v83, v83\n"
    "v_cvt_pk_bf16_f32 v80, v80, v81\n"
    "v_smfmac_f32_16x16x64_bf16 v[44:47], v[108:111], a[200:207], v1\n"
    "v_fmamk_f32 v84, v84, 0x3e0293ee, v78\n"
    "v_fmamk_f32 v85, v85, 0x3e0293ee, v78\n"
    "v_exp_f32 v84, v84\n"
    "v_exp_f32 v85, v85\n"
    "v_cvt_pk_bf16_f32 v81, v82, v83\n"
    "v_smfmac_f32_16x16x64_bf16 v[40:43], v[112:115], a[208:215], v1\n"
    "v_fmamk_f32 v86, v86, 0x3e0293ee, v78\n"
    "v_fmamk_f32 v87, v87, 0x3e0293ee, v78\n"
    "v_exp_f32 v86, v86\n"
    "v_exp_f32 v87, v87\n"
    "v_cvt_pk_bf16_f32 v82, v84, v85\n"
    "ds_read_b128 v[104:107], v2 offset:24576\n"
    "ds_read_b128 v[108:111], v2 offset:25600\n"
    "v_smfmac_f32_16x16x64_bf16 v[44:47], v[112:115], a[216:223], v1\n"
    "v_fmamk_f32 v64, v64, 0x3e0293ee, v78\n"
    "v_fmamk_f32 v65, v65, 0x3e0293ee, v78\n"
    "v_exp_f32 v64, v64\n"
    "v_exp_f32 v65, v65\n"
    "v_cvt_pk_bf16_f32 v83, v86, v87\n"
    "v_smfmac_f32_16x16x64_bf16 v[40:43], v[116:119], a[224:231], v1\n"
    "v_fmamk_f32 v66, v66, 0x3e0293ee, v78\n"
    "v_fmamk_f32 v67, v67, 0x3e0293ee, v78\n"
    "v_exp_f32 v66, v66\n"
    "v_exp_f32 v67, v67\n"
    "v_cvt_pk_bf16_f32 v64, v64, v65\n"
    "v_smfmac_f32_16x16x64_bf16 v[44:47], v[116:119], a[232:239], v1\n"
    "v_cvt_pk_bf16_f32 v65, v66, v67\n"
    "v_smfmac_f32_16x16x64_bf16 v[40:43], v[120:123], a[240:247], v1\n"
    "ds_read_b128 v[112:115], v2 offset:26624\n"
    "ds_read_b128 v[116:119], v2 offset:27648\n"
    "v_smfmac_f32_16x16x64_bf16 v[44:47], v[120:123], a[248:255], v1\n"
    "s_waitcnt lgkmcnt(14)\n"
    "v_mfma_f32_16x16x32_bf16 v[72:75], v[68:71], v[80:83], 0\n"
    "v_mfma_f32_16x16x32_bf16 v[84:87], v[32:35], v[80:83], 0\n"
    "ds_read_b128 v[120:123], v11 offset:31872\n"
    "ds_read_b128 v[68:71], v11 offset:31872\n"
    "v_mfma_f32_16x16x32_bf16 v[132:135], v[36:39], v[80:83], 0\n"
    "ds_read_b128 v[32:35], v2 offset:28672\n"
    "ds_read_b128 v[36:39], v2 offset:29696\n"
    "v_cvt_pk_bf16_f32 v40, v40, v41\n"
    "v_cvt_pk_bf16_f32 v41, v42, v43\n"
    "v_mfma_f32_16x16x16_bf16 v[72:75], v[76:77], v[64:65], v[72:75]\n"
    "v_cvt_pk_bf16_f32 v42, v44, v45\n"
    "v_cvt_pk_bf16_f32 v43, v46, v47\n"
    "v_mfma_f32_16x16x16_bf16 v[84:87], v[40:41], v[64:65], v[84:87]\n"
    "ds_read_b128 v[80:83], v11 offset:31872\n"
    "ds_read_b128 v[44:47], v11 offset:31872\n"
    "v_mfma_f32_16x16x16_bf16 v[132:135], v[42:43], v[64:65], v[132:135]\n"
    "ds_write_b128 v5, v[40:43] offset:22784\n"
    "ds_read_b128 v[64:67], v11 offset:31872\n"
    "s_and_b32 s49, s48, 1\n"
    "s_lshl_b32 s70, s49, 3\n"
    "v_rcp_f32 v78, v72\n"
    "s_nop 0\n"
    "v_pk_mul_f32 v[84:85], v[84:85], v[78:79] op_sel_hi:[1,0]\n"
    "v_pk_mul_f32 v[86:87], v[86:87], v[78:79] op_sel_hi:[1,0]\n"
    "s_nop 0\n"
    "v_pk_mul_f32 v[132:133], v[132:133], v[78:79] op_sel_hi:[1,0]\n"
    "v_pk_mul_f32 v[134:135], v[134:135], v[78:79] op_sel_hi:[1,0]\n"
    "v_cvt_pk_bf16_f32 v84, v84, v132\n"
    "v_cvt_pk_bf16_f32 v85, v85, v133\n"
    "v_cvt_pk_bf16_f32 v86, v86, v134\n"
    "v_cvt_pk_bf16_f32 v87, v87, v135\n"
    "ds_write_b128 v6, v[84:87] offset:26880\n"
    "s_cmp_eq_u32 s48, 63\n"
    "s_waitcnt lgkmcnt(0)\n"
    "s_barrier\n"
    "s_cbranch_scc1 .Lend_%=\n"
    "ds_read_b128 v[132:135], v0 offset:26880\n"
    "ds_read_b128 v[72:75], v0 offset:27904\n"
    "s_cmp_lt_u32 %2, 2\n"
    "s_waitcnt lgkmcnt(1)\n"
    "v_smfmac_f32_16x16x64_bf16 v[68:71], v[132:135], v[88:95], v1\n"
    "v_smfmac_f32_16x16x64_bf16 v[80:83], v[132:135], v[96:103], v1\n"
    "ds_read_b128 v[88:91], v2 offset:30720\n"
    "ds_read_b128 v[92:95], v2 offset:31744\n"
    "s_waitcnt lgkmcnt(2)\n"
    "v_smfmac_f32_16x16x64_bf16 v[68:71], v[72:75], v[48:55], v1\n"
    "ds_read_b128 v[96:99], v2 offset:32768\n"
    "ds_read_b128 v[100:103], v2 offset:33792\n"
    "v_smfmac_f32_16x16x64_bf16 v[80:83], v[72:75], v[56:63], v1\n"
    "v_smfmac_f32_16x16x64_bf16 v[120:123], v[132:135], v[124:131], v1\n"
    "v_smfmac_f32_16x16x64_bf16 v[44:47], v[132:135], v[104:111], v1\n"
    "v_smfmac_f32_16x16x64_bf16 v[120:123], v[72:75], v[112:119], v1\n"
    "v_permlane32_swap_b32 v68, v80\n"
    "v_permlane32_swap_b32 v69, v81\n"
    "v_smfmac_f32_16x16x64_bf16 v[44:47], v[72:75], v[32:39], v1\n"
    "s_waitcnt lgkmcnt(0)\n"
    "s_cbranch_scc1 .Lx1_%=\n"
    "v_smfmac_f32_16x16x64_bf16 v[64:67], v[132:135], v[88:95], v1\n"
    "v_smfmac_f32_16x16x64_bf16 v[64:67], v[72:75], v[96:103], v1\n"
    ".Lx1_%=:\n"
    "v_permlane32_swap_b32 v70, v82\n"
    "v_permlane32_swap_b32 v71, v83\n"
    "v_pk_add_f32 v[68:69], v[68:69], v[80:81]\n"
    "v_pk_add_f32 v[70:71], v[70:71], v[82:83]\n"
    "v_bfi_b32 v68, 15, s49, v68\n"
    "v_bfi_b32 v70, 15, s49, v70\n"
    "buffer_store_dwordx4 v[68:71], v7, s[40:43], 0 offen sc1\n"
    "v_permlane32_swap_b32 v120, v44\n"
    "v_permlane32_swap_b32 v121, v45\n"
    "v_permlane32_swap_b32 v122, v46\n"
    "v_permlane32_swap_b32 v123, v47\n"
    "v_pk_add_f32 v[120:121], v[120:121], v[44:45]\n"
    "v_pk_add_f32 v[122:123], v[122:123], v[46:47]\n"
    "v_bfi_b32 v120, 15, s49, v120\n"
    "v_bfi_b32 v122, 15, s49, v122\n"
    "buffer_store_dwordx4 v[120:123], v7, s[40:43], s74 offen sc1\n"
    "s_cbranch_scc1 .Lx2_%=\n"
    "v_permlane32_swap_b32 v64, v66\n"
    "v_permlane32_swap_b32 v65, v67\n"
    "v_pk_add_f32 v[64:65], v[64:65], v[66:67]\n"
    "v_bfi_b32 v64, 15, s49, v64\n"
    "buffer_store_dwordx2 v[64:65], v8, s[40:43], 0 offen sc1\n"
    ".Lx2_%=:\n"
    "s_memtime s[50:51]\n"
    "s_waitcnt lgkmcnt(0)\n"
    "s_mov_b32 s52, s50\n"
    "s_and_b32 s73, s48, 3\n"
    "s_cmp_eq_u32 s73, 3\n"
    "s_cselect_b32 s58, 96, 0\n"
    "s_cmp_eq_u32 %2, 3\n"
    "s_cbranch_scc1 .Lw3_%=\n"
    "ds_read_b128 v[32:35], v2\n"
    "ds_read_b128 v[36:39], v2 offset:1024\n"
    "ds_read_b128 v[40:43], v2 offset:2048\n"
    "ds_read_b128 v[44:47], v2 offset:3072\n"
    "ds_read_b128 v[48:51], v2 offset:4096\n"
    "ds_read_b128 v[52:55], v2 offset:5120\n"
    "ds_read_b128 v[56:59], v2 offset:6144\n"
    "ds_read_b128 v[60:63], v2 offset:7168\n"
    "ds_read_b128 v[64:67], v2 offset:8192\n"
    "ds_read_b128 v[68:71], v2 offset:9216\n"
    "ds_read_b128 v[72:75], v2 offset:10240\n"
    "ds_read_b128 v[76:79], v2 offset:11264\n"
    "ds_read_b128 v[80:83], v2 offset:12288\n"
    "ds_read_b128 v[84:87], v2 offset:13312\n"
    "v_mov_b64 v[124:125], 0\n"
    "v_mov_b64 v[126:127], 0\n"
    "v_mov_b64 v[128:129], 0\n"
    "v_mov_b64 v[130:131], 0\n"
    "s_mul_i32 s73, s48, 3\n"
    "s_add_u32 s73, s73, s94\n"
    "s_and_b32 s51, s73, 1\n"
    "s_lshl_b32 s51, s51, 12\n"
    "s_lshr_b32 s73, s73, 1\n"
    "s_lshl_b32 s73, s73, 14\n"
    "s_add_u32 s73, s73, s51\n"
    "s_add_u32 s66, s73, s68\n"
    "s_mul_i32 s73, s48, 3\n"
    "s_add_u32 s73, s73, %2\n"
    "s_and_b32 s51, s73, 1\n"
    "s_lshl_b32 s51, s51, 12\n"
    "s_lshr_b32 s73, s73, 1\n"
    "s_lshl_b32 s73, s73, 14\n"
    "s_add_u32 s73, s73, s51\n"
    "s_add_u32 s67, s73, s68\n"
    "s_waitcnt lgkmcnt(0)\n"
    "s_cmp_lt_i32 s71, 0\n"
    "s_cbranch_scc1 .Lnored_%=\n"
    "s_mov_b32 s56, 0\n"
    "s_sub_u32 s53, s54, s58\n"
    "s_waitcnt lgkmcnt(0)\n"
    ".Ltl4_%=:\n"
    "s_memtime s[50:51]\n"
    "s_waitcnt lgkmcnt(0)\n"
    "s_sub_u32 s50, s50, s52\n"
    "s_cmp_lt_u32 s50, s53\n"
    "s_cbranch_scc1 .Ltl4_%=\n"
    ".Lred_%=:\n"
    "buffer_load_dwordx4 v[88:91], v0, s[40:43], s65 offen sc1\n"
    "buffer_load_dwordx4 v[92:95], v0, s[40:43], s65 offen offset:1024 sc1\n"
    "buffer_load_dwordx4 v[96:99], v0, s[40:43], s65 offen offset:2048 sc1\n"
    "buffer_load_dwordx4 v[100:103], v0, s[40:43], s65 offen offset:3072 sc1\n"
    "s_waitcnt vmcnt(2)\n"
    "v_add3_u32 v104, v88, v90, v92\n"
    "v_pk_add_f32 v[108:109], v[88:89], v[92:93]\n"
    "v_pk_add_f32 v[110:111], v[90:91], v[94:95]\n"
    "s_waitcnt vmcnt(1)\n"
    "v_add3_u32 v104, v104, v94, v96\n"
    "v_pk_add_f32 v[108:109], v[108:109], v[96:97]\n"
    "v_pk_add_f32 v[110:111], v[110:111], v[98:99]\n"
    "s_waitcnt vmcnt(0)\n"
    "v_add3_u32 v104, v104, v98, v100\n"
    "v_pk_add_f32 v[108:109], v[108:109], v[100:101]\n"
    "v_pk_add_f32 v[110:111], v[110:111], v[102:103]\n"
    "v_add_u32 v104, v104, v102\n"
    "v_and_b32 v104, 15, v104\n"
    "v_permlane32_swap_b32 v108, v110\n"
    "v_permlane32_swap_b32 v109, v111\n"
    "v_cmp_ne_u32 vcc, s70, v104\n"
    "v_pk_add_f32 v[108:109], v[108:109], v[110:111]\n"
    "s_cbranch_vccz .Lredok_%=\n"
    "s_add_u32 s56, s56, 1\n"
    "s_cmp_lt_u32 s56, 3\n"
    "s_cbranch_scc1 .Lred_%=\n"
    "s_sleep 8\n"
    "s_cmp_lt_u32 s56, s57\n"
    "s_cbranch_scc1 .Lred_%=\n"
    "s_mov_b32 s57, 0\n"
    "v_mov_b32 v135, 1\n"
    "global_store_dword v11, v135, %1 sc1\n"
    ".Lredok_%=:\n"
    "s_nop 0\n"
    "v_permlane16_swap_b32 v108, v109\n"
    "v_add_f32 v108, v108, v109\n"
    "s_nop 1\n"
    "v_add_f32_dpp v108, v108, v108 row_ror:8 row_mask:0xf bank_mask:0xf\n"
    "s_nop 1\n"
    "v_mov_b32_dpp v109, v108 quad_perm:[1,0,3,2] row_mask:0xf bank_mask:0xf\n"
    "v_cvt_pk_bf16_f32 v108, v108, v109\n"
    "v_min_u32 v108, -2, v108\n"
    "s_nop 1\n"
    "v_mov_b32_dpp v109, v108 row_shl:2 row_mask:0xf bank_mask:0xf\n"
    "v_mov_b32_dpp v110, v108 row_shl:4 row_mask:0xf bank_mask:0xf\n"
    "v_mov_b32_dpp v111, v108 row_shl:6 row_mask:0xf bank_mask:0xf\n"
    "buffer_store_dwordx4 v[108:111], v9, s[40:43], s66 offen sc1\n"
    "s_cmp_eq_u32 s56, 0\n"
    "s_cbranch_scc0 .Lad5_a%=\n"
    "s_cmp_gt_u32 s54, 300\n"
    "s_cselect_b32 s73, s58, 0\n"
    "s_sub_u32 s54, s54, s73\n"
    "s_branch .Lad5_x%=\n"
    ".Lad5_a%=:\n"
    "s_add_u32 s73, s56, s58\n"
    "s_cmp_le_u32 s73, 2\n"
    "s_cselect_b32 s73, 96, 0\n"
    "s_add_u32 s54, s54, s73\n"
    "s_min_u32 s54, s54, 6000\n"
    ".Lad5_x%=:\n"
    ".Lnored_%=:\n"
    "s_waitcnt vmcnt(2)\n"
    "s_cmp_lg_u32 s59, 0\n"
    "s_cbranch_scc1 .Lkn6_%=\n"
    "s_waitcnt lgkmcnt(0)\n"
    "s_add_u32 s73, s48, 1\n"
    "s_and_b32 s73, s73, 63\n"
    "s_lshl_b32 s73, s73, 2\n"
    "s_add_u32 s73, s73, s92\n"
    "s_mov_b32 s53, s73\n"
    "s_mov_b32 s56, 0\n"
    "v_mov_b32 v134, s53\n"
    ".Lpoll7_%=:\n"
    "global_load_dword v135, v134, %1 sc1\n"
    "s_waitcnt vmcnt(0)\n"
    "v_readfirstlane_b32 s73, v135\n"
    "s_cmp_ge_u32 s73, 1\n"
    "s_cbranch_scc1 .Lpoll7_x%=\n"
    "s_add_u32 s56, s56, 1\n"
    "s_cmp_lt_u32 s56, 3\n"
    "s_cbranch_scc1 .Lpoll7_%=\n"
    "s_sleep 8\n"
    "s_cmp_lt_u32 s56, s57\n"
    "s_cbranch_scc1 .Lpoll7_%=\n"
    "s_mov_b32 s57, 0\n"
    "v_mov_b32 v135, 1\n"
    "global_store_dword v11, v135, %1 sc1\n"
    ".Lpoll7_x%=:\n"
    "buffer_load_dwordx4 v[12:15], v5, s[40:43], s60 offen sc1\n"
    "buffer_load_dwordx4 v[16:19], v5, s[40:43], s61 offen sc1\n"
    "buffer_load_dwordx4 v[20:23], v5, s[40:43], s62 offen sc1\n"
    "buffer_load_dwordx4 v[24:27], v5, s[40:43], s63 offen sc1\n"
    "buffer_load_dwordx4 v[28:31], v5, s[40:43], s64 offen sc1\n"
    "s_waitcnt vmcnt(0)\n"
    ".Lkn6_%=:\n"
    "s_add_u32 s73, s48, 2\n"
    "s_and_b32 s73, s73, 63\n"
    "s_lshl_b32 s73, s73, 2\n"
    "s_add_u32 s73, s73, s92\n"
    "v_mov_b32 v134, s73\n"
    "global_load_dword v133, v134, %1 sc1\n"
    "s_add_u32 s73, s48, 2\n"
    "s_and_b32 s73, s73, 63\n"
    "s_lshr_b32 s51, s73, 2\n"
    "s_xor_b32 s51, s51, s69\n"
    "s_and_b32 s51, s51, 1\n"
    "s_lshl_b32 s51, s51, 13\n"
    "s_lshl_b32 s73, s73, 5\n"
    "s_add_u32 s73, s73, %4\n"
    "s_lshl_b32 s73, s73, 14\n"
    "s_add_u32 s73, s73, s51\n"
    "s_add_u32 s60, s73, 0x6800000\n"
    "s_add_u32 s61, s73, 0x6801000\n"
    "s_add_u32 s62, s73, 0x8800000\n"
    "s_add_u32 s63, s73, 0x8801000\n"
    "s_lshl_b32 s51, %5, 12\n"
    "s_add_u32 s73, s73, s51\n"
    "s_add_u32 s64, s73, 0x4800000\n"
    "s_mov_b32 s56, 0\n"
    "s_sub_u32 s53, s55, s58\n"
    "s_waitcnt lgkmcnt(0)\n"
    ".Ltl8_%=:\n"
    "s_memtime s[50:51]\n"
    "s_waitcnt lgkmcnt(0)\n"
    "s_sub_u32 s50, s50, s52\n"
    "s_cmp_lt_u32 s50, s53\n"
    "s_cbranch_scc1 .Ltl8_%=\n"
    "buffer_load_dwordx4 v[88:91], v0, s[40:43], s67 offen sc1\n"
    "buffer_load_dwordx4 v[92:95], v0, s[40:43], s67 offen offset:1024 sc1\n"
    "buffer_load_dwordx4 v[96:99], v0, s[40:43], s67 offen offset:2048 sc1\n"
    "s_waitcnt lgkmcnt(0)\n"
    "s_barrier\n"
    "ds_write_b128 v5, v[12:15]\n"
    "ds_write_b128 v5, v[16:19] offset:4096\n"
    "ds_write_b128 v5, v[20:23] offset:8192\n"
    "ds_write_b128 v5, v[24:27] offset:12288\n"
    "ds_write_b128 v5, v[28:31] offset:16384\n"
    "s_waitcnt vmcnt(3)\n"
    "v_readfirstlane_b32 s59, v133\n"
    ".Lgchk_%=:\n"
    "s_waitcnt vmcnt(1)\n"
    "v_max3_u32 v100, v88, v90, v92\n"
    "ds_write_b128 v10, v[88:91] offset:20480\n"
    "ds_write_b128 v10, v[92:95] offset:21504\n"
    "s_waitcnt vmcnt(0)\n"
    "v_max3_u32 v100, v100, v94, v96\n"
    "ds_write_b128 v10, v[96:99] offset:22528\n"
    "v_max_u32 v100, v100, v98\n"
    "v_cmp_eq_u32 vcc, -1, v100\n"
    "s_cbranch_vccz .Lgok_%=\n"
    "buffer_load_dwordx4 v[88:91], v0, s[40:43], s67 offen sc1\n"
    "buffer_load_dwordx4 v[92:95], v0, s[40:43], s67 offen offset:1024 sc1\n"
    "buffer_load_dwordx4 v[96:99], v0, s[40:43], s67 offen offset:2048 sc1\n"
    "s_add_u32 s56, s56, 1\n"
    "s_cmp_lt_u32 s56, 3\n"
    "s_cbranch_scc1 .Lgchk_%=\n"
    "s_sleep 8\n"
    "s_cmp_lt_u32 s56, s57\n"
    "s_cbranch_scc1 .Lgchk_%=\n"
    "s_mov_b32 s57, 0\n"
    "v_mov_b32 v135, 1\n"
    "global_store_dword v11, v135, %1 sc1\n"
    "s_waitcnt vmcnt(0)\n"
    ".Lgok_%=:\n"
    "s_cmp_eq_u32 s56, 0\n"
    "s_cbranch_scc0 .Lad9_a%=\n"
    "s_cmp_gt_u32 s55, 900\n"
    "s_cselect_b32 s73, s58, 0\n"
    "s_sub_u32 s55, s55, s73\n"
    "s_branch .Lad9_x%=\n"
    ".Lad9_a%=:\n"
    "s_add_u32 s73, s56, s58\n"
    "s_cmp_le_u32 s73, 2\n"
    "s_cselect_b32 s73, 96, 0\n"
    "s_add_u32 s55, s55, s73\n"
    "s_min_u32 s55, s55, 6000\n"
    ".Lad9_x%=:\n"
    "s_add_u32 s48, s48, 1\n"
    "s_waitcnt lgkmcnt(0)\n"
    "s_barrier\n"
    "s_branch .Lstep_%=\n"
    ".Lw3_%=:\n"
    "ds_read_b128 v[32:35], v0 offset:16384\n"
    "ds_read_b128 v[48:51], v0\n"
    "ds_read_b128 v[64:67], v0 offset:4096\n"
    "ds_read_b128 v[80:83], v4 offset:20480\n"
    "ds_read_b128 v[36:39], v0 offset:17408\n"
    "ds_read_b128 v[52:55], v0 offset:1024\n"
    "ds_read_b128 v[68:71], v0 offset:5120\n"
    "ds_read_b128 v[84:87], v4 offset:20544\n"
    "ds_read_b128 v[40:43], v0 offset:18432\n"
    "ds_read_b128 v[56:59], v0 offset:2048\n"
    "ds_read_b128 v[72:75], v0 offset:6144\n"
    "ds_read_b128 v[88:91], v4 offset:20608\n"
    "ds_read_b128 v[44:47], v0 offset:19456\n"
    "ds_read_b128 v[60:63], v0 offset:3072\n"
    "ds_read_b128 v[76:79], v0 offset:7168\n"
    "ds_read_b128 v[92:95], v4 offset:20672\n"
    "s_waitcnt lgkmcnt(14)\n"
    "v_mfma_f32_16x16x32_bf16 v[96:99], v[48:51], v[32:35], 0\n"
    "s_waitcnt lgkmcnt(13)\n"
    "v_mfma_f32_16x16x32_bf16 v[100:103], v[64:67], v[32:35], 0\n"
    "s_waitcnt lgkmcnt(12)\n"
    "v_mfma_f32_16x16x32_bf16 v[104:107], v[80:83], v[32:35], 0\n"
    "s_waitcnt lgkmcnt(10)\n"
    "v_mfma_f32_16x16x32_bf16 v[96:99], v[52:55], v[36:39], v[96:99]\n"
    "s_waitcnt lgkmcnt(9)\n"
    "v_mfma_f32_16x16x32_bf16 v[100:103], v[68:71], v[36:39], v[100:103]\n"
    "s_waitcnt lgkmcnt(8)\n"
    "v_mfma_f32_16x16x32_bf16 v[104:107], v[84:87], v[36:39], v[104:107]\n"
    "s_waitcnt lgkmcnt(6)\n"
    "v_mfma_f32_16x16x32_bf16 v[96:99], v[56:59], v[40:43], v[96:99]\n"
    "s_waitcnt lgkmcnt(5)\n"
    "v_mfma_f32_16x16x32_bf16 v[100:103], v[72:75], v[40:43], v[100:103]\n"
    "s_waitcnt lgkmcnt(4)\n"
    "v_mfma_f32_16x16x32_bf16 v[104:107], v[88:91], v[40:43], v[104:107]\n"
    "s_waitcnt lgkmcnt(2)\n"
    "v_mfma_f32_16x16x32_bf16 v[96:99], v[60:63], v[44:47], v[96:99]\n"
    "s_waitcnt lgkmcnt(1)\n"
    "v_mfma_f32_16x16x32_bf16 v[100:103], v[76:79], v[44:47], v[100:103]\n"
    "s_waitcnt lgkmcnt(0)\n"
    "v_mfma_f32_16x16x32_bf16 v[104:107], v[92:95], v[44:47], v[104:107]\n"
    "ds_read_b128 v[32:35], v0 offset:8192\n"
    "ds_read_b128 v[36:39], v0 offset:12288\n"
    "ds_read_b128 v[40:43], v0 offset:9216\n"
    "ds_read_b128 v[44:47], v0 offset:13312\n"
    "ds_read_b128 v[48:51], v0 offset:10240\n"
    "ds_read_b128 v[52:55], v0 offset:14336\n"
    "ds_read_b128 v[56:59], v0 offset:11264\n"
    "ds_read_b128 v[60:63], v0 offset:15360\n"
    "ds_read_b128 v[64:67], v0 offset:22784\n"
    "ds_read_b128 v[68:71], v0 offset:23808\n"
    "ds_read_b128 v[72:75], v0 offset:24832\n"
    "ds_read_b128 v[76:79], v0 offset:25856\n"
    "ds_read_b128 v[80:83], v11 offset:31888\n"
    "ds_read_b64 v[84:85], v11 offset:31904\n"
    "v_mov_b32 v86, 0xd01502f9\n"
    "v_cndmask_b32 v96, v86, v96, s[76:77]\n"
    "v_cndmask_b32 v97, v86, v97, s[78:79]\n"
    "v_cndmask_b32 v98, v86, v98, s[80:81]\n"
    "v_cndmask_b32 v99, v86, v99, s[82:83]\n"
    "v_cndmask_b32 v100, v86, v100, s[84:85]\n"
    "v_cndmask_b32 v101, v86, v101, s[86:87]\n"
    "v_cndmask_b32 v102, v86, v102, s[88:89]\n"
    "v_cndmask_b32 v103, v86, v103, s[90:91]\n"
    "v_maximum3_f32 v87, v104, v105, v106\n"
    "v_maximum3_f32 v87, v87, v107, v96\n"
    "v_maximum3_f32 v87, v87, v97, v98\n"
    "v_maximum3_f32 v87, v87, v99, v100\n"
    "v_maximum3_f32 v87, v87, v101, v102\n"
    "v_maximum3_f32 v87, v87, v103, v103\n"
    "v_mov_b32 v86, v87\n"
    "s_nop 1\n"
    "v_permlane16_swap_b32 v86, v87\n"
    "v_maximum3_f32 v87, v87, v86, v86\n"
    "v_mov_b32 v86, v87\n"
    "s_nop 1\n"
    "v_permlane32_swap_b32 v86, v87\n"
    "v_maximum3_f32 v87, v87, v86, v86\n"
    "v_mul_f32 v87, 0xbe0293ee, v87\n"
    "v_fmamk_f32 v96, v96, 0x3e0293ee, v87\n"
    "v_fmamk_f32 v97, v97, 0x3e0293ee, v87\n"
    "v_fmamk_f32 v98, v98, 0x3e0293ee, v87\n"
    "v_fmamk_f32 v99, v99, 0x3e0293ee, v87\n"
    "v_fmamk_f32 v100, v100, 0x3e0293ee, v87\n"
    "v_fmamk_f32 v101, v101, 0x3e0293ee, v87\n"
    "v_fmamk_f32 v102, v102, 0x3e0293ee, v87\n"
    "v_fmamk_f32 v103, v103, 0x3e0293ee, v87\n"
    "v_fmamk_f32 v104, v104, 0x3e0293ee, v87\n"
    "v_fmamk_f32 v105, v105, 0x3e0293ee, v87\n"
    "v_fmamk_f32 v106, v106, 0x3e0293ee, v87\n"
    "v_fmamk_f32 v107, v107, 0x3e0293ee, v87\n"
    "v_exp_f32 v96, v96\n"
    "v_exp_f32 v97, v97\n"
    "v_exp_f32 v98, v98\n"
    "v_exp_f32 v99, v99\n"
    "v_exp_f32 v100, v100\n"
    "v_exp_f32 v101, v101\n"
    "v_exp_f32 v102, v102\n"
    "v_exp_f32 v103, v103\n"
    "v_exp_f32 v104, v104\n"
    "v_exp_f32 v105, v105\n"
    "v_exp_f32 v106, v106\n"
    "v_exp_f32 v107, v107\n"
    "v_cvt_pk_bf16_f32 v96, v96, v97\n"
    "v_cvt_pk_bf16_f32 v97, v98, v99\n"
    "v_cvt_pk_bf16_f32 v98, v100, v101\n"
    "v_cvt_pk_bf16_f32 v99, v102, v103\n"
    "v_cvt_pk_bf16_f32 v104, v104, v105\n"
    "v_cvt_pk_bf16_f32 v105, v106, v107\n"
    "s_waitcnt lgkmcnt(1)\n"
    "v_mfma_f32_16x16x32_bf16 v[108:111], v[80:83], v[96:99], 0\n"
    "v_mfma_f32_16x16x32_bf16 v[88:91], v[32:35], v[96:99], 0\n"
    "v_mfma_f32_16x16x32_bf16 v[120:123], v[36:39], v[96:99], 0\n"
    "v_mfma_f32_16x16x32_bf16 v[92:95], v[40:43], v[96:99], 0\n"
    "s_waitcnt lgkmcnt(0)\n"
    "v_mfma_f32_16x16x16_bf16 v[108:111], v[84:85], v[104:105], v[108:111]\n"
    "v_mfma_f32_16x16x32_bf16 v[124:127], v[44:47], v[96:99], 0\n"
    "v_mfma_f32_16x16x32_bf16 v[112:115], v[48:51], v[96:99], 0\n"
    "v_mfma_f32_16x16x32_bf16 v[128:131], v[52:55], v[96:99], 0\n"
    "v_mfma_f32_16x16x32_bf16 v[116:119], v[56:59], v[96:99], 0\n"
    "v_mfma_f32_16x16x32_bf16 v[132:135], v[60:63], v[96:99], 0\n"
    "v_rcp_f32 v86, v108\n"
    "v_mfma_f32_16x16x16_bf16 v[88:91], v[64:65], v[104:105], v[88:91]\n"
    "v_mfma_f32_16x16x16_bf16 v[120:123], v[66:67], v[104:105], v[120:123]\n"
    "v_mfma_f32_16x16x16_bf16 v[92:95], v[68:69], v[104:105], v[92:95]\n"
    "v_mfma_f32_16x16x16_bf16 v[124:127], v[70:71], v[104:105], v[124:127]\n"
    "v_mfma_f32_16x16x16_bf16 v[112:115], v[72:73], v[104:105], v[112:115]\n"
    "v_mfma_f32_16x16x16_bf16 v[128:131], v[74:75], v[104:105], v[128:131]\n"
    "v_mfma_f32_16x16x16_bf16 v[116:119], v[76:77], v[104:105], v[116:119]\n"
    "v_mfma_f32_16x16x16_bf16 v[132:135], v[78:79], v[104:105], v[132:135]\n"
    "v_pk_mul_f32 v[88:89], v[88:89], v[86:87] op_sel_hi:[1,0]\n"
    "v_pk_mul_f32 v[90:91], v[90:91], v[86:87] op_sel_hi:[1,0]\n"
    "v_pk_mul_f32 v[120:121], v[120:121], v[86:87] op_sel_hi:[1,0]\n"
    "v_pk_mul_f32 v[122:123], v[122:123], v[86:87] op_sel_hi:[1,0]\n"
    "v_cvt_pk_bf16_f32 v88, v88, v120\n"
    "v_cvt_pk_bf16_f32 v89, v89, v121\n"
    "v_cvt_pk_bf16_f32 v90, v90, v122\n"
    "v_cvt_pk_bf16_f32 v91, v91, v123\n"
    "v_pk_mul_f32 v[92:93], v[92:93], v[86:87] op_sel_hi:[1,0]\n"
    "v_pk_mul_f32 v[94:95], v[94:95], v[86:87] op_sel_hi:[1,0]\n"
    "v_pk_mul_f32 v[124:125], v[124:125], v[86:87] op_sel_hi:[1,0]\n"
    "v_pk_mul_f32 v[126:127], v[126:127], v[86:87] op_sel_hi:[1,0]\n"
    "v_cvt_pk_bf16_f32 v92, v92, v124\n"
    "v_cvt_pk_bf16_f32 v93, v93, v125\n"
    "v_cvt_pk_bf16_f32 v94, v94, v126\n"
    "v_cvt_pk_bf16_f32 v95, v95, v127\n"
    "v_pk_mul_f32 v[112:113], v[112:113], v[86:87] op_sel_hi:[1,0]\n"
    "v_pk_mul_f32 v[114:115], v[114:115], v[86:87] op_sel_hi:[1,0]\n"
    "v_pk_mul_f32 v[128:129], v[128:129], v[86:87] op_sel_hi:[1,0]\n"
    "v_pk_mul_f32 v[130:131], v[130:131], v[86:87] op_sel_hi:[1,0]\n"
    "v_cvt_pk_bf16_f32 v112, v112, v128\n"
    "v_cvt_pk_bf16_f32 v113, v113, v129\n"
    "v_cvt_pk_bf16_f32 v114, v114, v130\n"
    "v_cvt_pk_bf16_f32 v115, v115, v131\n"
    "v_pk_mul_f32 v[116:117], v[116:117], v[86:87] op_sel_hi:[1,0]\n"
    "v_pk_mul_f32 v[118:119], v[118:119], v[86:87] op_sel_hi:[1,0]\n"
    "v_pk_mul_f32 v[132:133], v[132:133], v[86:87] op_sel_hi:[1,0]\n"
    "v_pk_mul_f32 v[134:135], v[134:135], v[86:87] op_sel_hi:[1,0]\n"
    "v_cvt_pk_bf16_f32 v116, v116, v132\n"
    "v_cvt_pk_bf16_f32 v117, v117, v133\n"
    "v_cvt_pk_bf16_f32 v118, v118, v134\n"
    "v_cvt_pk_bf16_f32 v119, v119, v135\n"
    "ds_read_b128 v[32:35], v2\n"
    "ds_read_b128 v[36:39], v2 offset:1024\n"
    "ds_read_b128 v[40:43], v2 offset:2048\n"
    "ds_read_b128 v[44:47], v2 offset:3072\n"
    "ds_read_b128 v[48:51], v2 offset:4096\n"
    "ds_read_b128 v[52:55], v2 offset:5120\n"
    "ds_read_b128 v[56:59], v2 offset:6144\n"
    "ds_read_b128 v[60:63], v2 offset:7168\n"
    "ds_read_b128 v[64:67], v2 offset:8192\n"
    "ds_read_b128 v[68:71], v2 offset:9216\n"
    "ds_read_b128 v[72:75], v2 offset:10240\n"
    "ds_read_b128 v[76:79], v2 offset:11264\n"
    "ds_read_b128 v[80:83], v2 offset:12288\n"
    "ds_read_b128 v[84:87], v2 offset:13312\n"
    "v_mov_b64 v[124:125], 0\n"
    "v_mov_b64 v[126:127], 0\n"
    "v_mov_b64 v[128:129], 0\n"
    "v_mov_b64 v[130:131], 0\n"
    "s_waitcnt vmcnt(0)\n"
    "s_cmp_lg_u32 s59, 0\n"
    "s_cbranch_scc1 .Lkn10_%=\n"
    "s_waitcnt lgkmcnt(0)\n"
    "s_add_u32 s73, s48, 1\n"
    "s_and_b32 s73, s73, 63\n"
    "s_lshl_b32 s73, s73, 2\n"
    "s_add_u32 s73, s73, s92\n"
    "s_mov_b32 s53, s73\n"
    "s_mov_b32 s56, 0\n"
    "v_mov_b32 v134, s53\n"
    ".Lpoll11_%=:\n"
    "global_load_dword v135, v134, %1 sc1\n"
    "s_waitcnt vmcnt(0)\n"
    "v_readfirstlane_b32 s73, v135\n"
    "s_cmp_ge_u32 s73, 1\n"
    "s_cbranch_scc1 .Lpoll11_x%=\n"
    "s_add_u32 s56, s56, 1\n"
    "s_cmp_lt_u32 s56, 3\n"
    "s_cbranch_scc1 .Lpoll11_%=\n"
    "s_sleep 8\n"
    "s_cmp_lt_u32 s56, s57\n"
    "s_cbranch_scc1 .Lpoll11_%=\n"
    "s_mov_b32 s57, 0\n"
    "v_mov_b32 v135, 1\n"
    "global_store_dword v11, v135, %1 sc1\n"
    ".Lpoll11_x%=:\n"
    "buffer_load_dwordx4 v[12:15], v5, s[40:43], s60 offen sc1\n"
    "buffer_load_dwordx4 v[16:19], v5, s[40:43], s61 offen sc1\n"
    "buffer_load_dwordx4 v[20:23], v5, s[40:43], s62 offen sc1\n"
    "buffer_load_dwordx4 v[24:27], v5, s[40:43], s63 offen sc1\n"
    "buffer_load_dwordx4 v[28:31], v5, s[40:43], s64 offen sc1\n"
    "s_waitcnt vmcnt(0)\n"
    ".Lkn10_%=:\n"
    "s_add_u32 s73, s48, 2\n"
    "s_and_b32 s73, s73, 63\n"
    "s_lshl_b32 s73, s73, 2\n"
    "s_add_u32 s73, s73, s92\n"
    "v_mov_b32 v134, s73\n"
    "global_load_dword v133, v134, %1 sc1\n"
    "s_add_u32 s73, s48, 2\n"
    "s_and_b32 s73, s73, 63\n"
    "s_lshr_b32 s51, s73, 2\n"
    "s_xor_b32 s51, s51, s69\n"
    "s_and_b32 s51, s51, 1\n"
    "s_lshl_b32 s51, s51, 13\n"
    "s_lshl_b32 s73, s73, 5\n"
    "s_add_u32 s73, s73, %4\n"
    "s_lshl_b32 s73, s73, 14\n"
    "s_add_u32 s73, s73, s51\n"
    "s_add_u32 s60, s73, 0x6800000\n"
    "s_add_u32 s61, s73, 0x6801000\n"
    "s_add_u32 s62, s73, 0x8800000\n"
    "s_add_u32 s63, s73, 0x8801000\n"
    "s_lshl_b32 s51, %5, 12\n"
    "s_add_u32 s73, s73, s51\n"
    "s_add_u32 s64, s73, 0x4800000\n"
    "s_mov_b32 s73, 1700\n"
    "s_sub_u32 s53, s73, s58\n"
    "s_waitcnt lgkmcnt(0)\n"
    ".Ltl12_%=:\n"
    "s_memtime s[50:51]\n"
    "s_waitcnt lgkmcnt(0)\n"
    "s_sub_u32 s50, s50, s52\n"
    "s_cmp_lt_u32 s50, s53\n"
    "s_cbranch_scc1 .Ltl12_%=\n"
    "s_lshr_b32 s51, s48, 2\n"
    "s_xor_b32 s51, s51, s69\n"
    "s_and_b32 s51, s51, 1\n"
    "s_lshl_b32 s51, s51, 13\n"
    "s_lshl_b32 s73, s48, 19\n"
    "s_add_u32 s73, s73, s75\n"
    "s_add_u32 s73, s73, s51\n"
    "buffer_store_dwordx4 v[88:91], v0, s[40:43], s73 offen sc1\n"
    "buffer_store_dwordx4 v[92:95], v0, s[40:43], s73 offen offset:1024 sc1\n"
    "buffer_store_dwordx4 v[112:115], v0, s[40:43], s73 offen offset:2048 sc1\n"
    "buffer_store_dwordx4 v[116:119], v0, s[40:43], s73 offen offset:3072 sc1\n"
    "s_waitcnt lgkmcnt(0)\n"
    "s_barrier\n"
    "ds_write_b128 v5, v[12:15]\n"
    "ds_write_b128 v5, v[16:19] offset:4096\n"
    "ds_write_b128 v5, v[20:23] offset:8192\n"
    "ds_write_b128 v5, v[24:27] offset:12288\n"
    "ds_write_b128 v5, v[28:31] offset:16384\n"
    "s_waitcnt vmcnt(0)\n"
    "v_readfirstlane_b32 s59, v133\n"
    "s_lshl_b32 s73, s48, 8\n"
    "s_add_u32 s73, s73, s93\n"
    "v_mov_b32 v134, s73\n"
    "v_mov_b32 v135, 1\n"
    "s_mov_b64 exec, 1\n"
    "global_atomic_add v134, v135, %1\n"
    "s_mov_b64 exec, -1\n"
    "s_add_u32 s48, s48, 1\n"
    "s_waitcnt lgkmcnt(0)\n"
    "s_barrier\n"
    "s_branch .Lstep_%=\n"
    ".Lend_%=:\n"
    "s_cmp_eq_u32 %2, 3\n"
    "s_cbranch_scc0 .Lout_%=\n"
    "ds_read_b128 v[32:35], v0 offset:16384\n"
    "ds_read_b128 v[48:51], v0\n"
    "ds_read_b128 v[64:67], v0 offset:4096\n"
    "ds_read_b128 v[80:83], v4 offset:20480\n"
    "ds_read_b128 v[36:39], v0 offset:17408\n"
    "ds_read_b128 v[52:55], v0 offset:1024\n"
    "ds_read_b128 v[68:71], v0 offset:5120\n"
    "ds_read_b128 v[84:87], v4 offset:20544\n"
    "ds_read_b128 v[40:43], v0 offset:18432\n"
    "ds_read_b128 v[56:59], v0 offset:2048\n"
    "ds_read_b128 v[72:75], v0 offset:6144\n"
    "ds_read_b128 v[88:91], v4 offset:20608\n"
    "ds_read_b128 v[44:47], v0 offset:19456\n"
    "ds_read_b128 v[60:63], v0 offset:3072\n"
    "ds_read_b128 v[76:79], v0 offset:7168\n"
    "ds_read_b128 v[92:95], v4 offset:20672\n"
    "s_waitcnt lgkmcnt(14)\n"
    "v_mfma_f32_16x16x32_bf16 v[96:99], v[48:51], v[32:35], 0\n"
    "s_waitcnt lgkmcnt(13)\n"
    "v_mfma_f32_16x16x32_bf16 v[100:103], v[64:67], v[32:35], 0\n"
    "s_waitcnt lgkmcnt(12)\n"
    "v_mfma_f32_16x16x32_bf16 v[104:107], v[80:83], v[32:35], 0\n"
    "s_waitcnt lgkmcnt(10)\n"
    "v_mfma_f32_16x16x32_bf16 v[96:99], v[52:55], v[36:39], v[96:99]\n"
    "s_waitcnt lgkmcnt(9)\n"
    "v_mfma_f32_16x16x32_bf16 v[100:103], v[68:71], v[36:39], v[100:103]\n"
    "s_waitcnt lgkmcnt(8)\n"
    "v_mfma_f32_16x16x32_bf16 v[104:107], v[84:87], v[36:39], v[104:107]\n"
    "s_waitcnt lgkmcnt(6)\n"
    "v_mfma_f32_16x16x32_bf16 v[96:99], v[56:59], v[40:43], v[96:99]\n"
    "s_waitcnt lgkmcnt(5)\n"
    "v_mfma_f32_16x16x32_bf16 v[100:103], v[72:75], v[40:43], v[100:103]\n"
    "s_waitcnt lgkmcnt(4)\n"
    "v_mfma_f32_16x16x32_bf16 v[104:107], v[88:91], v[40:43], v[104:107]\n"
    "s_waitcnt lgkmcnt(2)\n"
    "v_mfma_f32_16x16x32_bf16 v[96:99], v[60:63], v[44:47], v[96:99]\n"
    "s_waitcnt lgkmcnt(1)\n"
    "v_mfma_f32_16x16x32_bf16 v[100:103], v[76:79], v[44:47], v[100:103]\n"
    "s_waitcnt lgkmcnt(0)\n"
    "v_mfma_f32_16x16x32_bf16 v[104:107], v[92:95], v[44:47], v[104:107]\n"
    "ds_read_b128 v[32:35], v0 offset:8192\n"
    "ds_read_b128 v[36:39], v0 offset:12288\n"
    "ds_read_b128 v[40:43], v0 offset:9216\n"
    "ds_read_b128 v[44:47], v0 offset:13312\n"
    "ds_read_b128 v[48:51], v0 offset:10240\n"
    "ds_read_b128 v[52:55], v0 offset:14336\n"
    "ds_read_b128 v[56:59], v0 offset:11264\n"
    "ds_read_b128 v[60:63], v0 offset:15360\n"
    "ds_read_b128 v[64:67], v0 offset:22784\n"
    "ds_read_b128 v[68:71], v0 offset:23808\n"
    "ds_read_b128 v[72:75], v0 offset:24832\n"
    "ds_read_b128 v[76:79], v0 offset:25856\n"
    "ds_read_b128 v[80:83], v11 offset:31888\n"
    "ds_read_b64 v[84:85], v11 offset:31904\n"
    "v_mov_b32 v86, 0xd01502f9\n"
    "v_cndmask_b32 v96, v86, v96, s[76:77]\n"
    "v_cndmask_b32 v97, v86, v97, s[78:79]\n"
    "v_cndmask_b32 v98, v86, v98, s[80:81]\n"
    "v_cndmask_b32 v99, v86, v99, s[82:83]\n"
    "v_cndmask_b32 v100, v86, v100, s[84:85]\n"
    "v_cndmask_b32 v101, v86, v101, s[86:87]\n"
    "v_cndmask_b32 v102, v86, v102, s[88:89]\n"
    "v_cndmask_b32 v103, v86, v103, s[90:91]\n"
    "v_maximum3_f32 v87, v104, v105, v106\n"
    "v_maximum3_f32 v87, v87, v107, v96\n"
    "v_maximum3_f32 v87, v87, v97, v98\n"
    "v_maximum3_f32 v87, v87, v99, v100\n"
    "v_maximum3_f32 v87, v87, v101, v102\n"
    "v_maximum3_f32 v87, v87, v103, v103\n"
    "v_mov_b32 v86, v87\n"
    "s_nop 1\n"
    "v_permlane16_swap_b32 v86, v87\n"
    "v_maximum3_f32 v87, v87, v86, v86\n"
    "v_mov_b32 v86, v87\n"
    "s_nop 1\n"
    "v_permlane32_swap_b32 v86, v87\n"
    "v_maximum3_f32 v87, v87, v86, v86\n"
    "v_mul_f32 v87, 0xbe0293ee, v87\n"
    "v_fmamk_f32 v96, v96, 0x3e0293ee, v87\n"
    "v_fmamk_f32 v97, v97, 0x3e0293ee, v87\n"
    "v_fmamk_f32 v98, v98, 0x3e0293ee, v87\n"
    "v_fmamk_f32 v99, v99, 0x3e0293ee, v87\n"
    "v_fmamk_f32 v100, v100, 0x3e0293ee, v87\n"
    "v_fmamk_f32 v101, v101, 0x3e0293ee, v87\n"
    "v_fmamk_f32 v102, v102, 0x3e0293ee, v87\n"
    "v_fmamk_f32 v103, v103, 0x3e0293ee, v87\n"
    "v_fmamk_f32 v104, v104, 0x3e0293ee, v87\n"
    "v_fmamk_f32 v105, v105, 0x3e0293ee, v87\n"
    "v_fmamk_f32 v106, v106, 0x3e0293ee, v87\n"
    "v_fmamk_f32 v107, v107, 0x3e0293ee, v87\n"
    "v_exp_f32 v96, v96\n"
    "v_exp_f32 v97, v97\n"
    "v_exp_f32 v98, v98\n"
    "v_exp_f32 v99, v99\n"
    "v_exp_f32 v100, v100\n"
    "v_exp_f32 v101, v101\n"
    "v_exp_f32 v102, v102\n"
    "v_exp_f32 v103, v103\n"
    "v_exp_f32 v104, v104\n"
    "v_exp_f32 v105, v105\n"
    "v_exp_f32 v106, v106\n"
    "v_exp_f32 v107, v107\n"
    "v_cvt_pk_bf16_f32 v96, v96, v97\n"
    "v_cvt_pk_bf16_f32 v97, v98, v99\n"
    "v_cvt_pk_bf16_f32 v98, v100, v101\n"
    "v_cvt_pk_bf16_f32 v99, v102, v103\n"
    "v_cvt_pk_bf16_f32 v104, v104, v105\n"
    "v_cvt_pk_bf16_f32 v105, v106, v107\n"
    "s_waitcnt lgkmcnt(1)\n"
    "v_mfma_f32_16x16x32_bf16 v[108:111], v[80:83], v[96:99], 0\n"
    "v_mfma_f32_16x16x32_bf16 v[88:91], v[32:35], v[96:99], 0\n"
    "v_mfma_f32_16x16x32_bf16 v[120:123], v[36:39], v[96:99], 0\n"
    "v_mfma_f32_16x16x32_bf16 v[92:95], v[40:43], v[96:99], 0\n"
    "s_waitcnt lgkmcnt(0)\n"
    "v_mfma_f32_16x16x16_bf16 v[108:111], v[84:85], v[104:105], v[108:111]\n"
    "v_mfma_f32_16x16x32_bf16 v[124:127], v[44:47], v[96:99], 0\n"
    "v_mfma_f32_16x16x32_bf16 v[112:115], v[48:51], v[96:99], 0\n"
    "v_mfma_f32_16x16x32_bf16 v[128:131], v[52:55], v[96:99], 0\n"
    "v_mfma_f32_16x16x32_bf16 v[116:119], v[56:59], v[96:99], 0\n"
    "v_mfma_f32_16x16x32_bf16 v[132:135], v[60:63], v[96:99], 0\n"
    "v_rcp_f32 v86, v108\n"
    "v_mfma_f32_16x16x16_bf16 v[88:91], v[64:65], v[104:105], v[88:91]\n"
    "v_mfma_f32_16x16x16_bf16 v[120:123], v[66:67], v[104:105], v[120:123]\n"
    "v_mfma_f32_16x16x16_bf16 v[92:95], v[68:69], v[104:105], v[92:95]\n"
    "v_mfma_f32_16x16x16_bf16 v[124:127], v[70:71], v[104:105], v[124:127]\n"
    "v_mfma_f32_16x16x16_bf16 v[112:115], v[72:73], v[104:105], v[112:115]\n"
    "v_mfma_f32_16x16x16_bf16 v[128:131], v[74:75], v[104:105], v[128:131]\n"
    "v_mfma_f32_16x16x16_bf16 v[116:119], v[76:77], v[104:105], v[116:119]\n"
    "v_mfma_f32_16x16x16_bf16 v[132:135], v[78:79], v[104:105], v[132:135]\n"
    "v_pk_mul_f32 v[88:89], v[88:89], v[86:87] op_sel_hi:[1,0]\n"
    "v_pk_mul_f32 v[90:91], v[90:91], v[86:87] op_sel_hi:[1,0]\n"
    "v_pk_mul_f32 v[120:121], v[120:121], v[86:87] op_sel_hi:[1,0]\n"
    "v_pk_mul_f32 v[122:123], v[122:123], v[86:87] op_sel_hi:[1,0]\n"
    "v_cvt_pk_bf16_f32 v88, v88, v120\n"
    "v_cvt_pk_bf16_f32 v89, v89, v121\n"
    "v_cvt_pk_bf16_f32 v90, v90, v122\n"
    "v_cvt_pk_bf16_f32 v91, v91, v123\n"
    "v_pk_mul_f32 v[92:93], v[92:93], v[86:87] op_sel_hi:[1,0]\n"
    "v_pk_mul_f32 v[94:95], v[94:95], v[86:87] op_sel_hi:[1,0]\n"
    "v_pk_mul_f32 v[124:125], v[124:125], v[86:87] op_sel_hi:[1,0]\n"
    "v_pk_mul_f32 v[126:127], v[126:127], v[86:87] op_sel_hi:[1,0]\n"
    "v_cvt_pk_bf16_f32 v92, v92, v124\n"
    "v_cvt_pk_bf16_f32 v93, v93, v125\n"
    "v_cvt_pk_bf16_f32 v94, v94, v126\n"
    "v_cvt_pk_bf16_f32 v95, v95, v127\n"
    "v_pk_mul_f32 v[112:113], v[112:113], v[86:87] op_sel_hi:[1,0]\n"
    "v_pk_mul_f32 v[114:115], v[114:115], v[86:87] op_sel_hi:[1,0]\n"
    "v_pk_mul_f32 v[128:129], v[128:129], v[86:87] op_sel_hi:[1,0]\n"
    "v_pk_mul_f32 v[130:131], v[130:131], v[86:87] op_sel_hi:[1,0]\n"
    "v_cvt_pk_bf16_f32 v112, v112, v128\n"
    "v_cvt_pk_bf16_f32 v113, v113, v129\n"
    "v_cvt_pk_bf16_f32 v114, v114, v130\n"
    "v_cvt_pk_bf16_f32 v115, v115, v131\n"
    "v_pk_mul_f32 v[116:117], v[116:117], v[86:87] op_sel_hi:[1,0]\n"
    "v_pk_mul_f32 v[118:119], v[118:119], v[86:87] op_sel_hi:[1,0]\n"
    "v_pk_mul_f32 v[132:133], v[132:133], v[86:87] op_sel_hi:[1,0]\n"
    "v_pk_mul_f32 v[134:135], v[134:135], v[86:87] op_sel_hi:[1,0]\n"
    "v_cvt_pk_bf16_f32 v116, v116, v132\n"
    "v_cvt_pk_bf16_f32 v117, v117, v133\n"
    "v_cvt_pk_bf16_f32 v118, v118, v134\n"
    "v_cvt_pk_bf16_f32 v119, v119, v135\n"
    "s_lshr_b32 s51, s48, 2\n"
    "s_xor_b32 s51, s51, s69\n"
    "s_and_b32 s51, s51, 1\n"
    "s_lshl_b32 s51, s51, 13\n"
    "s_lshl_b32 s73, s48, 19\n"
    "s_add_u32 s73, s73, s75\n"
    "s_add_u32 s73, s73, s51\n"
    "buffer_store_dwordx4 v[88:91], v0, s[40:43], s73 offen sc1\n"
    "buffer_store_dwordx4 v[92:95], v0, s[40:43], s73 offen offset:1024 sc1\n"
    "buffer_store_dwordx4 v[112:115], v0, s[40:43], s73 offen offset:2048 sc1\n"
    "buffer_store_dwordx4 v[116:119], v0, s[40:43], s73 offen offset:3072 sc1\n"
    "s_waitcnt vmcnt(0)\n"
    "s_lshl_b32 s73, s48, 8\n"
    "s_add_u32 s73, s73, s93\n"
    "v_mov_b32 v134, s73\n"
    "v_mov_b32 v135, 1\n"
    "s_mov_b64 exec, 1\n"
    "global_atomic_add v134, v135, %1\n"
    "s_mov_b64 exec, -1\n"
    ".Lout_%=:\n"
    "s_waitcnt vmcnt(0)\n"
    "s_endpgm\n"
    ".p2align 6\n"
    ".Lrole1_%=:\n"
    "s_mov_b64 s[40:41], %0\n"
    "s_and_b32 s41, s41, 0xffff\n"
    "s_mov_b32 s42, 0xc800000\n"
    "s_mov_b32 s43, 0x20000\n"
    "s_mov_b32 s54, 131072\n"
    "v_mbcnt_lo_u32_b32 v7, -1, 0\n"
    "v_mbcnt_hi_u32_b32 v7, -1, v7\n"
    "s_lshl_b32 s51, %2, 8\n"
    "s_add_u32 s51, s51, 147456\n"
    "v_lshl_add_u32 v7, v7, 2, s51\n"
    "ds_read_b32 v0, v7\n"
    "ds_read_b32 v1, v7 offset:1024\n"
    "ds_read_b32 v2, v7 offset:2048\n"
    "ds_read_b32 v3, v7 offset:3072\n"
    "ds_read_b32 v4, v7 offset:4096\n"
    "ds_read_b32 v8, v7 offset:5120\n"
    "ds_read_b32 v9, v7 offset:6144\n"
    "ds_read_b32 v10, v7 offset:7168\n"
    "s_waitcnt lgkmcnt(0)\n"
    "s_barrier\n"
    "s_lshl_b32 s56, %2, 10\n"
    "s_mov_b64 s[44:45], %10\n"
    "s_and_b32 s45, s45, 0xffff\n"
    "s_mov_b32 s46, 0x480000\n"
    "s_mov_b32 s47, 0x20000\n"
    "s_add_u32 m0, s56, 0\n"
    "s_add_u32 s57, %8, 0\n"
    "buffer_load_dwordx4 v8, s[44:47], s57 offen lds\n"
    "s_add_u32 m0, s56, 4096\n"
    "s_add_u32 s57, %8, 131072\n"
    "buffer_load_dwordx4 v8, s[44:47], s57 offen lds\n"
    "s_add_u32 m0, s56, 8192\n"
    "s_add_u32 s57, %8, 262144\n"
    "buffer_load_dwordx4 v8, s[44:47], s57 offen lds\n"
    "s_add_u32 m0, s56, 12288\n"
    "s_add_u32 s57, %8, 393216\n"
    "buffer_load_dwordx4 v8, s[44:47], s57 offen lds\n"
    "s_add_u32 m0, s56, 16384\n"
    "s_add_u32 s57, %8, 524288\n"
    "buffer_load_dwordx4 v8, s[44:47], s57 offen lds\n"
    "s_add_u32 m0, s56, 20480\n"
    "s_add_u32 s57, %8, 655360\n"
    "buffer_load_dwordx4 v8, s[44:47], s57 offen lds\n"
    "s_add_u32 m0, s56, 24576\n"
    "s_add_u32 s57, %8, 786432\n"
    "buffer_load_dwordx4 v8, s[44:47], s57 offen lds\n"
    "s_add_u32 m0, s56, 28672\n"
    "s_add_u32 s57, %8, 917504\n"
    "buffer_load_dwordx4 v8, s[44:47], s57 offen lds\n"
    "s_add_u32 m0, s56, 32768\n"
    "s_add_u32 s57, %8, 1048576\n"
    "buffer_load_dwordx4 v8, s[44:47], s57 offen lds\n"
    "s_add_u32 m0, s56, 36864\n"
    "s_add_u32 s57, %8, 1179648\n"
    "buffer_load_dwordx4 v8, s[44:47], s57 offen lds\n"
    "s_add_u32 m0, s56, 40960\n"
    "s_add_u32 s57, %8, 1310720\n"
    "buffer_load_dwordx4 v8, s[44:47], s57 offen lds\n"
    "s_add_u32 m0, s56, 45056\n"
    "s_add_u32 s57, %8, 1441792\n"
    "buffer_load_dwordx4 v8, s[44:47], s57 offen lds\n"
    "s_add_u32 m0, s56, 49152\n"
    "s_add_u32 s57, %8, 1572864\n"
    "buffer_load_dwordx4 v8, s[44:47], s57 offen lds\n"
    "s_add_u32 m0, s56, 53248\n"
    "s_add_u32 s57, %8, 1703936\n"
    "buffer_load_dwordx4 v8, s[44:47], s57 offen lds\n"
    "s_add_u32 m0, s56, 57344\n"
    "s_add_u32 s57, %8, 1835008\n"
    "buffer_load_dwordx4 v8, s[44:47], s57 offen lds\n"
    "s_add_u32 m0, s56, 61440\n"
    "s_add_u32 s57, %8, 1966080\n"
    "buffer_load_dwordx4 v8, s[44:47], s57 offen lds\n"
    "s_add_u32 m0, s56, 65536\n"
    "s_add_u32 s57, %8, 2097152\n"
    "buffer_load_dwordx4 v8, s[44:47], s57 offen lds\n"
    "s_add_u32 m0, s56, 69632\n"
    "s_add_u32 s57, %8, 2228224\n"
    "buffer_load_dwordx4 v8, s[44:47], s57 offen lds\n"
    "s_add_u32 m0, s56, 73728\n"
    "s_add_u32 s57, %8, 2359296\n"
    "buffer_load_dwordx4 v8, s[44:47], s57 offen lds\n"
    "s_add_u32 m0, s56, 77824\n"
    "s_add_u32 s57, %8, 2490368\n"
    "buffer_load_dwordx4 v8, s[44:47], s57 offen lds\n"
    "s_add_u32 m0, s56, 81920\n"
    "s_add_u32 s57, %8, 2621440\n"
    "buffer_load_dwordx4 v8, s[44:47], s57 offen lds\n"
    "s_add_u32 m0, s56, 86016\n"
    "s_add_u32 s57, %8, 2752512\n"
    "buffer_load_dwordx4 v8, s[44:47], s57 offen lds\n"
    "s_add_u32 m0, s56, 90112\n"
    "s_add_u32 s57, %8, 2883584\n"
    "buffer_load_dwordx4 v8, s[44:47], s57 offen lds\n"
    "s_add_u32 m0, s56, 94208\n"
    "s_add_u32 s57, %8, 3014656\n"
    "buffer_load_dwordx4 v8, s[44:47], s57 offen lds\n"
    "s_add_u32 m0, s56, 98304\n"
    "s_add_u32 s57, %8, 3145728\n"
    "buffer_load_dwordx4 v8, s[44:47], s57 offen lds\n"
    "s_add_u32 m0, s56, 102400\n"
    "s_add_u32 s57, %8, 3276800\n"
    "buffer_load_dwordx4 v8, s[44:47], s57 offen lds\n"
    "s_add_u32 m0, s56, 106496\n"
    "s_add_u32 s57, %8, 3407872\n"
    "buffer_load_dwordx4 v8, s[44:47], s57 offen lds\n"
    "s_add_u32 m0, s56, 110592\n"
    "s_add_u32 s57, %8, 3538944\n"
    "buffer_load_dwordx4 v8, s[44:47], s57 offen lds\n"
    "s_add_u32 m0, s56, 114688\n"
    "s_add_u32 s57, %8, 3670016\n"
    "buffer_load_dwordx4 v8, s[44:47], s57 offen lds\n"
    "s_add_u32 m0, s56, 118784\n"
    "s_add_u32 s57, %8, 3801088\n"
    "buffer_load_dwordx4 v8, s[44:47], s57 offen lds\n"
    "s_add_u32 m0, s56, 122880\n"
    "s_add_u32 s57, %8, 3932160\n"
    "buffer_load_dwordx4 v8, s[44:47], s57 offen lds\n"
    "s_add_u32 m0, s56, 126976\n"
    "s_add_u32 s57, %8, 4063232\n"
    "buffer_load_dwordx4 v8, s[44:47], s57 offen lds\n"
    "s_add_u32 m0, s56, 131072\n"
    "s_add_u32 s57, %8, 4194304\n"
    "buffer_load_dwordx4 v8, s[44:47], s57 offen lds\n"
    "s_add_u32 m0, s56, 135168\n"
    "s_add_u32 s57, %8, 4325376\n"
    "buffer_load_dwordx4 v8, s[44:47], s57 offen lds\n"
    "s_add_u32 m0, s56, 139264\n"
    "s_add_u32 s57, %8, 4456448\n"
    "buffer_load_dwordx4 v8, s[44:47], s57 offen lds\n"
    "s_add_u32 m0, s56, 143360\n"
    "s_add_u32 s57, %8, 4587520\n"
    "buffer_load_dwordx4 v8, s[44:47], s57 offen lds\n"
    "s_waitcnt vmcnt(32) lgkmcnt(0)\n"
    "s_barrier\n"
    "ds_read_b64_tr_b16 a[0:1], v9\n"
    "ds_read_b64_tr_b16 a[2:3], v9 offset:1024\n"
    "ds_read_b64_tr_b16 a[4:5], v10\n"
    "ds_read_b64_tr_b16 a[6:7], v10 offset:1024\n"
    "ds_read_b64_tr_b16 a[8:9], v9 offset:8192\n"
    "ds_read_b64_tr_b16 a[10:11], v9 offset:9216\n"
    "ds_read_b64_tr_b16 a[12:13], v10 offset:8192\n"
    "ds_read_b64_tr_b16 a[14:15], v10 offset:9216\n"
    "s_waitcnt vmcnt(28) lgkmcnt(0)\n"
    "s_barrier\n"
    "s_mov_b64 s[44:45], %11\n"
    "s_and_b32 s45, s45, 0xffff\n"
    "s_mov_b32 s46, 0x480000\n"
    "s_mov_b32 s47, 0x20000\n"
    "s_add_u32 m0, s56, 0\n"
    "s_add_u32 s57, %8, 0\n"
    "buffer_load_dwordx4 v8, s[44:47], s57 offen lds\n"
    "s_add_u32 m0, s56, 4096\n"
    "s_add_u32 s57, %8, 131072\n"
    "buffer_load_dwordx4 v8, s[44:47], s57 offen lds\n"
    "s_add_u32 m0, s56, 8192\n"
    "s_add_u32 s57, %8, 262144\n"
    "buffer_load_dwordx4 v8, s[44:47], s57 offen lds\n"
    "s_add_u32 m0, s56, 12288\n"
    "s_add_u32 s57, %8, 393216\n"
    "buffer_load_dwordx4 v8, s[44:47], s57 offen lds\n"
    "ds_read_b64_tr_b16 a[16:17], v9 offset:16384\n"
    "ds_read_b64_tr_b16 a[18:19], v9 offset:17408\n"
    "ds_read_b64_tr_b16 a[20:21], v10 offset:16384\n"
    "ds_read_b64_tr_b16 a[22:23], v10 offset:17408\n"
    "ds_read_b64_tr_b16 a[24:25], v9 offset:24576\n"
    "ds_read_b64_tr_b16 a[26:27], v9 offset:25600\n"
    "ds_read_b64_tr_b16 a[28:29], v10 offset:24576\n"
    "ds_read_b64_tr_b16 a[30:31], v10 offset:25600\n"
    "s_waitcnt vmcnt(28) lgkmcnt(0)\n"
    "s_barrier\n"
    "s_add_u32 m0, s56, 16384\n"
    "s_add_u32 s57, %8, 524288\n"
    "buffer_load_dwordx4 v8, s[44:47], s57 offen lds\n"
    "s_add_u32 m0, s56, 20480\n"
    "s_add_u32 s57, %8, 655360\n"
    "buffer_load_dwordx4 v8, s[44:47], s57 offen lds\n"
    "s_add_u32 m0, s56, 24576\n"
    "s_add_u32 s57, %8, 786432\n"
    "buffer_load_dwordx4 v8, s[44:47], s57 offen lds\n"
    "s_add_u32 m0, s56, 28672\n"
    "s_add_u32 s57, %8, 917504\n"
    "buffer_load_dwordx4 v8, s[44:47], s57 offen lds\n"
    "ds_read_b64_tr_b16 a[32:33], v9 offset:32768\n"
    "ds_read_b64_tr_b16 a[34:35], v9 offset:33792\n"
    "ds_read_b64_tr_b16 a[36:37], v10 offset:32768\n"
    "ds_read_b64_tr_b16 a[38:39], v10 offset:33792\n"
    "ds_read_b64_tr_b16 a[40:41], v9 offset:40960\n"
    "ds_read_b64_tr_b16 a[42:43], v9 offset:41984\n"
    "ds_read_b64_tr_b16 a[44:45], v10 offset:40960\n"
    "ds_read_b64_tr_b16 a[46:47], v10 offset:41984\n"
    "s_waitcnt vmcnt(28) lgkmcnt(0)\n"
    "s_barrier\n"
    "s_add_u32 m0, s56, 32768\n"
    "s_add_u32 s57, %8, 1048576\n"
    "buffer_load_dwordx4 v8, s[44:47], s57 offen lds\n"
    "s_add_u32 m0, s56, 36864\n"
    "s_add_u32 s57, %8, 1179648\n"
    "buffer_load_dwordx4 v8, s[44:47], s57 offen lds\n"
    "s_add_u32 m0, s56, 40960\n"
    "s_add_u32 s57, %8, 1310720\n"
    "buffer_load_dwordx4 v8, s[44:47], s57 offen lds\n"
    "s_add_u32 m0, s56, 45056\n"
    "s_add_u32 s57, %8, 1441792\n"
    "buffer_load_dwordx4 v8, s[44:47], s57 offen lds\n"
    "ds_read_b64_tr_b16 a[48:49], v9 offset:49152\n"
    "ds_read_b64_tr_b16 a[50:51], v9 offset:50176\n"
    "ds_read_b64_tr_b16 a[52:53], v10 offset:49152\n"
    "ds_read_b64_tr_b16 a[54:55], v10 offset:50176\n"
    "ds_read_b64_tr_b16 a[56:57], v9 offset:57344\n"
    "ds_read_b64_tr_b16 a[58:59], v9 offset:58368\n"
    "ds_read_b64_tr_b16 a[60:61], v10 offset:57344\n"
    "ds_read_b64_tr_b16 a[62:63], v10 offset:58368\n"
    "s_waitcnt vmcnt(28) lgkmcnt(0)\n"
    "s_barrier\n"
    "s_add_u32 m0, s56, 49152\n"
    "s_add_u32 s57, %8, 1572864\n"
    "buffer_load_dwordx4 v8, s[44:47], s57 offen lds\n"
    "s_add_u32 m0, s56, 53248\n"
    "s_add_u32 s57, %8, 1703936\n"
    "buffer_load_dwordx4 v8, s[44:47], s57 offen lds\n"
    "s_add_u32 m0, s56, 57344\n"
    "s_add_u32 s57, %8, 1835008\n"
    "buffer_load_dwordx4 v8, s[44:47], s57 offen lds\n"
    "s_add_u32 m0, s56, 61440\n"
    "s_add_u32 s57, %8, 1966080\n"
    "buffer_load_dwordx4 v8, s[44:47], s57 offen lds\n"
    "v_add_u32 v14, 0x10000, v9\n"
    "ds_read_b64_tr_b16 a[64:65], v14\n"
    "ds_read_b64_tr_b16 a[66:67], v14 offset:1024\n"
    "v_add_u32 v13, 0x10000, v10\n"
    "ds_read_b64_tr_b16 a[68:69], v13\n"
    "ds_read_b64_tr_b16 a[70:71], v13 offset:1024\n"
    "ds_read_b64_tr_b16 a[72:73], v14 offset:8192\n"
    "ds_read_b64_tr_b16 a[74:75], v14 offset:9216\n"
    "ds_read_b64_tr_b16 a[76:77], v13 offset:8192\n"
    "ds_read_b64_tr_b16 a[78:79], v13 offset:9216\n"
    "s_waitcnt vmcnt(28) lgkmcnt(0)\n"
    "s_barrier\n"
    "s_add_u32 m0, s56, 65536\n"
    "s_add_u32 s57, %8, 2097152\n"
    "buffer_load_dwordx4 v8, s[44:47], s57 offen lds\n"
    "s_add_u32 m0, s56, 69632\n"
    "s_add_u32 s57, %8, 2228224\n"
    "buffer_load_dwordx4 v8, s[44:47], s57 offen lds\n"
    "s_add_u32 m0, s56, 73728\n"
    "s_add_u32 s57, %8, 2359296\n"
    "buffer_load_dwordx4 v8, s[44:47], s57 offen lds\n"
    "s_add_u32 m0, s56, 77824\n"
    "s_add_u32 s57, %8, 2490368\n"
    "buffer_load_dwordx4 v8, s[44:47], s57 offen lds\n"
    "ds_read_b64_tr_b16 a[80:81], v14 offset:16384\n"
    "ds_read_b64_tr_b16 a[82:83], v14 offset:17408\n"
    "ds_read_b64_tr_b16 a[84:85], v13 offset:16384\n"
    "ds_read_b64_tr_b16 a[86:87], v13 offset:17408\n"
    "ds_read_b64_tr_b16 a[88:89], v14 offset:24576\n"
    "ds_read_b64_tr_b16 a[90:91], v14 offset:25600\n"
    "ds_read_b64_tr_b16 a[92:93], v13 offset:24576\n"
    "ds_read_b64_tr_b16 a[94:95], v13 offset:25600\n"
    "s_waitcnt vmcnt(28) lgkmcnt(0)\n"
    "s_barrier\n"
    "s_add_u32 m0, s56, 81920\n"
    "s_add_u32 s57, %8, 2621440\n"
    "buffer_load_dwordx4 v8, s[44:47], s57 offen lds\n"
    "s_add_u32 m0, s56, 86016\n"
    "s_add_u32 s57, %8, 2752512\n"
    "buffer_load_dwordx4 v8, s[44:47], s57 offen lds\n"
    "s_add_u32 m0, s56, 90112\n"
    "s_add_u32 s57, %8, 2883584\n"
    "buffer_load_dwordx4 v8, s[44:47], s57 offen lds\n"
    "s_add_u32 m0, s56, 94208\n"
    "s_add_u32 s57, %8, 3014656\n"
    "buffer_load_dwordx4 v8, s[44:47], s57 offen lds\n"
    "ds_read_b64_tr_b16 a[96:97], v14 offset:32768\n"
    "ds_read_b64_tr_b16 a[98:99], v14 offset:33792\n"
    "ds_read_b64_tr_b16 a[100:101], v13 offset:32768\n"
    "ds_read_b64_tr_b16 a[102:103], v13 offset:33792\n"
    "ds_read_b64_tr_b16 a[104:105], v14 offset:40960\n"
    "ds_read_b64_tr_b16 a[106:107], v14 offset:41984\n"
    "ds_read_b64_tr_b16 a[108:109], v13 offset:40960\n"
    "ds_read_b64_tr_b16 a[110:111], v13 offset:41984\n"
    "s_waitcnt vmcnt(28) lgkmcnt(0)\n"
    "s_barrier\n"
    "s_add_u32 m0, s56, 98304\n"
    "s_add_u32 s57, %8, 3145728\n"
    "buffer_load_dwordx4 v8, s[44:47], s57 offen lds\n"
    "s_add_u32 m0, s56, 102400\n"
    "s_add_u32 s57, %8, 3276800\n"
    "buffer_load_dwordx4 v8, s[44:47], s57 offen lds\n"
    "s_add_u32 m0, s56, 106496\n"
    "s_add_u32 s57, %8, 3407872\n"
    "buffer_load_dwordx4 v8, s[44:47], s57 offen lds\n"
    "s_add_u32 m0, s56, 110592\n"
    "s_add_u32 s57, %8, 3538944\n"
    "buffer_load_dwordx4 v8, s[44:47], s57 offen lds\n"
    "ds_read_b64_tr_b16 a[112:113], v14 offset:49152\n"
    "ds_read_b64_tr_b16 a[114:115], v14 offset:50176\n"
    "ds_read_b64_tr_b16 a[116:117], v13 offset:49152\n"
    "ds_read_b64_tr_b16 a[118:119], v13 offset:50176\n"
    "ds_read_b64_tr_b16 a[120:121], v14 offset:57344\n"
    "ds_read_b64_tr_b16 a[122:123], v14 offset:58368\n"
    "ds_read_b64_tr_b16 a[124:125], v13 offset:57344\n"
    "ds_read_b64_tr_b16 a[126:127], v13 offset:58368\n"
    "s_waitcnt vmcnt(28) lgkmcnt(0)\n"
    "s_barrier\n"
    "s_add_u32 m0, s56, 114688\n"
    "s_add_u32 s57, %8, 3670016\n"
    "buffer_load_dwordx4 v8, s[44:47], s57 offen lds\n"
    "s_add_u32 m0, s56, 118784\n"
    "s_add_u32 s57, %8, 3801088\n"
    "buffer_load_dwordx4 v8, s[44:47], s57 offen lds\n"
    "s_add_u32 m0, s56, 122880\n"
    "s_add_u32 s57, %8, 3932160\n"
    "buffer_load_dwordx4 v8, s[44:47], s57 offen lds\n"
    "s_add_u32 m0, s56, 126976\n"
    "s_add_u32 s57, %8, 4063232\n"
    "buffer_load_dwordx4 v8, s[44:47], s57 offen lds\n"
    "v_add_u32 v12, 0x20000, v9\n"
    "ds_read_b64_tr_b16 a[128:129], v12\n"
    "ds_read_b64_tr_b16 a[130:131], v12 offset:1024\n"
    "v_add_u32 v11, 0x20000, v10\n"
    "ds_read_b64_tr_b16 a[132:133], v11\n"
    "ds_read_b64_tr_b16 a[134:135], v11 offset:1024\n"
    "ds_read_b64_tr_b16 a[136:137], v12 offset:8192\n"
    "ds_read_b64_tr_b16 a[138:139], v12 offset:9216\n"
    "ds_read_b64_tr_b16 a[140:141], v11 offset:8192\n"
    "ds_read_b64_tr_b16 a[142:143], v11 offset:9216\n"
    "s_waitcnt vmcnt(28) lgkmcnt(0)\n"
    "s_barrier\n"
    "s_add_u32 m0, s56, 131072\n"
    "s_add_u32 s57, %8, 4194304\n"
    "buffer_load_dwordx4 v8, s[44:47], s57 offen lds\n"
    "s_add_u32 m0, s56, 135168\n"
    "s_add_u32 s57, %8, 4325376\n"
    "buffer_load_dwordx4 v8, s[44:47], s57 offen lds\n"
    "s_add_u32 m0, s56, 139264\n"
    "s_add_u32 s57, %8, 4456448\n"
    "buffer_load_dwordx4 v8, s[44:47], s57 offen lds\n"
    "s_add_u32 m0, s56, 143360\n"
    "s_add_u32 s57, %8, 4587520\n"
    "buffer_load_dwordx4 v8, s[44:47], s57 offen lds\n"
    "ds_read_b64_tr_b16 a[144:145], v9\n"
    "ds_read_b64_tr_b16 a[146:147], v9 offset:1024\n"
    "ds_read_b64_tr_b16 a[148:149], v10\n"
    "ds_read_b64_tr_b16 a[150:151], v10 offset:1024\n"
    "ds_read_b64_tr_b16 a[152:153], v9 offset:8192\n"
    "ds_read_b64_tr_b16 a[154:155], v9 offset:9216\n"
    "ds_read_b64_tr_b16 a[156:157], v10 offset:8192\n"
    "ds_read_b64_tr_b16 a[158:159], v10 offset:9216\n"
    "s_waitcnt vmcnt(28) lgkmcnt(0)\n"
    "s_barrier\n"
    "s_mov_b64 s[44:45], %12\n"
    "s_and_b32 s45, s45, 0xffff\n"
    "s_mov_b32 s46, 0x480000\n"
    "s_mov_b32 s47, 0x20000\n"
    "s_add_u32 m0, s56, 0\n"
    "s_add_u32 s57, %8, 0\n"
    "buffer_load_dwordx4 v8, s[44:47], s57 offen lds\n"
    "s_add_u32 m0, s56, 4096\n"
    "s_add_u32 s57, %8, 131072\n"
    "buffer_load_dwordx4 v8, s[44:47], s57 offen lds\n"
    "s_add_u32 m0, s56, 8192\n"
    "s_add_u32 s57, %8, 262144\n"
    "buffer_load_dwordx4 v8, s[44:47], s57 offen lds\n"
    "s_add_u32 m0, s56, 12288\n"
    "s_add_u32 s57, %8, 393216\n"
    "buffer_load_dwordx4 v8, s[44:47], s57 offen lds\n"
    "ds_read_b64_tr_b16 a[160:161], v9 offset:16384\n"
    "ds_read_b64_tr_b16 a[162:163], v9 offset:17408\n"
    "ds_read_b64_tr_b16 a[164:165], v10 offset:16384\n"
    "ds_read_b64_tr_b16 a[166:167], v10 offset:17408\n"
    "ds_read_b64_tr_b16 a[168:169], v9 offset:24576\n"
    "ds_read_b64_tr_b16 a[170:171], v9 offset:25600\n"
    "ds_read_b64_tr_b16 a[172:173], v10 offset:24576\n"
    "ds_read_b64_tr_b16 a[174:175], v10 offset:25600\n"
    "s_waitcnt vmcnt(28) lgkmcnt(0)\n"
    "s_barrier\n"
    "s_add_u32 m0, s56, 16384\n"
    "s_add_u32 s57, %8, 524288\n"
    "buffer_load_dwordx4 v8, s[44:47], s57 offen lds\n"
    "s_add_u32 m0, s56, 20480\n"
    "s_add_u32 s57, %8, 655360\n"
    "buffer_load_dwordx4 v8, s[44:47], s57 offen lds\n"
    "s_add_u32 m0, s56, 24576\n"
    "s_add_u32 s57, %8, 786432\n"
    "buffer_load_dwordx4 v8, s[44:47], s57 offen lds\n"
    "s_add_u32 m0, s56, 28672\n"
    "s_add_u32 s57, %8, 917504\n"
    "buffer_load_dwordx4 v8, s[44:47], s57 offen lds\n"
    "ds_read_b64_tr_b16 a[176:177], v9 offset:32768\n"
    "ds_read_b64_tr_b16 a[178:179], v9 offset:33792\n"
    "ds_read_b64_tr_b16 a[180:181], v10 offset:32768\n"
    "ds_read_b64_tr_b16 a[182:183], v10 offset:33792\n"
    "ds_read_b64_tr_b16 a[184:185], v9 offset:40960\n"
    "ds_read_b64_tr_b16 a[186:187], v9 offset:41984\n"
    "ds_read_b64_tr_b16 a[188:189], v10 offset:40960\n"
    "ds_read_b64_tr_b16 a[190:191], v10 offset:41984\n"
    "s_waitcnt vmcnt(28) lgkmcnt(0)\n"
    "s_barrier\n"
    "s_add_u32 m0, s56, 32768\n"
    "s_add_u32 s57, %8, 1048576\n"
    "buffer_load_dwordx4 v8, s[44:47], s57 offen lds\n"
    "s_add_u32 m0, s56, 36864\n"
    "s_add_u32 s57, %8, 1179648\n"
    "buffer_load_dwordx4 v8, s[44:47], s57 offen lds\n"
    "s_add_u32 m0, s56, 40960\n"
    "s_add_u32 s57, %8, 1310720\n"
    "buffer_load_dwordx4 v8, s[44:47], s57 offen lds\n"
    "s_add_u32 m0, s56, 45056\n"
    "s_add_u32 s57, %8, 1441792\n"
    "buffer_load_dwordx4 v8, s[44:47], s57 offen lds\n"
    "ds_read_b64_tr_b16 a[192:193], v9 offset:49152\n"
    "ds_read_b64_tr_b16 a[194:195], v9 offset:50176\n"
    "ds_read_b64_tr_b16 a[196:197], v10 offset:49152\n"
    "ds_read_b64_tr_b16 a[198:199], v10 offset:50176\n"
    "ds_read_b64_tr_b16 a[200:201], v9 offset:57344\n"
    "ds_read_b64_tr_b16 a[202:203], v9 offset:58368\n"
    "ds_read_b64_tr_b16 a[204:205], v10 offset:57344\n"
    "ds_read_b64_tr_b16 a[206:207], v10 offset:58368\n"
    "s_waitcnt vmcnt(28) lgkmcnt(0)\n"
    "s_barrier\n"
    "s_add_u32 m0, s56, 49152\n"
    "s_add_u32 s57, %8, 1572864\n"
    "buffer_load_dwordx4 v8, s[44:47], s57 offen lds\n"
    "s_add_u32 m0, s56, 53248\n"
    "s_add_u32 s57, %8, 1703936\n"
    "buffer_load_dwordx4 v8, s[44:47], s57 offen lds\n"
    "s_add_u32 m0, s56, 57344\n"
    "s_add_u32 s57, %8, 1835008\n"
    "buffer_load_dwordx4 v8, s[44:47], s57 offen lds\n"
    "s_add_u32 m0, s56, 61440\n"
    "s_add_u32 s57, %8, 1966080\n"
    "buffer_load_dwordx4 v8, s[44:47], s57 offen lds\n"
    "ds_read_b64_tr_b16 a[208:209], v14\n"
    "ds_read_b64_tr_b16 a[210:211], v14 offset:1024\n"
    "ds_read_b64_tr_b16 a[212:213], v13\n"
    "ds_read_b64_tr_b16 a[214:215], v13 offset:1024\n"
    "ds_read_b64_tr_b16 a[216:217], v14 offset:8192\n"
    "ds_read_b64_tr_b16 a[218:219], v14 offset:9216\n"
    "ds_read_b64_tr_b16 a[220:221], v13 offset:8192\n"
    "ds_read_b64_tr_b16 a[222:223], v13 offset:9216\n"
    "s_waitcnt vmcnt(28) lgkmcnt(0)\n"
    "s_barrier\n"
    "s_add_u32 m0, s56, 65536\n"
    "s_add_u32 s57, %8, 2097152\n"
    "buffer_load_dwordx4 v8, s[44:47], s57 offen lds\n"
    "s_add_u32 m0, s56, 69632\n"
    "s_add_u32 s57, %8, 2228224\n"
    "buffer_load_dwordx4 v8, s[44:47], s57 offen lds\n"
    "s_add_u32 m0, s56, 73728\n"
    "s_add_u32 s57, %8, 2359296\n"
    "buffer_load_dwordx4 v8, s[44:47], s57 offen lds\n"
    "s_add_u32 m0, s56, 77824\n"
    "s_add_u32 s57, %8, 2490368\n"
    "buffer_load_dwordx4 v8, s[44:47], s57 offen lds\n"
    "ds_read_b64_tr_b16 a[224:225], v14 offset:16384\n"
    "ds_read_b64_tr_b16 a[226:227], v14 offset:17408\n"
    "ds_read_b64_tr_b16 a[228:229], v13 offset:16384\n"
    "ds_read_b64_tr_b16 a[230:231], v13 offset:17408\n"
    "ds_read_b64_tr_b16 a[232:233], v14 offset:24576\n"
    "ds_read_b64_tr_b16 a[234:235], v14 offset:25600\n"
    "ds_read_b64_tr_b16 a[236:237], v13 offset:24576\n"
    "ds_read_b64_tr_b16 a[238:239], v13 offset:25600\n"
    "s_waitcnt vmcnt(28) lgkmcnt(0)\n"
    "s_barrier\n"
    "s_add_u32 m0, s56, 81920\n"
    "s_add_u32 s57, %8, 2621440\n"
    "buffer_load_dwordx4 v8, s[44:47], s57 offen lds\n"
    "s_add_u32 m0, s56, 86016\n"
    "s_add_u32 s57, %8, 2752512\n"
    "buffer_load_dwordx4 v8, s[44:47], s57 offen lds\n"
    "s_add_u32 m0, s56, 90112\n"
    "s_add_u32 s57, %8, 2883584\n"
    "buffer_load_dwordx4 v8, s[44:47], s57 offen lds\n"
    "s_add_u32 m0, s56, 94208\n"
    "s_add_u32 s57, %8, 3014656\n"
    "buffer_load_dwordx4 v8, s[44:47], s57 offen lds\n"
    "ds_read_b64_tr_b16 a[240:241], v14 offset:32768\n"
    "ds_read_b64_tr_b16 a[242:243], v14 offset:33792\n"
    "ds_read_b64_tr_b16 a[244:245], v13 offset:32768\n"
    "ds_read_b64_tr_b16 a[246:247], v13 offset:33792\n"
    "ds_read_b64_tr_b16 a[248:249], v14 offset:40960\n"
    "ds_read_b64_tr_b16 a[250:251], v14 offset:41984\n"
    "ds_read_b64_tr_b16 a[252:253], v13 offset:40960\n"
    "ds_read_b64_tr_b16 a[254:255], v13 offset:41984\n"
    "s_waitcnt vmcnt(28) lgkmcnt(0)\n"
    "s_barrier\n"
    "s_add_u32 m0, s56, 98304\n"
    "s_add_u32 s57, %8, 3145728\n"
    "buffer_load_dwordx4 v8, s[44:47], s57 offen lds\n"
    "s_add_u32 m0, s56, 102400\n"
    "s_add_u32 s57, %8, 3276800\n"
    "buffer_load_dwordx4 v8, s[44:47], s57 offen lds\n"
    "s_add_u32 m0, s56, 106496\n"
    "s_add_u32 s57, %8, 3407872\n"
    "buffer_load_dwordx4 v8, s[44:47], s57 offen lds\n"
    "s_add_u32 m0, s56, 110592\n"
    "s_add_u32 s57, %8, 3538944\n"
    "buffer_load_dwordx4 v8, s[44:47], s57 offen lds\n"
    "ds_read_b64_tr_b16 v[80:81], v14 offset:49152\n"
    "ds_read_b64_tr_b16 v[82:83], v14 offset:50176\n"
    "ds_read_b64_tr_b16 v[84:85], v13 offset:49152\n"
    "ds_read_b64_tr_b16 v[86:87], v13 offset:50176\n"
    "ds_read_b64_tr_b16 v[88:89], v14 offset:57344\n"
    "ds_read_b64_tr_b16 v[90:91], v14 offset:58368\n"
    "ds_read_b64_tr_b16 v[92:93], v13 offset:57344\n"
    "ds_read_b64_tr_b16 v[94:95], v13 offset:58368\n"
    "s_waitcnt vmcnt(28) lgkmcnt(0)\n"
    "s_barrier\n"
    "s_add_u32 m0, s56, 114688\n"
    "s_add_u32 s57, %8, 3670016\n"
    "buffer_load_dwordx4 v8, s[44:47], s57 offen lds\n"
    "s_add_u32 m0, s56, 118784\n"
    "s_add_u32 s57, %8, 3801088\n"
    "buffer_load_dwordx4 v8, s[44:47], s57 offen lds\n"
    "s_add_u32 m0, s56, 122880\n"
    "s_add_u32 s57, %8, 3932160\n"
    "buffer_load_dwordx4 v8, s[44:47], s57 offen lds\n"
    "s_add_u32 m0, s56, 126976\n"
    "s_add_u32 s57, %8, 4063232\n"
    "buffer_load_dwordx4 v8, s[44:47], s57 offen lds\n"
    "ds_read_b64_tr_b16 v[96:97], v12\n"
    "ds_read_b64_tr_b16 v[98:99], v12 offset:1024\n"
    "ds_read_b64_tr_b16 v[100:101], v11\n"
    "ds_read_b64_tr_b16 v[102:103], v11 offset:1024\n"
    "ds_read_b64_tr_b16 v[104:105], v12 offset:8192\n"
    "ds_read_b64_tr_b16 v[106:107], v12 offset:9216\n"
    "ds_read_b64_tr_b16 v[108:109], v11 offset:8192\n"
    "ds_read_b64_tr_b16 v[110:111], v11 offset:9216\n"
    "s_waitcnt vmcnt(28) lgkmcnt(0)\n"
    "s_barrier\n"
    "s_add_u32 m0, s56, 131072\n"
    "s_add_u32 s57, %8, 4194304\n"
    "buffer_load_dwordx4 v8, s[44:47], s57 offen lds\n"
    "s_add_u32 m0, s56, 135168\n"
    "s_add_u32 s57, %8, 4325376\n"
    "buffer_load_dwordx4 v8, s[44:47], s57 offen lds\n"
    "s_add_u32 m0, s56, 139264\n"
    "s_add_u32 s57, %8, 4456448\n"
    "buffer_load_dwordx4 v8, s[44:47], s57 offen lds\n"
    "s_add_u32 m0, s56, 143360\n"
    "s_add_u32 s57, %8, 4587520\n"
    "buffer_load_dwordx4 v8, s[44:47], s57 offen lds\n"
    "ds_read_b64_tr_b16 v[112:113], v9\n"
    "ds_read_b64_tr_b16 v[114:115], v9 offset:1024\n"
    "ds_read_b64_tr_b16 v[116:117], v10\n"
    "ds_read_b64_tr_b16 v[118:119], v10 offset:1024\n"
    "ds_read_b64_tr_b16 v[120:121], v9 offset:8192\n"
    "ds_read_b64_tr_b16 v[122:123], v9 offset:9216\n"
    "ds_read_b64_tr_b16 v[124:125], v10 offset:8192\n"
    "ds_read_b64_tr_b16 v[126:127], v10 offset:9216\n"
    "s_waitcnt vmcnt(28) lgkmcnt(0)\n"
    "s_barrier\n"
    "ds_read_b64_tr_b16 v[128:129], v9 offset:16384\n"
    "ds_read_b64_tr_b16 v[130:131], v9 offset:17408\n"
    "ds_read_b64_tr_b16 v[132:133], v10 offset:16384\n"
    "ds_read_b64_tr_b16 v[134:135], v10 offset:17408\n"
    "ds_read_b64_tr_b16 v[136:137], v9 offset:24576\n"
    "ds_read_b64_tr_b16 v[138:139], v9 offset:25600\n"
    "ds_read_b64_tr_b16 v[140:141], v10 offset:24576\n"
    "ds_read_b64_tr_b16 v[142:143], v10 offset:25600\n"
    "s_waitcnt vmcnt(24) lgkmcnt(0)\n"
    "s_barrier\n"
    "ds_read_b64_tr_b16 v[144:145], v9 offset:32768\n"
    "ds_read_b64_tr_b16 v[146:147], v9 offset:33792\n"
    "ds_read_b64_tr_b16 v[148:149], v10 offset:32768\n"
    "ds_read_b64_tr_b16 v[150:151], v10 offset:33792\n"
    "ds_read_b64_tr_b16 v[152:153], v9 offset:40960\n"
    "ds_read_b64_tr_b16 v[154:155], v9 offset:41984\n"
    "ds_read_b64_tr_b16 v[156:157], v10 offset:40960\n"
    "ds_read_b64_tr_b16 v[158:159], v10 offset:41984\n"
    "s_waitcnt vmcnt(20) lgkmcnt(0)\n"
    "s_barrier\n"
    "ds_read_b64_tr_b16 v[160:161], v9 offset:49152\n"
    "ds_read_b64_tr_b16 v[162:163], v9 offset:50176\n"
    "ds_read_b64_tr_b16 v[164:165], v10 offset:49152\n"
    "ds_read_b64_tr_b16 v[166:167], v10 offset:50176\n"
    "ds_read_b64_tr_b16 v[168:169], v9 offset:57344\n"
    "ds_read_b64_tr_b16 v[170:171], v9 offset:58368\n"
    "ds_read_b64_tr_b16 v[172:173], v10 offset:57344\n"
    "ds_read_b64_tr_b16 v[174:175], v10 offset:58368\n"
    "s_waitcnt vmcnt(16) lgkmcnt(0)\n"
    "s_barrier\n"
    "ds_read_b64_tr_b16 v[176:177], v14\n"
    "ds_read_b64_tr_b16 v[178:179], v14 offset:1024\n"
    "ds_read_b64_tr_b16 v[180:181], v13\n"
    "ds_read_b64_tr_b16 v[182:183], v13 offset:1024\n"
    "ds_read_b64_tr_b16 v[184:185], v14 offset:8192\n"
    "ds_read_b64_tr_b16 v[186:187], v14 offset:9216\n"
    "ds_read_b64_tr_b16 v[188:189], v13 offset:8192\n"
    "ds_read_b64_tr_b16 v[190:191], v13 offset:9216\n"
    "s_waitcnt vmcnt(12) lgkmcnt(0)\n"
    "s_barrier\n"
    "ds_read_b64_tr_b16 v[192:193], v14 offset:16384\n"
    "ds_read_b64_tr_b16 v[194:195], v14 offset:17408\n"
    "ds_read_b64_tr_b16 v[196:197], v13 offset:16384\n"
    "ds_read_b64_tr_b16 v[198:199], v13 offset:17408\n"
    "ds_read_b64_tr_b16 v[200:201], v14 offset:24576\n"
    "ds_read_b64_tr_b16 v[202:203], v14 offset:25600\n"
    "ds_read_b64_tr_b16 v[204:205], v13 offset:24576\n"
    "ds_read_b64_tr_b16 v[206:207], v13 offset:25600\n"
    "s_waitcnt vmcnt(8) lgkmcnt(0)\n"
    "s_barrier\n"
    "ds_read_b64_tr_b16 v[208:209], v14 offset:32768\n"
    "ds_read_b64_tr_b16 v[210:211], v14 offset:33792\n"
    "ds_read_b64_tr_b16 v[212:213], v13 offset:32768\n"
    "ds_read_b64_tr_b16 v[214:215], v13 offset:33792\n"
    "ds_read_b64_tr_b16 v[216:217], v14 offset:40960\n"
    "ds_read_b64_tr_b16 v[218:219], v14 offset:41984\n"
    "ds_read_b64_tr_b16 v[220:221], v13 offset:40960\n"
    "ds_read_b64_tr_b16 v[222:223], v13 offset:41984\n"
    "s_waitcnt vmcnt(4) lgkmcnt(0)\n"
    "s_barrier\n"
    "ds_read_b64_tr_b16 v[224:225], v14 offset:49152\n"
    "ds_read_b64_tr_b16 v[226:227], v14 offset:50176\n"
    "ds_read_b64_tr_b16 v[228:229], v13 offset:49152\n"
    "ds_read_b64_tr_b16 v[230:231], v13 offset:50176\n"
    "ds_read_b64_tr_b16 v[232:233], v14 offset:57344\n"
    "ds_read_b64_tr_b16 v[234:235], v14 offset:58368\n"
    "ds_read_b64_tr_b16 v[236:237], v13 offset:57344\n"
    "ds_read_b64_tr_b16 v[238:239], v13 offset:58368\n"
    "s_waitcnt vmcnt(0) lgkmcnt(0)\n"
    "s_barrier\n"
    "ds_read_b64_tr_b16 v[240:241], v12\n"
    "ds_read_b64_tr_b16 v[242:243], v12 offset:1024\n"
    "ds_read_b64_tr_b16 v[244:245], v11\n"
    "ds_read_b64_tr_b16 v[246:247], v11 offset:1024\n"
    "ds_read_b64_tr_b16 v[248:249], v12 offset:8192\n"
    "ds_read_b64_tr_b16 v[250:251], v12 offset:9216\n"
    "ds_read_b64_tr_b16 v[252:253], v11 offset:8192\n"
    "ds_read_b64_tr_b16 v[254:255], v11 offset:9216\n"
    "s_waitcnt lgkmcnt(0)\n"
    "s_barrier\n"
    "s_mov_b32 s53, 0\n"
    "v_mov_b32 v6, 64\n"
    ".Lhp1_%=:\n"
    "global_load_dword v5, v6, %1 sc1\n"
    "s_waitcnt vmcnt(0)\n"
    "v_readfirstlane_b32 s51, v5\n"
    "s_cmp_ge_u32 s51, 4\n"
    "s_cbranch_scc1 .Lhp1_x%=\n"
    "s_add_u32 s53, s53, 1\n"
    "s_sleep 2\n"
    "s_cmp_lt_u32 s53, s54\n"
    "s_cbranch_scc1 .Lhp1_%=\n"
    "s_mov_b32 s54, 0\n"
    "v_mov_b32 v6, 0\n"
    "v_mov_b32 v5, 1\n"
    "global_store_dword v6, v5, %1 sc1\n"
    ".Lhp1_x%=:\n"
    "s_xor_b32 s55, s51, %9\n"
    "s_mov_b32 s58, %6\n"
    "s_mov_b32 s48, -1\n"
    "s_branch .Lhwait_%=\n"
    ".Lhtop_%=:\n"
    "s_waitcnt lgkmcnt(0)\n"
    "s_barrier\n"
    "ds_read_b128 v[40:43], v0\n"
    "ds_read_b128 v[44:47], v0 offset:18432\n"
    "ds_read_b128 v[48:51], v1 offset:1024\n"
    "ds_read_b128 v[52:55], v1 offset:19456\n"
    "s_add_u32 s52, s48, 1\n"
    "s_and_b32 s52, s52, 63\n"
    "s_mul_i32 s52, s52, 64\n"
    "s_add_u32 s52, s52, %4\n"
    "v_mov_b32 v6, s52\n"
    "global_load_dword v5, v6, %1 sc1\n"
    "s_add_u32 s51, s48, 0\n"
    "s_lshr_b32 s52, s51, 2\n"
    "s_xor_b32 s52, s52, s55\n"
    "s_and_b32 s52, s52, 1\n"
    "s_lshl_b32 s52, s52, 13\n"
    "s_lshl_b32 s51, s51, 19\n"
    "s_add_u32 s51, s51, s52\n"
    "s_add_u32 s62, s51, %7\n"
    "s_add_u32 s63, s62, 0x1000\n"
    "s_add_u32 s64, s62, 0x2000000\n"
    "s_add_u32 s65, s62, 0x2001000\n"
    "s_add_u32 s66, s62, 0x4000000\n"
    "s_add_u32 s67, s62, 0x4001000\n"
    "s_waitcnt lgkmcnt(3)\n"
    "v_mfma_f32_16x16x32_bf16 v[8:11], a[0:3], v[40:43], 0\n"
    "ds_read_b128 v[56:59], v0 offset:2048\n"
    "ds_read_b128 v[60:63], v0 offset:20480\n"
    "s_waitcnt lgkmcnt(4)\n"
    "v_mfma_f32_16x16x32_bf16 v[12:15], a[0:3], v[44:47], 0\n"
    "v_mfma_f32_16x16x32_bf16 v[16:19], a[4:7], v[40:43], 0\n"
    "v_mfma_f32_16x16x32_bf16 v[20:23], a[4:7], v[44:47], 0\n"
    "s_waitcnt lgkmcnt(3)\n"
    "v_mfma_f32_16x16x32_bf16 v[8:11], a[8:11], v[48:51], v[8:11]\n"
    "ds_read_b128 v[40:43], v1 offset:3072\n"
    "ds_read_b128 v[44:47], v1 offset:21504\n"
    "s_waitcnt lgkmcnt(4)\n"
    "v_mfma_f32_16x16x32_bf16 v[12:15], a[8:11], v[52:55], v[12:15]\n"
    "v_mfma_f32_16x16x32_bf16 v[16:19], a[12:15], v[48:51], v[16:19]\n"
    "v_mfma_f32_16x16x32_bf16 v[20:23], a[12:15], v[52:55], v[20:23]\n"
    "s_waitcnt lgkmcnt(3)\n"
    "v_mfma_f32_16x16x32_bf16 v[8:11], a[16:19], v[56:59], v[8:11]\n"
    "ds_read_b128 v[48:51], v0 offset:4096\n"
    "ds_read_b128 v[52:55], v0 offset:22528\n"
    "s_waitcnt lgkmcnt(4)\n"
    "v_mfma_f32_16x16x32_bf16 v[12:15], a[16:19], v[60:63], v[12:15]\n"
    "v_mfma_f32_16x16x32_bf16 v[16:19], a[20:23], v[56:59], v[16:19]\n"
    "v_mfma_f32_16x16x32_bf16 v[20:23], a[20:23], v[60:63], v[20:23]\n"
    "s_waitcnt lgkmcnt(3)\n"
    "v_mfma_f32_16x16x32_bf16 v[8:11], a[24:27], v[40:43], v[8:11]\n"
    "ds_read_b128 v[56:59], v1 offset:5120\n"
    "ds_read_b128 v[60:63], v1 offset:23552\n"
    "s_waitcnt lgkmcnt(4)\n"
    "v_mfma_f32_16x16x32_bf16 v[12:15], a[24:27], v[44:47], v[12:15]\n"
    "v_mfma_f32_16x16x32_bf16 v[16:19], a[28:31], v[40:43], v[16:19]\n"
    "v_mfma_f32_16x16x32_bf16 v[20:23], a[28:31], v[44:47], v[20:23]\n"
    "s_waitcnt lgkmcnt(3)\n"
    "v_mfma_f32_16x16x32_bf16 v[8:11], a[32:35], v[48:51], v[8:11]\n"
    "ds_read_b128 v[40:43], v0 offset:6144\n"
    "ds_read_b128 v[44:47], v0 offset:24576\n"
    "s_waitcnt lgkmcnt(4)\n"
    "v_mfma_f32_16x16x32_bf16 v[12:15], a[32:35], v[52:55], v[12:15]\n"
    "v_mfma_f32_16x16x32_bf16 v[16:19], a[36:39], v[48:51], v[16:19]\n"
    "v_mfma_f32_16x16x32_bf16 v[20:23], a[36:39], v[52:55], v[20:23]\n"
    "s_waitcnt lgkmcnt(3)\n"
    "v_mfma_f32_16x16x32_bf16 v[8:11], a[40:43], v[56:59], v[8:11]\n"
    "ds_read_b128 v[48:51], v1 offset:7168\n"
    "ds_read_b128 v[52:55], v1 offset:25600\n"
    "s_waitcnt lgkmcnt(4)\n"
    "v_mfma_f32_16x16x32_bf16 v[12:15], a[40:43], v[60:63], v[12:15]\n"
    "v_mfma_f32_16x16x32_bf16 v[16:19], a[44:47], v[56:59], v[16:19]\n"
    "v_mfma_f32_16x16x32_bf16 v[20:23], a[44:47], v[60:63], v[20:23]\n"
    "s_waitcnt lgkmcnt(3)\n"
    "v_mfma_f32_16x16x32_bf16 v[8:11], a[48:51], v[40:43], v[8:11]\n"
    "ds_read_b128 v[56:59], v0 offset:8192\n"
    "ds_read_b128 v[60:63], v0 offset:26624\n"
    "s_waitcnt lgkmcnt(4)\n"
    "v_mfma_f32_16x16x32_bf16 v[12:15], a[48:51], v[44:47], v[12:15]\n"
    "v_mfma_f32_16x16x32_bf16 v[16:19], a[52:55], v[40:43], v[16:19]\n"
    "v_mfma_f32_16x16x32_bf16 v[20:23], a[52:55], v[44:47], v[20:23]\n"
    "s_waitcnt lgkmcnt(3)\n"
    "v_mfma_f32_16x16x32_bf16 v[8:11], a[56:59], v[48:51], v[8:11]\n"
    "ds_read_b128 v[40:43], v1 offset:9216\n"
    "ds_read_b128 v[44:47], v1 offset:27648\n"
    "s_waitcnt lgkmcnt(4)\n"
    "v_mfma_f32_16x16x32_bf16 v[12:15], a[56:59], v[52:55], v[12:15]\n"
    "v_mfma_f32_16x16x32_bf16 v[16:19], a[60:63], v[48:51], v[16:19]\n"
    "v_mfma_f32_16x16x32_bf16 v[20:23], a[60:63], v[52:55], v[20:23]\n"
    "s_waitcnt lgkmcnt(3)\n"
    "v_mfma_f32_16x16x32_bf16 v[8:11], a[64:67], v[56:59], v[8:11]\n"
    "ds_read_b128 v[48:51], v0 offset:10240\n"
    "ds_read_b128 v[52:55], v0 offset:28672\n"
    "s_waitcnt lgkmcnt(4)\n"
    "v_mfma_f32_16x16x32_bf16 v[12:15], a[64:67], v[60:63], v[12:15]\n"
    "v_mfma_f32_16x16x32_bf16 v[16:19], a[68:71], v[56:59], v[16:19]\n"
    "v_mfma_f32_16x16x32_bf16 v[20:23], a[68:71], v[60:63], v[20:23]\n"
    "s_waitcnt lgkmcnt(3)\n"
    "v_mfma_f32_16x16x32_bf16 v[8:11], a[72:75], v[40:43], v[8:11]\n"
    "ds_read_b128 v[56:59], v1 offset:11264\n"
    "ds_read_b128 v[60:63], v1 offset:29696\n"
    "s_waitcnt lgkmcnt(4)\n"
    "v_mfma_f32_16x16x32_bf16 v[12:15], a[72:75], v[44:47], v[12:15]\n"
    "v_mfma_f32_16x16x32_bf16 v[16:19], a[76:79], v[40:43], v[16:19]\n"
    "v_mfma_f32_16x16x32_bf16 v[20:23], a[76:79], v[44:47], v[20:23]\n"
    "s_waitcnt lgkmcnt(3)\n"
    "v_mfma_f32_16x16x32_bf16 v[8:11], a[80:83], v[48:51], v[8:11]\n"
    "ds_read_b128 v[40:43], v0 offset:12288\n"
    "ds_read_b128 v[44:47], v0 offset:30720\n"
    "s_waitcnt lgkmcnt(4)\n"
    "v_mfma_f32_16x16x32_bf16 v[12:15], a[80:83], v[52:55], v[12:15]\n"
    "v_mfma_f32_16x16x32_bf16 v[16:19], a[84:87], v[48:51], v[16:19]\n"
    "v_mfma_f32_16x16x32_bf16 v[20:23], a[84:87], v[52:55], v[20:23]\n"
    "s_waitcnt lgkmcnt(3)\n"
    "v_mfma_f32_16x16x32_bf16 v[8:11], a[88:91], v[56:59], v[8:11]\n"
    "ds_read_b128 v[48:51], v1 offset:13312\n"
    "ds_read_b128 v[52:55], v1 offset:31744\n"
    "s_waitcnt lgkmcnt(4)\n"
    "v_mfma_f32_16x16x32_bf16 v[12:15], a[88:91], v[60:63], v[12:15]\n"
    "v_mfma_f32_16x16x32_bf16 v[16:19], a[92:95], v[56:59], v[16:19]\n"
    "v_mfma_f32_16x16x32_bf16 v[20:23], a[92:95], v[60:63], v[20:23]\n"
    "s_waitcnt lgkmcnt(3)\n"
    "v_mfma_f32_16x16x32_bf16 v[8:11], a[96:99], v[40:43], v[8:11]\n"
    "ds_read_b128 v[56:59], v0 offset:14336\n"
    "ds_read_b128 v[60:63], v0 offset:32768\n"
    "s_waitcnt lgkmcnt(4)\n"
    "v_mfma_f32_16x16x32_bf16 v[12:15], a[96:99], v[44:47], v[12:15]\n"
    "v_mfma_f32_16x16x32_bf16 v[16:19], a[100:103], v[40:43], v[16:19]\n"
    "v_mfma_f32_16x16x32_bf16 v[20:23], a[100:103], v[44:47], v[20:23]\n"
    "s_waitcnt lgkmcnt(3)\n"
    "v_mfma_f32_16x16x32_bf16 v[8:11], a[104:107], v[48:51], v[8:11]\n"
    "ds_read_b128 v[40:43], v1 offset:15360\n"
    "ds_read_b128 v[44:47], v1 offset:33792\n"
    "s_waitcnt lgkmcnt(4)\n"
    "v_mfma_f32_16x16x32_bf16 v[12:15], a[104:107], v[52:55], v[12:15]\n"
    "v_mfma_f32_16x16x32_bf16 v[16:19], a[108:111], v[48:51], v[16:19]\n"
    "v_mfma_f32_16x16x32_bf16 v[20:23], a[108:111], v[52:55], v[20:23]\n"
    "s_waitcnt lgkmcnt(3)\n"
    "v_mfma_f32_16x16x32_bf16 v[8:11], a[112:115], v[56:59], v[8:11]\n"
    "ds_read_b128 v[48:51], v0 offset:16384\n"
    "ds_read_b128 v[52:55], v0 offset:34816\n"
    "s_waitcnt lgkmcnt(4)\n"
    "v_mfma_f32_16x16x32_bf16 v[12:15], a[112:115], v[60:63], v[12:15]\n"
    "s_lshl_b32 s51, %2, 2\n"
    "s_add_u32 s51, s51, 36864\n"
    "v_mov_b32 v6, s51\n"
    "s_waitcnt vmcnt(0)\n"
    "ds_write_b32 v6, v5\n"
    "v_mfma_f32_16x16x32_bf16 v[16:19], a[116:119], v[56:59], v[16:19]\n"
    "v_mfma_f32_16x16x32_bf16 v[20:23], a[116:119], v[60:63], v[20:23]\n"
    "s_waitcnt lgkmcnt(4)\n"
    "v_mfma_f32_16x16x32_bf16 v[8:11], a[120:123], v[40:43], v[8:11]\n"
    "ds_read_b128 v[56:59], v1 offset:17408\n"
    "ds_read_b128 v[60:63], v1 offset:35840\n"
    "s_waitcnt lgkmcnt(5)\n"
    "v_mfma_f32_16x16x32_bf16 v[12:15], a[120:123], v[44:47], v[12:15]\n"
    "v_mfma_f32_16x16x32_bf16 v[16:19], a[124:127], v[40:43], v[16:19]\n"
    "s_waitcnt lgkmcnt(2)\n"
    "s_barrier\n"
    "s_cmp_eq_u32 s50, 0\n"
    "s_cbranch_scc1 .Lhc2_%=\n"
    "s_sub_u32 s51, s48, 1\n"
    "s_mul_i32 s51, s51, 4\n"
    "s_add_u32 s51, s51, %5\n"
    "v_mov_b32 v6, s51\n"
    "v_mov_b32 v5, 1\n"
    "s_mov_b64 exec, 1\n"
    "global_store_dword v6, v5, %1 sc1\n"
    "s_mov_b64 exec, -1\n"
    ".Lhc2_%=:\n"
    "v_mov_b32 v6, 0x9000\n"
    "ds_read_b32 v5, v6\n"
    "v_mfma_f32_16x16x32_bf16 v[20:23], a[124:127], v[44:47], v[20:23]\n"
    "v_mfma_f32_16x16x32_bf16 v[8:11], a[128:131], v[48:51], v[8:11]\n"
    "ds_read_b128 v[40:43], v0\n"
    "ds_read_b128 v[44:47], v0 offset:18432\n"
    "v_mfma_f32_16x16x32_bf16 v[12:15], a[128:131], v[52:55], v[12:15]\n"
    "v_mfma_f32_16x16x32_bf16 v[16:19], a[132:135], v[48:51], v[16:19]\n"
    "v_mfma_f32_16x16x32_bf16 v[20:23], a[132:135], v[52:55], v[20:23]\n"
    "s_waitcnt lgkmcnt(4)\n"
    "v_mfma_f32_16x16x32_bf16 v[8:11], a[136:139], v[56:59], v[8:11]\n"
    "ds_read_b128 v[48:51], v1 offset:1024\n"
    "ds_read_b128 v[52:55], v1 offset:19456\n"
    "s_waitcnt lgkmcnt(5)\n"
    "v_mfma_f32_16x16x32_bf16 v[12:15], a[136:139], v[60:63], v[12:15]\n"
    "v_mfma_f32_16x16x32_bf16 v[16:19], a[140:143], v[56:59], v[16:19]\n"
    "s_waitcnt lgkmcnt(4)\n"
    "v_readfirstlane_b32 s51, v5\n"
    "s_cmp_ge_u32 s51, 2\n"
    "s_cselect_b32 s49, 1, 0\n"
    "s_cmp_eq_u32 s48, 63\n"
    "s_cselect_b32 s49, 0, s49\n"
    "v_mfma_f32_16x16x32_bf16 v[20:23], a[140:143], v[60:63], v[20:23]\n"
    "s_waitcnt lgkmcnt(3)\n"
    "v_mfma_f32_16x16x32_bf16 v[24:27], a[144:147], v[40:43], 0\n"
    "ds_read_b128 v[56:59], v0 offset:2048\n"
    "ds_read_b128 v[60:63], v0 offset:20480\n"
    "s_waitcnt lgkmcnt(4)\n"
    "v_mfma_f32_16x16x32_bf16 v[28:31], a[144:147], v[44:47], 0\n"
    "v_mfma_f32_16x16x32_bf16 v[32:35], a[148:151], v[40:43], 0\n"
    "v_mfma_f32_16x16x32_bf16 v[36:39], a[148:151], v[44:47], 0\n"
    "v_cvt_pk_bf16_f32 v8, v8, v16\n"
    "v_cvt_pk_bf16_f32 v9, v9, v17\n"
    "s_waitcnt lgkmcnt(3)\n"
    "v_mfma_f32_16x16x32_bf16 v[24:27], a[152:155], v[48:51], v[24:27]\n"
    "ds_read_b128 v[40:43], v1 offset:3072\n"
    "ds_read_b128 v[44:47], v1 offset:21504\n"
    "s_waitcnt lgkmcnt(4)\n"
    "v_mfma_f32_16x16x32_bf16 v[28:31], a[152:155], v[52:55], v[28:31]\n"
    "v_cvt_pk_bf16_f32 v10, v10, v18\n"
    "v_cvt_pk_bf16_f32 v11, v11, v19\n"
    "v_mfma_f32_16x16x32_bf16 v[32:35], a[156:159], v[48:51], v[32:35]\n"
    "buffer_store_dwordx4 v[8:11], v4, s[40:43], s62 offen sc1\n"
    "v_cvt_pk_bf16_f32 v12, v12, v20\n"
    "v_mfma_f32_16x16x32_bf16 v[36:39], a[156:159], v[52:55], v[36:39]\n"
    "v_cvt_pk_bf16_f32 v13, v13, v21\n"
    "v_cvt_pk_bf16_f32 v14, v14, v22\n"
    "s_waitcnt lgkmcnt(3)\n"
    "v_mfma_f32_16x16x32_bf16 v[24:27], a[160:163], v[56:59], v[24:27]\n"
    "ds_read_b128 v[48:51], v0 offset:4096\n"
    "ds_read_b128 v[52:55], v0 offset:22528\n"
    "s_waitcnt lgkmcnt(4)\n"
    "v_mfma_f32_16x16x32_bf16 v[28:31], a[160:163], v[60:63], v[28:31]\n"
    "v_cvt_pk_bf16_f32 v15, v15, v23\n"
    "buffer_store_dwordx4 v[12:15], v4, s[40:43], s63 offen sc1\n"
    "v_mfma_f32_16x16x32_bf16 v[32:35], a[164:167], v[56:59], v[32:35]\n"
    "v_mfma_f32_16x16x32_bf16 v[36:39], a[164:167], v[60:63], v[36:39]\n"
    "s_cmp_eq_u32 s49, 0\n"
    "s_cbranch_scc1 .Lhc3_%=\n"
    "buffer_load_dwordx4 v[8:11], v3, s[40:43], s58 offen sc1\n"
    "buffer_load_dwordx4 v[12:15], v3, s[40:43], s58 offen offset:128 sc1\n"
    "buffer_load_dwordx4 v[16:19], v3, s[40:43], s58 offen offset:256 sc1\n"
    "buffer_load_dwordx4 v[20:23], v3, s[40:43], s58 offen offset:384 sc1\n"
    "buffer_load_dwordx4 v[64:67], v3, s[40:43], s58 offen offset:512 sc1\n"
    "buffer_load_dwordx4 v[68:71], v3, s[40:43], s58 offen offset:640 sc1\n"
    "buffer_load_dwordx4 v[72:75], v3, s[40:43], s58 offen offset:768 sc1\n"
    "buffer_load_dwordx4 v[76:79], v3, s[40:43], s58 offen offset:896 sc1\n"
    ".Lhc3_%=:\n"
    "s_waitcnt lgkmcnt(3)\n"
    "v_mfma_f32_16x16x32_bf16 v[24:27], a[168:171], v[40:43], v[24:27]\n"
    "ds_read_b128 v[56:59], v1 offset:5120\n"
    "ds_read_b128 v[60:63], v1 offset:23552\n"
    "s_waitcnt lgkmcnt(4)\n"
    "v_mfma_f32_16x16x32_bf16 v[28:31], a[168:171], v[44:47], v[28:31]\n"
    "v_mfma_f32_16x16x32_bf16 v[32:35], a[172:175], v[40:43], v[32:35]\n"
    "v_mfma_f32_16x16x32_bf16 v[36:39], a[172:175], v[44:47], v[36:39]\n"
    "s_waitcnt lgkmcnt(3)\n"
    "v_mfma_f32_16x16x32_bf16 v[24:27], a[176:179], v[48:51], v[24:27]\n"
    "ds_read_b128 v[40:43], v0 offset:6144\n"
    "ds_read_b128 v[44:47], v0 offset:24576\n"
    "s_waitcnt lgkmcnt(4)\n"
    "v_mfma_f32_16x16x32_bf16 v[28:31], a[176:179], v[52:55], v[28:31]\n"
    "v_mfma_f32_16x16x32_bf16 v[32:35], a[180:183], v[48:51], v[32:35]\n"
    "v_mfma_f32_16x16x32_bf16 v[36:39], a[180:183], v[52:55], v[36:39]\n"
    "s_waitcnt lgkmcnt(3)\n"
    "v_mfma_f32_16x16x32_bf16 v[24:27], a[184:187], v[56:59], v[24:27]\n"
    "ds_read_b128 v[48:51], v1 offset:7168\n"
    "ds_read_b128 v[52:55], v1 offset:25600\n"
    "s_waitcnt lgkmcnt(4)\n"
    "v_mfma_f32_16x16x32_bf16 v[28:31], a[184:187], v[60:63], v[28:31]\n"
    "v_mfma_f32_16x16x32_bf16 v[32:35], a[188:191], v[56:59], v[32:35]\n"
    "v_mfma_f32_16x16x32_bf16 v[36:39], a[188:191], v[60:63], v[36:39]\n"
    "s_waitcnt lgkmcnt(3)\n"
    "v_mfma_f32_16x16x32_bf16 v[24:27], a[192:195], v[40:43], v[24:27]\n"
    "ds_read_b128 v[56:59], v0 offset:8192\n"
    "ds_read_b128 v[60:63], v0 offset:26624\n"
    "s_waitcnt lgkmcnt(4)\n"
    "v_mfma_f32_16x16x32_bf16 v[28:31], a[192:195], v[44:47], v[28:31]\n"
    "v_mfma_f32_16x16x32_bf16 v[32:35], a[196:199], v[40:43], v[32:35]\n"
    "v_mfma_f32_16x16x32_bf16 v[36:39], a[196:199], v[44:47], v[36:39]\n"
    "s_waitcnt lgkmcnt(3)\n"
    "v_mfma_f32_16x16x32_bf16 v[24:27], a[200:203], v[48:51], v[24:27]\n"
    "ds_read_b128 v[40:43], v1 offset:9216\n"
    "ds_read_b128 v[44:47], v1 offset:27648\n"
    "s_waitcnt lgkmcnt(4)\n"
    "v_mfma_f32_16x16x32_bf16 v[28:31], a[200:203], v[52:55], v[28:31]\n"
    "v_mfma_f32_16x16x32_bf16 v[32:35], a[204:207], v[48:51], v[32:35]\n"
    "v_mfma_f32_16x16x32_bf16 v[36:39], a[204:207], v[52:55], v[36:39]\n"
    "s_waitcnt lgkmcnt(3)\n"
    "v_mfma_f32_16x16x32_bf16 v[24:27], a[208:211], v[56:59], v[24:27]\n"
    "ds_read_b128 v[48:51], v0 offset:10240\n"
    "ds_read_b128 v[52:55], v0 offset:28672\n"
    "s_waitcnt lgkmcnt(4)\n"
    "v_mfma_f32_16x16x32_bf16 v[28:31], a[208:211], v[60:63], v[28:31]\n"
    "v_mfma_f32_16x16x32_bf16 v[32:35], a[212:215], v[56:59], v[32:35]\n"
    "v_mfma_f32_16x16x32_bf16 v[36:39], a[212:215], v[60:63], v[36:39]\n"
    "s_waitcnt lgkmcnt(3)\n"
    "v_mfma_f32_16x16x32_bf16 v[24:27], a[216:219], v[40:43], v[24:27]\n"
    "ds_read_b128 v[56:59], v1 offset:11264\n"
    "ds_read_b128 v[60:63], v1 offset:29696\n"
    "s_waitcnt lgkmcnt(4)\n"
    "v_mfma_f32_16x16x32_bf16 v[28:31], a[216:219], v[44:47], v[28:31]\n"
    "v_mfma_f32_16x16x32_bf16 v[32:35], a[220:223], v[40:43], v[32:35]\n"
    "v_mfma_f32_16x16x32_bf16 v[36:39], a[220:223], v[44:47], v[36:39]\n"
    "s_waitcnt lgkmcnt(3)\n"
    "v_mfma_f32_16x16x32_bf16 v[24:27], a[224:227], v[48:51], v[24:27]\n"
    "ds_read_b128 v[40:43], v0 offset:12288\n"
    "ds_read_b128 v[44:47], v0 offset:30720\n"
    "s_waitcnt lgkmcnt(4)\n"
    "v_mfma_f32_16x16x32_bf16 v[28:31], a[224:227], v[52:55], v[28:31]\n"
    "v_mfma_f32_16x16x32_bf16 v[32:35], a[228:231], v[48:51], v[32:35]\n"
    "v_mfma_f32_16x16x32_bf16 v[36:39], a[228:231], v[52:55], v[36:39]\n"
    "s_waitcnt lgkmcnt(3)\n"
    "v_mfma_f32_16x16x32_bf16 v[24:27], a[232:235], v[56:59], v[24:27]\n"
    "ds_read_b128 v[48:51], v1 offset:13312\n"
    "ds_read_b128 v[52:55], v1 offset:31744\n"
    "s_waitcnt lgkmcnt(4)\n"
    "v_mfma_f32_16x16x32_bf16 v[28:31], a[232:235], v[60:63], v[28:31]\n"
    "v_mfma_f32_16x16x32_bf16 v[32:35], a[236:239], v[56:59], v[32:35]\n"
    "v_mfma_f32_16x16x32_bf16 v[36:39], a[236:239], v[60:63], v[36:39]\n"
    "s_waitcnt lgkmcnt(3)\n"
    "v_mfma_f32_16x16x32_bf16 v[24:27], a[240:243], v[40:43], v[24:27]\n"
    "ds_read_b128 v[56:59], v0 offset:14336\n"
    "ds_read_b128 v[60:63], v0 offset:32768\n"
    "s_waitcnt lgkmcnt(4)\n"
    "v_mfma_f32_16x16x32_bf16 v[28:31], a[240:243], v[44:47], v[28:31]\n"
    "v_mfma_f32_16x16x32_bf16 v[32:35], a[244:247], v[40:43], v[32:35]\n"
    "v_mfma_f32_16x16x32_bf16 v[36:39], a[244:247], v[44:47], v[36:39]\n"
    "s_waitcnt lgkmcnt(3)\n"
    "v_mfma_f32_16x16x32_bf16 v[24:27], a[248:251], v[48:51], v[24:27]\n"
    "ds_read_b128 v[40:43], v1 offset:15360\n"
    "ds_read_b128 v[44:47], v1 offset:33792\n"
    "s_waitcnt lgkmcnt(4)\n"
    "v_mfma_f32_16x16x32_bf16 v[28:31], a[248:251], v[52:55], v[28:31]\n"
    "v_mfma_f32_16x16x32_bf16 v[32:35], a[252:255], v[48:51], v[32:35]\n"
    "v_mfma_f32_16x16x32_bf16 v[36:39], a[252:255], v[52:55], v[36:39]\n"
    "s_waitcnt lgkmcnt(3)\n"
    "v_mfma_f32_16x16x32_bf16 v[24:27], v[80:83], v[56:59], v[24:27]\n"
    "ds_read_b128 v[48:51], v0 offset:16384\n"
    "ds_read_b128 v[52:55], v0 offset:34816\n"
    "s_waitcnt lgkmcnt(4)\n"
    "v_mfma_f32_16x16x32_bf16 v[28:31], v[80:83], v[60:63], v[28:31]\n"
    "v_mfma_f32_16x16x32_bf16 v[32:35], v[84:87], v[56:59], v[32:35]\n"
    "v_mfma_f32_16x16x32_bf16 v[36:39], v[84:87], v[60:63], v[36:39]\n"
    "s_waitcnt lgkmcnt(3)\n"
    "v_mfma_f32_16x16x32_bf16 v[24:27], v[88:91], v[40:43], v[24:27]\n"
    "ds_read_b128 v[56:59], v1 offset:17408\n"
    "ds_read_b128 v[60:63], v1 offset:35840\n"
    "s_waitcnt lgkmcnt(4)\n"
    "v_mfma_f32_16x16x32_bf16 v[28:31], v[88:91], v[44:47], v[28:31]\n"
    "v_mfma_f32_16x16x32_bf16 v[32:35], v[92:95], v[40:43], v[32:35]\n"
    "v_mfma_f32_16x16x32_bf16 v[36:39], v[92:95], v[44:47], v[36:39]\n"
    "s_waitcnt lgkmcnt(3)\n"
    "v_mfma_f32_16x16x32_bf16 v[24:27], v[96:99], v[48:51], v[24:27]\n"
    "ds_read_b128 v[40:43], v0\n"
    "ds_read_b128 v[44:47], v0 offset:18432\n"
    "s_waitcnt lgkmcnt(4)\n"
    "v_mfma_f32_16x16x32_bf16 v[28:31], v[96:99], v[52:55], v[28:31]\n"
    "v_mfma_f32_16x16x32_bf16 v[32:35], v[100:103], v[48:51], v[32:35]\n"
    "v_mfma_f32_16x16x32_bf16 v[36:39], v[100:103], v[52:55], v[36:39]\n"
    "s_waitcnt lgkmcnt(3)\n"
    "v_mfma_f32_16x16x32_bf16 v[24:27], v[104:107], v[56:59], v[24:27]\n"
    "ds_read_b128 v[48:51], v1 offset:1024\n"
    "ds_read_b128 v[52:55], v1 offset:19456\n"
    "s_cmp_eq_u32 s49, 0\n"
    "s_cbranch_scc1 .Lhc4_%=\n"
    "s_waitcnt vmcnt(0)\n"
    "ds_write_b128 v2, v[8:11]\n"
    "ds_write_b128 v2, v[12:15] offset:2048\n"
    "ds_write_b128 v2, v[16:19] offset:4096\n"
    "ds_write_b128 v2, v[20:23] offset:6144\n"
    "ds_write_b128 v2, v[64:67] offset:8192\n"
    "ds_write_b128 v2, v[68:71] offset:10240\n"
    "ds_write_b128 v2, v[72:75] offset:12288\n"
    "ds_write_b128 v2, v[76:79] offset:14336\n"
    "buffer_load_dwordx4 v[64:67], v3, s[40:43], s58 offen offset:1024 sc1\n"
    ".Lhc4_%=:\n"
    "s_waitcnt lgkmcnt(4)\n"
    "v_mfma_f32_16x16x32_bf16 v[28:31], v[104:107], v[60:63], v[28:31]\n"
    "v_mfma_f32_16x16x32_bf16 v[32:35], v[108:111], v[56:59], v[32:35]\n"
    "v_mfma_f32_16x16x32_bf16 v[36:39], v[108:111], v[60:63], v[36:39]\n"
    "s_waitcnt lgkmcnt(3)\n"
    "v_mfma_f32_16x16x32_bf16 v[8:11], v[40:43], v[112:115], 0\n"
    "ds_read_b128 v[56:59], v0 offset:2048\n"
    "ds_read_b128 v[60:63], v0 offset:20480\n"
    "s_waitcnt lgkmcnt(4)\n"
    "v_mfma_f32_16x16x32_bf16 v[12:15], v[44:47], v[112:115], 0\n"
    "v_mfma_f32_16x16x32_bf16 v[16:19], v[40:43], v[116:119], 0\n"
    "v_mfma_f32_16x16x32_bf16 v[20:23], v[44:47], v[116:119], 0\n"
    "v_cvt_pk_bf16_f32 v24, v24, v32\n"
    "v_cvt_pk_bf16_f32 v25, v25, v33\n"
    "s_waitcnt lgkmcnt(3)\n"
    "v_mfma_f32_16x16x32_bf16 v[8:11], v[48:51], v[120:123], v[8:11]\n"
    "ds_read_b128 v[40:43], v1 offset:3072\n"
    "ds_read_b128 v[44:47], v1 offset:21504\n"
    "s_waitcnt lgkmcnt(4)\n"
    "v_mfma_f32_16x16x32_bf16 v[12:15], v[52:55], v[120:123], v[12:15]\n"
    "v_cvt_pk_bf16_f32 v26, v26, v34\n"
    "v_cvt_pk_bf16_f32 v27, v27, v35\n"
    "v_mfma_f32_16x16x32_bf16 v[16:19], v[48:51], v[124:127], v[16:19]\n"
    "buffer_store_dwordx4 v[24:27], v4, s[40:43], s64 offen sc1\n"
    "v_cvt_pk_bf16_f32 v28, v28, v36\n"
    "v_mfma_f32_16x16x32_bf16 v[20:23], v[52:55], v[124:127], v[20:23]\n"
    "v_cvt_pk_bf16_f32 v29, v29, v37\n"
    "v_cvt_pk_bf16_f32 v30, v30, v38\n"
    "s_waitcnt lgkmcnt(3)\n"
    "v_mfma_f32_16x16x32_bf16 v[8:11], v[56:59], v[128:131], v[8:11]\n"
    "ds_read_b128 v[48:51], v0 offset:4096\n"
    "ds_read_b128 v[52:55], v0 offset:22528\n"
    "s_waitcnt lgkmcnt(4)\n"
    "v_mfma_f32_16x16x32_bf16 v[12:15], v[60:63], v[128:131], v[12:15]\n"
    "v_cvt_pk_bf16_f32 v31, v31, v39\n"
    "buffer_store_dwordx4 v[28:31], v4, s[40:43], s65 offen sc1\n"
    "v_mfma_f32_16x16x32_bf16 v[16:19], v[56:59], v[132:135], v[16:19]\n"
    "v_mfma_f32_16x16x32_bf16 v[20:23], v[60:63], v[132:135], v[20:23]\n"
    "s_waitcnt lgkmcnt(3)\n"
    "v_mfma_f32_16x16x32_bf16 v[8:11], v[40:43], v[136:139], v[8:11]\n"
    "ds_read_b128 v[56:59], v1 offset:5120\n"
    "ds_read_b128 v[60:63], v1 offset:23552\n"
    "s_waitcnt lgkmcnt(4)\n"
    "v_mfma_f32_16x16x32_bf16 v[12:15], v[44:47], v[136:139], v[12:15]\n"
    "v_mfma_f32_16x16x32_bf16 v[16:19], v[40:43], v[140:143], v[16:19]\n"
    "v_mfma_f32_16x16x32_bf16 v[20:23], v[44:47], v[140:143], v[20:23]\n"
    "s_waitcnt lgkmcnt(3)\n"
    "v_mfma_f32_16x16x32_bf16 v[8:11], v[48:51], v[144:147], v[8:11]\n"
    "ds_read_b128 v[40:43], v0 offset:6144\n"
    "ds_read_b128 v[44:47], v0 offset:24576\n"
    "s_waitcnt lgkmcnt(4)\n"
    "v_mfma_f32_16x16x32_bf16 v[12:15], v[52:55], v[144:147], v[12:15]\n"
    "v_mfma_f32_16x16x32_bf16 v[16:19], v[48:51], v[148:151], v[16:19]\n"
    "v_mfma_f32_16x16x32_bf16 v[20:23], v[52:55], v[148:151], v[20:23]\n"
    "s_waitcnt lgkmcnt(3)\n"
    "v_mfma_f32_16x16x32_bf16 v[8:11], v[56:59], v[152:155], v[8:11]\n"
    "ds_read_b128 v[48:51], v1 offset:7168\n"
    "ds_read_b128 v[52:55], v1 offset:25600\n"
    "s_waitcnt lgkmcnt(4)\n"
    "v_mfma_f32_16x16x32_bf16 v[12:15], v[60:63], v[152:155], v[12:15]\n"
    "v_mfma_f32_16x16x32_bf16 v[16:19], v[56:59], v[156:159], v[16:19]\n"
    "v_mfma_f32_16x16x32_bf16 v[20:23], v[60:63], v[156:159], v[20:23]\n"
    "s_waitcnt lgkmcnt(3)\n"
    "v_mfma_f32_16x16x32_bf16 v[8:11], v[40:43], v[160:163], v[8:11]\n"
    "ds_read_b128 v[56:59], v0 offset:8192\n"
    "ds_read_b128 v[60:63], v0 offset:26624\n"
    "s_waitcnt lgkmcnt(4)\n"
    "v_mfma_f32_16x16x32_bf16 v[12:15], v[44:47], v[160:163], v[12:15]\n"
    "v_mfma_f32_16x16x32_bf16 v[16:19], v[40:43], v[164:167], v[16:19]\n"
    "v_mfma_f32_16x16x32_bf16 v[20:23], v[44:47], v[164:167], v[20:23]\n"
    "s_waitcnt lgkmcnt(3)\n"
    "v_mfma_f32_16x16x32_bf16 v[8:11], v[48:51], v[168:171], v[8:11]\n"
    "ds_read_b128 v[40:43], v1 offset:9216\n"
    "ds_read_b128 v[44:47], v1 offset:27648\n"
    "s_waitcnt lgkmcnt(4)\n"
    "v_mfma_f32_16x16x32_bf16 v[12:15], v[52:55], v[168:171], v[12:15]\n"
    "v_mfma_f32_16x16x32_bf16 v[16:19], v[48:51], v[172:175], v[16:19]\n"
    "v_mfma_f32_16x16x32_bf16 v[20:23], v[52:55], v[172:175], v[20:23]\n"
    "s_waitcnt lgkmcnt(3)\n"
    "v_mfma_f32_16x16x32_bf16 v[8:11], v[56:59], v[176:179], v[8:11]\n"
    "ds_read_b128 v[48:51], v0 offset:10240\n"
    "ds_read_b128 v[52:55], v0 offset:28672\n"
    "s_waitcnt lgkmcnt(4)\n"
    "v_mfma_f32_16x16x32_bf16 v[12:15], v[60:63], v[176:179], v[12:15]\n"
    "v_mfma_f32_16x16x32_bf16 v[16:19], v[56:59], v[180:183], v[16:19]\n"
    "v_mfma_f32_16x16x32_bf16 v[20:23], v[60:63], v[180:183], v[20:23]\n"
    "s_waitcnt lgkmcnt(3)\n"
    "v_mfma_f32_16x16x32_bf16 v[8:11], v[40:43], v[184:187], v[8:11]\n"
    "ds_read_b128 v[56:59], v1 offset:11264\n"
    "ds_read_b128 v[60:63], v1 offset:29696\n"
    "s_waitcnt lgkmcnt(4)\n"
    "v_mfma_f32_16x16x32_bf16 v[12:15], v[44:47], v[184:187], v[12:15]\n"
    "v_mfma_f32_16x16x32_bf16 v[16:19], v[40:43], v[188:191], v[16:19]\n"
    "v_mfma_f32_16x16x32_bf16 v[20:23], v[44:47], v[188:191], v[20:23]\n"
    "s_waitcnt lgkmcnt(3)\n"
    "v_mfma_f32_16x16x32_bf16 v[8:11], v[48:51], v[192:195], v[8:11]\n"
    "ds_read_b128 v[40:43], v0 offset:12288\n"
    "ds_read_b128 v[44:47], v0 offset:30720\n"
    "s_waitcnt lgkmcnt(4)\n"
    "v_mfma_f32_16x16x32_bf16 v[12:15], v[52:55], v[192:195], v[12:15]\n"
    "v_mfma_f32_16x16x32_bf16 v[16:19], v[48:51], v[196:199], v[16:19]\n"
    "v_mfma_f32_16x16x32_bf16 v[20:23], v[52:55], v[196:199], v[20:23]\n"
    "s_waitcnt lgkmcnt(3)\n"
    "v_mfma_f32_16x16x32_bf16 v[8:11], v[56:59], v[200:203], v[8:11]\n"
    "ds_read_b128 v[48:51], v1 offset:13312\n"
    "ds_read_b128 v[52:55], v1 offset:31744\n"
    "s_waitcnt lgkmcnt(4)\n"
    "v_mfma_f32_16x16x32_bf16 v[12:15], v[60:63], v[200:203], v[12:15]\n"
    "v_mfma_f32_16x16x32_bf16 v[16:19], v[56:59], v[204:207], v[16:19]\n"
    "v_mfma_f32_16x16x32_bf16 v[20:23], v[60:63], v[204:207], v[20:23]\n"
    "s_waitcnt lgkmcnt(3)\n"
    "v_mfma_f32_16x16x32_bf16 v[8:11], v[40:43], v[208:211], v[8:11]\n"
    "ds_read_b128 v[56:59], v0 offset:14336\n"
    "ds_read_b128 v[60:63], v0 offset:32768\n"
    "s_waitcnt lgkmcnt(4)\n"
    "v_mfma_f32_16x16x32_bf16 v[12:15], v[44:47], v[208:211], v[12:15]\n"
    "v_mfma_f32_16x16x32_bf16 v[16:19], v[40:43], v[212:215], v[16:19]\n"
    "v_mfma_f32_16x16x32_bf16 v[20:23], v[44:47], v[212:215], v[20:23]\n"
    "s_waitcnt lgkmcnt(3)\n"
    "v_mfma_f32_16x16x32_bf16 v[8:11], v[48:51], v[216:219], v[8:11]\n"
    "ds_read_b128 v[40:43], v1 offset:15360\n"
    "ds_read_b128 v[44:47], v1 offset:33792\n"
    "s_waitcnt lgkmcnt(4)\n"
    "v_mfma_f32_16x16x32_bf16 v[12:15], v[52:55], v[216:219], v[12:15]\n"
    "v_mfma_f32_16x16x32_bf16 v[16:19], v[48:51], v[220:223], v[16:19]\n"
    "v_mfma_f32_16x16x32_bf16 v[20:23], v[52:55], v[220:223], v[20:23]\n"
    "s_waitcnt lgkmcnt(3)\n"
    "v_mfma_f32_16x16x32_bf16 v[8:11], v[56:59], v[224:227], v[8:11]\n"
    "ds_read_b128 v[48:51], v0 offset:16384\n"
    "ds_read_b128 v[52:55], v0 offset:34816\n"
    "s_waitcnt lgkmcnt(4)\n"
    "v_mfma_f32_16x16x32_bf16 v[12:15], v[60:63], v[224:227], v[12:15]\n"
    "v_mfma_f32_16x16x32_bf16 v[16:19], v[56:59], v[228:231], v[16:19]\n"
    "v_mfma_f32_16x16x32_bf16 v[20:23], v[60:63], v[228:231], v[20:23]\n"
    "s_waitcnt lgkmcnt(3)\n"
    "v_mfma_f32_16x16x32_bf16 v[8:11], v[40:43], v[232:235], v[8:11]\n"
    "ds_read_b128 v[56:59], v1 offset:17408\n"
    "ds_read_b128 v[60:63], v1 offset:35840\n"
    "s_waitcnt lgkmcnt(4)\n"
    "v_mfma_f32_16x16x32_bf16 v[12:15], v[44:47], v[232:235], v[12:15]\n"
    "v_mfma_f32_16x16x32_bf16 v[16:19], v[40:43], v[236:239], v[16:19]\n"
    "v_mfma_f32_16x16x32_bf16 v[20:23], v[44:47], v[236:239], v[20:23]\n"
    "s_waitcnt lgkmcnt(3)\n"
    "v_mfma_f32_16x16x32_bf16 v[8:11], v[48:51], v[240:243], v[8:11]\n"
    "s_waitcnt lgkmcnt(2)\n"
    "v_mfma_f32_16x16x32_bf16 v[12:15], v[52:55], v[240:243], v[12:15]\n"
    "v_mfma_f32_16x16x32_bf16 v[16:19], v[48:51], v[244:247], v[16:19]\n"
    "s_cmp_eq_u32 s49, 0\n"
    "s_cbranch_scc1 .Lhc5_%=\n"
    "s_waitcnt vmcnt(2)\n"
    "ds_write_b128 v2, v[64:67] offset:16384\n"
    ".Lhc5_%=:\n"
    "v_mfma_f32_16x16x32_bf16 v[20:23], v[52:55], v[244:247], v[20:23]\n"
    "s_waitcnt lgkmcnt(1)\n"
    "v_mfma_f32_16x16x32_bf16 v[8:11], v[56:59], v[248:251], v[8:11]\n"
    "s_waitcnt lgkmcnt(0)\n"
    "v_mfma_f32_16x16x32_bf16 v[12:15], v[60:63], v[248:251], v[12:15]\n"
    "v_mfma_f32_16x16x32_bf16 v[16:19], v[56:59], v[252:255], v[16:19]\n"
    "v_mfma_f32_16x16x32_bf16 v[20:23], v[60:63], v[252:255], v[20:23]\n"
    "v_cvt_pk_bf16_f32 v8, v8, v9\n"
    "v_cvt_pk_bf16_f32 v9, v10, v11\n"
    "v_cvt_pk_bf16_f32 v10, v12, v13\n"
    "v_cvt_pk_bf16_f32 v11, v14, v15\n"
    "buffer_store_dwordx4 v[8:11], v4, s[40:43], s66 offen sc1\n"
    "v_cvt_pk_bf16_f32 v16, v16, v17\n"
    "v_cvt_pk_bf16_f32 v17, v18, v19\n"
    "s_nop 1\n"
    "v_cvt_pk_bf16_f32 v18, v20, v21\n"
    "v_cvt_pk_bf16_f32 v19, v22, v23\n"
    "buffer_store_dwordx4 v[16:19], v4, s[40:43], s67 offen sc1\n"
    "s_cmp_lg_u32 s49, 0\n"
    "s_cbranch_scc1 .Lhgo_%=\n"
    "s_waitcnt vmcnt(0) lgkmcnt(0)\n"
    "s_barrier\n"
    "s_cmp_eq_u32 %2, 0\n"
    "s_cselect_b32 s50, 1, 0\n"
    "s_cmp_eq_u32 s50, 0\n"
    "s_cbranch_scc1 .Lhc6_%=\n"
    "s_sub_u32 s51, s48, 0\n"
    "s_mul_i32 s51, s51, 4\n"
    "s_add_u32 s51, s51, %5\n"
    "v_mov_b32 v6, s51\n"
    "v_mov_b32 v5, 1\n"
    "s_mov_b64 exec, 1\n"
    "global_store_dword v6, v5, %1 sc1\n"
    "s_mov_b64 exec, -1\n"
    ".Lhc6_%=:\n"
    "s_cmp_eq_u32 s48, 63\n"
    "s_cbranch_scc1 .Lhend_%=\n"
    ".Lhwait_%=:\n"
    "s_cmp_eq_u32 %2, 0\n"
    "s_cbranch_scc0 .Lhnp_%=\n"
    "s_add_u32 s52, s48, 1\n"
    "s_and_b32 s52, s52, 63\n"
    "s_mul_i32 s52, s52, 64\n"
    "s_add_u32 s52, s52, %4\n"
    "s_mov_b32 s53, 0\n"
    "v_mov_b32 v6, s52\n"
    ".Lhp7_%=:\n"
    "global_load_dword v5, v6, %1 sc1\n"
    "s_waitcnt vmcnt(0)\n"
    "v_readfirstlane_b32 s51, v5\n"
    "s_cmp_ge_u32 s51, 2\n"
    "s_cbranch_scc1 .Lhp7_x%=\n"
    "s_add_u32 s53, s53, 1\n"
    "s_sleep 2\n"
    "s_cmp_lt_u32 s53, s54\n"
    "s_cbranch_scc1 .Lhp7_%=\n"
    "s_mov_b32 s54, 0\n"
    "v_mov_b32 v6, 0\n"
    "v_mov_b32 v5, 1\n"
    "global_store_dword v6, v5, %1 sc1\n"
    ".Lhp7_x%=:\n"
    ".Lhnp_%=:\n"
    "s_barrier\n"
    "buffer_load_dwordx4 v[24:27], v3, s[40:43], s58 offen sc1\n"
    "buffer_load_dwordx4 v[28:31], v3, s[40:43], s58 offen offset:128 sc1\n"
    "buffer_load_dwordx4 v[32:35], v3, s[40:43], s58 offen offset:256 sc1\n"
    "buffer_load_dwordx4 v[36:39], v3, s[40:43], s58 offen offset:384 sc1\n"
    "buffer_load_dwordx4 v[40:43], v3, s[40:43], s58 offen offset:512 sc1\n"
    "buffer_load_dwordx4 v[44:47], v3, s[40:43], s58 offen offset:640 sc1\n"
    "buffer_load_dwordx4 v[48:51], v3, s[40:43], s58 offen offset:768 sc1\n"
    "buffer_load_dwordx4 v[52:55], v3, s[40:43], s58 offen offset:896 sc1\n"
    "buffer_load_dwordx4 v[56:59], v3, s[40:43], s58 offen offset:1024 sc1\n"
    "s_waitcnt vmcnt(8)\n"
    "ds_write_b128 v2, v[24:27]\n"
    "s_waitcnt vmcnt(7)\n"
    "ds_write_b128 v2, v[28:31] offset:2048\n"
    "s_waitcnt vmcnt(6)\n"
    "ds_write_b128 v2, v[32:35] offset:4096\n"
    "s_waitcnt vmcnt(5)\n"
    "ds_write_b128 v2, v[36:39] offset:6144\n"
    "s_waitcnt vmcnt(4)\n"
    "ds_write_b128 v2, v[40:43] offset:8192\n"
    "s_waitcnt vmcnt(3)\n"
    "ds_write_b128 v2, v[44:47] offset:10240\n"
    "s_waitcnt vmcnt(2)\n"
    "ds_write_b128 v2, v[48:51] offset:12288\n"
    "s_waitcnt vmcnt(1)\n"
    "ds_write_b128 v2, v[52:55] offset:14336\n"
    "s_waitcnt vmcnt(0)\n"
    "ds_write_b128 v2, v[56:59] offset:16384\n"
    "s_mov_b32 s50, 0\n"
    "s_branch .Lhnext_%=\n"
    ".Lhgo_%=:\n"
    "s_cmp_eq_u32 %2, 0\n"
    "s_cselect_b32 s50, 1, 0\n"
    ".Lhnext_%=:\n"
    "s_add_u32 s48, s48, 1\n"
    "s_add_u32 s58, s58, 36864\n"
    "v_xor_b32 v0, 0x10000, v0\n"
    "v_xor_b32 v1, 0x10000, v1\n"
    "v_xor_b32 v2, 0x10000, v2\n"
    "s_branch .Lhtop_%=\n"
    ".Lhend_%=:\n"
    "s_waitcnt vmcnt(0)\n"
    "s_endpgm\n"
    ".p2align 6\n"
    ".Lrole2_%=:\n"
    "s_mov_b64 s[40:41], %0\n"
    "s_and_b32 s41, s41, 0xffff\n"
    "s_mov_b32 s42, 0xc800000\n"
    "s_mov_b32 s43, 0x20000\n"
    "s_mov_b32 s54, 131072\n"
    "v_mbcnt_lo_u32_b32 v7, -1, 0\n"
    "v_mbcnt_hi_u32_b32 v7, -1, v7\n"
    "s_lshl_b32 s51, %2, 8\n"
    "s_add_u32 s51, s51, 147456\n"
    "v_lshl_add_u32 v7, v7, 2, s51\n"
    "ds_read_b32 v0, v7\n"
    "ds_read_b32 v1, v7 offset:1024\n"
    "ds_read_b32 v2, v7 offset:2048\n"
    "ds_read_b32 v3, v7 offset:3072\n"
    "ds_read_b32 v8, v7 offset:4096\n"
    "ds_read_b32 v9, v7 offset:5120\n"
    "ds_read_b32 v10, v7 offset:6144\n"
    "ds_read_b32 v11, v7 offset:7168\n"
    "s_waitcnt lgkmcnt(0)\n"
    "s_barrier\n"
    "s_lshl_b32 s56, %2, 10\n"
    "s_mov_b64 s[44:45], %10\n"
    "s_and_b32 s45, s45, 0xffff\n"
    "s_mov_b32 s46, 0x480000\n"
    "s_mov_b32 s47, 0x20000\n"
    "s_add_u32 m0, s56, 0\n"
    "s_add_u32 s57, %8, 0\n"
    "buffer_load_dwordx4 v8, s[44:47], s57 offen lds\n"
    "s_add_u32 m0, s56, 4096\n"
    "s_add_u32 s57, %8, 128\n"
    "buffer_load_dwordx4 v8, s[44:47], s57 offen lds\n"
    "s_add_u32 m0, s56, 8192\n"
    "s_add_u32 s57, %8, 256\n"
    "buffer_load_dwordx4 v8, s[44:47], s57 offen lds\n"
    "s_add_u32 m0, s56, 12288\n"
    "s_add_u32 s57, %8, 36864\n"
    "buffer_load_dwordx4 v8, s[44:47], s57 offen lds\n"
    "s_add_u32 m0, s56, 16384\n"
    "s_add_u32 s57, %8, 36992\n"
    "buffer_load_dwordx4 v8, s[44:47], s57 offen lds\n"
    "s_add_u32 m0, s56, 20480\n"
    "s_add_u32 s57, %8, 37120\n"
    "buffer_load_dwordx4 v8, s[44:47], s57 offen lds\n"
    "s_add_u32 m0, s56, 24576\n"
    "s_add_u32 s57, %8, 73728\n"
    "buffer_load_dwordx4 v8, s[44:47], s57 offen lds\n"
    "s_add_u32 m0, s56, 28672\n"
    "s_add_u32 s57, %8, 73856\n"
    "buffer_load_dwordx4 v8, s[44:47], s57 offen lds\n"
    "s_add_u32 m0, s56, 32768\n"
    "s_add_u32 s57, %8, 73984\n"
    "buffer_load_dwordx4 v8, s[44:47], s57 offen lds\n"
    "s_add_u32 m0, s56, 36864\n"
    "s_add_u32 s57, %8, 110592\n"
    "buffer_load_dwordx4 v8, s[44:47], s57 offen lds\n"
    "s_add_u32 m0, s56, 40960\n"
    "s_add_u32 s57, %8, 110720\n"
    "buffer_load_dwordx4 v8, s[44:47], s57 offen lds\n"
    "s_add_u32 m0, s56, 45056\n"
    "s_add_u32 s57, %8, 110848\n"
    "buffer_load_dwordx4 v8, s[44:47], s57 offen lds\n"
    "s_add_u32 m0, s56, 49152\n"
    "s_add_u32 s57, %8, 147456\n"
    "buffer_load_dwordx4 v8, s[44:47], s57 offen lds\n"
    "s_add_u32 m0, s56, 53248\n"
    "s_add_u32 s57, %8, 147584\n"
    "buffer_load_dwordx4 v8, s[44:47], s57 offen lds\n"
    "s_add_u32 m0, s56, 57344\n"
    "s_add_u32 s57, %8, 147712\n"
    "buffer_load_dwordx4 v8, s[44:47], s57 offen lds\n"
    "s_add_u32 m0, s56, 61440\n"
    "s_add_u32 s57, %8, 184320\n"
    "buffer_load_dwordx4 v8, s[44:47], s57 offen lds\n"
    "s_add_u32 m0, s56, 65536\n"
    "s_add_u32 s57, %8, 184448\n"
    "buffer_load_dwordx4 v8, s[44:47], s57 offen lds\n"
    "s_add_u32 m0, s56, 69632\n"
    "s_add_u32 s57, %8, 184576\n"
    "buffer_load_dwordx4 v8, s[44:47], s57 offen lds\n"
    "s_add_u32 m0, s56, 73728\n"
    "s_add_u32 s57, %8, 221184\n"
    "buffer_load_dwordx4 v8, s[44:47], s57 offen lds\n"
    "s_add_u32 m0, s56, 77824\n"
    "s_add_u32 s57, %8, 221312\n"
    "buffer_load_dwordx4 v8, s[44:47], s57 offen lds\n"
    "s_add_u32 m0, s56, 81920\n"
    "s_add_u32 s57, %8, 221440\n"
    "buffer_load_dwordx4 v8, s[44:47], s57 offen lds\n"
    "s_add_u32 m0, s56, 86016\n"
    "s_add_u32 s57, %8, 258048\n"
    "buffer_load_dwordx4 v8, s[44:47], s57 offen lds\n"
    "s_add_u32 m0, s56, 90112\n"
    "s_add_u32 s57, %8, 258176\n"
    "buffer_load_dwordx4 v8, s[44:47], s57 offen lds\n"
    "s_add_u32 m0, s56, 94208\n"
    "s_add_u32 s57, %8, 258304\n"
    "buffer_load_dwordx4 v8, s[44:47], s57 offen lds\n"
    "s_add_u32 m0, s56, 98304\n"
    "s_add_u32 s57, %8, 294912\n"
    "buffer_load_dwordx4 v8, s[44:47], s57 offen lds\n"
    "s_add_u32 m0, s56, 102400\n"
    "s_add_u32 s57, %8, 295040\n"
    "buffer_load_dwordx4 v8, s[44:47], s57 offen lds\n"
    "s_add_u32 m0, s56, 106496\n"
    "s_add_u32 s57, %8, 295168\n"
    "buffer_load_dwordx4 v8, s[44:47], s57 offen lds\n"
    "s_add_u32 m0, s56, 110592\n"
    "s_add_u32 s57, %8, 331776\n"
    "buffer_load_dwordx4 v8, s[44:47], s57 offen lds\n"
    "s_add_u32 m0, s56, 114688\n"
    "s_add_u32 s57, %8, 331904\n"
    "buffer_load_dwordx4 v8, s[44:47], s57 offen lds\n"
    "s_add_u32 m0, s56, 118784\n"
    "s_add_u32 s57, %8, 332032\n"
    "buffer_load_dwordx4 v8, s[44:47], s57 offen lds\n"
    "s_add_u32 m0, s56, 122880\n"
    "s_add_u32 s57, %8, 368640\n"
    "buffer_load_dwordx4 v8, s[44:47], s57 offen lds\n"
    "s_add_u32 m0, s56, 126976\n"
    "s_add_u32 s57, %8, 368768\n"
    "buffer_load_dwordx4 v8, s[44:47], s57 offen lds\n"
    "s_add_u32 m0, s56, 131072\n"
    "s_add_u32 s57, %8, 368896\n"
    "buffer_load_dwordx4 v8, s[44:47], s57 offen lds\n"
    "s_add_u32 m0, s56, 135168\n"
    "s_add_u32 s57, %8, 405504\n"
    "buffer_load_dwordx4 v8, s[44:47], s57 offen lds\n"
    "s_add_u32 m0, s56, 139264\n"
    "s_add_u32 s57, %8, 405632\n"
    "buffer_load_dwordx4 v8, s[44:47], s57 offen lds\n"
    "s_add_u32 m0, s56, 143360\n"
    "s_add_u32 s57, %8, 405760\n"
    "buffer_load_dwordx4 v8, s[44:47], s57 offen lds\n"
    "s_waitcnt vmcnt(33) lgkmcnt(0)\n"
    "s_barrier\n"
    "ds_read_b64_tr_b16 a[0:1], v9\n"
    "ds_read_b64_tr_b16 a[2:3], v9 offset:256\n"
    "ds_read_b64_tr_b16 a[4:5], v10\n"
    "ds_read_b64_tr_b16 a[6:7], v10 offset:256\n"
    "ds_read_b64_tr_b16 a[8:9], v11\n"
    "ds_read_b64_tr_b16 a[10:11], v11 offset:256\n"
    "s_waitcnt vmcnt(30) lgkmcnt(0)\n"
    "s_barrier\n"
    "s_add_u32 m0, s56, 0\n"
    "s_add_u32 s57, %8, 442368\n"
    "buffer_load_dwordx4 v8, s[44:47], s57 offen lds\n"
    "s_add_u32 m0, s56, 4096\n"
    "s_add_u32 s57, %8, 442496\n"
    "buffer_load_dwordx4 v8, s[44:47], s57 offen lds\n"
    "s_add_u32 m0, s56, 8192\n"
    "s_add_u32 s57, %8, 442624\n"
    "buffer_load_dwordx4 v8, s[44:47], s57 offen lds\n"
    "ds_read_b64_tr_b16 a[12:13], v9 offset:12288\n"
    "ds_read_b64_tr_b16 a[14:15], v9 offset:12544\n"
    "ds_read_b64_tr_b16 a[16:17], v10 offset:12288\n"
    "ds_read_b64_tr_b16 a[18:19], v10 offset:12544\n"
    "ds_read_b64_tr_b16 a[20:21], v11 offset:12288\n"
    "ds_read_b64_tr_b16 a[22:23], v11 offset:12544\n"
    "s_waitcnt vmcnt(30) lgkmcnt(0)\n"
    "s_barrier\n"
    "s_add_u32 m0, s56, 12288\n"
    "s_add_u32 s57, %8, 479232\n"
    "buffer_load_dwordx4 v8, s[44:47], s57 offen lds\n"
    "s_add_u32 m0, s56, 16384\n"
    "s_add_u32 s57, %8, 479360\n"
    "buffer_load_dwordx4 v8, s[44:47], s57 offen lds\n"
    "s_add_u32 m0, s56, 20480\n"
    "s_add_u32 s57, %8, 479488\n"
    "buffer_load_dwordx4 v8, s[44:47], s57 offen lds\n"
    "ds_read_b64_tr_b16 a[24:25], v9 offset:24576\n"
    "ds_read_b64_tr_b16 a[26:27], v9 offset:24832\n"
    "ds_read_b64_tr_b16 a[28:29], v10 offset:24576\n"
    "ds_read_b64_tr_b16 a[30:31], v10 offset:24832\n"
    "ds_read_b64_tr_b16 a[32:33], v11 offset:24576\n"
    "ds_read_b64_tr_b16 a[34:35], v11 offset:24832\n"
    "s_waitcnt vmcnt(30) lgkmcnt(0)\n"
    "s_barrier\n"
    "s_add_u32 m0, s56, 24576\n"
    "s_add_u32 s57, %8, 516096\n"
    "buffer_load_dwordx4 v8, s[44:47], s57 offen lds\n"
    "s_add_u32 m0, s56, 28672\n"
    "s_add_u32 s57, %8, 516224\n"
    "buffer_load_dwordx4 v8, s[44:47], s57 offen lds\n"
    "s_add_u32 m0, s56, 32768\n"
    "s_add_u32 s57, %8, 516352\n"
    "buffer_load_dwordx4 v8, s[44:47], s57 offen lds\n"
    "ds_read_b64_tr_b16 a[36:37], v9 offset:36864\n"
    "ds_read_b64_tr_b16 a[38:39], v9 offset:37120\n"
    "ds_read_b64_tr_b16 a[40:41], v10 offset:36864\n"
    "ds_read_b64_tr_b16 a[42:43], v10 offset:37120\n"
    "ds_read_b64_tr_b16 a[44:45], v11 offset:36864\n"
    "ds_read_b64_tr_b16 a[46:47], v11 offset:37120\n"
    "s_waitcnt vmcnt(30) lgkmcnt(0)\n"
    "s_barrier\n"
    "s_add_u32 m0, s56, 36864\n"
    "s_add_u32 s57, %8, 552960\n"
    "buffer_load_dwordx4 v8, s[44:47], s57 offen lds\n"
    "s_add_u32 m0, s56, 40960\n"
    "s_add_u32 s57, %8, 553088\n"
    "buffer_load_dwordx4 v8, s[44:47], s57 offen lds\n"
    "s_add_u32 m0, s56, 45056\n"
    "s_add_u32 s57, %8, 553216\n"
    "buffer_load_dwordx4 v8, s[44:47], s57 offen lds\n"
    "ds_read_b64_tr_b16 a[48:49], v9 offset:49152\n"
    "ds_read_b64_tr_b16 a[50:51], v9 offset:49408\n"
    "ds_read_b64_tr_b16 a[52:53], v10 offset:49152\n"
    "ds_read_b64_tr_b16 a[54:55], v10 offset:49408\n"
    "ds_read_b64_tr_b16 a[56:57], v11 offset:49152\n"
    "ds_read_b64_tr_b16 a[58:59], v11 offset:49408\n"
    "s_waitcnt vmcnt(30) lgkmcnt(0)\n"
    "s_barrier\n"
    "s_add_u32 m0, s56, 49152\n"
    "s_add_u32 s57, %8, 589824\n"
    "buffer_load_dwordx4 v8, s[44:47], s57 offen lds\n"
    "s_add_u32 m0, s56, 53248\n"
    "s_add_u32 s57, %8, 589952\n"
    "buffer_load_dwordx4 v8, s[44:47], s57 offen lds\n"
    "s_add_u32 m0, s56, 57344\n"
    "s_add_u32 s57, %8, 590080\n"
    "buffer_load_dwordx4 v8, s[44:47], s57 offen lds\n"
    "ds_read_b64_tr_b16 a[60:61], v9 offset:61440\n"
    "ds_read_b64_tr_b16 a[62:63], v9 offset:61696\n"
    "ds_read_b64_tr_b16 a[64:65], v10 offset:61440\n"
    "ds_read_b64_tr_b16 a[66:67], v10 offset:61696\n"
    "ds_read_b64_tr_b16 a[68:69], v11 offset:61440\n"
    "ds_read_b64_tr_b16 a[70:71], v11 offset:61696\n"
    "s_waitcnt vmcnt(30) lgkmcnt(0)\n"
    "s_barrier\n"
    "s_add_u32 m0, s56, 61440\n"
    "s_add_u32 s57, %8, 626688\n"
    "buffer_load_dwordx4 v8, s[44:47], s57 offen lds\n"
    "s_add_u32 m0, s56, 65536\n"
    "s_add_u32 s57, %8, 626816\n"
    "buffer_load_dwordx4 v8, s[44:47], s57 offen lds\n"
    "s_add_u32 m0, s56, 69632\n"
    "s_add_u32 s57, %8, 626944\n"
    "buffer_load_dwordx4 v8, s[44:47], s57 offen lds\n"
    "v_add_u32 v17, 0x10000, v9\n"
    "ds_read_b64_tr_b16 a[72:73], v17 offset:8192\n"
    "ds_read_b64_tr_b16 a[74:75], v17 offset:8448\n"
    "v_add_u32 v16, 0x10000, v10\n"
    "ds_read_b64_tr_b16 a[76:77], v16 offset:8192\n"
    "ds_read_b64_tr_b16 a[78:79], v16 offset:8448\n"
    "v_add_u32 v15, 0x10000, v11\n"
    "ds_read_b64_tr_b16 a[80:81], v15 offset:8192\n"
    "ds_read_b64_tr_b16 a[82:83], v15 offset:8448\n"
    "s_waitcnt vmcnt(30) lgkmcnt(0)\n"
    "s_barrier\n"
    "s_add_u32 m0, s56, 73728\n"
    "s_add_u32 s57, %8, 663552\n"
    "buffer_load_dwordx4 v8, s[44:47], s57 offen lds\n"
    "s_add_u32 m0, s56, 77824\n"
    "s_add_u32 s57, %8, 663680\n"
    "buffer_load_dwordx4 v8, s[44:47], s57 offen lds\n"
    "s_add_u32 m0, s56, 81920\n"
    "s_add_u32 s57, %8, 663808\n"
    "buffer_load_dwordx4 v8, s[44:47], s57 offen lds\n"
    "ds_read_b64_tr_b16 a[84:85], v17 offset:20480\n"
    "ds_read_b64_tr_b16 a[86:87], v17 offset:20736\n"
    "ds_read_b64_tr_b16 a[88:89], v16 offset:20480\n"
    "ds_read_b64_tr_b16 a[90:91], v16 offset:20736\n"
    "ds_read_b64_tr_b16 a[92:93], v15 offset:20480\n"
    "ds_read_b64_tr_b16 a[94:95], v15 offset:20736\n"
    "s_waitcnt vmcnt(30) lgkmcnt(0)\n"
    "s_barrier\n"
    "s_add_u32 m0, s56, 86016\n"
    "s_add_u32 s57, %8, 700416\n"
    "buffer_load_dwordx4 v8, s[44:47], s57 offen lds\n"
    "s_add_u32 m0, s56, 90112\n"
    "s_add_u32 s57, %8, 700544\n"
    "buffer_load_dwordx4 v8, s[44:47], s57 offen lds\n"
    "s_add_u32 m0, s56, 94208\n"
    "s_add_u32 s57, %8, 700672\n"
    "buffer_load_dwordx4 v8, s[44:47], s57 offen lds\n"
    "ds_read_b64_tr_b16 a[96:97], v17 offset:32768\n"
    "ds_read_b64_tr_b16 a[98:99], v17 offset:33024\n"
    "ds_read_b64_tr_b16 a[100:101], v16 offset:32768\n"
    "ds_read_b64_tr_b16 a[102:103], v16 offset:33024\n"
    "ds_read_b64_tr_b16 a[104:105], v15 offset:32768\n"
    "ds_read_b64_tr_b16 a[106:107], v15 offset:33024\n"
    "s_waitcnt vmcnt(30) lgkmcnt(0)\n"
    "s_barrier\n"
    "s_add_u32 m0, s56, 98304\n"
    "s_add_u32 s57, %8, 737280\n"
    "buffer_load_dwordx4 v8, s[44:47], s57 offen lds\n"
    "s_add_u32 m0, s56, 102400\n"
    "s_add_u32 s57, %8, 737408\n"
    "buffer_load_dwordx4 v8, s[44:47], s57 offen lds\n"
    "s_add_u32 m0, s56, 106496\n"
    "s_add_u32 s57, %8, 737536\n"
    "buffer_load_dwordx4 v8, s[44:47], s57 offen lds\n"
    "ds_read_b64_tr_b16 a[108:109], v17 offset:45056\n"
    "ds_read_b64_tr_b16 a[110:111], v17 offset:45312\n"
    "ds_read_b64_tr_b16 a[112:113], v16 offset:45056\n"
    "ds_read_b64_tr_b16 a[114:115], v16 offset:45312\n"
    "ds_read_b64_tr_b16 a[116:117], v15 offset:45056\n"
    "ds_read_b64_tr_b16 a[118:119], v15 offset:45312\n"
    "s_waitcnt vmcnt(30) lgkmcnt(0)\n"
    "s_barrier\n"
    "s_add_u32 m0, s56, 110592\n"
    "s_add_u32 s57, %8, 774144\n"
    "buffer_load_dwordx4 v8, s[44:47], s57 offen lds\n"
    "s_add_u32 m0, s56, 114688\n"
    "s_add_u32 s57, %8, 774272\n"
    "buffer_load_dwordx4 v8, s[44:47], s57 offen lds\n"
    "s_add_u32 m0, s56, 118784\n"
    "s_add_u32 s57, %8, 774400\n"
    "buffer_load_dwordx4 v8, s[44:47], s57 offen lds\n"
    "ds_read_b64_tr_b16 a[120:121], v17 offset:57344\n"
    "ds_read_b64_tr_b16 a[122:123], v17 offset:57600\n"
    "ds_read_b64_tr_b16 a[124:125], v16 offset:57344\n"
    "ds_read_b64_tr_b16 a[126:127], v16 offset:57600\n"
    "ds_read_b64_tr_b16 a[128:129], v15 offset:57344\n"
    "ds_read_b64_tr_b16 a[130:131], v15 offset:57600\n"
    "s_waitcnt vmcnt(30) lgkmcnt(0)\n"
    "s_barrier\n"
    "s_add_u32 m0, s56, 122880\n"
    "s_add_u32 s57, %8, 811008\n"
    "buffer_load_dwordx4 v8, s[44:47], s57 offen lds\n"
    "s_add_u32 m0, s56, 126976\n"
    "s_add_u32 s57, %8, 811136\n"
    "buffer_load_dwordx4 v8, s[44:47], s57 offen lds\n"
    "s_add_u32 m0, s56, 131072\n"
    "s_add_u32 s57, %8, 811264\n"
    "buffer_load_dwordx4 v8, s[44:47], s57 offen lds\n"
    "v_add_u32 v14, 0x20000, v9\n"
    "ds_read_b64_tr_b16 a[132:133], v14 offset:4096\n"
    "ds_read_b64_tr_b16 a[134:135], v14 offset:4352\n"
    "v_add_u32 v13, 0x20000, v10\n"
    "ds_read_b64_tr_b16 a[136:137], v13 offset:4096\n"
    "ds_read_b64_tr_b16 a[138:139], v13 offset:4352\n"
    "v_add_u32 v12, 0x20000, v11\n"
    "ds_read_b64_tr_b16 a[140:141], v12 offset:4096\n"
    "ds_read_b64_tr_b16 a[142:143], v12 offset:4352\n"
    "s_waitcnt vmcnt(30) lgkmcnt(0)\n"
    "s_barrier\n"
    "s_add_u32 m0, s56, 135168\n"
    "s_add_u32 s57, %8, 847872\n"
    "buffer_load_dwordx4 v8, s[44:47], s57 offen lds\n"
    "s_add_u32 m0, s56, 139264\n"
    "s_add_u32 s57, %8, 848000\n"
    "buffer_load_dwordx4 v8, s[44:47], s57 offen lds\n"
    "s_add_u32 m0, s56, 143360\n"
    "s_add_u32 s57, %8, 848128\n"
    "buffer_load_dwordx4 v8, s[44:47], s57 offen lds\n"
    "ds_read_b64_tr_b16 a[144:145], v9\n"
    "ds_read_b64_tr_b16 a[146:147], v9 offset:256\n"
    "ds_read_b64_tr_b16 a[148:149], v10\n"
    "ds_read_b64_tr_b16 a[150:151], v10 offset:256\n"
    "ds_read_b64_tr_b16 a[152:153], v11\n"
    "ds_read_b64_tr_b16 a[154:155], v11 offset:256\n"
    "s_waitcnt vmcnt(30) lgkmcnt(0)\n"
    "s_barrier\n"
    "s_add_u32 m0, s56, 0\n"
    "s_add_u32 s57, %8, 884736\n"
    "buffer_load_dwordx4 v8, s[44:47], s57 offen lds\n"
    "s_add_u32 m0, s56, 4096\n"
    "s_add_u32 s57, %8, 884864\n"
    "buffer_load_dwordx4 v8, s[44:47], s57 offen lds\n"
    "s_add_u32 m0, s56, 8192\n"
    "s_add_u32 s57, %8, 884992\n"
    "buffer_load_dwordx4 v8, s[44:47], s57 offen lds\n"
    "ds_read_b64_tr_b16 a[156:157], v9 offset:12288\n"
    "ds_read_b64_tr_b16 a[158:159], v9 offset:12544\n"
    "ds_read_b64_tr_b16 a[160:161], v10 offset:12288\n"
    "ds_read_b64_tr_b16 a[162:163], v10 offset:12544\n"
    "ds_read_b64_tr_b16 a[164:165], v11 offset:12288\n"
    "ds_read_b64_tr_b16 a[166:167], v11 offset:12544\n"
    "s_waitcnt vmcnt(30) lgkmcnt(0)\n"
    "s_barrier\n"
    "s_add_u32 m0, s56, 12288\n"
    "s_add_u32 s57, %8, 921600\n"
    "buffer_load_dwordx4 v8, s[44:47], s57 offen lds\n"
    "s_add_u32 m0, s56, 16384\n"
    "s_add_u32 s57, %8, 921728\n"
    "buffer_load_dwordx4 v8, s[44:47], s57 offen lds\n"
    "s_add_u32 m0, s56, 20480\n"
    "s_add_u32 s57, %8, 921856\n"
    "buffer_load_dwordx4 v8, s[44:47], s57 offen lds\n"
    "ds_read_b64_tr_b16 a[168:169], v9 offset:24576\n"
    "ds_read_b64_tr_b16 a[170:171], v9 offset:24832\n"
    "ds_read_b64_tr_b16 a[172:173], v10 offset:24576\n"
    "ds_read_b64_tr_b16 a[174:175], v10 offset:24832\n"
    "ds_read_b64_tr_b16 a[176:177], v11 offset:24576\n"
    "ds_read_b64_tr_b16 a[178:179], v11 offset:24832\n"
    "s_waitcnt vmcnt(30) lgkmcnt(0)\n"
    "s_barrier\n"
    "s_add_u32 m0, s56, 24576\n"
    "s_add_u32 s57, %8, 958464\n"
    "buffer_load_dwordx4 v8, s[44:47], s57 offen lds\n"
    "s_add_u32 m0, s56, 28672\n"
    "s_add_u32 s57, %8, 958592\n"
    "buffer_load_dwordx4 v8, s[44:47], s57 offen lds\n"
    "s_add_u32 m0, s56, 32768\n"
    "s_add_u32 s57, %8, 958720\n"
    "buffer_load_dwordx4 v8, s[44:47], s57 offen lds\n"
    "ds_read_b64_tr_b16 a[180:181], v9 offset:36864\n"
    "ds_read_b64_tr_b16 a[182:183], v9 offset:37120\n"
    "ds_read_b64_tr_b16 a[184:185], v10 offset:36864\n"
    "ds_read_b64_tr_b16 a[186:187], v10 offset:37120\n"
    "ds_read_b64_tr_b16 a[188:189], v11 offset:36864\n"
    "ds_read_b64_tr_b16 a[190:191], v11 offset:37120\n"
    "s_waitcnt vmcnt(30) lgkmcnt(0)\n"
    "s_barrier\n"
    "s_add_u32 m0, s56, 36864\n"
    "s_add_u32 s57, %8, 995328\n"
    "buffer_load_dwordx4 v8, s[44:47], s57 offen lds\n"
    "s_add_u32 m0, s56, 40960\n"
    "s_add_u32 s57, %8, 995456\n"
    "buffer_load_dwordx4 v8, s[44:47], s57 offen lds\n"
    "s_add_u32 m0, s56, 45056\n"
    "s_add_u32 s57, %8, 995584\n"
    "buffer_load_dwordx4 v8, s[44:47], s57 offen lds\n"
    "ds_read_b64_tr_b16 a[192:193], v9 offset:49152\n"
    "ds_read_b64_tr_b16 a[194:195], v9 offset:49408\n"
    "ds_read_b64_tr_b16 a[196:197], v10 offset:49152\n"
    "ds_read_b64_tr_b16 a[198:199], v10 offset:49408\n"
    "ds_read_b64_tr_b16 a[200:201], v11 offset:49152\n"
    "ds_read_b64_tr_b16 a[202:203], v11 offset:49408\n"
    "s_waitcnt vmcnt(30) lgkmcnt(0)\n"
    "s_barrier\n"
    "s_add_u32 m0, s56, 49152\n"
    "s_add_u32 s57, %8, 1032192\n"
    "buffer_load_dwordx4 v8, s[44:47], s57 offen lds\n"
    "s_add_u32 m0, s56, 53248\n"
    "s_add_u32 s57, %8, 1032320\n"
    "buffer_load_dwordx4 v8, s[44:47], s57 offen lds\n"
    "s_add_u32 m0, s56, 57344\n"
    "s_add_u32 s57, %8, 1032448\n"
    "buffer_load_dwordx4 v8, s[44:47], s57 offen lds\n"
    "ds_read_b64_tr_b16 a[204:205], v9 offset:61440\n"
    "ds_read_b64_tr_b16 a[206:207], v9 offset:61696\n"
    "ds_read_b64_tr_b16 a[208:209], v10 offset:61440\n"
    "ds_read_b64_tr_b16 a[210:211], v10 offset:61696\n"
    "ds_read_b64_tr_b16 a[212:213], v11 offset:61440\n"
    "ds_read_b64_tr_b16 a[214:215], v11 offset:61696\n"
    "s_waitcnt vmcnt(30) lgkmcnt(0)\n"
    "s_barrier\n"
    "s_add_u32 m0, s56, 61440\n"
    "s_add_u32 s57, %8, 1069056\n"
    "buffer_load_dwordx4 v8, s[44:47], s57 offen lds\n"
    "s_add_u32 m0, s56, 65536\n"
    "s_add_u32 s57, %8, 1069184\n"
    "buffer_load_dwordx4 v8, s[44:47], s57 offen lds\n"
    "s_add_u32 m0, s56, 69632\n"
    "s_add_u32 s57, %8, 1069312\n"
    "buffer_load_dwordx4 v8, s[44:47], s57 offen lds\n"
    "ds_read_b64_tr_b16 a[216:217], v17 offset:8192\n"
    "ds_read_b64_tr_b16 a[218:219], v17 offset:8448\n"
    "ds_read_b64_tr_b16 a[220:221], v16 offset:8192\n"
    "ds_read_b64_tr_b16 a[222:223], v16 offset:8448\n"
    "ds_read_b64_tr_b16 a[224:225], v15 offset:8192\n"
    "ds_read_b64_tr_b16 a[226:227], v15 offset:8448\n"
    "s_waitcnt vmcnt(30) lgkmcnt(0)\n"
    "s_barrier\n"
    "s_add_u32 m0, s56, 73728\n"
    "s_add_u32 s57, %8, 1105920\n"
    "buffer_load_dwordx4 v8, s[44:47], s57 offen lds\n"
    "s_add_u32 m0, s56, 77824\n"
    "s_add_u32 s57, %8, 1106048\n"
    "buffer_load_dwordx4 v8, s[44:47], s57 offen lds\n"
    "s_add_u32 m0, s56, 81920\n"
    "s_add_u32 s57, %8, 1106176\n"
    "buffer_load_dwordx4 v8, s[44:47], s57 offen lds\n"
    "ds_read_b64_tr_b16 a[228:229], v17 offset:20480\n"
    "ds_read_b64_tr_b16 a[230:231], v17 offset:20736\n"
    "ds_read_b64_tr_b16 a[232:233], v16 offset:20480\n"
    "ds_read_b64_tr_b16 a[234:235], v16 offset:20736\n"
    "ds_read_b64_tr_b16 a[236:237], v15 offset:20480\n"
    "ds_read_b64_tr_b16 a[238:239], v15 offset:20736\n"
    "s_waitcnt vmcnt(30) lgkmcnt(0)\n"
    "s_barrier\n"
    "s_add_u32 m0, s56, 86016\n"
    "s_add_u32 s57, %8, 1142784\n"
    "buffer_load_dwordx4 v8, s[44:47], s57 offen lds\n"
    "s_add_u32 m0, s56, 90112\n"
    "s_add_u32 s57, %8, 1142912\n"
    "buffer_load_dwordx4 v8, s[44:47], s57 offen lds\n"
    "s_add_u32 m0, s56, 94208\n"
    "s_add_u32 s57, %8, 1143040\n"
    "buffer_load_dwordx4 v8, s[44:47], s57 offen lds\n"
    "ds_read_b64_tr_b16 a[240:241], v17 offset:32768\n"
    "ds_read_b64_tr_b16 a[242:243], v17 offset:33024\n"
    "ds_read_b64_tr_b16 a[244:245], v16 offset:32768\n"
    "ds_read_b64_tr_b16 a[246:247], v16 offset:33024\n"
    "ds_read_b64_tr_b16 a[248:249], v15 offset:32768\n"
    "ds_read_b64_tr_b16 a[250:251], v15 offset:33024\n"
    "s_waitcnt vmcnt(30) lgkmcnt(0)\n"
    "s_barrier\n"
    "ds_read_b64_tr_b16 a[252:253], v17 offset:45056\n"
    "ds_read_b64_tr_b16 a[254:255], v17 offset:45312\n"
    "ds_read_b64_tr_b16 v[128:129], v16 offset:45056\n"
    "ds_read_b64_tr_b16 v[130:131], v16 offset:45312\n"
    "ds_read_b64_tr_b16 v[132:133], v15 offset:45056\n"
    "ds_read_b64_tr_b16 v[134:135], v15 offset:45312\n"
    "s_waitcnt vmcnt(27) lgkmcnt(0)\n"
    "s_barrier\n"
    "ds_read_b64_tr_b16 v[136:137], v17 offset:57344\n"
    "ds_read_b64_tr_b16 v[138:139], v17 offset:57600\n"
    "ds_read_b64_tr_b16 v[140:141], v16 offset:57344\n"
    "ds_read_b64_tr_b16 v[142:143], v16 offset:57600\n"
    "ds_read_b64_tr_b16 v[144:145], v15 offset:57344\n"
    "ds_read_b64_tr_b16 v[146:147], v15 offset:57600\n"
    "s_waitcnt vmcnt(24) lgkmcnt(0)\n"
    "s_barrier\n"
    "ds_read_b64_tr_b16 v[148:149], v14 offset:4096\n"
    "ds_read_b64_tr_b16 v[150:151], v14 offset:4352\n"
    "ds_read_b64_tr_b16 v[152:153], v13 offset:4096\n"
    "ds_read_b64_tr_b16 v[154:155], v13 offset:4352\n"
    "ds_read_b64_tr_b16 v[156:157], v12 offset:4096\n"
    "ds_read_b64_tr_b16 v[158:159], v12 offset:4352\n"
    "s_waitcnt vmcnt(21) lgkmcnt(0)\n"
    "s_barrier\n"
    "ds_read_b64_tr_b16 v[160:161], v9\n"
    "ds_read_b64_tr_b16 v[162:163], v9 offset:256\n"
    "ds_read_b64_tr_b16 v[164:165], v10\n"
    "ds_read_b64_tr_b16 v[166:167], v10 offset:256\n"
    "ds_read_b64_tr_b16 v[168:169], v11\n"
    "ds_read_b64_tr_b16 v[170:171], v11 offset:256\n"
    "s_waitcnt vmcnt(18) lgkmcnt(0)\n"
    "s_barrier\n"
    "ds_read_b64_tr_b16 v[172:173], v9 offset:12288\n"
    "ds_read_b64_tr_b16 v[174:175], v9 offset:12544\n"
    "ds_read_b64_tr_b16 v[176:177], v10 offset:12288\n"
    "ds_read_b64_tr_b16 v[178:179], v10 offset:12544\n"
    "ds_read_b64_tr_b16 v[180:181], v11 offset:12288\n"
    "ds_read_b64_tr_b16 v[182:183], v11 offset:12544\n"
    "s_waitcnt vmcnt(15) lgkmcnt(0)\n"
    "s_barrier\n"
    "ds_read_b64_tr_b16 v[184:185], v9 offset:24576\n"
    "ds_read_b64_tr_b16 v[186:187], v9 offset:24832\n"
    "ds_read_b64_tr_b16 v[188:189], v10 offset:24576\n"
    "ds_read_b64_tr_b16 v[190:191], v10 offset:24832\n"
    "ds_read_b64_tr_b16 v[192:193], v11 offset:24576\n"
    "ds_read_b64_tr_b16 v[194:195], v11 offset:24832\n"
    "s_waitcnt vmcnt(12) lgkmcnt(0)\n"
    "s_barrier\n"
    "ds_read_b64_tr_b16 v[196:197], v9 offset:36864\n"
    "ds_read_b64_tr_b16 v[198:199], v9 offset:37120\n"
    "ds_read_b64_tr_b16 v[200:201], v10 offset:36864\n"
    "ds_read_b64_tr_b16 v[202:203], v10 offset:37120\n"
    "ds_read_b64_tr_b16 v[204:205], v11 offset:36864\n"
    "ds_read_b64_tr_b16 v[206:207], v11 offset:37120\n"
    "s_waitcnt vmcnt(9) lgkmcnt(0)\n"
    "s_barrier\n"
    "ds_read_b64_tr_b16 v[208:209], v9 offset:49152\n"
    "ds_read_b64_tr_b16 v[210:211], v9 offset:49408\n"
    "ds_read_b64_tr_b16 v[212:213], v10 offset:49152\n"
    "ds_read_b64_tr_b16 v[214:215], v10 offset:49408\n"
    "ds_read_b64_tr_b16 v[216:217], v11 offset:49152\n"
    "ds_read_b64_tr_b16 v[218:219], v11 offset:49408\n"
    "s_waitcnt vmcnt(6) lgkmcnt(0)\n"
    "s_barrier\n"
    "ds_read_b64_tr_b16 v[220:221], v9 offset:61440\n"
    "ds_read_b64_tr_b16 v[222:223], v9 offset:61696\n"
    "ds_read_b64_tr_b16 v[224:225], v10 offset:61440\n"
    "ds_read_b64_tr_b16 v[226:227], v10 offset:61696\n"
    "ds_read_b64_tr_b16 v[228:229], v11 offset:61440\n"
    "ds_read_b64_tr_b16 v[230:231], v11 offset:61696\n"
    "s_waitcnt vmcnt(3) lgkmcnt(0)\n"
    "s_barrier\n"
    "ds_read_b64_tr_b16 v[232:233], v17 offset:8192\n"
    "ds_read_b64_tr_b16 v[234:235], v17 offset:8448\n"
    "ds_read_b64_tr_b16 v[236:237], v16 offset:8192\n"
    "ds_read_b64_tr_b16 v[238:239], v16 offset:8448\n"
    "ds_read_b64_tr_b16 v[240:241], v15 offset:8192\n"
    "ds_read_b64_tr_b16 v[242:243], v15 offset:8448\n"
    "s_waitcnt vmcnt(0) lgkmcnt(0)\n"
    "s_barrier\n"
    "ds_read_b64_tr_b16 v[244:245], v17 offset:20480\n"
    "ds_read_b64_tr_b16 v[246:247], v17 offset:20736\n"
    "ds_read_b64_tr_b16 v[248:249], v16 offset:20480\n"
    "ds_read_b64_tr_b16 v[250:251], v16 offset:20736\n"
    "ds_read_b64_tr_b16 v[252:253], v15 offset:20480\n"
    "ds_read_b64_tr_b16 v[254:255], v15 offset:20736\n"
    "s_waitcnt lgkmcnt(0)\n"
    "s_barrier\n"
    "s_mov_b32 s53, 0\n"
    "v_mov_b32 v6, 64\n"
    ".Lwp1_%=:\n"
    "global_load_dword v5, v6, %1 sc1\n"
    "s_waitcnt vmcnt(0)\n"
    "v_readfirstlane_b32 s51, v5\n"
    "s_cmp_ge_u32 s51, 4\n"
    "s_cbranch_scc1 .Lwp1_x%=\n"
    "s_add_u32 s53, s53, 1\n"
    "s_sleep 2\n"
    "s_cmp_lt_u32 s53, s54\n"
    "s_cbranch_scc1 .Lwp1_%=\n"
    "s_mov_b32 s54, 0\n"
    "v_mov_b32 v6, 0\n"
    "v_mov_b32 v5, 1\n"
    "global_store_dword v6, v5, %1 sc1\n"
    ".Lwp1_x%=:\n"
    "s_xor_b32 s55, s51, %9\n"
    "s_mov_b32 s48, -1\n"
    "s_add_u32 s51, s48, 1\n"
    "s_lshr_b32 s52, s51, 2\n"
    "s_xor_b32 s52, s52, s55\n"
    "s_and_b32 s52, s52, 1\n"
    "s_lshl_b32 s52, s52, 13\n"
    "s_lshl_b32 s51, s51, 19\n"
    "s_add_u32 s51, s51, s52\n"
    "s_add_u32 s58, s51, %6\n"
    "s_add_u32 s59, s58, 4096\n"
    "s_add_u32 s60, s58, 16384\n"
    "s_add_u32 s61, s58, 20480\n"
    "s_sub_u32 s62, %7, 294912\n"
    "s_mov_b32 s48, -1\n"
    "s_branch .Lwwait_%=\n"
    ".Lwtop_%=:\n"
    "s_waitcnt lgkmcnt(0)\n"
    "s_barrier\n"
    "ds_read_b128 v[32:35], v0\n"
    "ds_read_b128 v[36:39], v0 offset:4096\n"
    "ds_read_b128 v[40:43], v0 offset:1024\n"
    "ds_read_b128 v[44:47], v0 offset:5120\n"
    "ds_read_b128 v[48:51], v0 offset:2048\n"
    "ds_read_b128 v[52:55], v0 offset:6144\n"
    "s_add_u32 s52, s48, 1\n"
    "s_and_b32 s52, s52, 63\n"
    "s_mul_i32 s52, s52, 256\n"
    "s_add_u32 s52, s52, %4\n"
    "v_mov_b32 v6, s52\n"
    "global_load_dword v5, v6, %1 sc1\n"
    "s_add_u32 s51, s48, 1\n"
    "s_lshr_b32 s52, s51, 2\n"
    "s_xor_b32 s52, s52, s55\n"
    "s_and_b32 s52, s52, 1\n"
    "s_lshl_b32 s52, s52, 13\n"
    "s_lshl_b32 s51, s51, 19\n"
    "s_add_u32 s51, s51, s52\n"
    "s_add_u32 s58, s51, %6\n"
    "s_add_u32 s59, s58, 4096\n"
    "s_add_u32 s60, s58, 16384\n"
    "s_add_u32 s61, s58, 20480\n"
    "s_add_u32 s63, s62, 36864\n"
    "s_waitcnt lgkmcnt(5)\n"
    "v_mfma_f32_16x16x32_bf16 v[8:11], a[0:3], v[32:35], 0\n"
    "ds_read_b128 v[56:59], v0 offset:3072\n"
    "ds_read_b128 v[60:63], v0 offset:7168\n"
    "s_waitcnt lgkmcnt(6)\n"
    "v_mfma_f32_16x16x32_bf16 v[12:15], a[0:3], v[36:39], 0\n"
    "v_mfma_f32_16x16x32_bf16 v[16:19], a[4:7], v[32:35], 0\n"
    "v_mfma_f32_16x16x32_bf16 v[20:23], a[4:7], v[36:39], 0\n"
    "v_mfma_f32_16x16x32_bf16 v[24:27], a[8:11], v[32:35], 0\n"
    "v_mfma_f32_16x16x32_bf16 v[28:31], a[8:11], v[36:39], 0\n"
    "s_waitcnt lgkmcnt(5)\n"
    "v_mfma_f32_16x16x32_bf16 v[8:11], a[12:15], v[40:43], v[8:11]\n"
    "ds_read_b128 v[32:35], v0 offset:8192\n"
    "ds_read_b128 v[36:39], v0 offset:12288\n"
    "s_waitcnt lgkmcnt(6)\n"
    "v_mfma_f32_16x16x32_bf16 v[12:15], a[12:15], v[44:47], v[12:15]\n"
    "v_mfma_f32_16x16x32_bf16 v[16:19], a[16:19], v[40:43], v[16:19]\n"
    "v_mfma_f32_16x16x32_bf16 v[20:23], a[16:19], v[44:47], v[20:23]\n"
    "v_mfma_f32_16x16x32_bf16 v[24:27], a[20:23], v[40:43], v[24:27]\n"
    "v_mfma_f32_16x16x32_bf16 v[28:31], a[20:23], v[44:47], v[28:31]\n"
    "s_waitcnt lgkmcnt(5)\n"
    "v_mfma_f32_16x16x32_bf16 v[8:11], a[24:27], v[48:51], v[8:11]\n"
    "ds_read_b128 v[40:43], v0 offset:9216\n"
    "ds_read_b128 v[44:47], v0 offset:13312\n"
    "s_waitcnt lgkmcnt(6)\n"
    "v_mfma_f32_16x16x32_bf16 v[12:15], a[24:27], v[52:55], v[12:15]\n"
    "v_mfma_f32_16x16x32_bf16 v[16:19], a[28:31], v[48:51], v[16:19]\n"
    "v_mfma_f32_16x16x32_bf16 v[20:23], a[28:31], v[52:55], v[20:23]\n"
    "v_mfma_f32_16x16x32_bf16 v[24:27], a[32:35], v[48:51], v[24:27]\n"
    "v_mfma_f32_16x16x32_bf16 v[28:31], a[32:35], v[52:55], v[28:31]\n"
    "s_waitcnt lgkmcnt(5)\n"
    "v_mfma_f32_16x16x32_bf16 v[8:11], a[36:39], v[56:59], v[8:11]\n"
    "ds_read_b128 v[48:51], v0 offset:10240\n"
    "ds_read_b128 v[52:55], v0 offset:14336\n"
    "s_waitcnt lgkmcnt(6)\n"
    "v_mfma_f32_16x16x32_bf16 v[12:15], a[36:39], v[60:63], v[12:15]\n"
    "v_mfma_f32_16x16x32_bf16 v[16:19], a[40:43], v[56:59], v[16:19]\n"
    "v_mfma_f32_16x16x32_bf16 v[20:23], a[40:43], v[60:63], v[20:23]\n"
    "v_mfma_f32_16x16x32_bf16 v[24:27], a[44:47], v[56:59], v[24:27]\n"
    "v_mfma_f32_16x16x32_bf16 v[28:31], a[44:47], v[60:63], v[28:31]\n"
    "s_waitcnt lgkmcnt(5)\n"
    "v_mfma_f32_16x16x32_bf16 v[8:11], a[48:51], v[32:35], v[8:11]\n"
    "ds_read_b128 v[56:59], v0 offset:11264\n"
    "ds_read_b128 v[60:63], v0 offset:15360\n"
    "s_waitcnt lgkmcnt(6)\n"
    "v_mfma_f32_16x16x32_bf16 v[12:15], a[48:51], v[36:39], v[12:15]\n"
    "v_mfma_f32_16x16x32_bf16 v[16:19], a[52:55], v[32:35], v[16:19]\n"
    "v_mfma_f32_16x16x32_bf16 v[20:23], a[52:55], v[36:39], v[20:23]\n"
    "v_mfma_f32_16x16x32_bf16 v[24:27], a[56:59], v[32:35], v[24:27]\n"
    "v_mfma_f32_16x16x32_bf16 v[28:31], a[56:59], v[36:39], v[28:31]\n"
    "s_waitcnt lgkmcnt(5)\n"
    "v_mfma_f32_16x16x32_bf16 v[8:11], a[60:63], v[40:43], v[8:11]\n"
    "ds_read_b128 v[32:35], v0 offset:16384\n"
    "ds_read_b128 v[36:39], v0 offset:20480\n"
    "s_waitcnt lgkmcnt(6)\n"
    "v_mfma_f32_16x16x32_bf16 v[12:15], a[60:63], v[44:47], v[12:15]\n"
    "v_mfma_f32_16x16x32_bf16 v[16:19], a[64:67], v[40:43], v[16:19]\n"
    "v_mfma_f32_16x16x32_bf16 v[20:23], a[64:67], v[44:47], v[20:23]\n"
    "v_mfma_f32_16x16x32_bf16 v[24:27], a[68:71], v[40:43], v[24:27]\n"
    "v_mfma_f32_16x16x32_bf16 v[28:31], a[68:71], v[44:47], v[28:31]\n"
    "s_waitcnt lgkmcnt(5)\n"
    "v_mfma_f32_16x16x32_bf16 v[8:11], a[72:75], v[48:51], v[8:11]\n"
    "ds_read_b128 v[40:43], v0 offset:17408\n"
    "ds_read_b128 v[44:47], v0 offset:21504\n"
    "s_waitcnt lgkmcnt(6)\n"
    "v_mfma_f32_16x16x32_bf16 v[12:15], a[72:75], v[52:55], v[12:15]\n"
    "v_mfma_f32_16x16x32_bf16 v[16:19], a[76:79], v[48:51], v[16:19]\n"
    "v_mfma_f32_16x16x32_bf16 v[20:23], a[76:79], v[52:55], v[20:23]\n"
    "v_mfma_f32_16x16x32_bf16 v[24:27], a[80:83], v[48:51], v[24:27]\n"
    "v_mfma_f32_16x16x32_bf16 v[28:31], a[80:83], v[52:55], v[28:31]\n"
    "s_waitcnt lgkmcnt(5)\n"
    "v_mfma_f32_16x16x32_bf16 v[8:11], a[84:87], v[56:59], v[8:11]\n"
    "ds_read_b128 v[48:51], v0 offset:18432\n"
    "ds_read_b128 v[52:55], v0 offset:22528\n"
    "s_waitcnt lgkmcnt(6)\n"
    "v_mfma_f32_16x16x32_bf16 v[12:15], a[84:87], v[60:63], v[12:15]\n"
    "v_mfma_f32_16x16x32_bf16 v[16:19], a[88:91], v[56:59], v[16:19]\n"
    "v_mfma_f32_16x16x32_bf16 v[20:23], a[88:91], v[60:63], v[20:23]\n"
    "v_mfma_f32_16x16x32_bf16 v[24:27], a[92:95], v[56:59], v[24:27]\n"
    "v_mfma_f32_16x16x32_bf16 v[28:31], a[92:95], v[60:63], v[28:31]\n"
    "s_waitcnt lgkmcnt(5)\n"
    "v_mfma_f32_16x16x32_bf16 v[8:11], a[96:99], v[32:35], v[8:11]\n"
    "ds_read_b128 v[56:59], v0 offset:19456\n"
    "ds_read_b128 v[60:63], v0 offset:23552\n"
    "s_waitcnt lgkmcnt(6)\n"
    "v_mfma_f32_16x16x32_bf16 v[12:15], a[96:99], v[36:39], v[12:15]\n"
    "v_mfma_f32_16x16x32_bf16 v[16:19], a[100:103], v[32:35], v[16:19]\n"
    "v_mfma_f32_16x16x32_bf16 v[20:23], a[100:103], v[36:39], v[20:23]\n"
    "v_mfma_f32_16x16x32_bf16 v[24:27], a[104:107], v[32:35], v[24:27]\n"
    "v_mfma_f32_16x16x32_bf16 v[28:31], a[104:107], v[36:39], v[28:31]\n"
    "s_waitcnt lgkmcnt(5)\n"
    "v_mfma_f32_16x16x32_bf16 v[8:11], a[108:111], v[40:43], v[8:11]\n"
    "ds_read_b128 v[32:35], v0 offset:24576\n"
    "ds_read_b128 v[36:39], v0 offset:28672\n"
    "s_waitcnt lgkmcnt(6)\n"
    "v_mfma_f32_16x16x32_bf16 v[12:15], a[108:111], v[44:47], v[12:15]\n"
    "v_mfma_f32_16x16x32_bf16 v[16:19], a[112:115], v[40:43], v[16:19]\n"
    "v_mfma_f32_16x16x32_bf16 v[20:23], a[112:115], v[44:47], v[20:23]\n"
    "s_lshl_b32 s51, %2, 2\n"
    "s_add_u32 s51, s51, 131072\n"
    "v_mov_b32 v6, s51\n"
    "s_waitcnt vmcnt(0)\n"
    "ds_write_b32 v6, v5\n"
    "v_mfma_f32_16x16x32_bf16 v[24:27], a[116:119], v[40:43], v[24:27]\n"
    "v_mfma_f32_16x16x32_bf16 v[28:31], a[116:119], v[44:47], v[28:31]\n"
    "s_waitcnt lgkmcnt(6)\n"
    "v_mfma_f32_16x16x32_bf16 v[8:11], a[120:123], v[48:51], v[8:11]\n"
    "ds_read_b128 v[40:43], v0 offset:25600\n"
    "ds_read_b128 v[44:47], v0 offset:29696\n"
    "s_waitcnt lgkmcnt(7)\n"
    "v_mfma_f32_16x16x32_bf16 v[12:15], a[120:123], v[52:55], v[12:15]\n"
    "v_mfma_f32_16x16x32_bf16 v[16:19], a[124:127], v[48:51], v[16:19]\n"
    "v_mfma_f32_16x16x32_bf16 v[20:23], a[124:127], v[52:55], v[20:23]\n"
    "s_waitcnt lgkmcnt(2)\n"
    "s_barrier\n"
    "s_cmp_eq_u32 s50, 0\n"
    "s_cbranch_scc1 .Lwc2_%=\n"
    "s_sub_u32 s51, s48, 1\n"
    "s_mul_i32 s51, s51, 64\n"
    "s_add_u32 s51, s51, %5\n"
    "v_mov_b32 v6, s51\n"
    "v_mov_b32 v5, 1\n"
    "s_mov_b64 exec, 1\n"
    "global_atomic_add v6, v5, %1\n"
    "s_mov_b64 exec, -1\n"
    ".Lwc2_%=:\n"
    "v_mov_b32 v6, 0x20000\n"
    "ds_read_b32 v5, v6\n"
    "v_mfma_f32_16x16x32_bf16 v[24:27], a[128:131], v[48:51], v[24:27]\n"
    "v_mfma_f32_16x16x32_bf16 v[28:31], a[128:131], v[52:55], v[28:31]\n"
    "v_mfma_f32_16x16x32_bf16 v[8:11], a[132:135], v[56:59], v[8:11]\n"
    "ds_read_b128 v[48:51], v0 offset:26624\n"
    "ds_read_b128 v[52:55], v0 offset:30720\n"
    "v_mfma_f32_16x16x32_bf16 v[12:15], a[132:135], v[60:63], v[12:15]\n"
    "v_mfma_f32_16x16x32_bf16 v[16:19], a[136:139], v[56:59], v[16:19]\n"
    "v_mfma_f32_16x16x32_bf16 v[20:23], a[136:139], v[60:63], v[20:23]\n"
    "v_mfma_f32_16x16x32_bf16 v[24:27], a[140:143], v[56:59], v[24:27]\n"
    "s_waitcnt lgkmcnt(2)\n"
    "v_readfirstlane_b32 s51, v5\n"
    "s_cmp_ge_u32 s51, 16\n"
    "s_cselect_b32 s49, 1, 0\n"
    "s_cmp_eq_u32 s48, 63\n"
    "s_cselect_b32 s49, 0, s49\n"
    "v_mfma_f32_16x16x32_bf16 v[28:31], a[140:143], v[60:63], v[28:31]\n"
    "s_cmp_eq_u32 s49, 0\n"
    "s_cbranch_scc1 .Lwc3_%=\n"
    "buffer_load_dwordx4 v[64:67], v2, s[40:43], s58 offen sc1\n"
    "buffer_load_dwordx4 v[68:71], v2, s[40:43], s58 offen offset:1024 sc1\n"
    "buffer_load_dwordx4 v[72:75], v2, s[40:43], s58 offen offset:2048 sc1\n"
    "buffer_load_dwordx4 v[76:79], v2, s[40:43], s58 offen offset:3072 sc1\n"
    "buffer_load_dwordx4 v[80:83], v2, s[40:43], s59 offen sc1\n"
    "buffer_load_dwordx4 v[84:87], v2, s[40:43], s59 offen offset:1024 sc1\n"
    "buffer_load_dwordx4 v[88:91], v2, s[40:43], s59 offen offset:2048 sc1\n"
    "buffer_load_dwordx4 v[92:95], v2, s[40:43], s59 offen offset:3072 sc1\n"
    "buffer_load_dwordx4 v[96:99], v2, s[40:43], s60 offen sc1\n"
    "buffer_load_dwordx4 v[100:103], v2, s[40:43], s60 offen offset:1024 sc1\n"
    "buffer_load_dwordx4 v[104:107], v2, s[40:43], s60 offen offset:2048 sc1\n"
    "buffer_load_dwordx4 v[108:111], v2, s[40:43], s60 offen offset:3072 sc1\n"
    "buffer_load_dwordx4 v[112:115], v2, s[40:43], s61 offen sc1\n"
    "buffer_load_dwordx4 v[116:119], v2, s[40:43], s61 offen offset:1024 sc1\n"
    "buffer_load_dwordx4 v[120:123], v2, s[40:43], s61 offen offset:2048 sc1\n"
    "buffer_load_dwordx4 v[124:127], v2, s[40:43], s61 offen offset:3072 sc1\n"
    ".Lwc3_%=:\n"
    "v_mfma_f32_16x16x32_bf16 v[8:11], a[144:147], v[32:35], v[8:11]\n"
    "ds_read_b128 v[56:59], v0 offset:27648\n"
    "ds_read_b128 v[60:63], v0 offset:31744\n"
    "v_mfma_f32_16x16x32_bf16 v[12:15], a[144:147], v[36:39], v[12:15]\n"
    "v_mfma_f32_16x16x32_bf16 v[16:19], a[148:151], v[32:35], v[16:19]\n"
    "v_mfma_f32_16x16x32_bf16 v[20:23], a[148:151], v[36:39], v[20:23]\n"
    "v_mfma_f32_16x16x32_bf16 v[24:27], a[152:155], v[32:35], v[24:27]\n"
    "v_mfma_f32_16x16x32_bf16 v[28:31], a[152:155], v[36:39], v[28:31]\n"
    "v_mfma_f32_16x16x32_bf16 v[8:11], a[156:159], v[40:43], v[8:11]\n"
    "ds_read_b128 v[32:35], v0 offset:32768\n"
    "ds_read_b128 v[36:39], v0 offset:36864\n"
    "v_mfma_f32_16x16x32_bf16 v[12:15], a[156:159], v[44:47], v[12:15]\n"
    "v_mfma_f32_16x16x32_bf16 v[16:19], a[160:163], v[40:43], v[16:19]\n"
    "v_mfma_f32_16x16x32_bf16 v[20:23], a[160:163], v[44:47], v[20:23]\n"
    "v_mfma_f32_16x16x32_bf16 v[24:27], a[164:167], v[40:43], v[24:27]\n"
    "v_mfma_f32_16x16x32_bf16 v[28:31], a[164:167], v[44:47], v[28:31]\n"
    "s_waitcnt lgkmcnt(5)\n"
    "v_mfma_f32_16x16x32_bf16 v[8:11], a[168:171], v[48:51], v[8:11]\n"
    "ds_read_b128 v[40:43], v0 offset:33792\n"
    "ds_read_b128 v[44:47], v0 offset:37888\n"
    "s_waitcnt lgkmcnt(6)\n"
    "v_mfma_f32_16x16x32_bf16 v[12:15], a[168:171], v[52:55], v[12:15]\n"
    "v_mfma_f32_16x16x32_bf16 v[16:19], a[172:175], v[48:51], v[16:19]\n"
    "v_mfma_f32_16x16x32_bf16 v[20:23], a[172:175], v[52:55], v[20:23]\n"
    "v_mfma_f32_16x16x32_bf16 v[24:27], a[176:179], v[48:51], v[24:27]\n"
    "v_mfma_f32_16x16x32_bf16 v[28:31], a[176:179], v[52:55], v[28:31]\n"
    "s_waitcnt lgkmcnt(5)\n"
    "v_mfma_f32_16x16x32_bf16 v[8:11], a[180:183], v[56:59], v[8:11]\n"
    "ds_read_b128 v[48:51], v0 offset:34816\n"
    "ds_read_b128 v[52:55], v0 offset:38912\n"
    "s_waitcnt lgkmcnt(6)\n"
    "v_mfma_f32_16x16x32_bf16 v[12:15], a[180:183], v[60:63], v[12:15]\n"
    "v_mfma_f32_16x16x32_bf16 v[16:19], a[184:187], v[56:59], v[16:19]\n"
    "v_mfma_f32_16x16x32_bf16 v[20:23], a[184:187], v[60:63], v[20:23]\n"
    "v_mfma_f32_16x16x32_bf16 v[24:27], a[188:191], v[56:59], v[24:27]\n"
    "v_mfma_f32_16x16x32_bf16 v[28:31], a[188:191], v[60:63], v[28:31]\n"
    "s_waitcnt lgkmcnt(5)\n"
    "v_mfma_f32_16x16x32_bf16 v[8:11], a[192:195], v[32:35], v[8:11]\n"
    "ds_read_b128 v[56:59], v0 offset:35840\n"
    "ds_read_b128 v[60:63], v0 offset:39936\n"
    "s_waitcnt lgkmcnt(6)\n"
    "v_mfma_f32_16x16x32_bf16 v[12:15], a[192:195], v[36:39], v[12:15]\n"
    "v_mfma_f32_16x16x32_bf16 v[16:19], a[196:199], v[32:35], v[16:19]\n"
    "v_mfma_f32_16x16x32_bf16 v[20:23], a[196:199], v[36:39], v[20:23]\n"
    "v_mfma_f32_16x16x32_bf16 v[24:27], a[200:203], v[32:35], v[24:27]\n"
    "v_mfma_f32_16x16x32_bf16 v[28:31], a[200:203], v[36:39], v[28:31]\n"
    "s_waitcnt lgkmcnt(5)\n"
    "v_mfma_f32_16x16x32_bf16 v[8:11], a[204:207], v[40:43], v[8:11]\n"
    "ds_read_b128 v[32:35], v0 offset:40960\n"
    "ds_read_b128 v[36:39], v0 offset:45056\n"
    "s_waitcnt lgkmcnt(6)\n"
    "v_mfma_f32_16x16x32_bf16 v[12:15], a[204:207], v[44:47], v[12:15]\n"
    "v_mfma_f32_16x16x32_bf16 v[16:19], a[208:211], v[40:43], v[16:19]\n"
    "v_mfma_f32_16x16x32_bf16 v[20:23], a[208:211], v[44:47], v[20:23]\n"
    "v_mfma_f32_16x16x32_bf16 v[24:27], a[212:215], v[40:43], v[24:27]\n"
    "v_mfma_f32_16x16x32_bf16 v[28:31], a[212:215], v[44:47], v[28:31]\n"
    "s_waitcnt lgkmcnt(5)\n"
    "v_mfma_f32_16x16x32_bf16 v[8:11], a[216:219], v[48:51], v[8:11]\n"
    "ds_read_b128 v[40:43], v0 offset:41984\n"
    "ds_read_b128 v[44:47], v0 offset:46080\n"
    "s_waitcnt lgkmcnt(6)\n"
    "v_mfma_f32_16x16x32_bf16 v[12:15], a[216:219], v[52:55], v[12:15]\n"
    "v_mfma_f32_16x16x32_bf16 v[16:19], a[220:223], v[48:51], v[16:19]\n"
    "v_mfma_f32_16x16x32_bf16 v[20:23], a[220:223], v[52:55], v[20:23]\n"
    "v_mfma_f32_16x16x32_bf16 v[24:27], a[224:227], v[48:51], v[24:27]\n"
    "v_mfma_f32_16x16x32_bf16 v[28:31], a[224:227], v[52:55], v[28:31]\n"
    "s_waitcnt lgkmcnt(5)\n"
    "v_mfma_f32_16x16x32_bf16 v[8:11], a[228:231], v[56:59], v[8:11]\n"
    "ds_read_b128 v[48:51], v0 offset:43008\n"
    "ds_read_b128 v[52:55], v0 offset:47104\n"
    "s_waitcnt lgkmcnt(6)\n"
    "v_mfma_f32_16x16x32_bf16 v[12:15], a[228:231], v[60:63], v[12:15]\n"
    "v_mfma_f32_16x16x32_bf16 v[16:19], a[232:235], v[56:59], v[16:19]\n"
    "v_mfma_f32_16x16x32_bf16 v[20:23], a[232:235], v[60:63], v[20:23]\n"
    "v_mfma_f32_16x16x32_bf16 v[24:27], a[236:239], v[56:59], v[24:27]\n"
    "v_mfma_f32_16x16x32_bf16 v[28:31], a[236:239], v[60:63], v[28:31]\n"
    "s_waitcnt lgkmcnt(5)\n"
    "v_mfma_f32_16x16x32_bf16 v[8:11], a[240:243], v[32:35], v[8:11]\n"
    "ds_read_b128 v[56:59], v0 offset:44032\n"
    "ds_read_b128 v[60:63], v0 offset:48128\n"
    "s_waitcnt lgkmcnt(6)\n"
    "v_mfma_f32_16x16x32_bf16 v[12:15], a[240:243], v[36:39], v[12:15]\n"
    "v_mfma_f32_16x16x32_bf16 v[16:19], a[244:247], v[32:35], v[16:19]\n"
    "v_mfma_f32_16x16x32_bf16 v[20:23], a[244:247], v[36:39], v[20:23]\n"
    "v_mfma_f32_16x16x32_bf16 v[24:27], a[248:251], v[32:35], v[24:27]\n"
    "v_mfma_f32_16x16x32_bf16 v[28:31], a[248:251], v[36:39], v[28:31]\n"
    "s_waitcnt lgkmcnt(5)\n"
    "v_mfma_f32_16x16x32_bf16 v[8:11], a[252:255], v[40:43], v[8:11]\n"
    "ds_read_b128 v[32:35], v0 offset:49152\n"
    "ds_read_b128 v[36:39], v0 offset:53248\n"
    "s_waitcnt lgkmcnt(6)\n"
    "v_mfma_f32_16x16x32_bf16 v[12:15], a[252:255], v[44:47], v[12:15]\n"
    "v_mfma_f32_16x16x32_bf16 v[16:19], v[128:131], v[40:43], v[16:19]\n"
    "v_mfma_f32_16x16x32_bf16 v[20:23], v[128:131], v[44:47], v[20:23]\n"
    "v_mfma_f32_16x16x32_bf16 v[24:27], v[132:135], v[40:43], v[24:27]\n"
    "v_mfma_f32_16x16x32_bf16 v[28:31], v[132:135], v[44:47], v[28:31]\n"
    "s_waitcnt lgkmcnt(5)\n"
    "v_mfma_f32_16x16x32_bf16 v[8:11], v[136:139], v[48:51], v[8:11]\n"
    "ds_read_b128 v[40:43], v0 offset:50176\n"
    "ds_read_b128 v[44:47], v0 offset:54272\n"
    "s_waitcnt lgkmcnt(6)\n"
    "v_mfma_f32_16x16x32_bf16 v[12:15], v[136:139], v[52:55], v[12:15]\n"
    "v_mfma_f32_16x16x32_bf16 v[16:19], v[140:143], v[48:51], v[16:19]\n"
    "v_mfma_f32_16x16x32_bf16 v[20:23], v[140:143], v[52:55], v[20:23]\n"
    "v_mfma_f32_16x16x32_bf16 v[24:27], v[144:147], v[48:51], v[24:27]\n"
    "v_mfma_f32_16x16x32_bf16 v[28:31], v[144:147], v[52:55], v[28:31]\n"
    "s_waitcnt lgkmcnt(5)\n"
    "v_mfma_f32_16x16x32_bf16 v[8:11], v[148:151], v[56:59], v[8:11]\n"
    "ds_read_b128 v[48:51], v0 offset:51200\n"
    "ds_read_b128 v[52:55], v0 offset:55296\n"
    "s_waitcnt lgkmcnt(6)\n"
    "v_mfma_f32_16x16x32_bf16 v[12:15], v[148:151], v[60:63], v[12:15]\n"
    "v_mfma_f32_16x16x32_bf16 v[16:19], v[152:155], v[56:59], v[16:19]\n"
    "v_mfma_f32_16x16x32_bf16 v[20:23], v[152:155], v[60:63], v[20:23]\n"
    "v_mfma_f32_16x16x32_bf16 v[24:27], v[156:159], v[56:59], v[24:27]\n"
    "v_mfma_f32_16x16x32_bf16 v[28:31], v[156:159], v[60:63], v[28:31]\n"
    "s_waitcnt lgkmcnt(5)\n"
    "v_mfma_f32_16x16x32_bf16 v[8:11], v[160:163], v[32:35], v[8:11]\n"
    "ds_read_b128 v[56:59], v0 offset:52224\n"
    "ds_read_b128 v[60:63], v0 offset:56320\n"
    "s_waitcnt lgkmcnt(6)\n"
    "v_mfma_f32_16x16x32_bf16 v[12:15], v[160:163], v[36:39], v[12:15]\n"
    "v_mfma_f32_16x16x32_bf16 v[16:19], v[164:167], v[32:35], v[16:19]\n"
    "v_mfma_f32_16x16x32_bf16 v[20:23], v[164:167], v[36:39], v[20:23]\n"
    "v_mfma_f32_16x16x32_bf16 v[24:27], v[168:171], v[32:35], v[24:27]\n"
    "v_mfma_f32_16x16x32_bf16 v[28:31], v[168:171], v[36:39], v[28:31]\n"
    "s_waitcnt lgkmcnt(5)\n"
    "v_mfma_f32_16x16x32_bf16 v[8:11], v[172:175], v[40:43], v[8:11]\n"
    "ds_read_b128 v[32:35], v0 offset:57344\n"
    "ds_read_b128 v[36:39], v0 offset:61440\n"
    "s_waitcnt lgkmcnt(6)\n"
    "v_mfma_f32_16x16x32_bf16 v[12:15], v[172:175], v[44:47], v[12:15]\n"
    "v_mfma_f32_16x16x32_bf16 v[16:19], v[176:179], v[40:43], v[16:19]\n"
    "v_mfma_f32_16x16x32_bf16 v[20:23], v[176:179], v[44:47], v[20:23]\n"
    "v_mfma_f32_16x16x32_bf16 v[24:27], v[180:183], v[40:43], v[24:27]\n"
    "v_mfma_f32_16x16x32_bf16 v[28:31], v[180:183], v[44:47], v[28:31]\n"
    "s_waitcnt lgkmcnt(5)\n"
    "v_mfma_f32_16x16x32_bf16 v[8:11], v[184:187], v[48:51], v[8:11]\n"
    "ds_read_b128 v[40:43], v0 offset:58368\n"
    "ds_read_b128 v[44:47], v0 offset:62464\n"
    "s_waitcnt lgkmcnt(6)\n"
    "v_mfma_f32_16x16x32_bf16 v[12:15], v[184:187], v[52:55], v[12:15]\n"
    "v_mfma_f32_16x16x32_bf16 v[16:19], v[188:191], v[48:51], v[16:19]\n"
    "v_mfma_f32_16x16x32_bf16 v[20:23], v[188:191], v[52:55], v[20:23]\n"
    "v_mfma_f32_16x16x32_bf16 v[24:27], v[192:195], v[48:51], v[24:27]\n"
    "s_cmp_eq_u32 s49, 0\n"
    "s_cbranch_scc1 .Lwc4_%=\n"
    "s_waitcnt vmcnt(12)\n"
    "ds_write_b128 v1, v[64:67]\n"
    "ds_write_b128 v1, v[68:71] offset:1024\n"
    "ds_write_b128 v1, v[72:75] offset:2048\n"
    "ds_write_b128 v1, v[76:79] offset:3072\n"
    ".Lwc4_%=:\n"
    "v_mfma_f32_16x16x32_bf16 v[28:31], v[192:195], v[52:55], v[28:31]\n"
    "s_waitcnt lgkmcnt(5)\n"
    "v_mfma_f32_16x16x32_bf16 v[8:11], v[196:199], v[56:59], v[8:11]\n"
    "ds_read_b128 v[48:51], v0 offset:59392\n"
    "ds_read_b128 v[52:55], v0 offset:63488\n"
    "s_waitcnt lgkmcnt(6)\n"
    "v_mfma_f32_16x16x32_bf16 v[12:15], v[196:199], v[60:63], v[12:15]\n"
    "v_mfma_f32_16x16x32_bf16 v[16:19], v[200:203], v[56:59], v[16:19]\n"
    "v_mfma_f32_16x16x32_bf16 v[20:23], v[200:203], v[60:63], v[20:23]\n"
    "v_mfma_f32_16x16x32_bf16 v[24:27], v[204:207], v[56:59], v[24:27]\n"
    "v_mfma_f32_16x16x32_bf16 v[28:31], v[204:207], v[60:63], v[28:31]\n"
    "s_waitcnt lgkmcnt(5)\n"
    "v_mfma_f32_16x16x32_bf16 v[8:11], v[208:211], v[32:35], v[8:11]\n"
    "ds_read_b128 v[56:59], v0 offset:60416\n"
    "ds_read_b128 v[60:63], v0 offset:64512\n"
    "s_cmp_eq_u32 s49, 0\n"
    "s_cbranch_scc1 .Lwc5_%=\n"
    "s_waitcnt vmcnt(8)\n"
    "ds_write_b128 v1, v[80:83] offset:4096\n"
    "ds_write_b128 v1, v[84:87] offset:5120\n"
    "ds_write_b128 v1, v[88:91] offset:6144\n"
    "ds_write_b128 v1, v[92:95] offset:7168\n"
    ".Lwc5_%=:\n"
    "s_waitcnt lgkmcnt(6)\n"
    "v_mfma_f32_16x16x32_bf16 v[12:15], v[208:211], v[36:39], v[12:15]\n"
    "v_mfma_f32_16x16x32_bf16 v[16:19], v[212:215], v[32:35], v[16:19]\n"
    "v_mfma_f32_16x16x32_bf16 v[20:23], v[212:215], v[36:39], v[20:23]\n"
    "v_mfma_f32_16x16x32_bf16 v[24:27], v[216:219], v[32:35], v[24:27]\n"
    "v_mfma_f32_16x16x32_bf16 v[28:31], v[216:219], v[36:39], v[28:31]\n"
    "s_waitcnt lgkmcnt(5)\n"
    "v_mfma_f32_16x16x32_bf16 v[8:11], v[220:223], v[40:43], v[8:11]\n"
    "s_waitcnt lgkmcnt(4)\n"
    "v_mfma_f32_16x16x32_bf16 v[12:15], v[220:223], v[44:47], v[12:15]\n"
    "v_mfma_f32_16x16x32_bf16 v[16:19], v[224:227], v[40:43], v[16:19]\n"
    "s_cmp_eq_u32 s49, 0\n"
    "s_cbranch_scc1 .Lwc6_%=\n"
    "s_waitcnt vmcnt(4)\n"
    "ds_write_b128 v1, v[96:99] offset:8192\n"
    "ds_write_b128 v1, v[100:103] offset:9216\n"
    "ds_write_b128 v1, v[104:107] offset:10240\n"
    "ds_write_b128 v1, v[108:111] offset:11264\n"
    ".Lwc6_%=:\n"
    "v_mfma_f32_16x16x32_bf16 v[20:23], v[224:227], v[44:47], v[20:23]\n"
    "v_mfma_f32_16x16x32_bf16 v[24:27], v[228:231], v[40:43], v[24:27]\n"
    "v_mfma_f32_16x16x32_bf16 v[28:31], v[228:231], v[44:47], v[28:31]\n"
    "s_waitcnt lgkmcnt(3)\n"
    "v_mfma_f32_16x16x32_bf16 v[8:11], v[232:235], v[48:51], v[8:11]\n"
    "s_waitcnt lgkmcnt(2)\n"
    "v_mfma_f32_16x16x32_bf16 v[12:15], v[232:235], v[52:55], v[12:15]\n"
    "v_mfma_f32_16x16x32_bf16 v[16:19], v[236:239], v[48:51], v[16:19]\n"
    "v_mfma_f32_16x16x32_bf16 v[20:23], v[236:239], v[52:55], v[20:23]\n"
    "v_mfma_f32_16x16x32_bf16 v[24:27], v[240:243], v[48:51], v[24:27]\n"
    "s_cmp_eq_u32 s49, 0\n"
    "s_cbranch_scc1 .Lwc7_%=\n"
    "s_waitcnt vmcnt(0)\n"
    "ds_write_b128 v1, v[112:115] offset:12288\n"
    "ds_write_b128 v1, v[116:119] offset:13312\n"
    "ds_write_b128 v1, v[120:123] offset:14336\n"
    "ds_write_b128 v1, v[124:127] offset:15360\n"
    ".Lwc7_%=:\n"
    "v_mfma_f32_16x16x32_bf16 v[28:31], v[240:243], v[52:55], v[28:31]\n"
    "s_waitcnt lgkmcnt(1)\n"
    "v_mfma_f32_16x16x32_bf16 v[8:11], v[244:247], v[56:59], v[8:11]\n"
    "s_waitcnt lgkmcnt(0)\n"
    "v_mfma_f32_16x16x32_bf16 v[12:15], v[244:247], v[60:63], v[12:15]\n"
    "v_mfma_f32_16x16x32_bf16 v[16:19], v[248:251], v[56:59], v[16:19]\n"
    "v_mfma_f32_16x16x32_bf16 v[20:23], v[248:251], v[60:63], v[20:23]\n"
    "v_mfma_f32_16x16x32_bf16 v[24:27], v[252:255], v[56:59], v[24:27]\n"
    "v_mfma_f32_16x16x32_bf16 v[28:31], v[252:255], v[60:63], v[28:31]\n"
    "buffer_store_dwordx4 v[8:11], v3, s[40:43], s62 offen sc1\n"
    "buffer_store_dwordx4 v[12:15], v3, s[40:43], s63 offen sc1\n"
    "buffer_store_dwordx4 v[16:19], v3, s[40:43], s62 offen offset:64 sc1\n"
    "buffer_store_dwordx4 v[20:23], v3, s[40:43], s63 offen offset:64 sc1\n"
    "s_nop 0\n"
    "buffer_store_dwordx4 v[24:27], v3, s[40:43], s62 offen offset:128 sc1\n"
    "s_nop 2\n"
    "buffer_store_dwordx4 v[28:31], v3, s[40:43], s63 offen offset:128 sc1\n"
    "s_cmp_lg_u32 s49, 0\n"
    "s_cbranch_scc1 .Lwgo_%=\n"
    "s_waitcnt vmcnt(0) lgkmcnt(0)\n"
    "s_barrier\n"
    "s_cmp_eq_u32 %2, 0\n"
    "s_cselect_b32 s50, 1, 0\n"
    "s_cmp_eq_u32 s50, 0\n"
    "s_cbranch_scc1 .Lwc8_%=\n"
    "s_sub_u32 s51, s48, 0\n"
    "s_mul_i32 s51, s51, 64\n"
    "s_add_u32 s51, s51, %5\n"
    "v_mov_b32 v6, s51\n"
    "v_mov_b32 v5, 1\n"
    "s_mov_b64 exec, 1\n"
    "global_atomic_add v6, v5, %1\n"
    "s_mov_b64 exec, -1\n"
    ".Lwc8_%=:\n"
    "s_cmp_eq_u32 s48, 63\n"
    "s_cbranch_scc1 .Lwend_%=\n"
    ".Lwwait_%=:\n"
    "s_cmp_eq_u32 %2, 0\n"
    "s_cbranch_scc0 .Lwnp_%=\n"
    "s_add_u32 s52, s48, 1\n"
    "s_and_b32 s52, s52, 63\n"
    "s_mul_i32 s52, s52, 256\n"
    "s_add_u32 s52, s52, %4\n"
    "s_mov_b32 s53, 0\n"
    "v_mov_b32 v6, s52\n"
    ".Lwp9_%=:\n"
    "global_load_dword v5, v6, %1 sc1\n"
    "s_waitcnt vmcnt(0)\n"
    "v_readfirstlane_b32 s51, v5\n"
    "s_cmp_ge_u32 s51, 16\n"
    "s_cbranch_scc1 .Lwp9_x%=\n"
    "s_add_u32 s53, s53, 1\n"
    "s_sleep 2\n"
    "s_cmp_lt_u32 s53, s54\n"
    "s_cbranch_scc1 .Lwp9_%=\n"
    "s_mov_b32 s54, 0\n"
    "v_mov_b32 v6, 0\n"
    "v_mov_b32 v5, 1\n"
    "global_store_dword v6, v5, %1 sc1\n"
    ".Lwp9_x%=:\n"
    ".Lwnp_%=:\n"
    "s_barrier\n"
    "buffer_load_dwordx4 v[64:67], v2, s[40:43], s58 offen sc1\n"
    "buffer_load_dwordx4 v[68:71], v2, s[40:43], s58 offen offset:1024 sc1\n"
    "buffer_load_dwordx4 v[72:75], v2, s[40:43], s58 offen offset:2048 sc1\n"
    "buffer_load_dwordx4 v[76:79], v2, s[40:43], s58 offen offset:3072 sc1\n"
    "buffer_load_dwordx4 v[80:83], v2, s[40:43], s59 offen sc1\n"
    "buffer_load_dwordx4 v[84:87], v2, s[40:43], s59 offen offset:1024 sc1\n"
    "buffer_load_dwordx4 v[88:91], v2, s[40:43], s59 offen offset:2048 sc1\n"
    "buffer_load_dwordx4 v[92:95], v2, s[40:43], s59 offen offset:3072 sc1\n"
    "buffer_load_dwordx4 v[96:99], v2, s[40:43], s60 offen sc1\n"
    "buffer_load_dwordx4 v[100:103], v2, s[40:43], s60 offen offset:1024 sc1\n"
    "buffer_load_dwordx4 v[104:107], v2, s[40:43], s60 offen offset:2048 sc1\n"
    "buffer_load_dwordx4 v[108:111], v2, s[40:43], s60 offen offset:3072 sc1\n"
    "buffer_load_dwordx4 v[112:115], v2, s[40:43], s61 offen sc1\n"
    "buffer_load_dwordx4 v[116:119], v2, s[40:43], s61 offen offset:1024 sc1\n"
    "buffer_load_dwordx4 v[120:123], v2, s[40:43], s61 offen offset:2048 sc1\n"
    "buffer_load_dwordx4 v[124:127], v2, s[40:43], s61 offen offset:3072 sc1\n"
    "s_waitcnt vmcnt(15)\n"
    "ds_write_b128 v1, v[64:67]\n"
    "s_waitcnt vmcnt(14)\n"
    "ds_write_b128 v1, v[68:71] offset:1024\n"
    "s_waitcnt vmcnt(13)\n"
    "ds_write_b128 v1, v[72:75] offset:2048\n"
    "s_waitcnt vmcnt(12)\n"
    "ds_write_b128 v1, v[76:79] offset:3072\n"
    "s_waitcnt vmcnt(11)\n"
    "ds_write_b128 v1, v[80:83] offset:4096\n"
    "s_waitcnt vmcnt(10)\n"
    "ds_write_b128 v1, v[84:87] offset:5120\n"
    "s_waitcnt vmcnt(9)\n"
    "ds_write_b128 v1, v[88:91] offset:6144\n"
    "s_waitcnt vmcnt(8)\n"
    "ds_write_b128 v1, v[92:95] offset:7168\n"
    "s_waitcnt vmcnt(7)\n"
    "ds_write_b128 v1, v[96:99] offset:8192\n"
    "s_waitcnt vmcnt(6)\n"
    "ds_write_b128 v1, v[100:103] offset:9216\n"
    "s_waitcnt vmcnt(5)\n"
    "ds_write_b128 v1, v[104:107] offset:10240\n"
    "s_waitcnt vmcnt(4)\n"
    "ds_write_b128 v1, v[108:111] offset:11264\n"
    "s_waitcnt vmcnt(3)\n"
    "ds_write_b128 v1, v[112:115] offset:12288\n"
    "s_waitcnt vmcnt(2)\n"
    "ds_write_b128 v1, v[116:119] offset:13312\n"
    "s_waitcnt vmcnt(1)\n"
    "ds_write_b128 v1, v[120:123] offset:14336\n"
    "s_waitcnt vmcnt(0)\n"
    "ds_write_b128 v1, v[124:127] offset:15360\n"
    "s_mov_b32 s50, 0\n"
    "s_branch .Lwnext_%=\n"
    ".Lwgo_%=:\n"
    "s_cmp_eq_u32 %2, 0\n"
    "s_cselect_b32 s50, 1, 0\n"
    ".Lwnext_%=:\n"
    "s_add_u32 s48, s48, 1\n"
    "s_add_u32 s62, s62, 294912\n"
    "v_xor_b32 v0, 0x10000, v0\n"
    "v_xor_b32 v1, 0x10000, v1\n"
    "s_branch .Lwtop_%=\n"
    ".Lwend_%=:\n"
    "s_waitcnt vmcnt(0)\n"
    "s_endpgm\n"
    ".p2align 6\n"
    ".Lrole3_%=:\n"
    "s_mov_b64 s[40:41], %0\n"
    "s_and_b32 s41, s41, 0xffff\n"
    "s_mov_b32 s42, 0xc800000\n"
    "s_mov_b32 s43, 0x20000\n"
    "s_mov_b32 s54, 131072\n"
    "v_mbcnt_lo_u32_b32 v92, -1, 0\n"
    "v_mbcnt_hi_u32_b32 v92, -1, v92\n"
    "s_lshl_b32 s51, %2, 8\n"
    "s_add_u32 s51, s51, 147456\n"
    "v_lshl_add_u32 v92, v92, 2, s51\n"
    "ds_read_b32 v0, v92\n"
    "ds_read_b32 v1, v92 offset:1024\n"
    "ds_read_b32 v2, v92 offset:2048\n"
    "ds_read_b32 v3, v92 offset:3072\n"
    "ds_read_b32 v4, v92 offset:4096\n"
    "ds_read_b32 v5, v92 offset:5120\n"
    "ds_read_b32 v6, v92 offset:6144\n"
    "ds_read_b32 v7, v92 offset:7168\n"
    "ds_read_b32 v8, v92 offset:8192\n"
    "ds_read_b32 v9, v92 offset:9216\n"
    "ds_read_b32 v72, v92 offset:10240\n"
    "ds_read_b32 v90, v92 offset:11264\n"
    "ds_read_b32 v91, v92 offset:12288\n"
    "s_waitcnt lgkmcnt(0)\n"
    "s_barrier\n"
    "v_mbcnt_lo_u32_b32 v92, -1, 0\n"
    "v_mbcnt_hi_u32_b32 v92, -1, v92\n"
    "v_add_u32 v93, 0, v92\n"
    "v_mul_u32_u24 v94, 7282, v93\n"
    "v_lshrrev_b32 v94, 16, v94\n"
    "v_mul_u32_u24 v94, 3312, v94\n"
    "v_lshl_add_u32 v80, v93, 4, v94\n"
    "v_add_u32 v93, 64, v92\n"
    "v_mul_u32_u24 v94, 7282, v93\n"
    "v_lshrrev_b32 v94, 16, v94\n"
    "v_mul_u32_u24 v94, 3312, v94\n"
    "v_lshl_add_u32 v81, v93, 4, v94\n"
    "v_add_u32 v93, 128, v92\n"
    "v_mul_u32_u24 v94, 7282, v93\n"
    "v_lshrrev_b32 v94, 16, v94\n"
    "v_mul_u32_u24 v94, 3312, v94\n"
    "v_lshl_add_u32 v82, v93, 4, v94\n"
    "v_add_u32 v93, 192, v92\n"
    "v_mul_u32_u24 v94, 7282, v93\n"
    "v_lshrrev_b32 v94, 16, v94\n"
    "v_mul_u32_u24 v94, 3312, v94\n"
    "v_lshl_add_u32 v83, v93, 4, v94\n"
    "v_add_u32 v93, 256, v92\n"
    "v_mul_u32_u24 v94, 7282, v93\n"
    "v_lshrrev_b32 v94, 16, v94\n"
    "v_mul_u32_u24 v94, 3312, v94\n"
    "v_lshl_add_u32 v84, v93, 4, v94\n"
    "v_add_u32 v93, 320, v92\n"
    "v_mul_u32_u24 v94, 7282, v93\n"
    "v_lshrrev_b32 v94, 16, v94\n"
    "v_mul_u32_u24 v94, 3312, v94\n"
    "v_lshl_add_u32 v85, v93, 4, v94\n"
    "v_add_u32 v93, 384, v92\n"
    "v_mul_u32_u24 v94, 7282, v93\n"
    "v_lshrrev_b32 v94, 16, v94\n"
    "v_mul_u32_u24 v94, 3312, v94\n"
    "v_lshl_add_u32 v86, v93, 4, v94\n"
    "v_add_u32 v93, 448, v92\n"
    "v_mul_u32_u24 v94, 7282, v93\n"
    "v_lshrrev_b32 v94, 16, v94\n"
    "v_mul_u32_u24 v94, 3312, v94\n"
    "v_lshl_add_u32 v87, v93, 4, v94\n"
    "v_add_u32 v93, 512, v92\n"
    "v_mul_u32_u24 v94, 7282, v93\n"
    "v_lshrrev_b32 v94, 16, v94\n"
    "v_mul_u32_u24 v94, 3312, v94\n"
    "v_lshl_add_u32 v88, v93, 4, v94\n"
    "s_min_u32 s51, %2, 2\n"
    "s_mul_i32 s56, s51, 9216\n"
    "s_mul_i32 s51, s51, 1152\n"
    "s_add_u32 s55, s51, %8\n"
    "s_mov_b64 s[44:45], %10\n"
    "s_and_b32 s45, s45, 0xffff\n"
    "s_mov_b32 s46, 0x1e6000\n"
    "s_mov_b32 s47, 0x20000\n"
    "s_cmp_lt_u32 %2, 3\n"
    "s_cselect_b32 s49, 1, 0\n"
    "s_cmp_eq_u32 s49, 0\n"
    "s_cbranch_scc1 .Lac1_%=\n"
    "s_add_u32 s57, s55, 0\n"
    "s_add_u32 m0, s56, 0\n"
    "s_nop 0\n"
    "buffer_load_dwordx4 v80, s[44:47], s57 offen lds\n"
    "s_add_u32 m0, s56, 1024\n"
    "s_nop 0\n"
    "buffer_load_dwordx4 v81, s[44:47], s57 offen lds\n"
    "s_add_u32 m0, s56, 2048\n"
    "s_nop 0\n"
    "buffer_load_dwordx4 v82, s[44:47], s57 offen lds\n"
    "s_add_u32 m0, s56, 3072\n"
    "s_nop 0\n"
    "buffer_load_dwordx4 v83, s[44:47], s57 offen lds\n"
    "s_add_u32 m0, s56, 4096\n"
    "s_nop 0\n"
    "buffer_load_dwordx4 v84, s[44:47], s57 offen lds\n"
    "s_add_u32 m0, s56, 5120\n"
    "s_nop 0\n"
    "buffer_load_dwordx4 v85, s[44:47], s57 offen lds\n"
    "s_add_u32 m0, s56, 6144\n"
    "s_nop 0\n"
    "buffer_load_dwordx4 v86, s[44:47], s57 offen lds\n"
    "s_add_u32 m0, s56, 7168\n"
    "s_nop 0\n"
    "buffer_load_dwordx4 v87, s[44:47], s57 offen lds\n"
    "s_add_u32 m0, s56, 8192\n"
    "s_nop 0\n"
    "buffer_load_dwordx4 v88, s[44:47], s57 offen lds\n"
    ".Lac1_%=:\n"
    "s_cmp_eq_u32 s49, 0\n"
    "s_cbranch_scc1 .Lac2_%=\n"
    "s_add_u32 s57, s55, 221184\n"
    "s_add_u32 m0, s56, 27648\n"
    "s_nop 0\n"
    "buffer_load_dwordx4 v80, s[44:47], s57 offen lds\n"
    "s_add_u32 m0, s56, 28672\n"
    "s_nop 0\n"
    "buffer_load_dwordx4 v81, s[44:47], s57 offen lds\n"
    "s_add_u32 m0, s56, 29696\n"
    "s_nop 0\n"
    "buffer_load_dwordx4 v82, s[44:47], s57 offen lds\n"
    "s_add_u32 m0, s56, 30720\n"
    "s_nop 0\n"
    "buffer_load_dwordx4 v83, s[44:47], s57 offen lds\n"
    "s_add_u32 m0, s56, 31744\n"
    "s_nop 0\n"
    "buffer_load_dwordx4 v84, s[44:47], s57 offen lds\n"
    "s_add_u32 m0, s56, 32768\n"
    "s_nop 0\n"
    "buffer_load_dwordx4 v85, s[44:47], s57 offen lds\n"
    "s_add_u32 m0, s56, 33792\n"
    "s_nop 0\n"
    "buffer_load_dwordx4 v86, s[44:47], s57 offen lds\n"
    "s_add_u32 m0, s56, 34816\n"
    "s_nop 0\n"
    "buffer_load_dwordx4 v87, s[44:47], s57 offen lds\n"
    "s_add_u32 m0, s56, 35840\n"
    "s_nop 0\n"
    "buffer_load_dwordx4 v88, s[44:47], s57 offen lds\n"
    ".Lac2_%=:\n"
    "s_cmp_eq_u32 s49, 0\n"
    "s_cbranch_scc1 .Lac3_%=\n"
    "s_add_u32 s57, s55, 442368\n"
    "s_add_u32 m0, s56, 55296\n"
    "s_nop 0\n"
    "buffer_load_dwordx4 v80, s[44:47], s57 offen lds\n"
    "s_add_u32 m0, s56, 56320\n"
    "s_nop 0\n"
    "buffer_load_dwordx4 v81, s[44:47], s57 offen lds\n"
    "s_add_u32 m0, s56, 57344\n"
    "s_nop 0\n"
    "buffer_load_dwordx4 v82, s[44:47], s57 offen lds\n"
    "s_add_u32 m0, s56, 58368\n"
    "s_nop 0\n"
    "buffer_load_dwordx4 v83, s[44:47], s57 offen lds\n"
    "s_add_u32 m0, s56, 59392\n"
    "s_nop 0\n"
    "buffer_load_dwordx4 v84, s[44:47], s57 offen lds\n"
    "s_add_u32 m0, s56, 60416\n"
    "s_nop 0\n"
    "buffer_load_dwordx4 v85, s[44:47], s57 offen lds\n"
    "s_add_u32 m0, s56, 61440\n"
    "s_nop 0\n"
    "buffer_load_dwordx4 v86, s[44:47], s57 offen lds\n"
    "s_add_u32 m0, s56, 62464\n"
    "s_nop 0\n"
    "buffer_load_dwordx4 v87, s[44:47], s57 offen lds\n"
    "s_add_u32 m0, s56, 63488\n"
    "s_nop 0\n"
    "buffer_load_dwordx4 v88, s[44:47], s57 offen lds\n"
    ".Lac3_%=:\n"
    "s_cmp_eq_u32 s49, 0\n"
    "s_cbranch_scc1 .Lac4_%=\n"
    "s_add_u32 s57, s55, 663552\n"
    "s_add_u32 m0, s56, 82944\n"
    "s_nop 0\n"
    "buffer_load_dwordx4 v80, s[44:47], s57 offen lds\n"
    "s_add_u32 m0, s56, 83968\n"
    "s_nop 0\n"
    "buffer_load_dwordx4 v81, s[44:47], s57 offen lds\n"
    "s_add_u32 m0, s56, 84992\n"
    "s_nop 0\n"
    "buffer_load_dwordx4 v82, s[44:47], s57 offen lds\n"
    "s_add_u32 m0, s56, 86016\n"
    "s_nop 0\n"
    "buffer_load_dwordx4 v83, s[44:47], s57 offen lds\n"
    "s_add_u32 m0, s56, 87040\n"
    "s_nop 0\n"
    "buffer_load_dwordx4 v84, s[44:47], s57 offen lds\n"
    "s_add_u32 m0, s56, 88064\n"
    "s_nop 0\n"
    "buffer_load_dwordx4 v85, s[44:47], s57 offen lds\n"
    "s_add_u32 m0, s56, 89088\n"
    "s_nop 0\n"
    "buffer_load_dwordx4 v86, s[44:47], s57 offen lds\n"
    "s_add_u32 m0, s56, 90112\n"
    "s_nop 0\n"
    "buffer_load_dwordx4 v87, s[44:47], s57 offen lds\n"
    "s_add_u32 m0, s56, 91136\n"
    "s_nop 0\n"
    "buffer_load_dwordx4 v88, s[44:47], s57 offen lds\n"
    ".Lac4_%=:\n"
    "s_cmp_eq_u32 s49, 0\n"
    "s_cbranch_scc1 .Lac5_%=\n"
    "s_add_u32 s57, s55, 884736\n"
    "s_add_u32 m0, s56, 110592\n"
    "s_nop 0\n"
    "buffer_load_dwordx4 v80, s[44:47], s57 offen lds\n"
    "s_add_u32 m0, s56, 111616\n"
    "s_nop 0\n"
    "buffer_load_dwordx4 v81, s[44:47], s57 offen lds\n"
    "s_add_u32 m0, s56, 112640\n"
    "s_nop 0\n"
    "buffer_load_dwordx4 v82, s[44:47], s57 offen lds\n"
    "s_add_u32 m0, s56, 113664\n"
    "s_nop 0\n"
    "buffer_load_dwordx4 v83, s[44:47], s57 offen lds\n"
    "s_add_u32 m0, s56, 114688\n"
    "s_nop 0\n"
    "buffer_load_dwordx4 v84, s[44:47], s57 offen lds\n"
    "s_add_u32 m0, s56, 115712\n"
    "s_nop 0\n"
    "buffer_load_dwordx4 v85, s[44:47], s57 offen lds\n"
    "s_add_u32 m0, s56, 116736\n"
    "s_nop 0\n"
    "buffer_load_dwordx4 v86, s[44:47], s57 offen lds\n"
    "s_add_u32 m0, s56, 117760\n"
    "s_nop 0\n"
    "buffer_load_dwordx4 v87, s[44:47], s57 offen lds\n"
    "s_add_u32 m0, s56, 118784\n"
    "s_nop 0\n"
    "buffer_load_dwordx4 v88, s[44:47], s57 offen lds\n"
    ".Lac5_%=:\n"
    "s_waitcnt vmcnt(36) lgkmcnt(0)\n"
    "s_barrier\n"
    "ds_read_b64_tr_b16 a[0:1], v90\n"
    "ds_read_b64_tr_b16 a[2:3], v90 offset:576\n"
    "ds_read_b64_tr_b16 a[72:73], v90 offset:9216\n"
    "ds_read_b64_tr_b16 a[74:75], v90 offset:9792\n"
    "ds_read_b64_tr_b16 a[144:145], v90 offset:18432\n"
    "ds_read_b64_tr_b16 a[146:147], v90 offset:19008\n"
    "ds_read_b64_tr_b16 a[216:217], v91\n"
    "ds_read_b64_tr_b16 a[218:219], v91 offset:576\n"
    "ds_read_b64_tr_b16 a[4:5], v90 offset:4608\n"
    "ds_read_b64_tr_b16 a[6:7], v90 offset:5184\n"
    "ds_read_b64_tr_b16 a[76:77], v90 offset:13824\n"
    "ds_read_b64_tr_b16 a[78:79], v90 offset:14400\n"
    "ds_read_b64_tr_b16 a[148:149], v90 offset:23040\n"
    "ds_read_b64_tr_b16 a[150:151], v90 offset:23616\n"
    "ds_read_b64_tr_b16 a[220:221], v91 offset:4608\n"
    "ds_read_b64_tr_b16 a[222:223], v91 offset:5184\n"
    "s_waitcnt vmcnt(27) lgkmcnt(0)\n"
    "s_barrier\n"
    "s_cmp_eq_u32 s49, 0\n"
    "s_cbranch_scc1 .Lac6_%=\n"
    "s_add_u32 s57, s55, 1105920\n"
    "s_add_u32 m0, s56, 0\n"
    "s_nop 0\n"
    "buffer_load_dwordx4 v80, s[44:47], s57 offen lds\n"
    "s_add_u32 m0, s56, 1024\n"
    "s_nop 0\n"
    "buffer_load_dwordx4 v81, s[44:47], s57 offen lds\n"
    "s_add_u32 m0, s56, 2048\n"
    "s_nop 0\n"
    "buffer_load_dwordx4 v82, s[44:47], s57 offen lds\n"
    "s_add_u32 m0, s56, 3072\n"
    "s_nop 0\n"
    "buffer_load_dwordx4 v83, s[44:47], s57 offen lds\n"
    "s_add_u32 m0, s56, 4096\n"
    "s_nop 0\n"
    "buffer_load_dwordx4 v84, s[44:47], s57 offen lds\n"
    "s_add_u32 m0, s56, 5120\n"
    "s_nop 0\n"
    "buffer_load_dwordx4 v85, s[44:47], s57 offen lds\n"
    "s_add_u32 m0, s56, 6144\n"
    "s_nop 0\n"
    "buffer_load_dwordx4 v86, s[44:47], s57 offen lds\n"
    "s_add_u32 m0, s56, 7168\n"
    "s_nop 0\n"
    "buffer_load_dwordx4 v87, s[44:47], s57 offen lds\n"
    "s_add_u32 m0, s56, 8192\n"
    "s_nop 0\n"
    "buffer_load_dwordx4 v88, s[44:47], s57 offen lds\n"
    ".Lac6_%=:\n"
    "ds_read_b64_tr_b16 a[8:9], v90 offset:27648\n"
    "ds_read_b64_tr_b16 a[10:11], v90 offset:28224\n"
    "ds_read_b64_tr_b16 a[80:81], v90 offset:36864\n"
    "ds_read_b64_tr_b16 a[82:83], v90 offset:37440\n"
    "ds_read_b64_tr_b16 a[152:153], v90 offset:46080\n"
    "ds_read_b64_tr_b16 a[154:155], v90 offset:46656\n"
    "ds_read_b64_tr_b16 a[224:225], v91 offset:27648\n"
    "ds_read_b64_tr_b16 a[226:227], v91 offset:28224\n"
    "ds_read_b64_tr_b16 a[12:13], v90 offset:32256\n"
    "ds_read_b64_tr_b16 a[14:15], v90 offset:32832\n"
    "ds_read_b64_tr_b16 a[84:85], v90 offset:41472\n"
    "ds_read_b64_tr_b16 a[86:87], v90 offset:42048\n"
    "ds_read_b64_tr_b16 a[156:157], v90 offset:50688\n"
    "ds_read_b64_tr_b16 a[158:159], v90 offset:51264\n"
    "ds_read_b64_tr_b16 a[228:229], v91 offset:32256\n"
    "ds_read_b64_tr_b16 a[230:231], v91 offset:32832\n"
    "s_waitcnt vmcnt(27) lgkmcnt(0)\n"
    "s_barrier\n"
    "s_cmp_eq_u32 s49, 0\n"
    "s_cbranch_scc1 .Lac7_%=\n"
    "s_add_u32 s57, s55, 1327104\n"
    "s_add_u32 m0, s56, 27648\n"
    "s_nop 0\n"
    "buffer_load_dwordx4 v80, s[44:47], s57 offen lds\n"
    "s_add_u32 m0, s56, 28672\n"
    "s_nop 0\n"
    "buffer_load_dwordx4 v81, s[44:47], s57 offen lds\n"
    "s_add_u32 m0, s56, 29696\n"
    "s_nop 0\n"
    "buffer_load_dwordx4 v82, s[44:47], s57 offen lds\n"
    "s_add_u32 m0, s56, 30720\n"
    "s_nop 0\n"
    "buffer_load_dwordx4 v83, s[44:47], s57 offen lds\n"
    "s_add_u32 m0, s56, 31744\n"
    "s_nop 0\n"
    "buffer_load_dwordx4 v84, s[44:47], s57 offen lds\n"
    "s_add_u32 m0, s56, 32768\n"
    "s_nop 0\n"
    "buffer_load_dwordx4 v85, s[44:47], s57 offen lds\n"
    "s_add_u32 m0, s56, 33792\n"
    "s_nop 0\n"
    "buffer_load_dwordx4 v86, s[44:47], s57 offen lds\n"
    "s_add_u32 m0, s56, 34816\n"
    "s_nop 0\n"
    "buffer_load_dwordx4 v87, s[44:47], s57 offen lds\n"
    "s_add_u32 m0, s56, 35840\n"
    "s_nop 0\n"
    "buffer_load_dwordx4 v88, s[44:47], s57 offen lds\n"
    ".Lac7_%=:\n"
    "ds_read_b64_tr_b16 a[16:17], v90 offset:55296\n"
    "ds_read_b64_tr_b16 a[18:19], v90 offset:55872\n"
    "ds_read_b64_tr_b16 a[88:89], v90 offset:64512\n"
    "ds_read_b64_tr_b16 a[90:91], v90 offset:65088\n"
    "v_add_u32 v99, 0x10000, v90\n"
    "ds_read_b64_tr_b16 a[160:161], v99 offset:8192\n"
    "ds_read_b64_tr_b16 a[162:163], v99 offset:8768\n"
    "ds_read_b64_tr_b16 a[232:233], v91 offset:55296\n"
    "ds_read_b64_tr_b16 a[234:235], v91 offset:55872\n"
    "ds_read_b64_tr_b16 a[20:21], v90 offset:59904\n"
    "ds_read_b64_tr_b16 a[22:23], v90 offset:60480\n"
    "ds_read_b64_tr_b16 a[92:93], v99 offset:3584\n"
    "ds_read_b64_tr_b16 a[94:95], v99 offset:4160\n"
    "ds_read_b64_tr_b16 a[164:165], v99 offset:12800\n"
    "ds_read_b64_tr_b16 a[166:167], v99 offset:13376\n"
    "ds_read_b64_tr_b16 a[236:237], v91 offset:59904\n"
    "ds_read_b64_tr_b16 a[238:239], v91 offset:60480\n"
    "s_waitcnt vmcnt(27) lgkmcnt(0)\n"
    "s_barrier\n"
    "s_cmp_eq_u32 s49, 0\n"
    "s_cbranch_scc1 .Lac8_%=\n"
    "s_add_u32 s57, s55, 1548288\n"
    "s_add_u32 m0, s56, 55296\n"
    "s_nop 0\n"
    "buffer_load_dwordx4 v80, s[44:47], s57 offen lds\n"
    "s_add_u32 m0, s56, 56320\n"
    "s_nop 0\n"
    "buffer_load_dwordx4 v81, s[44:47], s57 offen lds\n"
    "s_add_u32 m0, s56, 57344\n"
    "s_nop 0\n"
    "buffer_load_dwordx4 v82, s[44:47], s57 offen lds\n"
    "s_add_u32 m0, s56, 58368\n"
    "s_nop 0\n"
    "buffer_load_dwordx4 v83, s[44:47], s57 offen lds\n"
    "s_add_u32 m0, s56, 59392\n"
    "s_nop 0\n"
    "buffer_load_dwordx4 v84, s[44:47], s57 offen lds\n"
    "s_add_u32 m0, s56, 60416\n"
    "s_nop 0\n"
    "buffer_load_dwordx4 v85, s[44:47], s57 offen lds\n"
    "s_add_u32 m0, s56, 61440\n"
    "s_nop 0\n"
    "buffer_load_dwordx4 v86, s[44:47], s57 offen lds\n"
    "s_add_u32 m0, s56, 62464\n"
    "s_nop 0\n"
    "buffer_load_dwordx4 v87, s[44:47], s57 offen lds\n"
    "s_add_u32 m0, s56, 63488\n"
    "s_nop 0\n"
    "buffer_load_dwordx4 v88, s[44:47], s57 offen lds\n"
    ".Lac8_%=:\n"
    "ds_read_b64_tr_b16 a[24:25], v99 offset:17408\n"
    "ds_read_b64_tr_b16 a[26:27], v99 offset:17984\n"
    "ds_read_b64_tr_b16 a[96:97], v99 offset:26624\n"
    "ds_read_b64_tr_b16 a[98:99], v99 offset:27200\n"
    "ds_read_b64_tr_b16 a[168:169], v99 offset:35840\n"
    "ds_read_b64_tr_b16 a[170:171], v99 offset:36416\n"
    "v_add_u32 v98, 0x10000, v91\n"
    "ds_read_b64_tr_b16 a[240:241], v98 offset:17408\n"
    "ds_read_b64_tr_b16 a[242:243], v98 offset:17984\n"
    "ds_read_b64_tr_b16 a[28:29], v99 offset:22016\n"
    "ds_read_b64_tr_b16 a[30:31], v99 offset:22592\n"
    "ds_read_b64_tr_b16 a[100:101], v99 offset:31232\n"
    "ds_read_b64_tr_b16 a[102:103], v99 offset:31808\n"
    "ds_read_b64_tr_b16 a[172:173], v99 offset:40448\n"
    "ds_read_b64_tr_b16 a[174:175], v99 offset:41024\n"
    "ds_read_b64_tr_b16 a[244:245], v98 offset:22016\n"
    "ds_read_b64_tr_b16 a[246:247], v98 offset:22592\n"
    "s_waitcnt vmcnt(27) lgkmcnt(0)\n"
    "s_barrier\n"
    "s_cmp_eq_u32 s49, 0\n"
    "s_cbranch_scc1 .Lac9_%=\n"
    "s_add_u32 s57, s55, 1769472\n"
    "s_add_u32 m0, s56, 82944\n"
    "s_nop 0\n"
    "buffer_load_dwordx4 v80, s[44:47], s57 offen lds\n"
    "s_add_u32 m0, s56, 83968\n"
    "s_nop 0\n"
    "buffer_load_dwordx4 v81, s[44:47], s57 offen lds\n"
    "s_add_u32 m0, s56, 84992\n"
    "s_nop 0\n"
    "buffer_load_dwordx4 v82, s[44:47], s57 offen lds\n"
    "s_add_u32 m0, s56, 86016\n"
    "s_nop 0\n"
    "buffer_load_dwordx4 v83, s[44:47], s57 offen lds\n"
    "s_add_u32 m0, s56, 87040\n"
    "s_nop 0\n"
    "buffer_load_dwordx4 v84, s[44:47], s57 offen lds\n"
    "s_add_u32 m0, s56, 88064\n"
    "s_nop 0\n"
    "buffer_load_dwordx4 v85, s[44:47], s57 offen lds\n"
    "s_add_u32 m0, s56, 89088\n"
    "s_nop 0\n"
    "buffer_load_dwordx4 v86, s[44:47], s57 offen lds\n"
    "s_add_u32 m0, s56, 90112\n"
    "s_nop 0\n"
    "buffer_load_dwordx4 v87, s[44:47], s57 offen lds\n"
    "s_add_u32 m0, s56, 91136\n"
    "s_nop 0\n"
    "buffer_load_dwordx4 v88, s[44:47], s57 offen lds\n"
    ".Lac9_%=:\n"
    "ds_read_b64_tr_b16 a[32:33], v99 offset:45056\n"
    "ds_read_b64_tr_b16 a[34:35], v99 offset:45632\n"
    "ds_read_b64_tr_b16 a[104:105], v99 offset:54272\n"
    "ds_read_b64_tr_b16 a[106:107], v99 offset:54848\n"
    "ds_read_b64_tr_b16 a[176:177], v99 offset:63488\n"
    "ds_read_b64_tr_b16 a[178:179], v99 offset:64064\n"
    "ds_read_b64_tr_b16 a[248:249], v98 offset:45056\n"
    "ds_read_b64_tr_b16 a[250:251], v98 offset:45632\n"
    "ds_read_b64_tr_b16 a[36:37], v99 offset:49664\n"
    "ds_read_b64_tr_b16 a[38:39], v99 offset:50240\n"
    "ds_read_b64_tr_b16 a[108:109], v99 offset:58880\n"
    "ds_read_b64_tr_b16 a[110:111], v99 offset:59456\n"
    "v_add_u32 v97, 0x20000, v90\n"
    "ds_read_b64_tr_b16 a[180:181], v97 offset:2560\n"
    "ds_read_b64_tr_b16 a[182:183], v97 offset:3136\n"
    "ds_read_b64_tr_b16 a[252:253], v98 offset:49664\n"
    "ds_read_b64_tr_b16 a[254:255], v98 offset:50240\n"
    "s_waitcnt vmcnt(27) lgkmcnt(0)\n"
    "s_barrier\n"
    "ds_read_b64_tr_b16 a[40:41], v90\n"
    "ds_read_b64_tr_b16 a[42:43], v90 offset:576\n"
    "ds_read_b64_tr_b16 a[112:113], v90 offset:9216\n"
    "ds_read_b64_tr_b16 a[114:115], v90 offset:9792\n"
    "ds_read_b64_tr_b16 a[184:185], v90 offset:18432\n"
    "ds_read_b64_tr_b16 a[186:187], v90 offset:19008\n"
    "ds_read_b64_tr_b16 v[224:225], v91\n"
    "ds_read_b64_tr_b16 v[226:227], v91 offset:576\n"
    "ds_read_b64_tr_b16 a[44:45], v90 offset:4608\n"
    "ds_read_b64_tr_b16 a[46:47], v90 offset:5184\n"
    "ds_read_b64_tr_b16 a[116:117], v90 offset:13824\n"
    "ds_read_b64_tr_b16 a[118:119], v90 offset:14400\n"
    "ds_read_b64_tr_b16 a[188:189], v90 offset:23040\n"
    "ds_read_b64_tr_b16 a[190:191], v90 offset:23616\n"
    "ds_read_b64_tr_b16 v[228:229], v91 offset:4608\n"
    "ds_read_b64_tr_b16 v[230:231], v91 offset:5184\n"
    "s_waitcnt vmcnt(18) lgkmcnt(0)\n"
    "s_barrier\n"
    "ds_read_b64_tr_b16 a[48:49], v90 offset:27648\n"
    "ds_read_b64_tr_b16 a[50:51], v90 offset:28224\n"
    "ds_read_b64_tr_b16 a[120:121], v90 offset:36864\n"
    "ds_read_b64_tr_b16 a[122:123], v90 offset:37440\n"
    "ds_read_b64_tr_b16 a[192:193], v90 offset:46080\n"
    "ds_read_b64_tr_b16 a[194:195], v90 offset:46656\n"
    "ds_read_b64_tr_b16 v[232:233], v91 offset:27648\n"
    "ds_read_b64_tr_b16 v[234:235], v91 offset:28224\n"
    "ds_read_b64_tr_b16 a[52:53], v90 offset:32256\n"
    "ds_read_b64_tr_b16 a[54:55], v90 offset:32832\n"
    "ds_read_b64_tr_b16 a[124:125], v90 offset:41472\n"
    "ds_read_b64_tr_b16 a[126:127], v90 offset:42048\n"
    "ds_read_b64_tr_b16 a[196:197], v90 offset:50688\n"
    "ds_read_b64_tr_b16 a[198:199], v90 offset:51264\n"
    "ds_read_b64_tr_b16 v[236:237], v91 offset:32256\n"
    "ds_read_b64_tr_b16 v[238:239], v91 offset:32832\n"
    "s_waitcnt vmcnt(9) lgkmcnt(0)\n"
    "s_barrier\n"
    "ds_read_b64_tr_b16 a[56:57], v90 offset:55296\n"
    "ds_read_b64_tr_b16 a[58:59], v90 offset:55872\n"
    "ds_read_b64_tr_b16 a[128:129], v90 offset:64512\n"
    "ds_read_b64_tr_b16 a[130:131], v90 offset:65088\n"
    "ds_read_b64_tr_b16 a[200:201], v99 offset:8192\n"
    "ds_read_b64_tr_b16 a[202:203], v99 offset:8768\n"
    "ds_read_b64_tr_b16 v[240:241], v91 offset:55296\n"
    "ds_read_b64_tr_b16 v[242:243], v91 offset:55872\n"
    "ds_read_b64_tr_b16 a[60:61], v90 offset:59904\n"
    "ds_read_b64_tr_b16 a[62:63], v90 offset:60480\n"
    "ds_read_b64_tr_b16 a[132:133], v99 offset:3584\n"
    "ds_read_b64_tr_b16 a[134:135], v99 offset:4160\n"
    "ds_read_b64_tr_b16 a[204:205], v99 offset:12800\n"
    "ds_read_b64_tr_b16 a[206:207], v99 offset:13376\n"
    "ds_read_b64_tr_b16 v[244:245], v91 offset:59904\n"
    "ds_read_b64_tr_b16 v[246:247], v91 offset:60480\n"
    "s_waitcnt vmcnt(0) lgkmcnt(0)\n"
    "s_barrier\n"
    "ds_read_b64_tr_b16 a[64:65], v99 offset:17408\n"
    "ds_read_b64_tr_b16 a[66:67], v99 offset:17984\n"
    "ds_read_b64_tr_b16 a[136:137], v99 offset:26624\n"
    "ds_read_b64_tr_b16 a[138:139], v99 offset:27200\n"
    "ds_read_b64_tr_b16 a[208:209], v99 offset:35840\n"
    "ds_read_b64_tr_b16 a[210:211], v99 offset:36416\n"
    "ds_read_b64_tr_b16 v[248:249], v98 offset:17408\n"
    "ds_read_b64_tr_b16 v[250:251], v98 offset:17984\n"
    "ds_read_b64_tr_b16 a[68:69], v99 offset:22016\n"
    "ds_read_b64_tr_b16 a[70:71], v99 offset:22592\n"
    "ds_read_b64_tr_b16 a[140:141], v99 offset:31232\n"
    "ds_read_b64_tr_b16 a[142:143], v99 offset:31808\n"
    "ds_read_b64_tr_b16 a[212:213], v99 offset:40448\n"
    "ds_read_b64_tr_b16 a[214:215], v99 offset:41024\n"
    "ds_read_b64_tr_b16 v[252:253], v98 offset:22016\n"
    "ds_read_b64_tr_b16 v[254:255], v98 offset:22592\n"
    "s_waitcnt lgkmcnt(0)\n"
    "s_barrier\n"
    "v_mov_b32 v40, 0\n"
    "v_mov_b32 v41, 0\n"
    "v_mov_b32 v42, 0\n"
    "v_mov_b32 v43, 0\n"
    "v_mov_b32 v44, 0\n"
    "v_mov_b32 v45, 0\n"
    "v_mov_b32 v46, 0\n"
    "v_mov_b32 v47, 0\n"
    "v_mov_b32 v48, 0\n"
    "v_mov_b32 v49, 0\n"
    "v_mov_b32 v50, 0\n"
    "v_mov_b32 v51, 0\n"
    "v_mov_b32 v52, 0\n"
    "v_mov_b32 v53, 0\n"
    "v_mov_b32 v54, 0\n"
    "v_mov_b32 v55, 0\n"
    "v_mov_b32 v56, 0\n"
    "v_mov_b32 v57, 0\n"
    "v_mov_b32 v58, 0\n"
    "v_mov_b32 v59, 0\n"
    "v_mov_b32 v60, 0\n"
    "v_mov_b32 v61, 0\n"
    "v_mov_b32 v62, 0\n"
    "v_mov_b32 v63, 0\n"
    "v_mov_b32 v73, 0xf149f2ca\n"
    "v_mov_b32 v64, v73\n"
    "v_add_u32 v74, 7, v72\n"
    "v_cmp_gt_u32 vcc, 8, v74\n"
    "s_nop 1\n"
    "v_cndmask_b32 v68, v73, v40, vcc\n"
    "v_mov_b32 v65, v73\n"
    "v_add_u32 v74, 8, v72\n"
    "v_cmp_gt_u32 vcc, 8, v74\n"
    "s_nop 1\n"
    "v_cndmask_b32 v69, v73, v40, vcc\n"
    "v_mov_b32 v66, v73\n"
    "v_add_u32 v74, 9, v72\n"
    "v_cmp_gt_u32 vcc, 8, v74\n"
    "s_nop 1\n"
    "v_cndmask_b32 v70, v73, v40, vcc\n"
    "v_mov_b32 v67, v73\n"
    "v_add_u32 v74, 10, v72\n"
    "v_cmp_gt_u32 vcc, 8, v74\n"
    "s_nop 1\n"
    "v_cndmask_b32 v71, v73, v40, vcc\n"
    "s_mov_b32 s58, %6\n"
    "s_sub_u32 s62, %7, 36864\n"
    "s_mov_b32 s48, -1\n"
    "s_branch .Lawait_%=\n"
    ".Latop_%=:\n"
    "s_waitcnt lgkmcnt(0)\n"
    "s_barrier\n"
    "ds_read_b128 v[80:83], v0\n"
    "ds_read_b128 v[84:87], v0 offset:18432\n"
    "ds_read_b128 v[88:91], v1 offset:1024\n"
    "ds_read_b128 v[92:95], v1 offset:19456\n"
    "ds_read_b128 v[96:99], v0 offset:2048\n"
    "ds_read_b128 v[100:103], v0 offset:20480\n"
    "s_cmp_eq_u32 s50, 0\n"
    "s_cbranch_scc1 .Lac10_%=\n"
    "s_sub_u32 s51, s48, 1\n"
    "s_mul_i32 s51, s51, 64\n"
    "s_add_u32 s51, s51, %5\n"
    "v_mov_b32 v11, s51\n"
    "v_mov_b32 v10, 1\n"
    "s_mov_b64 exec, 1\n"
    "global_atomic_add v11, v10, %1\n"
    "s_mov_b64 exec, -1\n"
    ".Lac10_%=:\n"
    "s_add_u32 s52, s48, 1\n"
    "s_and_b32 s52, s52, 63\n"
    "s_mul_i32 s52, s52, 64\n"
    "s_add_u32 s52, s52, %4\n"
    "v_mov_b32 v11, s52\n"
    "global_load_dword v10, v11, %1 sc1\n"
    "s_cmp_eq_u32 %2, 1\n"
    "s_cbranch_scc1 .Laq1_%=\n"
    "s_cmp_eq_u32 %2, 2\n"
    "s_cbranch_scc1 .Laq2_%=\n"
    "s_cmp_eq_u32 %2, 3\n"
    "s_cbranch_scc1 .Laq3_%=\n"
    "s_waitcnt lgkmcnt(5)\n"
    "v_mfma_f32_16x16x32_bf16 v[36:39], a[216:219], v[80:83], 0\n"
    "ds_read_b128 v[104:107], v1 offset:3072\n"
    "ds_read_b128 v[108:111], v1 offset:21504\n"
    "v_mfma_f32_16x16x32_bf16 v[12:15], a[0:3], v[80:83], 0\n"
    "s_waitcnt lgkmcnt(6)\n"
    "v_mfma_f32_16x16x32_bf16 v[16:19], a[0:3], v[84:87], 0\n"
    "s_waitcnt lgkmcnt(5)\n"
    "v_mfma_f32_16x16x32_bf16 v[36:39], a[220:223], v[88:91], v[36:39]\n"
    "ds_read_b128 v[112:115], v0 offset:4096\n"
    "ds_read_b128 v[116:119], v0 offset:22528\n"
    "v_mfma_f32_16x16x32_bf16 v[12:15], a[4:7], v[88:91], v[12:15]\n"
    "s_waitcnt lgkmcnt(6)\n"
    "v_mfma_f32_16x16x32_bf16 v[16:19], a[4:7], v[92:95], v[16:19]\n"
    "s_waitcnt lgkmcnt(5)\n"
    "v_mfma_f32_16x16x32_bf16 v[36:39], a[224:227], v[96:99], v[36:39]\n"
    "ds_read_b128 v[120:123], v1 offset:5120\n"
    "ds_read_b128 v[124:127], v1 offset:23552\n"
    "v_mfma_f32_16x16x32_bf16 v[12:15], a[8:11], v[96:99], v[12:15]\n"
    "s_waitcnt lgkmcnt(6)\n"
    "v_mfma_f32_16x16x32_bf16 v[16:19], a[8:11], v[100:103], v[16:19]\n"
    "s_waitcnt lgkmcnt(5)\n"
    "v_mfma_f32_16x16x32_bf16 v[36:39], a[228:231], v[104:107], v[36:39]\n"
    "ds_read_b128 v[128:131], v0 offset:6144\n"
    "ds_read_b128 v[132:135], v0 offset:24576\n"
    "v_mfma_f32_16x16x32_bf16 v[12:15], a[12:15], v[104:107], v[12:15]\n"
    "s_waitcnt lgkmcnt(6)\n"
    "v_mfma_f32_16x16x32_bf16 v[16:19], a[12:15], v[108:111], v[16:19]\n"
    "s_waitcnt lgkmcnt(5)\n"
    "v_mfma_f32_16x16x32_bf16 v[36:39], a[232:235], v[112:115], v[36:39]\n"
    "ds_read_b128 v[136:139], v1 offset:7168\n"
    "ds_read_b128 v[140:143], v1 offset:25600\n"
    "v_mfma_f32_16x16x32_bf16 v[12:15], a[16:19], v[112:115], v[12:15]\n"
    "s_waitcnt lgkmcnt(6)\n"
    "v_mfma_f32_16x16x32_bf16 v[16:19], a[16:19], v[116:119], v[16:19]\n"
    "s_waitcnt lgkmcnt(5)\n"
    "v_mfma_f32_16x16x32_bf16 v[36:39], a[236:239], v[120:123], v[36:39]\n"
    "ds_read_b128 v[144:147], v0 offset:8192\n"
    "ds_read_b128 v[148:151], v0 offset:26624\n"
    "v_mfma_f32_16x16x32_bf16 v[12:15], a[20:23], v[120:123], v[12:15]\n"
    "s_waitcnt lgkmcnt(6)\n"
    "v_mfma_f32_16x16x32_bf16 v[16:19], a[20:23], v[124:127], v[16:19]\n"
    "s_waitcnt lgkmcnt(5)\n"
    "v_mfma_f32_16x16x32_bf16 v[36:39], a[240:243], v[128:131], v[36:39]\n"
    "ds_read_b128 v[152:155], v1 offset:9216\n"
    "ds_read_b128 v[156:159], v1 offset:27648\n"
    "v_mfma_f32_16x16x32_bf16 v[12:15], a[24:27], v[128:131], v[12:15]\n"
    "s_waitcnt lgkmcnt(6)\n"
    "v_mfma_f32_16x16x32_bf16 v[16:19], a[24:27], v[132:135], v[16:19]\n"
    "s_waitcnt lgkmcnt(5)\n"
    "v_mfma_f32_16x16x32_bf16 v[36:39], a[244:247], v[136:139], v[36:39]\n"
    "ds_read_b128 v[160:163], v0 offset:10240\n"
    "ds_read_b128 v[164:167], v0 offset:28672\n"
    "v_mfma_f32_16x16x32_bf16 v[12:15], a[28:31], v[136:139], v[12:15]\n"
    "s_waitcnt lgkmcnt(6)\n"
    "v_mfma_f32_16x16x32_bf16 v[16:19], a[28:31], v[140:143], v[16:19]\n"
    "s_waitcnt lgkmcnt(5)\n"
    "v_mfma_f32_16x16x32_bf16 v[36:39], a[248:251], v[144:147], v[36:39]\n"
    "ds_read_b128 v[168:171], v1 offset:11264\n"
    "ds_read_b128 v[172:175], v1 offset:29696\n"
    "v_mfma_f32_16x16x32_bf16 v[12:15], a[32:35], v[144:147], v[12:15]\n"
    "s_waitcnt lgkmcnt(6)\n"
    "v_mfma_f32_16x16x32_bf16 v[16:19], a[32:35], v[148:151], v[16:19]\n"
    "s_waitcnt lgkmcnt(5)\n"
    "v_mfma_f32_16x16x32_bf16 v[36:39], a[252:255], v[152:155], v[36:39]\n"
    "ds_read_b128 v[176:179], v0 offset:12288\n"
    "ds_read_b128 v[180:183], v0 offset:30720\n"
    "v_mfma_f32_16x16x32_bf16 v[12:15], a[36:39], v[152:155], v[12:15]\n"
    "s_waitcnt lgkmcnt(6)\n"
    "v_mfma_f32_16x16x32_bf16 v[16:19], a[36:39], v[156:159], v[16:19]\n"
    "s_waitcnt lgkmcnt(5)\n"
    "v_mfma_f32_16x16x32_bf16 v[36:39], v[224:227], v[160:163], v[36:39]\n"
    "ds_read_b128 v[184:187], v1 offset:13312\n"
    "ds_read_b128 v[188:191], v1 offset:31744\n"
    "v_mfma_f32_16x16x32_bf16 v[12:15], a[40:43], v[160:163], v[12:15]\n"
    "s_waitcnt lgkmcnt(6)\n"
    "v_mfma_f32_16x16x32_bf16 v[16:19], a[40:43], v[164:167], v[16:19]\n"
    "s_waitcnt lgkmcnt(5)\n"
    "v_mfma_f32_16x16x32_bf16 v[36:39], v[228:231], v[168:171], v[36:39]\n"
    "ds_read_b128 v[192:195], v0 offset:14336\n"
    "ds_read_b128 v[196:199], v0 offset:32768\n"
    "v_mfma_f32_16x16x32_bf16 v[12:15], a[44:47], v[168:171], v[12:15]\n"
    "s_waitcnt lgkmcnt(6)\n"
    "v_mfma_f32_16x16x32_bf16 v[16:19], a[44:47], v[172:175], v[16:19]\n"
    "s_waitcnt lgkmcnt(5)\n"
    "v_mfma_f32_16x16x32_bf16 v[36:39], v[232:235], v[176:179], v[36:39]\n"
    "ds_read_b128 v[200:203], v1 offset:15360\n"
    "ds_read_b128 v[204:207], v1 offset:33792\n"
    "v_mfma_f32_16x16x32_bf16 v[12:15], a[48:51], v[176:179], v[12:15]\n"
    "s_waitcnt lgkmcnt(6)\n"
    "v_mfma_f32_16x16x32_bf16 v[16:19], a[48:51], v[180:183], v[16:19]\n"
    "s_waitcnt lgkmcnt(5)\n"
    "v_mfma_f32_16x16x32_bf16 v[36:39], v[236:239], v[184:187], v[36:39]\n"
    "ds_read_b128 v[208:211], v0 offset:16384\n"
    "ds_read_b128 v[212:215], v0 offset:34816\n"
    "v_mfma_f32_16x16x32_bf16 v[12:15], a[52:55], v[184:187], v[12:15]\n"
    "s_waitcnt lgkmcnt(6)\n"
    "v_mfma_f32_16x16x32_bf16 v[16:19], a[52:55], v[188:191], v[16:19]\n"
    "s_waitcnt lgkmcnt(5)\n"
    "v_mfma_f32_16x16x32_bf16 v[36:39], v[240:243], v[192:195], v[36:39]\n"
    "ds_read_b128 v[216:219], v1 offset:17408\n"
    "ds_read_b128 v[220:223], v1 offset:35840\n"
    "v_mfma_f32_16x16x32_bf16 v[12:15], a[56:59], v[192:195], v[12:15]\n"
    "s_waitcnt lgkmcnt(6)\n"
    "v_mfma_f32_16x16x32_bf16 v[16:19], a[56:59], v[196:199], v[16:19]\n"
    "s_waitcnt lgkmcnt(5)\n"
    "v_mfma_f32_16x16x32_bf16 v[36:39], v[244:247], v[200:203], v[36:39]\n"
    "v_mfma_f32_16x16x32_bf16 v[12:15], a[60:63], v[200:203], v[12:15]\n"
    "s_waitcnt lgkmcnt(4)\n"
    "v_mfma_f32_16x16x32_bf16 v[16:19], a[60:63], v[204:207], v[16:19]\n"
    "s_waitcnt lgkmcnt(3)\n"
    "v_mfma_f32_16x16x32_bf16 v[36:39], v[248:251], v[208:211], v[36:39]\n"
    "v_mfma_f32_16x16x32_bf16 v[12:15], a[64:67], v[208:211], v[12:15]\n"
    "s_waitcnt lgkmcnt(2)\n"
    "v_mfma_f32_16x16x32_bf16 v[16:19], a[64:67], v[212:215], v[16:19]\n"
    "s_waitcnt lgkmcnt(1)\n"
    "v_mfma_f32_16x16x32_bf16 v[36:39], v[252:255], v[216:219], v[36:39]\n"
    "v_mfma_f32_16x16x32_bf16 v[12:15], a[68:71], v[216:219], v[12:15]\n"
    "s_waitcnt lgkmcnt(0)\n"
    "v_mfma_f32_16x16x32_bf16 v[16:19], a[68:71], v[220:223], v[16:19]\n"
    "s_branch .Lak_%=\n"
    ".Laq1_%=:\n"
    "s_waitcnt lgkmcnt(4)\n"
    "v_mfma_f32_16x16x32_bf16 v[36:39], a[216:219], v[84:87], 0\n"
    "ds_read_b128 v[104:107], v1 offset:3072\n"
    "ds_read_b128 v[108:111], v1 offset:21504\n"
    "v_mfma_f32_16x16x32_bf16 v[12:15], a[0:3], v[80:83], 0\n"
    "v_mfma_f32_16x16x32_bf16 v[16:19], a[0:3], v[84:87], 0\n"
    "s_waitcnt lgkmcnt(4)\n"
    "v_mfma_f32_16x16x32_bf16 v[36:39], a[220:223], v[92:95], v[36:39]\n"
    "ds_read_b128 v[112:115], v0 offset:4096\n"
    "ds_read_b128 v[116:119], v0 offset:22528\n"
    "v_mfma_f32_16x16x32_bf16 v[12:15], a[4:7], v[88:91], v[12:15]\n"
    "v_mfma_f32_16x16x32_bf16 v[16:19], a[4:7], v[92:95], v[16:19]\n"
    "s_waitcnt lgkmcnt(4)\n"
    "v_mfma_f32_16x16x32_bf16 v[36:39], a[224:227], v[100:103], v[36:39]\n"
    "ds_read_b128 v[120:123], v1 offset:5120\n"
    "ds_read_b128 v[124:127], v1 offset:23552\n"
    "v_mfma_f32_16x16x32_bf16 v[12:15], a[8:11], v[96:99], v[12:15]\n"
    "v_mfma_f32_16x16x32_bf16 v[16:19], a[8:11], v[100:103], v[16:19]\n"
    "s_waitcnt lgkmcnt(4)\n"
    "v_mfma_f32_16x16x32_bf16 v[36:39], a[228:231], v[108:111], v[36:39]\n"
    "ds_read_b128 v[128:131], v0 offset:6144\n"
    "ds_read_b128 v[132:135], v0 offset:24576\n"
    "v_mfma_f32_16x16x32_bf16 v[12:15], a[12:15], v[104:107], v[12:15]\n"
    "v_mfma_f32_16x16x32_bf16 v[16:19], a[12:15], v[108:111], v[16:19]\n"
    "s_waitcnt lgkmcnt(4)\n"
    "v_mfma_f32_16x16x32_bf16 v[36:39], a[232:235], v[116:119], v[36:39]\n"
    "ds_read_b128 v[136:139], v1 offset:7168\n"
    "ds_read_b128 v[140:143], v1 offset:25600\n"
    "v_mfma_f32_16x16x32_bf16 v[12:15], a[16:19], v[112:115], v[12:15]\n"
    "v_mfma_f32_16x16x32_bf16 v[16:19], a[16:19], v[116:119], v[16:19]\n"
    "s_waitcnt lgkmcnt(4)\n"
    "v_mfma_f32_16x16x32_bf16 v[36:39], a[236:239], v[124:127], v[36:39]\n"
    "ds_read_b128 v[144:147], v0 offset:8192\n"
    "ds_read_b128 v[148:151], v0 offset:26624\n"
    "v_mfma_f32_16x16x32_bf16 v[12:15], a[20:23], v[120:123], v[12:15]\n"
    "v_mfma_f32_16x16x32_bf16 v[16:19], a[20:23], v[124:127], v[16:19]\n"
    "s_waitcnt lgkmcnt(4)\n"
    "v_mfma_f32_16x16x32_bf16 v[36:39], a[240:243], v[132:135], v[36:39]\n"
    "ds_read_b128 v[152:155], v1 offset:9216\n"
    "ds_read_b128 v[156:159], v1 offset:27648\n"
    "v_mfma_f32_16x16x32_bf16 v[12:15], a[24:27], v[128:131], v[12:15]\n"
    "v_mfma_f32_16x16x32_bf16 v[16:19], a[24:27], v[132:135], v[16:19]\n"
    "s_waitcnt lgkmcnt(4)\n"
    "v_mfma_f32_16x16x32_bf16 v[36:39], a[244:247], v[140:143], v[36:39]\n"
    "ds_read_b128 v[160:163], v0 offset:10240\n"
    "ds_read_b128 v[164:167], v0 offset:28672\n"
    "v_mfma_f32_16x16x32_bf16 v[12:15], a[28:31], v[136:139], v[12:15]\n"
    "v_mfma_f32_16x16x32_bf16 v[16:19], a[28:31], v[140:143], v[16:19]\n"
    "s_waitcnt lgkmcnt(4)\n"
    "v_mfma_f32_16x16x32_bf16 v[36:39], a[248:251], v[148:151], v[36:39]\n"
    "ds_read_b128 v[168:171], v1 offset:11264\n"
    "ds_read_b128 v[172:175], v1 offset:29696\n"
    "v_mfma_f32_16x16x32_bf16 v[12:15], a[32:35], v[144:147], v[12:15]\n"
    "v_mfma_f32_16x16x32_bf16 v[16:19], a[32:35], v[148:151], v[16:19]\n"
    "s_waitcnt lgkmcnt(4)\n"
    "v_mfma_f32_16x16x32_bf16 v[36:39], a[252:255], v[156:159], v[36:39]\n"
    "ds_read_b128 v[176:179], v0 offset:12288\n"
    "ds_read_b128 v[180:183], v0 offset:30720\n"
    "v_mfma_f32_16x16x32_bf16 v[12:15], a[36:39], v[152:155], v[12:15]\n"
    "v_mfma_f32_16x16x32_bf16 v[16:19], a[36:39], v[156:159], v[16:19]\n"
    "s_waitcnt lgkmcnt(4)\n"
    "v_mfma_f32_16x16x32_bf16 v[36:39], v[224:227], v[164:167], v[36:39]\n"
    "ds_read_b128 v[184:187], v1 offset:13312\n"
    "ds_read_b128 v[188:191], v1 offset:31744\n"
    "v_mfma_f32_16x16x32_bf16 v[12:15], a[40:43], v[160:163], v[12:15]\n"
    "v_mfma_f32_16x16x32_bf16 v[16:19], a[40:43], v[164:167], v[16:19]\n"
    "s_waitcnt lgkmcnt(4)\n"
    "v_mfma_f32_16x16x32_bf16 v[36:39], v[228:231], v[172:175], v[36:39]\n"
    "ds_read_b128 v[192:195], v0 offset:14336\n"
    "ds_read_b128 v[196:199], v0 offset:32768\n"
    "v_mfma_f32_16x16x32_bf16 v[12:15], a[44:47], v[168:171], v[12:15]\n"
    "v_mfma_f32_16x16x32_bf16 v[16:19], a[44:47], v[172:175], v[16:19]\n"
    "s_waitcnt lgkmcnt(4)\n"
    "v_mfma_f32_16x16x32_bf16 v[36:39], v[232:235], v[180:183], v[36:39]\n"
    "ds_read_b128 v[200:203], v1 offset:15360\n"
    "ds_read_b128 v[204:207], v1 offset:33792\n"
    "v_mfma_f32_16x16x32_bf16 v[12:15], a[48:51], v[176:179], v[12:15]\n"
    "v_mfma_f32_16x16x32_bf16 v[16:19], a[48:51], v[180:183], v[16:19]\n"
    "s_waitcnt lgkmcnt(4)\n"
    "v_mfma_f32_16x16x32_bf16 v[36:39], v[236:239], v[188:191], v[36:39]\n"
    "ds_read_b128 v[208:211], v0 offset:16384\n"
    "ds_read_b128 v[212:215], v0 offset:34816\n"
    "v_mfma_f32_16x16x32_bf16 v[12:15], a[52:55], v[184:187], v[12:15]\n"
    "v_mfma_f32_16x16x32_bf16 v[16:19], a[52:55], v[188:191], v[16:19]\n"
    "s_waitcnt lgkmcnt(4)\n"
    "v_mfma_f32_16x16x32_bf16 v[36:39], v[240:243], v[196:199], v[36:39]\n"
    "ds_read_b128 v[216:219], v1 offset:17408\n"
    "ds_read_b128 v[220:223], v1 offset:35840\n"
    "v_mfma_f32_16x16x32_bf16 v[12:15], a[56:59], v[192:195], v[12:15]\n"
    "v_mfma_f32_16x16x32_bf16 v[16:19], a[56:59], v[196:199], v[16:19]\n"
    "s_waitcnt lgkmcnt(4)\n"
    "v_mfma_f32_16x16x32_bf16 v[36:39], v[244:247], v[204:207], v[36:39]\n"
    "v_mfma_f32_16x16x32_bf16 v[12:15], a[60:63], v[200:203], v[12:15]\n"
    "v_mfma_f32_16x16x32_bf16 v[16:19], a[60:63], v[204:207], v[16:19]\n"
    "s_waitcnt lgkmcnt(2)\n"
    "v_mfma_f32_16x16x32_bf16 v[36:39], v[248:251], v[212:215], v[36:39]\n"
    "v_mfma_f32_16x16x32_bf16 v[12:15], a[64:67], v[208:211], v[12:15]\n"
    "v_mfma_f32_16x16x32_bf16 v[16:19], a[64:67], v[212:215], v[16:19]\n"
    "s_waitcnt lgkmcnt(0)\n"
    "v_mfma_f32_16x16x32_bf16 v[36:39], v[252:255], v[220:223], v[36:39]\n"
    "v_mfma_f32_16x16x32_bf16 v[12:15], a[68:71], v[216:219], v[12:15]\n"
    "v_mfma_f32_16x16x32_bf16 v[16:19], a[68:71], v[220:223], v[16:19]\n"
    "s_branch .Lak_%=\n"
    ".Laq2_%=:\n"
    "s_waitcnt lgkmcnt(5)\n"
    "v_mfma_f32_16x16x32_bf16 v[36:39], v[80:83], a[216:219], 0\n"
    "ds_read_b128 v[104:107], v1 offset:3072\n"
    "ds_read_b128 v[108:111], v1 offset:21504\n"
    "v_mfma_f32_16x16x32_bf16 v[12:15], a[0:3], v[80:83], 0\n"
    "s_waitcnt lgkmcnt(6)\n"
    "v_mfma_f32_16x16x32_bf16 v[16:19], a[0:3], v[84:87], 0\n"
    "s_waitcnt lgkmcnt(5)\n"
    "v_mfma_f32_16x16x32_bf16 v[36:39], v[88:91], a[220:223], v[36:39]\n"
    "ds_read_b128 v[112:115], v0 offset:4096\n"
    "ds_read_b128 v[116:119], v0 offset:22528\n"
    "v_mfma_f32_16x16x32_bf16 v[12:15], a[4:7], v[88:91], v[12:15]\n"
    "s_waitcnt lgkmcnt(6)\n"
    "v_mfma_f32_16x16x32_bf16 v[16:19], a[4:7], v[92:95], v[16:19]\n"
    "s_waitcnt lgkmcnt(5)\n"
    "v_mfma_f32_16x16x32_bf16 v[36:39], v[96:99], a[224:227], v[36:39]\n"
    "ds_read_b128 v[120:123], v1 offset:5120\n"
    "ds_read_b128 v[124:127], v1 offset:23552\n"
    "v_mfma_f32_16x16x32_bf16 v[12:15], a[8:11], v[96:99], v[12:15]\n"
    "s_waitcnt lgkmcnt(6)\n"
    "v_mfma_f32_16x16x32_bf16 v[16:19], a[8:11], v[100:103], v[16:19]\n"
    "s_waitcnt lgkmcnt(5)\n"
    "v_mfma_f32_16x16x32_bf16 v[36:39], v[104:107], a[228:231], v[36:39]\n"
    "ds_read_b128 v[128:131], v0 offset:6144\n"
    "ds_read_b128 v[132:135], v0 offset:24576\n"
    "v_mfma_f32_16x16x32_bf16 v[12:15], a[12:15], v[104:107], v[12:15]\n"
    "s_waitcnt lgkmcnt(6)\n"
    "v_mfma_f32_16x16x32_bf16 v[16:19], a[12:15], v[108:111], v[16:19]\n"
    "s_waitcnt lgkmcnt(5)\n"
    "v_mfma_f32_16x16x32_bf16 v[36:39], v[112:115], a[232:235], v[36:39]\n"
    "ds_read_b128 v[136:139], v1 offset:7168\n"
    "ds_read_b128 v[140:143], v1 offset:25600\n"
    "v_mfma_f32_16x16x32_bf16 v[12:15], a[16:19], v[112:115], v[12:15]\n"
    "s_waitcnt lgkmcnt(6)\n"
    "v_mfma_f32_16x16x32_bf16 v[16:19], a[16:19], v[116:119], v[16:19]\n"
    "s_waitcnt lgkmcnt(5)\n"
    "v_mfma_f32_16x16x32_bf16 v[36:39], v[120:123], a[236:239], v[36:39]\n"
    "ds_read_b128 v[144:147], v0 offset:8192\n"
    "ds_read_b128 v[148:151], v0 offset:26624\n"
    "v_mfma_f32_16x16x32_bf16 v[12:15], a[20:23], v[120:123], v[12:15]\n"
    "s_waitcnt lgkmcnt(6)\n"
    "v_mfma_f32_16x16x32_bf16 v[16:19], a[20:23], v[124:127], v[16:19]\n"
    "s_waitcnt lgkmcnt(5)\n"
    "v_mfma_f32_16x16x32_bf16 v[36:39], v[128:131], a[240:243], v[36:39]\n"
    "ds_read_b128 v[152:155], v1 offset:9216\n"
    "ds_read_b128 v[156:159], v1 offset:27648\n"
    "v_mfma_f32_16x16x32_bf16 v[12:15], a[24:27], v[128:131], v[12:15]\n"
    "s_waitcnt lgkmcnt(6)\n"
    "v_mfma_f32_16x16x32_bf16 v[16:19], a[24:27], v[132:135], v[16:19]\n"
    "s_waitcnt lgkmcnt(5)\n"
    "v_mfma_f32_16x16x32_bf16 v[36:39], v[136:139], a[244:247], v[36:39]\n"
    "ds_read_b128 v[160:163], v0 offset:10240\n"
    "ds_read_b128 v[164:167], v0 offset:28672\n"
    "v_mfma_f32_16x16x32_bf16 v[12:15], a[28:31], v[136:139], v[12:15]\n"
    "s_waitcnt lgkmcnt(6)\n"
    "v_mfma_f32_16x16x32_bf16 v[16:19], a[28:31], v[140:143], v[16:19]\n"
    "s_waitcnt lgkmcnt(5)\n"
    "v_mfma_f32_16x16x32_bf16 v[36:39], v[144:147], a[248:251], v[36:39]\n"
    "ds_read_b128 v[168:171], v1 offset:11264\n"
    "ds_read_b128 v[172:175], v1 offset:29696\n"
    "v_mfma_f32_16x16x32_bf16 v[12:15], a[32:35], v[144:147], v[12:15]\n"
    "s_waitcnt lgkmcnt(6)\n"
    "v_mfma_f32_16x16x32_bf16 v[16:19], a[32:35], v[148:151], v[16:19]\n"
    "s_waitcnt lgkmcnt(5)\n"
    "v_mfma_f32_16x16x32_bf16 v[36:39], v[152:155], a[252:255], v[36:39]\n"
    "ds_read_b128 v[176:179], v0 offset:12288\n"
    "ds_read_b128 v[180:183], v0 offset:30720\n"
    "v_mfma_f32_16x16x32_bf16 v[12:15], a[36:39], v[152:155], v[12:15]\n"
    "s_waitcnt lgkmcnt(6)\n"
    "v_mfma_f32_16x16x32_bf16 v[16:19], a[36:39], v[156:159], v[16:19]\n"
    "s_waitcnt lgkmcnt(5)\n"
    "v_mfma_f32_16x16x32_bf16 v[36:39], v[160:163], v[224:227], v[36:39]\n"
    "ds_read_b128 v[184:187], v1 offset:13312\n"
    "ds_read_b128 v[188:191], v1 offset:31744\n"
    "v_mfma_f32_16x16x32_bf16 v[12:15], a[40:43], v[160:163], v[12:15]\n"
    "s_waitcnt lgkmcnt(6)\n"
    "v_mfma_f32_16x16x32_bf16 v[16:19], a[40:43], v[164:167], v[16:19]\n"
    "s_waitcnt lgkmcnt(5)\n"
    "v_mfma_f32_16x16x32_bf16 v[36:39], v[168:171], v[228:231], v[36:39]\n"
    "ds_read_b128 v[192:195], v0 offset:14336\n"
    "ds_read_b128 v[196:199], v0 offset:32768\n"
    "v_mfma_f32_16x16x32_bf16 v[12:15], a[44:47], v[168:171], v[12:15]\n"
    "s_waitcnt lgkmcnt(6)\n"
    "v_mfma_f32_16x16x32_bf16 v[16:19], a[44:47], v[172:175], v[16:19]\n"
    "s_waitcnt lgkmcnt(5)\n"
    "v_mfma_f32_16x16x32_bf16 v[36:39], v[176:179], v[232:235], v[36:39]\n"
    "ds_read_b128 v[200:203], v1 offset:15360\n"
    "ds_read_b128 v[204:207], v1 offset:33792\n"
    "v_mfma_f32_16x16x32_bf16 v[12:15], a[48:51], v[176:179], v[12:15]\n"
    "s_waitcnt lgkmcnt(6)\n"
    "v_mfma_f32_16x16x32_bf16 v[16:19], a[48:51], v[180:183], v[16:19]\n"
    "s_waitcnt lgkmcnt(5)\n"
    "v_mfma_f32_16x16x32_bf16 v[36:39], v[184:187], v[236:239], v[36:39]\n"
    "ds_read_b128 v[208:211], v0 offset:16384\n"
    "ds_read_b128 v[212:215], v0 offset:34816\n"
    "v_mfma_f32_16x16x32_bf16 v[12:15], a[52:55], v[184:187], v[12:15]\n"
    "s_waitcnt lgkmcnt(6)\n"
    "v_mfma_f32_16x16x32_bf16 v[16:19], a[52:55], v[188:191], v[16:19]\n"
    "s_waitcnt lgkmcnt(5)\n"
    "v_mfma_f32_16x16x32_bf16 v[36:39], v[192:195], v[240:243], v[36:39]\n"
    "ds_read_b128 v[216:219], v1 offset:17408\n"
    "ds_read_b128 v[220:223], v1 offset:35840\n"
    "v_mfma_f32_16x16x32_bf16 v[12:15], a[56:59], v[192:195], v[12:15]\n"
    "s_waitcnt lgkmcnt(6)\n"
    "v_mfma_f32_16x16x32_bf16 v[16:19], a[56:59], v[196:199], v[16:19]\n"
    "s_waitcnt lgkmcnt(5)\n"
    "v_mfma_f32_16x16x32_bf16 v[36:39], v[200:203], v[244:247], v[36:39]\n"
    "v_mfma_f32_16x16x32_bf16 v[12:15], a[60:63], v[200:203], v[12:15]\n"
    "s_waitcnt lgkmcnt(4)\n"
    "v_mfma_f32_16x16x32_bf16 v[16:19], a[60:63], v[204:207], v[16:19]\n"
    "s_waitcnt lgkmcnt(3)\n"
    "v_mfma_f32_16x16x32_bf16 v[36:39], v[208:211], v[248:251], v[36:39]\n"
    "v_mfma_f32_16x16x32_bf16 v[12:15], a[64:67], v[208:211], v[12:15]\n"
    "s_waitcnt lgkmcnt(2)\n"
    "v_mfma_f32_16x16x32_bf16 v[16:19], a[64:67], v[212:215], v[16:19]\n"
    "s_waitcnt lgkmcnt(1)\n"
    "v_mfma_f32_16x16x32_bf16 v[36:39], v[216:219], v[252:255], v[36:39]\n"
    "v_mfma_f32_16x16x32_bf16 v[12:15], a[68:71], v[216:219], v[12:15]\n"
    "s_waitcnt lgkmcnt(0)\n"
    "v_mfma_f32_16x16x32_bf16 v[16:19], a[68:71], v[220:223], v[16:19]\n"
    "s_branch .Lak_%=\n"
    ".Laq3_%=:\n"
    "s_waitcnt lgkmcnt(4)\n"
    "v_mfma_f32_16x16x32_bf16 v[36:39], v[84:87], a[216:219], 0\n"
    "ds_read_b128 v[104:107], v1 offset:3072\n"
    "ds_read_b128 v[108:111], v1 offset:21504\n"
    "v_mfma_f32_16x16x32_bf16 v[12:15], a[0:3], v[80:83], 0\n"
    "v_mfma_f32_16x16x32_bf16 v[16:19], a[0:3], v[84:87], 0\n"
    "s_waitcnt lgkmcnt(4)\n"
    "v_mfma_f32_16x16x32_bf16 v[36:39], v[92:95], a[220:223], v[36:39]\n"
    "ds_read_b128 v[112:115], v0 offset:4096\n"
    "ds_read_b128 v[116:119], v0 offset:22528\n"
    "v_mfma_f32_16x16x32_bf16 v[12:15], a[4:7], v[88:91], v[12:15]\n"
    "v_mfma_f32_16x16x32_bf16 v[16:19], a[4:7], v[92:95], v[16:19]\n"
    "s_waitcnt lgkmcnt(4)\n"
    "v_mfma_f32_16x16x32_bf16 v[36:39], v[100:103], a[224:227], v[36:39]\n"
    "ds_read_b128 v[120:123], v1 offset:5120\n"
    "ds_read_b128 v[124:127], v1 offset:23552\n"
    "v_mfma_f32_16x16x32_bf16 v[12:15], a[8:11], v[96:99], v[12:15]\n"
    "v_mfma_f32_16x16x32_bf16 v[16:19], a[8:11], v[100:103], v[16:19]\n"
    "s_waitcnt lgkmcnt(4)\n"
    "v_mfma_f32_16x16x32_bf16 v[36:39], v[108:111], a[228:231], v[36:39]\n"
    "ds_read_b128 v[128:131], v0 offset:6144\n"
    "ds_read_b128 v[132:135], v0 offset:24576\n"
    "v_mfma_f32_16x16x32_bf16 v[12:15], a[12:15], v[104:107], v[12:15]\n"
    "v_mfma_f32_16x16x32_bf16 v[16:19], a[12:15], v[108:111], v[16:19]\n"
    "s_waitcnt lgkmcnt(4)\n"
    "v_mfma_f32_16x16x32_bf16 v[36:39], v[116:119], a[232:235], v[36:39]\n"
    "ds_read_b128 v[136:139], v1 offset:7168\n"
    "ds_read_b128 v[140:143], v1 offset:25600\n"
    "v_mfma_f32_16x16x32_bf16 v[12:15], a[16:19], v[112:115], v[12:15]\n"
    "v_mfma_f32_16x16x32_bf16 v[16:19], a[16:19], v[116:119], v[16:19]\n"
    "s_waitcnt lgkmcnt(4)\n"
    "v_mfma_f32_16x16x32_bf16 v[36:39], v[124:127], a[236:239], v[36:39]\n"
    "ds_read_b128 v[144:147], v0 offset:8192\n"
    "ds_read_b128 v[148:151], v0 offset:26624\n"
    "v_mfma_f32_16x16x32_bf16 v[12:15], a[20:23], v[120:123], v[12:15]\n"
    "v_mfma_f32_16x16x32_bf16 v[16:19], a[20:23], v[124:127], v[16:19]\n"
    "s_waitcnt lgkmcnt(4)\n"
    "v_mfma_f32_16x16x32_bf16 v[36:39], v[132:135], a[240:243], v[36:39]\n"
    "ds_read_b128 v[152:155], v1 offset:9216\n"
    "ds_read_b128 v[156:159], v1 offset:27648\n"
    "v_mfma_f32_16x16x32_bf16 v[12:15], a[24:27], v[128:131], v[12:15]\n"
    "v_mfma_f32_16x16x32_bf16 v[16:19], a[24:27], v[132:135], v[16:19]\n"
    "s_waitcnt lgkmcnt(4)\n"
    "v_mfma_f32_16x16x32_bf16 v[36:39], v[140:143], a[244:247], v[36:39]\n"
    "ds_read_b128 v[160:163], v0 offset:10240\n"
    "ds_read_b128 v[164:167], v0 offset:28672\n"
    "v_mfma_f32_16x16x32_bf16 v[12:15], a[28:31], v[136:139], v[12:15]\n"
    "v_mfma_f32_16x16x32_bf16 v[16:19], a[28:31], v[140:143], v[16:19]\n"
    "s_waitcnt lgkmcnt(4)\n"
    "v_mfma_f32_16x16x32_bf16 v[36:39], v[148:151], a[248:251], v[36:39]\n"
    "ds_read_b128 v[168:171], v1 offset:11264\n"
    "ds_read_b128 v[172:175], v1 offset:29696\n"
    "v_mfma_f32_16x16x32_bf16 v[12:15], a[32:35], v[144:147], v[12:15]\n"
    "v_mfma_f32_16x16x32_bf16 v[16:19], a[32:35], v[148:151], v[16:19]\n"
    "s_waitcnt lgkmcnt(4)\n"
    "v_mfma_f32_16x16x32_bf16 v[36:39], v[156:159], a[252:255], v[36:39]\n"
    "ds_read_b128 v[176:179], v0 offset:12288\n"
    "ds_read_b128 v[180:183], v0 offset:30720\n"
    "v_mfma_f32_16x16x32_bf16 v[12:15], a[36:39], v[152:155], v[12:15]\n"
    "v_mfma_f32_16x16x32_bf16 v[16:19], a[36:39], v[156:159], v[16:19]\n"
    "s_waitcnt lgkmcnt(4)\n"
    "v_mfma_f32_16x16x32_bf16 v[36:39], v[164:167], v[224:227], v[36:39]\n"
    "ds_read_b128 v[184:187], v1 offset:13312\n"
    "ds_read_b128 v[188:191], v1 offset:31744\n"
    "v_mfma_f32_16x16x32_bf16 v[12:15], a[40:43], v[160:163], v[12:15]\n"
    "v_mfma_f32_16x16x32_bf16 v[16:19], a[40:43], v[164:167], v[16:19]\n"
    "s_waitcnt lgkmcnt(4)\n"
    "v_mfma_f32_16x16x32_bf16 v[36:39], v[172:175], v[228:231], v[36:39]\n"
    "ds_read_b128 v[192:195], v0 offset:14336\n"
    "ds_read_b128 v[196:199], v0 offset:32768\n"
    "v_mfma_f32_16x16x32_bf16 v[12:15], a[44:47], v[168:171], v[12:15]\n"
    "v_mfma_f32_16x16x32_bf16 v[16:19], a[44:47], v[172:175], v[16:19]\n"
    "s_waitcnt lgkmcnt(4)\n"
    "v_mfma_f32_16x16x32_bf16 v[36:39], v[180:183], v[232:235], v[36:39]\n"
    "ds_read_b128 v[200:203], v1 offset:15360\n"
    "ds_read_b128 v[204:207], v1 offset:33792\n"
    "v_mfma_f32_16x16x32_bf16 v[12:15], a[48:51], v[176:179], v[12:15]\n"
    "v_mfma_f32_16x16x32_bf16 v[16:19], a[48:51], v[180:183], v[16:19]\n"
    "s_waitcnt lgkmcnt(4)\n"
    "v_mfma_f32_16x16x32_bf16 v[36:39], v[188:191], v[236:239], v[36:39]\n"
    "ds_read_b128 v[208:211], v0 offset:16384\n"
    "ds_read_b128 v[212:215], v0 offset:34816\n"
    "v_mfma_f32_16x16x32_bf16 v[12:15], a[52:55], v[184:187], v[12:15]\n"
    "v_mfma_f32_16x16x32_bf16 v[16:19], a[52:55], v[188:191], v[16:19]\n"
    "s_waitcnt lgkmcnt(4)\n"
    "v_mfma_f32_16x16x32_bf16 v[36:39], v[196:199], v[240:243], v[36:39]\n"
    "ds_read_b128 v[216:219], v1 offset:17408\n"
    "ds_read_b128 v[220:223], v1 offset:35840\n"
    "v_mfma_f32_16x16x32_bf16 v[12:15], a[56:59], v[192:195], v[12:15]\n"
    "v_mfma_f32_16x16x32_bf16 v[16:19], a[56:59], v[196:199], v[16:19]\n"
    "s_waitcnt lgkmcnt(4)\n"
    "v_mfma_f32_16x16x32_bf16 v[36:39], v[204:207], v[244:247], v[36:39]\n"
    "v_mfma_f32_16x16x32_bf16 v[12:15], a[60:63], v[200:203], v[12:15]\n"
    "v_mfma_f32_16x16x32_bf16 v[16:19], a[60:63], v[204:207], v[16:19]\n"
    "s_waitcnt lgkmcnt(2)\n"
    "v_mfma_f32_16x16x32_bf16 v[36:39], v[212:215], v[248:251], v[36:39]\n"
    "v_mfma_f32_16x16x32_bf16 v[12:15], a[64:67], v[208:211], v[12:15]\n"
    "v_mfma_f32_16x16x32_bf16 v[16:19], a[64:67], v[212:215], v[16:19]\n"
    "s_waitcnt lgkmcnt(0)\n"
    "v_mfma_f32_16x16x32_bf16 v[36:39], v[220:223], v[252:255], v[36:39]\n"
    "v_mfma_f32_16x16x32_bf16 v[12:15], a[68:71], v[216:219], v[12:15]\n"
    "v_mfma_f32_16x16x32_bf16 v[16:19], a[68:71], v[220:223], v[16:19]\n"
    ".Lak_%=:\n"
    "v_mfma_f32_16x16x32_bf16 v[20:23], a[72:75], v[80:83], 0\n"
    "v_cvt_pk_bf16_f32 v36, v36, v37\n"
    "v_cvt_pk_bf16_f32 v37, v38, v39\n"
    "v_mfma_f32_16x16x32_bf16 v[24:27], a[72:75], v[84:87], 0\n"
    "ds_write_b64 v6, v[36:37]\n"
    "s_lshl_b32 s51, %2, 2\n"
    "s_add_u32 s51, s51, 36864\n"
    "v_mov_b32 v11, s51\n"
    "s_waitcnt vmcnt(0)\n"
    "ds_write_b32 v11, v10\n"
    "v_mfma_f32_16x16x32_bf16 v[20:23], a[76:79], v[88:91], v[20:23]\n"
    "v_cvt_pk_bf16_f32 v12, v12, v13\n"
    "v_cvt_pk_bf16_f32 v13, v14, v15\n"
    "v_mfma_f32_16x16x32_bf16 v[24:27], a[76:79], v[92:95], v[24:27]\n"
    "v_cvt_pk_bf16_f32 v16, v16, v17\n"
    "v_cvt_pk_bf16_f32 v17, v18, v19\n"
    "v_mfma_f32_16x16x32_bf16 v[20:23], a[80:83], v[96:99], v[20:23]\n"
    "v_mov_b32 v15, 0\n"
    "v_mov_b32 v19, 0\n"
    "v_mfma_f32_16x16x32_bf16 v[24:27], a[80:83], v[100:103], v[24:27]\n"
    "v_mfma_f32_16x16x32_bf16 v[20:23], a[84:87], v[104:107], v[20:23]\n"
    "v_mfma_f32_16x16x32_bf16 v[24:27], a[84:87], v[108:111], v[24:27]\n"
    "s_waitcnt lgkmcnt(0)\n"
    "s_barrier\n"
    "v_mov_b32 v11, 0x9000\n"
    "ds_read_b32 v10, v11\n"
    "ds_read_b32 v14, v7\n"
    "ds_read_b32 v74, v8\n"
    "ds_read_b64 v[58:59], v9\n"
    "ds_read_b32 v18, v7 offset:512\n"
    "ds_read_b32 v75, v8 offset:512\n"
    "ds_read_b64 v[62:63], v9 offset:512\n"
    "v_mfma_f32_16x16x32_bf16 v[20:23], a[88:91], v[112:115], v[20:23]\n"
    "v_mfma_f32_16x16x32_bf16 v[24:27], a[88:91], v[116:119], v[24:27]\n"
    "v_mfma_f32_16x16x32_bf16 v[20:23], a[92:95], v[120:123], v[20:23]\n"
    "v_mfma_f32_16x16x32_bf16 v[24:27], a[92:95], v[124:127], v[24:27]\n"
    "v_mfma_f32_16x16x32_bf16 v[20:23], a[96:99], v[128:131], v[20:23]\n"
    "v_mfma_f32_16x16x32_bf16 v[24:27], a[96:99], v[132:135], v[24:27]\n"
    "v_mfma_f32_16x16x32_bf16 v[20:23], a[100:103], v[136:139], v[20:23]\n"
    "v_mfma_f32_16x16x32_bf16 v[24:27], a[100:103], v[140:143], v[24:27]\n"
    "v_mfma_f32_16x16x32_bf16 v[20:23], a[104:107], v[144:147], v[20:23]\n"
    "s_waitcnt lgkmcnt(6)\n"
    "v_readfirstlane_b32 s51, v10\n"
    "s_cmp_ge_u32 s51, 4\n"
    "s_cselect_b32 s49, 1, 0\n"
    "s_cmp_eq_u32 s48, 63\n"
    "s_cselect_b32 s49, 0, s49\n"
    "v_mfma_f32_16x16x32_bf16 v[24:27], a[104:107], v[148:151], v[24:27]\n"
    "v_mfma_f32_16x16x32_bf16 v[20:23], a[108:111], v[152:155], v[20:23]\n"
    "v_mfma_f32_16x16x32_bf16 v[24:27], a[108:111], v[156:159], v[24:27]\n"
    "v_mfma_f32_16x16x32_bf16 v[20:23], a[112:115], v[160:163], v[20:23]\n"
    "v_mfma_f32_16x16x32_bf16 v[24:27], a[112:115], v[164:167], v[24:27]\n"
    "v_mfma_f32_16x16x32_bf16 v[20:23], a[116:119], v[168:171], v[20:23]\n"
    "v_mfma_f32_16x16x32_bf16 v[24:27], a[116:119], v[172:175], v[24:27]\n"
    "v_mfma_f32_16x16x32_bf16 v[20:23], a[120:123], v[176:179], v[20:23]\n"
    "v_mfma_f32_16x16x32_bf16 v[24:27], a[120:123], v[180:183], v[24:27]\n"
    "v_mfma_f32_16x16x32_bf16 v[20:23], a[124:127], v[184:187], v[20:23]\n"
    "v_mfma_f32_16x16x32_bf16 v[24:27], a[124:127], v[188:191], v[24:27]\n"
    "v_mfma_f32_16x16x32_bf16 v[20:23], a[128:131], v[192:195], v[20:23]\n"
    "v_mfma_f32_16x16x32_bf16 v[24:27], a[128:131], v[196:199], v[24:27]\n"
    "v_mfma_f32_16x16x32_bf16 v[20:23], a[132:135], v[200:203], v[20:23]\n"
    "v_mfma_f32_16x16x32_bf16 v[24:27], a[132:135], v[204:207], v[24:27]\n"
    "v_mfma_f32_16x16x32_bf16 v[20:23], a[136:139], v[208:211], v[20:23]\n"
    "v_mfma_f32_16x16x32_bf16 v[24:27], a[136:139], v[212:215], v[24:27]\n"
    "v_mfma_f32_16x16x32_bf16 v[20:23], a[140:143], v[216:219], v[20:23]\n"
    "v_mfma_f32_16x16x32_bf16 v[24:27], a[140:143], v[220:223], v[24:27]\n"
    "v_mfma_f32_16x16x32_bf16 v[28:31], v[80:83], a[144:147], 0\n"
    "v_mfma_f32_16x16x32_bf16 v[32:35], v[84:87], a[144:147], 0\n"
    "v_mfma_f32_16x16x32_bf16 v[28:31], v[88:91], a[148:151], v[28:31]\n"
    "v_cvt_pk_bf16_f32 v20, v20, v21\n"
    "v_cvt_pk_bf16_f32 v21, v22, v23\n"
    "v_mfma_f32_16x16x32_bf16 v[32:35], v[92:95], a[148:151], v[32:35]\n"
    "v_cvt_pk_bf16_f32 v24, v24, v25\n"
    "v_cvt_pk_bf16_f32 v25, v26, v27\n"
    "v_mfma_f32_16x16x32_bf16 v[28:31], v[96:99], a[152:155], v[28:31]\n"
    "s_waitcnt lgkmcnt(4)\n"
    "v_mov_b32 v22, v74\n"
    "v_mov_b32 v23, 0\n"
    "v_mfma_f32_16x16x32_bf16 v[80:83], v[40:43], v[12:15], v[64:67]\n"
    "v_mfma_f32_16x16x32_bf16 v[32:35], v[100:103], a[152:155], v[32:35]\n"
    "s_waitcnt lgkmcnt(1)\n"
    "v_mov_b32 v26, v75\n"
    "v_mov_b32 v27, 0\n"
    "v_mfma_f32_16x16x32_bf16 v[88:91], v[44:47], v[16:19], v[64:67]\n"
    "v_mfma_f32_16x16x32_bf16 v[28:31], v[104:107], a[156:159], v[28:31]\n"
    "v_mfma_f32_16x16x32_bf16 v[84:87], v[20:23], v[12:15], v[68:71]\n"
    "v_mfma_f32_16x16x32_bf16 v[92:95], v[24:27], v[16:19], v[68:71]\n"
    "v_mfma_f32_16x16x32_bf16 v[32:35], v[108:111], a[156:159], v[32:35]\n"
    "v_mov_b32 v40, v20\n"
    "v_mov_b32 v41, v21\n"
    "v_mov_b32 v42, v22\n"
    "v_mfma_f32_16x16x32_bf16 v[28:31], v[112:115], a[160:163], v[28:31]\n"
    "v_mov_b32 v44, v24\n"
    "v_mov_b32 v45, v25\n"
    "v_mov_b32 v46, v26\n"
    "v_mfma_f32_16x16x32_bf16 v[32:35], v[116:119], a[160:163], v[32:35]\n"
    "v_maximum3_f32 v96, v80, v81, v82\n"
    "v_maximum3_f32 v98, v88, v89, v90\n"
    "v_mfma_f32_16x16x32_bf16 v[28:31], v[120:123], a[164:167], v[28:31]\n"
    "v_maximum3_f32 v96, v96, v83, v84\n"
    "v_maximum3_f32 v98, v98, v91, v92\n"
    "v_mfma_f32_16x16x32_bf16 v[32:35], v[124:127], a[164:167], v[32:35]\n"
    "v_maximum3_f32 v96, v96, v85, v86\n"
    "v_maximum3_f32 v98, v98, v93, v94\n"
    "v_mfma_f32_16x16x32_bf16 v[28:31], v[128:131], a[168:171], v[28:31]\n"
    "v_maximum3_f32 v96, v96, v87, v87\n"
    "v_maximum3_f32 v98, v98, v95, v95\n"
    "v_mfma_f32_16x16x32_bf16 v[32:35], v[132:135], a[168:171], v[32:35]\n"
    "v_mov_b32 v97, v96\n"
    "v_mov_b32 v99, v98\n"
    "v_mfma_f32_16x16x32_bf16 v[28:31], v[136:139], a[172:175], v[28:31]\n"
    "v_permlane16_swap_b32 v97, v96\n"
    "v_permlane16_swap_b32 v99, v98\n"
    "v_mfma_f32_16x16x32_bf16 v[32:35], v[140:143], a[172:175], v[32:35]\n"
    "v_maximum3_f32 v96, v96, v97, v97\n"
    "v_maximum3_f32 v98, v98, v99, v99\n"
    "v_mfma_f32_16x16x32_bf16 v[28:31], v[144:147], a[176:179], v[28:31]\n"
    "v_mov_b32 v97, v96\n"
    "v_mov_b32 v99, v98\n"
    "v_mfma_f32_16x16x32_bf16 v[32:35], v[148:151], a[176:179], v[32:35]\n"
    "v_permlane32_swap_b32 v97, v96\n"
    "v_permlane32_swap_b32 v99, v98\n"
    "v_mfma_f32_16x16x32_bf16 v[28:31], v[152:155], a[180:183], v[28:31]\n"
    "v_maximum3_f32 v96, v96, v97, v97\n"
    "v_maximum3_f32 v98, v98, v99, v99\n"
    "v_mfma_f32_16x16x32_bf16 v[32:35], v[156:159], a[180:183], v[32:35]\n"
    "v_mul_f32 v96, 0xbeae1a93, v96\n"
    "v_mul_f32 v98, 0xbeae1a93, v98\n"
    "s_cmp_eq_u32 s49, 0\n"
    "s_cbranch_scc1 .Lac11_%=\n"
    "buffer_load_dwordx4 v[112:115], v3, s[40:43], s58 offen sc1\n"
    "buffer_load_dwordx4 v[116:119], v3, s[40:43], s58 offen offset:128 sc1\n"
    "buffer_load_dwordx4 v[120:123], v3, s[40:43], s58 offen offset:256 sc1\n"
    "buffer_load_dwordx4 v[124:127], v3, s[40:43], s58 offen offset:384 sc1\n"
    "buffer_load_dwordx4 v[128:131], v3, s[40:43], s58 offen offset:512 sc1\n"
    "buffer_load_dwordx4 v[132:135], v3, s[40:43], s58 offen offset:640 sc1\n"
    "buffer_load_dwordx4 v[136:139], v3, s[40:43], s58 offen offset:768 sc1\n"
    "buffer_load_dwordx4 v[140:143], v3, s[40:43], s58 offen offset:896 sc1\n"
    "buffer_load_dwordx4 v[144:147], v3, s[40:43], s58 offen offset:1024 sc1\n"
    ".Lac11_%=:\n"
    "v_mfma_f32_16x16x32_bf16 v[28:31], v[160:163], a[184:187], v[28:31]\n"
    "v_fmamk_f32 v80, v80, 0x3eae1a93, v96\n"
    "v_fmamk_f32 v88, v88, 0x3eae1a93, v98\n"
    "v_mfma_f32_16x16x32_bf16 v[32:35], v[164:167], a[184:187], v[32:35]\n"
    "v_fmamk_f32 v81, v81, 0x3eae1a93, v96\n"
    "v_fmamk_f32 v89, v89, 0x3eae1a93, v98\n"
    "v_mfma_f32_16x16x32_bf16 v[28:31], v[168:171], a[188:191], v[28:31]\n"
    "v_fmamk_f32 v82, v82, 0x3eae1a93, v96\n"
    "v_fmamk_f32 v90, v90, 0x3eae1a93, v98\n"
    "v_mfma_f32_16x16x32_bf16 v[32:35], v[172:175], a[188:191], v[32:35]\n"
    "v_fmamk_f32 v83, v83, 0x3eae1a93, v96\n"
    "v_fmamk_f32 v91, v91, 0x3eae1a93, v98\n"
    "v_mfma_f32_16x16x32_bf16 v[28:31], v[176:179], a[192:195], v[28:31]\n"
    "v_fmamk_f32 v84, v84, 0x3eae1a93, v96\n"
    "v_fmamk_f32 v92, v92, 0x3eae1a93, v98\n"
    "v_mfma_f32_16x16x32_bf16 v[32:35], v[180:183], a[192:195], v[32:35]\n"
    "v_fmamk_f32 v85, v85, 0x3eae1a93, v96\n"
    "v_fmamk_f32 v93, v93, 0x3eae1a93, v98\n"
    "v_mfma_f32_16x16x32_bf16 v[28:31], v[184:187], a[196:199], v[28:31]\n"
    "v_fmamk_f32 v86, v86, 0x3eae1a93, v96\n"
    "v_fmamk_f32 v94, v94, 0x3eae1a93, v98\n"
    "v_mfma_f32_16x16x32_bf16 v[32:35], v[188:191], a[196:199], v[32:35]\n"
    "v_fmamk_f32 v87, v87, 0x3eae1a93, v96\n"
    "v_fmamk_f32 v95, v95, 0x3eae1a93, v98\n"
    "v_mfma_f32_16x16x32_bf16 v[28:31], v[192:195], a[200:203], v[28:31]\n"
    "v_exp_f32 v80, v80\n"
    "v_exp_f32 v88, v88\n"
    "v_mfma_f32_16x16x32_bf16 v[32:35], v[196:199], a[200:203], v[32:35]\n"
    "v_exp_f32 v81, v81\n"
    "v_exp_f32 v89, v89\n"
    "v_mfma_f32_16x16x32_bf16 v[28:31], v[200:203], a[204:207], v[28:31]\n"
    "v_exp_f32 v82, v82\n"
    "v_exp_f32 v90, v90\n"
    "v_mfma_f32_16x16x32_bf16 v[32:35], v[204:207], a[204:207], v[32:35]\n"
    "v_exp_f32 v83, v83\n"
    "v_exp_f32 v91, v91\n"
    "v_mfma_f32_16x16x32_bf16 v[28:31], v[208:211], a[208:211], v[28:31]\n"
    "v_exp_f32 v84, v84\n"
    "v_exp_f32 v92, v92\n"
    "v_mfma_f32_16x16x32_bf16 v[32:35], v[212:215], a[208:211], v[32:35]\n"
    "v_exp_f32 v85, v85\n"
    "v_exp_f32 v93, v93\n"
    "v_mfma_f32_16x16x32_bf16 v[28:31], v[216:219], a[212:215], v[28:31]\n"
    "v_exp_f32 v86, v86\n"
    "v_exp_f32 v94, v94\n"
    "v_mfma_f32_16x16x32_bf16 v[32:35], v[220:223], a[212:215], v[32:35]\n"
    "v_exp_f32 v87, v87\n"
    "v_exp_f32 v95, v95\n"
    "v_cvt_pk_bf16_f32 v80, v80, v81\n"
    "v_cvt_pk_bf16_f32 v88, v88, v89\n"
    "v_cvt_pk_bf16_f32 v81, v82, v83\n"
    "v_cvt_pk_bf16_f32 v89, v90, v91\n"
    "v_cvt_pk_bf16_f32 v82, v84, v85\n"
    "v_cvt_pk_bf16_f32 v90, v92, v93\n"
    "v_cvt_pk_bf16_f32 v83, v86, v87\n"
    "v_cvt_pk_bf16_f32 v91, v94, v95\n"
    "s_nop 0\n"
    "v_mfma_f32_16x16x32_bf16 v[148:151], v[56:59], v[80:83], 0\n"
    "s_waitcnt lgkmcnt(0)\n"
    "v_mfma_f32_16x16x32_bf16 v[152:155], v[60:63], v[88:91], 0\n"
    "v_cvt_pk_bf16_f32 v50, v28, v29\n"
    "v_cvt_pk_bf16_f32 v51, v30, v31\n"
    "v_cvt_pk_bf16_f32 v54, v32, v33\n"
    "v_cvt_pk_bf16_f32 v55, v34, v35\n"
    "v_mfma_f32_16x16x32_bf16 v[100:103], v[48:51], v[80:83], 0\n"
    "s_nop 0\n"
    "v_mfma_f32_16x16x32_bf16 v[104:107], v[52:55], v[88:91], 0\n"
    "v_cmp_le_i32 vcc, 9, v72\n"
    "s_nop 1\n"
    "v_cndmask_b32 v64, v73, v15, vcc\n"
    "v_cmp_le_i32 vcc, 8, v72\n"
    "s_nop 1\n"
    "v_cndmask_b32 v65, v73, v15, vcc\n"
    "v_cmp_le_i32 vcc, 7, v72\n"
    "s_nop 1\n"
    "v_cndmask_b32 v66, v73, v15, vcc\n"
    "v_cmp_le_i32 vcc, 6, v72\n"
    "s_nop 1\n"
    "v_cndmask_b32 v67, v73, v15, vcc\n"
    "v_rcp_f32 v96, v150\n"
    "v_rcp_f32 v98, v154\n"
    "v_mov_b64 v[48:49], v[50:51]\n"
    "v_mov_b64 v[56:57], v[58:59]\n"
    "v_mov_b64 v[52:53], v[54:55]\n"
    "v_mov_b64 v[60:61], v[62:63]\n"
    "v_pk_mul_f32 v[148:149], v[148:149], v[96:97] op_sel_hi:[1,0]\n"
    "v_pk_mul_f32 v[152:153], v[152:153], v[98:99] op_sel_hi:[1,0]\n"
    "v_pk_mul_f32 v[100:101], v[100:101], v[96:97] op_sel_hi:[1,0]\n"
    "v_pk_mul_f32 v[102:103], v[102:103], v[96:97] op_sel_hi:[1,0]\n"
    "v_pk_mul_f32 v[104:105], v[104:105], v[98:99] op_sel_hi:[1,0]\n"
    "v_pk_mul_f32 v[106:107], v[106:107], v[98:99] op_sel_hi:[1,0]\n"
    "v_cvt_pk_bf16_f32 v100, v100, v101\n"
    "v_cvt_pk_bf16_f32 v101, v102, v103\n"
    "v_cvt_pk_bf16_f32 v102, v104, v105\n"
    "v_cvt_pk_bf16_f32 v103, v106, v107\n"
    "buffer_store_dwordx4 v[100:103], v4, s[40:43], s62 offen sc1\n"
    "v_cvt_pk_bf16_f32 v148, v148, v149\n"
    "v_cvt_pk_bf16_f32 v149, v152, v153\n"
    "buffer_store_dwordx2 v[148:149], v5, s[40:43], s62 offen sc1\n"
    "s_cmp_lg_u32 s49, 0\n"
    "s_cbranch_scc1 .Lago_%=\n"
    "s_waitcnt vmcnt(0) lgkmcnt(0)\n"
    "s_barrier\n"
    "s_cmp_eq_u32 %2, 0\n"
    "s_cselect_b32 s50, 1, 0\n"
    "s_cmp_eq_u32 s50, 0\n"
    "s_cbranch_scc1 .Lac12_%=\n"
    "s_sub_u32 s51, s48, 0\n"
    "s_mul_i32 s51, s51, 64\n"
    "s_add_u32 s51, s51, %5\n"
    "v_mov_b32 v11, s51\n"
    "v_mov_b32 v10, 1\n"
    "s_mov_b64 exec, 1\n"
    "global_atomic_add v11, v10, %1\n"
    "s_mov_b64 exec, -1\n"
    ".Lac12_%=:\n"
    "s_cmp_eq_u32 s48, 63\n"
    "s_cbranch_scc1 .Laend_%=\n"
    ".Lawait_%=:\n"
    "s_cmp_eq_u32 %2, 0\n"
    "s_cbranch_scc0 .Lanp_%=\n"
    "s_add_u32 s52, s48, 1\n"
    "s_and_b32 s52, s52, 63\n"
    "s_mul_i32 s52, s52, 64\n"
    "s_add_u32 s52, s52, %4\n"
    "s_mov_b32 s53, 0\n"
    "v_mov_b32 v11, s52\n"
    ".Lap13_%=:\n"
    "global_load_dword v10, v11, %1 sc1\n"
    "s_waitcnt vmcnt(0)\n"
    "v_readfirstlane_b32 s51, v10\n"
    "s_cmp_ge_u32 s51, 4\n"
    "s_cbranch_scc1 .Lap13_x%=\n"
    "s_add_u32 s53, s53, 1\n"
    "s_sleep 2\n"
    "s_cmp_lt_u32 s53, s54\n"
    "s_cbranch_scc1 .Lap13_%=\n"
    "s_mov_b32 s54, 0\n"
    "v_mov_b32 v11, 0\n"
    "v_mov_b32 v10, 1\n"
    "global_store_dword v11, v10, %1 sc1\n"
    ".Lap13_x%=:\n"
    ".Lanp_%=:\n"
    "s_barrier\n"
    "buffer_load_dwordx4 v[112:115], v3, s[40:43], s58 offen sc1\n"
    "buffer_load_dwordx4 v[116:119], v3, s[40:43], s58 offen offset:128 sc1\n"
    "buffer_load_dwordx4 v[120:123], v3, s[40:43], s58 offen offset:256 sc1\n"
    "buffer_load_dwordx4 v[124:127], v3, s[40:43], s58 offen offset:384 sc1\n"
    "buffer_load_dwordx4 v[128:131], v3, s[40:43], s58 offen offset:512 sc1\n"
    "buffer_load_dwordx4 v[132:135], v3, s[40:43], s58 offen offset:640 sc1\n"
    "buffer_load_dwordx4 v[136:139], v3, s[40:43], s58 offen offset:768 sc1\n"
    "buffer_load_dwordx4 v[140:143], v3, s[40:43], s58 offen offset:896 sc1\n"
    "buffer_load_dwordx4 v[144:147], v3, s[40:43], s58 offen offset:1024 sc1\n"
    "s_waitcnt vmcnt(8)\n"
    "ds_write_b128 v2, v[112:115]\n"
    "s_waitcnt vmcnt(7)\n"
    "ds_write_b128 v2, v[116:119] offset:2048\n"
    "s_waitcnt vmcnt(6)\n"
    "ds_write_b128 v2, v[120:123] offset:4096\n"
    "s_waitcnt vmcnt(5)\n"
    "ds_write_b128 v2, v[124:127] offset:6144\n"
    "s_waitcnt vmcnt(4)\n"
    "ds_write_b128 v2, v[128:131] offset:8192\n"
    "s_waitcnt vmcnt(3)\n"
    "ds_write_b128 v2, v[132:135] offset:10240\n"
    "s_waitcnt vmcnt(2)\n"
    "ds_write_b128 v2, v[136:139] offset:12288\n"
    "s_waitcnt vmcnt(1)\n"
    "ds_write_b128 v2, v[140:143] offset:14336\n"
    "s_waitcnt vmcnt(0)\n"
    "ds_write_b128 v2, v[144:147] offset:16384\n"
    "s_mov_b32 s50, 0\n"
    "s_branch .Lanext_%=\n"
    ".Lago_%=:\n"
    "s_waitcnt vmcnt(2)\n"
    "ds_write_b128 v2, v[112:115]\n"
    "ds_write_b128 v2, v[116:119] offset:2048\n"
    "ds_write_b128 v2, v[120:123] offset:4096\n"
    "ds_write_b128 v2, v[124:127] offset:6144\n"
    "ds_write_b128 v2, v[128:131] offset:8192\n"
    "ds_write_b128 v2, v[132:135] offset:10240\n"
    "ds_write_b128 v2, v[136:139] offset:12288\n"
    "ds_write_b128 v2, v[140:143] offset:14336\n"
    "ds_write_b128 v2, v[144:147] offset:16384\n"
    "s_waitcnt vmcnt(0)\n"
    "s_cmp_eq_u32 %2, 0\n"
    "s_cselect_b32 s50, 1, 0\n"
    ".Lanext_%=:\n"
    "s_add_u32 s48, s48, 1\n"
    "s_add_u32 s58, s58, 36864\n"
    "s_add_u32 s62, s62, 36864\n"
    "s_branch .Latop_%=\n"
    ".Laend_%=:\n"
    "s_waitcnt vmcnt(0)\n"
    "s_endpgm\n"
    ".p2align 6\n"
    ".Lrole4_%=:\n"
    "s_mov_b64 s[40:41], %0\n"
    "s_and_b32 s41, s41, 0xffff\n"
    "s_mov_b32 s42, 0xc800000\n"
    "s_mov_b32 s43, 0x20000\n"
    "s_mov_b32 s54, 131072\n"
    "v_mbcnt_lo_u32_b32 v20, -1, 0\n"
    "v_mbcnt_hi_u32_b32 v20, -1, v20\n"
    "s_lshl_b32 s51, %2, 8\n"
    "s_add_u32 s51, s51, 147456\n"
    "v_lshl_add_u32 v20, v20, 2, s51\n"
    "ds_read_b32 v0, v20\n"
    "ds_read_b32 v1, v20 offset:1024\n"
    "ds_read_b32 v2, v20 offset:2048\n"
    "ds_read_b32 v3, v20 offset:3072\n"
    "ds_read_b32 v4, v20 offset:4096\n"
    "ds_read_b32 v7, v20 offset:5120\n"
    "ds_read_b32 v8, v20 offset:6144\n"
    "ds_read_b32 v9, v20 offset:7168\n"
    "ds_read_b32 v10, v20 offset:8192\n"
    "s_waitcnt lgkmcnt(0)\n"
    "s_barrier\n"
    "s_lshl_b32 s56, %2, 10\n"
    "s_mov_b64 s[44:45], %10\n"
    "s_and_b32 s45, s45, 0xffff\n"
    "s_mov_b32 s46, 0xa2000\n"
    "s_mov_b32 s47, 0x20000\n"
    "s_add_u32 m0, s56, 0\n"
    "s_add_u32 s57, %8, 0\n"
    "buffer_load_dwordx4 v8, s[44:47], s57 offen lds\n"
    "s_add_u32 m0, s56, 36864\n"
    "s_add_u32 s57, %8, 64\n"
    "buffer_load_dwordx4 v8, s[44:47], s57 offen lds\n"
    "s_add_u32 m0, s56, 73728\n"
    "s_add_u32 s57, %8, 128\n"
    "buffer_load_dwordx4 v8, s[44:47], s57 offen lds\n"
    "s_add_u32 m0, s56, 4096\n"
    "s_add_u32 s57, %8, 73728\n"
    "buffer_load_dwordx4 v8, s[44:47], s57 offen lds\n"
    "s_add_u32 m0, s56, 40960\n"
    "s_add_u32 s57, %8, 73792\n"
    "buffer_load_dwordx4 v8, s[44:47], s57 offen lds\n"
    "s_add_u32 m0, s56, 77824\n"
    "s_add_u32 s57, %8, 73856\n"
    "buffer_load_dwordx4 v8, s[44:47], s57 offen lds\n"
    "s_add_u32 m0, s56, 8192\n"
    "s_add_u32 s57, %8, 147456\n"
    "buffer_load_dwordx4 v8, s[44:47], s57 offen lds\n"
    "s_add_u32 m0, s56, 45056\n"
    "s_add_u32 s57, %8, 147520\n"
    "buffer_load_dwordx4 v8, s[44:47], s57 offen lds\n"
    "s_add_u32 m0, s56, 81920\n"
    "s_add_u32 s57, %8, 147584\n"
    "buffer_load_dwordx4 v8, s[44:47], s57 offen lds\n"
    "s_add_u32 m0, s56, 12288\n"
    "s_add_u32 s57, %8, 221184\n"
    "buffer_load_dwordx4 v8, s[44:47], s57 offen lds\n"
    "s_add_u32 m0, s56, 49152\n"
    "s_add_u32 s57, %8, 221248\n"
    "buffer_load_dwordx4 v8, s[44:47], s57 offen lds\n"
    "s_add_u32 m0, s56, 86016\n"
    "s_add_u32 s57, %8, 221312\n"
    "buffer_load_dwordx4 v8, s[44:47], s57 offen lds\n"
    "s_add_u32 m0, s56, 16384\n"
    "s_add_u32 s57, %8, 294912\n"
    "buffer_load_dwordx4 v8, s[44:47], s57 offen lds\n"
    "s_add_u32 m0, s56, 53248\n"
    "s_add_u32 s57, %8, 294976\n"
    "buffer_load_dwordx4 v8, s[44:47], s57 offen lds\n"
    "s_add_u32 m0, s56, 90112\n"
    "s_add_u32 s57, %8, 295040\n"
    "buffer_load_dwordx4 v8, s[44:47], s57 offen lds\n"
    "s_add_u32 m0, s56, 20480\n"
    "s_add_u32 s57, %8, 368640\n"
    "buffer_load_dwordx4 v8, s[44:47], s57 offen lds\n"
    "s_add_u32 m0, s56, 57344\n"
    "s_add_u32 s57, %8, 368704\n"
    "buffer_load_dwordx4 v8, s[44:47], s57 offen lds\n"
    "s_add_u32 m0, s56, 94208\n"
    "s_add_u32 s57, %8, 368768\n"
    "buffer_load_dwordx4 v8, s[44:47], s57 offen lds\n"
    "s_add_u32 m0, s56, 24576\n"
    "s_add_u32 s57, %8, 442368\n"
    "buffer_load_dwordx4 v8, s[44:47], s57 offen lds\n"
    "s_add_u32 m0, s56, 61440\n"
    "s_add_u32 s57, %8, 442432\n"
    "buffer_load_dwordx4 v8, s[44:47], s57 offen lds\n"
    "s_add_u32 m0, s56, 98304\n"
    "s_add_u32 s57, %8, 442496\n"
    "buffer_load_dwordx4 v8, s[44:47], s57 offen lds\n"
    "s_add_u32 m0, s56, 28672\n"
    "s_add_u32 s57, %8, 516096\n"
    "buffer_load_dwordx4 v8, s[44:47], s57 offen lds\n"
    "s_add_u32 m0, s56, 65536\n"
    "s_add_u32 s57, %8, 516160\n"
    "buffer_load_dwordx4 v8, s[44:47], s57 offen lds\n"
    "s_add_u32 m0, s56, 102400\n"
    "s_add_u32 s57, %8, 516224\n"
    "buffer_load_dwordx4 v8, s[44:47], s57 offen lds\n"
    "s_add_u32 m0, s56, 32768\n"
    "s_add_u32 s57, %8, 589824\n"
    "buffer_load_dwordx4 v8, s[44:47], s57 offen lds\n"
    "s_add_u32 m0, s56, 69632\n"
    "s_add_u32 s57, %8, 589888\n"
    "buffer_load_dwordx4 v8, s[44:47], s57 offen lds\n"
    "s_add_u32 m0, s56, 106496\n"
    "s_add_u32 s57, %8, 589952\n"
    "buffer_load_dwordx4 v8, s[44:47], s57 offen lds\n"
    "s_waitcnt vmcnt(0) lgkmcnt(0)\n"
    "s_barrier\n"
    "ds_read_b64_tr_b16 a[0:1], v9\n"
    "ds_read_b64_tr_b16 a[2:3], v9 offset:1280\n"
    "ds_read_b64_tr_b16 a[4:5], v9 offset:36864\n"
    "ds_read_b64_tr_b16 a[6:7], v9 offset:38144\n"
    "v_add_u32 v17, 0x10000, v9\n"
    "ds_read_b64_tr_b16 a[8:9], v17 offset:8192\n"
    "ds_read_b64_tr_b16 a[10:11], v17 offset:9472\n"
    "ds_read_b64_tr_b16 a[12:13], v9 offset:2304\n"
    "ds_read_b64_tr_b16 a[14:15], v9 offset:3584\n"
    "ds_read_b64_tr_b16 a[16:17], v9 offset:39168\n"
    "ds_read_b64_tr_b16 a[18:19], v9 offset:40448\n"
    "ds_read_b64_tr_b16 a[20:21], v17 offset:10496\n"
    "ds_read_b64_tr_b16 a[22:23], v17 offset:11776\n"
    "ds_read_b64_tr_b16 a[24:25], v9 offset:4608\n"
    "ds_read_b64_tr_b16 a[26:27], v9 offset:5888\n"
    "ds_read_b64_tr_b16 a[28:29], v9 offset:41472\n"
    "ds_read_b64_tr_b16 a[30:31], v9 offset:42752\n"
    "ds_read_b64_tr_b16 a[32:33], v17 offset:12800\n"
    "ds_read_b64_tr_b16 a[34:35], v17 offset:14080\n"
    "ds_read_b64_tr_b16 a[36:37], v9 offset:6912\n"
    "ds_read_b64_tr_b16 a[38:39], v9 offset:8192\n"
    "ds_read_b64_tr_b16 a[40:41], v9 offset:43776\n"
    "ds_read_b64_tr_b16 a[42:43], v9 offset:45056\n"
    "ds_read_b64_tr_b16 a[44:45], v17 offset:15104\n"
    "ds_read_b64_tr_b16 a[46:47], v17 offset:16384\n"
    "ds_read_b64_tr_b16 a[48:49], v9 offset:9216\n"
    "ds_read_b64_tr_b16 a[50:51], v9 offset:10496\n"
    "ds_read_b64_tr_b16 a[52:53], v9 offset:46080\n"
    "ds_read_b64_tr_b16 a[54:55], v9 offset:47360\n"
    "ds_read_b64_tr_b16 a[56:57], v17 offset:17408\n"
    "ds_read_b64_tr_b16 a[58:59], v17 offset:18688\n"
    "ds_read_b64_tr_b16 a[60:61], v9 offset:11520\n"
    "ds_read_b64_tr_b16 a[62:63], v9 offset:12800\n"
    "ds_read_b64_tr_b16 a[64:65], v9 offset:48384\n"
    "ds_read_b64_tr_b16 a[66:67], v9 offset:49664\n"
    "ds_read_b64_tr_b16 a[68:69], v17 offset:19712\n"
    "ds_read_b64_tr_b16 a[70:71], v17 offset:20992\n"
    "ds_read_b64_tr_b16 a[72:73], v9 offset:13824\n"
    "ds_read_b64_tr_b16 a[74:75], v9 offset:15104\n"
    "ds_read_b64_tr_b16 a[76:77], v9 offset:50688\n"
    "ds_read_b64_tr_b16 a[78:79], v9 offset:51968\n"
    "ds_read_b64_tr_b16 a[80:81], v17 offset:22016\n"
    "ds_read_b64_tr_b16 a[82:83], v17 offset:23296\n"
    "ds_read_b64_tr_b16 a[84:85], v9 offset:16128\n"
    "ds_read_b64_tr_b16 a[86:87], v9 offset:17408\n"
    "ds_read_b64_tr_b16 a[88:89], v9 offset:52992\n"
    "ds_read_b64_tr_b16 a[90:91], v9 offset:54272\n"
    "ds_read_b64_tr_b16 a[92:93], v17 offset:24320\n"
    "ds_read_b64_tr_b16 a[94:95], v17 offset:25600\n"
    "ds_read_b64_tr_b16 a[96:97], v9 offset:18432\n"
    "ds_read_b64_tr_b16 a[98:99], v9 offset:19712\n"
    "ds_read_b64_tr_b16 a[100:101], v9 offset:55296\n"
    "ds_read_b64_tr_b16 a[102:103], v9 offset:56576\n"
    "ds_read_b64_tr_b16 a[104:105], v17 offset:26624\n"
    "ds_read_b64_tr_b16 a[106:107], v17 offset:27904\n"
    "ds_read_b64_tr_b16 a[108:109], v9 offset:20736\n"
    "ds_read_b64_tr_b16 a[110:111], v9 offset:22016\n"
    "ds_read_b64_tr_b16 a[112:113], v9 offset:57600\n"
    "ds_read_b64_tr_b16 a[114:115], v9 offset:58880\n"
    "ds_read_b64_tr_b16 a[116:117], v17 offset:28928\n"
    "ds_read_b64_tr_b16 a[118:119], v17 offset:30208\n"
    "ds_read_b64_tr_b16 a[120:121], v9 offset:23040\n"
    "ds_read_b64_tr_b16 a[122:123], v9 offset:24320\n"
    "ds_read_b64_tr_b16 a[124:125], v9 offset:59904\n"
    "ds_read_b64_tr_b16 a[126:127], v9 offset:61184\n"
    "ds_read_b64_tr_b16 a[128:129], v17 offset:31232\n"
    "ds_read_b64_tr_b16 a[130:131], v17 offset:32512\n"
    "ds_read_b64_tr_b16 a[132:133], v9 offset:25344\n"
    "ds_read_b64_tr_b16 a[134:135], v9 offset:26624\n"
    "ds_read_b64_tr_b16 a[136:137], v9 offset:62208\n"
    "ds_read_b64_tr_b16 a[138:139], v9 offset:63488\n"
    "ds_read_b64_tr_b16 a[140:141], v17 offset:33536\n"
    "ds_read_b64_tr_b16 a[142:143], v17 offset:34816\n"
    "ds_read_b64_tr_b16 a[144:145], v9 offset:27648\n"
    "ds_read_b64_tr_b16 a[146:147], v9 offset:28928\n"
    "ds_read_b64_tr_b16 a[148:149], v9 offset:64512\n"
    "ds_read_b64_tr_b16 a[150:151], v17 offset:256\n"
    "ds_read_b64_tr_b16 a[152:153], v17 offset:35840\n"
    "ds_read_b64_tr_b16 a[154:155], v17 offset:37120\n"
    "ds_read_b64_tr_b16 a[156:157], v9 offset:29952\n"
    "ds_read_b64_tr_b16 a[158:159], v9 offset:31232\n"
    "ds_read_b64_tr_b16 a[160:161], v17 offset:1280\n"
    "ds_read_b64_tr_b16 a[162:163], v17 offset:2560\n"
    "ds_read_b64_tr_b16 a[164:165], v17 offset:38144\n"
    "ds_read_b64_tr_b16 a[166:167], v17 offset:39424\n"
    "ds_read_b64_tr_b16 a[168:169], v9 offset:32256\n"
    "ds_read_b64_tr_b16 a[170:171], v9 offset:33536\n"
    "ds_read_b64_tr_b16 a[172:173], v17 offset:3584\n"
    "ds_read_b64_tr_b16 a[174:175], v17 offset:4864\n"
    "ds_read_b64_tr_b16 a[176:177], v17 offset:40448\n"
    "ds_read_b64_tr_b16 a[178:179], v17 offset:41728\n"
    "ds_read_b64_tr_b16 a[180:181], v9 offset:34560\n"
    "ds_read_b64_tr_b16 a[182:183], v9 offset:35840\n"
    "ds_read_b64_tr_b16 a[184:185], v17 offset:5888\n"
    "ds_read_b64_tr_b16 a[186:187], v17 offset:7168\n"
    "ds_read_b64_tr_b16 a[188:189], v17 offset:42752\n"
    "ds_read_b64_tr_b16 a[190:191], v17 offset:44032\n"
    "ds_read_b64_tr_b16 a[192:193], v10\n"
    "ds_read_b64_tr_b16 a[194:195], v10 offset:2304\n"
    "ds_read_b64_tr_b16 a[196:197], v10 offset:36864\n"
    "ds_read_b64_tr_b16 a[198:199], v10 offset:39168\n"
    "v_add_u32 v16, 0x10000, v10\n"
    "ds_read_b64_tr_b16 a[200:201], v16 offset:8192\n"
    "ds_read_b64_tr_b16 a[202:203], v16 offset:10496\n"
    "ds_read_b64_tr_b16 a[204:205], v10 offset:18432\n"
    "ds_read_b64_tr_b16 a[206:207], v10 offset:20736\n"
    "ds_read_b64_tr_b16 a[208:209], v10 offset:55296\n"
    "ds_read_b64_tr_b16 a[210:211], v10 offset:57600\n"
    "ds_read_b64_tr_b16 a[212:213], v16 offset:26624\n"
    "ds_read_b64_tr_b16 a[214:215], v16 offset:28928\n"
    "s_waitcnt lgkmcnt(0)\n"
    "s_barrier\n"
    "s_mov_b32 s58, %6\n"
    "s_add_u32 s59, %6, 4096\n"
    "s_sub_u32 s62, %7, 73728\n"
    "s_mov_b32 s48, -1\n"
    "s_branch .Lbwait_%=\n"
    ".Lbtop_%=:\n"
    "s_waitcnt lgkmcnt(0)\n"
    "s_barrier\n"
    "ds_read_b128 v[24:27], v0\n"
    "ds_read_b128 v[28:31], v0 offset:1024\n"
    "ds_read_b128 v[32:35], v0 offset:2048\n"
    "ds_read_b128 v[36:39], v0 offset:3072\n"
    "s_cmp_eq_u32 s50, 0\n"
    "s_cbranch_scc1 .Lbc1_%=\n"
    "s_sub_u32 s51, s48, 1\n"
    "s_mul_i32 s51, s51, 64\n"
    "s_add_u32 s51, s51, %5\n"
    "v_mov_b32 v6, s51\n"
    "v_mov_b32 v5, 1\n"
    "s_mov_b64 exec, 1\n"
    "global_atomic_add v6, v5, %1\n"
    "s_mov_b64 exec, -1\n"
    ".Lbc1_%=:\n"
    "s_add_u32 s52, s48, 1\n"
    "s_and_b32 s52, s52, 63\n"
    "s_mul_i32 s52, s52, 64\n"
    "s_add_u32 s52, s52, %4\n"
    "v_mov_b32 v6, s52\n"
    "global_load_dword v5, v6, %1 sc1\n"
    "s_waitcnt lgkmcnt(3)\n"
    "v_mfma_f32_16x16x32_bf16 v[8:11], a[0:3], v[24:27], 0\n"
    "ds_read_b128 v[40:43], v0 offset:4096\n"
    "v_mfma_f32_16x16x32_bf16 v[12:15], a[4:7], v[24:27], 0\n"
    "ds_read_b128 v[44:47], v0 offset:5120\n"
    "v_mfma_f32_16x16x32_bf16 v[16:19], a[8:11], v[24:27], 0\n"
    "ds_read_b128 v[48:51], v0 offset:6144\n"
    "s_waitcnt lgkmcnt(5)\n"
    "v_mfma_f32_16x16x32_bf16 v[8:11], a[12:15], v[28:31], v[8:11]\n"
    "ds_read_b128 v[52:55], v0 offset:7168\n"
    "v_mfma_f32_16x16x32_bf16 v[12:15], a[16:19], v[28:31], v[12:15]\n"
    "ds_read_b128 v[56:59], v0 offset:8192\n"
    "v_mfma_f32_16x16x32_bf16 v[16:19], a[20:23], v[28:31], v[16:19]\n"
    "ds_read_b128 v[60:63], v0 offset:9216\n"
    "s_waitcnt lgkmcnt(7)\n"
    "v_mfma_f32_16x16x32_bf16 v[8:11], a[24:27], v[32:35], v[8:11]\n"
    "ds_read_b128 v[64:67], v0 offset:10240\n"
    "v_mfma_f32_16x16x32_bf16 v[12:15], a[28:31], v[32:35], v[12:15]\n"
    "ds_read_b128 v[68:71], v0 offset:11264\n"
    "v_mfma_f32_16x16x32_bf16 v[16:19], a[32:35], v[32:35], v[16:19]\n"
    "ds_read_b128 v[72:75], v0 offset:12288\n"
    "s_waitcnt lgkmcnt(9)\n"
    "v_mfma_f32_16x16x32_bf16 v[8:11], a[36:39], v[36:39], v[8:11]\n"
    "ds_read_b128 v[76:79], v0 offset:13312\n"
    "v_mfma_f32_16x16x32_bf16 v[12:15], a[40:43], v[36:39], v[12:15]\n"
    "ds_read_b128 v[80:83], v0 offset:14336\n"
    "v_mfma_f32_16x16x32_bf16 v[16:19], a[44:47], v[36:39], v[16:19]\n"
    "ds_read_b128 v[84:87], v0 offset:15360\n"
    "s_waitcnt lgkmcnt(11)\n"
    "v_mfma_f32_16x16x32_bf16 v[8:11], a[48:51], v[40:43], v[8:11]\n"
    "ds_read_b128 v[88:91], v0 offset:16384\n"
    "v_mfma_f32_16x16x32_bf16 v[12:15], a[52:55], v[40:43], v[12:15]\n"
    "ds_read_b128 v[92:95], v0 offset:17408\n"
    "v_mfma_f32_16x16x32_bf16 v[16:19], a[56:59], v[40:43], v[16:19]\n"
    "s_waitcnt lgkmcnt(12)\n"
    "v_mfma_f32_16x16x32_bf16 v[8:11], a[60:63], v[44:47], v[8:11]\n"
    "v_mfma_f32_16x16x32_bf16 v[12:15], a[64:67], v[44:47], v[12:15]\n"
    "v_mfma_f32_16x16x32_bf16 v[16:19], a[68:71], v[44:47], v[16:19]\n"
    "s_waitcnt lgkmcnt(11)\n"
    "v_mfma_f32_16x16x32_bf16 v[8:11], a[72:75], v[48:51], v[8:11]\n"
    "v_mfma_f32_16x16x32_bf16 v[12:15], a[76:79], v[48:51], v[12:15]\n"
    "v_mfma_f32_16x16x32_bf16 v[16:19], a[80:83], v[48:51], v[16:19]\n"
    "s_waitcnt lgkmcnt(10)\n"
    "v_mfma_f32_16x16x32_bf16 v[8:11], a[84:87], v[52:55], v[8:11]\n"
    "v_mfma_f32_16x16x32_bf16 v[12:15], a[88:91], v[52:55], v[12:15]\n"
    "v_mfma_f32_16x16x32_bf16 v[16:19], a[92:95], v[52:55], v[16:19]\n"
    "s_waitcnt lgkmcnt(9)\n"
    "v_mfma_f32_16x16x32_bf16 v[8:11], a[96:99], v[56:59], v[8:11]\n"
    "v_mfma_f32_16x16x32_bf16 v[12:15], a[100:103], v[56:59], v[12:15]\n"
    "v_mfma_f32_16x16x32_bf16 v[16:19], a[104:107], v[56:59], v[16:19]\n"
    "s_waitcnt lgkmcnt(8)\n"
    "v_mfma_f32_16x16x32_bf16 v[8:11], a[108:111], v[60:63], v[8:11]\n"
    "v_mfma_f32_16x16x32_bf16 v[12:15], a[112:115], v[60:63], v[12:15]\n"
    "v_mfma_f32_16x16x32_bf16 v[16:19], a[116:119], v[60:63], v[16:19]\n"
    "s_waitcnt lgkmcnt(7)\n"
    "v_mfma_f32_16x16x32_bf16 v[8:11], a[120:123], v[64:67], v[8:11]\n"
    "v_mfma_f32_16x16x32_bf16 v[12:15], a[124:127], v[64:67], v[12:15]\n"
    "v_mfma_f32_16x16x32_bf16 v[16:19], a[128:131], v[64:67], v[16:19]\n"
    "s_waitcnt lgkmcnt(6)\n"
    "v_mfma_f32_16x16x32_bf16 v[8:11], a[132:135], v[68:71], v[8:11]\n"
    "v_mfma_f32_16x16x32_bf16 v[12:15], a[136:139], v[68:71], v[12:15]\n"
    "v_mfma_f32_16x16x32_bf16 v[16:19], a[140:143], v[68:71], v[16:19]\n"
    "s_waitcnt lgkmcnt(5)\n"
    "v_mfma_f32_16x16x32_bf16 v[8:11], a[144:147], v[72:75], v[8:11]\n"
    "v_mfma_f32_16x16x32_bf16 v[12:15], a[148:151], v[72:75], v[12:15]\n"
    "v_mfma_f32_16x16x32_bf16 v[16:19], a[152:155], v[72:75], v[16:19]\n"
    "s_waitcnt lgkmcnt(4)\n"
    "v_mfma_f32_16x16x32_bf16 v[8:11], a[156:159], v[76:79], v[8:11]\n"
    "v_mfma_f32_16x16x32_bf16 v[12:15], a[160:163], v[76:79], v[12:15]\n"
    "s_lshl_b32 s51, %2, 2\n"
    "s_add_u32 s51, s51, 36864\n"
    "v_mov_b32 v6, s51\n"
    "s_waitcnt vmcnt(0)\n"
    "ds_write_b32 v6, v5\n"
    "v_mfma_f32_16x16x32_bf16 v[16:19], a[164:167], v[76:79], v[16:19]\n"
    "s_waitcnt lgkmcnt(4)\n"
    "v_mfma_f32_16x16x32_bf16 v[8:11], a[168:171], v[80:83], v[8:11]\n"
    "v_mfma_f32_16x16x32_bf16 v[12:15], a[172:175], v[80:83], v[12:15]\n"
    "v_mfma_f32_16x16x32_bf16 v[16:19], a[176:179], v[80:83], v[16:19]\n"
    "s_waitcnt lgkmcnt(0)\n"
    "s_barrier\n"
    "v_mov_b32 v6, 0x9000\n"
    "ds_read_b32 v5, v6\n"
    "v_mfma_f32_16x16x32_bf16 v[8:11], a[180:183], v[84:87], v[8:11]\n"
    "v_mfma_f32_16x16x32_bf16 v[12:15], a[184:187], v[84:87], v[12:15]\n"
    "v_mfma_f32_16x16x32_bf16 v[16:19], a[188:191], v[84:87], v[16:19]\n"
    "v_mfma_f32_16x16x32_bf16 v[8:11], a[192:195], v[88:91], v[8:11]\n"
    "v_mfma_f32_16x16x32_bf16 v[12:15], a[196:199], v[88:91], v[12:15]\n"
    "v_mfma_f32_16x16x32_bf16 v[16:19], a[200:203], v[88:91], v[16:19]\n"
    "v_mfma_f32_16x16x32_bf16 v[8:11], a[204:207], v[92:95], v[8:11]\n"
    "s_waitcnt lgkmcnt(0)\n"
    "v_readfirstlane_b32 s51, v5\n"
    "s_cmp_ge_u32 s51, 8\n"
    "s_cselect_b32 s49, 1, 0\n"
    "s_cmp_eq_u32 s48, 63\n"
    "s_cselect_b32 s49, 0, s49\n"
    "v_mfma_f32_16x16x32_bf16 v[12:15], a[208:211], v[92:95], v[12:15]\n"
    "v_mfma_f32_16x16x32_bf16 v[16:19], a[212:215], v[92:95], v[16:19]\n"
    "s_cmp_eq_u32 s49, 0\n"
    "s_cbranch_scc1 .Lbc2_%=\n"
    "buffer_load_dwordx4 v[100:103], v2, s[40:43], s58 offen sc1\n"
    "buffer_load_dwordx4 v[104:107], v2, s[40:43], s58 offen offset:1024 sc1\n"
    "buffer_load_dwordx4 v[108:111], v2, s[40:43], s58 offen offset:2048 sc1\n"
    "buffer_load_dwordx4 v[112:115], v2, s[40:43], s58 offen offset:3072 sc1\n"
    "buffer_load_dwordx4 v[116:119], v2, s[40:43], s59 offen sc1\n"
    "buffer_load_dwordx4 v[120:123], v2, s[40:43], s59 offen offset:1024 sc1\n"
    "buffer_load_dwordx4 v[124:127], v2, s[40:43], s59 offen offset:2048 sc1\n"
    "buffer_load_dwordx4 v[128:131], v2, s[40:43], s59 offen offset:3072 sc1\n"
    "buffer_load_dwordx4 v[132:135], v7, s[40:43], s58 offen sc1\n"
    ".Lbc2_%=:\n"
    "buffer_store_dwordx4 v[8:11], v3, s[40:43], s62 offen sc1\n"
    "s_nop 1\n"
    "buffer_store_dwordx4 v[12:15], v3, s[40:43], s62 offen offset:64 sc1\n"
    "s_nop 2\n"
    "buffer_store_dwordx4 v[16:19], v3, s[40:43], s62 offen offset:128 sc1\n"
    "s_cmp_lg_u32 s49, 0\n"
    "s_cbranch_scc1 .Lbgo_%=\n"
    "s_waitcnt vmcnt(0) lgkmcnt(0)\n"
    "s_barrier\n"
    "s_cmp_eq_u32 %2, 0\n"
    "s_cselect_b32 s50, 1, 0\n"
    "s_cmp_eq_u32 s50, 0\n"
    "s_cbranch_scc1 .Lbc3_%=\n"
    "s_sub_u32 s51, s48, 0\n"
    "s_mul_i32 s51, s51, 64\n"
    "s_add_u32 s51, s51, %5\n"
    "v_mov_b32 v6, s51\n"
    "v_mov_b32 v5, 1\n"
    "s_mov_b64 exec, 1\n"
    "global_atomic_add v6, v5, %1\n"
    "s_mov_b64 exec, -1\n"
    ".Lbc3_%=:\n"
    "s_cmp_eq_u32 s48, 63\n"
    "s_cbranch_scc1 .Lbend_%=\n"
    ".Lbwait_%=:\n"
    "s_cmp_eq_u32 %2, 0\n"
    "s_cbranch_scc0 .Lbnp_%=\n"
    "s_add_u32 s52, s48, 1\n"
    "s_and_b32 s52, s52, 63\n"
    "s_mul_i32 s52, s52, 64\n"
    "s_add_u32 s52, s52, %4\n"
    "s_mov_b32 s53, 0\n"
    "v_mov_b32 v6, s52\n"
    ".Lbp4_%=:\n"
    "global_load_dword v5, v6, %1 sc1\n"
    "s_waitcnt vmcnt(0)\n"
    "v_readfirstlane_b32 s51, v5\n"
    "s_cmp_ge_u32 s51, 8\n"
    "s_cbranch_scc1 .Lbp4_x%=\n"
    "s_add_u32 s53, s53, 1\n"
    "s_sleep 2\n"
    "s_cmp_lt_u32 s53, s54\n"
    "s_cbranch_scc1 .Lbp4_%=\n"
    "s_mov_b32 s54, 0\n"
    "v_mov_b32 v6, 0\n"
    "v_mov_b32 v5, 1\n"
    "global_store_dword v6, v5, %1 sc1\n"
    ".Lbp4_x%=:\n"
    ".Lbnp_%=:\n"
    "s_barrier\n"
    "buffer_load_dwordx4 v[100:103], v2, s[40:43], s58 offen sc1\n"
    "buffer_load_dwordx4 v[104:107], v2, s[40:43], s58 offen offset:1024 sc1\n"
    "buffer_load_dwordx4 v[108:111], v2, s[40:43], s58 offen offset:2048 sc1\n"
    "buffer_load_dwordx4 v[112:115], v2, s[40:43], s58 offen offset:3072 sc1\n"
    "buffer_load_dwordx4 v[116:119], v2, s[40:43], s59 offen sc1\n"
    "buffer_load_dwordx4 v[120:123], v2, s[40:43], s59 offen offset:1024 sc1\n"
    "buffer_load_dwordx4 v[124:127], v2, s[40:43], s59 offen offset:2048 sc1\n"
    "buffer_load_dwordx4 v[128:131], v2, s[40:43], s59 offen offset:3072 sc1\n"
    "buffer_load_dwordx4 v[132:135], v7, s[40:43], s58 offen sc1\n"
    "s_waitcnt vmcnt(7)\n"
    "v_swap_b32 v102, v104\n"
    "v_swap_b32 v103, v105\n"
    "ds_write_b128 v1, v[100:103]\n"
    "ds_write_b128 v1, v[104:107] offset:18432\n"
    "s_waitcnt vmcnt(5)\n"
    "v_swap_b32 v110, v112\n"
    "v_swap_b32 v111, v113\n"
    "ds_write_b128 v1, v[108:111] offset:1024\n"
    "ds_write_b128 v1, v[112:115] offset:19456\n"
    "s_waitcnt vmcnt(3)\n"
    "v_swap_b32 v118, v120\n"
    "v_swap_b32 v119, v121\n"
    "ds_write_b128 v1, v[116:119] offset:2048\n"
    "ds_write_b128 v1, v[120:123] offset:20480\n"
    "s_waitcnt vmcnt(1)\n"
    "v_swap_b32 v126, v128\n"
    "v_swap_b32 v127, v129\n"
    "ds_write_b128 v1, v[124:127] offset:3072\n"
    "ds_write_b128 v1, v[128:131] offset:21504\n"
    "s_waitcnt vmcnt(0)\n"
    "ds_write_b32 v4, v132\n"
    "ds_write_b32 v4, v133 offset:18432\n"
    "ds_write_b32 v4, v134 offset:16\n"
    "ds_write_b32 v4, v135 offset:18448\n"
    "s_mov_b32 s50, 0\n"
    "s_branch .Lbnext_%=\n"
    ".Lbgo_%=:\n"
    "s_waitcnt vmcnt(3)\n"
    "v_swap_b32 v102, v104\n"
    "v_swap_b32 v103, v105\n"
    "ds_write_b128 v1, v[100:103]\n"
    "ds_write_b128 v1, v[104:107] offset:18432\n"
    "v_swap_b32 v110, v112\n"
    "v_swap_b32 v111, v113\n"
    "ds_write_b128 v1, v[108:111] offset:1024\n"
    "ds_write_b128 v1, v[112:115] offset:19456\n"
    "v_swap_b32 v118, v120\n"
    "v_swap_b32 v119, v121\n"
    "ds_write_b128 v1, v[116:119] offset:2048\n"
    "ds_write_b128 v1, v[120:123] offset:20480\n"
    "v_swap_b32 v126, v128\n"
    "v_swap_b32 v127, v129\n"
    "ds_write_b128 v1, v[124:127] offset:3072\n"
    "ds_write_b128 v1, v[128:131] offset:21504\n"
    "ds_write_b32 v4, v132\n"
    "ds_write_b32 v4, v133 offset:18432\n"
    "ds_write_b32 v4, v134 offset:16\n"
    "ds_write_b32 v4, v135 offset:18448\n"
    "s_waitcnt vmcnt(0)\n"
    "s_cmp_eq_u32 %2, 0\n"
    "s_cselect_b32 s50, 1, 0\n"
    ".Lbnext_%=:\n"
    "s_add_u32 s48, s48, 1\n"
    "s_add_u32 s58, s58, 36864\n"
    "s_add_u32 s59, s59, 36864\n"
    "s_add_u32 s62, s62, 73728\n"
    "s_branch .Lbtop_%=\n"
    ".Lbend_%=:\n"
    "s_waitcnt vmcnt(0)\n"
    ".Lrole5_%=:\n"
        :: "s"(wd), "s"(ws), "s"(wave), "s"(rid), "s"(P.fin), "s"(P.fout), "s"(P.src), "s"(P.dst), "s"(P.woff), "s"(layer), "s"(w0), "s"(Wk), "s"(Wv), "s"(Wo),
           "s"(mem0), "s"(P.role) : CLOBBERS);
}

extern "C" void kernel_launch(void* const* d_in, const int* in_sizes, int n_in, void* d_out, int out_size, void* d_ws,
                              size_t ws_size, hipStream_t stream) {
    hipMemsetAsync(d_ws, 0, SYNC_BYTES, stream);
    mega_kernel<<<256, 256, 0, stream>>>((const u16*)d_in[0], (const u16*)d_in[1], (const u16*)d_in[2], (const u16*)d_in[4],
                                         (const u16*)d_in[5], (const u16*)d_in[6], (const u16*)d_in[7], (const u16*)d_in[8],
                                         (u16*)d_out, (char*)d_ws);
}
```

```cpp
#include <hip/hip_runtime.h>
#include <hip/hip_bf16.h>
#include <stdint.h>

typedef unsigned short u16;
typedef unsigned u32;
typedef unsigned long long u64;
using bf16x8 = __attribute__((ext_vector_type(8))) short;
using bf16x4 = __attribute__((ext_vector_type(4))) short;
using f32x4 = __attribute__((ext_vector_type(4))) float;
using f32x2 = __attribute__((ext_vector_type(2))) float;
using v4u = __attribute__((ext_vector_type(4))) unsigned;
using v2u = __attribute__((ext_vector_type(2))) unsigned;
typedef __bf16 bfv2 __attribute__((ext_vector_type(2)));
#define GAS __attribute__((address_space(1)))
typedef GAS unsigned gu32;
#define RLX_AGENT __ATOMIC_RELAXED, __HIP_MEMORY_SCOPE_AGENT
typedef __amdgpu_buffer_rsrc_t rsrc_t;

constexpr int S_ = 2048, D_ = 576, NSEG = 64, SEG = 32, NB = 4096, NQA = 1728;

constexpr u32 OFF_ABORT = 0;
constexpr u32 OFF_POL = 64;
constexpr u32 OFF_RDY = 128;
constexpr u32 OFF_NEVER = 192;
constexpr u32 OFF_ROLE = 256;
constexpr u32 OFF_FA = 4096;
constexpr u32 OFF_FO = OFF_FA + 8192;
constexpr u32 OFF_FB = OFF_FO + 8192;
constexpr u32 OFF_FP = OFF_FB + 8192;
constexpr u32 OFF_COB = OFF_FP + 24576;
constexpr u32 OFF_FKV = OFF_COB + 32768;
constexpr u32 SYNC_BYTES = OFF_FKV + 32768;
constexpr u32 MB2 = 2u << 20;
constexpr u32 OFF_XA = 0;
constexpr u32 OFF_XB = MB2;
constexpr u32 XB = S_ * D_ * 2;
constexpr u32 OFF_TA = 2 * MB2;
constexpr u32 OFF_TB = 6 * MB2;
constexpr u32 OFF_OA = 10 * MB2;
constexpr u32 OFF_PB = 14 * MB2;
constexpr u32 HB_ = 2 * 64 * 32 * 8192;
constexpr u32 OFF_QX = 30 * MB2;
constexpr u32 OFF_KX = OFF_QX + HB_;
constexpr u32 OFF_VX = OFF_KX + HB_;
constexpr u32 OFF_OX = OFF_VX + HB_;
constexpr u32 WS_BYTES = OFF_OX + HB_;
constexpr u32 L_TAB = 147456, L_IT = L_TAB + 11264, L_EX = 138240, L_W = 31936;

#define STAMP(i, k) do {} while (0)
#define PH(k) do {} while (0)
__device__ __forceinline__ float bf2f(u16 v) { return __uint_as_float(((u32)v) << 16); }
__device__ __forceinline__ float bflo(u32 v) { return __uint_as_float(v << 16); }
__device__ __forceinline__ float bfhi(u32 v) { return __uint_as_float(v & 0xffff0000u); }
__device__ __forceinline__ u32 pack2(float a, float b) { bfv2 v = {(__bf16)a, (__bf16)b}; return __builtin_bit_cast(u32, v); }
__device__ __forceinline__ float rbf(float f) { return (float)(__bf16)f; }
__device__ __forceinline__ v4u ld16(rsrc_t r, u32 off) { return __builtin_amdgcn_raw_buffer_load_b128(r, off, 0, 16); }
__device__ __forceinline__ v2u ld8(rsrc_t r, u32 off) { return __builtin_amdgcn_raw_buffer_load_b64(r, off, 0, 16); }
__device__ __forceinline__ void st16(rsrc_t r, u32 off, v4u v) { __builtin_amdgcn_raw_buffer_store_b128(v, r, off, 0, 16); }
__device__ __forceinline__ void st8(rsrc_t r, u32 off, v2u v) { __builtin_amdgcn_raw_buffer_store_b64(v, r, off, 0, 16); }
__device__ __forceinline__ void st4(rsrc_t r, u32 off, u32 v) { __builtin_amdgcn_raw_buffer_store_b32(v, r, off, 0, 16); }
#define DRAIN() asm volatile("s_waitcnt vmcnt(0)" ::: "memory")
#define CBAR() asm volatile("" ::: "memory")
__device__ __forceinline__ u32 ldw(char* ws, u32 off) { return __hip_atomic_load((gu32*)(ws + off), RLX_AGENT); }
__device__ __forceinline__ void setw(char* ws, u32 off) { __hip_atomic_store((gu32*)(ws + off), 1u, RLX_AGENT); }
__device__ __forceinline__ u32 fl(u32 i) { return (i >> 5) * 1024 + (i & 31) * 4; }
constexpr u32 SPIN_CAP = 1u << 20;
#define SLP_W 2
#define SLP_C 8
__device__ __forceinline__ bool spin_fail(char* ws, u32& spins) {
    ++spins;
    if ((spins & 63u) == 0u) {
        if (ldw(ws, OFF_ABORT)) return true;
        if (spins > SPIN_CAP) { __hip_atomic_store((gu32*)(ws + OFF_ABORT), 1u, RLX_AGENT); return true; }
    }
    return false;
}
__device__ __forceinline__ void wait_set(char* ws, u32 off) {
    u32 spins = 0;
    while (__builtin_amdgcn_ballot_w64(ldw(ws, off) == 0)) {
        if (spin_fail(ws, spins)) break;
        __builtin_amdgcn_s_sleep(SLP_W);
    }
    CBAR();
}
constexpr u32 OOB = 0x80000000u;
__device__ __forceinline__ u32 cgo(u32 cg) { return (cg >> 5) * 16384 + (cg & 31) * 256; }

template <int N, bool FIN>
__device__ __forceinline__ void t_item(int rid, u32 i, u32 j, u32 m, u32 pol, u32 w, rsrc_t ro, char* ws, rsrc_t rs) {
    const u32 lane = threadIdx.x & 63, s = i & 63, side = (((s >> 5) ^ pol ^ m) & 1) << 13;
    v4u x[N], p[N][4];
    u32 v[N];
    wait_set(ws, OFF_FB + m * 4096 + j * 128 + fl(i));
#pragma unroll
    for (int u = 0; u < N; ++u) { v[u] = cgo(12 * j + 4 * (w + u) + (lane >> 4)) + (lane & 15) * 16; x[u] = ld16(rs, OFF_TB + (i << 16) + (FIN ? side : m << 13) + v[u]); }
    wait_set(ws, OFF_FP + (m * 3 + (j >> 1)) * 4096 + (lane & 3) * 128 + fl(i));
    STAMP(i, 0);
#pragma unroll
    for (int u = 0; u < N; ++u)
#pragma unroll
        for (int k = 0; k < 4; ++k) p[u][k] = __builtin_amdgcn_raw_buffer_load_b128(rs, v[u], OFF_PB + (i << 18) + (k << 16) + ((((i >> 3) ^ pol ^ m) & 1) << 13), 16);
#pragma unroll
    for (int u = 0; u < N; ++u) {
#pragma unroll
        for (int e = 0; e < 4; ++e) {
            f32x2 a = {bflo(x[u][e]), bfhi(x[u][e])};
#pragma unroll
            for (int k = 0; k < 4; ++k) a += f32x2{bflo(p[u][k][e]), bfhi(p[u][k][e])};
            x[u][e] = pack2(a[0], a[1]);
        }
        if (FIN) __builtin_amdgcn_raw_buffer_store_b128(x[u], ro, (s * 32 + 2 * (lane & 15) + m) * 1152 + (12 * j + 4 * (w + u) + (lane >> 4)) * 16, 0, 0);
        else st16(rs, OFF_TA + ((64 + s) << 16) + side + v[u], x[u]);
    }
    STAMP(i, 1);
}
__device__ __forceinline__ bool t_last(u32* cnt) {
    DRAIN();
    u32 o = 0;
    if ((threadIdx.x & 63) == 0) o = __hip_atomic_fetch_add(cnt, 1u, __ATOMIC_RELAXED, __HIP_MEMORY_SCOPE_WORKGROUP);
    return __builtin_amdgcn_readfirstlane(o) == 2;
}
__device__ __forceinline__ void role_t(int rid, u32 j, u32 m, const u16* x0, u16* out, char* ws, rsrc_t rs, char* smem) {
    const u32 lane = threadIdx.x & 63, w = __builtin_amdgcn_readfirstlane(threadIdx.x >> 6);
    u32* cnt = (u32*)smem + 64;
    cnt[threadIdx.x] = 0;
    __syncthreads();
    const rsrc_t ro = __builtin_amdgcn_make_buffer_rsrc(out, 0, XB, 0x00020000);
    if (w < 3) {
        const rsrc_t rx = __builtin_amdgcn_make_buffer_rsrc((void*)x0, 0, XB, 0x00020000);
        const u32 cg = 12 * j + 4 * w + (lane >> 4), vx = (2 * (lane & 15) + m) * 1152 + cg * 16, v = OFF_TA + (m << 13) + cgo(cg) + (lane & 15) * 16;
        for (u32 s0 = 0; s0 < 64; s0 += s0 < 4 ? 2 : 10) {
            const u32 n = s0 < 4 ? 2 : 10;
            v4u x[10];
#pragma unroll
            for (u32 u = 0; u < 10; ++u) x[u] = __builtin_amdgcn_raw_buffer_load_b128(rx, u < n ? vx + (s0 + u) * 36864 : OOB, 0, 0);
#pragma unroll
            for (u32 u = 0; u < 10; ++u) st16(rs, u < n ? v + ((s0 + u) << 16) : OOB, x[u]);
            if (t_last(cnt + s0) && lane < n) setw(ws, OFF_FA + m * 4096 + j * 128 + fl(s0 + lane));
        }
    }
    wait_set(ws, OFF_POL);
    const u32 pol = ldw(ws, OFF_POL);
    if (w == 3) {
        for (u32 i = 64; i < 127; ++i) t_item<3, true>(rid, i, j, m, pol, 0, ro, ws, rs);
        return;
    }
    for (u32 i = 0; i < 64; ++i) {
        t_item<1, false>(rid, i, j, m, pol, w, ro, ws, rs);
        if (t_last(cnt + 64 + i) && lane == 0) setw(ws, OFF_FA + m * 4096 + j * 128 + fl(64 + i));
    }
    t_item<1, true>(rid, 127, j, m, pol, w, ro, ws, rs);
}

#define CLOBBERS "memory", "vcc", "scc", \
    "v0", "v1", "v2", "v3", "v4", "v5", "v6", "v7", "v8", "v9", "v10", "v11", "v12", "v13", "v14", "v15", "v16", "v17", "v18", "v19", "v20", "v21", "v22", "v23", \
    "v24", "v25", "v26", "v27", "v28", "v29", "v30", "v31", "v32", "v33", "v34", "v35", "v36", "v37", "v38", "v39", "v40", "v41", "v42", "v43", "v44", "v45", "v46", "v47", \
    "v48", "v49", "v50", "v51", "v52", "v53", "v54", "v55", "v56", "v57", "v58", "v59", "v60", "v61", "v62", "v63", "v64", "v65", "v66", "v67", "v68", "v69", "v70", "v71", \
    "v72", "v73", "v74", "v75", "v76", "v77", "v78", "v79", "v80", "v81", "v82", "v83", "v84", "v85", "v86", "v87", "v88", "v89", "v90", "v91", "v92", "v93", "v94", "v95", \
    "v96", "v97", "v98", "v99", "v100", "v101", "v102", "v103", "v104", "v105", "v106", "v107", "v108", "v109", "v110", "v111", "v112", "v113", "v114", "v115", "v116", "v117", "v118", "v119", \
    "v120", "v121", "v122", "v123", "v124", "v125", "v126", "v127", "v128", "v129", "v130", "v131", "v132", "v133", "v134", "v135", "v136", "v137", "v138", "v139", "v140", "v141", "v142", "v143", \
    "v144", "v145", "v146", "v147", "v148", "v149", "v150", "v151", "v152", "v153", "v154", "v155", "v156", "v157", "v158", "v159", "v160", "v161", "v162", "v163", "v164", "v165", "v166", "v167", \
    "v168", "v169", "v170", "v171", "v172", "v173", "v174", "v175", "v176", "v177", "v178", "v179", "v180", "v181", "v182", "v183", "v184", "v185", "v186", "v187", "v188", "v189", "v190", "v191", \
    "v192", "v193", "v194", "v195", "v196", "v197", "v198", "v199", "v200", "v201", "v202", "v203", "v204", "v205", "v206", "v207", "v208", "v209", "v210", "v211", "v212", "v213", "v214", "v215", \
    "v216", "v217", "v218", "v219", "v220", "v221", "v222", "v223", "v224", "v225", "v226", "v227", "v228", "v229", "v230", "v231", "v232", "v233", "v234", "v235", "v236", "v237", "v238", "v239", \
    "v240", "v241", "v242", "v243", "v244", "v245", "v246", "v247", "v248", "v249", "v250", "v251", "v252", "v253", "v254", "v255", "a0", "a1", "a2", "a3", "a4", "a5", "a6", "a7", \
    "a8", "a9", "a10", "a11", "a12", "a13", "a14", "a15", "a16", "a17", "a18", "a19", "a20", "a21", "a22", "a23", "a24", "a25", "a26", "a27", "a28", "a29", "a30", "a31", \
    "a32", "a33", "a34", "a35", "a36", "a37", "a38", "a39", "a40", "a41", "a42", "a43", "a44", "a45", "a46", "a47", "a48", "a49", "a50", "a51", "a52", "a53", "a54", "a55", \
    "a56", "a57", "a58", "a59", "a60", "a61", "a62", "a63", "a64", "a65", "a66", "a67", "a68", "a69", "a70", "a71", "a72", "a73", "a74", "a75", "a76", "a77", "a78", "a79", \
    "a80", "a81", "a82", "a83", "a84", "a85", "a86", "a87", "a88", "a89", "a90", "a91", "a92", "a93", "a94", "a95", "a96", "a97", "a98", "a99", "a100", "a101", "a102", "a103", \
    "a104", "a105", "a106", "a107", "a108", "a109", "a110", "a111", "a112", "a113", "a114", "a115", "a116", "a117", "a118", "a119", "a120", "a121", "a122", "a123", "a124", "a125", "a126", "a127", \
    "a128", "a129", "a130", "a131", "a132", "a133", "a134", "a135", "a136", "a137", "a138", "a139", "a140", "a141", "a142", "a143", "a144", "a145", "a146", "a147", "a148", "a149", "a150", "a151", \
    "a152", "a153", "a154", "a155", "a156", "a157", "a158", "a159", "a160", "a161", "a162", "a163", "a164", "a165", "a166", "a167", "a168", "a169", "a170", "a171", "a172", "a173", "a174", "a175", \
    "a176", "a177", "a178", "a179", "a180", "a181", "a182", "a183", "a184", "a185", "a186", "a187", "a188", "a189", "a190", "a191", "a192", "a193", "a194", "a195", "a196", "a197", "a198", "a199", \
    "a200", "a201", "a202", "a203", "a204", "a205", "a206", "a207", "a208", "a209", "a210", "a211", "a212", "a213", "a214", "a215", "a216", "a217", "a218", "a219", "a220", "a221", "a222", "a223", \
    "a224", "a225", "a226", "a227", "a228", "a229", "a230", "a231", "a232", "a233", "a234", "a235", "a236", "a237", "a238", "a239", "a240", "a241", "a242", "a243", "a244", "a245", "a246", "a247", \
    "a248", "a249", "a250", "a251", "a252", "a253", "a254", "a255", "s38", "s39", "s40", "s41", "s42", "s43", "s44", "s45", "s46", "s47", "s48", "s49", "s50", "s51", "s52", "s53", \
    "s54", "s55", "s56", "s57", "s58", "s59", "s60", "s61", "s62", "s63", "s64", "s65", "s66", "s67", "s68", "s69", "s70", "s71", "s72", "s73", "s74", "s75", "s76", "s77", \
    "s78", "s79", "s80", "s81", "s82", "s83", "s84", "s85", "s86", "s87", "s88", "s89", "s90", "s91", "s92", "s93", "s94", "s95", "s96", "s97", "s98", "s99", "s100", "s101"
#define LANES() const int tid = threadIdx.x, lane = tid & 63, c = lane & 15, g = lane >> 4, q = c >> 2, p4 = c & 3; \
    const int wave = __builtin_amdgcn_readfirstlane(tid >> 6); u32* tab = (u32*)(smem + L_TAB) + tid; (void)q; (void)p4; (void)g
struct Par { u32 role, h, p, woff; };
__device__ __forceinline__ u32 rows(u32 i, u32 m) { return (i << 16) + (i < 64 ? m << 13 : (((i >> 5 ^ m) & 1) << 13) + 1); }
__device__ __forceinline__ u32 slot(u32 i) { return ((i & 63) << 19) + (((i >> 2 ^ i >> 6) & 1) << 13) + 1; }
__device__ __forceinline__ u32 part(u32 i, u32 m) { return (i << 18) + (((i >> 3 ^ m) & 1) << 13) + 1; }
template <class FS, class FD>
__device__ __forceinline__ void items(u32 K, u32 fin, u32 fout, FS src, FD dst, char* smem) {
    const u32 n = threadIdx.x - 1;
    auto idx = [&](u32 k) { const u32 t = k - K; return t < 2 * (64 - K) ? ((t & 1) ? K : 64) + (t >> 1) : k & 127; };
    if (threadIdx.x < 130) {
        v4u* row = (v4u*)(smem + L_IT) + 2 * threadIdx.x;
        row[0] = v4u{n + 1 < 128 ? fin + fl(idx(n + 1)) : OFF_NEVER, src(idx(n + 1)), dst(idx(n)), fout + fl(idx(n - 1))};
        row[1] = v4u{0u, idx(n) & 63 ? 0u : 0xf149f2cau, (idx(n) ^ idx(n - 1)) & 64, idx(n)};
    }
}
#define KA 11
#define KB 10
#define KH 9
#define KW 9

__device__ __forceinline__ Par role_h(u32 m, u32 h, char* smem) {
    LANES();
    tab[0] = 0x10000 + wave * 8192 + lane * 16;
    tab[256] = tid * 16;
    tab[2 * 256] = cgo(16 * wave) + lane * 16;
    tab[3 * 256] = 0x6000 + c * 4;
    tab[4 * 256] = 0x10000 + (q * 16 + c) * 16 + p4 * 4;
    tab[5 * 256] = (8 * wave + g) * 8192 + (c ^ ((g | (wave & 1) << 2) << 1)) * 16;
    tab[6 * 256] = (4 * g + q) * 256 + ((2 * wave) ^ (q | (g & 1) << 2)) * 32 + p4 * 8;
    tab[7 * 256] = tab[6 * 256] ^ 32;
    items(KH, OFF_FB + m * 4096, OFF_FKV + (h >> 2) * 4096 + (h & 3) * 256 + m * 128, [&](u32 i) { return OFF_TB + rows(i, m); }, [&](u32 i) { return OFF_QX + h * 16384 + m * 4096 + slot(i); }, smem);
    return Par{1, 0, 0, h * 256};
}

__device__ __forceinline__ Par role_w(u32 m, u32 kb, u32 nb, char* smem) {
    LANES();
    tab[0] = wave * 8192 + lane * 16;
    tab[256] = wave * 32768 + lane * 16;
    tab[512] = cgo(24 * nb + 6 * wave + g) + c * 16;
    tab[768] = (256 * wave + 4 * g + (c >> 3)) * 1152 + ((lane & 7) ^ ((g & 1) << 2)) * 16;
#pragma unroll
    for (int t = 0; t < 3; ++t) {
        const int col = wave * 96 + (t < 2 ? p4 * 16 + t * 8 : 64 + p4 * 8);
        tab[(4 + t) * 256] = (col >> 7) * 2048 + (q & 1) * 1024 + (2 * g + (q >> 1)) * 128 + ((col & 127) ^ ((g & 1) << 6));
    }
    tab[7 * 256] = (g & 1) ? OOB : cgo(24 * nb + 6 * wave + 4 + (g >> 1)) + c * 16;
    items(KW, OFF_COB + (m * 4 + kb) * 4096, OFF_FP + (m * 3 + nb) * 4096 + kb * 128, [&](u32 i) { return OFF_OX + kb * 8 * 16384 + m * 4096 + slot(i); },
          [&](u32 i) { return OFF_PB + kb * 65536 + part(i, m); }, smem);
    return Par{2, 0, 0, kb * 1024 * 1152 + nb * 384};
}

__device__ __forceinline__ Par role_a2(u32 m, u32 j, char* smem) {
    LANES();
    const int hd = tid >> 2, w = wave < 3 ? wave : 2;
    tab[0] = 0x8000 + lane * 16;
    tab[256] = wave * 4096 + lane * 16;
    tab[512] = cgo(16 * wave) + lane * 16;
    tab[768] = wave < 3 ? cgo(12 * j + 4 * wave + g) + c * 16 : OOB;
    tab[4 * 256] = (16 + (hd >> 4)) * 1024 + (((hd >> 2) & 3) * 16 + 4 * (tid & 3)) * 16 + (hd & 3) * 4;
    tab[5 * 256] = wave < 2 ? 32768 + tid * 16 : OOB;
    tab[6 * 256] = (16 * wave + (lane >> 2)) * 1152 + (lane & 3) * 16;
    tab[7 * 256] = w * 36864 + p4 * 16 + (20 * (g >> 1) + 8 * (g & 1) + q) * 64;
    tab[8 * 256] = w * 36864 + p4 * 16 + (72 * g + 16 + q) * 64;
    items(KB, OFF_FO + m * 4096, OFF_FB + m * 4096 + j * 128, [&](u32 i) { return OFF_OA + rows(i, m); }, [&](u32 i) { return OFF_TB + rows(i, m); }, smem);
    return Par{4, 0, 0, j * 192};
}

__device__ __forceinline__ Par role_a1(u32 m, u32 hg, char* smem) {
    LANES();
    const int hd = 4 * hg + wave;
    tab -= 768;
    tab[0] = lane * 16;
    tab[256] = wave * 5120 + lane * 16;
    tab[2 * 256] = !(g & 1) ? cgo(2 * hd + (g >> 1)) + c * 16 : g == 1 && !p4 ? 32768 + hd * 64 + c * 4 : OOB;
    tab[3 * 256] = cgo(16 * wave) + lane * 16;
    tab[4 * 256] = wave < 2 ? 32768 + tid * 16 : OOB;
    tab[5 * 256] = tid * 16;
    tab[6 * 256] = L_EX + ((wave >> 1) * 16 + c) * 16 + (wave & 1) * 8;
    tab[7 * 256] = g ? L_EX + 4096 : tab[6 * 256] + 512;
    tab[8 * 256] = L_EX + 2048 + (g * 16 + 2 * wave + (c & 1)) * 16;
    tab[9 * 256] = 0x7000 + c * 4;
    tab[10 * 256] = (q * 16 + c) * 16 + p4 * 4;
    tab[11 * 256] = 4 * g - c;
    tab[12 * 256] = (8 * g + q) * 144 + 36 * wave + 4 * (wave & 1) + p4 * 8;
    tab[13 * 256] = (wave < 2 ? p4 >> 1 : 2) * 9216 + (8 * g + q) * 144 + 72 * (p4 & 1) + 32;
    if (tid < 2) *(v4u*)(smem + L_EX + 4096 + tid * 1024) = v4u{0u, 0u, 0u, 0u};
    items(KA, OFF_FA + m * 4096, OFF_FO + m * 4096 + hg * 128, [&](u32 i) { return OFF_TA + rows(i, m); }, [&](u32 i) { return OFF_OA + rows(i, m); }, smem);
    return Par{3, 0, 0, hg * 144};
}

#define SB() __builtin_amdgcn_sched_barrier(0)
__device__ __forceinline__ u32 now32() { return (u32)__builtin_amdgcn_s_memtime(); }
__device__ __forceinline__ Par role_chain(u32 layer, u32 h, u32 p, rsrc_t rs, char* smem) {
    LANES();
    {
        const v4u ff = {~0u, ~0u, ~0u, ~0u};
#pragma unroll
        for (int i = 0; i < 6; ++i) {
            const int pg = (layer * 64 + 2 * h + p) * 6 + i;
            st16(rs, (pg < 384 ? OFF_XA + pg * 4096 : OFF_XB + (pg - 384) * 4096) + tid * 16, ff);
        }
    }
    const int ro = 16 * (q & 1) + 4 * (q >> 1) + 8 * (g >> 1) + 2 * (g & 1);
    tab[0] = lane * 16;
    tab[256] = L_W + (wave * 15 + (wave == 3 ? 2 : 0)) * 2048 + lane * 16;
    tab[2 * 256] = (4 * wave + g) * 8192 + (c ^ (((g & 1) | wave << 1) << 1)) * 16;
    tab[3 * 256] = (16 * (g >> 1) + 4 * (g & 1) + 8 * (q >> 1) + (q & 1)) * 256 + ((2 * wave) ^ ((q & 1) | (g & 1) << 1 | (q >> 1) << 2)) * 32 + p4 * 8;
    tab[4 * 256] = tab[3 * 256] ^ 32;
    tab[5 * 256] = (2 * wave + (lane >> 5)) * 1152 + ((lane & 31) ^ (wave << 1)) * 16;
    tab[6 * 256] = ro * 512 + ((2 * wave) ^ ((g & 1) | (q >> 1) << 1)) * 32 + p4 * 8;
    tab[7 * 256] = tab[6 * 256] ^ 32;
    tab[8 * 256] = (32 * wave + (lane >> 2)) * 1152 + (lane & 3) * 16;
    tab[9 * 256] = ro * 64 + (wave & 1) * 32 + p4 * 8;
    return Par{0, h, p, 0};
}

#define MAP 2
constexpr int RW = 90, RA1 = 24, RA2 = MAP == 2 ? 184 : 56, RTT = 58;
#define NPOL 6
static_assert(OFF_POL == 64u);
static_assert(OFF_RDY == 128u);
static_assert(OFF_COB == 53248u);
static_assert(OFF_FKV == 86016u);
static_assert(OFF_XA == 0u);
static_assert(OFF_XB == 2097152u);
static_assert(OFF_TA == 4194304u);
static_assert(OFF_OA == 20971520u);
static_assert(OFF_NEVER == 192u);
static_assert(L_IT == 158720u);
static_assert(OFF_QX == 62914560u);
static_assert(OFF_KX == 96468992u);
static_assert(OFF_VX == 130023424u);
static_assert(OFF_OX == 163577856u);
static_assert(WS_BYTES == 197132288u);
static_assert(L_TAB == 147456u);
static_assert(L_EX == 138240u);
static_assert(L_W == 31936);
__device__ __forceinline__ void measure_pol(int side, char* ws, rsrc_t rs, char* smem) {
    const int wave = __builtin_amdgcn_readfirstlane(threadIdx.x >> 6);
    u32 best[2] = {~0u, ~0u};
    for (int n = 0; n < NPOL; ++n)
#pragma unroll
        for (int i = 0; i < 2; ++i) {
            SB();
            const u32 t0 = now32();
            SB();
            st4(rs, (wave & 1) * MB2 + (508 + 2 * i + (wave >> 1)) * 4096 + n * 128, t0);
            DRAIN();
            const u32 t = now32() - t0;
            best[i] = t < best[i] ? t : best[i];
        }
    int* d = (int*)smem;
    if ((threadIdx.x & 63) == 0) d[wave] = (int)(best[0] - best[1]);
    __syncthreads();
    if (threadIdx.x == 0) __hip_atomic_store((gu32*)(ws + OFF_POL), 4u | ((d[0] + d[2] >= 0) ^ side) | ((d[1] + d[3] >= 0) ^ side) << 1, RLX_AGENT);
}

__global__ __launch_bounds__(256, 1) void mega_kernel(const u16* __restrict__ x0, const u16* __restrict__ wqkv, const u16* __restrict__ woA,
                                                     const u16* __restrict__ mem0, const u16* __restrict__ Wq, const u16* __restrict__ Wk,
                                                     const u16* __restrict__ Wv, const u16* __restrict__ Wo, u16* out, char* ws) {
    __shared__ __attribute__((aligned(16))) char smem[163840];
    char* wd = (char*)(((uintptr_t)ws + SYNC_BYTES + MB2 - 1) & ~(uintptr_t)(MB2 - 1));
    const rsrc_t rs = __builtin_amdgcn_make_buffer_rsrc(wd, 0, WS_BYTES, 0x00020000);
    u32 xcc;
    asm volatile("s_getreg_b32 %0, hwreg(HW_REG_XCC_ID)" : "=s"(xcc));
    xcc &= 7;
    if (threadIdx.x == 0) {
        u32 x = xcc, t;
        for (;;) {
            t = __hip_atomic_fetch_add((gu32*)(ws + OFF_ROLE + x * 64), 1u, RLX_AGENT);
            if (t < 32) break;
            x = (x + 1) & 7;
        }
        *(u32*)smem = x * 32 + t;
    }
    {
        const int sh = blockIdx.x;
        v4u a[23];
        const int t = threadIdx.x;
        a[20] = ((const v4u*)wqkv)[sh * 486 + t]; a[21] = ((const v4u*)wqkv)[sh * 486 + (t + 256 < 486 ? t + 256 : t)];
        a[22] = ((const v4u*)woA)[sh * 162 + (t < 162 ? t : 0)];
#pragma unroll
        for (int i = 0; i < 5; ++i) {
            const int c = sh * 1152 + (t + 256 * i < 1152 ? t + 256 * i : t);
            a[i] = ((const v4u*)Wq)[c]; a[5 + i] = ((const v4u*)Wk)[c]; a[10 + i] = ((const v4u*)Wv)[c]; a[15 + i] = ((const v4u*)Wo)[c];
        }
#pragma unroll
        for (int i = 0; i < 23; ++i) asm volatile("" :: "v"(a[i]));
    }
    __syncthreads();
    const int rid = __builtin_amdgcn_readfirstlane(*(const u32*)smem);
    __syncthreads();
    STAMP(159, 0);
    const int xq = (rid >> 5) & 3, t = rid & 31, layer = rid >> 7;
    const int u = t - 24;
    const int role = t < 16 ? 0 : t < 24 ? 1 : xq == 0 ? 3 : u < 2 ? 4 : xq == 1 ? 5 : 2;
    const int ix = role == 3 ? u : role == 4 ? 2 * xq - 2 + u : role == 5 ? u - 2 : (xq * 6 + u - 5) % 12;
    if (rid == RW) measure_pol(xcc >> 2, ws, rs, smem);
    Par P{5, 0, 0, 0};
    const u16* w0 = role == 2 ? Wo : role == 3 ? wqkv : role == 4 ? woA : Wq;
    if (role == 0) P = role_chain(layer, xq * 8 + (t >> 1), t & 1, rs, smem);
    else if (role == 1) P = role_h(layer, xq * 8 + t - 16, smem);
    else if (role == 2) P = role_w(layer, ix / 3, ix % 3, smem);
    else if (role == 3) P = role_a1(layer, ix, smem);
    else if (role == 4) P = role_a2(layer ^ (MAP == 2), ix, smem);
    else { role_t(rid, ix, layer, x0, out, ws, rs, smem); return; }
    const int wave = __builtin_amdgcn_readfirstlane(threadIdx.x >> 6);
    asm volatile(
    "s_cmp_eq_u32 %13, 1\n"
    "s_cbranch_scc1 .Lrole1_%=\n"
    "s_cmp_eq_u32 %13, 2\n"
    "s_cbranch_scc1 .Lrole2_%=\n"
    "s_cmp_eq_u32 %13, 3\n"
    "s_cbranch_scc1 .Lrole3_%=\n"
    "s_cmp_eq_u32 %13, 4\n"
    "s_cbranch_scc1 .Lrole4_%=\n"
    "s_cmp_eq_u32 %13, 5\n"
    "s_cbranch_scc1 .Lrole5_%=\n"
    "s_mov_b64 s[40:41], %0\n"
    "s_and_b32 s41, s41, 0xffff\n"
    "s_mov_b32 s42, 0xbc00000\n"
    "s_mov_b32 s43, 0x20000\n"
    "s_mov_b32 s57, 131072\n"
    "v_mbcnt_lo_u32_b32 v10, -1, 0\n"
    "v_mbcnt_hi_u32_b32 v10, -1, v10\n"
    "s_lshl_b32 s73, %2, 8\n"
    "s_add_u32 s73, s73, 147456\n"
    "v_lshl_add_u32 v10, v10, 2, s73\n"
    "ds_read_b32 v0, v10\n"
    "ds_read_b32 v2, v10 offset:1024\n"
    "ds_read_b32 v1, v10 offset:2048\n"
    "ds_read_b32 v3, v10 offset:3072\n"
    "ds_read_b32 v4, v10 offset:4096\n"
    "ds_read_b32 v5, v10 offset:5120\n"
    "ds_read_b32 v6, v10 offset:6144\n"
    "ds_read_b32 v7, v10 offset:7168\n"
    "ds_read_b32 v8, v10 offset:8192\n"
    "ds_read_b32 v9, v10 offset:9216\n"
    "s_waitcnt lgkmcnt(0)\n"
    "s_barrier\n"
    "s_lshl_b32 s60, %4, 8\n"
    "s_lshl_b32 s61, %2, 10\n"
    "s_mul_i32 s63, %4, 147456\n"
    "s_mul_i32 s62, %5, 576\n"
    "s_add_u32 s63, s63, s62\n"
    "s_mov_b64 s[44:45], %11\n"
    "s_and_b32 s45, s45, 0xffff\n"
    "s_mov_b32 s46, 0x480000\n"
    "s_mov_b32 s47, 0x20000\n"
    "s_lshl_b32 s73, s61, 1\n"
    "s_add_u32 m0, s73, 0\n"
    "s_add_u32 s62, s63, 512\n"
    "buffer_load_dwordx4 v8, s[44:47], s62 offen lds\n"
    "s_lshl_b32 s73, s61, 1\n"
    "s_add_u32 m0, s73, 1024\n"
    "s_add_u32 s62, s63, 18944\n"
    "buffer_load_dwordx4 v8, s[44:47], s62 offen lds\n"
    "s_mov_b64 s[44:45], %8\n"
    "s_and_b32 s45, s45, 0xffff\n"
    "s_mov_b32 s46, 0x480000\n"
    "s_mov_b32 s47, 0x20000\n"
    "s_add_u32 m0, s61, 16384\n"
    "s_add_u32 s62, s60, 2097152\n"
    "buffer_load_dwordx4 v1, s[44:47], s62 offen lds\n"
    "s_add_u32 m0, s61, 20480\n"
    "s_add_u32 s62, s60, 2228224\n"
    "buffer_load_dwordx4 v1, s[44:47], s62 offen lds\n"
    "s_add_u32 m0, s61, 24576\n"
    "s_add_u32 s62, s60, 2359296\n"
    "buffer_load_dwordx4 v1, s[44:47], s62 offen lds\n"
    "s_add_u32 m0, s61, 28672\n"
    "s_add_u32 s62, s60, 2490368\n"
    "buffer_load_dwordx4 v1, s[44:47], s62 offen lds\n"
    "s_add_u32 m0, s61, 32768\n"
    "s_add_u32 s62, s60, 2621440\n"
    "buffer_load_dwordx4 v1, s[44:47], s62 offen lds\n"
    "s_add_u32 m0, s61, 36864\n"
    "s_add_u32 s62, s60, 2752512\n"
    "buffer_load_dwordx4 v1, s[44:47], s62 offen lds\n"
    "s_add_u32 m0, s61, 40960\n"
    "s_add_u32 s62, s60, 2883584\n"
    "buffer_load_dwordx4 v1, s[44:47], s62 offen lds\n"
    "s_add_u32 m0, s61, 45056\n"
    "s_add_u32 s62, s60, 3014656\n"
    "buffer_load_dwordx4 v1, s[44:47], s62 offen lds\n"
    "s_add_u32 m0, s61, 49152\n"
    "s_add_u32 s62, s60, 3145728\n"
    "buffer_load_dwordx4 v1, s[44:47], s62 offen lds\n"
    "s_add_u32 m0, s61, 53248\n"
    "s_add_u32 s62, s60, 3276800\n"
    "buffer_load_dwordx4 v1, s[44:47], s62 offen lds\n"
    "s_add_u32 m0, s61, 57344\n"
    "s_add_u32 s62, s60, 3407872\n"
    "buffer_load_dwordx4 v1, s[44:47], s62 offen lds\n"
    "s_add_u32 m0, s61, 61440\n"
    "s_add_u32 s62, s60, 3538944\n"
    "buffer_load_dwordx4 v1, s[44:47], s62 offen lds\n"
    "s_add_u32 m0, s61, 65536\n"
    "s_add_u32 s62, s60, 3670016\n"
    "buffer_load_dwordx4 v1, s[44:47], s62 offen lds\n"
    "s_add_u32 m0, s61, 69632\n"
    "s_add_u32 s62, s60, 3801088\n"
    "buffer_load_dwordx4 v1, s[44:47], s62 offen lds\n"
    "s_add_u32 m0, s61, 73728\n"
    "s_add_u32 s62, s60, 3932160\n"
    "buffer_load_dwordx4 v1, s[44:47], s62 offen lds\n"
    "s_add_u32 m0, s61, 77824\n"
    "s_add_u32 s62, s60, 4063232\n"
    "buffer_load_dwordx4 v1, s[44:47], s62 offen lds\n"
    "s_add_u32 m0, s61, 81920\n"
    "s_add_u32 s62, s60, 4194304\n"
    "buffer_load_dwordx4 v1, s[44:47], s62 offen lds\n"
    "s_add_u32 m0, s61, 86016\n"
    "s_add_u32 s62, s60, 4325376\n"
    "buffer_load_dwordx4 v1, s[44:47], s62 offen lds\n"
    "s_add_u32 m0, s61, 90112\n"
    "s_add_u32 s62, s60, 4456448\n"
    "buffer_load_dwordx4 v1, s[44:47], s62 offen lds\n"
    "s_add_u32 m0, s61, 94208\n"
    "s_add_u32 s62, s60, 4587520\n"
    "buffer_load_dwordx4 v1, s[44:47], s62 offen lds\n"
    "s_mov_b64 s[44:45], %9\n"
    "s_and_b32 s45, s45, 0xffff\n"
    "s_mov_b32 s46, 0x480000\n"
    "s_mov_b32 s47, 0x20000\n"
    "s_add_u32 m0, s61, 98304\n"
    "s_add_u32 s62, s60, 0\n"
    "buffer_load_dwordx4 v1, s[44:47], s62 offen lds\n"
    "s_add_u32 m0, s61, 102400\n"
    "s_add_u32 s62, s60, 131072\n"
    "buffer_load_dwordx4 v1, s[44:47], s62 offen lds\n"
    "s_add_u32 m0, s61, 106496\n"
    "s_add_u32 s62, s60, 262144\n"
    "buffer_load_dwordx4 v1, s[44:47], s62 offen lds\n"
    "s_add_u32 m0, s61, 110592\n"
    "s_add_u32 s62, s60, 393216\n"
    "buffer_load_dwordx4 v1, s[44:47], s62 offen lds\n"
    "s_add_u32 m0, s61, 114688\n"
    "s_add_u32 s62, s60, 524288\n"
    "buffer_load_dwordx4 v1, s[44:47], s62 offen lds\n"
    "s_add_u32 m0, s61, 118784\n"
    "s_add_u32 s62, s60, 655360\n"
    "buffer_load_dwordx4 v1, s[44:47], s62 offen lds\n"
    "s_add_u32 m0, s61, 122880\n"
    "s_add_u32 s62, s60, 786432\n"
    "buffer_load_dwordx4 v1, s[44:47], s62 offen lds\n"
    "s_add_u32 m0, s61, 126976\n"
    "s_add_u32 s62, s60, 917504\n"
    "buffer_load_dwordx4 v1, s[44:47], s62 offen lds\n"
    "s_add_u32 m0, s61, 131072\n"
    "s_add_u32 s62, s60, 1048576\n"
    "buffer_load_dwordx4 v1, s[44:47], s62 offen lds\n"
    "s_add_u32 m0, s61, 135168\n"
    "s_add_u32 s62, s60, 1179648\n"
    "buffer_load_dwordx4 v1, s[44:47], s62 offen lds\n"
    "s_add_u32 m0, s61, 139264\n"
    "s_add_u32 s62, s60, 1310720\n"
    "buffer_load_dwordx4 v1, s[44:47], s62 offen lds\n"
    "s_add_u32 m0, s61, 143360\n"
    "s_add_u32 s62, s60, 1441792\n"
    "buffer_load_dwordx4 v1, s[44:47], s62 offen lds\n"
    "s_add_u32 m0, s61, 147456\n"
    "s_add_u32 s62, s60, 1572864\n"
    "buffer_load_dwordx4 v1, s[44:47], s62 offen lds\n"
    "s_add_u32 m0, s61, 151552\n"
    "s_add_u32 s62, s60, 1703936\n"
    "buffer_load_dwordx4 v1, s[44:47], s62 offen lds\n"
    "s_add_u32 m0, s61, 155648\n"
    "s_add_u32 s62, s60, 1835008\n"
    "buffer_load_dwordx4 v1, s[44:47], s62 offen lds\n"
    "s_add_u32 m0, s61, 159744\n"
    "s_add_u32 s62, s60, 1966080\n"
    "buffer_load_dwordx4 v1, s[44:47], s62 offen lds\n"
    "s_waitcnt vmcnt(32) lgkmcnt(0)\n"
    "s_barrier\n"
    "ds_read_b64_tr_b16 v[144:145], v3 offset:16384\n"
    "ds_read_b64_tr_b16 v[146:147], v3 offset:16896\n"
    "ds_read_b64_tr_b16 v[148:149], v3 offset:24576\n"
    "ds_read_b64_tr_b16 v[150:151], v3 offset:25088\n"
    "ds_read_b64_tr_b16 v[152:153], v4 offset:16384\n"
    "ds_read_b64_tr_b16 v[154:155], v4 offset:16896\n"
    "ds_read_b64_tr_b16 v[156:157], v4 offset:24576\n"
    "ds_read_b64_tr_b16 v[158:159], v4 offset:25088\n"
    "s_waitcnt vmcnt(28) lgkmcnt(0)\n"
    "s_barrier\n"
    "s_add_u32 m0, s61, 16384\n"
    "s_add_u32 s62, s60, 2097152\n"
    "buffer_load_dwordx4 v1, s[44:47], s62 offen lds\n"
    "s_add_u32 m0, s61, 20480\n"
    "s_add_u32 s62, s60, 2228224\n"
    "buffer_load_dwordx4 v1, s[44:47], s62 offen lds\n"
    "s_add_u32 m0, s61, 24576\n"
    "s_add_u32 s62, s60, 2359296\n"
    "buffer_load_dwordx4 v1, s[44:47], s62 offen lds\n"
    "s_add_u32 m0, s61, 28672\n"
    "s_add_u32 s62, s60, 2490368\n"
    "buffer_load_dwordx4 v1, s[44:47], s62 offen lds\n"
    "ds_read_b64_tr_b16 v[160:161], v3 offset:32768\n"
    "ds_read_b64_tr_b16 v[162:163], v3 offset:33280\n"
    "ds_read_b64_tr_b16 v[164:165], v3 offset:40960\n"
    "ds_read_b64_tr_b16 v[166:167], v3 offset:41472\n"
    "ds_read_b64_tr_b16 v[168:169], v4 offset:32768\n"
    "ds_read_b64_tr_b16 v[170:171], v4 offset:33280\n"
    "ds_read_b64_tr_b16 v[172:173], v4 offset:40960\n"
    "ds_read_b64_tr_b16 v[174:175], v4 offset:41472\n"
    "s_waitcnt vmcnt(28) lgkmcnt(0)\n"
    "s_barrier\n"
    "s_add_u32 m0, s61, 32768\n"
    "s_add_u32 s62, s60, 2621440\n"
    "buffer_load_dwordx4 v1, s[44:47], s62 offen lds\n"
    "s_add_u32 m0, s61, 36864\n"
    "s_add_u32 s62, s60, 2752512\n"
    "buffer_load_dwordx4 v1, s[44:47], s62 offen lds\n"
    "s_add_u32 m0, s61, 40960\n"
    "s_add_u32 s62, s60, 2883584\n"
    "buffer_load_dwordx4 v1, s[44:47], s62 offen lds\n"
    "s_add_u32 m0, s61, 45056\n"
    "s_add_u32 s62, s60, 3014656\n"
    "buffer_load_dwordx4 v1, s[44:47], s62 offen lds\n"
    "ds_read_b64_tr_b16 v[176:177], v3 offset:49152\n"
    "ds_read_b64_tr_b16 v[178:179], v3 offset:49664\n"
    "ds_read_b64_tr_b16 v[180:181], v3 offset:57344\n"
    "ds_read_b64_tr_b16 v[182:183], v3 offset:57856\n"
    "ds_read_b64_tr_b16 v[184:185], v4 offset:49152\n"
    "ds_read_b64_tr_b16 v[186:187], v4 offset:49664\n"
    "ds_read_b64_tr_b16 v[188:189], v4 offset:57344\n"
    "ds_read_b64_tr_b16 v[190:191], v4 offset:57856\n"
    "s_waitcnt vmcnt(28) lgkmcnt(0)\n"
    "s_barrier\n"
    "s_add_u32 m0, s61, 49152\n"
    "s_add_u32 s62, s60, 3145728\n"
    "buffer_load_dwordx4 v1, s[44:47], s62 offen lds\n"
    "s_add_u32 m0, s61, 53248\n"
    "s_add_u32 s62, s60, 3276800\n"
    "buffer_load_dwordx4 v1, s[44:47], s62 offen lds\n"
    "s_add_u32 m0, s61, 57344\n"
    "s_add_u32 s62, s60, 3407872\n"
    "buffer_load_dwordx4 v1, s[44:47], s62 offen lds\n"
    "s_add_u32 m0, s61, 61440\n"
    "s_add_u32 s62, s60, 3538944\n"
    "buffer_load_dwordx4 v1, s[44:47], s62 offen lds\n"
    "v_add_u32 v31, 0x10000, v3\n"
    "ds_read_b64_tr_b16 v[192:193], v31\n"
    "ds_read_b64_tr_b16 v[194:195], v31 offset:512\n"
    "ds_read_b64_tr_b16 v[196:197], v31 offset:8192\n"
    "ds_read_b64_tr_b16 v[198:199], v31 offset:8704\n"
    "v_add_u32 v30, 0x10000, v4\n"
    "ds_read_b64_tr_b16 v[200:201], v30\n"
    "ds_read_b64_tr_b16 v[202:203], v30 offset:512\n"
    "ds_read_b64_tr_b16 v[204:205], v30 offset:8192\n"
    "ds_read_b64_tr_b16 v[206:207], v30 offset:8704\n"
    "s_waitcnt vmcnt(28) lgkmcnt(0)\n"
    "s_barrier\n"
    "s_add_u32 m0, s61, 65536\n"
    "s_add_u32 s62, s60, 3670016\n"
    "buffer_load_dwordx4 v1, s[44:47], s62 offen lds\n"
    "s_add_u32 m0, s61, 69632\n"
    "s_add_u32 s62, s60, 3801088\n"
    "buffer_load_dwordx4 v1, s[44:47], s62 offen lds\n"
    "s_add_u32 m0, s61, 73728\n"
    "s_add_u32 s62, s60, 3932160\n"
    "buffer_load_dwordx4 v1, s[44:47], s62 offen lds\n"
    "s_add_u32 m0, s61, 77824\n"
    "s_add_u32 s62, s60, 4063232\n"
    "buffer_load_dwordx4 v1, s[44:47], s62 offen lds\n"
    "ds_read_b64_tr_b16 v[208:209], v31 offset:16384\n"
    "ds_read_b64_tr_b16 v[210:211], v31 offset:16896\n"
    "ds_read_b64_tr_b16 v[212:213], v31 offset:24576\n"
    "ds_read_b64_tr_b16 v[214:215], v31 offset:25088\n"
    "ds_read_b64_tr_b16 v[216:217], v30 offset:16384\n"
    "ds_read_b64_tr_b16 v[218:219], v30 offset:16896\n"
    "ds_read_b64_tr_b16 v[220:221], v30 offset:24576\n"
    "ds_read_b64_tr_b16 v[222:223], v30 offset:25088\n"
    "s_waitcnt vmcnt(28) lgkmcnt(0)\n"
    "s_barrier\n"
    "s_add_u32 m0, s61, 81920\n"
    "s_add_u32 s62, s60, 4194304\n"
    "buffer_load_dwordx4 v1, s[44:47], s62 offen lds\n"
    "s_add_u32 m0, s61, 86016\n"
    "s_add_u32 s62, s60, 4325376\n"
    "buffer_load_dwordx4 v1, s[44:47], s62 offen lds\n"
    "s_add_u32 m0, s61, 90112\n"
    "s_add_u32 s62, s60, 4456448\n"
    "buffer_load_dwordx4 v1, s[44:47], s62 offen lds\n"
    "s_add_u32 m0, s61, 94208\n"
    "s_add_u32 s62, s60, 4587520\n"
    "buffer_load_dwordx4 v1, s[44:47], s62 offen lds\n"
    "ds_read_b64_tr_b16 v[224:225], v31 offset:32768\n"
    "ds_read_b64_tr_b16 v[226:227], v31 offset:33280\n"
    "ds_read_b64_tr_b16 v[228:229], v31 offset:40960\n"
    "ds_read_b64_tr_b16 v[230:231], v31 offset:41472\n"
    "ds_read_b64_tr_b16 v[232:233], v30 offset:32768\n"
    "ds_read_b64_tr_b16 v[234:235], v30 offset:33280\n"
    "ds_read_b64_tr_b16 v[236:237], v30 offset:40960\n"
    "ds_read_b64_tr_b16 v[238:239], v30 offset:41472\n"
    "s_waitcnt vmcnt(28) lgkmcnt(0)\n"
    "s_barrier\n"
    "s_mov_b64 s[44:45], %10\n"
    "s_and_b32 s45, s45, 0xffff\n"
    "s_mov_b32 s46, 0x480000\n"
    "s_mov_b32 s47, 0x20000\n"
    "s_add_u32 m0, s61, 98304\n"
    "s_add_u32 s62, s60, 0\n"
    "buffer_load_dwordx4 v1, s[44:47], s62 offen lds\n"
    "s_add_u32 m0, s61, 102400\n"
    "s_add_u32 s62, s60, 131072\n"
    "buffer_load_dwordx4 v1, s[44:47], s62 offen lds\n"
    "s_add_u32 m0, s61, 106496\n"
    "s_add_u32 s62, s60, 262144\n"
    "buffer_load_dwordx4 v1, s[44:47], s62 offen lds\n"
    "s_add_u32 m0, s61, 110592\n"
    "s_add_u32 s62, s60, 393216\n"
    "buffer_load_dwordx4 v1, s[44:47], s62 offen lds\n"
    "ds_read_b64_tr_b16 v[240:241], v31 offset:49152\n"
    "ds_read_b64_tr_b16 v[242:243], v31 offset:49664\n"
    "ds_read_b64_tr_b16 v[244:245], v31 offset:57344\n"
    "ds_read_b64_tr_b16 v[246:247], v31 offset:57856\n"
    "ds_read_b64_tr_b16 v[248:249], v30 offset:49152\n"
    "ds_read_b64_tr_b16 v[250:251], v30 offset:49664\n"
    "ds_read_b64_tr_b16 v[252:253], v30 offset:57344\n"
    "ds_read_b64_tr_b16 v[254:255], v30 offset:57856\n"
    "s_waitcnt vmcnt(28) lgkmcnt(0)\n"
    "s_barrier\n"
    "s_add_u32 m0, s61, 114688\n"
    "s_add_u32 s62, s60, 524288\n"
    "buffer_load_dwordx4 v1, s[44:47], s62 offen lds\n"
    "s_add_u32 m0, s61, 118784\n"
    "s_add_u32 s62, s60, 655360\n"
    "buffer_load_dwordx4 v1, s[44:47], s62 offen lds\n"
    "s_add_u32 m0, s61, 122880\n"
    "s_add_u32 s62, s60, 786432\n"
    "buffer_load_dwordx4 v1, s[44:47], s62 offen lds\n"
    "s_add_u32 m0, s61, 126976\n"
    "s_add_u32 s62, s60, 917504\n"
    "buffer_load_dwordx4 v1, s[44:47], s62 offen lds\n"
    "v_add_u32 v29, 0x20000, v3\n"
    "ds_read_b64_tr_b16 a[0:1], v29\n"
    "ds_read_b64_tr_b16 a[2:3], v29 offset:512\n"
    "ds_read_b64_tr_b16 a[4:5], v29 offset:8192\n"
    "ds_read_b64_tr_b16 a[6:7], v29 offset:8704\n"
    "v_add_u32 v28, 0x20000, v4\n"
    "ds_read_b64_tr_b16 a[8:9], v28\n"
    "ds_read_b64_tr_b16 a[10:11], v28 offset:512\n"
    "ds_read_b64_tr_b16 a[12:13], v28 offset:8192\n"
    "ds_read_b64_tr_b16 a[14:15], v28 offset:8704\n"
    "s_waitcnt vmcnt(28) lgkmcnt(0)\n"
    "s_barrier\n"
    "s_add_u32 m0, s61, 131072\n"
    "s_add_u32 s62, s60, 1048576\n"
    "buffer_load_dwordx4 v1, s[44:47], s62 offen lds\n"
    "s_add_u32 m0, s61, 135168\n"
    "s_add_u32 s62, s60, 1179648\n"
    "buffer_load_dwordx4 v1, s[44:47], s62 offen lds\n"
    "s_add_u32 m0, s61, 139264\n"
    "s_add_u32 s62, s60, 1310720\n"
    "buffer_load_dwordx4 v1, s[44:47], s62 offen lds\n"
    "s_add_u32 m0, s61, 143360\n"
    "s_add_u32 s62, s60, 1441792\n"
    "buffer_load_dwordx4 v1, s[44:47], s62 offen lds\n"
    "ds_read_b64_tr_b16 a[16:17], v29 offset:16384\n"
    "ds_read_b64_tr_b16 a[18:19], v29 offset:16896\n"
    "ds_read_b64_tr_b16 a[20:21], v29 offset:24576\n"
    "ds_read_b64_tr_b16 a[22:23], v29 offset:25088\n"
    "ds_read_b64_tr_b16 a[24:25], v28 offset:16384\n"
    "ds_read_b64_tr_b16 a[26:27], v28 offset:16896\n"
    "ds_read_b64_tr_b16 a[28:29], v28 offset:24576\n"
    "ds_read_b64_tr_b16 a[30:31], v28 offset:25088\n"
    "s_waitcnt vmcnt(28) lgkmcnt(0)\n"
    "s_barrier\n"
    "s_add_u32 m0, s61, 147456\n"
    "s_add_u32 s62, s60, 1572864\n"
    "buffer_load_dwordx4 v1, s[44:47], s62 offen lds\n"
    "s_add_u32 m0, s61, 151552\n"
    "s_add_u32 s62, s60, 1703936\n"
    "buffer_load_dwordx4 v1, s[44:47], s62 offen lds\n"
    "s_add_u32 m0, s61, 155648\n"
    "s_add_u32 s62, s60, 1835008\n"
    "buffer_load_dwordx4 v1, s[44:47], s62 offen lds\n"
    "s_add_u32 m0, s61, 159744\n"
    "s_add_u32 s62, s60, 1966080\n"
    "buffer_load_dwordx4 v1, s[44:47], s62 offen lds\n"
    "ds_read_b64_tr_b16 a[32:33], v3 offset:16384\n"
    "ds_read_b64_tr_b16 a[34:35], v3 offset:16896\n"
    "ds_read_b64_tr_b16 a[36:37], v3 offset:24576\n"
    "ds_read_b64_tr_b16 a[38:39], v3 offset:25088\n"
    "ds_read_b64_tr_b16 a[40:41], v4 offset:16384\n"
    "ds_read_b64_tr_b16 a[42:43], v4 offset:16896\n"
    "ds_read_b64_tr_b16 a[44:45], v4 offset:24576\n"
    "ds_read_b64_tr_b16 a[46:47], v4 offset:25088\n"
    "s_waitcnt vmcnt(28) lgkmcnt(0)\n"
    "s_barrier\n"
    "s_add_u32 m0, s61, 16384\n"
    "s_add_u32 s62, s60, 2097152\n"
    "buffer_load_dwordx4 v1, s[44:47], s62 offen lds\n"
    "s_add_u32 m0, s61, 20480\n"
    "s_add_u32 s62, s60, 2228224\n"
    "buffer_load_dwordx4 v1, s[44:47], s62 offen lds\n"
    "s_add_u32 m0, s61, 24576\n"
    "s_add_u32 s62, s60, 2359296\n"
    "buffer_load_dwordx4 v1, s[44:47], s62 offen lds\n"
    "s_add_u32 m0, s61, 28672\n"
    "s_add_u32 s62, s60, 2490368\n"
    "buffer_load_dwordx4 v1, s[44:47], s62 offen lds\n"
    "ds_read_b64_tr_b16 a[48:49], v3 offset:32768\n"
    "ds_read_b64_tr_b16 a[50:51], v3 offset:33280\n"
    "ds_read_b64_tr_b16 a[52:53], v3 offset:40960\n"
    "ds_read_b64_tr_b16 a[54:55], v3 offset:41472\n"
    "ds_read_b64_tr_b16 a[56:57], v4 offset:32768\n"
    "ds_read_b64_tr_b16 a[58:59], v4 offset:33280\n"
    "ds_read_b64_tr_b16 a[60:61], v4 offset:40960\n"
    "ds_read_b64_tr_b16 a[62:63], v4 offset:41472\n"
    "s_waitcnt vmcnt(28) lgkmcnt(0)\n"
    "s_barrier\n"
    "s_add_u32 m0, s61, 32768\n"
    "s_add_u32 s62, s60, 2621440\n"
    "buffer_load_dwordx4 v1, s[44:47], s62 offen lds\n"
    "s_add_u32 m0, s61, 36864\n"
    "s_add_u32 s62, s60, 2752512\n"
    "buffer_load_dwordx4 v1, s[44:47], s62 offen lds\n"
    "s_add_u32 m0, s61, 40960\n"
    "s_add_u32 s62, s60, 2883584\n"
    "buffer_load_dwordx4 v1, s[44:47], s62 offen lds\n"
    "s_add_u32 m0, s61, 45056\n"
    "s_add_u32 s62, s60, 3014656\n"
    "buffer_load_dwordx4 v1, s[44:47], s62 offen lds\n"
    "ds_read_b64_tr_b16 a[64:65], v3 offset:49152\n"
    "ds_read_b64_tr_b16 a[66:67], v3 offset:49664\n"
    "ds_read_b64_tr_b16 a[68:69], v3 offset:57344\n"
    "ds_read_b64_tr_b16 a[70:71], v3 offset:57856\n"
    "ds_read_b64_tr_b16 a[72:73], v4 offset:49152\n"
    "ds_read_b64_tr_b16 a[74:75], v4 offset:49664\n"
    "ds_read_b64_tr_b16 a[76:77], v4 offset:57344\n"
    "ds_read_b64_tr_b16 a[78:79], v4 offset:57856\n"
    "s_waitcnt vmcnt(28) lgkmcnt(0)\n"
    "s_barrier\n"
    "s_add_u32 m0, s61, 49152\n"
    "s_add_u32 s62, s60, 3145728\n"
    "buffer_load_dwordx4 v1, s[44:47], s62 offen lds\n"
    "s_add_u32 m0, s61, 53248\n"
    "s_add_u32 s62, s60, 3276800\n"
    "buffer_load_dwordx4 v1, s[44:47], s62 offen lds\n"
    "s_add_u32 m0, s61, 57344\n"
    "s_add_u32 s62, s60, 3407872\n"
    "buffer_load_dwordx4 v1, s[44:47], s62 offen lds\n"
    "s_add_u32 m0, s61, 61440\n"
    "s_add_u32 s62, s60, 3538944\n"
    "buffer_load_dwordx4 v1, s[44:47], s62 offen lds\n"
    "ds_read_b64_tr_b16 a[80:81], v31\n"
    "ds_read_b64_tr_b16 a[82:83], v31 offset:512\n"
    "ds_read_b64_tr_b16 a[84:85], v31 offset:8192\n"
    "ds_read_b64_tr_b16 a[86:87], v31 offset:8704\n"
    "ds_read_b64_tr_b16 a[88:89], v30\n"
    "ds_read_b64_tr_b16 a[90:91], v30 offset:512\n"
    "ds_read_b64_tr_b16 a[92:93], v30 offset:8192\n"
    "ds_read_b64_tr_b16 a[94:95], v30 offset:8704\n"
    "s_waitcnt vmcnt(28) lgkmcnt(0)\n"
    "s_barrier\n"
    "s_add_u32 m0, s61, 65536\n"
    "s_add_u32 s62, s60, 3670016\n"
    "buffer_load_dwordx4 v1, s[44:47], s62 offen lds\n"
    "s_add_u32 m0, s61, 69632\n"
    "s_add_u32 s62, s60, 3801088\n"
    "buffer_load_dwordx4 v1, s[44:47], s62 offen lds\n"
    "s_add_u32 m0, s61, 73728\n"
    "s_add_u32 s62, s60, 3932160\n"
    "buffer_load_dwordx4 v1, s[44:47], s62 offen lds\n"
    "s_add_u32 m0, s61, 77824\n"
    "s_add_u32 s62, s60, 4063232\n"
    "buffer_load_dwordx4 v1, s[44:47], s62 offen lds\n"
    "ds_read_b64_tr_b16 a[96:97], v31 offset:16384\n"
    "ds_read_b64_tr_b16 a[98:99], v31 offset:16896\n"
    "ds_read_b64_tr_b16 a[100:101], v31 offset:24576\n"
    "ds_read_b64_tr_b16 a[102:103], v31 offset:25088\n"
    "ds_read_b64_tr_b16 a[104:105], v30 offset:16384\n"
    "ds_read_b64_tr_b16 a[106:107], v30 offset:16896\n"
    "ds_read_b64_tr_b16 a[108:109], v30 offset:24576\n"
    "ds_read_b64_tr_b16 a[110:111], v30 offset:25088\n"
    "s_waitcnt vmcnt(28) lgkmcnt(0)\n"
    "s_barrier\n"
    "s_add_u32 m0, s61, 81920\n"
    "s_add_u32 s62, s60, 4194304\n"
    "buffer_load_dwordx4 v1, s[44:47], s62 offen lds\n"
    "s_add_u32 m0, s61, 86016\n"
    "s_add_u32 s62, s60, 4325376\n"
    "buffer_load_dwordx4 v1, s[44:47], s62 offen lds\n"
    "s_add_u32 m0, s61, 90112\n"
    "s_add_u32 s62, s60, 4456448\n"
    "buffer_load_dwordx4 v1, s[44:47], s62 offen lds\n"
    "s_add_u32 m0, s61, 94208\n"
    "s_add_u32 s62, s60, 4587520\n"
    "buffer_load_dwordx4 v1, s[44:47], s62 offen lds\n"
    "ds_read_b64_tr_b16 a[112:113], v31 offset:32768\n"
    "ds_read_b64_tr_b16 a[114:115], v31 offset:33280\n"
    "ds_read_b64_tr_b16 a[116:117], v31 offset:40960\n"
    "ds_read_b64_tr_b16 a[118:119], v31 offset:41472\n"
    "ds_read_b64_tr_b16 a[120:121], v30 offset:32768\n"
    "ds_read_b64_tr_b16 a[122:123], v30 offset:33280\n"
    "ds_read_b64_tr_b16 a[124:125], v30 offset:40960\n"
    "ds_read_b64_tr_b16 a[126:127], v30 offset:41472\n"
    "s_waitcnt vmcnt(28) lgkmcnt(0)\n"
    "s_barrier\n"
    "s_mov_b64 s[44:45], %8\n"
    "s_and_b32 s45, s45, 0xffff\n"
    "s_mov_b32 s46, 0x480000\n"
    "s_mov_b32 s47, 0x20000\n"
    "s_add_u32 m0, s61, 98304\n"
    "s_add_u32 s62, s60, 0\n"
    "buffer_load_dwordx4 v1, s[44:47], s62 offen lds\n"
    "s_add_u32 m0, s61, 102400\n"
    "s_add_u32 s62, s60, 131072\n"
    "buffer_load_dwordx4 v1, s[44:47], s62 offen lds\n"
    "s_add_u32 m0, s61, 106496\n"
    "s_add_u32 s62, s60, 262144\n"
    "buffer_load_dwordx4 v1, s[44:47], s62 offen lds\n"
    "s_add_u32 m0, s61, 110592\n"
    "s_add_u32 s62, s60, 393216\n"
    "buffer_load_dwordx4 v1, s[44:47], s62 offen lds\n"
    "ds_read_b64_tr_b16 a[128:129], v31 offset:49152\n"
    "ds_read_b64_tr_b16 a[130:131], v31 offset:49664\n"
    "ds_read_b64_tr_b16 a[132:133], v31 offset:57344\n"
    "ds_read_b64_tr_b16 a[134:135], v31 offset:57856\n"
    "ds_read_b64_tr_b16 a[136:137], v30 offset:49152\n"
    "ds_read_b64_tr_b16 a[138:139], v30 offset:49664\n"
    "ds_read_b64_tr_b16 a[140:141], v30 offset:57344\n"
    "ds_read_b64_tr_b16 a[142:143], v30 offset:57856\n"
    "s_waitcnt vmcnt(28) lgkmcnt(0)\n"
    "s_barrier\n"
    "s_add_u32 m0, s61, 114688\n"
    "s_add_u32 s62, s60, 524288\n"
    "buffer_load_dwordx4 v1, s[44:47], s62 offen lds\n"
    "s_add_u32 m0, s61, 118784\n"
    "s_add_u32 s62, s60, 655360\n"
    "buffer_load_dwordx4 v1, s[44:47], s62 offen lds\n"
    "s_add_u32 m0, s61, 122880\n"
    "s_add_u32 s62, s60, 786432\n"
    "buffer_load_dwordx4 v1, s[44:47], s62 offen lds\n"
    "s_add_u32 m0, s61, 126976\n"
    "s_add_u32 s62, s60, 917504\n"
    "buffer_load_dwordx4 v1, s[44:47], s62 offen lds\n"
    "ds_read_b64_tr_b16 a[144:145], v29\n"
    "ds_read_b64_tr_b16 a[146:147], v29 offset:512\n"
    "ds_read_b64_tr_b16 a[148:149], v29 offset:8192\n"
    "ds_read_b64_tr_b16 a[150:151], v29 offset:8704\n"
    "ds_read_b64_tr_b16 a[152:153], v28\n"
    "ds_read_b64_tr_b16 a[154:155], v28 offset:512\n"
    "ds_read_b64_tr_b16 a[156:157], v28 offset:8192\n"
    "ds_read_b64_tr_b16 a[158:159], v28 offset:8704\n"
    "s_waitcnt vmcnt(28) lgkmcnt(0)\n"
    "s_barrier\n"
    "s_add_u32 m0, s61, 131072\n"
    "s_add_u32 s62, s60, 1048576\n"
    "buffer_load_dwordx4 v1, s[44:47], s62 offen lds\n"
    "s_add_u32 m0, s61, 135168\n"
    "s_add_u32 s62, s60, 1179648\n"
    "buffer_load_dwordx4 v1, s[44:47], s62 offen lds\n"
    "s_add_u32 m0, s61, 139264\n"
    "s_add_u32 s62, s60, 1310720\n"
    "buffer_load_dwordx4 v1, s[44:47], s62 offen lds\n"
    "s_add_u32 m0, s61, 143360\n"
    "s_add_u32 s62, s60, 1441792\n"
    "buffer_load_dwordx4 v1, s[44:47], s62 offen lds\n"
    "ds_read_b64_tr_b16 a[160:161], v29 offset:16384\n"
    "ds_read_b64_tr_b16 a[162:163], v29 offset:16896\n"
    "ds_read_b64_tr_b16 a[164:165], v29 offset:24576\n"
    "ds_read_b64_tr_b16 a[166:167], v29 offset:25088\n"
    "ds_read_b64_tr_b16 a[168:169], v28 offset:16384\n"
    "ds_read_b64_tr_b16 a[170:171], v28 offset:16896\n"
    "ds_read_b64_tr_b16 a[172:173], v28 offset:24576\n"
    "ds_read_b64_tr_b16 a[174:175], v28 offset:25088\n"
    "s_waitcnt vmcnt(28) lgkmcnt(0)\n"
    "s_barrier\n"
    "s_add_u32 m0, s61, 147456\n"
    "s_add_u32 s62, s60, 1572864\n"
    "buffer_load_dwordx4 v1, s[44:47], s62 offen lds\n"
    "s_add_u32 m0, s61, 151552\n"
    "s_add_u32 s62, s60, 1703936\n"
    "buffer_load_dwordx4 v1, s[44:47], s62 offen lds\n"
    "s_add_u32 m0, s61, 155648\n"
    "s_add_u32 s62, s60, 1835008\n"
    "buffer_load_dwordx4 v1, s[44:47], s62 offen lds\n"
    "s_add_u32 m0, s61, 159744\n"
    "s_add_u32 s62, s60, 1966080\n"
    "buffer_load_dwordx4 v1, s[44:47], s62 offen lds\n"
    "ds_read_b64_tr_b16 a[176:177], v3 offset:16384\n"
    "ds_read_b64_tr_b16 a[178:179], v3 offset:16896\n"
    "ds_read_b64_tr_b16 a[180:181], v3 offset:24576\n"
    "ds_read_b64_tr_b16 a[182:183], v3 offset:25088\n"
    "ds_read_b64_tr_b16 a[184:185], v4 offset:16384\n"
    "ds_read_b64_tr_b16 a[186:187], v4 offset:16896\n"
    "ds_read_b64_tr_b16 a[188:189], v4 offset:24576\n"
    "ds_read_b64_tr_b16 a[190:191], v4 offset:25088\n"
    "s_waitcnt vmcnt(28) lgkmcnt(0)\n"
    "s_barrier\n"
    "s_mov_b64 s[44:45], %11\n"
    "s_and_b32 s45, s45, 0xffff\n"
    "s_mov_b32 s46, 0x480000\n"
    "s_mov_b32 s47, 0x20000\n"
    "s_add_u32 m0, s61, 16384\n"
    "s_add_u32 s62, s63, 0\n"
    "buffer_load_dwordx4 v5, s[44:47], s62 offen lds\n"
    "s_add_u32 m0, s61, 20480\n"
    "s_add_u32 s62, s63, 9216\n"
    "buffer_load_dwordx4 v5, s[44:47], s62 offen lds\n"
    "s_add_u32 m0, s61, 24576\n"
    "s_add_u32 s62, s63, 18432\n"
    "buffer_load_dwordx4 v5, s[44:47], s62 offen lds\n"
    "s_add_u32 m0, s61, 28672\n"
    "s_add_u32 s62, s63, 27648\n"
    "buffer_load_dwordx4 v5, s[44:47], s62 offen lds\n"
    "ds_read_b64_tr_b16 a[192:193], v3 offset:32768\n"
    "ds_read_b64_tr_b16 a[194:195], v3 offset:33280\n"
    "ds_read_b64_tr_b16 a[196:197], v3 offset:40960\n"
    "ds_read_b64_tr_b16 a[198:199], v3 offset:41472\n"
    "ds_read_b64_tr_b16 a[200:201], v4 offset:32768\n"
    "ds_read_b64_tr_b16 a[202:203], v4 offset:33280\n"
    "ds_read_b64_tr_b16 a[204:205], v4 offset:40960\n"
    "ds_read_b64_tr_b16 a[206:207], v4 offset:41472\n"
    "s_waitcnt vmcnt(28) lgkmcnt(0)\n"
    "s_barrier\n"
    "s_add_u32 m0, s61, 32768\n"
    "s_add_u32 s62, s63, 36864\n"
    "buffer_load_dwordx4 v5, s[44:47], s62 offen lds\n"
    "s_add_u32 m0, s61, 36864\n"
    "s_add_u32 s62, s63, 46080\n"
    "buffer_load_dwordx4 v5, s[44:47], s62 offen lds\n"
    "s_add_u32 m0, s61, 40960\n"
    "s_add_u32 s62, s63, 55296\n"
    "buffer_load_dwordx4 v5, s[44:47], s62 offen lds\n"
    "s_add_u32 m0, s61, 45056\n"
    "s_add_u32 s62, s63, 64512\n"
    "buffer_load_dwordx4 v5, s[44:47], s62 offen lds\n"
    "ds_read_b64_tr_b16 a[208:209], v3 offset:49152\n"
    "ds_read_b64_tr_b16 a[210:211], v3 offset:49664\n"
    "ds_read_b64_tr_b16 a[212:213], v3 offset:57344\n"
    "ds_read_b64_tr_b16 a[214:215], v3 offset:57856\n"
    "ds_read_b64_tr_b16 a[216:217], v4 offset:49152\n"
    "ds_read_b64_tr_b16 a[218:219], v4 offset:49664\n"
    "ds_read_b64_tr_b16 a[220:221], v4 offset:57344\n"
    "ds_read_b64_tr_b16 a[222:223], v4 offset:57856\n"
    "s_waitcnt vmcnt(28) lgkmcnt(0)\n"
    "s_barrier\n"
    "s_add_u32 m0, s61, 49152\n"
    "s_add_u32 s62, s63, 73728\n"
    "buffer_load_dwordx4 v5, s[44:47], s62 offen lds\n"
    "s_add_u32 m0, s61, 53248\n"
    "s_add_u32 s62, s63, 82944\n"
    "buffer_load_dwordx4 v5, s[44:47], s62 offen lds\n"
    "s_add_u32 m0, s61, 57344\n"
    "s_add_u32 s62, s63, 92160\n"
    "buffer_load_dwordx4 v5, s[44:47], s62 offen lds\n"
    "s_add_u32 m0, s61, 61440\n"
    "s_add_u32 s62, s63, 101376\n"
    "buffer_load_dwordx4 v5, s[44:47], s62 offen lds\n"
    "ds_read_b64_tr_b16 a[224:225], v31\n"
    "ds_read_b64_tr_b16 a[226:227], v31 offset:512\n"
    "ds_read_b64_tr_b16 a[228:229], v31 offset:8192\n"
    "ds_read_b64_tr_b16 a[230:231], v31 offset:8704\n"
    "ds_read_b64_tr_b16 a[232:233], v30\n"
    "ds_read_b64_tr_b16 a[234:235], v30 offset:512\n"
    "ds_read_b64_tr_b16 a[236:237], v30 offset:8192\n"
    "ds_read_b64_tr_b16 a[238:239], v30 offset:8704\n"
    "s_waitcnt vmcnt(28) lgkmcnt(0)\n"
    "s_barrier\n"
    "s_add_u32 m0, s61, 65536\n"
    "s_add_u32 s62, s63, 110592\n"
    "buffer_load_dwordx4 v5, s[44:47], s62 offen lds\n"
    "s_add_u32 m0, s61, 69632\n"
    "s_add_u32 s62, s63, 119808\n"
    "buffer_load_dwordx4 v5, s[44:47], s62 offen lds\n"
    "s_add_u32 m0, s61, 73728\n"
    "s_add_u32 s62, s63, 129024\n"
    "buffer_load_dwordx4 v5, s[44:47], s62 offen lds\n"
    "s_add_u32 m0, s61, 77824\n"
    "s_add_u32 s62, s63, 138240\n"
    "buffer_load_dwordx4 v5, s[44:47], s62 offen lds\n"
    "ds_read_b64_tr_b16 a[240:241], v31 offset:16384\n"
    "ds_read_b64_tr_b16 a[242:243], v31 offset:16896\n"
    "ds_read_b64_tr_b16 a[244:245], v31 offset:24576\n"
    "ds_read_b64_tr_b16 a[246:247], v31 offset:25088\n"
    "ds_read_b64_tr_b16 a[248:249], v30 offset:16384\n"
    "ds_read_b64_tr_b16 a[250:251], v30 offset:16896\n"
    "ds_read_b64_tr_b16 a[252:253], v30 offset:24576\n"
    "ds_read_b64_tr_b16 a[254:255], v30 offset:25088\n"
    "s_waitcnt vmcnt(28) lgkmcnt(0)\n"
    "s_barrier\n"
    "ds_read_b64_tr_b16 v[32:33], v31 offset:32768\n"
    "ds_read_b64_tr_b16 v[34:35], v31 offset:33280\n"
    "ds_read_b64_tr_b16 v[36:37], v31 offset:40960\n"
    "ds_read_b64_tr_b16 v[38:39], v31 offset:41472\n"
    "ds_read_b64_tr_b16 v[40:41], v30 offset:32768\n"
    "ds_read_b64_tr_b16 v[42:43], v30 offset:33280\n"
    "ds_read_b64_tr_b16 v[44:45], v30 offset:40960\n"
    "ds_read_b64_tr_b16 v[46:47], v30 offset:41472\n"
    "s_waitcnt vmcnt(24) lgkmcnt(0)\n"
    "s_barrier\n"
    "ds_read_b64_tr_b16 v[48:49], v31 offset:49152\n"
    "ds_read_b64_tr_b16 v[50:51], v31 offset:49664\n"
    "ds_read_b64_tr_b16 v[52:53], v31 offset:57344\n"
    "ds_read_b64_tr_b16 v[54:55], v31 offset:57856\n"
    "ds_read_b64_tr_b16 v[56:57], v30 offset:49152\n"
    "ds_read_b64_tr_b16 v[58:59], v30 offset:49664\n"
    "ds_read_b64_tr_b16 v[60:61], v30 offset:57344\n"
    "ds_read_b64_tr_b16 v[62:63], v30 offset:57856\n"
    "s_waitcnt vmcnt(20) lgkmcnt(0)\n"
    "s_barrier\n"
    "ds_read_b64_tr_b16 v[64:65], v29\n"
    "ds_read_b64_tr_b16 v[66:67], v29 offset:512\n"
    "ds_read_b64_tr_b16 v[68:69], v29 offset:8192\n"
    "ds_read_b64_tr_b16 v[70:71], v29 offset:8704\n"
    "ds_read_b64_tr_b16 v[72:73], v28\n"
    "ds_read_b64_tr_b16 v[74:75], v28 offset:512\n"
    "ds_read_b64_tr_b16 v[76:77], v28 offset:8192\n"
    "ds_read_b64_tr_b16 v[78:79], v28 offset:8704\n"
    "s_waitcnt vmcnt(16) lgkmcnt(0)\n"
    "s_barrier\n"
    "ds_read_b64_tr_b16 v[80:81], v29 offset:16384\n"
    "ds_read_b64_tr_b16 v[82:83], v29 offset:16896\n"
    "ds_read_b64_tr_b16 v[84:85], v29 offset:24576\n"
    "ds_read_b64_tr_b16 v[86:87], v29 offset:25088\n"
    "ds_read_b64_tr_b16 v[136:137], v28 offset:16384\n"
    "ds_read_b64_tr_b16 v[138:139], v28 offset:16896\n"
    "ds_read_b64_tr_b16 v[140:141], v28 offset:24576\n"
    "ds_read_b64_tr_b16 v[142:143], v28 offset:25088\n"
    "s_waitcnt vmcnt(12) lgkmcnt(0)\n"
    "s_barrier\n"
    "ds_read_b64_tr_b16 v[88:89], v6 offset:16384\n"
    "ds_read_b64_tr_b16 v[90:91], v6 offset:16896\n"
    "ds_read_b64_tr_b16 v[120:121], v6 offset:16640\n"
    "ds_read_b64_tr_b16 v[122:123], v6 offset:17152\n"
    "ds_read_b64_tr_b16 v[96:97], v7 offset:16384\n"
    "ds_read_b64_tr_b16 v[98:99], v7 offset:16896\n"
    "ds_read_b64_tr_b16 v[128:129], v7 offset:16640\n"
    "ds_read_b64_tr_b16 v[130:131], v7 offset:17152\n"
    "s_waitcnt vmcnt(8) lgkmcnt(0)\n"
    "s_barrier\n"
    "ds_read_b64_tr_b16 v[92:93], v6 offset:32768\n"
    "ds_read_b64_tr_b16 v[94:95], v6 offset:33280\n"
    "ds_read_b64_tr_b16 v[124:125], v6 offset:33024\n"
    "ds_read_b64_tr_b16 v[126:127], v6 offset:33536\n"
    "ds_read_b64_tr_b16 v[100:101], v7 offset:32768\n"
    "ds_read_b64_tr_b16 v[102:103], v7 offset:33280\n"
    "ds_read_b64_tr_b16 v[132:133], v7 offset:33024\n"
    "ds_read_b64_tr_b16 v[134:135], v7 offset:33536\n"
    "s_waitcnt vmcnt(4) lgkmcnt(0)\n"
    "s_barrier\n"
    "ds_read_b64_tr_b16 v[104:105], v6 offset:49152\n"
    "ds_read_b64_tr_b16 v[106:107], v6 offset:49664\n"
    "ds_read_b64_tr_b16 v[12:13], v6 offset:49408\n"
    "ds_read_b64_tr_b16 v[14:15], v6 offset:49920\n"
    "ds_read_b64_tr_b16 v[112:113], v7 offset:49152\n"
    "ds_read_b64_tr_b16 v[114:115], v7 offset:49664\n"
    "ds_read_b64_tr_b16 v[20:21], v7 offset:49408\n"
    "ds_read_b64_tr_b16 v[22:23], v7 offset:49920\n"
    "s_waitcnt vmcnt(0) lgkmcnt(0)\n"
    "s_barrier\n"
    "v_add_u32 v11, 0x10000, v6\n"
    "ds_read_b64_tr_b16 v[108:109], v11\n"
    "ds_read_b64_tr_b16 v[110:111], v11 offset:512\n"
    "ds_read_b64_tr_b16 v[16:17], v11 offset:256\n"
    "ds_read_b64_tr_b16 v[18:19], v11 offset:768\n"
    "v_add_u32 v10, 0x10000, v7\n"
    "ds_read_b64_tr_b16 v[116:117], v10\n"
    "ds_read_b64_tr_b16 v[118:119], v10 offset:512\n"
    "ds_read_b64_tr_b16 v[24:25], v10 offset:256\n"
    "ds_read_b64_tr_b16 v[26:27], v10 offset:768\n"
    "s_waitcnt lgkmcnt(0)\n"
    "s_barrier\n"
    "ds_write_b128 v2, v[88:91] offset:14336\n"
    "ds_write_b128 v2, v[92:95] offset:15360\n"
    "ds_write_b128 v2, v[96:99] offset:16384\n"
    "ds_write_b128 v2, v[100:103] offset:17408\n"
    "ds_write_b128 v2, v[104:107] offset:18432\n"
    "ds_write_b128 v2, v[108:111] offset:19456\n"
    "ds_write_b128 v2, v[112:115] offset:20480\n"
    "ds_write_b128 v2, v[116:119] offset:21504\n"
    "ds_write_b128 v2, v[120:123] offset:22528\n"
    "ds_write_b128 v2, v[124:127] offset:23552\n"
    "ds_write_b128 v2, v[128:131] offset:24576\n"
    "ds_write_b128 v2, v[132:135] offset:25600\n"
    "ds_write_b128 v2, v[12:15] offset:26624\n"
    "ds_write_b128 v2, v[16:19] offset:27648\n"
    "ds_write_b128 v2, v[20:23] offset:28672\n"
    "ds_write_b128 v2, v[24:27] offset:29696\n"
    "ds_read_b64_tr_b16 v[88:89], v9\n"
    "ds_read_b64_tr_b16 v[90:91], v9 offset:64\n"
    "ds_read_b64_tr_b16 v[92:93], v9 offset:2048\n"
    "ds_read_b64_tr_b16 v[94:95], v9 offset:2112\n"
    "ds_read_b64_tr_b16 v[96:97], v9 offset:4096\n"
    "ds_read_b64_tr_b16 v[98:99], v9 offset:4160\n"
    "ds_read_b64_tr_b16 v[100:101], v9 offset:6144\n"
    "ds_read_b64_tr_b16 v[102:103], v9 offset:6208\n"
    "ds_write_b128 v2, v[32:35]\n"
    "ds_write_b128 v2, v[36:39] offset:1024\n"
    "ds_write_b128 v2, v[40:43] offset:2048\n"
    "ds_write_b128 v2, v[44:47] offset:3072\n"
    "ds_write_b128 v2, v[48:51] offset:4096\n"
    "ds_write_b128 v2, v[52:55] offset:5120\n"
    "ds_write_b128 v2, v[56:59] offset:6144\n"
    "ds_write_b128 v2, v[60:63] offset:7168\n"
    "ds_write_b128 v2, v[64:67] offset:8192\n"
    "ds_write_b128 v2, v[68:71] offset:9216\n"
    "ds_write_b128 v2, v[72:75] offset:10240\n"
    "ds_write_b128 v2, v[76:79] offset:11264\n"
    "ds_write_b128 v2, v[80:83] offset:12288\n"
    "ds_write_b128 v2, v[84:87] offset:13312\n"
    "v_lshrrev_b32 v112, 4, v0\n"
    "v_and_b32 v113, 15, v112\n"
    "v_lshrrev_b32 v114, 4, v112\n"
    "v_mov_b32 v11, 0\n"
    "v_and_b32 v115, 7, v112\n"
    "v_mul_u32_u24 v115, 1152, v115\n"
    "v_lshrrev_b32 v116, 3, v112\n"
    "v_lshl_add_u32 v117, v116, 4, v115\n"
    "v_and_b32 v115, 1, v114\n"
    "v_lshrrev_b32 v116, 1, v114\n"
    "v_mov_b32 v120, 0\n"
    "v_mov_b32 v121, 0\n"
    "v_mov_b32 v122, 0\n"
    "v_mov_b32 v123, 0\n"
    "ds_write_b128 v11, v[120:123] offset:31872\n"
    "v_mov_b32 v124, 0x3f803f80\n"
    "v_mov_b32 v125, 0x3f803f80\n"
    "v_mov_b32 v126, 0x3f803f80\n"
    "v_mov_b32 v127, 0x3f803f80\n"
    "ds_write_b128 v11, v[124:127] offset:31888\n"
    "v_mov_b32 v128, 0x3f003f00\n"
    "v_mov_b32 v129, 0x3f003f00\n"
    "v_mov_b32 v130, 0x3f003f00\n"
    "v_mov_b32 v131, 0x3f003f00\n"
    "ds_write_b128 v11, v[128:131] offset:31904\n"
    "s_waitcnt lgkmcnt(0)\n"
    "s_cmp_lt_u32 %2, 2\n"
    "s_cbranch_scc1 .Lsk_%=\n"
    "ds_write_b128 v2, v[88:91] offset:30720\n"
    "ds_write_b128 v2, v[92:95] offset:31744\n"
    "ds_write_b128 v2, v[96:99] offset:32768\n"
    "ds_write_b128 v2, v[100:103] offset:33792\n"
    ".Lsk_%=:\n"
    "s_cmp_eq_u32 %2, 3\n"
    "s_cbranch_scc1 .Lsm_%=\n"
    "s_mov_b64 s[44:45], %12\n"
    "s_and_b32 s45, s45, 0xffff\n"
    "s_mov_b32 s46, 0x2400\n"
    "s_mov_b32 s47, 0x20000\n"
    "s_mul_i32 s62, %2, 384\n"
    "s_mul_i32 s61, %2, 3072\n"
    "s_add_u32 m0, s61, 20480\n"
    "s_add_u32 s73, s62, 0\n"
    "buffer_load_dwordx4 v117, s[44:47], s73 offen lds\n"
    "s_add_u32 m0, s61, 21504\n"
    "s_add_u32 s73, s62, 128\n"
    "buffer_load_dwordx4 v117, s[44:47], s73 offen lds\n"
    "s_add_u32 m0, s61, 22528\n"
    "s_add_u32 s73, s62, 256\n"
    "buffer_load_dwordx4 v117, s[44:47], s73 offen lds\n"
    ".Lsm_%=:\n"
    "s_waitcnt vmcnt(0) lgkmcnt(0)\n"
    "s_barrier\n"
    "s_cmp_eq_u32 %2, 0\n"
    "s_cbranch_scc0 .Lnr_%=\n"
    "v_mov_b32 v134, 128\n"
    "v_mov_b32 v135, 1\n"
    "s_mov_b64 exec, 1\n"
    "global_atomic_add v134, v135, %1\n"
    "s_mov_b64 exec, -1\n"
    ".Lnr_%=:\n"
    "v_mov_b32 v1, 0x4444\n"
    "v_mov_b32 v118, 0xeeee\n"
    "v_cmp_gt_u32 vcc, 8, v113\n"
    "s_nop 1\n"
    "v_cndmask_b32 v1, v118, v1, vcc\n"
    "s_lshl_b32 s73, %2, 10\n"
    "v_add_u32 v5, s73, v0\n"
    "v_and_b32 v119, 7, v113\n"
    "v_lshrrev_b32 v120, 3, v113\n"
    "v_lshl_add_u32 v3, v115, 1, v116\n"
    "v_mul_u32_u24 v3, 544, v3\n"
    "v_lshl_add_u32 v3, v113, 2, v3\n"
    "s_lshl_b32 s73, %2, 6\n"
    "s_add_u32 s73, s73, 20480\n"
    "v_add_u32 v3, s73, v3\n"
    "v_mul_u32_u24 v4, 272, v119\n"
    "v_lshl_add_u32 v4, v114, 4, v4\n"
    "v_lshlrev_b32 v6, 7, v114\n"
    "v_lshl_add_u32 v6, v119, 4, v6\n"
    "s_lshl_b32 s73, %2, 9\n"
    "v_add_u32 v6, s73, v6\n"
    "s_mov_b32 s56, 0\n"
    "v_mov_b32 v134, 64\n"
    ".Lpoll1_%=:\n"
    "global_load_dword v135, v134, %1 sc1\n"
    "s_waitcnt vmcnt(0)\n"
    "v_readfirstlane_b32 s73, v135\n"
    "s_cmp_ge_u32 s73, 4\n"
    "s_cbranch_scc1 .Lpoll1_x%=\n"
    "s_add_u32 s56, s56, 1\n"
    "s_cmp_lt_u32 s56, 3\n"
    "s_cbranch_scc1 .Lpoll1_%=\n"
    "s_sleep 8\n"
    "s_cmp_lt_u32 s56, s57\n"
    "s_cbranch_scc1 .Lpoll1_%=\n"
    "s_mov_b32 s57, 0\n"
    "v_mov_b32 v135, 1\n"
    "global_store_dword v11, v135, %1 sc1\n"
    ".Lpoll1_x%=:\n"
    "s_xor_b32 s69, s73, %7\n"
    "s_lshr_b32 s51, s73, 1\n"
    "s_xor_b32 s51, s51, %7\n"
    "s_and_b32 s51, s51, 1\n"
    "s_lshl_b32 s51, s51, 13\n"
    "s_add_u32 s68, s51, 0x200000\n"
    "s_and_b32 s60, s69, 1\n"
    "s_lshl_b32 s60, s60, 13\n"
    "v_lshl_add_u32 v8, v120, 2, v115\n"
    "v_lshlrev_b32 v8, 12, v8\n"
    "v_lshl_add_u32 v8, v119, 4, v8\n"
    "v_lshl_add_u32 v7, v116, 15, v8\n"
    "v_lshl_add_u32 v8, v116, 3, v8\n"
    "s_mul_i32 s73, %5, 589824\n"
    "s_lshl_b32 s51, %4, 7\n"
    "s_add_u32 s73, s73, s51\n"
    "s_add_u32 s73, s73, s60\n"
    "s_lshl_b32 s51, %2, 16\n"
    "s_add_u32 s51, s51, s73\n"
    "v_add_u32 v7, s51, v7\n"
    "s_and_b32 s51, %2, 1\n"
    "s_lshl_b32 s51, s51, 15\n"
    "s_add_u32 s51, s51, 524288\n"
    "s_add_u32 s51, s51, s73\n"
    "v_add_u32 v8, s51, v8\n"
    "s_mov_b32 s74, 262144\n"
    "s_lshl_b32 s61, %4, 1\n"
    "s_add_u32 s61, s61, %5\n"
    "s_lshr_b32 s73, s61, 2\n"
    "s_add_u32 s73, s73, 128\n"
    "s_and_b32 s51, s61, 3\n"
    "s_cmp_eq_u32 s51, 0\n"
    "s_cselect_b32 s71, s73, -1\n"
    "s_lshl_b32 s73, s61, 1\n"
    "s_add_u32 s73, s73, %2\n"
    "s_cmp_lt_u32 %2, 2\n"
    "s_cselect_b32 s71, s73, s71\n"
    "s_max_i32 s61, s71, 0\n"
    "s_lshr_b32 s73, s61, 1\n"
    "s_lshl_b32 s73, s73, 14\n"
    "s_and_b32 s51, s61, 1\n"
    "s_lshl_b32 s51, s51, 12\n"
    "s_add_u32 s65, s73, s51\n"
    "s_add_u32 s65, s65, s60\n"
    "s_cmp_ge_u32 s61, 48\n"
    "s_cselect_b32 s94, 1, 0\n"
    "s_cmp_ge_u32 s61, 96\n"
    "s_cselect_b32 s94, 2, s94\n"
    "s_mul_i32 s73, s94, 48\n"
    "s_sub_u32 s73, s61, s73\n"
    "s_lshl_b32 s73, s73, 6\n"
    "v_lshlrev_b32 v9, 4, v114\n"
    "v_add_u32 v9, s73, v9\n"
    "v_bfrev_b32 v118, 1\n"
    "v_cmp_eq_u32 vcc, 0, v113\n"
    "s_nop 1\n"
    "v_cndmask_b32 v9, v118, v9, vcc\n"
    "s_mul_i32 s73, %2, 3072\n"
    "v_add_u32 v10, s73, v0\n"
    "s_cmp_eq_u32 %2, 3\n"
    "s_cbranch_scc0 .Lnw3_%=\n"
    "v_lshl_add_u32 v10, v113, 1, %5\n"
    "v_lshlrev_b32 v118, 3, v114\n"
    "v_sub_u32 v10, v10, v118\n"
    "v_cmp_le_i32 s[76:77], 0, v10\n"
    "v_cmp_le_i32 s[78:79], 2, v10\n"
    "v_cmp_le_i32 s[80:81], 4, v10\n"
    "v_cmp_le_i32 s[82:83], 6, v10\n"
    "v_cmp_le_i32 s[84:85], 1, v10\n"
    "v_cmp_le_i32 s[86:87], 3, v10\n"
    "v_cmp_le_i32 s[88:89], 5, v10\n"
    "v_cmp_le_i32 s[90:91], 7, v10\n"
    ".Lnw3_%=:\n"
    "s_lshr_b32 s73, %4, 2\n"
    "s_lshl1_add_u32 s73, s73, %7\n"
    "s_and_b32 s51, %4, 3\n"
    "s_lshl3_add_u32 s73, s73, s51\n"
    "s_lshl_b32 s73, s73, 8\n"
    "s_add_u32 s92, s73, 86016\n"
    "s_lshr_b32 s51, %4, 3\n"
    "s_lshl2_add_u32 s73, %5, s51\n"
    "s_lshl1_add_u32 s73, s73, %7\n"
    "s_and_b32 s51, %4, 7\n"
    "s_lshl4_add_u32 s73, s73, s51\n"
    "s_lshl_b32 s73, s73, 7\n"
    "s_add_u32 s93, s73, 53248\n"
    "s_lshl_b32 s73, %5, 12\n"
    "s_lshl_b32 s51, %4, 14\n"
    "s_add_u32 s73, s73, s51\n"
    "s_add_u32 s75, s73, 0x9c00000\n"
    "s_mov_b32 s48, 0\n"
    "s_mov_b32 s54, 64\n"
    "s_mov_b32 s55, 112\n"
    "s_mov_b32 s96, 1280\n"
    "s_mov_b32 s56, 0\n"
    "v_mov_b32 v134, s92\n"
    ".Lpoll2_%=:\n"
    "global_load_dword v132, v134, %1 sc1\n"
    "global_load_dword v133, v134, %1 offset:128 sc1\n"
    "s_waitcnt vmcnt(0)\n"
    "v_and_b32 v133, v132, v133\n"
    "s_nop 0\n"
    "v_readfirstlane_b32 s73, v133\n"
    "s_cmp_ge_u32 s73, 1\n"
    "s_cbranch_scc1 .Lpoll2_x%=\n"
    "s_add_u32 s56, s56, 1\n"
    "s_cmp_lt_u32 s56, 3\n"
    "s_cbranch_scc1 .Lpoll2_%=\n"
    "s_sleep 8\n"
    "s_cmp_lt_u32 s56, s57\n"
    "s_cbranch_scc1 .Lpoll2_%=\n"
    "s_mov_b32 s57, 0\n"
    "v_mov_b32 v135, 1\n"
    "global_store_dword v11, v135, %1 sc1\n"
    ".Lpoll2_x%=:\n"
    "s_add_u32 s73, s48, 0\n"
    "s_and_b32 s73, s73, 63\n"
    "s_lshr_b32 s51, s73, 2\n"
    "s_xor_b32 s51, s51, s69\n"
    "s_and_b32 s51, s51, 1\n"
    "s_lshl_b32 s51, s51, 13\n"
    "s_lshl_b32 s73, s73, 5\n"
    "s_add_u32 s73, s73, %4\n"
    "s_lshl_b32 s73, s73, 14\n"
    "s_add_u32 s73, s73, s51\n"
    "s_add_u32 s60, s73, 0x5c00000\n"
    "s_add_u32 s61, s73, 0x5c01000\n"
    "s_add_u32 s62, s73, 0x7c00000\n"
    "s_add_u32 s63, s73, 0x7c01000\n"
    "s_lshl_b32 s51, %5, 12\n"
    "s_add_u32 s73, s73, s51\n"
    "s_add_u32 s64, s73, 0x3c00000\n"
    "buffer_load_dwordx4 v[12:15], v5, s[40:43], s60 offen sc1\n"
    "buffer_load_dwordx4 v[16:19], v5, s[40:43], s61 offen sc1\n"
    "buffer_load_dwordx4 v[20:23], v5, s[40:43], s62 offen sc1\n"
    "buffer_load_dwordx4 v[24:27], v5, s[40:43], s63 offen sc1\n"
    "buffer_load_dwordx4 v[28:31], v5, s[40:43], s64 offen sc1\n"
    "s_waitcnt vmcnt(4)\n"
    "ds_write_b128 v5, v[12:15]\n"
    "s_waitcnt vmcnt(3)\n"
    "ds_write_b128 v5, v[16:19] offset:4096\n"
    "s_waitcnt vmcnt(0)\n"
    "ds_write_b128 v5, v[28:31] offset:16384\n"
    "v_swap_b32 v22, v24\n"
    "v_swap_b32 v23, v25\n"
    "ds_write_b128 v5, v[20:23] offset:8192\n"
    "ds_write_b128 v5, v[24:27] offset:12288\n"
    "s_add_u32 s73, s48, 1\n"
    "s_and_b32 s51, s73, 32\n"
    "s_and_b32 s73, s73, 31\n"
    "s_lshl3_add_u32 s73, s51, s73\n"
    "s_lshl2_add_u32 s73, s73, s92\n"
    "v_mov_b32 v134, s73\n"
    "global_load_dword v132, v134, %1 sc1\n"
    "global_load_dword v133, v134, %1 offset:128 sc1\n"
    "s_waitcnt vmcnt(0)\n"
    "v_and_b32 v133, v132, v133\n"
    "s_nop 0\n"
    "v_readfirstlane_b32 s59, v133\n"
    "s_mov_b32 s56, 0\n"
    "v_mov_b32 v134, 128\n"
    ".Lpoll3_%=:\n"
    "global_load_dword v135, v134, %1 sc1\n"
    "s_waitcnt vmcnt(0)\n"
    "v_readfirstlane_b32 s73, v135\n"
    "s_cmp_ge_u32 s73, 128\n"
    "s_cbranch_scc1 .Lpoll3_x%=\n"
    "s_add_u32 s56, s56, 1\n"
    "s_cmp_lt_u32 s56, 3\n"
    "s_cbranch_scc1 .Lpoll3_%=\n"
    "s_sleep 8\n"
    "s_cmp_lt_u32 s56, s57\n"
    "s_cbranch_scc1 .Lpoll3_%=\n"
    "s_mov_b32 s57, 0\n"
    "v_mov_b32 v135, 1\n"
    "global_store_dword v11, v135, %1 sc1\n"
    ".Lpoll3_x%=:\n"
    "s_memrealtime s[50:51]\n"
    "s_waitcnt lgkmcnt(0)\n"
    "v_mov_b32 v134, s50\n"
    "ds_write_b32 v11, v134 offset:31920\n"
    "s_mov_b32 s58, s50\n"
    "s_add_u32 s73, s48, 1\n"
    "s_and_b32 s73, s73, 63\n"
    "s_lshr_b32 s51, s73, 2\n"
    "s_xor_b32 s51, s51, s69\n"
    "s_and_b32 s51, s51, 1\n"
    "s_lshl_b32 s51, s51, 13\n"
    "s_lshl_b32 s73, s73, 5\n"
    "s_add_u32 s73, s73, %4\n"
    "s_lshl_b32 s73, s73, 14\n"
    "s_add_u32 s73, s73, s51\n"
    "s_add_u32 s60, s73, 0x5c00000\n"
    "s_add_u32 s61, s73, 0x5c01000\n"
    "s_add_u32 s62, s73, 0x7c00000\n"
    "s_add_u32 s63, s73, 0x7c01000\n"
    "s_lshl_b32 s51, %5, 12\n"
    "s_add_u32 s73, s73, s51\n"
    "s_add_u32 s64, s73, 0x3c00000\n"
    "ds_read_b128 v[32:35], v2\n"
    "ds_read_b128 v[36:39], v2 offset:1024\n"
    "ds_read_b128 v[40:43], v2 offset:2048\n"
    "ds_read_b128 v[44:47], v2 offset:3072\n"
    "ds_read_b128 v[48:51], v2 offset:4096\n"
    "ds_read_b128 v[52:55], v2 offset:5120\n"
    "ds_read_b128 v[56:59], v2 offset:6144\n"
    "ds_read_b128 v[60:63], v2 offset:7168\n"
    "ds_read_b128 v[64:67], v2 offset:8192\n"
    "ds_read_b128 v[68:71], v2 offset:9216\n"
    "ds_read_b128 v[72:75], v2 offset:10240\n"
    "ds_read_b128 v[76:79], v2 offset:11264\n"
    "ds_read_b128 v[80:83], v2 offset:12288\n"
    "ds_read_b128 v[84:87], v2 offset:13312\n"
    "v_mov_b64 v[124:125], 0\n"
    "v_mov_b64 v[126:127], 0\n"
    "v_mov_b64 v[128:129], 0\n"
    "v_mov_b64 v[130:131], 0\n"
    "s_waitcnt lgkmcnt(0)\n"
    "s_barrier\n"
    ".Lstep_%=:\n"
    "ds_read_b128 v[88:91], v0 offset:20480\n"
    "ds_read_b128 v[92:95], v0 offset:21504\n"
    "ds_read_b128 v[96:99], v0 offset:22528\n"
    "ds_read_b128 v[100:103], v0 offset:23552\n"
    "ds_read_b128 v[104:107], v0 offset:24576\n"
    "ds_read_b128 v[108:111], v0 offset:25600\n"
    "ds_read_b128 v[112:115], v0 offset:26624\n"
    "ds_read_b128 v[116:119], v0 offset:27648\n"
    "ds_read_b128 v[120:123], v0 offset:28672\n"
    "buffer_load_dwordx4 v[12:15], v5, s[40:43], s60 offen sc1\n"
    "buffer_load_dwordx4 v[16:19], v5, s[40:43], s61 offen sc1\n"
    "buffer_load_dwordx4 v[20:23], v5, s[40:43], s62 offen sc1\n"
    "buffer_load_dwordx4 v[24:27], v5, s[40:43], s63 offen sc1\n"
    "buffer_load_dwordx4 v[28:31], v5, s[40:43], s64 offen sc1\n"
    "s_waitcnt lgkmcnt(8)\n"
    "v_smfmac_f32_16x16x64_bf16 v[124:127], v[88:91], v[32:39], v1\n"
    "v_smfmac_f32_16x16x64_bf16 v[128:131], v[88:91], v[40:47], v1\n"
    "s_waitcnt lgkmcnt(7)\n"
    "v_smfmac_f32_16x16x64_bf16 v[124:127], v[92:95], v[48:55], v1\n"
    "v_mov_b64 v[32:33], 0\n"
    "v_mov_b64 v[34:35], 0\n"
    "v_smfmac_f32_16x16x64_bf16 v[128:131], v[92:95], v[56:63], v1\n"
    "v_mov_b64 v[36:37], 0\n"
    "v_mov_b64 v[38:39], 0\n"
    "s_waitcnt lgkmcnt(6)\n"
    "v_smfmac_f32_16x16x64_bf16 v[124:127], v[96:99], v[64:71], v1\n"
    "v_smfmac_f32_16x16x64_bf16 v[128:131], v[96:99], v[72:79], v1\n"
    "s_waitcnt lgkmcnt(5)\n"
    "v_smfmac_f32_16x16x64_bf16 v[124:127], v[100:103], v[80:87], v1\n"
    "v_smfmac_f32_16x16x64_bf16 v[128:131], v[100:103], v[136:143], v1\n"
    "s_waitcnt lgkmcnt(4)\n"
    "v_smfmac_f32_16x16x64_bf16 v[124:127], v[104:107], v[144:151], v1\n"
    "ds_read_b128 v[40:43], v0\n"
    "v_smfmac_f32_16x16x64_bf16 v[128:131], v[104:107], v[152:159], v1\n"
    "ds_read_b128 v[44:47], v0 offset:4096\n"
    "s_waitcnt lgkmcnt(5)\n"
    "v_smfmac_f32_16x16x64_bf16 v[124:127], v[108:111], v[160:167], v1\n"
    "ds_read_b128 v[48:51], v0 offset:1024\n"
    "v_smfmac_f32_16x16x64_bf16 v[128:131], v[108:111], v[168:175], v1\n"
    "ds_read_b128 v[52:55], v0 offset:5120\n"
    "s_waitcnt lgkmcnt(6)\n"
    "v_smfmac_f32_16x16x64_bf16 v[124:127], v[112:115], v[176:183], v1\n"
    "ds_read_b128 v[56:59], v0 offset:2048\n"
    "v_smfmac_f32_16x16x64_bf16 v[128:131], v[112:115], v[184:191], v1\n"
    "ds_read_b128 v[60:63], v0 offset:6144\n"
    "s_waitcnt lgkmcnt(7)\n"
    "v_smfmac_f32_16x16x64_bf16 v[124:127], v[116:119], v[192:199], v1\n"
    "ds_read_b128 v[64:67], v0 offset:3072\n"
    "v_smfmac_f32_16x16x64_bf16 v[128:131], v[116:119], v[200:207], v1\n"
    "ds_read_b128 v[68:71], v0 offset:7168\n"
    "s_waitcnt lgkmcnt(8)\n"
    "v_smfmac_f32_16x16x64_bf16 v[124:127], v[120:123], v[208:215], v1\n"
    "v_smfmac_f32_16x16x64_bf16 v[128:131], v[120:123], v[216:223], v1\n"
    "v_smfmac_f32_16x16x64_bf16 v[32:35], v[88:91], v[224:231], v1\n"
    "v_smfmac_f32_16x16x64_bf16 v[36:39], v[88:91], v[232:239], v1\n"
    "v_smfmac_f32_16x16x64_bf16 v[32:35], v[92:95], v[240:247], v1\n"
    "v_permlane32_swap_b32 v124, v126\n"
    "v_permlane32_swap_b32 v128, v130\n"
    "v_smfmac_f32_16x16x64_bf16 v[36:39], v[92:95], v[248:255], v1\n"
    "v_permlane32_swap_b32 v125, v127\n"
    "v_permlane32_swap_b32 v129, v131\n"
    "v_smfmac_f32_16x16x64_bf16 v[32:35], v[96:99], a[0:7], v1\n"
    "v_add_f32 v124, v124, v126\n"
    "v_add_f32 v128, v128, v130\n"
    "v_smfmac_f32_16x16x64_bf16 v[36:39], v[96:99], a[8:15], v1\n"
    "v_add_f32 v125, v125, v127\n"
    "v_add_f32 v129, v129, v131\n"
    "v_smfmac_f32_16x16x64_bf16 v[32:35], v[100:103], a[16:23], v1\n"
    "v_cvt_pk_bf16_f32 v76, v124, v128\n"
    "v_cvt_pk_bf16_f32 v77, v125, v129\n"
    "ds_write_b32 v3, v76 offset:9216\n"
    "ds_write_b32 v3, v77 offset:9488\n"
    "v_smfmac_f32_16x16x64_bf16 v[36:39], v[100:103], a[24:31], v1\n"
    "v_smfmac_f32_16x16x64_bf16 v[32:35], v[104:107], a[32:39], v1\n"
    "v_smfmac_f32_16x16x64_bf16 v[36:39], v[104:107], a[40:47], v1\n"
    "v_smfmac_f32_16x16x64_bf16 v[32:35], v[108:111], a[48:55], v1\n"
    "s_waitcnt lgkmcnt(0)\n"
    "s_barrier\n"
    "ds_read_b128 v[124:127], v4 offset:29696\n"
    "v_smfmac_f32_16x16x64_bf16 v[36:39], v[108:111], a[56:63], v1\n"
    "ds_read_b128 v[128:131], v4 offset:29760\n"
    "v_smfmac_f32_16x16x64_bf16 v[32:35], v[112:115], a[64:71], v1\n"
    "ds_read_b128 v[132:135], v4 offset:29824\n"
    "v_smfmac_f32_16x16x64_bf16 v[36:39], v[112:115], a[72:79], v1\n"
    "ds_read_b128 v[72:75], v4 offset:29888\n"
    "v_smfmac_f32_16x16x64_bf16 v[32:35], v[116:119], a[80:87], v1\n"
    "v_smfmac_f32_16x16x64_bf16 v[36:39], v[116:119], a[88:95], v1\n"
    "v_smfmac_f32_16x16x64_bf16 v[32:35], v[120:123], a[96:103], v1\n"
    "v_smfmac_f32_16x16x64_bf16 v[36:39], v[120:123], a[104:111], v1\n"
    "s_waitcnt lgkmcnt(3)\n"
    "v_mfma_f32_16x16x32_bf16 v[80:83], v[40:43], v[124:127], 0\n"
    "v_mfma_f32_16x16x32_bf16 v[84:87], v[44:47], v[124:127], 0\n"
    "ds_read_b128 v[40:43], v11 offset:31872\n"
    "ds_read_b128 v[44:47], v11 offset:31872\n"
    "s_waitcnt lgkmcnt(4)\n"
    "v_mfma_f32_16x16x32_bf16 v[80:83], v[48:51], v[128:131], v[80:83]\n"
    "v_permlane32_swap_b32 v32, v34\n"
    "v_permlane32_swap_b32 v36, v38\n"
    "v_mfma_f32_16x16x32_bf16 v[84:87], v[52:55], v[128:131], v[84:87]\n"
    "v_permlane32_swap_b32 v33, v35\n"
    "v_permlane32_swap_b32 v37, v39\n"
    "s_waitcnt lgkmcnt(3)\n"
    "v_mfma_f32_16x16x32_bf16 v[80:83], v[56:59], v[132:135], v[80:83]\n"
    "v_add_f32 v32, v32, v34\n"
    "v_add_f32 v36, v36, v38\n"
    "v_mfma_f32_16x16x32_bf16 v[84:87], v[60:63], v[132:135], v[84:87]\n"
    "v_add_f32 v33, v33, v35\n"
    "v_add_f32 v37, v37, v39\n"
    "s_waitcnt lgkmcnt(2)\n"
    "v_mfma_f32_16x16x32_bf16 v[80:83], v[64:67], v[72:75], v[80:83]\n"
    "v_cvt_pk_bf16_f32 v76, v32, v36\n"
    "v_cvt_pk_bf16_f32 v77, v33, v37\n"
    "ds_write2_b32 v3, v76, v77 offset1:68\n"
    "v_mfma_f32_16x16x32_bf16 v[84:87], v[68:71], v[72:75], v[84:87]\n"
    "s_waitcnt lgkmcnt(2)\n"
    "v_smfmac_f32_16x16x64_bf16 v[40:43], v[88:91], a[112:119], v1\n"
    "s_waitcnt lgkmcnt(1)\n"
    "v_smfmac_f32_16x16x64_bf16 v[44:47], v[88:91], a[120:127], v1\n"
    "s_waitcnt lgkmcnt(0)\n"
    "s_barrier\n"
    "ds_read_b128 v[48:51], v4 offset:20480\n"
    "ds_read_b128 v[52:55], v4 offset:20544\n"
    "ds_read_b128 v[56:59], v4 offset:20608\n"
    "ds_read_b128 v[60:63], v4 offset:20672\n"
    "v_smfmac_f32_16x16x64_bf16 v[40:43], v[92:95], a[128:135], v1\n"
    "v_maximum3_f32 v78, v80, v81, v82\n"
    "v_maximum3_f32 v78, v78, v83, v84\n"
    "v_smfmac_f32_16x16x64_bf16 v[44:47], v[92:95], a[136:143], v1\n"
    "v_maximum3_f32 v78, v78, v85, v86\n"
    "v_maximum3_f32 v78, v78, v87, v87\n"
    "ds_read_b128 v[32:35], v5 offset:8192\n"
    "ds_read_b128 v[36:39], v5 offset:12288\n"
    "v_smfmac_f32_16x16x64_bf16 v[40:43], v[96:99], a[144:151], v1\n"
    "v_mov_b32 v79, v78\n"
    "ds_read_b128 v[68:71], v11 offset:31888\n"
    "ds_read_b64 v[76:77], v11 offset:31904\n"
    "v_smfmac_f32_16x16x64_bf16 v[44:47], v[96:99], a[152:159], v1\n"
    "v_permlane32_swap_b32 v79, v78\n"
    "v_maximum3_f32 v78, v78, v79, v79\n"
    "s_waitcnt lgkmcnt(7)\n"
    "v_mfma_f32_16x16x32_bf16 v[64:67], v[48:51], v[124:127], 0\n"
    "ds_read_b128 v[88:91], v2 offset:14336\n"
    "ds_read_b128 v[92:95], v2 offset:15360\n"
    "s_waitcnt lgkmcnt(8)\n"
    "v_mfma_f32_16x16x32_bf16 v[64:67], v[52:55], v[128:131], v[64:67]\n"
    "s_waitcnt lgkmcnt(7)\n"
    "v_mfma_f32_16x16x32_bf16 v[64:67], v[56:59], v[132:135], v[64:67]\n"
    "s_waitcnt lgkmcnt(6)\n"
    "v_mfma_f32_16x16x32_bf16 v[64:67], v[60:63], v[72:75], v[64:67]\n"
    "v_smfmac_f32_16x16x64_bf16 v[40:43], v[100:103], a[160:167], v1\n"
    "ds_read_b128 v[48:51], v2 offset:18432\n"
    "ds_read_b128 v[52:55], v2 offset:19456\n"
    "v_smfmac_f32_16x16x64_bf16 v[44:47], v[100:103], a[168:175], v1\n"
    "ds_read_b128 v[56:59], v2 offset:20480\n"
    "ds_read_b128 v[60:63], v2 offset:21504\n"
    "v_maximum3_f32 v79, v64, v65, v66\n"
    "v_maximum3_f32 v78, v79, v67, v78\n"
    "v_mov_b32 v79, v78\n"
    "v_smfmac_f32_16x16x64_bf16 v[40:43], v[104:107], a[176:183], v1\n"
    "ds_read_b128 v[96:99], v2 offset:16384\n"
    "ds_read_b128 v[100:103], v2 offset:17408\n"
    "v_permlane16_swap_b32 v79, v78\n"
    "v_maximum3_f32 v78, v78, v79, v79\n"
    "v_mul_f32 v78, 0xbe0293ee, v78\n"
    "v_smfmac_f32_16x16x64_bf16 v[44:47], v[104:107], a[184:191], v1\n"
    "v_fmamk_f32 v80, v80, 0x3e0293ee, v78\n"
    "v_fmamk_f32 v81, v81, 0x3e0293ee, v78\n"
    "v_exp_f32 v80, v80\n"
    "v_exp_f32 v81, v81\n"
    "ds_read_b128 v[124:127], v2 offset:22528\n"
    "ds_read_b128 v[128:131], v2 offset:23552\n"
    "v_smfmac_f32_16x16x64_bf16 v[40:43], v[108:111], a[192:199], v1\n"
    "v_fmamk_f32 v82, v82, 0x3e0293ee, v78\n"
    "v_fmamk_f32 v83, v83, 0x3e0293ee, v78\n"
    "v_exp_f32 v82, v82\n"
    "v_exp_f32 v83, v83\n"
    "v_cvt_pk_bf16_f32 v80, v80, v81\n"
    "v_smfmac_f32_16x16x64_bf16 v[44:47], v[108:111], a[200:207], v1\n"
    "v_fmamk_f32 v84, v84, 0x3e0293ee, v78\n"
    "v_fmamk_f32 v85, v85, 0x3e0293ee, v78\n"
    "v_exp_f32 v84, v84\n"
    "v_exp_f32 v85, v85\n"
    "v_cvt_pk_bf16_f32 v81, v82, v83\n"
    "v_smfmac_f32_16x16x64_bf16 v[40:43], v[112:115], a[208:215], v1\n"
    "v_fmamk_f32 v86, v86, 0x3e0293ee, v78\n"
    "v_fmamk_f32 v87, v87, 0x3e0293ee, v78\n"
    "v_exp_f32 v86, v86\n"
    "v_exp_f32 v87, v87\n"
    "v_cvt_pk_bf16_f32 v82, v84, v85\n"
    "ds_read_b128 v[104:107], v2 offset:24576\n"
    "ds_read_b128 v[108:111], v2 offset:25600\n"
    "v_smfmac_f32_16x16x64_bf16 v[44:47], v[112:115], a[216:223], v1\n"
    "v_fmamk_f32 v64, v64, 0x3e0293ee, v78\n"
    "v_fmamk_f32 v65, v65, 0x3e0293ee, v78\n"
    "v_exp_f32 v64, v64\n"
    "v_exp_f32 v65, v65\n"
    "v_cvt_pk_bf16_f32 v83, v86, v87\n"
    "v_smfmac_f32_16x16x64_bf16 v[40:43], v[116:119], a[224:231], v1\n"
    "v_fmamk_f32 v66, v66, 0x3e0293ee, v78\n"
    "v_fmamk_f32 v67, v67, 0x3e0293ee, v78\n"
    "v_exp_f32 v66, v66\n"
    "v_exp_f32 v67, v67\n"
    "v_cvt_pk_bf16_f32 v64, v64, v65\n"
    "v_smfmac_f32_16x16x64_bf16 v[44:47], v[116:119], a[232:239], v1\n"
    "v_cvt_pk_bf16_f32 v65, v66, v67\n"
    "v_smfmac_f32_16x16x64_bf16 v[40:43], v[120:123], a[240:247], v1\n"
    "ds_read_b128 v[112:115], v2 offset:26624\n"
    "ds_read_b128 v[116:119], v2 offset:27648\n"
    "v_smfmac_f32_16x16x64_bf16 v[44:47], v[120:123], a[248:255], v1\n"
    "s_waitcnt lgkmcnt(14)\n"
    "v_mfma_f32_16x16x32_bf16 v[72:75], v[68:71], v[80:83], 0\n"
    "v_mfma_f32_16x16x32_bf16 v[84:87], v[32:35], v[80:83], 0\n"
    "ds_read_b128 v[120:123], v11 offset:31872\n"
    "ds_read_b128 v[68:71], v11 offset:31872\n"
    "v_mfma_f32_16x16x32_bf16 v[132:135], v[36:39], v[80:83], 0\n"
    "ds_read_b128 v[32:35], v2 offset:28672\n"
    "ds_read_b128 v[36:39], v2 offset:29696\n"
    "v_cvt_pk_bf16_f32 v40, v40, v41\n"
    "v_cvt_pk_bf16_f32 v41, v42, v43\n"
    "s_waitcnt lgkmcnt(14)\n"
    "v_mfma_f32_16x16x16_bf16 v[72:75], v[76:77], v[64:65], v[72:75]\n"
    "v_cvt_pk_bf16_f32 v42, v44, v45\n"
    "v_cvt_pk_bf16_f32 v43, v46, v47\n"
    "v_mfma_f32_16x16x16_bf16 v[84:87], v[40:41], v[64:65], v[84:87]\n"
    "ds_read_b128 v[80:83], v11 offset:31872\n"
    "ds_read_b128 v[44:47], v11 offset:31872\n"
    "v_mfma_f32_16x16x16_bf16 v[132:135], v[42:43], v[64:65], v[132:135]\n"
    "ds_write_b128 v5, v[40:43] offset:22784\n"
    "ds_read_b128 v[64:67], v11 offset:31872\n"
    "s_and_b32 s49, s48, 1\n"
    "s_lshl_b32 s70, s49, 3\n"
    "v_rcp_f32 v78, v72\n"
    "s_nop 0\n"
    "v_pk_mul_f32 v[84:85], v[84:85], v[78:79] op_sel_hi:[1,0]\n"
    "v_pk_mul_f32 v[86:87], v[86:87], v[78:79] op_sel_hi:[1,0]\n"
    "s_nop 0\n"
    "v_pk_mul_f32 v[132:133], v[132:133], v[78:79] op_sel_hi:[1,0]\n"
    "v_pk_mul_f32 v[134:135], v[134:135], v[78:79] op_sel_hi:[1,0]\n"
    "v_cvt_pk_bf16_f32 v84, v84, v132\n"
    "v_cvt_pk_bf16_f32 v85, v85, v133\n"
    "v_cvt_pk_bf16_f32 v86, v86, v134\n"
    "v_cvt_pk_bf16_f32 v87, v87, v135\n"
    "ds_write_b128 v6, v[84:87] offset:26880\n"
    "s_cmp_eq_u32 s48, 63\n"
    "s_waitcnt lgkmcnt(0)\n"
    "s_barrier\n"
    "s_cbranch_scc1 .Lend_%=\n"
    "ds_read_b128 v[132:135], v0 offset:26880\n"
    "ds_read_b128 v[72:75], v0 offset:27904\n"
    "s_cmp_lt_u32 %2, 2\n"
    "s_waitcnt lgkmcnt(1)\n"
    "v_smfmac_f32_16x16x64_bf16 v[68:71], v[132:135], v[88:95], v1\n"
    "v_smfmac_f32_16x16x64_bf16 v[80:83], v[132:135], v[96:103], v1\n"
    "ds_read_b128 v[88:91], v2 offset:30720\n"
    "ds_read_b128 v[92:95], v2 offset:31744\n"
    "s_waitcnt lgkmcnt(2)\n"
    "v_smfmac_f32_16x16x64_bf16 v[68:71], v[72:75], v[48:55], v1\n"
    "ds_read_b128 v[96:99], v2 offset:32768\n"
    "ds_read_b128 v[100:103], v2 offset:33792\n"
    "v_smfmac_f32_16x16x64_bf16 v[80:83], v[72:75], v[56:63], v1\n"
    "v_smfmac_f32_16x16x64_bf16 v[120:123], v[132:135], v[124:131], v1\n"
    "v_smfmac_f32_16x16x64_bf16 v[44:47], v[132:135], v[104:111], v1\n"
    "v_smfmac_f32_16x16x64_bf16 v[120:123], v[72:75], v[112:119], v1\n"
    "v_permlane32_swap_b32 v68, v80\n"
    "v_permlane32_swap_b32 v69, v81\n"
    "v_smfmac_f32_16x16x64_bf16 v[44:47], v[72:75], v[32:39], v1\n"
    "s_waitcnt lgkmcnt(0)\n"
    "s_cbranch_scc1 .Lx1_%=\n"
    "v_smfmac_f32_16x16x64_bf16 v[64:67], v[132:135], v[88:95], v1\n"
    "v_smfmac_f32_16x16x64_bf16 v[64:67], v[72:75], v[96:103], v1\n"
    ".Lx1_%=:\n"
    "v_permlane32_swap_b32 v70, v82\n"
    "v_permlane32_swap_b32 v71, v83\n"
    "v_pk_add_f32 v[68:69], v[68:69], v[80:81]\n"
    "v_pk_add_f32 v[70:71], v[70:71], v[82:83]\n"
    "v_bfi_b32 v68, 15, s49, v68\n"
    "v_bfi_b32 v70, 15, s49, v70\n"
    "buffer_store_dwordx4 v[68:71], v7, s[40:43], 0 offen sc1\n"
    "v_permlane32_swap_b32 v120, v44\n"
    "v_permlane32_swap_b32 v121, v45\n"
    "v_permlane32_swap_b32 v122, v46\n"
    "v_permlane32_swap_b32 v123, v47\n"
    "v_pk_add_f32 v[120:121], v[120:121], v[44:45]\n"
    "v_pk_add_f32 v[122:123], v[122:123], v[46:47]\n"
    "v_bfi_b32 v120, 15, s49, v120\n"
    "v_bfi_b32 v122, 15, s49, v122\n"
    "buffer_store_dwordx4 v[120:123], v7, s[40:43], s74 offen sc1\n"
    "s_cbranch_scc1 .Lx2_%=\n"
    "v_permlane32_swap_b32 v64, v66\n"
    "v_permlane32_swap_b32 v65, v67\n"
    "v_pk_add_f32 v[64:65], v[64:65], v[66:67]\n"
    "v_bfi_b32 v64, 15, s49, v64\n"
    "buffer_store_dwordx2 v[64:65], v8, s[40:43], 0 offen sc1\n"
    ".Lx2_%=:\n"
    "ds_read_b32 v135, v11 offset:31920\n"
    "s_memrealtime s[38:39]\n"
    "s_waitcnt lgkmcnt(0)\n"
    "v_readfirstlane_b32 s95, v135\n"
    "s_add_u32 s53, s54, 16\n"
    "s_sub_u32 s73, s38, s58\n"
    "s_lshl_b32 s73, s73, 3\n"
    "s_sub_u32 s73, s73, s96\n"
    "s_ashr_i32 s73, s73, 2\n"
    "s_max_i32 s73, s73, -32\n"
    "s_min_i32 s73, s73, 16\n"
    "s_add_u32 s96, s96, s73\n"
    "s_add_u32 s73, s38, s53\n"
    ".Lsh4_%=:\n"
    "s_sub_u32 s51, s95, s73\n"
    "s_cmp_lt_u32 s51, 1024\n"
    "s_cbranch_scc1 .Lsh4_x%=\n"
    "s_add_u32 s95, s95, 48\n"
    "s_cmp_lt_u32 s51, -1024\n"
    "s_cselect_b32 s95, s73, s95\n"
    "s_branch .Lsh4_%=\n"
    ".Lsh4_x%=:\n"
    "s_cmp_eq_u32 %2, 3\n"
    "s_cbranch_scc1 .Lw3_%=\n"
    "ds_read_b128 v[32:35], v2\n"
    "ds_read_b128 v[36:39], v2 offset:1024\n"
    "ds_read_b128 v[40:43], v2 offset:2048\n"
    "ds_read_b128 v[44:47], v2 offset:3072\n"
    "ds_read_b128 v[48:51], v2 offset:4096\n"
    "ds_read_b128 v[52:55], v2 offset:5120\n"
    "ds_read_b128 v[56:59], v2 offset:6144\n"
    "ds_read_b128 v[60:63], v2 offset:7168\n"
    "ds_read_b128 v[64:67], v2 offset:8192\n"
    "ds_read_b128 v[68:71], v2 offset:9216\n"
    "ds_read_b128 v[72:75], v2 offset:10240\n"
    "ds_read_b128 v[76:79], v2 offset:11264\n"
    "ds_read_b128 v[80:83], v2 offset:12288\n"
    "ds_read_b128 v[84:87], v2 offset:13312\n"
    "v_mov_b64 v[124:125], 0\n"
    "v_mov_b64 v[126:127], 0\n"
    "v_mov_b64 v[128:129], 0\n"
    "v_mov_b64 v[130:131], 0\n"
    "s_mul_i32 s73, s48, 3\n"
    "s_add_u32 s73, s73, s94\n"
    "s_and_b32 s51, s73, 1\n"
    "s_lshl_b32 s51, s51, 12\n"
    "s_lshr_b32 s73, s73, 1\n"
    "s_lshl_b32 s73, s73, 14\n"
    "s_add_u32 s73, s73, s51\n"
    "s_add_u32 s66, s73, s68\n"
    "s_mul_i32 s73, s48, 3\n"
    "s_add_u32 s73, s73, %2\n"
    "s_and_b32 s51, s73, 1\n"
    "s_lshl_b32 s51, s51, 12\n"
    "s_lshr_b32 s73, s73, 1\n"
    "s_lshl_b32 s73, s73, 14\n"
    "s_add_u32 s73, s73, s51\n"
    "s_add_u32 s67, s73, s68\n"
    "s_waitcnt lgkmcnt(0)\n"
    "s_cmp_lt_i32 s71, 0\n"
    "s_cbranch_scc1 .Lnored_%=\n"
    "s_mov_b32 s56, 0\n"
    ".Lred_%=:\n"
    "s_sub_u32 s53, s95, s54\n"
    "s_waitcnt lgkmcnt(0)\n"
    ".Lgl5_%=:\n"
    "s_memrealtime s[50:51]\n"
    "s_waitcnt lgkmcnt(0)\n"
    "s_sub_u32 s50, s50, s53\n"
    "s_cmp_lt_i32 s50, 0\n"
    "s_cbranch_scc1 .Lgl5_%=\n"
    "buffer_load_dwordx4 v[88:91], v0, s[40:43], s65 offen sc1\n"
    "buffer_load_dwordx4 v[92:95], v0, s[40:43], s65 offen offset:1024 sc1\n"
    "buffer_load_dwordx4 v[96:99], v0, s[40:43], s65 offen offset:2048 sc1\n"
    "buffer_load_dwordx4 v[100:103], v0, s[40:43], s65 offen offset:3072 sc1\n"
    "s_lshr_b32 s73, s96, 3\n"
    "s_add_u32 s73, s73, s95\n"
    "s_add_u32 s73, s73, s55\n"
    "s_and_b32 s73, s73, -2\n"
    "v_mov_b32 v105, s73\n"
    "s_waitcnt vmcnt(2)\n"
    "v_add3_u32 v104, v88, v90, v92\n"
    "v_pk_add_f32 v[108:109], v[88:89], v[92:93]\n"
    "v_pk_add_f32 v[110:111], v[90:91], v[94:95]\n"
    "s_waitcnt vmcnt(1)\n"
    "v_add3_u32 v104, v104, v94, v96\n"
    "v_pk_add_f32 v[108:109], v[108:109], v[96:97]\n"
    "v_pk_add_f32 v[110:111], v[110:111], v[98:99]\n"
    "s_waitcnt vmcnt(0)\n"
    "v_add3_u32 v104, v104, v98, v100\n"
    "v_pk_add_f32 v[108:109], v[108:109], v[100:101]\n"
    "v_pk_add_f32 v[110:111], v[110:111], v[102:103]\n"
    "v_add_u32 v104, v104, v102\n"
    "v_and_b32 v104, 15, v104\n"
    "v_permlane32_swap_b32 v108, v110\n"
    "v_permlane32_swap_b32 v109, v111\n"
    "v_cmp_ne_u32 vcc, s70, v104\n"
    "v_pk_add_f32 v[108:109], v[108:109], v[110:111]\n"
    "s_cbranch_vccz .Lredok_%=\n"
    "s_memrealtime s[38:39]\n"
    "s_waitcnt lgkmcnt(0)\n"
    "s_add_u32 s73, s38, 44\n"
    ".Lsh6_%=:\n"
    "s_sub_u32 s51, s95, s73\n"
    "s_cmp_lt_u32 s51, 1024\n"
    "s_cbranch_scc1 .Lsh6_x%=\n"
    "s_add_u32 s95, s95, 48\n"
    "s_cmp_lt_u32 s51, -1024\n"
    "s_cselect_b32 s95, s73, s95\n"
    "s_branch .Lsh6_%=\n"
    ".Lsh6_x%=:\n"
    "s_add_u32 s56, s56, 1\n"
    "s_cmp_lt_u32 s56, s57\n"
    "s_cbranch_scc1 .Lred_%=\n"
    "s_mov_b32 s57, 0\n"
    "v_mov_b32 v135, 1\n"
    "global_store_dword v11, v135, %1 sc1\n"
    ".Lredok_%=:\n"
    "s_nop 0\n"
    "v_permlane16_swap_b32 v108, v109\n"
    "v_add_f32 v108, v108, v109\n"
    "s_nop 1\n"
    "v_add_f32_dpp v108, v108, v108 row_ror:8 row_mask:0xf bank_mask:0xf\n"
    "s_nop 1\n"
    "v_mov_b32_dpp v109, v108 quad_perm:[1,0,3,2] row_mask:0xf bank_mask:0xf\n"
    "v_cvt_pk_bf16_f32 v108, v108, v109\n"
    "v_min_u32 v108, -2, v108\n"
    "s_nop 1\n"
    "v_mov_b32_dpp v109, v108 row_shl:2 row_mask:0xf bank_mask:0xf\n"
    "v_mov_b32_dpp v110, v108 row_shl:4 row_mask:0xf bank_mask:0xf\n"
    "v_mov_b32_dpp v111, v108 row_shl:6 row_mask:0xf bank_mask:0xf\n"
    "buffer_store_dwordx4 v[108:111], v9, s[40:43], s66 offen sc1\n"
    "s_cmp_eq_u32 s71, 0\n"
    "s_cbranch_scc0 .Lnl_%=\n"
    "s_mov_b64 exec, 1\n"
    "buffer_store_dword v105, v11, s[40:43], s66 offen offset:3072 sc1\n"
    "s_mov_b64 exec, -1\n"
    ".Lnl_%=:\n"
    ".Lnored_%=:\n"
    "s_waitcnt vmcnt(2)\n"
    "s_cmp_eq_u32 %2, 0\n"
    "s_cselect_b32 s73, 0, 0x80000000\n"
    "v_mov_b32 v106, s73\n"
    "s_lshl_b32 s73, %2, 2\n"
    "v_mov_b32 v105, s73\n"
    "s_cmp_lg_u32 s59, 0\n"
    "s_cbranch_scc1 .Lkn7_%=\n"
    "s_waitcnt lgkmcnt(0)\n"
    "s_add_u32 s73, s48, 1\n"
    "s_and_b32 s51, s73, 32\n"
    "s_and_b32 s73, s73, 31\n"
    "s_lshl3_add_u32 s73, s51, s73\n"
    "s_lshl2_add_u32 s73, s73, s92\n"
    "s_mov_b32 s53, s73\n"
    "s_mov_b32 s56, 0\n"
    "v_mov_b32 v134, s53\n"
    ".Lpoll8_%=:\n"
    "global_load_dword v132, v134, %1 sc1\n"
    "global_load_dword v133, v134, %1 offset:128 sc1\n"
    "s_waitcnt vmcnt(0)\n"
    "v_and_b32 v133, v132, v133\n"
    "s_nop 0\n"
    "v_readfirstlane_b32 s73, v133\n"
    "s_cmp_ge_u32 s73, 1\n"
    "s_cbranch_scc1 .Lpoll8_x%=\n"
    "s_add_u32 s56, s56, 1\n"
    "s_cmp_lt_u32 s56, 3\n"
    "s_cbranch_scc1 .Lpoll8_%=\n"
    "s_sleep 8\n"
    "s_cmp_lt_u32 s56, s57\n"
    "s_cbranch_scc1 .Lpoll8_%=\n"
    "s_mov_b32 s57, 0\n"
    "v_mov_b32 v135, 1\n"
    "global_store_dword v11, v135, %1 sc1\n"
    ".Lpoll8_x%=:\n"
    "buffer_load_dwordx4 v[12:15], v5, s[40:43], s60 offen sc1\n"
    "buffer_load_dwordx4 v[16:19], v5, s[40:43], s61 offen sc1\n"
    "buffer_load_dwordx4 v[20:23], v5, s[40:43], s62 offen sc1\n"
    "buffer_load_dwordx4 v[24:27], v5, s[40:43], s63 offen sc1\n"
    "buffer_load_dwordx4 v[28:31], v5, s[40:43], s64 offen sc1\n"
    "s_waitcnt vmcnt(0)\n"
    ".Lkn7_%=:\n"
    "s_add_u32 s73, s48, 2\n"
    "s_and_b32 s51, s73, 32\n"
    "s_and_b32 s73, s73, 31\n"
    "s_lshl3_add_u32 s73, s51, s73\n"
    "s_lshl2_add_u32 s73, s73, s92\n"
    "v_mov_b32 v134, s73\n"
    "global_load_dword v132, v134, %1 sc1\n"
    "global_load_dword v133, v134, %1 offset:128 sc1\n"
    "s_add_u32 s73, s48, 2\n"
    "s_and_b32 s73, s73, 63\n"
    "s_lshr_b32 s51, s73, 2\n"
    "s_xor_b32 s51, s51, s69\n"
    "s_and_b32 s51, s51, 1\n"
    "s_lshl_b32 s51, s51, 13\n"
    "s_lshl_b32 s73, s73, 5\n"
    "s_add_u32 s73, s73, %4\n"
    "s_lshl_b32 s73, s73, 14\n"
    "s_add_u32 s73, s73, s51\n"
    "s_add_u32 s60, s73, 0x5c00000\n"
    "s_add_u32 s61, s73, 0x5c01000\n"
    "s_add_u32 s62, s73, 0x7c00000\n"
    "s_add_u32 s63, s73, 0x7c01000\n"
    "s_lshl_b32 s51, %5, 12\n"
    "s_add_u32 s73, s73, s51\n"
    "s_add_u32 s64, s73, 0x3c00000\n"
    "s_mov_b32 s56, 0\n"
    "s_sub_u32 s53, s95, 0\n"
    "s_waitcnt lgkmcnt(0)\n"
    ".Lgl9_%=:\n"
    "s_memrealtime s[50:51]\n"
    "s_waitcnt lgkmcnt(0)\n"
    "s_sub_u32 s50, s50, s53\n"
    "s_cmp_lt_i32 s50, 0\n"
    "s_cbranch_scc1 .Lgl9_%=\n"
    "buffer_load_dwordx4 v[88:91], v0, s[40:43], s67 offen sc1\n"
    "buffer_load_dwordx4 v[92:95], v0, s[40:43], s67 offen offset:1024 sc1\n"
    "buffer_load_dwordx4 v[96:99], v0, s[40:43], s67 offen offset:2048 sc1\n"
    "buffer_load_dword v101, v106, s[40:43], s67 offen offset:3072 sc1\n"
    "s_waitcnt lgkmcnt(0)\n"
    "s_barrier\n"
    "ds_write_b128 v5, v[12:15]\n"
    "ds_write_b128 v5, v[16:19] offset:4096\n"
    "ds_write_b128 v5, v[28:31] offset:16384\n"
    "v_swap_b32 v22, v24\n"
    "v_swap_b32 v23, v25\n"
    "ds_write_b128 v5, v[20:23] offset:8192\n"
    "ds_write_b128 v5, v[24:27] offset:12288\n"
    "s_waitcnt vmcnt(4)\n"
    "v_and_b32 v133, v132, v133\n"
    "s_nop 0\n"
    "v_readfirstlane_b32 s59, v133\n"
    ".Lgchk_%=:\n"
    "s_waitcnt vmcnt(2)\n"
    "v_max3_u32 v100, v88, v90, v92\n"
    "ds_write_b128 v10, v[88:91] offset:20480\n"
    "ds_write_b128 v10, v[92:95] offset:21504\n"
    "s_waitcnt vmcnt(1)\n"
    "v_max3_u32 v100, v100, v94, v96\n"
    "ds_write_b128 v10, v[96:99] offset:22528\n"
    "s_waitcnt vmcnt(0)\n"
    "v_max3_u32 v100, v100, v98, v101\n"
    "ds_write_b32 v105, v101 offset:31920\n"
    "v_cmp_eq_u32 vcc, -1, v100\n"
    "s_cbranch_vccz .Lgok_%=\n"
    "s_add_u32 s95, s95, 48\n"
    "s_memrealtime s[38:39]\n"
    "s_waitcnt lgkmcnt(0)\n"
    "s_add_u32 s73, s38, -12\n"
    ".Lsh10_%=:\n"
    "s_sub_u32 s51, s95, s73\n"
    "s_cmp_lt_u32 s51, 1024\n"
    "s_cbranch_scc1 .Lsh10_x%=\n"
    "s_add_u32 s95, s95, 48\n"
    "s_cmp_lt_u32 s51, -1024\n"
    "s_cselect_b32 s95, s73, s95\n"
    "s_branch .Lsh10_%=\n"
    ".Lsh10_x%=:\n"
    "s_sub_u32 s53, s95, 0\n"
    "s_waitcnt lgkmcnt(0)\n"
    ".Lgl11_%=:\n"
    "s_memrealtime s[50:51]\n"
    "s_waitcnt lgkmcnt(0)\n"
    "s_sub_u32 s50, s50, s53\n"
    "s_cmp_lt_i32 s50, 0\n"
    "s_cbranch_scc1 .Lgl11_%=\n"
    "buffer_load_dwordx4 v[88:91], v0, s[40:43], s67 offen sc1\n"
    "buffer_load_dwordx4 v[92:95], v0, s[40:43], s67 offen offset:1024 sc1\n"
    "buffer_load_dwordx4 v[96:99], v0, s[40:43], s67 offen offset:2048 sc1\n"
    "buffer_load_dword v101, v106, s[40:43], s67 offen offset:3072 sc1\n"
    "s_add_u32 s56, s56, 1\n"
    "s_cmp_lt_u32 s56, s57\n"
    "s_cbranch_scc1 .Lgchk_%=\n"
    "s_mov_b32 s57, 0\n"
    "v_mov_b32 v135, 1\n"
    "global_store_dword v11, v135, %1 sc1\n"
    "s_waitcnt vmcnt(0)\n"
    ".Lgok_%=:\n"
    "s_mov_b32 s58, s95\n"
    "s_add_u32 s48, s48, 1\n"
    "s_waitcnt lgkmcnt(0)\n"
    "s_barrier\n"
    "s_branch .Lstep_%=\n"
    ".Lw3_%=:\n"
    "ds_read_b128 v[32:35], v0 offset:16384\n"
    "ds_read_b128 v[48:51], v0\n"
    "ds_read_b128 v[64:67], v0 offset:4096\n"
    "ds_read_b128 v[80:83], v4 offset:20480\n"
    "ds_read_b128 v[36:39], v0 offset:17408\n"
    "ds_read_b128 v[52:55], v0 offset:1024\n"
    "ds_read_b128 v[68:71], v0 offset:5120\n"
    "ds_read_b128 v[84:87], v4 offset:20544\n"
    "ds_read_b128 v[40:43], v0 offset:18432\n"
    "ds_read_b128 v[56:59], v0 offset:2048\n"
    "ds_read_b128 v[72:75], v0 offset:6144\n"
    "ds_read_b128 v[88:91], v4 offset:20608\n"
    "ds_read_b128 v[44:47], v0 offset:19456\n"
    "ds_read_b128 v[60:63], v0 offset:3072\n"
    "ds_read_b128 v[76:79], v0 offset:7168\n"
    "ds_read_b128 v[92:95], v4 offset:20672\n"
    "s_waitcnt lgkmcnt(14)\n"
    "v_mfma_f32_16x16x32_bf16 v[96:99], v[48:51], v[32:35], 0\n"
    "s_waitcnt lgkmcnt(13)\n"
    "v_mfma_f32_16x16x32_bf16 v[100:103], v[64:67], v[32:35], 0\n"
    "s_waitcnt lgkmcnt(12)\n"
    "v_mfma_f32_16x16x32_bf16 v[104:107], v[80:83], v[32:35], 0\n"
    "s_waitcnt lgkmcnt(10)\n"
    "v_mfma_f32_16x16x32_bf16 v[96:99], v[52:55], v[36:39], v[96:99]\n"
    "s_waitcnt lgkmcnt(9)\n"
    "v_mfma_f32_16x16x32_bf16 v[100:103], v[68:71], v[36:39], v[100:103]\n"
    "s_waitcnt lgkmcnt(8)\n"
    "v_mfma_f32_16x16x32_bf16 v[104:107], v[84:87], v[36:39], v[104:107]\n"
    "s_waitcnt lgkmcnt(6)\n"
    "v_mfma_f32_16x16x32_bf16 v[96:99], v[56:59], v[40:43], v[96:99]\n"
    "s_waitcnt lgkmcnt(5)\n"
    "v_mfma_f32_16x16x32_bf16 v[100:103], v[72:75], v[40:43], v[100:103]\n"
    "s_waitcnt lgkmcnt(4)\n"
    "v_mfma_f32_16x16x32_bf16 v[104:107], v[88:91], v[40:43], v[104:107]\n"
    "s_waitcnt lgkmcnt(2)\n"
    "v_mfma_f32_16x16x32_bf16 v[96:99], v[60:63], v[44:47], v[96:99]\n"
    "s_waitcnt lgkmcnt(1)\n"
    "v_mfma_f32_16x16x32_bf16 v[100:103], v[76:79], v[44:47], v[100:103]\n"
    "s_waitcnt lgkmcnt(0)\n"
    "v_mfma_f32_16x16x32_bf16 v[104:107], v[92:95], v[44:47], v[104:107]\n"
    "ds_read_b128 v[32:35], v0 offset:8192\n"
    "ds_read_b128 v[36:39], v0 offset:12288\n"
    "ds_read_b128 v[40:43], v0 offset:9216\n"
    "ds_read_b128 v[44:47], v0 offset:13312\n"
    "ds_read_b128 v[48:51], v0 offset:10240\n"
    "ds_read_b128 v[52:55], v0 offset:14336\n"
    "ds_read_b128 v[56:59], v0 offset:11264\n"
    "ds_read_b128 v[60:63], v0 offset:15360\n"
    "ds_read_b128 v[64:67], v0 offset:22784\n"
    "ds_read_b128 v[68:71], v0 offset:23808\n"
    "ds_read_b128 v[72:75], v0 offset:24832\n"
    "ds_read_b128 v[76:79], v0 offset:25856\n"
    "ds_read_b128 v[80:83], v11 offset:31888\n"
    "ds_read_b64 v[84:85], v11 offset:31904\n"
    "v_mov_b32 v86, 0xd01502f9\n"
    "v_cndmask_b32 v96, v86, v96, s[76:77]\n"
    "v_cndmask_b32 v97, v86, v97, s[78:79]\n"
    "v_cndmask_b32 v98, v86, v98, s[80:81]\n"
    "v_cndmask_b32 v99, v86, v99, s[82:83]\n"
    "v_cndmask_b32 v100, v86, v100, s[84:85]\n"
    "v_cndmask_b32 v101, v86, v101, s[86:87]\n"
    "v_cndmask_b32 v102, v86, v102, s[88:89]\n"
    "v_cndmask_b32 v103, v86, v103, s[90:91]\n"
    "v_maximum3_f32 v87, v104, v105, v106\n"
    "v_maximum3_f32 v87, v87, v107, v96\n"
    "v_maximum3_f32 v87, v87, v97, v98\n"
    "v_maximum3_f32 v87, v87, v99, v100\n"
    "v_maximum3_f32 v87, v87, v101, v102\n"
    "v_maximum3_f32 v87, v87, v103, v103\n"
    "v_mov_b32 v86, v87\n"
    "s_nop 1\n"
    "v_permlane16_swap_b32 v86, v87\n"
    "v_maximum3_f32 v87, v87, v86, v86\n"
    "v_mov_b32 v86, v87\n"
    "s_nop 1\n"
    "v_permlane32_swap_b32 v86, v87\n"
    "v_maximum3_f32 v87, v87, v86, v86\n"
    "v_mul_f32 v87, 0xbe0293ee, v87\n"
    "v_fmamk_f32 v96, v96, 0x3e0293ee, v87\n"
    "v_fmamk_f32 v97, v97, 0x3e0293ee, v87\n"
    "v_fmamk_f32 v98, v98, 0x3e0293ee, v87\n"
    "v_fmamk_f32 v99, v99, 0x3e0293ee, v87\n"
    "v_fmamk_f32 v100, v100, 0x3e0293ee, v87\n"
    "v_fmamk_f32 v101, v101, 0x3e0293ee, v87\n"
    "v_fmamk_f32 v102, v102, 0x3e0293ee, v87\n"
    "v_fmamk_f32 v103, v103, 0x3e0293ee, v87\n"
    "v_fmamk_f32 v104, v104, 0x3e0293ee, v87\n"
    "v_fmamk_f32 v105, v105, 0x3e0293ee, v87\n"
    "v_fmamk_f32 v106, v106, 0x3e0293ee, v87\n"
    "v_fmamk_f32 v107, v107, 0x3e0293ee, v87\n"
    "v_exp_f32 v96, v96\n"
    "v_exp_f32 v97, v97\n"
    "v_exp_f32 v98, v98\n"
    "v_exp_f32 v99, v99\n"
    "v_exp_f32 v100, v100\n"
    "v_exp_f32 v101, v101\n"
    "v_exp_f32 v102, v102\n"
    "v_exp_f32 v103, v103\n"
    "v_exp_f32 v104, v104\n"
    "v_exp_f32 v105, v105\n"
    "v_exp_f32 v106, v106\n"
    "v_exp_f32 v107, v107\n"
    "v_cvt_pk_bf16_f32 v96, v96, v97\n"
    "v_cvt_pk_bf16_f32 v97, v98, v99\n"
    "v_cvt_pk_bf16_f32 v98, v100, v101\n"
    "v_cvt_pk_bf16_f32 v99, v102, v103\n"
    "v_cvt_pk_bf16_f32 v104, v104, v105\n"
    "v_cvt_pk_bf16_f32 v105, v106, v107\n"
    "s_waitcnt lgkmcnt(1)\n"
    "v_mfma_f32_16x16x32_bf16 v[108:111], v[80:83], v[96:99], 0\n"
    "v_mfma_f32_16x16x32_bf16 v[88:91], v[32:35], v[96:99], 0\n"
    "v_mfma_f32_16x16x32_bf16 v[120:123], v[36:39], v[96:99], 0\n"
    "v_mfma_f32_16x16x32_bf16 v[92:95], v[40:43], v[96:99], 0\n"
    "s_waitcnt lgkmcnt(0)\n"
    "v_mfma_f32_16x16x16_bf16 v[108:111], v[84:85], v[104:105], v[108:111]\n"
    "v_mfma_f32_16x16x32_bf16 v[124:127], v[44:47], v[96:99], 0\n"
    "v_mfma_f32_16x16x32_bf16 v[112:115], v[48:51], v[96:99], 0\n"
    "v_mfma_f32_16x16x32_bf16 v[128:131], v[52:55], v[96:99], 0\n"
    "v_mfma_f32_16x16x32_bf16 v[116:119], v[56:59], v[96:99], 0\n"
    "v_mfma_f32_16x16x32_bf16 v[132:135], v[60:63], v[96:99], 0\n"
    "v_rcp_f32 v86, v108\n"
    "v_mfma_f32_16x16x16_bf16 v[88:91], v[64:65], v[104:105], v[88:91]\n"
    "v_mfma_f32_16x16x16_bf16 v[120:123], v[66:67], v[104:105], v[120:123]\n"
    "v_mfma_f32_16x16x16_bf16 v[92:95], v[68:69], v[104:105], v[92:95]\n"
    "v_mfma_f32_16x16x16_bf16 v[124:127], v[70:71], v[104:105], v[124:127]\n"
    "v_mfma_f32_16x16x16_bf16 v[112:115], v[72:73], v[104:105], v[112:115]\n"
    "v_mfma_f32_16x16x16_bf16 v[128:131], v[74:75], v[104:105], v[128:131]\n"
    "v_mfma_f32_16x16x16_bf16 v[116:119], v[76:77], v[104:105], v[116:119]\n"
    "v_mfma_f32_16x16x16_bf16 v[132:135], v[78:79], v[104:105], v[132:135]\n"
    "v_pk_mul_f32 v[88:89], v[88:89], v[86:87] op_sel_hi:[1,0]\n"
    "v_pk_mul_f32 v[90:91], v[90:91], v[86:87] op_sel_hi:[1,0]\n"
    "v_pk_mul_f32 v[120:121], v[120:121], v[86:87] op_sel_hi:[1,0]\n"
    "v_pk_mul_f32 v[122:123], v[122:123], v[86:87] op_sel_hi:[1,0]\n"
    "v_cvt_pk_bf16_f32 v88, v88, v120\n"
    "v_cvt_pk_bf16_f32 v89, v89, v121\n"
    "v_cvt_pk_bf16_f32 v90, v90, v122\n"
    "v_cvt_pk_bf16_f32 v91, v91, v123\n"
    "v_pk_mul_f32 v[92:93], v[92:93], v[86:87] op_sel_hi:[1,0]\n"
    "v_pk_mul_f32 v[94:95], v[94:95], v[86:87] op_sel_hi:[1,0]\n"
    "v_pk_mul_f32 v[124:125], v[124:125], v[86:87] op_sel_hi:[1,0]\n"
    "v_pk_mul_f32 v[126:127], v[126:127], v[86:87] op_sel_hi:[1,0]\n"
    "v_cvt_pk_bf16_f32 v92, v92, v124\n"
    "v_cvt_pk_bf16_f32 v93, v93, v125\n"
    "v_cvt_pk_bf16_f32 v94, v94, v126\n"
    "v_cvt_pk_bf16_f32 v95, v95, v127\n"
    "v_pk_mul_f32 v[112:113], v[112:113], v[86:87] op_sel_hi:[1,0]\n"
    "v_pk_mul_f32 v[114:115], v[114:115], v[86:87] op_sel_hi:[1,0]\n"
    "v_pk_mul_f32 v[128:129], v[128:129], v[86:87] op_sel_hi:[1,0]\n"
    "v_pk_mul_f32 v[130:131], v[130:131], v[86:87] op_sel_hi:[1,0]\n"
    "v_cvt_pk_bf16_f32 v112, v112, v128\n"
    "v_cvt_pk_bf16_f32 v113, v113, v129\n"
    "v_cvt_pk_bf16_f32 v114, v114, v130\n"
    "v_cvt_pk_bf16_f32 v115, v115, v131\n"
    "v_pk_mul_f32 v[116:117], v[116:117], v[86:87] op_sel_hi:[1,0]\n"
    "v_pk_mul_f32 v[118:119], v[118:119], v[86:87] op_sel_hi:[1,0]\n"
    "v_pk_mul_f32 v[132:133], v[132:133], v[86:87] op_sel_hi:[1,0]\n"
    "v_pk_mul_f32 v[134:135], v[134:135], v[86:87] op_sel_hi:[1,0]\n"
    "v_cvt_pk_bf16_f32 v116, v116, v132\n"
    "v_cvt_pk_bf16_f32 v117, v117, v133\n"
    "v_cvt_pk_bf16_f32 v118, v118, v134\n"
    "v_cvt_pk_bf16_f32 v119, v119, v135\n"
    "ds_read_b128 v[32:35], v2\n"
    "ds_read_b128 v[36:39], v2 offset:1024\n"
    "ds_read_b128 v[40:43], v2 offset:2048\n"
    "ds_read_b128 v[44:47], v2 offset:3072\n"
    "ds_read_b128 v[48:51], v2 offset:4096\n"
    "ds_read_b128 v[52:55], v2 offset:5120\n"
    "ds_read_b128 v[56:59], v2 offset:6144\n"
    "ds_read_b128 v[60:63], v2 offset:7168\n"
    "ds_read_b128 v[64:67], v2 offset:8192\n"
    "ds_read_b128 v[68:71], v2 offset:9216\n"
    "ds_read_b128 v[72:75], v2 offset:10240\n"
    "ds_read_b128 v[76:79], v2 offset:11264\n"
    "ds_read_b128 v[80:83], v2 offset:12288\n"
    "ds_read_b128 v[84:87], v2 offset:13312\n"
    "v_mov_b64 v[124:125], 0\n"
    "v_mov_b64 v[126:127], 0\n"
    "v_mov_b64 v[128:129], 0\n"
    "v_mov_b64 v[130:131], 0\n"
    "s_waitcnt vmcnt(0)\n"
    "s_cmp_lg_u32 s59, 0\n"
    "s_cbranch_scc1 .Lkn12_%=\n"
    "s_waitcnt lgkmcnt(0)\n"
    "s_add_u32 s73, s48, 1\n"
    "s_and_b32 s51, s73, 32\n"
    "s_and_b32 s73, s73, 31\n"
    "s_lshl3_add_u32 s73, s51, s73\n"
    "s_lshl2_add_u32 s73, s73, s92\n"
    "s_mov_b32 s53, s73\n"
    "s_mov_b32 s56, 0\n"
    "v_mov_b32 v134, s53\n"
    ".Lpoll13_%=:\n"
    "global_load_dword v132, v134, %1 sc1\n"
    "global_load_dword v133, v134, %1 offset:128 sc1\n"
    "s_waitcnt vmcnt(0)\n"
    "v_and_b32 v133, v132, v133\n"
    "s_nop 0\n"
    "v_readfirstlane_b32 s73, v133\n"
    "s_cmp_ge_u32 s73, 1\n"
    "s_cbranch_scc1 .Lpoll13_x%=\n"
    "s_add_u32 s56, s56, 1\n"
    "s_cmp_lt_u32 s56, 3\n"
    "s_cbranch_scc1 .Lpoll13_%=\n"
    "s_sleep 8\n"
    "s_cmp_lt_u32 s56, s57\n"
    "s_cbranch_scc1 .Lpoll13_%=\n"
    "s_mov_b32 s57, 0\n"
    "v_mov_b32 v135, 1\n"
    "global_store_dword v11, v135, %1 sc1\n"
    ".Lpoll13_x%=:\n"
    "buffer_load_dwordx4 v[12:15], v5, s[40:43], s60 offen sc1\n"
    "buffer_load_dwordx4 v[16:19], v5, s[40:43], s61 offen sc1\n"
    "buffer_load_dwordx4 v[20:23], v5, s[40:43], s62 offen sc1\n"
    "buffer_load_dwordx4 v[24:27], v5, s[40:43], s63 offen sc1\n"
    "buffer_load_dwordx4 v[28:31], v5, s[40:43], s64 offen sc1\n"
    "s_waitcnt vmcnt(0)\n"
    ".Lkn12_%=:\n"
    "s_add_u32 s73, s48, 2\n"
    "s_and_b32 s51, s73, 32\n"
    "s_and_b32 s73, s73, 31\n"
    "s_lshl3_add_u32 s73, s51, s73\n"
    "s_lshl2_add_u32 s73, s73, s92\n"
    "v_mov_b32 v134, s73\n"
    "global_load_dword v132, v134, %1 sc1\n"
    "global_load_dword v133, v134, %1 offset:128 sc1\n"
    "s_add_u32 s73, s48, 2\n"
    "s_and_b32 s73, s73, 63\n"
    "s_lshr_b32 s51, s73, 2\n"
    "s_xor_b32 s51, s51, s69\n"
    "s_and_b32 s51, s51, 1\n"
    "s_lshl_b32 s51, s51, 13\n"
    "s_lshl_b32 s73, s73, 5\n"
    "s_add_u32 s73, s73, %4\n"
    "s_lshl_b32 s73, s73, 14\n"
    "s_add_u32 s73, s73, s51\n"
    "s_add_u32 s60, s73, 0x5c00000\n"
    "s_add_u32 s61, s73, 0x5c01000\n"
    "s_add_u32 s62, s73, 0x7c00000\n"
    "s_add_u32 s63, s73, 0x7c01000\n"
    "s_lshl_b32 s51, %5, 12\n"
    "s_add_u32 s73, s73, s51\n"
    "s_add_u32 s64, s73, 0x3c00000\n"
    "s_sub_u32 s53, s95, 16\n"
    "s_waitcnt lgkmcnt(0)\n"
    ".Lgl14_%=:\n"
    "s_memrealtime s[50:51]\n"
    "s_waitcnt lgkmcnt(0)\n"
    "s_sub_u32 s50, s50, s53\n"
    "s_cmp_lt_i32 s50, 0\n"
    "s_cbranch_scc1 .Lgl14_%=\n"
    "s_lshr_b32 s51, s48, 2\n"
    "s_xor_b32 s51, s51, s69\n"
    "s_and_b32 s51, s51, 1\n"
    "s_lshl_b32 s51, s51, 13\n"
    "s_lshl_b32 s73, s48, 19\n"
    "s_add_u32 s73, s73, s75\n"
    "s_add_u32 s73, s73, s51\n"
    "buffer_store_dwordx4 v[88:91], v0, s[40:43], s73 offen sc1\n"
    "buffer_store_dwordx4 v[92:95], v0, s[40:43], s73 offen offset:1024 sc1\n"
    "buffer_store_dwordx4 v[112:115], v0, s[40:43], s73 offen offset:2048 sc1\n"
    "buffer_store_dwordx4 v[116:119], v0, s[40:43], s73 offen offset:3072 sc1\n"
    "s_waitcnt lgkmcnt(0)\n"
    "s_barrier\n"
    "ds_write_b128 v5, v[12:15]\n"
    "ds_write_b128 v5, v[16:19] offset:4096\n"
    "ds_write_b128 v5, v[28:31] offset:16384\n"
    "v_swap_b32 v22, v24\n"
    "v_swap_b32 v23, v25\n"
    "ds_write_b128 v5, v[20:23] offset:8192\n"
    "ds_write_b128 v5, v[24:27] offset:12288\n"
    "s_waitcnt vmcnt(0)\n"
    "v_and_b32 v133, v132, v133\n"
    "s_nop 0\n"
    "v_readfirstlane_b32 s59, v133\n"
    "s_add_u32 s73, s48, 0\n"
    "s_and_b32 s51, s73, 32\n"
    "s_and_b32 s73, s73, 31\n"
    "s_lshl3_add_u32 s73, s51, s73\n"
    "s_lshl2_add_u32 s73, s73, s93\n"
    "v_mov_b32 v134, s73\n"
    "v_mov_b32 v135, 1\n"
    "s_mov_b64 exec, 1\n"
    "global_store_dword v134, v135, %1 sc1\n"
    "s_mov_b64 exec, -1\n"
    "s_add_u32 s48, s48, 1\n"
    "s_waitcnt lgkmcnt(0)\n"
    "s_barrier\n"
    "s_branch .Lstep_%=\n"
    ".Lend_%=:\n"
    "s_cmp_eq_u32 %2, 3\n"
    "s_cbranch_scc0 .Lout_%=\n"
    "ds_read_b128 v[32:35], v0 offset:16384\n"
    "ds_read_b128 v[48:51], v0\n"
    "ds_read_b128 v[64:67], v0 offset:4096\n"
    "ds_read_b128 v[80:83], v4 offset:20480\n"
    "ds_read_b128 v[36:39], v0 offset:17408\n"
    "ds_read_b128 v[52:55], v0 offset:1024\n"
    "ds_read_b128 v[68:71], v0 offset:5120\n"
    "ds_read_b128 v[84:87], v4 offset:20544\n"
    "ds_read_b128 v[40:43], v0 offset:18432\n"
    "ds_read_b128 v[56:59], v0 offset:2048\n"
    "ds_read_b128 v[72:75], v0 offset:6144\n"
    "ds_read_b128 v[88:91], v4 offset:20608\n"
    "ds_read_b128 v[44:47], v0 offset:19456\n"
    "ds_read_b128 v[60:63], v0 offset:3072\n"
    "ds_read_b128 v[76:79], v0 offset:7168\n"
    "ds_read_b128 v[92:95], v4 offset:20672\n"
    "s_waitcnt lgkmcnt(14)\n"
    "v_mfma_f32_16x16x32_bf16 v[96:99], v[48:51], v[32:35], 0\n"
    "s_waitcnt lgkmcnt(13)\n"
    "v_mfma_f32_16x16x32_bf16 v[100:103], v[64:67], v[32:35], 0\n"
    "s_waitcnt lgkmcnt(12)\n"
    "v_mfma_f32_16x16x32_bf16 v[104:107], v[80:83], v[32:35], 0\n"
    "s_waitcnt lgkmcnt(10)\n"
    "v_mfma_f32_16x16x32_bf16 v[96:99], v[52:55], v[36:39], v[96:99]\n"
    "s_waitcnt lgkmcnt(9)\n"
    "v_mfma_f32_16x16x32_bf16 v[100:103], v[68:71], v[36:39], v[100:103]\n"
    "s_waitcnt lgkmcnt(8)\n"
    "v_mfma_f32_16x16x32_bf16 v[104:107], v[84:87], v[36:39], v[104:107]\n"
    "s_waitcnt lgkmcnt(6)\n"
    "v_mfma_f32_16x16x32_bf16 v[96:99], v[56:59], v[40:43], v[96:99]\n"
    "s_waitcnt lgkmcnt(5)\n"
    "v_mfma_f32_16x16x32_bf16 v[100:103], v[72:75], v[40:43], v[100:103]\n"
    "s_waitcnt lgkmcnt(4)\n"
    "v_mfma_f32_16x16x32_bf16 v[104:107], v[88:91], v[40:43], v[104:107]\n"
    "s_waitcnt lgkmcnt(2)\n"
    "v_mfma_f32_16x16x32_bf16 v[96:99], v[60:63], v[44:47], v[96:99]\n"
    "s_waitcnt lgkmcnt(1)\n"
    "v_mfma_f32_16x16x32_bf16 v[100:103], v[76:79], v[44:47], v[100:103]\n"
    "s_waitcnt lgkmcnt(0)\n"
    "v_mfma_f32_16x16x32_bf16 v[104:107], v[92:95], v[44:47], v[104:107]\n"
    "ds_read_b128 v[32:35], v0 offset:8192\n"
    "ds_read_b128 v[36:39], v0 offset:12288\n"
    "ds_read_b128 v[40:43], v0 offset:9216\n"
    "ds_read_b128 v[44:47], v0 offset:13312\n"
    "ds_read_b128 v[48:51], v0 offset:10240\n"
    "ds_read_b128 v[52:55], v0 offset:14336\n"
    "ds_read_b128 v[56:59], v0 offset:11264\n"
    "ds_read_b128 v[60:63], v0 offset:15360\n"
    "ds_read_b128 v[64:67], v0 offset:22784\n"
    "ds_read_b128 v[68:71], v0 offset:23808\n"
    "ds_read_b128 v[72:75], v0 offset:24832\n"
    "ds_read_b128 v[76:79], v0 offset:25856\n"
    "ds_read_b128 v[80:83], v11 offset:31888\n"
    "ds_read_b64 v[84:85], v11 offset:31904\n"
    "v_mov_b32 v86, 0xd01502f9\n"
    "v_cndmask_b32 v96, v86, v96, s[76:77]\n"
    "v_cndmask_b32 v97, v86, v97, s[78:79]\n"
    "v_cndmask_b32 v98, v86, v98, s[80:81]\n"
    "v_cndmask_b32 v99, v86, v99, s[82:83]\n"
    "v_cndmask_b32 v100, v86, v100, s[84:85]\n"
    "v_cndmask_b32 v101, v86, v101, s[86:87]\n"
    "v_cndmask_b32 v102, v86, v102, s[88:89]\n"
    "v_cndmask_b32 v103, v86, v103, s[90:91]\n"
    "v_maximum3_f32 v87, v104, v105, v106\n"
    "v_maximum3_f32 v87, v87, v107, v96\n"
    "v_maximum3_f32 v87, v87, v97, v98\n"
    "v_maximum3_f32 v87, v87, v99, v100\n"
    "v_maximum3_f32 v87, v87, v101, v102\n"
    "v_maximum3_f32 v87, v87, v103, v103\n"
    "v_mov_b32 v86, v87\n"
    "s_nop 1\n"
    "v_permlane16_swap_b32 v86, v87\n"
    "v_maximum3_f32 v87, v87, v86, v86\n"
    "v_mov_b32 v86, v87\n"
    "s_nop 1\n"
    "v_permlane32_swap_b32 v86, v87\n"
    "v_maximum3_f32 v87, v87, v86, v86\n"
    "v_mul_f32 v87, 0xbe0293ee, v87\n"
    "v_fmamk_f32 v96, v96, 0x3e0293ee, v87\n"
    "v_fmamk_f32 v97, v97, 0x3e0293ee, v87\n"
    "v_fmamk_f32 v98, v98, 0x3e0293ee, v87\n"
    "v_fmamk_f32 v99, v99, 0x3e0293ee, v87\n"
    "v_fmamk_f32 v100, v100, 0x3e0293ee, v87\n"
    "v_fmamk_f32 v101, v101, 0x3e0293ee, v87\n"
    "v_fmamk_f32 v102, v102, 0x3e0293ee, v87\n"
    "v_fmamk_f32 v103, v103, 0x3e0293ee, v87\n"
    "v_fmamk_f32 v104, v104, 0x3e0293ee, v87\n"
    "v_fmamk_f32 v105, v105, 0x3e0293ee, v87\n"
    "v_fmamk_f32 v106, v106, 0x3e0293ee, v87\n"
    "v_fmamk_f32 v107, v107, 0x3e0293ee, v87\n"
    "v_exp_f32 v96, v96\n"
    "v_exp_f32 v97, v97\n"
    "v_exp_f32 v98, v98\n"
    "v_exp_f32 v99, v99\n"
    "v_exp_f32 v100, v100\n"
    "v_exp_f32 v101, v101\n"
    "v_exp_f32 v102, v102\n"
    "v_exp_f32 v103, v103\n"
    "v_exp_f32 v104, v104\n"
    "v_exp_f32 v105, v105\n"
    "v_exp_f32 v106, v106\n"
    "v_exp_f32 v107, v107\n"
    "v_cvt_pk_bf16_f32 v96, v96, v97\n"
    "v_cvt_pk_bf16_f32 v97, v98, v99\n"
    "v_cvt_pk_bf16_f32 v98, v100, v101\n"
    "v_cvt_pk_bf16_f32 v99, v102, v103\n"
    "v_cvt_pk_bf16_f32 v104, v104, v105\n"
    "v_cvt_pk_bf16_f32 v105, v106, v107\n"
    "s_waitcnt lgkmcnt(1)\n"
    "v_mfma_f32_16x16x32_bf16 v[108:111], v[80:83], v[96:99], 0\n"
    "v_mfma_f32_16x16x32_bf16 v[88:91], v[32:35], v[96:99], 0\n"
    "v_mfma_f32_16x16x32_bf16 v[120:123], v[36:39], v[96:99], 0\n"
    "v_mfma_f32_16x16x32_bf16 v[92:95], v[40:43], v[96:99], 0\n"
    "s_waitcnt lgkmcnt(0)\n"
    "v_mfma_f32_16x16x16_bf16 v[108:111], v[84:85], v[104:105], v[108:111]\n"
    "v_mfma_f32_16x16x32_bf16 v[124:127], v[44:47], v[96:99], 0\n"
    "v_mfma_f32_16x16x32_bf16 v[112:115], v[48:51], v[96:99], 0\n"
    "v_mfma_f32_16x16x32_bf16 v[128:131], v[52:55], v[96:99], 0\n"
    "v_mfma_f32_16x16x32_bf16 v[116:119], v[56:59], v[96:99], 0\n"
    "v_mfma_f32_16x16x32_bf16 v[132:135], v[60:63], v[96:99], 0\n"
    "v_rcp_f32 v86, v108\n"
    "v_mfma_f32_16x16x16_bf16 v[88:91], v[64:65], v[104:105], v[88:91]\n"
    "v_mfma_f32_16x16x16_bf16 v[120:123], v[66:67], v[104:105], v[120:123]\n"
    "v_mfma_f32_16x16x16_bf16 v[92:95], v[68:69], v[104:105], v[92:95]\n"
    "v_mfma_f32_16x16x16_bf16 v[124:127], v[70:71], v[104:105], v[124:127]\n"
    "v_mfma_f32_16x16x16_bf16 v[112:115], v[72:73], v[104:105], v[112:115]\n"
    "v_mfma_f32_16x16x16_bf16 v[128:131], v[74:75], v[104:105], v[128:131]\n"
    "v_mfma_f32_16x16x16_bf16 v[116:119], v[76:77], v[104:105], v[116:119]\n"
    "v_mfma_f32_16x16x16_bf16 v[132:135], v[78:79], v[104:105], v[132:135]\n"
    "v_pk_mul_f32 v[88:89], v[88:89], v[86:87] op_sel_hi:[1,0]\n"
    "v_pk_mul_f32 v[90:91], v[90:91], v[86:87] op_sel_hi:[1,0]\n"
    "v_pk_mul_f32 v[120:121], v[120:121], v[86:87] op_sel_hi:[1,0]\n"
    "v_pk_mul_f32 v[122:123], v[122:123], v[86:87] op_sel_hi:[1,0]\n"
    "v_cvt_pk_bf16_f32 v88, v88, v120\n"
    "v_cvt_pk_bf16_f32 v89, v89, v121\n"
    "v_cvt_pk_bf16_f32 v90, v90, v122\n"
    "v_cvt_pk_bf16_f32 v91, v91, v123\n"
    "v_pk_mul_f32 v[92:93], v[92:93], v[86:87] op_sel_hi:[1,0]\n"
    "v_pk_mul_f32 v[94:95], v[94:95], v[86:87] op_sel_hi:[1,0]\n"
    "v_pk_mul_f32 v[124:125], v[124:125], v[86:87] op_sel_hi:[1,0]\n"
    "v_pk_mul_f32 v[126:127], v[126:127], v[86:87] op_sel_hi:[1,0]\n"
    "v_cvt_pk_bf16_f32 v92, v92, v124\n"
    "v_cvt_pk_bf16_f32 v93, v93, v125\n"
    "v_cvt_pk_bf16_f32 v94, v94, v126\n"
    "v_cvt_pk_bf16_f32 v95, v95, v127\n"
    "v_pk_mul_f32 v[112:113], v[112:113], v[86:87] op_sel_hi:[1,0]\n"
    "v_pk_mul_f32 v[114:115], v[114:115], v[86:87] op_sel_hi:[1,0]\n"
    "v_pk_mul_f32 v[128:129], v[128:129], v[86:87] op_sel_hi:[1,0]\n"
    "v_pk_mul_f32 v[130:131], v[130:131], v[86:87] op_sel_hi:[1,0]\n"
    "v_cvt_pk_bf16_f32 v112, v112, v128\n"
    "v_cvt_pk_bf16_f32 v113, v113, v129\n"
    "v_cvt_pk_bf16_f32 v114, v114, v130\n"
    "v_cvt_pk_bf16_f32 v115, v115, v131\n"
    "v_pk_mul_f32 v[116:117], v[116:117], v[86:87] op_sel_hi:[1,0]\n"
    "v_pk_mul_f32 v[118:119], v[118:119], v[86:87] op_sel_hi:[1,0]\n"
    "v_pk_mul_f32 v[132:133], v[132:133], v[86:87] op_sel_hi:[1,0]\n"
    "v_pk_mul_f32 v[134:135], v[134:135], v[86:87] op_sel_hi:[1,0]\n"
    "v_cvt_pk_bf16_f32 v116, v116, v132\n"
    "v_cvt_pk_bf16_f32 v117, v117, v133\n"
    "v_cvt_pk_bf16_f32 v118, v118, v134\n"
    "v_cvt_pk_bf16_f32 v119, v119, v135\n"
    "s_lshr_b32 s51, s48, 2\n"
    "s_xor_b32 s51, s51, s69\n"
    "s_and_b32 s51, s51, 1\n"
    "s_lshl_b32 s51, s51, 13\n"
    "s_lshl_b32 s73, s48, 19\n"
    "s_add_u32 s73, s73, s75\n"
    "s_add_u32 s73, s73, s51\n"
    "buffer_store_dwordx4 v[88:91], v0, s[40:43], s73 offen sc1\n"
    "buffer_store_dwordx4 v[92:95], v0, s[40:43], s73 offen offset:1024 sc1\n"
    "buffer_store_dwordx4 v[112:115], v0, s[40:43], s73 offen offset:2048 sc1\n"
    "buffer_store_dwordx4 v[116:119], v0, s[40:43], s73 offen offset:3072 sc1\n"
    "s_waitcnt vmcnt(0)\n"
    "s_add_u32 s73, s48, 0\n"
    "s_and_b32 s51, s73, 32\n"
    "s_and_b32 s73, s73, 31\n"
    "s_lshl3_add_u32 s73, s51, s73\n"
    "s_lshl2_add_u32 s73, s73, s93\n"
    "v_mov_b32 v134, s73\n"
    "v_mov_b32 v135, 1\n"
    "s_mov_b64 exec, 1\n"
    "global_store_dword v134, v135, %1 sc1\n"
    "s_mov_b64 exec, -1\n"
    ".Lout_%=:\n"
    "s_waitcnt vmcnt(0)\n"
    "s_endpgm\n"
    ".p2align 6\n"
    ".Lrole1_%=:\n"
    "s_mov_b64 s[40:41], %0\n"
    "s_and_b32 s41, s41, 0xffff\n"
    "s_mov_b32 s42, 0xbc00000\n"
    "s_mov_b32 s43, 0x20000\n"
    "s_mov_b64 s[92:93], %0\n"
    "s_and_b32 s93, s93, 0xffff\n"
    "s_mov_b32 s94, 0xbc00000\n"
    "s_mov_b32 s95, 0x20000\n"
    "s_mov_b32 s53, 131072\n"
    "s_mov_b64 s[56:57], 0\n"
    "v_mbcnt_lo_u32_b32 v7, -1, 0\n"
    "v_mbcnt_hi_u32_b32 v7, -1, v7\n"
    "s_lshl_b32 s50, %2, 8\n"
    "s_add_u32 s50, s50, 147456\n"
    "v_lshl_add_u32 v7, v7, 2, s50\n"
    "ds_read_b32 v1, v7\n"
    "ds_read_b32 v2, v7 offset:1024\n"
    "ds_read_b32 v3, v7 offset:2048\n"
    "ds_read_b32 v5, v7 offset:3072\n"
    "ds_read_b32 v6, v7 offset:4096\n"
    "ds_read_b32 v48, v7 offset:5120\n"
    "ds_read_b32 v49, v7 offset:6144\n"
    "ds_read_b32 v50, v7 offset:7168\n"
    "s_waitcnt lgkmcnt(0)\n"
    "s_barrier\n"
    "s_lshl_b32 s54, %2, 10\n"
    "s_add_u32 s88, s40, 32768\n"
    "s_mov_b32 s89, s41\n"
    "s_cmp_lt_u32 %2, 2\n"
    "s_cselect_b32 s96, s42, 0\n"
    "s_mov_b32 s91, s43\n"
    "s_mov_b32 s86, 0x3f803f80\n"
    "s_mov_b32 s87, 0x3f803f80\n"
    "v_mov_b64 v[76:77], s[86:87]\n"
    "v_mov_b64 v[78:79], s[86:87]\n"
    "s_mov_b64 s[44:45], %8\n"
    "s_and_b32 s45, s45, 0xffff\n"
    "s_mov_b32 s46, 0x480000\n"
    "s_mov_b32 s47, 0x20000\n"
    "s_add_u32 m0, s54, 0\n"
    "s_add_u32 s55, %6, 0\n"
    "buffer_load_dwordx4 v48, s[44:47], s55 offen lds\n"
    "s_add_u32 m0, s54, 4096\n"
    "s_add_u32 s55, %6, 1048576\n"
    "buffer_load_dwordx4 v48, s[44:47], s55 offen lds\n"
    "s_add_u32 m0, s54, 8192\n"
    "s_add_u32 s55, %6, 2097152\n"
    "buffer_load_dwordx4 v48, s[44:47], s55 offen lds\n"
    "s_add_u32 m0, s54, 12288\n"
    "s_add_u32 s55, %6, 3145728\n"
    "buffer_load_dwordx4 v48, s[44:47], s55 offen lds\n"
    "s_add_u32 m0, s54, 16384\n"
    "s_add_u32 s55, %6, 32768\n"
    "buffer_load_dwordx4 v48, s[44:47], s55 offen lds\n"
    "s_add_u32 m0, s54, 20480\n"
    "s_add_u32 s55, %6, 1081344\n"
    "buffer_load_dwordx4 v48, s[44:47], s55 offen lds\n"
    "s_add_u32 m0, s54, 24576\n"
    "s_add_u32 s55, %6, 2129920\n"
    "buffer_load_dwordx4 v48, s[44:47], s55 offen lds\n"
    "s_add_u32 m0, s54, 28672\n"
    "s_add_u32 s55, %6, 3178496\n"
    "buffer_load_dwordx4 v48, s[44:47], s55 offen lds\n"
    "s_add_u32 m0, s54, 32768\n"
    "s_add_u32 s55, %6, 262144\n"
    "buffer_load_dwordx4 v48, s[44:47], s55 offen lds\n"
    "s_add_u32 m0, s54, 36864\n"
    "s_add_u32 s55, %6, 1310720\n"
    "buffer_load_dwordx4 v48, s[44:47], s55 offen lds\n"
    "s_add_u32 m0, s54, 40960\n"
    "s_add_u32 s55, %6, 2359296\n"
    "buffer_load_dwordx4 v48, s[44:47], s55 offen lds\n"
    "s_add_u32 m0, s54, 45056\n"
    "s_add_u32 s55, %6, 3407872\n"
    "buffer_load_dwordx4 v48, s[44:47], s55 offen lds\n"
    "s_add_u32 m0, s54, 49152\n"
    "s_add_u32 s55, %6, 294912\n"
    "buffer_load_dwordx4 v48, s[44:47], s55 offen lds\n"
    "s_add_u32 m0, s54, 53248\n"
    "s_add_u32 s55, %6, 1343488\n"
    "buffer_load_dwordx4 v48, s[44:47], s55 offen lds\n"
    "s_add_u32 m0, s54, 57344\n"
    "s_add_u32 s55, %6, 2392064\n"
    "buffer_load_dwordx4 v48, s[44:47], s55 offen lds\n"
    "s_add_u32 m0, s54, 61440\n"
    "s_add_u32 s55, %6, 3440640\n"
    "buffer_load_dwordx4 v48, s[44:47], s55 offen lds\n"
    "s_add_u32 m0, s54, 65536\n"
    "s_add_u32 s55, %6, 524288\n"
    "buffer_load_dwordx4 v48, s[44:47], s55 offen lds\n"
    "s_add_u32 m0, s54, 69632\n"
    "s_add_u32 s55, %6, 1572864\n"
    "buffer_load_dwordx4 v48, s[44:47], s55 offen lds\n"
    "s_add_u32 m0, s54, 73728\n"
    "s_add_u32 s55, %6, 2621440\n"
    "buffer_load_dwordx4 v48, s[44:47], s55 offen lds\n"
    "s_add_u32 m0, s54, 77824\n"
    "s_add_u32 s55, %6, 3670016\n"
    "buffer_load_dwordx4 v48, s[44:47], s55 offen lds\n"
    "s_add_u32 m0, s54, 81920\n"
    "s_add_u32 s55, %6, 557056\n"
    "buffer_load_dwordx4 v48, s[44:47], s55 offen lds\n"
    "s_add_u32 m0, s54, 86016\n"
    "s_add_u32 s55, %6, 1605632\n"
    "buffer_load_dwordx4 v48, s[44:47], s55 offen lds\n"
    "s_add_u32 m0, s54, 90112\n"
    "s_add_u32 s55, %6, 2654208\n"
    "buffer_load_dwordx4 v48, s[44:47], s55 offen lds\n"
    "s_add_u32 m0, s54, 94208\n"
    "s_add_u32 s55, %6, 3702784\n"
    "buffer_load_dwordx4 v48, s[44:47], s55 offen lds\n"
    "s_add_u32 m0, s54, 98304\n"
    "s_add_u32 s55, %6, 786432\n"
    "buffer_load_dwordx4 v48, s[44:47], s55 offen lds\n"
    "s_add_u32 m0, s54, 102400\n"
    "s_add_u32 s55, %6, 1835008\n"
    "buffer_load_dwordx4 v48, s[44:47], s55 offen lds\n"
    "s_add_u32 m0, s54, 106496\n"
    "s_add_u32 s55, %6, 2883584\n"
    "buffer_load_dwordx4 v48, s[44:47], s55 offen lds\n"
    "s_add_u32 m0, s54, 110592\n"
    "s_add_u32 s55, %6, 3932160\n"
    "buffer_load_dwordx4 v48, s[44:47], s55 offen lds\n"
    "s_add_u32 m0, s54, 114688\n"
    "s_add_u32 s55, %6, 819200\n"
    "buffer_load_dwordx4 v48, s[44:47], s55 offen lds\n"
    "s_add_u32 m0, s54, 118784\n"
    "s_add_u32 s55, %6, 1867776\n"
    "buffer_load_dwordx4 v48, s[44:47], s55 offen lds\n"
    "s_add_u32 m0, s54, 122880\n"
    "s_add_u32 s55, %6, 2916352\n"
    "buffer_load_dwordx4 v48, s[44:47], s55 offen lds\n"
    "s_add_u32 m0, s54, 126976\n"
    "s_add_u32 s55, %6, 3964928\n"
    "buffer_load_dwordx4 v48, s[44:47], s55 offen lds\n"
    "s_add_u32 m0, s54, 131072\n"
    "s_add_u32 s55, %6, 4194304\n"
    "buffer_load_dwordx4 v48, s[44:47], s55 offen lds\n"
    "s_add_u32 m0, s54, 135168\n"
    "s_add_u32 s55, %6, 4227072\n"
    "buffer_load_dwordx4 v48, s[44:47], s55 offen lds\n"
    "s_add_u32 m0, s54, 139264\n"
    "s_add_u32 s55, %6, 4456448\n"
    "buffer_load_dwordx4 v48, s[44:47], s55 offen lds\n"
    "s_add_u32 m0, s54, 143360\n"
    "s_add_u32 s55, %6, 4489216\n"
    "buffer_load_dwordx4 v48, s[44:47], s55 offen lds\n"
    "s_xor_b32 s50, %2, 0\n"
    "s_lshl_b32 s50, s50, 12\n"
    "v_add_u32 v32, s50, v49\n"
    "v_add_u32 v33, s50, v50\n"
    "s_xor_b32 s50, %2, 1\n"
    "s_lshl_b32 s50, s50, 12\n"
    "v_add_u32 v34, s50, v49\n"
    "v_add_u32 v35, s50, v50\n"
    "s_xor_b32 s50, %2, 2\n"
    "s_lshl_b32 s50, s50, 12\n"
    "v_add_u32 v36, s50, v49\n"
    "v_add_u32 v37, s50, v50\n"
    "s_xor_b32 s50, %2, 3\n"
    "s_lshl_b32 s50, s50, 12\n"
    "v_add_u32 v38, s50, v49\n"
    "v_add_u32 v39, s50, v50\n"
    "s_waitcnt vmcnt(32) lgkmcnt(0)\n"
    "s_barrier\n"
    "ds_read_b64_tr_b16 a[0:1], v32\n"
    "ds_read_b64_tr_b16 a[4:5], v33\n"
    "ds_read_b64_tr_b16 a[32:33], v34\n"
    "ds_read_b64_tr_b16 a[36:37], v35\n"
    "ds_read_b64_tr_b16 a[64:65], v36\n"
    "ds_read_b64_tr_b16 a[68:69], v37\n"
    "ds_read_b64_tr_b16 a[96:97], v38\n"
    "ds_read_b64_tr_b16 a[100:101], v39\n"
    "s_waitcnt vmcnt(28) lgkmcnt(0)\n"
    "s_barrier\n"
    "s_mov_b64 s[44:45], %9\n"
    "s_and_b32 s45, s45, 0xffff\n"
    "s_mov_b32 s46, 0x480000\n"
    "s_mov_b32 s47, 0x20000\n"
    "s_add_u32 m0, s54, 0\n"
    "s_add_u32 s55, %6, 0\n"
    "buffer_load_dwordx4 v48, s[44:47], s55 offen lds\n"
    "s_add_u32 m0, s54, 4096\n"
    "s_add_u32 s55, %6, 1048576\n"
    "buffer_load_dwordx4 v48, s[44:47], s55 offen lds\n"
    "s_add_u32 m0, s54, 8192\n"
    "s_add_u32 s55, %6, 2097152\n"
    "buffer_load_dwordx4 v48, s[44:47], s55 offen lds\n"
    "s_add_u32 m0, s54, 12288\n"
    "s_add_u32 s55, %6, 3145728\n"
    "buffer_load_dwordx4 v48, s[44:47], s55 offen lds\n"
    "ds_read_b64_tr_b16 a[2:3], v32 offset:16384\n"
    "ds_read_b64_tr_b16 a[6:7], v33 offset:16384\n"
    "ds_read_b64_tr_b16 a[34:35], v34 offset:16384\n"
    "ds_read_b64_tr_b16 a[38:39], v35 offset:16384\n"
    "ds_read_b64_tr_b16 a[66:67], v36 offset:16384\n"
    "ds_read_b64_tr_b16 a[70:71], v37 offset:16384\n"
    "ds_read_b64_tr_b16 a[98:99], v38 offset:16384\n"
    "ds_read_b64_tr_b16 a[102:103], v39 offset:16384\n"
    "s_waitcnt vmcnt(28) lgkmcnt(0)\n"
    "s_barrier\n"
    "s_add_u32 m0, s54, 16384\n"
    "s_add_u32 s55, %6, 32768\n"
    "buffer_load_dwordx4 v48, s[44:47], s55 offen lds\n"
    "s_add_u32 m0, s54, 20480\n"
    "s_add_u32 s55, %6, 1081344\n"
    "buffer_load_dwordx4 v48, s[44:47], s55 offen lds\n"
    "s_add_u32 m0, s54, 24576\n"
    "s_add_u32 s55, %6, 2129920\n"
    "buffer_load_dwordx4 v48, s[44:47], s55 offen lds\n"
    "s_add_u32 m0, s54, 28672\n"
    "s_add_u32 s55, %6, 3178496\n"
    "buffer_load_dwordx4 v48, s[44:47], s55 offen lds\n"
    "v_mfma_f32_16x16x32_bf16 v[8:11], v[76:79], a[0:3], 0\n"
    "v_mfma_f32_16x16x32_bf16 v[12:15], v[76:79], a[4:7], 0\n"
    "v_mfma_f32_16x16x32_bf16 v[8:11], v[76:79], a[32:35], v[8:11]\n"
    "v_mfma_f32_16x16x32_bf16 v[12:15], v[76:79], a[36:39], v[12:15]\n"
    "v_mfma_f32_16x16x32_bf16 v[8:11], v[76:79], a[64:67], v[8:11]\n"
    "v_mfma_f32_16x16x32_bf16 v[12:15], v[76:79], a[68:71], v[12:15]\n"
    "v_mfma_f32_16x16x32_bf16 v[8:11], v[76:79], a[96:99], v[8:11]\n"
    "v_mfma_f32_16x16x32_bf16 v[12:15], v[76:79], a[100:103], v[12:15]\n"
    "ds_read_b64_tr_b16 a[8:9], v32 offset:32768\n"
    "ds_read_b64_tr_b16 a[12:13], v33 offset:32768\n"
    "ds_read_b64_tr_b16 a[40:41], v34 offset:32768\n"
    "ds_read_b64_tr_b16 a[44:45], v35 offset:32768\n"
    "ds_read_b64_tr_b16 a[72:73], v36 offset:32768\n"
    "ds_read_b64_tr_b16 a[76:77], v37 offset:32768\n"
    "ds_read_b64_tr_b16 a[104:105], v38 offset:32768\n"
    "ds_read_b64_tr_b16 a[108:109], v39 offset:32768\n"
    "s_waitcnt vmcnt(28) lgkmcnt(0)\n"
    "s_barrier\n"
    "s_add_u32 m0, s54, 32768\n"
    "s_add_u32 s55, %6, 262144\n"
    "buffer_load_dwordx4 v48, s[44:47], s55 offen lds\n"
    "s_add_u32 m0, s54, 36864\n"
    "s_add_u32 s55, %6, 1310720\n"
    "buffer_load_dwordx4 v48, s[44:47], s55 offen lds\n"
    "s_add_u32 m0, s54, 40960\n"
    "s_add_u32 s55, %6, 2359296\n"
    "buffer_load_dwordx4 v48, s[44:47], s55 offen lds\n"
    "s_add_u32 m0, s54, 45056\n"
    "s_add_u32 s55, %6, 3407872\n"
    "buffer_load_dwordx4 v48, s[44:47], s55 offen lds\n"
    "ds_read_b64_tr_b16 a[10:11], v32 offset:49152\n"
    "ds_read_b64_tr_b16 a[14:15], v33 offset:49152\n"
    "ds_read_b64_tr_b16 a[42:43], v34 offset:49152\n"
    "ds_read_b64_tr_b16 a[46:47], v35 offset:49152\n"
    "ds_read_b64_tr_b16 a[74:75], v36 offset:49152\n"
    "ds_read_b64_tr_b16 a[78:79], v37 offset:49152\n"
    "ds_read_b64_tr_b16 a[106:107], v38 offset:49152\n"
    "ds_read_b64_tr_b16 a[110:111], v39 offset:49152\n"
    "s_waitcnt vmcnt(28) lgkmcnt(0)\n"
    "s_barrier\n"
    "s_add_u32 m0, s54, 49152\n"
    "s_add_u32 s55, %6, 294912\n"
    "buffer_load_dwordx4 v48, s[44:47], s55 offen lds\n"
    "s_add_u32 m0, s54, 53248\n"
    "s_add_u32 s55, %6, 1343488\n"
    "buffer_load_dwordx4 v48, s[44:47], s55 offen lds\n"
    "s_add_u32 m0, s54, 57344\n"
    "s_add_u32 s55, %6, 2392064\n"
    "buffer_load_dwordx4 v48, s[44:47], s55 offen lds\n"
    "s_add_u32 m0, s54, 61440\n"
    "s_add_u32 s55, %6, 3440640\n"
    "buffer_load_dwordx4 v48, s[44:47], s55 offen lds\n"
    "v_mfma_f32_16x16x32_bf16 v[8:11], v[76:79], a[8:11], v[8:11]\n"
    "v_mfma_f32_16x16x32_bf16 v[12:15], v[76:79], a[12:15], v[12:15]\n"
    "v_mfma_f32_16x16x32_bf16 v[8:11], v[76:79], a[40:43], v[8:11]\n"
    "v_mfma_f32_16x16x32_bf16 v[12:15], v[76:79], a[44:47], v[12:15]\n"
    "v_mfma_f32_16x16x32_bf16 v[8:11], v[76:79], a[72:75], v[8:11]\n"
    "v_mfma_f32_16x16x32_bf16 v[12:15], v[76:79], a[76:79], v[12:15]\n"
    "v_mfma_f32_16x16x32_bf16 v[8:11], v[76:79], a[104:107], v[8:11]\n"
    "v_mfma_f32_16x16x32_bf16 v[12:15], v[76:79], a[108:111], v[12:15]\n"
    "v_add_u32 v71, 0x10000, v32\n"
    "ds_read_b64_tr_b16 a[16:17], v71\n"
    "v_add_u32 v70, 0x10000, v33\n"
    "ds_read_b64_tr_b16 a[20:21], v70\n"
    "v_add_u32 v69, 0x10000, v34\n"
    "ds_read_b64_tr_b16 a[48:49], v69\n"
    "v_add_u32 v68, 0x10000, v35\n"
    "ds_read_b64_tr_b16 a[52:53], v68\n"
    "v_add_u32 v67, 0x10000, v36\n"
    "ds_read_b64_tr_b16 a[80:81], v67\n"
    "v_add_u32 v66, 0x10000, v37\n"
    "ds_read_b64_tr_b16 a[84:85], v66\n"
    "v_add_u32 v65, 0x10000, v38\n"
    "ds_read_b64_tr_b16 a[112:113], v65\n"
    "v_add_u32 v64, 0x10000, v39\n"
    "ds_read_b64_tr_b16 a[116:117], v64\n"
    "s_waitcnt vmcnt(28) lgkmcnt(0)\n"
    "s_barrier\n"
    "s_add_u32 m0, s54, 65536\n"
    "s_add_u32 s55, %6, 524288\n"
    "buffer_load_dwordx4 v48, s[44:47], s55 offen lds\n"
    "s_add_u32 m0, s54, 69632\n"
    "s_add_u32 s55, %6, 1572864\n"
    "buffer_load_dwordx4 v48, s[44:47], s55 offen lds\n"
    "s_add_u32 m0, s54, 73728\n"
    "s_add_u32 s55, %6, 2621440\n"
    "buffer_load_dwordx4 v48, s[44:47], s55 offen lds\n"
    "s_add_u32 m0, s54, 77824\n"
    "s_add_u32 s55, %6, 3670016\n"
    "buffer_load_dwordx4 v48, s[44:47], s55 offen lds\n"
    "ds_read_b64_tr_b16 a[18:19], v71 offset:16384\n"
    "ds_read_b64_tr_b16 a[22:23], v70 offset:16384\n"
    "ds_read_b64_tr_b16 a[50:51], v69 offset:16384\n"
    "ds_read_b64_tr_b16 a[54:55], v68 offset:16384\n"
    "ds_read_b64_tr_b16 a[82:83], v67 offset:16384\n"
    "ds_read_b64_tr_b16 a[86:87], v66 offset:16384\n"
    "ds_read_b64_tr_b16 a[114:115], v65 offset:16384\n"
    "ds_read_b64_tr_b16 a[118:119], v64 offset:16384\n"
    "s_waitcnt vmcnt(28) lgkmcnt(0)\n"
    "s_barrier\n"
    "s_add_u32 m0, s54, 81920\n"
    "s_add_u32 s55, %6, 557056\n"
    "buffer_load_dwordx4 v48, s[44:47], s55 offen lds\n"
    "s_add_u32 m0, s54, 86016\n"
    "s_add_u32 s55, %6, 1605632\n"
    "buffer_load_dwordx4 v48, s[44:47], s55 offen lds\n"
    "s_add_u32 m0, s54, 90112\n"
    "s_add_u32 s55, %6, 2654208\n"
    "buffer_load_dwordx4 v48, s[44:47], s55 offen lds\n"
    "s_add_u32 m0, s54, 94208\n"
    "s_add_u32 s55, %6, 3702784\n"
    "buffer_load_dwordx4 v48, s[44:47], s55 offen lds\n"
    "v_mfma_f32_16x16x32_bf16 v[8:11], v[76:79], a[16:19], v[8:11]\n"
    "v_mfma_f32_16x16x32_bf16 v[12:15], v[76:79], a[20:23], v[12:15]\n"
    "v_mfma_f32_16x16x32_bf16 v[8:11], v[76:79], a[48:51], v[8:11]\n"
    "v_mfma_f32_16x16x32_bf16 v[12:15], v[76:79], a[52:55], v[12:15]\n"
    "v_mfma_f32_16x16x32_bf16 v[8:11], v[76:79], a[80:83], v[8:11]\n"
    "v_mfma_f32_16x16x32_bf16 v[12:15], v[76:79], a[84:87], v[12:15]\n"
    "v_mfma_f32_16x16x32_bf16 v[8:11], v[76:79], a[112:115], v[8:11]\n"
    "v_mfma_f32_16x16x32_bf16 v[12:15], v[76:79], a[116:119], v[12:15]\n"
    "ds_read_b64_tr_b16 a[24:25], v71 offset:32768\n"
    "ds_read_b64_tr_b16 a[28:29], v70 offset:32768\n"
    "ds_read_b64_tr_b16 a[56:57], v69 offset:32768\n"
    "ds_read_b64_tr_b16 a[60:61], v68 offset:32768\n"
    "ds_read_b64_tr_b16 a[88:89], v67 offset:32768\n"
    "ds_read_b64_tr_b16 a[92:93], v66 offset:32768\n"
    "ds_read_b64_tr_b16 a[120:121], v65 offset:32768\n"
    "ds_read_b64_tr_b16 a[124:125], v64 offset:32768\n"
    "s_waitcnt vmcnt(28) lgkmcnt(0)\n"
    "s_barrier\n"
    "s_add_u32 m0, s54, 98304\n"
    "s_add_u32 s55, %6, 786432\n"
    "buffer_load_dwordx4 v48, s[44:47], s55 offen lds\n"
    "s_add_u32 m0, s54, 102400\n"
    "s_add_u32 s55, %6, 1835008\n"
    "buffer_load_dwordx4 v48, s[44:47], s55 offen lds\n"
    "s_add_u32 m0, s54, 106496\n"
    "s_add_u32 s55, %6, 2883584\n"
    "buffer_load_dwordx4 v48, s[44:47], s55 offen lds\n"
    "s_add_u32 m0, s54, 110592\n"
    "s_add_u32 s55, %6, 3932160\n"
    "buffer_load_dwordx4 v48, s[44:47], s55 offen lds\n"
    "ds_read_b64_tr_b16 a[26:27], v71 offset:49152\n"
    "ds_read_b64_tr_b16 a[30:31], v70 offset:49152\n"
    "ds_read_b64_tr_b16 a[58:59], v69 offset:49152\n"
    "ds_read_b64_tr_b16 a[62:63], v68 offset:49152\n"
    "ds_read_b64_tr_b16 a[90:91], v67 offset:49152\n"
    "ds_read_b64_tr_b16 a[94:95], v66 offset:49152\n"
    "ds_read_b64_tr_b16 a[122:123], v65 offset:49152\n"
    "ds_read_b64_tr_b16 a[126:127], v64 offset:49152\n"
    "s_waitcnt vmcnt(28) lgkmcnt(0)\n"
    "s_barrier\n"
    "s_add_u32 m0, s54, 114688\n"
    "s_add_u32 s55, %6, 819200\n"
    "buffer_load_dwordx4 v48, s[44:47], s55 offen lds\n"
    "s_add_u32 m0, s54, 118784\n"
    "s_add_u32 s55, %6, 1867776\n"
    "buffer_load_dwordx4 v48, s[44:47], s55 offen lds\n"
    "s_add_u32 m0, s54, 122880\n"
    "s_add_u32 s55, %6, 2916352\n"
    "buffer_load_dwordx4 v48, s[44:47], s55 offen lds\n"
    "s_add_u32 m0, s54, 126976\n"
    "s_add_u32 s55, %6, 3964928\n"
    "buffer_load_dwordx4 v48, s[44:47], s55 offen lds\n"
    "v_mfma_f32_16x16x32_bf16 v[8:11], v[76:79], a[24:27], v[8:11]\n"
    "v_mfma_f32_16x16x32_bf16 v[12:15], v[76:79], a[28:31], v[12:15]\n"
    "v_mfma_f32_16x16x32_bf16 v[8:11], v[76:79], a[56:59], v[8:11]\n"
    "v_mfma_f32_16x16x32_bf16 v[12:15], v[76:79], a[60:63], v[12:15]\n"
    "v_mfma_f32_16x16x32_bf16 v[8:11], v[76:79], a[88:91], v[8:11]\n"
    "v_mfma_f32_16x16x32_bf16 v[12:15], v[76:79], a[92:95], v[12:15]\n"
    "v_mfma_f32_16x16x32_bf16 v[8:11], v[76:79], a[120:123], v[8:11]\n"
    "v_mfma_f32_16x16x32_bf16 v[12:15], v[76:79], a[124:127], v[12:15]\n"
    "v_add_u32 v63, 0x20000, v49\n"
    "ds_read_b64_tr_b16 a[128:129], v63\n"
    "v_add_u32 v62, 0x20000, v50\n"
    "ds_read_b64_tr_b16 a[132:133], v62\n"
    "ds_read_b64_tr_b16 a[130:131], v63 offset:4096\n"
    "ds_read_b64_tr_b16 a[134:135], v62 offset:4096\n"
    "ds_read_b64_tr_b16 a[136:137], v63 offset:8192\n"
    "ds_read_b64_tr_b16 a[140:141], v62 offset:8192\n"
    "ds_read_b64_tr_b16 a[138:139], v63 offset:12288\n"
    "ds_read_b64_tr_b16 a[142:143], v62 offset:12288\n"
    "s_waitcnt vmcnt(28) lgkmcnt(0)\n"
    "s_barrier\n"
    "s_add_u32 m0, s54, 131072\n"
    "s_add_u32 s55, %6, 4194304\n"
    "buffer_load_dwordx4 v48, s[44:47], s55 offen lds\n"
    "s_add_u32 m0, s54, 135168\n"
    "s_add_u32 s55, %6, 4227072\n"
    "buffer_load_dwordx4 v48, s[44:47], s55 offen lds\n"
    "s_add_u32 m0, s54, 139264\n"
    "s_add_u32 s55, %6, 4456448\n"
    "buffer_load_dwordx4 v48, s[44:47], s55 offen lds\n"
    "s_add_u32 m0, s54, 143360\n"
    "s_add_u32 s55, %6, 4489216\n"
    "buffer_load_dwordx4 v48, s[44:47], s55 offen lds\n"
    "v_mfma_f32_16x16x32_bf16 v[8:11], v[76:79], a[128:131], v[8:11]\n"
    "v_mfma_f32_16x16x32_bf16 v[12:15], v[76:79], a[132:135], v[12:15]\n"
    "v_mfma_f32_16x16x32_bf16 v[8:11], v[76:79], a[136:139], v[8:11]\n"
    "v_mfma_f32_16x16x32_bf16 v[12:15], v[76:79], a[140:143], v[12:15]\n"
    "ds_read_b64_tr_b16 a[144:145], v32\n"
    "ds_read_b64_tr_b16 a[148:149], v33\n"
    "ds_read_b64_tr_b16 a[176:177], v34\n"
    "ds_read_b64_tr_b16 a[180:181], v35\n"
    "ds_read_b64_tr_b16 a[208:209], v36\n"
    "ds_read_b64_tr_b16 a[212:213], v37\n"
    "ds_read_b64_tr_b16 a[240:241], v38\n"
    "ds_read_b64_tr_b16 a[244:245], v39\n"
    "s_waitcnt vmcnt(28) lgkmcnt(0)\n"
    "s_barrier\n"
    "s_mov_b64 s[44:45], %10\n"
    "s_and_b32 s45, s45, 0xffff\n"
    "s_mov_b32 s46, 0x480000\n"
    "s_mov_b32 s47, 0x20000\n"
    "s_add_u32 m0, s54, 0\n"
    "s_add_u32 s55, %6, 0\n"
    "buffer_load_dwordx4 v48, s[44:47], s55 offen lds\n"
    "s_add_u32 m0, s54, 4096\n"
    "s_add_u32 s55, %6, 1048576\n"
    "buffer_load_dwordx4 v48, s[44:47], s55 offen lds\n"
    "s_add_u32 m0, s54, 8192\n"
    "s_add_u32 s55, %6, 2097152\n"
    "buffer_load_dwordx4 v48, s[44:47], s55 offen lds\n"
    "s_add_u32 m0, s54, 12288\n"
    "s_add_u32 s55, %6, 3145728\n"
    "buffer_load_dwordx4 v48, s[44:47], s55 offen lds\n"
    "ds_read_b64_tr_b16 a[146:147], v32 offset:16384\n"
    "ds_read_b64_tr_b16 a[150:151], v33 offset:16384\n"
    "ds_read_b64_tr_b16 a[178:179], v34 offset:16384\n"
    "ds_read_b64_tr_b16 a[182:183], v35 offset:16384\n"
    "ds_read_b64_tr_b16 a[210:211], v36 offset:16384\n"
    "ds_read_b64_tr_b16 a[214:215], v37 offset:16384\n"
    "ds_read_b64_tr_b16 a[242:243], v38 offset:16384\n"
    "ds_read_b64_tr_b16 a[246:247], v39 offset:16384\n"
    "s_waitcnt vmcnt(28) lgkmcnt(0)\n"
    "s_barrier\n"
    "s_add_u32 m0, s54, 16384\n"
    "s_add_u32 s55, %6, 32768\n"
    "buffer_load_dwordx4 v48, s[44:47], s55 offen lds\n"
    "s_add_u32 m0, s54, 20480\n"
    "s_add_u32 s55, %6, 1081344\n"
    "buffer_load_dwordx4 v48, s[44:47], s55 offen lds\n"
    "s_add_u32 m0, s54, 24576\n"
    "s_add_u32 s55, %6, 2129920\n"
    "buffer_load_dwordx4 v48, s[44:47], s55 offen lds\n"
    "s_add_u32 m0, s54, 28672\n"
    "s_add_u32 s55, %6, 3178496\n"
    "buffer_load_dwordx4 v48, s[44:47], s55 offen lds\n"
    "v_mfma_f32_16x16x32_bf16 v[16:19], v[76:79], a[144:147], 0\n"
    "v_mfma_f32_16x16x32_bf16 v[20:23], v[76:79], a[148:151], 0\n"
    "v_mfma_f32_16x16x32_bf16 v[16:19], v[76:79], a[176:179], v[16:19]\n"
    "v_mfma_f32_16x16x32_bf16 v[20:23], v[76:79], a[180:183], v[20:23]\n"
    "v_mfma_f32_16x16x32_bf16 v[16:19], v[76:79], a[208:211], v[16:19]\n"
    "v_mfma_f32_16x16x32_bf16 v[20:23], v[76:79], a[212:215], v[20:23]\n"
    "v_mfma_f32_16x16x32_bf16 v[16:19], v[76:79], a[240:243], v[16:19]\n"
    "v_mfma_f32_16x16x32_bf16 v[20:23], v[76:79], a[244:247], v[20:23]\n"
    "ds_read_b64_tr_b16 a[152:153], v32 offset:32768\n"
    "ds_read_b64_tr_b16 a[156:157], v33 offset:32768\n"
    "ds_read_b64_tr_b16 a[184:185], v34 offset:32768\n"
    "ds_read_b64_tr_b16 a[188:189], v35 offset:32768\n"
    "ds_read_b64_tr_b16 a[216:217], v36 offset:32768\n"
    "ds_read_b64_tr_b16 a[220:221], v37 offset:32768\n"
    "ds_read_b64_tr_b16 a[248:249], v38 offset:32768\n"
    "ds_read_b64_tr_b16 a[252:253], v39 offset:32768\n"
    "s_waitcnt vmcnt(28) lgkmcnt(0)\n"
    "s_barrier\n"
    "s_add_u32 m0, s54, 32768\n"
    "s_add_u32 s55, %6, 262144\n"
    "buffer_load_dwordx4 v48, s[44:47], s55 offen lds\n"
    "s_add_u32 m0, s54, 36864\n"
    "s_add_u32 s55, %6, 1310720\n"
    "buffer_load_dwordx4 v48, s[44:47], s55 offen lds\n"
    "s_add_u32 m0, s54, 40960\n"
    "s_add_u32 s55, %6, 2359296\n"
    "buffer_load_dwordx4 v48, s[44:47], s55 offen lds\n"
    "s_add_u32 m0, s54, 45056\n"
    "s_add_u32 s55, %6, 3407872\n"
    "buffer_load_dwordx4 v48, s[44:47], s55 offen lds\n"
    "ds_read_b64_tr_b16 a[154:155], v32 offset:49152\n"
    "ds_read_b64_tr_b16 a[158:159], v33 offset:49152\n"
    "ds_read_b64_tr_b16 a[186:187], v34 offset:49152\n"
    "ds_read_b64_tr_b16 a[190:191], v35 offset:49152\n"
    "ds_read_b64_tr_b16 a[218:219], v36 offset:49152\n"
    "ds_read_b64_tr_b16 a[222:223], v37 offset:49152\n"
    "ds_read_b64_tr_b16 a[250:251], v38 offset:49152\n"
    "ds_read_b64_tr_b16 a[254:255], v39 offset:49152\n"
    "s_waitcnt vmcnt(28) lgkmcnt(0)\n"
    "s_barrier\n"
    "s_add_u32 m0, s54, 49152\n"
    "s_add_u32 s55, %6, 294912\n"
    "buffer_load_dwordx4 v48, s[44:47], s55 offen lds\n"
    "s_add_u32 m0, s54, 53248\n"
    "s_add_u32 s55, %6, 1343488\n"
    "buffer_load_dwordx4 v48, s[44:47], s55 offen lds\n"
    "s_add_u32 m0, s54, 57344\n"
    "s_add_u32 s55, %6, 2392064\n"
    "buffer_load_dwordx4 v48, s[44:47], s55 offen lds\n"
    "s_add_u32 m0, s54, 61440\n"
    "s_add_u32 s55, %6, 3440640\n"
    "buffer_load_dwordx4 v48, s[44:47], s55 offen lds\n"
    "v_mfma_f32_16x16x32_bf16 v[16:19], v[76:79], a[152:155], v[16:19]\n"
    "v_mfma_f32_16x16x32_bf16 v[20:23], v[76:79], a[156:159], v[20:23]\n"
    "v_mfma_f32_16x16x32_bf16 v[16:19], v[76:79], a[184:187], v[16:19]\n"
    "v_mfma_f32_16x16x32_bf16 v[20:23], v[76:79], a[188:191], v[20:23]\n"
    "v_mfma_f32_16x16x32_bf16 v[16:19], v[76:79], a[216:219], v[16:19]\n"
    "v_mfma_f32_16x16x32_bf16 v[20:23], v[76:79], a[220:223], v[20:23]\n"
    "v_mfma_f32_16x16x32_bf16 v[16:19], v[76:79], a[248:251], v[16:19]\n"
    "v_mfma_f32_16x16x32_bf16 v[20:23], v[76:79], a[252:255], v[20:23]\n"
    "ds_read_b64_tr_b16 a[160:161], v71\n"
    "ds_read_b64_tr_b16 a[164:165], v70\n"
    "ds_read_b64_tr_b16 a[192:193], v69\n"
    "ds_read_b64_tr_b16 a[196:197], v68\n"
    "ds_read_b64_tr_b16 a[224:225], v67\n"
    "ds_read_b64_tr_b16 a[228:229], v66\n"
    "ds_read_b64_tr_b16 v[84:85], v65\n"
    "ds_read_b64_tr_b16 v[88:89], v64\n"
    "s_waitcnt vmcnt(28) lgkmcnt(0)\n"
    "s_barrier\n"
    "s_add_u32 m0, s54, 65536\n"
    "s_add_u32 s55, %6, 524288\n"
    "buffer_load_dwordx4 v48, s[44:47], s55 offen lds\n"
    "s_add_u32 m0, s54, 69632\n"
    "s_add_u32 s55, %6, 1572864\n"
    "buffer_load_dwordx4 v48, s[44:47], s55 offen lds\n"
    "s_add_u32 m0, s54, 73728\n"
    "s_add_u32 s55, %6, 2621440\n"
    "buffer_load_dwordx4 v48, s[44:47], s55 offen lds\n"
    "s_add_u32 m0, s54, 77824\n"
    "s_add_u32 s55, %6, 3670016\n"
    "buffer_load_dwordx4 v48, s[44:47], s55 offen lds\n"
    "ds_read_b64_tr_b16 a[162:163], v71 offset:16384\n"
    "ds_read_b64_tr_b16 a[166:167], v70 offset:16384\n"
    "ds_read_b64_tr_b16 a[194:195], v69 offset:16384\n"
    "ds_read_b64_tr_b16 a[198:199], v68 offset:16384\n"
    "ds_read_b64_tr_b16 a[226:227], v67 offset:16384\n"
    "ds_read_b64_tr_b16 a[230:231], v66 offset:16384\n"
    "ds_read_b64_tr_b16 v[86:87], v65 offset:16384\n"
    "ds_read_b64_tr_b16 v[90:91], v64 offset:16384\n"
    "s_waitcnt vmcnt(28) lgkmcnt(0)\n"
    "s_barrier\n"
    "s_add_u32 m0, s54, 81920\n"
    "s_add_u32 s55, %6, 557056\n"
    "buffer_load_dwordx4 v48, s[44:47], s55 offen lds\n"
    "s_add_u32 m0, s54, 86016\n"
    "s_add_u32 s55, %6, 1605632\n"
    "buffer_load_dwordx4 v48, s[44:47], s55 offen lds\n"
    "s_add_u32 m0, s54, 90112\n"
    "s_add_u32 s55, %6, 2654208\n"
    "buffer_load_dwordx4 v48, s[44:47], s55 offen lds\n"
    "s_add_u32 m0, s54, 94208\n"
    "s_add_u32 s55, %6, 3702784\n"
    "buffer_load_dwordx4 v48, s[44:47], s55 offen lds\n"
    "v_mfma_f32_16x16x32_bf16 v[16:19], v[76:79], a[160:163], v[16:19]\n"
    "v_mfma_f32_16x16x32_bf16 v[20:23], v[76:79], a[164:167], v[20:23]\n"
    "v_mfma_f32_16x16x32_bf16 v[16:19], v[76:79], a[192:195], v[16:19]\n"
    "v_mfma_f32_16x16x32_bf16 v[20:23], v[76:79], a[196:199], v[20:23]\n"
    "v_mfma_f32_16x16x32_bf16 v[16:19], v[76:79], a[224:227], v[16:19]\n"
    "v_mfma_f32_16x16x32_bf16 v[20:23], v[76:79], a[228:231], v[20:23]\n"
    "v_mfma_f32_16x16x32_bf16 v[16:19], v[76:79], v[84:87], v[16:19]\n"
    "v_mfma_f32_16x16x32_bf16 v[20:23], v[76:79], v[88:91], v[20:23]\n"
    "ds_read_b64_tr_b16 a[168:169], v71 offset:32768\n"
    "ds_read_b64_tr_b16 a[172:173], v70 offset:32768\n"
    "ds_read_b64_tr_b16 a[200:201], v69 offset:32768\n"
    "ds_read_b64_tr_b16 a[204:205], v68 offset:32768\n"
    "ds_read_b64_tr_b16 a[232:233], v67 offset:32768\n"
    "ds_read_b64_tr_b16 a[236:237], v66 offset:32768\n"
    "ds_read_b64_tr_b16 v[92:93], v65 offset:32768\n"
    "ds_read_b64_tr_b16 v[96:97], v64 offset:32768\n"
    "s_waitcnt vmcnt(28) lgkmcnt(0)\n"
    "s_barrier\n"
    "s_add_u32 m0, s54, 98304\n"
    "s_add_u32 s55, %6, 786432\n"
    "buffer_load_dwordx4 v48, s[44:47], s55 offen lds\n"
    "s_add_u32 m0, s54, 102400\n"
    "s_add_u32 s55, %6, 1835008\n"
    "buffer_load_dwordx4 v48, s[44:47], s55 offen lds\n"
    "s_add_u32 m0, s54, 106496\n"
    "s_add_u32 s55, %6, 2883584\n"
    "buffer_load_dwordx4 v48, s[44:47], s55 offen lds\n"
    "s_add_u32 m0, s54, 110592\n"
    "s_add_u32 s55, %6, 3932160\n"
    "buffer_load_dwordx4 v48, s[44:47], s55 offen lds\n"
    "ds_read_b64_tr_b16 a[170:171], v71 offset:49152\n"
    "ds_read_b64_tr_b16 a[174:175], v70 offset:49152\n"
    "ds_read_b64_tr_b16 a[202:203], v69 offset:49152\n"
    "ds_read_b64_tr_b16 a[206:207], v68 offset:49152\n"
    "ds_read_b64_tr_b16 a[234:235], v67 offset:49152\n"
    "ds_read_b64_tr_b16 a[238:239], v66 offset:49152\n"
    "ds_read_b64_tr_b16 v[94:95], v65 offset:49152\n"
    "ds_read_b64_tr_b16 v[98:99], v64 offset:49152\n"
    "s_waitcnt vmcnt(28) lgkmcnt(0)\n"
    "s_barrier\n"
    "s_add_u32 m0, s54, 114688\n"
    "s_add_u32 s55, %6, 819200\n"
    "buffer_load_dwordx4 v48, s[44:47], s55 offen lds\n"
    "s_add_u32 m0, s54, 118784\n"
    "s_add_u32 s55, %6, 1867776\n"
    "buffer_load_dwordx4 v48, s[44:47], s55 offen lds\n"
    "s_add_u32 m0, s54, 122880\n"
    "s_add_u32 s55, %6, 2916352\n"
    "buffer_load_dwordx4 v48, s[44:47], s55 offen lds\n"
    "s_add_u32 m0, s54, 126976\n"
    "s_add_u32 s55, %6, 3964928\n"
    "buffer_load_dwordx4 v48, s[44:47], s55 offen lds\n"
    "v_mfma_f32_16x16x32_bf16 v[16:19], v[76:79], a[168:171], v[16:19]\n"
    "v_mfma_f32_16x16x32_bf16 v[20:23], v[76:79], a[172:175], v[20:23]\n"
    "v_mfma_f32_16x16x32_bf16 v[16:19], v[76:79], a[200:203], v[16:19]\n"
    "v_mfma_f32_16x16x32_bf16 v[20:23], v[76:79], a[204:207], v[20:23]\n"
    "v_mfma_f32_16x16x32_bf16 v[16:19], v[76:79], a[232:235], v[16:19]\n"
    "v_mfma_f32_16x16x32_bf16 v[20:23], v[76:79], a[236:239], v[20:23]\n"
    "v_mfma_f32_16x16x32_bf16 v[16:19], v[76:79], v[92:95], v[16:19]\n"
    "v_mfma_f32_16x16x32_bf16 v[20:23], v[76:79], v[96:99], v[20:23]\n"
    "ds_read_b64_tr_b16 v[100:101], v63\n"
    "ds_read_b64_tr_b16 v[104:105], v62\n"
    "ds_read_b64_tr_b16 v[102:103], v63 offset:4096\n"
    "ds_read_b64_tr_b16 v[106:107], v62 offset:4096\n"
    "ds_read_b64_tr_b16 v[108:109], v63 offset:8192\n"
    "ds_read_b64_tr_b16 v[112:113], v62 offset:8192\n"
    "ds_read_b64_tr_b16 v[110:111], v63 offset:12288\n"
    "ds_read_b64_tr_b16 v[114:115], v62 offset:12288\n"
    "s_waitcnt vmcnt(28) lgkmcnt(0)\n"
    "s_barrier\n"
    "s_add_u32 m0, s54, 131072\n"
    "s_add_u32 s55, %6, 4194304\n"
    "buffer_load_dwordx4 v48, s[44:47], s55 offen lds\n"
    "s_add_u32 m0, s54, 135168\n"
    "s_add_u32 s55, %6, 4227072\n"
    "buffer_load_dwordx4 v48, s[44:47], s55 offen lds\n"
    "s_add_u32 m0, s54, 139264\n"
    "s_add_u32 s55, %6, 4456448\n"
    "buffer_load_dwordx4 v48, s[44:47], s55 offen lds\n"
    "s_add_u32 m0, s54, 143360\n"
    "s_add_u32 s55, %6, 4489216\n"
    "buffer_load_dwordx4 v48, s[44:47], s55 offen lds\n"
    "v_mfma_f32_16x16x32_bf16 v[16:19], v[76:79], v[100:103], v[16:19]\n"
    "v_mfma_f32_16x16x32_bf16 v[20:23], v[76:79], v[104:107], v[20:23]\n"
    "v_mfma_f32_16x16x32_bf16 v[16:19], v[76:79], v[108:111], v[16:19]\n"
    "v_mfma_f32_16x16x32_bf16 v[20:23], v[76:79], v[112:115], v[20:23]\n"
    "ds_read_b64_tr_b16 v[116:117], v32\n"
    "ds_read_b64_tr_b16 v[120:121], v33\n"
    "ds_read_b64_tr_b16 v[148:149], v34\n"
    "ds_read_b64_tr_b16 v[152:153], v35\n"
    "ds_read_b64_tr_b16 v[180:181], v36\n"
    "ds_read_b64_tr_b16 v[184:185], v37\n"
    "ds_read_b64_tr_b16 v[212:213], v38\n"
    "ds_read_b64_tr_b16 v[216:217], v39\n"
    "s_waitcnt vmcnt(28) lgkmcnt(0)\n"
    "s_barrier\n"
    "ds_read_b64_tr_b16 v[118:119], v32 offset:16384\n"
    "ds_read_b64_tr_b16 v[122:123], v33 offset:16384\n"
    "ds_read_b64_tr_b16 v[150:151], v34 offset:16384\n"
    "ds_read_b64_tr_b16 v[154:155], v35 offset:16384\n"
    "ds_read_b64_tr_b16 v[182:183], v36 offset:16384\n"
    "ds_read_b64_tr_b16 v[186:187], v37 offset:16384\n"
    "ds_read_b64_tr_b16 v[214:215], v38 offset:16384\n"
    "ds_read_b64_tr_b16 v[218:219], v39 offset:16384\n"
    "s_waitcnt vmcnt(24) lgkmcnt(0)\n"
    "s_barrier\n"
    "v_mfma_f32_16x16x32_bf16 v[24:27], v[76:79], v[116:119], 0\n"
    "v_mfma_f32_16x16x32_bf16 v[28:31], v[76:79], v[120:123], 0\n"
    "v_mfma_f32_16x16x32_bf16 v[24:27], v[76:79], v[148:151], v[24:27]\n"
    "v_mfma_f32_16x16x32_bf16 v[28:31], v[76:79], v[152:155], v[28:31]\n"
    "v_mfma_f32_16x16x32_bf16 v[24:27], v[76:79], v[180:183], v[24:27]\n"
    "v_mfma_f32_16x16x32_bf16 v[28:31], v[76:79], v[184:187], v[28:31]\n"
    "v_mfma_f32_16x16x32_bf16 v[24:27], v[76:79], v[212:215], v[24:27]\n"
    "v_mfma_f32_16x16x32_bf16 v[28:31], v[76:79], v[216:219], v[28:31]\n"
    "ds_read_b64_tr_b16 v[124:125], v32 offset:32768\n"
    "ds_read_b64_tr_b16 v[128:129], v33 offset:32768\n"
    "ds_read_b64_tr_b16 v[156:157], v34 offset:32768\n"
    "ds_read_b64_tr_b16 v[160:161], v35 offset:32768\n"
    "ds_read_b64_tr_b16 v[188:189], v36 offset:32768\n"
    "ds_read_b64_tr_b16 v[192:193], v37 offset:32768\n"
    "ds_read_b64_tr_b16 v[220:221], v38 offset:32768\n"
    "ds_read_b64_tr_b16 v[224:225], v39 offset:32768\n"
    "s_waitcnt vmcnt(20) lgkmcnt(0)\n"
    "s_barrier\n"
    "ds_read_b64_tr_b16 v[126:127], v32 offset:49152\n"
    "ds_read_b64_tr_b16 v[130:131], v33 offset:49152\n"
    "ds_read_b64_tr_b16 v[158:159], v34 offset:49152\n"
    "ds_read_b64_tr_b16 v[162:163], v35 offset:49152\n"
    "ds_read_b64_tr_b16 v[190:191], v36 offset:49152\n"
    "ds_read_b64_tr_b16 v[194:195], v37 offset:49152\n"
    "ds_read_b64_tr_b16 v[222:223], v38 offset:49152\n"
    "ds_read_b64_tr_b16 v[226:227], v39 offset:49152\n"
    "s_waitcnt vmcnt(16) lgkmcnt(0)\n"
    "s_barrier\n"
    "v_mfma_f32_16x16x32_bf16 v[24:27], v[76:79], v[124:127], v[24:27]\n"
    "v_mfma_f32_16x16x32_bf16 v[28:31], v[76:79], v[128:131], v[28:31]\n"
    "v_mfma_f32_16x16x32_bf16 v[24:27], v[76:79], v[156:159], v[24:27]\n"
    "v_mfma_f32_16x16x32_bf16 v[28:31], v[76:79], v[160:163], v[28:31]\n"
    "v_mfma_f32_16x16x32_bf16 v[24:27], v[76:79], v[188:191], v[24:27]\n"
    "v_mfma_f32_16x16x32_bf16 v[28:31], v[76:79], v[192:195], v[28:31]\n"
    "v_mfma_f32_16x16x32_bf16 v[24:27], v[76:79], v[220:223], v[24:27]\n"
    "v_mfma_f32_16x16x32_bf16 v[28:31], v[76:79], v[224:227], v[28:31]\n"
    "ds_read_b64_tr_b16 v[132:133], v71\n"
    "ds_read_b64_tr_b16 v[136:137], v70\n"
    "ds_read_b64_tr_b16 v[164:165], v69\n"
    "ds_read_b64_tr_b16 v[168:169], v68\n"
    "ds_read_b64_tr_b16 v[196:197], v67\n"
    "ds_read_b64_tr_b16 v[200:201], v66\n"
    "ds_read_b64_tr_b16 v[228:229], v65\n"
    "ds_read_b64_tr_b16 v[232:233], v64\n"
    "s_waitcnt vmcnt(12) lgkmcnt(0)\n"
    "s_barrier\n"
    "ds_read_b64_tr_b16 v[134:135], v71 offset:16384\n"
    "ds_read_b64_tr_b16 v[138:139], v70 offset:16384\n"
    "ds_read_b64_tr_b16 v[166:167], v69 offset:16384\n"
    "ds_read_b64_tr_b16 v[170:171], v68 offset:16384\n"
    "ds_read_b64_tr_b16 v[198:199], v67 offset:16384\n"
    "ds_read_b64_tr_b16 v[202:203], v66 offset:16384\n"
    "ds_read_b64_tr_b16 v[230:231], v65 offset:16384\n"
    "ds_read_b64_tr_b16 v[234:235], v64 offset:16384\n"
    "s_waitcnt vmcnt(8) lgkmcnt(0)\n"
    "s_barrier\n"
    "v_mfma_f32_16x16x32_bf16 v[24:27], v[76:79], v[132:135], v[24:27]\n"
    "v_mfma_f32_16x16x32_bf16 v[28:31], v[76:79], v[136:139], v[28:31]\n"
    "v_mfma_f32_16x16x32_bf16 v[24:27], v[76:79], v[164:167], v[24:27]\n"
    "v_mfma_f32_16x16x32_bf16 v[28:31], v[76:79], v[168:171], v[28:31]\n"
    "v_mfma_f32_16x16x32_bf16 v[24:27], v[76:79], v[196:199], v[24:27]\n"
    "v_mfma_f32_16x16x32_bf16 v[28:31], v[76:79], v[200:203], v[28:31]\n"
    "v_mfma_f32_16x16x32_bf16 v[24:27], v[76:79], v[228:231], v[24:27]\n"
    "v_mfma_f32_16x16x32_bf16 v[28:31], v[76:79], v[232:235], v[28:31]\n"
    "ds_read_b64_tr_b16 v[140:141], v71 offset:32768\n"
    "ds_read_b64_tr_b16 v[144:145], v70 offset:32768\n"
    "ds_read_b64_tr_b16 v[172:173], v69 offset:32768\n"
    "ds_read_b64_tr_b16 v[176:177], v68 offset:32768\n"
    "ds_read_b64_tr_b16 v[204:205], v67 offset:32768\n"
    "ds_read_b64_tr_b16 v[208:209], v66 offset:32768\n"
    "ds_read_b64_tr_b16 v[236:237], v65 offset:32768\n"
    "ds_read_b64_tr_b16 v[240:241], v64 offset:32768\n"
    "s_waitcnt vmcnt(4) lgkmcnt(0)\n"
    "s_barrier\n"
    "ds_read_b64_tr_b16 v[142:143], v71 offset:49152\n"
    "ds_read_b64_tr_b16 v[146:147], v70 offset:49152\n"
    "ds_read_b64_tr_b16 v[174:175], v69 offset:49152\n"
    "ds_read_b64_tr_b16 v[178:179], v68 offset:49152\n"
    "ds_read_b64_tr_b16 v[206:207], v67 offset:49152\n"
    "ds_read_b64_tr_b16 v[210:211], v66 offset:49152\n"
    "ds_read_b64_tr_b16 v[238:239], v65 offset:49152\n"
    "ds_read_b64_tr_b16 v[242:243], v64 offset:49152\n"
    "s_waitcnt vmcnt(0) lgkmcnt(0)\n"
    "s_barrier\n"
    "v_mfma_f32_16x16x32_bf16 v[24:27], v[76:79], v[140:143], v[24:27]\n"
    "v_mfma_f32_16x16x32_bf16 v[28:31], v[76:79], v[144:147], v[28:31]\n"
    "v_mfma_f32_16x16x32_bf16 v[24:27], v[76:79], v[172:175], v[24:27]\n"
    "v_mfma_f32_16x16x32_bf16 v[28:31], v[76:79], v[176:179], v[28:31]\n"
    "v_mfma_f32_16x16x32_bf16 v[24:27], v[76:79], v[204:207], v[24:27]\n"
    "v_mfma_f32_16x16x32_bf16 v[28:31], v[76:79], v[208:211], v[28:31]\n"
    "v_mfma_f32_16x16x32_bf16 v[24:27], v[76:79], v[236:239], v[24:27]\n"
    "v_mfma_f32_16x16x32_bf16 v[28:31], v[76:79], v[240:243], v[28:31]\n"
    "ds_read_b64_tr_b16 v[244:245], v63\n"
    "ds_read_b64_tr_b16 v[248:249], v62\n"
    "ds_read_b64_tr_b16 v[246:247], v63 offset:4096\n"
    "ds_read_b64_tr_b16 v[250:251], v62 offset:4096\n"
    "ds_read_b64_tr_b16 v[252:253], v63 offset:8192\n"
    "ds_read_b64_tr_b16 v[72:73], v62 offset:8192\n"
    "ds_read_b64_tr_b16 v[254:255], v63 offset:12288\n"
    "ds_read_b64_tr_b16 v[74:75], v62 offset:12288\n"
    "s_waitcnt lgkmcnt(0)\n"
    "s_barrier\n"
    "v_mfma_f32_16x16x32_bf16 v[24:27], v[76:79], v[244:247], v[24:27]\n"
    "v_mfma_f32_16x16x32_bf16 v[28:31], v[76:79], v[248:251], v[28:31]\n"
    "v_mfma_f32_16x16x32_bf16 v[24:27], v[76:79], v[252:255], v[24:27]\n"
    "v_mfma_f32_16x16x32_bf16 v[28:31], v[76:79], v[72:75], v[28:31]\n"
    "ds_write_b128 v2, v[72:75] offset:20480\n"
    "v_cvt_pk_bf16_f32 v9, 0, v8\n"
    "v_sub_f32 v8, v8, v9\n"
    "v_cvt_pk_bf16_f32 v10, 0, v8\n"
    "v_sub_f32 v8, v8, v10\n"
    "v_cvt_pk_bf16_f32 v9, v9, v10\n"
    "v_cvt_pk_bf16_f32 v10, v8, 0\n"
    "s_mov_b64 vcc, 0xffff\n"
    "v_cndmask_b32 v32, 0, v9, vcc\n"
    "v_cndmask_b32 v33, 0, v10, vcc\n"
    "v_cvt_pk_bf16_f32 v13, 0, v12\n"
    "v_sub_f32 v12, v12, v13\n"
    "v_cvt_pk_bf16_f32 v14, 0, v12\n"
    "v_sub_f32 v12, v12, v14\n"
    "v_cvt_pk_bf16_f32 v13, v13, v14\n"
    "v_cvt_pk_bf16_f32 v14, v12, 0\n"
    "s_mov_b64 vcc, 0xffff\n"
    "v_cndmask_b32 v34, 0, v13, vcc\n"
    "v_cndmask_b32 v35, 0, v14, vcc\n"
    "ds_write_b128 v2, v[32:35] offset:8192\n"
    "v_cvt_pk_bf16_f32 v17, 0, v16\n"
    "v_sub_f32 v16, v16, v17\n"
    "v_cvt_pk_bf16_f32 v18, 0, v16\n"
    "v_sub_f32 v16, v16, v18\n"
    "v_cvt_pk_bf16_f32 v17, v17, v18\n"
    "v_cvt_pk_bf16_f32 v18, v16, 0\n"
    "s_mov_b64 vcc, 0xffff\n"
    "v_cndmask_b32 v36, 0, v17, vcc\n"
    "v_cndmask_b32 v37, 0, v18, vcc\n"
    "v_cvt_pk_bf16_f32 v21, 0, v20\n"
    "v_sub_f32 v20, v20, v21\n"
    "v_cvt_pk_bf16_f32 v22, 0, v20\n"
    "v_sub_f32 v20, v20, v22\n"
    "v_cvt_pk_bf16_f32 v21, v21, v22\n"
    "v_cvt_pk_bf16_f32 v22, v20, 0\n"
    "s_mov_b64 vcc, 0xffff\n"
    "v_cndmask_b32 v38, 0, v21, vcc\n"
    "v_cndmask_b32 v39, 0, v22, vcc\n"
    "ds_write_b128 v2, v[36:39] offset:12288\n"
    "v_cvt_pk_bf16_f32 v25, 0, v24\n"
    "v_sub_f32 v24, v24, v25\n"
    "v_cvt_pk_bf16_f32 v26, 0, v24\n"
    "v_sub_f32 v24, v24, v26\n"
    "v_cvt_pk_bf16_f32 v25, v25, v26\n"
    "v_cvt_pk_bf16_f32 v26, v24, 0\n"
    "s_mov_b64 vcc, 0xffff\n"
    "v_cndmask_b32 v40, 0, v25, vcc\n"
    "v_cndmask_b32 v41, 0, v26, vcc\n"
    "v_cvt_pk_bf16_f32 v29, 0, v28\n"
    "v_sub_f32 v28, v28, v29\n"
    "v_cvt_pk_bf16_f32 v30, 0, v28\n"
    "v_sub_f32 v28, v28, v30\n"
    "v_cvt_pk_bf16_f32 v29, v29, v30\n"
    "v_cvt_pk_bf16_f32 v30, v28, 0\n"
    "s_mov_b64 vcc, 0xffff\n"
    "v_cndmask_b32 v42, 0, v29, vcc\n"
    "v_cndmask_b32 v43, 0, v30, vcc\n"
    "ds_write_b128 v2, v[40:43] offset:16384\n"
    "v_mov_b32 v80, 64\n"
    "global_load_dword v81, v80, %1 sc1\n"
    "s_mov_b32 s52, 0\n"
    ".Lhp1_%=:\n"
    "s_waitcnt vmcnt(0)\n"
    "v_cmp_eq_u32 vcc, 0, v81\n"
    "s_cmp_eq_u64 vcc, 0\n"
    "s_cbranch_scc1 .Lhp1_x%=\n"
    "s_add_u32 s52, s52, 1\n"
    "s_sleep 2\n"
    "global_load_dword v81, v80, %1 sc1\n"
    "s_cmp_lt_u32 s52, s53\n"
    "s_cbranch_scc1 .Lhp1_%=\n"
    "s_mov_b32 s53, 0\n"
    "v_mov_b32 v82, 0\n"
    "v_mov_b32 v83, 1\n"
    "global_store_dword v82, v83, %1 sc1\n"
    "s_waitcnt vmcnt(0)\n"
    ".Lhp1_x%=:\n"
    "v_readfirstlane_b32 s50, v81\n"
    "v_mbcnt_lo_u32_b32 v80, -1, 0\n"
    "v_mbcnt_hi_u32_b32 v80, -1, v80\n"
    "v_min_u32 v4, 5, v80\n"
    "v_lshlrev_b32 v4, 7, v4\n"
    "s_lshl_b32 s51, %2, 6\n"
    "v_add_u32 v80, s51, v80\n"
    "v_min_u32 v80, 129, v80\n"
    "v_lshlrev_b32 v80, 5, v80\n"
    "v_add_u32 v7, 0x26c00, v80\n"
    "ds_read_b128 v[80:83], v7\n"
    "s_and_b32 s50, s50, 1\n"
    "s_waitcnt lgkmcnt(0)\n"
    "v_and_b32 v80, s50, v81\n"
    "v_lshlrev_b32 v80, 13, v80\n"
    "v_and_b32 v81, -2, v81\n"
    "v_xor_b32 v81, v80, v81\n"
    "v_and_b32 v80, s50, v82\n"
    "v_lshlrev_b32 v80, 13, v80\n"
    "v_and_b32 v82, -2, v82\n"
    "v_xor_b32 v82, v80, v82\n"
    "ds_write2_b32 v7, v81, v82 offset0:1 offset1:2\n"
    "s_waitcnt lgkmcnt(0)\n"
    "s_barrier\n"
    "v_mov_b32 v7, 0x26c00\n"
    "ds_read_b128 v[80:83], v7\n"
    "s_waitcnt lgkmcnt(0)\n"
    "v_readfirstlane_b32 s49, v81\n"
    "v_add_u32 v80, v80, v4\n"
    "s_branch .Lhwait_%=\n"
    ".Lhgo_%=:\n"
    "s_waitcnt vmcnt(2)\n"
    "v_mfma_f32_16x16x32_bf16 v[68:71], v[48:51], v[48:51], 0\n"
    "v_add_u32 v7, 32, v7\n"
    "ds_read_b128 v[80:83], v7\n"
    "v_mfma_f32_16x16x32_bf16 v[68:71], v[52:55], v[52:55], v[68:71]\n"
    "ds_write_b128 v1, v[48:51]\n"
    "ds_write_b128 v1, v[52:55] offset:1024\n"
    "v_mfma_f32_16x16x32_bf16 v[68:71], v[56:59], v[56:59], v[68:71]\n"
    "ds_write_b128 v1, v[56:59] offset:2048\n"
    "ds_write_b128 v1, v[60:63] offset:3072\n"
    "v_mfma_f32_16x16x32_bf16 v[68:71], v[60:63], v[60:63], v[68:71]\n"
    "v_mul_f32 v24, v24, v76\n"
    "v_mul_f32 v25, v25, v77\n"
    "v_mfma_f32_16x16x32_bf16 v[68:71], v[64:67], v[64:67], v[68:71]\n"
    "v_mul_f32 v26, v26, v78\n"
    "v_mul_f32 v27, v27, v79\n"
    "v_mfma_f32_16x16x32_bf16 v[8:11], a[0:3], v[48:51], 0\n"
    "v_mul_f32 v28, v28, v76\n"
    "v_mul_f32 v29, v29, v77\n"
    "v_mfma_f32_16x16x32_bf16 v[12:15], a[4:7], v[48:51], 0\n"
    "v_mul_f32 v30, v30, v78\n"
    "v_mul_f32 v31, v31, v79\n"
    "v_mfma_f32_16x16x32_bf16 v[16:19], a[144:147], v[48:51], 0\n"
    "v_mov_b64 v[76:77], s[86:87]\n"
    "v_mov_b64 v[78:79], s[86:87]\n"
    "v_mfma_f32_16x16x32_bf16 v[20:23], a[148:151], v[48:51], 0\n"
    "ds_write_b128 v1, v[64:67] offset:4096\n"
    "v_cvt_pk_bf16_f32 v24, v24, v25\n"
    "v_mfma_f32_16x16x32_bf16 v[72:75], v[76:79], v[48:51], 0\n"
    "v_cvt_pk_bf16_f32 v25, v26, v27\n"
    "v_cvt_pk_bf16_f32 v26, v28, v29\n"
    "v_mfma_f32_16x16x32_bf16 v[72:75], v[76:79], v[52:55], v[72:75]\n"
    "v_cvt_pk_bf16_f32 v27, v30, v31\n"
    "buffer_store_dwordx4 v[24:27], v2, s[40:43], s64 offen sc1\n"
    "v_mfma_f32_16x16x32_bf16 v[72:75], v[76:79], v[56:59], v[72:75]\n"
    "ds_write_b128 v1, v[68:71] offset:5120\n"
    "s_waitcnt lgkmcnt(6)\n"
    "v_readfirstlane_b32 s49, v81\n"
    "v_mfma_f32_16x16x32_bf16 v[72:75], v[76:79], v[60:63], v[72:75]\n"
    "v_readfirstlane_b32 s62, v82\n"
    "v_add_u32 v80, v80, v4\n"
    "v_mfma_f32_16x16x32_bf16 v[72:75], v[76:79], v[64:67], v[72:75]\n"
    ".Lhtop_%=:\n"
    "v_mfma_f32_16x16x32_bf16 v[24:27], v[48:51], v[116:119], 0\n"
    "global_load_dword v81, v80, %1 sc1\n"
    "s_add_u32 s63, s62, 0x2000000\n"
    "v_mfma_f32_16x16x32_bf16 v[28:31], v[48:51], v[120:123], 0\n"
    "s_add_u32 s64, s62, 0x4000000\n"
    "v_mfma_f32_16x16x32_bf16 v[8:11], a[8:11], v[52:55], v[8:11]\n"
    "ds_write_b128 v1, v[72:75] offset:6144\n"
    "v_xor_b32 v0, 0x2000, v1\n"
    "v_mfma_f32_16x16x32_bf16 v[12:15], a[12:15], v[52:55], v[12:15]\n"
    "v_mfma_f32_16x16x32_bf16 v[16:19], a[152:155], v[52:55], v[16:19]\n"
    "v_mfma_f32_16x16x32_bf16 v[20:23], a[156:159], v[52:55], v[20:23]\n"
    "v_mfma_f32_16x16x32_bf16 v[24:27], v[52:55], v[124:127], v[24:27]\n"
    "s_waitcnt lgkmcnt(0)\n"
    "s_barrier\n"
    "v_mfma_f32_16x16x32_bf16 v[28:31], v[52:55], v[128:131], v[28:31]\n"
    "ds_read_b128 v[32:35], v0\n"
    "ds_read_b128 v[36:39], v0 offset:1024\n"
    "v_mfma_f32_16x16x32_bf16 v[8:11], a[16:19], v[56:59], v[8:11]\n"
    "ds_read2st64_b32 v[68:69], v6 offset0:20 offset1:52\n"
    "ds_read2st64_b32 v[70:71], v6 offset0:84 offset1:116\n"
    "v_mfma_f32_16x16x32_bf16 v[12:15], a[20:23], v[56:59], v[12:15]\n"
    "ds_read2st64_b32 v[74:75], v6 offset0:88 offset1:120\n"
    "ds_read2st64_b32 v[72:73], v6 offset0:24 offset1:56\n"
    "v_mfma_f32_16x16x32_bf16 v[16:19], a[160:163], v[56:59], v[16:19]\n"
    "v_mov_b64 v[64:65], 0\n"
    "v_mfma_f32_16x16x32_bf16 v[20:23], a[164:167], v[56:59], v[20:23]\n"
    "v_mfma_f32_16x16x32_bf16 v[24:27], v[56:59], v[132:135], v[24:27]\n"
    "v_mfma_f32_16x16x32_bf16 v[28:31], v[56:59], v[136:139], v[28:31]\n"
    "ds_read_b128 v[48:51], v2 offset:8192\n"
    "ds_read_b128 v[52:55], v2 offset:12288\n"
    "v_mfma_f32_16x16x32_bf16 v[8:11], a[24:27], v[60:63], v[8:11]\n"
    "ds_read_b128 v[56:59], v2 offset:16384\n"
    "ds_read_b128 v[44:47], v0 offset:3072\n"
    "v_mfma_f32_16x16x32_bf16 v[12:15], a[28:31], v[60:63], v[12:15]\n"
    "ds_read_b128 v[40:43], v0 offset:2048\n"
    "v_mfma_f32_16x16x32_bf16 v[16:19], a[168:171], v[60:63], v[16:19]\n"
    "s_waitcnt lgkmcnt(5)\n"
    "v_add_f32 v72, v72, v73\n"
    "v_add_f32 v74, v74, v75\n"
    "v_mfma_f32_16x16x32_bf16 v[20:23], a[172:175], v[60:63], v[20:23]\n"
    "v_add_f32 v68, v68, v69\n"
    "v_add_f32 v70, v70, v71\n"
    "v_mfma_f32_16x16x32_bf16 v[24:27], v[60:63], v[140:143], v[24:27]\n"
    "v_add_f32 v72, v72, v74\n"
    "v_add_f32 v70, v68, v70\n"
    "v_mfma_f32_16x16x32_bf16 v[28:31], v[60:63], v[144:147], v[28:31]\n"
    "v_mul_f32 v72, 0xbae38e39, v72\n"
    "v_mul_f32 v70, 0x3ae38e39, v70\n"
    "v_mfma_f32_16x16x32_bf16 v[8:11], a[32:35], v[32:35], v[8:11]\n"
    "v_cvt_pk_bf16_f32 v62, v72, v72\n"
    "v_fma_f32 v70, -v72, v72, v70\n"
    "v_mfma_f32_16x16x32_bf16 v[12:15], a[36:39], v[32:35], v[12:15]\n"
    "v_cvt_pk_bf16_f32 v63, v72, v72\n"
    "v_max_f32 v70, 0, v70\n"
    "v_mfma_f32_16x16x32_bf16 v[16:19], a[176:179], v[32:35], v[16:19]\n"
    "v_cvt_pk_bf16_f32 v70, 0, v70\n"
    "v_add_f32 v70, 0x3727c5ac, v70\n"
    "v_mfma_f32_16x16x32_bf16 v[20:23], a[180:183], v[32:35], v[20:23]\n"
    "v_cvt_pk_bf16_f32 v70, 0, v70\n"
    "v_sqrt_f32 v70, v70\n"
    "v_mfma_f32_16x16x32_bf16 v[24:27], v[32:35], v[148:151], v[24:27]\n"
    "v_cvt_pk_bf16_f32 v70, 0, v70\n"
    "v_mfma_f32_16x16x32_bf16 v[28:31], v[32:35], v[152:155], v[28:31]\n"
    "v_rcp_f32 v70, v70\n"
    "v_mfma_f32_16x16x32_bf16 v[8:11], a[40:43], v[36:39], v[8:11]\n"
    "v_mov_b64 v[60:61], 0\n"
    "v_mfma_f32_16x16x32_bf16 v[12:15], a[44:47], v[36:39], v[12:15]\n"
    "ds_write_b32 v5, v70\n"
    "v_lshrrev_b32 v71, 4, v2\n"
    "v_mfma_f32_16x16x32_bf16 v[16:19], a[184:187], v[36:39], v[16:19]\n"
    "v_and_b32 v71, 48, v71\n"
    "ds_read_b128 v[76:79], v71 offset:24576\n"
    "v_mfma_f32_16x16x32_bf16 v[20:23], a[188:191], v[36:39], v[20:23]\n"
    "ds_read_b128 v[72:75], v2 offset:20480\n"
    "v_mfma_f32_16x16x32_bf16 v[24:27], v[36:39], v[156:159], v[24:27]\n"
    "v_xor_b32 v0, 0x4000, v1\n"
    "v_mfma_f32_16x16x32_bf16 v[28:31], v[36:39], v[160:163], v[28:31]\n"
    "s_waitcnt lgkmcnt(3)\n"
    "v_mfma_f32_16x16x32_bf16 v[8:11], a[48:51], v[40:43], v[8:11]\n"
    "ds_read_b128 v[36:39], v0 offset:1024\n"
    "ds_read_b128 v[32:35], v0\n"
    "v_mfma_f32_16x16x32_bf16 v[12:15], a[52:55], v[40:43], v[12:15]\n"
    "v_mfma_f32_16x16x32_bf16 v[16:19], a[192:195], v[40:43], v[16:19]\n"
    "v_mfma_f32_16x16x32_bf16 v[20:23], a[196:199], v[40:43], v[20:23]\n"
    "v_mfma_f32_16x16x32_bf16 v[24:27], v[40:43], v[164:167], v[24:27]\n"
    "v_mfma_f32_16x16x32_bf16 v[28:31], v[40:43], v[168:171], v[28:31]\n"
    "v_mfma_f32_16x16x32_bf16 v[8:11], v[48:51], v[62:65], v[8:11]\n"
    "v_mfma_f32_16x16x32_bf16 v[12:15], v[48:51], v[60:63], v[12:15]\n"
    "v_mfma_f32_16x16x32_bf16 v[16:19], v[52:55], v[62:65], v[16:19]\n"
    "s_waitcnt vmcnt(0)\n"
    "v_mov_b32 v82, 1\n"
    "v_mfma_f32_16x16x32_bf16 v[20:23], v[52:55], v[60:63], v[20:23]\n"
    "s_mov_b64 exec, s[56:57]\n"
    "ds_add_rtn_u32 v82, v7, v82 offset:16\n"
    "s_mov_b64 exec, -1\n"
    "v_mfma_f32_16x16x32_bf16 v[24:27], v[62:65], v[56:59], v[24:27]\n"
    "v_cmp_eq_u32 vcc, 0, v81\n"
    "s_cmp_eq_u64 vcc, 0\n"
    "v_mfma_f32_16x16x32_bf16 v[28:31], v[60:63], v[56:59], v[28:31]\n"
    "s_cselect_b32 s94, s42, 0\n"
    "s_cselect_b32 s90, s96, 0\n"
    "v_mfma_f32_16x16x32_bf16 v[8:11], a[56:59], v[44:47], v[8:11]\n"
    "global_load_dword v81, v80, %1 sc1\n"
    "v_mfma_f32_16x16x32_bf16 v[12:15], a[60:63], v[44:47], v[12:15]\n"
    "buffer_load_dwordx4 v[48:51], v3, s[92:95], s49 offen sc1\n"
    "v_mfma_f32_16x16x32_bf16 v[16:19], a[200:203], v[44:47], v[16:19]\n"
    "buffer_load_dwordx4 v[52:55], v3, s[92:95], s49 offen offset:1024 sc1\n"
    "v_mfma_f32_16x16x32_bf16 v[20:23], a[204:207], v[44:47], v[20:23]\n"
    "s_waitcnt lgkmcnt(0)\n"
    "v_readfirstlane_b32 s50, v82\n"
    "v_mfma_f32_16x16x32_bf16 v[24:27], v[44:47], v[172:175], v[24:27]\n"
    "buffer_load_dwordx4 v[56:59], v3, s[92:95], s49 offen offset:2048 sc1\n"
    "s_cmp_eq_u32 s50, 3\n"
    "v_mfma_f32_16x16x32_bf16 v[28:31], v[44:47], v[176:179], v[28:31]\n"
    "v_mfma_f32_16x16x32_bf16 v[8:11], a[64:67], v[32:35], v[8:11]\n"
    "ds_read_b128 v[44:47], v0 offset:3072\n"
    "ds_read_b128 v[40:43], v0 offset:2048\n"
    "v_mfma_f32_16x16x32_bf16 v[12:15], a[68:71], v[32:35], v[12:15]\n"
    "s_cselect_b64 exec, 1, 0\n"
    "global_store_dword v83, v82, %1 sc1\n"
    "s_mov_b64 exec, -1\n"
    "v_mfma_f32_16x16x32_bf16 v[16:19], a[208:211], v[32:35], v[16:19]\n"
    "buffer_load_dwordx4 v[60:63], v3, s[92:95], s49 offen offset:3072 sc1\n"
    "v_mfma_f32_16x16x32_bf16 v[20:23], a[212:215], v[32:35], v[20:23]\n"
    "buffer_load_dwordx4 v[64:67], v2, s[88:91], s49 offen sc1\n"
    "v_mfma_f32_16x16x32_bf16 v[24:27], v[32:35], v[180:183], v[24:27]\n"
    "v_mfma_f32_16x16x32_bf16 v[28:31], v[32:35], v[184:187], v[28:31]\n"
    "v_mfma_f32_16x16x32_bf16 v[8:11], a[72:75], v[36:39], v[8:11]\n"
    "v_mfma_f32_16x16x32_bf16 v[12:15], a[76:79], v[36:39], v[12:15]\n"
    "v_mfma_f32_16x16x32_bf16 v[16:19], a[216:219], v[36:39], v[16:19]\n"
    "v_mfma_f32_16x16x32_bf16 v[20:23], a[220:223], v[36:39], v[20:23]\n"
    "v_mfma_f32_16x16x32_bf16 v[24:27], v[36:39], v[188:191], v[24:27]\n"
    "v_xor_b32 v0, 0x6000, v1\n"
    "v_mfma_f32_16x16x32_bf16 v[28:31], v[36:39], v[192:195], v[28:31]\n"
    "s_waitcnt lgkmcnt(0)\n"
    "v_mfma_f32_16x16x32_bf16 v[8:11], a[80:83], v[40:43], v[8:11]\n"
    "ds_read_b128 v[36:39], v0 offset:1024\n"
    "ds_read_b128 v[32:35], v0\n"
    "v_mfma_f32_16x16x32_bf16 v[12:15], a[84:87], v[40:43], v[12:15]\n"
    "v_mfma_f32_16x16x32_bf16 v[16:19], a[224:227], v[40:43], v[16:19]\n"
    "v_mfma_f32_16x16x32_bf16 v[20:23], a[228:231], v[40:43], v[20:23]\n"
    "v_mfma_f32_16x16x32_bf16 v[24:27], v[40:43], v[196:199], v[24:27]\n"
    "v_mfma_f32_16x16x32_bf16 v[28:31], v[40:43], v[200:203], v[28:31]\n"
    "v_mfma_f32_16x16x32_bf16 v[8:11], a[88:91], v[44:47], v[8:11]\n"
    "v_mfma_f32_16x16x32_bf16 v[12:15], a[92:95], v[44:47], v[12:15]\n"
    "v_mfma_f32_16x16x32_bf16 v[16:19], a[232:235], v[44:47], v[16:19]\n"
    "v_mfma_f32_16x16x32_bf16 v[20:23], a[236:239], v[44:47], v[20:23]\n"
    "v_mfma_f32_16x16x32_bf16 v[24:27], v[44:47], v[204:207], v[24:27]\n"
    "v_mfma_f32_16x16x32_bf16 v[28:31], v[44:47], v[208:211], v[28:31]\n"
    "s_waitcnt lgkmcnt(0)\n"
    "v_mfma_f32_16x16x32_bf16 v[8:11], a[96:99], v[32:35], v[8:11]\n"
    "ds_read_b128 v[44:47], v0 offset:3072\n"
    "ds_read_b128 v[40:43], v0 offset:2048\n"
    "v_mfma_f32_16x16x32_bf16 v[12:15], a[100:103], v[32:35], v[12:15]\n"
    "v_mfma_f32_16x16x32_bf16 v[16:19], a[240:243], v[32:35], v[16:19]\n"
    "s_cmp_eq_u32 s94, 0\n"
    "s_cbranch_scc1 .Lhk2_%=\n"
    ".Lhk2_x%=:\n"
    "v_mfma_f32_16x16x32_bf16 v[20:23], a[244:247], v[32:35], v[20:23]\n"
    "v_mfma_f32_16x16x32_bf16 v[24:27], v[32:35], v[212:215], v[24:27]\n"
    "v_and_b32 v0, 0xffff9fff, v1\n"
    "v_mfma_f32_16x16x32_bf16 v[28:31], v[32:35], v[216:219], v[28:31]\n"
    "ds_read_b128 v[32:35], v0 offset:4096\n"
    "v_mfma_f32_16x16x32_bf16 v[8:11], a[104:107], v[36:39], v[8:11]\n"
    "v_mfma_f32_16x16x32_bf16 v[12:15], a[108:111], v[36:39], v[12:15]\n"
    "v_mfma_f32_16x16x32_bf16 v[16:19], a[248:251], v[36:39], v[16:19]\n"
    "v_mfma_f32_16x16x32_bf16 v[20:23], a[252:255], v[36:39], v[20:23]\n"
    "v_mfma_f32_16x16x32_bf16 v[24:27], v[36:39], v[220:223], v[24:27]\n"
    "v_mfma_f32_16x16x32_bf16 v[28:31], v[36:39], v[224:227], v[28:31]\n"
    "ds_read_b128 v[36:39], v0 offset:12288\n"
    "s_waitcnt lgkmcnt(2)\n"
    "v_mfma_f32_16x16x32_bf16 v[8:11], a[112:115], v[40:43], v[8:11]\n"
    "v_mfma_f32_16x16x32_bf16 v[12:15], a[116:119], v[40:43], v[12:15]\n"
    "v_xor_b32 v1, 0x8000, v1\n"
    "v_xor_b32 v6, 0x8000, v6\n"
    "v_mfma_f32_16x16x32_bf16 v[8:11], a[120:123], v[44:47], v[8:11]\n"
    "v_mfma_f32_16x16x32_bf16 v[12:15], a[124:127], v[44:47], v[12:15]\n"
    "s_waitcnt lgkmcnt(1)\n"
    "v_mfma_f32_16x16x32_bf16 v[8:11], a[128:131], v[32:35], v[8:11]\n"
    "v_mfma_f32_16x16x32_bf16 v[12:15], a[132:135], v[32:35], v[12:15]\n"
    "s_waitcnt lgkmcnt(0)\n"
    "v_mfma_f32_16x16x32_bf16 v[8:11], a[136:139], v[36:39], v[8:11]\n"
    "v_mfma_f32_16x16x32_bf16 v[12:15], a[140:143], v[36:39], v[12:15]\n"
    "v_mfma_f32_16x16x32_bf16 v[16:19], v[84:87], v[40:43], v[16:19]\n"
    "v_mfma_f32_16x16x32_bf16 v[20:23], v[88:91], v[40:43], v[20:23]\n"
    "v_mul_f32 v8, v8, v70\n"
    "v_mul_f32 v12, v12, v70\n"
    "v_mfma_f32_16x16x32_bf16 v[16:19], v[92:95], v[44:47], v[16:19]\n"
    "v_cvt_pk_bf16_f32 v8, v8, v12\n"
    "v_mul_f32 v9, v9, v70\n"
    "v_mfma_f32_16x16x32_bf16 v[20:23], v[96:99], v[44:47], v[20:23]\n"
    "v_mul_f32 v13, v13, v70\n"
    "v_cvt_pk_bf16_f32 v9, v9, v13\n"
    "v_mfma_f32_16x16x32_bf16 v[16:19], v[100:103], v[32:35], v[16:19]\n"
    "v_mul_f32 v10, v10, v70\n"
    "v_mul_f32 v14, v14, v70\n"
    "v_mfma_f32_16x16x32_bf16 v[20:23], v[104:107], v[32:35], v[20:23]\n"
    "v_cvt_pk_bf16_f32 v10, v10, v14\n"
    "v_mul_f32 v11, v11, v70\n"
    "v_mfma_f32_16x16x32_bf16 v[16:19], v[108:111], v[36:39], v[16:19]\n"
    "v_mul_f32 v15, v15, v70\n"
    "v_cvt_pk_bf16_f32 v11, v11, v15\n"
    "v_mfma_f32_16x16x32_bf16 v[20:23], v[112:115], v[36:39], v[20:23]\n"
    "buffer_store_dwordx4 v[8:11], v2, s[40:43], s62 offen sc1\n"
    "v_mfma_f32_16x16x32_bf16 v[24:27], v[40:43], v[228:231], v[24:27]\n"
    "v_mfma_f32_16x16x32_bf16 v[28:31], v[40:43], v[232:235], v[28:31]\n"
    "v_mul_f32 v16, v16, v70\n"
    "v_mul_f32 v20, v20, v70\n"
    "v_mfma_f32_16x16x32_bf16 v[24:27], v[44:47], v[236:239], v[24:27]\n"
    "v_cvt_pk_bf16_f32 v16, v16, v20\n"
    "v_mul_f32 v17, v17, v70\n"
    "v_mfma_f32_16x16x32_bf16 v[28:31], v[44:47], v[240:243], v[28:31]\n"
    "v_mul_f32 v21, v21, v70\n"
    "v_cvt_pk_bf16_f32 v17, v17, v21\n"
    "v_mfma_f32_16x16x32_bf16 v[24:27], v[32:35], v[244:247], v[24:27]\n"
    "v_mul_f32 v18, v18, v70\n"
    "v_mul_f32 v22, v22, v70\n"
    "v_mfma_f32_16x16x32_bf16 v[28:31], v[32:35], v[248:251], v[28:31]\n"
    "v_cvt_pk_bf16_f32 v18, v18, v22\n"
    "v_mul_f32 v19, v19, v70\n"
    "v_mfma_f32_16x16x32_bf16 v[24:27], v[36:39], v[252:255], v[24:27]\n"
    "v_mul_f32 v23, v23, v70\n"
    "v_cvt_pk_bf16_f32 v19, v19, v23\n"
    "v_mfma_f32_16x16x32_bf16 v[28:31], v[36:39], v[72:75], v[28:31]\n"
    "buffer_store_dwordx4 v[16:19], v2, s[40:43], s63 offen sc1\n"
    "s_mov_b32 s56, 1\n"
    "s_cmp_lg_u32 s94, 0\n"
    "s_cbranch_scc1 .Lhgo_%=\n"
    "s_nop 0\n"
    "v_mul_f32 v24, v24, v76\n"
    "v_mul_f32 v25, v25, v77\n"
    "v_cvt_pk_bf16_f32 v24, v24, v25\n"
    "v_mul_f32 v26, v26, v78\n"
    "v_mul_f32 v27, v27, v79\n"
    "v_cvt_pk_bf16_f32 v25, v26, v27\n"
    "v_mul_f32 v28, v28, v76\n"
    "v_mul_f32 v29, v29, v77\n"
    "v_cvt_pk_bf16_f32 v26, v28, v29\n"
    "v_mul_f32 v30, v30, v78\n"
    "v_mul_f32 v31, v31, v79\n"
    "v_cvt_pk_bf16_f32 v27, v30, v31\n"
    "buffer_store_dwordx4 v[24:27], v2, s[40:43], s64 offen sc1\n"
    "s_waitcnt vmcnt(3)\n"
    "v_cmp_eq_u32 vcc, 0, v81\n"
    "s_cmp_eq_u64 vcc, 0\n"
    "s_cbranch_scc1 .Lhld_%=\n"
    "ds_read_b32 v83, v7 offset:44\n"
    "global_load_dword v81, v80, %1 sc1\n"
    "s_waitcnt vmcnt(0)\n"
    "v_mov_b32 v82, 1\n"
    "s_mov_b64 exec, 1\n"
    "ds_add_rtn_u32 v82, v7, v82 offset:48\n"
    "s_mov_b64 exec, -1\n"
    "s_waitcnt lgkmcnt(0)\n"
    "v_readfirstlane_b32 s50, v82\n"
    "s_cmp_eq_u32 s50, 3\n"
    "s_cselect_b64 exec, 1, 0\n"
    "global_store_dword v83, v82, %1 sc1\n"
    "s_mov_b64 exec, -1\n"
    "s_mov_b32 s56, 0\n"
    "v_cmp_eq_u32 vcc, 0, v81\n"
    "s_cmp_eq_u64 vcc, 0\n"
    "s_cbranch_scc1 .Lhld_%=\n"
    "v_readfirstlane_b32 s50, v80\n"
    "s_cmp_eq_u32 s50, 192\n"
    "s_cbranch_scc1 .Lhend_%=\n"
    ".Lhwait_%=:\n"
    "global_load_dword v81, v80, %1 sc1\n"
    "s_mov_b32 s52, 0\n"
    ".Lhp3_%=:\n"
    "s_waitcnt vmcnt(0)\n"
    "v_cmp_eq_u32 vcc, 0, v81\n"
    "s_cmp_eq_u64 vcc, 0\n"
    "s_cbranch_scc1 .Lhp3_x%=\n"
    "s_add_u32 s52, s52, 1\n"
    "s_sleep 2\n"
    "global_load_dword v81, v80, %1 sc1\n"
    "s_cmp_lt_u32 s52, s53\n"
    "s_cbranch_scc1 .Lhp3_%=\n"
    "s_mov_b32 s53, 0\n"
    "v_mov_b32 v82, 0\n"
    "v_mov_b32 v83, 1\n"
    "global_store_dword v82, v83, %1 sc1\n"
    "s_waitcnt vmcnt(0)\n"
    ".Lhp3_x%=:\n"
    ".Lhld_%=:\n"
    "s_mov_b32 s94, s42\n"
    "s_mov_b32 s90, s96\n"
    "buffer_load_dwordx4 v[48:51], v3, s[92:95], s49 offen sc1\n"
    "buffer_load_dwordx4 v[52:55], v3, s[92:95], s49 offen offset:1024 sc1\n"
    "buffer_load_dwordx4 v[56:59], v3, s[92:95], s49 offen offset:2048 sc1\n"
    "buffer_load_dwordx4 v[60:63], v3, s[92:95], s49 offen offset:3072 sc1\n"
    "buffer_load_dwordx4 v[64:67], v2, s[88:91], s49 offen sc1\n"
    ".Lhin_%=:\n"
    "v_add_u32 v7, 32, v7\n"
    "ds_read_b128 v[80:83], v7\n"
    "s_waitcnt vmcnt(4)\n"
    "v_mfma_f32_16x16x32_bf16 v[68:71], v[48:51], v[48:51], 0\n"
    "s_waitcnt vmcnt(3)\n"
    "v_mfma_f32_16x16x32_bf16 v[68:71], v[52:55], v[52:55], v[68:71]\n"
    "ds_write_b128 v1, v[48:51]\n"
    "ds_write_b128 v1, v[52:55] offset:1024\n"
    "s_waitcnt vmcnt(2)\n"
    "v_mfma_f32_16x16x32_bf16 v[68:71], v[56:59], v[56:59], v[68:71]\n"
    "ds_write_b128 v1, v[56:59] offset:2048\n"
    "s_waitcnt vmcnt(1)\n"
    "ds_write_b128 v1, v[60:63] offset:3072\n"
    "v_mfma_f32_16x16x32_bf16 v[68:71], v[60:63], v[60:63], v[68:71]\n"
    "s_waitcnt vmcnt(0)\n"
    "v_mfma_f32_16x16x32_bf16 v[68:71], v[64:67], v[64:67], v[68:71]\n"
    "v_mfma_f32_16x16x32_bf16 v[8:11], a[0:3], v[48:51], 0\n"
    "v_mfma_f32_16x16x32_bf16 v[12:15], a[4:7], v[48:51], 0\n"
    "v_mfma_f32_16x16x32_bf16 v[16:19], a[144:147], v[48:51], 0\n"
    "v_mov_b64 v[76:77], s[86:87]\n"
    "v_mov_b64 v[78:79], s[86:87]\n"
    "v_mfma_f32_16x16x32_bf16 v[20:23], a[148:151], v[48:51], 0\n"
    "ds_write_b128 v1, v[64:67] offset:4096\n"
    "v_mfma_f32_16x16x32_bf16 v[72:75], v[76:79], v[48:51], 0\n"
    "v_mfma_f32_16x16x32_bf16 v[72:75], v[76:79], v[52:55], v[72:75]\n"
    "v_mfma_f32_16x16x32_bf16 v[72:75], v[76:79], v[56:59], v[72:75]\n"
    "ds_write_b128 v1, v[68:71] offset:5120\n"
    "s_waitcnt lgkmcnt(6)\n"
    "v_readfirstlane_b32 s49, v81\n"
    "v_mfma_f32_16x16x32_bf16 v[72:75], v[76:79], v[60:63], v[72:75]\n"
    "v_readfirstlane_b32 s62, v82\n"
    "v_add_u32 v80, v80, v4\n"
    "v_mfma_f32_16x16x32_bf16 v[72:75], v[76:79], v[64:67], v[72:75]\n"
    "s_cmp_eq_u32 s56, 0\n"
    "s_cbranch_scc1 .Lhc4_%=\n"
    "s_waitcnt vmcnt(0)\n"
    "v_mov_b32 v82, 1\n"
    "s_mov_b64 exec, 1\n"
    "ds_add_rtn_u32 v82, v7, v82 offset:16\n"
    "s_mov_b64 exec, -1\n"
    "s_waitcnt lgkmcnt(0)\n"
    "v_readfirstlane_b32 s50, v82\n"
    "s_cmp_eq_u32 s50, 3\n"
    "s_cselect_b64 exec, 1, 0\n"
    "global_store_dword v83, v82, %1 sc1\n"
    "s_mov_b64 exec, -1\n"
    "s_mov_b32 s56, 0\n"
    ".Lhc4_%=:\n"
    "s_branch .Lhtop_%=\n"
    ".Lhk2_%=:\n"
    "s_waitcnt vmcnt(5)\n"
    "v_cmp_eq_u32 vcc, 0, v81\n"
    "s_cmp_eq_u64 vcc, 0\n"
    "s_cselect_b32 s94, s42, 0\n"
    "s_cselect_b32 s90, s96, 0\n"
    "global_load_dword v81, v80, %1 sc1\n"
    "buffer_load_dwordx4 v[48:51], v3, s[92:95], s49 offen sc1\n"
    "buffer_load_dwordx4 v[52:55], v3, s[92:95], s49 offen offset:1024 sc1\n"
    "buffer_load_dwordx4 v[56:59], v3, s[92:95], s49 offen offset:2048 sc1\n"
    "buffer_load_dwordx4 v[60:63], v3, s[92:95], s49 offen offset:3072 sc1\n"
    "buffer_load_dwordx4 v[64:67], v2, s[88:91], s49 offen sc1\n"
    "s_branch .Lhk2_x%=\n"
    ".Lhend_%=:\n"
    "s_waitcnt vmcnt(0) lgkmcnt(0)\n"
    "s_endpgm\n"
    ".p2align 6\n"
    ".Lrole2_%=:\n"
    "s_mov_b64 s[40:41], %0\n"
    "s_and_b32 s41, s41, 0xffff\n"
    "s_mov_b32 s42, 0xbc00000\n"
    "s_mov_b32 s43, 0x20000\n"
    "s_mov_b64 s[92:93], %0\n"
    "s_and_b32 s93, s93, 0xffff\n"
    "s_mov_b32 s94, 0xbc00000\n"
    "s_mov_b32 s95, 0x20000\n"
    "s_mov_b32 s53, 131072\n"
    "s_mov_b64 s[56:57], 0\n"
    "v_mbcnt_lo_u32_b32 v7, -1, 0\n"
    "v_mbcnt_hi_u32_b32 v7, -1, v7\n"
    "s_lshl_b32 s50, %2, 8\n"
    "s_add_u32 s50, s50, 147456\n"
    "v_lshl_add_u32 v7, v7, 2, s50\n"
    "ds_read_b32 v1, v7\n"
    "ds_read_b32 v4, v7 offset:1024\n"
    "ds_read_b32 v2, v7 offset:2048\n"
    "ds_read_b32 v8, v7 offset:3072\n"
    "ds_read_b32 v9, v7 offset:4096\n"
    "ds_read_b32 v10, v7 offset:5120\n"
    "ds_read_b32 v11, v7 offset:6144\n"
    "ds_read_b32 v3, v7 offset:7168\n"
    "s_waitcnt lgkmcnt(0)\n"
    "s_barrier\n"
    "s_mul_i32 s54, %2, 6144\n"
    "v_add_u32 v5, 16384, v4\n"
    "s_xor_b32 s50, %2, 0\n"
    "s_mul_i32 s50, s50, 6144\n"
    "v_add_u32 v32, s50, v9\n"
    "v_add_u32 v33, s50, v10\n"
    "v_add_u32 v34, s50, v11\n"
    "s_xor_b32 s50, %2, 1\n"
    "s_mul_i32 s50, s50, 6144\n"
    "v_add_u32 v35, s50, v9\n"
    "v_add_u32 v36, s50, v10\n"
    "v_add_u32 v37, s50, v11\n"
    "s_xor_b32 s50, %2, 2\n"
    "s_mul_i32 s50, s50, 6144\n"
    "v_add_u32 v38, s50, v9\n"
    "v_add_u32 v39, s50, v10\n"
    "v_add_u32 v40, s50, v11\n"
    "s_xor_b32 s50, %2, 3\n"
    "s_mul_i32 s50, s50, 6144\n"
    "v_add_u32 v41, s50, v9\n"
    "v_add_u32 v42, s50, v10\n"
    "v_add_u32 v43, s50, v11\n"
    "s_mov_b64 s[44:45], %8\n"
    "s_and_b32 s45, s45, 0xffff\n"
    "s_mov_b32 s46, 0x480000\n"
    "s_mov_b32 s47, 0x20000\n"
    "s_add_u32 m0, s54, 0\n"
    "s_add_u32 s55, %6, 0\n"
    "buffer_load_dwordx4 v8, s[44:47], s55 offen lds\n"
    "s_add_u32 m0, s54, 1024\n"
    "s_add_u32 s55, %6, 18432\n"
    "buffer_load_dwordx4 v8, s[44:47], s55 offen lds\n"
    "s_add_u32 m0, s54, 2048\n"
    "s_add_u32 s55, %6, 128\n"
    "buffer_load_dwordx4 v8, s[44:47], s55 offen lds\n"
    "s_add_u32 m0, s54, 3072\n"
    "s_add_u32 s55, %6, 18560\n"
    "buffer_load_dwordx4 v8, s[44:47], s55 offen lds\n"
    "s_add_u32 m0, s54, 4096\n"
    "s_add_u32 s55, %6, 256\n"
    "buffer_load_dwordx4 v8, s[44:47], s55 offen lds\n"
    "s_add_u32 m0, s54, 5120\n"
    "s_add_u32 s55, %6, 18688\n"
    "buffer_load_dwordx4 v8, s[44:47], s55 offen lds\n"
    "s_add_u32 m0, s54, 24576\n"
    "s_add_u32 s55, %6, 2304\n"
    "buffer_load_dwordx4 v8, s[44:47], s55 offen lds\n"
    "s_add_u32 m0, s54, 25600\n"
    "s_add_u32 s55, %6, 20736\n"
    "buffer_load_dwordx4 v8, s[44:47], s55 offen lds\n"
    "s_add_u32 m0, s54, 26624\n"
    "s_add_u32 s55, %6, 2432\n"
    "buffer_load_dwordx4 v8, s[44:47], s55 offen lds\n"
    "s_add_u32 m0, s54, 27648\n"
    "s_add_u32 s55, %6, 20864\n"
    "buffer_load_dwordx4 v8, s[44:47], s55 offen lds\n"
    "s_add_u32 m0, s54, 28672\n"
    "s_add_u32 s55, %6, 2560\n"
    "buffer_load_dwordx4 v8, s[44:47], s55 offen lds\n"
    "s_add_u32 m0, s54, 29696\n"
    "s_add_u32 s55, %6, 20992\n"
    "buffer_load_dwordx4 v8, s[44:47], s55 offen lds\n"
    "s_add_u32 m0, s54, 49152\n"
    "s_add_u32 s55, %6, 36864\n"
    "buffer_load_dwordx4 v8, s[44:47], s55 offen lds\n"
    "s_add_u32 m0, s54, 50176\n"
    "s_add_u32 s55, %6, 55296\n"
    "buffer_load_dwordx4 v8, s[44:47], s55 offen lds\n"
    "s_add_u32 m0, s54, 51200\n"
    "s_add_u32 s55, %6, 36992\n"
    "buffer_load_dwordx4 v8, s[44:47], s55 offen lds\n"
    "s_add_u32 m0, s54, 52224\n"
    "s_add_u32 s55, %6, 55424\n"
    "buffer_load_dwordx4 v8, s[44:47], s55 offen lds\n"
    "s_add_u32 m0, s54, 53248\n"
    "s_add_u32 s55, %6, 37120\n"
    "buffer_load_dwordx4 v8, s[44:47], s55 offen lds\n"
    "s_add_u32 m0, s54, 54272\n"
    "s_add_u32 s55, %6, 55552\n"
    "buffer_load_dwordx4 v8, s[44:47], s55 offen lds\n"
    "s_add_u32 m0, s54, 73728\n"
    "s_add_u32 s55, %6, 39168\n"
    "buffer_load_dwordx4 v8, s[44:47], s55 offen lds\n"
    "s_add_u32 m0, s54, 74752\n"
    "s_add_u32 s55, %6, 57600\n"
    "buffer_load_dwordx4 v8, s[44:47], s55 offen lds\n"
    "s_add_u32 m0, s54, 75776\n"
    "s_add_u32 s55, %6, 39296\n"
    "buffer_load_dwordx4 v8, s[44:47], s55 offen lds\n"
    "s_add_u32 m0, s54, 76800\n"
    "s_add_u32 s55, %6, 57728\n"
    "buffer_load_dwordx4 v8, s[44:47], s55 offen lds\n"
    "s_add_u32 m0, s54, 77824\n"
    "s_add_u32 s55, %6, 39424\n"
    "buffer_load_dwordx4 v8, s[44:47], s55 offen lds\n"
    "s_add_u32 m0, s54, 78848\n"
    "s_add_u32 s55, %6, 57856\n"
    "buffer_load_dwordx4 v8, s[44:47], s55 offen lds\n"
    "s_add_u32 m0, s54, 98304\n"
    "s_add_u32 s55, %6, 73728\n"
    "buffer_load_dwordx4 v8, s[44:47], s55 offen lds\n"
    "s_add_u32 m0, s54, 99328\n"
    "s_add_u32 s55, %6, 92160\n"
    "buffer_load_dwordx4 v8, s[44:47], s55 offen lds\n"
    "s_add_u32 m0, s54, 100352\n"
    "s_add_u32 s55, %6, 73856\n"
    "buffer_load_dwordx4 v8, s[44:47], s55 offen lds\n"
    "s_add_u32 m0, s54, 101376\n"
    "s_add_u32 s55, %6, 92288\n"
    "buffer_load_dwordx4 v8, s[44:47], s55 offen lds\n"
    "s_add_u32 m0, s54, 102400\n"
    "s_add_u32 s55, %6, 73984\n"
    "buffer_load_dwordx4 v8, s[44:47], s55 offen lds\n"
    "s_add_u32 m0, s54, 103424\n"
    "s_add_u32 s55, %6, 92416\n"
    "buffer_load_dwordx4 v8, s[44:47], s55 offen lds\n"
    "s_add_u32 m0, s54, 122880\n"
    "s_add_u32 s55, %6, 76032\n"
    "buffer_load_dwordx4 v8, s[44:47], s55 offen lds\n"
    "s_add_u32 m0, s54, 123904\n"
    "s_add_u32 s55, %6, 94464\n"
    "buffer_load_dwordx4 v8, s[44:47], s55 offen lds\n"
    "s_add_u32 m0, s54, 124928\n"
    "s_add_u32 s55, %6, 76160\n"
    "buffer_load_dwordx4 v8, s[44:47], s55 offen lds\n"
    "s_add_u32 m0, s54, 125952\n"
    "s_add_u32 s55, %6, 94592\n"
    "buffer_load_dwordx4 v8, s[44:47], s55 offen lds\n"
    "s_add_u32 m0, s54, 126976\n"
    "s_add_u32 s55, %6, 76288\n"
    "buffer_load_dwordx4 v8, s[44:47], s55 offen lds\n"
    "s_add_u32 m0, s54, 128000\n"
    "s_add_u32 s55, %6, 94720\n"
    "buffer_load_dwordx4 v8, s[44:47], s55 offen lds\n"
    "s_waitcnt vmcnt(30) lgkmcnt(0)\n"
    "s_barrier\n"
    "ds_read_b64_tr_b16 a[0:1], v32\n"
    "ds_read_b64_tr_b16 a[4:5], v33\n"
    "ds_read_b64_tr_b16 a[8:9], v34\n"
    "ds_read_b64_tr_b16 a[96:97], v35\n"
    "ds_read_b64_tr_b16 a[100:101], v36\n"
    "ds_read_b64_tr_b16 a[104:105], v37\n"
    "ds_read_b64_tr_b16 a[192:193], v38\n"
    "ds_read_b64_tr_b16 a[196:197], v39\n"
    "ds_read_b64_tr_b16 a[200:201], v40\n"
    "ds_read_b64_tr_b16 v[160:161], v41\n"
    "ds_read_b64_tr_b16 v[164:165], v42\n"
    "ds_read_b64_tr_b16 v[168:169], v43\n"
    "s_waitcnt vmcnt(24) lgkmcnt(0)\n"
    "s_barrier\n"
    "s_add_u32 m0, s54, 0\n"
    "s_add_u32 s55, %6, 110592\n"
    "buffer_load_dwordx4 v8, s[44:47], s55 offen lds\n"
    "s_add_u32 m0, s54, 1024\n"
    "s_add_u32 s55, %6, 129024\n"
    "buffer_load_dwordx4 v8, s[44:47], s55 offen lds\n"
    "s_add_u32 m0, s54, 2048\n"
    "s_add_u32 s55, %6, 110720\n"
    "buffer_load_dwordx4 v8, s[44:47], s55 offen lds\n"
    "s_add_u32 m0, s54, 3072\n"
    "s_add_u32 s55, %6, 129152\n"
    "buffer_load_dwordx4 v8, s[44:47], s55 offen lds\n"
    "s_add_u32 m0, s54, 4096\n"
    "s_add_u32 s55, %6, 110848\n"
    "buffer_load_dwordx4 v8, s[44:47], s55 offen lds\n"
    "s_add_u32 m0, s54, 5120\n"
    "s_add_u32 s55, %6, 129280\n"
    "buffer_load_dwordx4 v8, s[44:47], s55 offen lds\n"
    "ds_read_b64_tr_b16 a[2:3], v32 offset:24576\n"
    "ds_read_b64_tr_b16 a[6:7], v33 offset:24576\n"
    "ds_read_b64_tr_b16 a[10:11], v34 offset:24576\n"
    "ds_read_b64_tr_b16 a[98:99], v35 offset:24576\n"
    "ds_read_b64_tr_b16 a[102:103], v36 offset:24576\n"
    "ds_read_b64_tr_b16 a[106:107], v37 offset:24576\n"
    "ds_read_b64_tr_b16 a[194:195], v38 offset:24576\n"
    "ds_read_b64_tr_b16 a[198:199], v39 offset:24576\n"
    "ds_read_b64_tr_b16 a[202:203], v40 offset:24576\n"
    "ds_read_b64_tr_b16 v[162:163], v41 offset:24576\n"
    "ds_read_b64_tr_b16 v[166:167], v42 offset:24576\n"
    "ds_read_b64_tr_b16 v[170:171], v43 offset:24576\n"
    "s_waitcnt vmcnt(24) lgkmcnt(0)\n"
    "s_barrier\n"
    "s_add_u32 m0, s54, 24576\n"
    "s_add_u32 s55, %6, 112896\n"
    "buffer_load_dwordx4 v8, s[44:47], s55 offen lds\n"
    "s_add_u32 m0, s54, 25600\n"
    "s_add_u32 s55, %6, 131328\n"
    "buffer_load_dwordx4 v8, s[44:47], s55 offen lds\n"
    "s_add_u32 m0, s54, 26624\n"
    "s_add_u32 s55, %6, 113024\n"
    "buffer_load_dwordx4 v8, s[44:47], s55 offen lds\n"
    "s_add_u32 m0, s54, 27648\n"
    "s_add_u32 s55, %6, 131456\n"
    "buffer_load_dwordx4 v8, s[44:47], s55 offen lds\n"
    "s_add_u32 m0, s54, 28672\n"
    "s_add_u32 s55, %6, 113152\n"
    "buffer_load_dwordx4 v8, s[44:47], s55 offen lds\n"
    "s_add_u32 m0, s54, 29696\n"
    "s_add_u32 s55, %6, 131584\n"
    "buffer_load_dwordx4 v8, s[44:47], s55 offen lds\n"
    "ds_read_b64_tr_b16 a[12:13], v32 offset:49152\n"
    "ds_read_b64_tr_b16 a[16:17], v33 offset:49152\n"
    "ds_read_b64_tr_b16 a[20:21], v34 offset:49152\n"
    "ds_read_b64_tr_b16 a[108:109], v35 offset:49152\n"
    "ds_read_b64_tr_b16 a[112:113], v36 offset:49152\n"
    "ds_read_b64_tr_b16 a[116:117], v37 offset:49152\n"
    "ds_read_b64_tr_b16 a[204:205], v38 offset:49152\n"
    "ds_read_b64_tr_b16 a[208:209], v39 offset:49152\n"
    "ds_read_b64_tr_b16 a[212:213], v40 offset:49152\n"
    "ds_read_b64_tr_b16 v[172:173], v41 offset:49152\n"
    "ds_read_b64_tr_b16 v[176:177], v42 offset:49152\n"
    "ds_read_b64_tr_b16 v[180:181], v43 offset:49152\n"
    "s_waitcnt vmcnt(24) lgkmcnt(0)\n"
    "s_barrier\n"
    "s_add_u32 m0, s54, 49152\n"
    "s_add_u32 s55, %6, 147456\n"
    "buffer_load_dwordx4 v8, s[44:47], s55 offen lds\n"
    "s_add_u32 m0, s54, 50176\n"
    "s_add_u32 s55, %6, 165888\n"
    "buffer_load_dwordx4 v8, s[44:47], s55 offen lds\n"
    "s_add_u32 m0, s54, 51200\n"
    "s_add_u32 s55, %6, 147584\n"
    "buffer_load_dwordx4 v8, s[44:47], s55 offen lds\n"
    "s_add_u32 m0, s54, 52224\n"
    "s_add_u32 s55, %6, 166016\n"
    "buffer_load_dwordx4 v8, s[44:47], s55 offen lds\n"
    "s_add_u32 m0, s54, 53248\n"
    "s_add_u32 s55, %6, 147712\n"
    "buffer_load_dwordx4 v8, s[44:47], s55 offen lds\n"
    "s_add_u32 m0, s54, 54272\n"
    "s_add_u32 s55, %6, 166144\n"
    "buffer_load_dwordx4 v8, s[44:47], s55 offen lds\n"
    "v_add_u32 v71, 0x10000, v32\n"
    "ds_read_b64_tr_b16 a[14:15], v71 offset:8192\n"
    "v_add_u32 v70, 0x10000, v33\n"
    "ds_read_b64_tr_b16 a[18:19], v70 offset:8192\n"
    "v_add_u32 v69, 0x10000, v34\n"
    "ds_read_b64_tr_b16 a[22:23], v69 offset:8192\n"
    "v_add_u32 v68, 0x10000, v35\n"
    "ds_read_b64_tr_b16 a[110:111], v68 offset:8192\n"
    "v_add_u32 v67, 0x10000, v36\n"
    "ds_read_b64_tr_b16 a[114:115], v67 offset:8192\n"
    "v_add_u32 v66, 0x10000, v37\n"
    "ds_read_b64_tr_b16 a[118:119], v66 offset:8192\n"
    "v_add_u32 v65, 0x10000, v38\n"
    "ds_read_b64_tr_b16 a[206:207], v65 offset:8192\n"
    "v_add_u32 v64, 0x10000, v39\n"
    "ds_read_b64_tr_b16 a[210:211], v64 offset:8192\n"
    "v_add_u32 v63, 0x10000, v40\n"
    "ds_read_b64_tr_b16 a[214:215], v63 offset:8192\n"
    "v_add_u32 v62, 0x10000, v41\n"
    "ds_read_b64_tr_b16 v[174:175], v62 offset:8192\n"
    "v_add_u32 v61, 0x10000, v42\n"
    "ds_read_b64_tr_b16 v[178:179], v61 offset:8192\n"
    "v_add_u32 v60, 0x10000, v43\n"
    "ds_read_b64_tr_b16 v[182:183], v60 offset:8192\n"
    "s_waitcnt vmcnt(24) lgkmcnt(0)\n"
    "s_barrier\n"
    "s_add_u32 m0, s54, 73728\n"
    "s_add_u32 s55, %6, 149760\n"
    "buffer_load_dwordx4 v8, s[44:47], s55 offen lds\n"
    "s_add_u32 m0, s54, 74752\n"
    "s_add_u32 s55, %6, 168192\n"
    "buffer_load_dwordx4 v8, s[44:47], s55 offen lds\n"
    "s_add_u32 m0, s54, 75776\n"
    "s_add_u32 s55, %6, 149888\n"
    "buffer_load_dwordx4 v8, s[44:47], s55 offen lds\n"
    "s_add_u32 m0, s54, 76800\n"
    "s_add_u32 s55, %6, 168320\n"
    "buffer_load_dwordx4 v8, s[44:47], s55 offen lds\n"
    "s_add_u32 m0, s54, 77824\n"
    "s_add_u32 s55, %6, 150016\n"
    "buffer_load_dwordx4 v8, s[44:47], s55 offen lds\n"
    "s_add_u32 m0, s54, 78848\n"
    "s_add_u32 s55, %6, 168448\n"
    "buffer_load_dwordx4 v8, s[44:47], s55 offen lds\n"
    "ds_read_b64_tr_b16 a[24:25], v71 offset:32768\n"
    "ds_read_b64_tr_b16 a[28:29], v70 offset:32768\n"
    "ds_read_b64_tr_b16 a[32:33], v69 offset:32768\n"
    "ds_read_b64_tr_b16 a[120:121], v68 offset:32768\n"
    "ds_read_b64_tr_b16 a[124:125], v67 offset:32768\n"
    "ds_read_b64_tr_b16 a[128:129], v66 offset:32768\n"
    "ds_read_b64_tr_b16 a[216:217], v65 offset:32768\n"
    "ds_read_b64_tr_b16 a[220:221], v64 offset:32768\n"
    "ds_read_b64_tr_b16 a[224:225], v63 offset:32768\n"
    "ds_read_b64_tr_b16 v[184:185], v62 offset:32768\n"
    "ds_read_b64_tr_b16 v[188:189], v61 offset:32768\n"
    "ds_read_b64_tr_b16 v[192:193], v60 offset:32768\n"
    "s_waitcnt vmcnt(24) lgkmcnt(0)\n"
    "s_barrier\n"
    "s_add_u32 m0, s54, 98304\n"
    "s_add_u32 s55, %6, 184320\n"
    "buffer_load_dwordx4 v8, s[44:47], s55 offen lds\n"
    "s_add_u32 m0, s54, 99328\n"
    "s_add_u32 s55, %6, 202752\n"
    "buffer_load_dwordx4 v8, s[44:47], s55 offen lds\n"
    "s_add_u32 m0, s54, 100352\n"
    "s_add_u32 s55, %6, 184448\n"
    "buffer_load_dwordx4 v8, s[44:47], s55 offen lds\n"
    "s_add_u32 m0, s54, 101376\n"
    "s_add_u32 s55, %6, 202880\n"
    "buffer_load_dwordx4 v8, s[44:47], s55 offen lds\n"
    "s_add_u32 m0, s54, 102400\n"
    "s_add_u32 s55, %6, 184576\n"
    "buffer_load_dwordx4 v8, s[44:47], s55 offen lds\n"
    "s_add_u32 m0, s54, 103424\n"
    "s_add_u32 s55, %6, 203008\n"
    "buffer_load_dwordx4 v8, s[44:47], s55 offen lds\n"
    "ds_read_b64_tr_b16 a[26:27], v71 offset:57344\n"
    "ds_read_b64_tr_b16 a[30:31], v70 offset:57344\n"
    "ds_read_b64_tr_b16 a[34:35], v69 offset:57344\n"
    "ds_read_b64_tr_b16 a[122:123], v68 offset:57344\n"
    "ds_read_b64_tr_b16 a[126:127], v67 offset:57344\n"
    "ds_read_b64_tr_b16 a[130:131], v66 offset:57344\n"
    "ds_read_b64_tr_b16 a[218:219], v65 offset:57344\n"
    "ds_read_b64_tr_b16 a[222:223], v64 offset:57344\n"
    "ds_read_b64_tr_b16 a[226:227], v63 offset:57344\n"
    "ds_read_b64_tr_b16 v[186:187], v62 offset:57344\n"
    "ds_read_b64_tr_b16 v[190:191], v61 offset:57344\n"
    "ds_read_b64_tr_b16 v[194:195], v60 offset:57344\n"
    "s_waitcnt vmcnt(24) lgkmcnt(0)\n"
    "s_barrier\n"
    "s_add_u32 m0, s54, 122880\n"
    "s_add_u32 s55, %6, 186624\n"
    "buffer_load_dwordx4 v8, s[44:47], s55 offen lds\n"
    "s_add_u32 m0, s54, 123904\n"
    "s_add_u32 s55, %6, 205056\n"
    "buffer_load_dwordx4 v8, s[44:47], s55 offen lds\n"
    "s_add_u32 m0, s54, 124928\n"
    "s_add_u32 s55, %6, 186752\n"
    "buffer_load_dwordx4 v8, s[44:47], s55 offen lds\n"
    "s_add_u32 m0, s54, 125952\n"
    "s_add_u32 s55, %6, 205184\n"
    "buffer_load_dwordx4 v8, s[44:47], s55 offen lds\n"
    "s_add_u32 m0, s54, 126976\n"
    "s_add_u32 s55, %6, 186880\n"
    "buffer_load_dwordx4 v8, s[44:47], s55 offen lds\n"
    "s_add_u32 m0, s54, 128000\n"
    "s_add_u32 s55, %6, 205312\n"
    "buffer_load_dwordx4 v8, s[44:47], s55 offen lds\n"
    "ds_read_b64_tr_b16 a[36:37], v32\n"
    "ds_read_b64_tr_b16 a[40:41], v33\n"
    "ds_read_b64_tr_b16 a[44:45], v34\n"
    "ds_read_b64_tr_b16 a[132:133], v35\n"
    "ds_read_b64_tr_b16 a[136:137], v36\n"
    "ds_read_b64_tr_b16 a[140:141], v37\n"
    "ds_read_b64_tr_b16 a[228:229], v38\n"
    "ds_read_b64_tr_b16 a[232:233], v39\n"
    "ds_read_b64_tr_b16 a[236:237], v40\n"
    "ds_read_b64_tr_b16 v[196:197], v41\n"
    "ds_read_b64_tr_b16 v[200:201], v42\n"
    "ds_read_b64_tr_b16 v[204:205], v43\n"
    "s_waitcnt vmcnt(24) lgkmcnt(0)\n"
    "s_barrier\n"
    "s_add_u32 m0, s54, 0\n"
    "s_add_u32 s55, %6, 221184\n"
    "buffer_load_dwordx4 v8, s[44:47], s55 offen lds\n"
    "s_add_u32 m0, s54, 1024\n"
    "s_add_u32 s55, %6, 239616\n"
    "buffer_load_dwordx4 v8, s[44:47], s55 offen lds\n"
    "s_add_u32 m0, s54, 2048\n"
    "s_add_u32 s55, %6, 221312\n"
    "buffer_load_dwordx4 v8, s[44:47], s55 offen lds\n"
    "s_add_u32 m0, s54, 3072\n"
    "s_add_u32 s55, %6, 239744\n"
    "buffer_load_dwordx4 v8, s[44:47], s55 offen lds\n"
    "s_add_u32 m0, s54, 4096\n"
    "s_add_u32 s55, %6, 221440\n"
    "buffer_load_dwordx4 v8, s[44:47], s55 offen lds\n"
    "s_add_u32 m0, s54, 5120\n"
    "s_add_u32 s55, %6, 239872\n"
    "buffer_load_dwordx4 v8, s[44:47], s55 offen lds\n"
    "ds_read_b64_tr_b16 a[38:39], v32 offset:24576\n"
    "ds_read_b64_tr_b16 a[42:43], v33 offset:24576\n"
    "ds_read_b64_tr_b16 a[46:47], v34 offset:24576\n"
    "ds_read_b64_tr_b16 a[134:135], v35 offset:24576\n"
    "ds_read_b64_tr_b16 a[138:139], v36 offset:24576\n"
    "ds_read_b64_tr_b16 a[142:143], v37 offset:24576\n"
    "ds_read_b64_tr_b16 a[230:231], v38 offset:24576\n"
    "ds_read_b64_tr_b16 a[234:235], v39 offset:24576\n"
    "ds_read_b64_tr_b16 a[238:239], v40 offset:24576\n"
    "ds_read_b64_tr_b16 v[198:199], v41 offset:24576\n"
    "ds_read_b64_tr_b16 v[202:203], v42 offset:24576\n"
    "ds_read_b64_tr_b16 v[206:207], v43 offset:24576\n"
    "s_waitcnt vmcnt(24) lgkmcnt(0)\n"
    "s_barrier\n"
    "s_add_u32 m0, s54, 24576\n"
    "s_add_u32 s55, %6, 223488\n"
    "buffer_load_dwordx4 v8, s[44:47], s55 offen lds\n"
    "s_add_u32 m0, s54, 25600\n"
    "s_add_u32 s55, %6, 241920\n"
    "buffer_load_dwordx4 v8, s[44:47], s55 offen lds\n"
    "s_add_u32 m0, s54, 26624\n"
    "s_add_u32 s55, %6, 223616\n"
    "buffer_load_dwordx4 v8, s[44:47], s55 offen lds\n"
    "s_add_u32 m0, s54, 27648\n"
    "s_add_u32 s55, %6, 242048\n"
    "buffer_load_dwordx4 v8, s[44:47], s55 offen lds\n"
    "s_add_u32 m0, s54, 28672\n"
    "s_add_u32 s55, %6, 223744\n"
    "buffer_load_dwordx4 v8, s[44:47], s55 offen lds\n"
    "s_add_u32 m0, s54, 29696\n"
    "s_add_u32 s55, %6, 242176\n"
    "buffer_load_dwordx4 v8, s[44:47], s55 offen lds\n"
    "ds_read_b64_tr_b16 a[48:49], v32 offset:49152\n"
    "ds_read_b64_tr_b16 a[52:53], v33 offset:49152\n"
    "ds_read_b64_tr_b16 a[56:57], v34 offset:49152\n"
    "ds_read_b64_tr_b16 a[144:145], v35 offset:49152\n"
    "ds_read_b64_tr_b16 a[148:149], v36 offset:49152\n"
    "ds_read_b64_tr_b16 a[152:153], v37 offset:49152\n"
    "ds_read_b64_tr_b16 a[240:241], v38 offset:49152\n"
    "ds_read_b64_tr_b16 a[244:245], v39 offset:49152\n"
    "ds_read_b64_tr_b16 a[248:249], v40 offset:49152\n"
    "ds_read_b64_tr_b16 v[208:209], v41 offset:49152\n"
    "ds_read_b64_tr_b16 v[212:213], v42 offset:49152\n"
    "ds_read_b64_tr_b16 v[216:217], v43 offset:49152\n"
    "s_waitcnt vmcnt(24) lgkmcnt(0)\n"
    "s_barrier\n"
    "s_add_u32 m0, s54, 49152\n"
    "s_add_u32 s55, %6, 258048\n"
    "buffer_load_dwordx4 v8, s[44:47], s55 offen lds\n"
    "s_add_u32 m0, s54, 50176\n"
    "s_add_u32 s55, %6, 276480\n"
    "buffer_load_dwordx4 v8, s[44:47], s55 offen lds\n"
    "s_add_u32 m0, s54, 51200\n"
    "s_add_u32 s55, %6, 258176\n"
    "buffer_load_dwordx4 v8, s[44:47], s55 offen lds\n"
    "s_add_u32 m0, s54, 52224\n"
    "s_add_u32 s55, %6, 276608\n"
    "buffer_load_dwordx4 v8, s[44:47], s55 offen lds\n"
    "s_add_u32 m0, s54, 53248\n"
    "s_add_u32 s55, %6, 258304\n"
    "buffer_load_dwordx4 v8, s[44:47], s55 offen lds\n"
    "s_add_u32 m0, s54, 54272\n"
    "s_add_u32 s55, %6, 276736\n"
    "buffer_load_dwordx4 v8, s[44:47], s55 offen lds\n"
    "ds_read_b64_tr_b16 a[50:51], v71 offset:8192\n"
    "ds_read_b64_tr_b16 a[54:55], v70 offset:8192\n"
    "ds_read_b64_tr_b16 a[58:59], v69 offset:8192\n"
    "ds_read_b64_tr_b16 a[146:147], v68 offset:8192\n"
    "ds_read_b64_tr_b16 a[150:151], v67 offset:8192\n"
    "ds_read_b64_tr_b16 a[154:155], v66 offset:8192\n"
    "ds_read_b64_tr_b16 a[242:243], v65 offset:8192\n"
    "ds_read_b64_tr_b16 a[246:247], v64 offset:8192\n"
    "ds_read_b64_tr_b16 a[250:251], v63 offset:8192\n"
    "ds_read_b64_tr_b16 v[210:211], v62 offset:8192\n"
    "ds_read_b64_tr_b16 v[214:215], v61 offset:8192\n"
    "ds_read_b64_tr_b16 v[218:219], v60 offset:8192\n"
    "s_waitcnt vmcnt(24) lgkmcnt(0)\n"
    "s_barrier\n"
    "s_add_u32 m0, s54, 73728\n"
    "s_add_u32 s55, %6, 260352\n"
    "buffer_load_dwordx4 v8, s[44:47], s55 offen lds\n"
    "s_add_u32 m0, s54, 74752\n"
    "s_add_u32 s55, %6, 278784\n"
    "buffer_load_dwordx4 v8, s[44:47], s55 offen lds\n"
    "s_add_u32 m0, s54, 75776\n"
    "s_add_u32 s55, %6, 260480\n"
    "buffer_load_dwordx4 v8, s[44:47], s55 offen lds\n"
    "s_add_u32 m0, s54, 76800\n"
    "s_add_u32 s55, %6, 278912\n"
    "buffer_load_dwordx4 v8, s[44:47], s55 offen lds\n"
    "s_add_u32 m0, s54, 77824\n"
    "s_add_u32 s55, %6, 260608\n"
    "buffer_load_dwordx4 v8, s[44:47], s55 offen lds\n"
    "s_add_u32 m0, s54, 78848\n"
    "s_add_u32 s55, %6, 279040\n"
    "buffer_load_dwordx4 v8, s[44:47], s55 offen lds\n"
    "ds_read_b64_tr_b16 a[60:61], v71 offset:32768\n"
    "ds_read_b64_tr_b16 a[64:65], v70 offset:32768\n"
    "ds_read_b64_tr_b16 a[68:69], v69 offset:32768\n"
    "ds_read_b64_tr_b16 a[156:157], v68 offset:32768\n"
    "ds_read_b64_tr_b16 a[160:161], v67 offset:32768\n"
    "ds_read_b64_tr_b16 a[164:165], v66 offset:32768\n"
    "ds_read_b64_tr_b16 a[252:253], v65 offset:32768\n"
    "ds_read_b64_tr_b16 v[128:129], v64 offset:32768\n"
    "ds_read_b64_tr_b16 v[132:133], v63 offset:32768\n"
    "ds_read_b64_tr_b16 v[220:221], v62 offset:32768\n"
    "ds_read_b64_tr_b16 v[224:225], v61 offset:32768\n"
    "ds_read_b64_tr_b16 v[228:229], v60 offset:32768\n"
    "s_waitcnt vmcnt(24) lgkmcnt(0)\n"
    "s_barrier\n"
    "ds_read_b64_tr_b16 a[62:63], v71 offset:57344\n"
    "ds_read_b64_tr_b16 a[66:67], v70 offset:57344\n"
    "ds_read_b64_tr_b16 a[70:71], v69 offset:57344\n"
    "ds_read_b64_tr_b16 a[158:159], v68 offset:57344\n"
    "ds_read_b64_tr_b16 a[162:163], v67 offset:57344\n"
    "ds_read_b64_tr_b16 a[166:167], v66 offset:57344\n"
    "ds_read_b64_tr_b16 a[254:255], v65 offset:57344\n"
    "ds_read_b64_tr_b16 v[130:131], v64 offset:57344\n"
    "ds_read_b64_tr_b16 v[134:135], v63 offset:57344\n"
    "ds_read_b64_tr_b16 v[222:223], v62 offset:57344\n"
    "ds_read_b64_tr_b16 v[226:227], v61 offset:57344\n"
    "ds_read_b64_tr_b16 v[230:231], v60 offset:57344\n"
    "s_waitcnt vmcnt(18) lgkmcnt(0)\n"
    "s_barrier\n"
    "ds_read_b64_tr_b16 a[72:73], v32\n"
    "ds_read_b64_tr_b16 a[76:77], v33\n"
    "ds_read_b64_tr_b16 a[80:81], v34\n"
    "ds_read_b64_tr_b16 a[168:169], v35\n"
    "ds_read_b64_tr_b16 a[172:173], v36\n"
    "ds_read_b64_tr_b16 a[176:177], v37\n"
    "ds_read_b64_tr_b16 v[136:137], v38\n"
    "ds_read_b64_tr_b16 v[140:141], v39\n"
    "ds_read_b64_tr_b16 v[144:145], v40\n"
    "ds_read_b64_tr_b16 v[232:233], v41\n"
    "ds_read_b64_tr_b16 v[236:237], v42\n"
    "ds_read_b64_tr_b16 v[240:241], v43\n"
    "s_waitcnt vmcnt(12) lgkmcnt(0)\n"
    "s_barrier\n"
    "ds_read_b64_tr_b16 a[74:75], v32 offset:24576\n"
    "ds_read_b64_tr_b16 a[78:79], v33 offset:24576\n"
    "ds_read_b64_tr_b16 a[82:83], v34 offset:24576\n"
    "ds_read_b64_tr_b16 a[170:171], v35 offset:24576\n"
    "ds_read_b64_tr_b16 a[174:175], v36 offset:24576\n"
    "ds_read_b64_tr_b16 a[178:179], v37 offset:24576\n"
    "ds_read_b64_tr_b16 v[138:139], v38 offset:24576\n"
    "ds_read_b64_tr_b16 v[142:143], v39 offset:24576\n"
    "ds_read_b64_tr_b16 v[146:147], v40 offset:24576\n"
    "ds_read_b64_tr_b16 v[234:235], v41 offset:24576\n"
    "ds_read_b64_tr_b16 v[238:239], v42 offset:24576\n"
    "ds_read_b64_tr_b16 v[242:243], v43 offset:24576\n"
    "s_waitcnt vmcnt(6) lgkmcnt(0)\n"
    "s_barrier\n"
    "ds_read_b64_tr_b16 a[84:85], v32 offset:49152\n"
    "ds_read_b64_tr_b16 a[88:89], v33 offset:49152\n"
    "ds_read_b64_tr_b16 a[92:93], v34 offset:49152\n"
    "ds_read_b64_tr_b16 a[180:181], v35 offset:49152\n"
    "ds_read_b64_tr_b16 a[184:185], v36 offset:49152\n"
    "ds_read_b64_tr_b16 a[188:189], v37 offset:49152\n"
    "ds_read_b64_tr_b16 v[148:149], v38 offset:49152\n"
    "ds_read_b64_tr_b16 v[152:153], v39 offset:49152\n"
    "ds_read_b64_tr_b16 v[156:157], v40 offset:49152\n"
    "ds_read_b64_tr_b16 v[244:245], v41 offset:49152\n"
    "ds_read_b64_tr_b16 v[248:249], v42 offset:49152\n"
    "ds_read_b64_tr_b16 v[252:253], v43 offset:49152\n"
    "s_waitcnt vmcnt(0) lgkmcnt(0)\n"
    "s_barrier\n"
    "ds_read_b64_tr_b16 a[86:87], v71 offset:8192\n"
    "ds_read_b64_tr_b16 a[90:91], v70 offset:8192\n"
    "ds_read_b64_tr_b16 a[94:95], v69 offset:8192\n"
    "ds_read_b64_tr_b16 a[182:183], v68 offset:8192\n"
    "ds_read_b64_tr_b16 a[186:187], v67 offset:8192\n"
    "ds_read_b64_tr_b16 a[190:191], v66 offset:8192\n"
    "ds_read_b64_tr_b16 v[150:151], v65 offset:8192\n"
    "ds_read_b64_tr_b16 v[154:155], v64 offset:8192\n"
    "ds_read_b64_tr_b16 v[158:159], v63 offset:8192\n"
    "ds_read_b64_tr_b16 v[246:247], v62 offset:8192\n"
    "ds_read_b64_tr_b16 v[250:251], v61 offset:8192\n"
    "ds_read_b64_tr_b16 v[254:255], v60 offset:8192\n"
    "s_waitcnt lgkmcnt(0)\n"
    "s_barrier\n"
    "v_mov_b32 v20, 64\n"
    "global_load_dword v21, v20, %1 sc1\n"
    "s_mov_b32 s52, 0\n"
    ".Lwp1_%=:\n"
    "s_waitcnt vmcnt(0)\n"
    "v_cmp_eq_u32 vcc, 0, v21\n"
    "s_cmp_eq_u64 vcc, 0\n"
    "s_cbranch_scc1 .Lwp1_x%=\n"
    "s_add_u32 s52, s52, 1\n"
    "s_sleep 2\n"
    "global_load_dword v21, v20, %1 sc1\n"
    "s_cmp_lt_u32 s52, s53\n"
    "s_cbranch_scc1 .Lwp1_%=\n"
    "s_mov_b32 s53, 0\n"
    "v_mov_b32 v22, 0\n"
    "v_mov_b32 v23, 1\n"
    "global_store_dword v22, v23, %1 sc1\n"
    "s_waitcnt vmcnt(0)\n"
    ".Lwp1_x%=:\n"
    "v_readfirstlane_b32 s50, v21\n"
    "v_mbcnt_lo_u32_b32 v20, -1, 0\n"
    "v_mbcnt_hi_u32_b32 v20, -1, v20\n"
    "v_min_u32 v6, 7, v20\n"
    "v_lshlrev_b32 v6, 7, v6\n"
    "s_lshl_b32 s51, %2, 6\n"
    "v_add_u32 v20, s51, v20\n"
    "v_min_u32 v20, 129, v20\n"
    "v_lshlrev_b32 v20, 5, v20\n"
    "v_add_u32 v7, 0x26c00, v20\n"
    "ds_read_b128 v[20:23], v7\n"
    "s_and_b32 s50, s50, 1\n"
    "s_waitcnt lgkmcnt(0)\n"
    "v_and_b32 v20, s50, v21\n"
    "v_lshlrev_b32 v20, 13, v20\n"
    "v_and_b32 v21, -2, v21\n"
    "v_xor_b32 v21, v20, v21\n"
    "v_and_b32 v20, s50, v22\n"
    "v_lshlrev_b32 v20, 13, v20\n"
    "v_and_b32 v22, -2, v22\n"
    "v_xor_b32 v22, v20, v22\n"
    "ds_write2_b32 v7, v21, v22 offset0:1 offset1:2\n"
    "s_waitcnt lgkmcnt(0)\n"
    "s_barrier\n"
    "v_mov_b32 v7, 0x26c00\n"
    "ds_read_b128 v[20:23], v7\n"
    "s_waitcnt lgkmcnt(0)\n"
    "v_readfirstlane_b32 s49, v21\n"
    "v_add_u32 v20, v20, v6\n"
    "s_branch .Lwwait_%=\n"
    ".Lwgo_%=:\n"
    "s_waitcnt vmcnt(1)\n"
    "v_mfma_f32_16x16x32_bf16 v[8:11], a[0:3], v[64:67], 0\n"
    "v_add_u32 v7, 32, v7\n"
    "ds_read_b128 v[20:23], v7\n"
    "v_mfma_f32_16x16x32_bf16 v[12:15], a[4:7], v[64:67], 0\n"
    "ds_write_b128 v1, v[64:67]\n"
    "ds_write_b128 v1, v[68:71] offset:1024\n"
    "v_mfma_f32_16x16x32_bf16 v[8:11], a[12:15], v[68:71], v[8:11]\n"
    "ds_write_b128 v1, v[72:75] offset:2048\n"
    "ds_write_b128 v1, v[76:79] offset:3072\n"
    "v_mfma_f32_16x16x32_bf16 v[12:15], a[16:19], v[68:71], v[12:15]\n"
    "v_cvt_pk_bf16_f32 v28, v16, v17\n"
    "v_cvt_pk_bf16_f32 v30, v16, v17\n"
    "v_mfma_f32_16x16x32_bf16 v[8:11], a[24:27], v[72:75], v[8:11]\n"
    "v_cvt_pk_bf16_f32 v29, v18, v19\n"
    "v_cvt_pk_bf16_f32 v31, v18, v19\n"
    "v_mfma_f32_16x16x32_bf16 v[12:15], a[28:31], v[72:75], v[12:15]\n"
    "ds_write_b128 v1, v[80:83] offset:4096\n"
    "ds_write_b128 v1, v[84:87] offset:5120\n"
    "v_mfma_f32_16x16x32_bf16 v[8:11], a[36:39], v[76:79], v[8:11]\n"
    "v_permlane16_swap_b32 v28, v30\n"
    "v_permlane16_swap_b32 v29, v31\n"
    "v_mfma_f32_16x16x32_bf16 v[12:15], a[40:43], v[76:79], v[12:15]\n"
    "ds_write_b128 v1, v[88:91] offset:6144\n"
    "ds_write_b128 v1, v[92:95] offset:7168\n"
    "v_mfma_f32_16x16x32_bf16 v[8:11], a[48:51], v[80:83], v[8:11]\n"
    "buffer_store_dwordx4 v[28:31], v3, s[40:43], s62 offen sc1\n"
    "v_xor_b32 v0, 0x2000, v1\n"
    "v_mfma_f32_16x16x32_bf16 v[12:15], a[52:55], v[80:83], v[12:15]\n"
    "s_waitcnt lgkmcnt(8)\n"
    "v_readfirstlane_b32 s49, v21\n"
    "v_mfma_f32_16x16x32_bf16 v[8:11], a[60:63], v[84:87], v[8:11]\n"
    "v_readfirstlane_b32 s62, v22\n"
    "v_add_u32 v20, v20, v6\n"
    "v_mfma_f32_16x16x32_bf16 v[12:15], a[64:67], v[84:87], v[12:15]\n"
    ".Lwtop_%=:\n"
    "v_mfma_f32_16x16x32_bf16 v[16:19], a[8:11], v[64:67], 0\n"
    "s_waitcnt lgkmcnt(0)\n"
    "s_barrier\n"
    "v_mfma_f32_16x16x32_bf16 v[16:19], a[20:23], v[68:71], v[16:19]\n"
    "ds_read_b128 v[32:35], v0\n"
    "ds_read_b128 v[36:39], v0 offset:1024\n"
    "v_mfma_f32_16x16x32_bf16 v[16:19], a[32:35], v[72:75], v[16:19]\n"
    "ds_read_b128 v[44:47], v0 offset:3072\n"
    "ds_read_b128 v[40:43], v0 offset:2048\n"
    "v_mfma_f32_16x16x32_bf16 v[16:19], a[44:47], v[76:79], v[16:19]\n"
    "ds_read_b128 v[52:55], v0 offset:5120\n"
    "ds_read_b128 v[48:51], v0 offset:4096\n"
    "v_mfma_f32_16x16x32_bf16 v[16:19], a[56:59], v[80:83], v[16:19]\n"
    "ds_read_b128 v[60:63], v0 offset:7168\n"
    "ds_read_b128 v[56:59], v0 offset:6144\n"
    "v_mfma_f32_16x16x32_bf16 v[16:19], a[68:71], v[84:87], v[16:19]\n"
    "global_load_dword v21, v20, %1 sc1\n"
    "v_mfma_f32_16x16x32_bf16 v[8:11], a[72:75], v[88:91], v[8:11]\n"
    "v_mfma_f32_16x16x32_bf16 v[12:15], a[76:79], v[88:91], v[12:15]\n"
    "v_mfma_f32_16x16x32_bf16 v[16:19], a[80:83], v[88:91], v[16:19]\n"
    "v_mfma_f32_16x16x32_bf16 v[8:11], a[84:87], v[92:95], v[8:11]\n"
    "v_mfma_f32_16x16x32_bf16 v[12:15], a[88:91], v[92:95], v[12:15]\n"
    "v_mfma_f32_16x16x32_bf16 v[16:19], a[92:95], v[92:95], v[16:19]\n"
    "s_waitcnt lgkmcnt(7)\n"
    "v_mfma_f32_16x16x32_bf16 v[8:11], a[96:99], v[32:35], v[8:11]\n"
    "v_mfma_f32_16x16x32_bf16 v[12:15], a[100:103], v[32:35], v[12:15]\n"
    "v_mfma_f32_16x16x32_bf16 v[16:19], a[104:107], v[32:35], v[16:19]\n"
    "s_waitcnt lgkmcnt(6)\n"
    "v_mfma_f32_16x16x32_bf16 v[8:11], a[108:111], v[36:39], v[8:11]\n"
    "v_mfma_f32_16x16x32_bf16 v[12:15], a[112:115], v[36:39], v[12:15]\n"
    "v_xor_b32 v0, 0x4000, v1\n"
    "v_mfma_f32_16x16x32_bf16 v[16:19], a[116:119], v[36:39], v[16:19]\n"
    "s_waitcnt lgkmcnt(4)\n"
    "v_mfma_f32_16x16x32_bf16 v[8:11], a[120:123], v[40:43], v[8:11]\n"
    "ds_read_b128 v[36:39], v0 offset:1024\n"
    "ds_read_b128 v[32:35], v0\n"
    "v_mfma_f32_16x16x32_bf16 v[12:15], a[124:127], v[40:43], v[12:15]\n"
    "v_mfma_f32_16x16x32_bf16 v[16:19], a[128:131], v[40:43], v[16:19]\n"
    "v_mfma_f32_16x16x32_bf16 v[8:11], a[132:135], v[44:47], v[8:11]\n"
    "v_mfma_f32_16x16x32_bf16 v[12:15], a[136:139], v[44:47], v[12:15]\n"
    "v_mfma_f32_16x16x32_bf16 v[16:19], a[140:143], v[44:47], v[16:19]\n"
    "s_waitcnt lgkmcnt(4)\n"
    "v_mfma_f32_16x16x32_bf16 v[8:11], a[144:147], v[48:51], v[8:11]\n"
    "ds_read_b128 v[44:47], v0 offset:3072\n"
    "ds_read_b128 v[40:43], v0 offset:2048\n"
    "v_mfma_f32_16x16x32_bf16 v[12:15], a[148:151], v[48:51], v[12:15]\n"
    "v_mfma_f32_16x16x32_bf16 v[16:19], a[152:155], v[48:51], v[16:19]\n"
    "v_mfma_f32_16x16x32_bf16 v[8:11], a[156:159], v[52:55], v[8:11]\n"
    "v_mfma_f32_16x16x32_bf16 v[12:15], a[160:163], v[52:55], v[12:15]\n"
    "v_mfma_f32_16x16x32_bf16 v[16:19], a[164:167], v[52:55], v[16:19]\n"
    "s_waitcnt lgkmcnt(4)\n"
    "v_mfma_f32_16x16x32_bf16 v[8:11], a[168:171], v[56:59], v[8:11]\n"
    "ds_read_b128 v[52:55], v0 offset:5120\n"
    "ds_read_b128 v[48:51], v0 offset:4096\n"
    "v_mfma_f32_16x16x32_bf16 v[12:15], a[172:175], v[56:59], v[12:15]\n"
    "v_mfma_f32_16x16x32_bf16 v[16:19], a[176:179], v[56:59], v[16:19]\n"
    "s_waitcnt vmcnt(0)\n"
    "v_mov_b32 v22, 1\n"
    "v_mfma_f32_16x16x32_bf16 v[8:11], a[180:183], v[60:63], v[8:11]\n"
    "s_mov_b64 exec, s[56:57]\n"
    "ds_add_rtn_u32 v22, v7, v22 offset:16\n"
    "s_mov_b64 exec, -1\n"
    "v_mfma_f32_16x16x32_bf16 v[12:15], a[184:187], v[60:63], v[12:15]\n"
    "v_cmp_eq_u32 vcc, 0, v21\n"
    "s_cmp_eq_u64 vcc, 0\n"
    "v_mfma_f32_16x16x32_bf16 v[16:19], a[188:191], v[60:63], v[16:19]\n"
    "s_waitcnt lgkmcnt(4)\n"
    "v_mfma_f32_16x16x32_bf16 v[8:11], a[192:195], v[32:35], v[8:11]\n"
    "ds_read_b128 v[60:63], v0 offset:7168\n"
    "ds_read_b128 v[56:59], v0 offset:6144\n"
    "v_mfma_f32_16x16x32_bf16 v[12:15], a[196:199], v[32:35], v[12:15]\n"
    "s_cselect_b32 s94, s42, 0\n"
    "v_mfma_f32_16x16x32_bf16 v[16:19], a[200:203], v[32:35], v[16:19]\n"
    "global_load_dword v21, v20, %1 sc1\n"
    "v_mfma_f32_16x16x32_bf16 v[8:11], a[204:207], v[36:39], v[8:11]\n"
    "buffer_load_dwordx4 v[64:67], v4, s[92:95], s49 offen sc1\n"
    "v_mfma_f32_16x16x32_bf16 v[12:15], a[208:211], v[36:39], v[12:15]\n"
    "v_xor_b32 v0, 0x6000, v1\n"
    "buffer_load_dwordx4 v[68:71], v4, s[92:95], s49 offen offset:1024 sc1\n"
    "v_mfma_f32_16x16x32_bf16 v[16:19], a[212:215], v[36:39], v[16:19]\n"
    "s_waitcnt lgkmcnt(4)\n"
    "v_mfma_f32_16x16x32_bf16 v[8:11], a[216:219], v[40:43], v[8:11]\n"
    "ds_read_b128 v[36:39], v0 offset:1024\n"
    "ds_read_b128 v[32:35], v0\n"
    "v_mfma_f32_16x16x32_bf16 v[12:15], a[220:223], v[40:43], v[12:15]\n"
    "s_waitcnt lgkmcnt(4)\n"
    "v_readfirstlane_b32 s50, v22\n"
    "v_mfma_f32_16x16x32_bf16 v[16:19], a[224:227], v[40:43], v[16:19]\n"
    "buffer_load_dwordx4 v[72:75], v4, s[92:95], s49 offen offset:2048 sc1\n"
    "s_cmp_eq_u32 s50, 3\n"
    "v_mfma_f32_16x16x32_bf16 v[8:11], a[228:231], v[44:47], v[8:11]\n"
    "s_cselect_b64 exec, 1, 0\n"
    "global_store_dword v23, v22, %1 sc1\n"
    "s_mov_b64 exec, -1\n"
    "v_mfma_f32_16x16x32_bf16 v[12:15], a[232:235], v[44:47], v[12:15]\n"
    "buffer_load_dwordx4 v[76:79], v4, s[92:95], s49 offen offset:3072 sc1\n"
    "v_mfma_f32_16x16x32_bf16 v[16:19], a[236:239], v[44:47], v[16:19]\n"
    "buffer_load_dwordx4 v[80:83], v5, s[92:95], s49 offen sc1\n"
    "v_mfma_f32_16x16x32_bf16 v[8:11], a[240:243], v[48:51], v[8:11]\n"
    "ds_read_b128 v[44:47], v0 offset:3072\n"
    "ds_read_b128 v[40:43], v0 offset:2048\n"
    "v_mfma_f32_16x16x32_bf16 v[12:15], a[244:247], v[48:51], v[12:15]\n"
    "buffer_load_dwordx4 v[84:87], v5, s[92:95], s49 offen offset:1024 sc1\n"
    "v_mfma_f32_16x16x32_bf16 v[16:19], a[248:251], v[48:51], v[16:19]\n"
    "buffer_load_dwordx4 v[88:91], v5, s[92:95], s49 offen offset:2048 sc1\n"
    "v_mfma_f32_16x16x32_bf16 v[8:11], a[252:255], v[52:55], v[8:11]\n"
    "buffer_load_dwordx4 v[92:95], v5, s[92:95], s49 offen offset:3072 sc1\n"
    "v_mfma_f32_16x16x32_bf16 v[12:15], v[128:131], v[52:55], v[12:15]\n"
    "v_mfma_f32_16x16x32_bf16 v[16:19], v[132:135], v[52:55], v[16:19]\n"
    "s_waitcnt lgkmcnt(4)\n"
    "v_mfma_f32_16x16x32_bf16 v[8:11], v[136:139], v[56:59], v[8:11]\n"
    "ds_read_b128 v[52:55], v0 offset:5120\n"
    "ds_read_b128 v[48:51], v0 offset:4096\n"
    "v_mfma_f32_16x16x32_bf16 v[12:15], v[140:143], v[56:59], v[12:15]\n"
    "v_mfma_f32_16x16x32_bf16 v[16:19], v[144:147], v[56:59], v[16:19]\n"
    "v_mfma_f32_16x16x32_bf16 v[8:11], v[148:151], v[60:63], v[8:11]\n"
    "v_mfma_f32_16x16x32_bf16 v[12:15], v[152:155], v[60:63], v[12:15]\n"
    "v_mfma_f32_16x16x32_bf16 v[16:19], v[156:159], v[60:63], v[16:19]\n"
    "s_waitcnt lgkmcnt(4)\n"
    "v_mfma_f32_16x16x32_bf16 v[8:11], v[160:163], v[32:35], v[8:11]\n"
    "ds_read_b128 v[60:63], v0 offset:7168\n"
    "ds_read_b128 v[56:59], v0 offset:6144\n"
    "v_mfma_f32_16x16x32_bf16 v[12:15], v[164:167], v[32:35], v[12:15]\n"
    "s_cmp_eq_u32 s94, 0\n"
    "s_cbranch_scc1 .Lwk2_%=\n"
    ".Lwk2_x%=:\n"
    "v_mfma_f32_16x16x32_bf16 v[16:19], v[168:171], v[32:35], v[16:19]\n"
    "v_mfma_f32_16x16x32_bf16 v[8:11], v[172:175], v[36:39], v[8:11]\n"
    "v_mfma_f32_16x16x32_bf16 v[12:15], v[176:179], v[36:39], v[12:15]\n"
    "v_mfma_f32_16x16x32_bf16 v[16:19], v[180:183], v[36:39], v[16:19]\n"
    "s_waitcnt lgkmcnt(4)\n"
    "v_mfma_f32_16x16x32_bf16 v[8:11], v[184:187], v[40:43], v[8:11]\n"
    "v_mfma_f32_16x16x32_bf16 v[12:15], v[188:191], v[40:43], v[12:15]\n"
    "v_mfma_f32_16x16x32_bf16 v[16:19], v[192:195], v[40:43], v[16:19]\n"
    "v_mfma_f32_16x16x32_bf16 v[8:11], v[196:199], v[44:47], v[8:11]\n"
    "v_xor_b32 v1, 0x8000, v1\n"
    "v_mfma_f32_16x16x32_bf16 v[12:15], v[200:203], v[44:47], v[12:15]\n"
    "s_waitcnt lgkmcnt(2)\n"
    "v_mfma_f32_16x16x32_bf16 v[8:11], v[208:211], v[48:51], v[8:11]\n"
    "v_mfma_f32_16x16x32_bf16 v[12:15], v[212:215], v[48:51], v[12:15]\n"
    "v_mfma_f32_16x16x32_bf16 v[8:11], v[220:223], v[52:55], v[8:11]\n"
    "v_mfma_f32_16x16x32_bf16 v[12:15], v[224:227], v[52:55], v[12:15]\n"
    "s_waitcnt lgkmcnt(0)\n"
    "v_mfma_f32_16x16x32_bf16 v[8:11], v[232:235], v[56:59], v[8:11]\n"
    "v_mfma_f32_16x16x32_bf16 v[12:15], v[236:239], v[56:59], v[12:15]\n"
    "v_mfma_f32_16x16x32_bf16 v[8:11], v[244:247], v[60:63], v[8:11]\n"
    "v_mfma_f32_16x16x32_bf16 v[12:15], v[248:251], v[60:63], v[12:15]\n"
    "v_mfma_f32_16x16x32_bf16 v[16:19], v[204:207], v[44:47], v[16:19]\n"
    "v_mfma_f32_16x16x32_bf16 v[16:19], v[216:219], v[48:51], v[16:19]\n"
    "v_cvt_pk_bf16_f32 v24, v8, v9\n"
    "v_cvt_pk_bf16_f32 v25, v10, v11\n"
    "v_mfma_f32_16x16x32_bf16 v[16:19], v[228:231], v[52:55], v[16:19]\n"
    "v_cvt_pk_bf16_f32 v26, v12, v13\n"
    "v_cvt_pk_bf16_f32 v27, v14, v15\n"
    "v_mfma_f32_16x16x32_bf16 v[16:19], v[240:243], v[56:59], v[16:19]\n"
    "buffer_store_dwordx4 v[24:27], v2, s[40:43], s62 offen sc1\n"
    "v_mfma_f32_16x16x32_bf16 v[16:19], v[252:255], v[60:63], v[16:19]\n"
    "s_mov_b32 s56, 1\n"
    "s_cmp_lg_u32 s94, 0\n"
    "s_cbranch_scc1 .Lwgo_%=\n"
    "s_nop 5\n"
    "v_cvt_pk_bf16_f32 v28, v16, v17\n"
    "v_cvt_pk_bf16_f32 v30, v16, v17\n"
    "v_cvt_pk_bf16_f32 v29, v18, v19\n"
    "v_cvt_pk_bf16_f32 v31, v18, v19\n"
    "v_permlane16_swap_b32 v28, v30\n"
    "s_nop 0\n"
    "v_permlane16_swap_b32 v29, v31\n"
    "buffer_store_dwordx4 v[28:31], v3, s[40:43], s62 offen sc1\n"
    "s_waitcnt vmcnt(2)\n"
    "v_cmp_eq_u32 vcc, 0, v21\n"
    "s_cmp_eq_u64 vcc, 0\n"
    "s_cbranch_scc1 .Lwld_%=\n"
    "ds_read_b32 v23, v7 offset:44\n"
    "global_load_dword v21, v20, %1 sc1\n"
    "s_waitcnt vmcnt(0)\n"
    "v_mov_b32 v22, 1\n"
    "s_mov_b64 exec, 1\n"
    "ds_add_rtn_u32 v22, v7, v22 offset:48\n"
    "s_mov_b64 exec, -1\n"
    "s_waitcnt lgkmcnt(0)\n"
    "v_readfirstlane_b32 s50, v22\n"
    "s_cmp_eq_u32 s50, 3\n"
    "s_cselect_b64 exec, 1, 0\n"
    "global_store_dword v23, v22, %1 sc1\n"
    "s_mov_b64 exec, -1\n"
    "s_mov_b32 s56, 0\n"
    "v_cmp_eq_u32 vcc, 0, v21\n"
    "s_cmp_eq_u64 vcc, 0\n"
    "s_cbranch_scc1 .Lwld_%=\n"
    "v_readfirstlane_b32 s50, v20\n"
    "s_cmp_eq_u32 s50, 192\n"
    "s_cbranch_scc1 .Lwend_%=\n"
    ".Lwwait_%=:\n"
    "global_load_dword v21, v20, %1 sc1\n"
    "s_mov_b32 s52, 0\n"
    ".Lwp3_%=:\n"
    "s_waitcnt vmcnt(0)\n"
    "v_cmp_eq_u32 vcc, 0, v21\n"
    "s_cmp_eq_u64 vcc, 0\n"
    "s_cbranch_scc1 .Lwp3_x%=\n"
    "s_add_u32 s52, s52, 1\n"
    "s_sleep 2\n"
    "global_load_dword v21, v20, %1 sc1\n"
    "s_cmp_lt_u32 s52, s53\n"
    "s_cbranch_scc1 .Lwp3_%=\n"
    "s_mov_b32 s53, 0\n"
    "v_mov_b32 v22, 0\n"
    "v_mov_b32 v23, 1\n"
    "global_store_dword v22, v23, %1 sc1\n"
    "s_waitcnt vmcnt(0)\n"
    ".Lwp3_x%=:\n"
    ".Lwld_%=:\n"
    "s_mov_b32 s94, s42\n"
    "buffer_load_dwordx4 v[64:67], v4, s[92:95], s49 offen sc1\n"
    "buffer_load_dwordx4 v[68:71], v4, s[92:95], s49 offen offset:1024 sc1\n"
    "buffer_load_dwordx4 v[72:75], v4, s[92:95], s49 offen offset:2048 sc1\n"
    "buffer_load_dwordx4 v[76:79], v4, s[92:95], s49 offen offset:3072 sc1\n"
    "buffer_load_dwordx4 v[80:83], v5, s[92:95], s49 offen sc1\n"
    "buffer_load_dwordx4 v[84:87], v5, s[92:95], s49 offen offset:1024 sc1\n"
    "buffer_load_dwordx4 v[88:91], v5, s[92:95], s49 offen offset:2048 sc1\n"
    "buffer_load_dwordx4 v[92:95], v5, s[92:95], s49 offen offset:3072 sc1\n"
    ".Lwin_%=:\n"
    "v_add_u32 v7, 32, v7\n"
    "ds_read_b128 v[20:23], v7\n"
    "s_waitcnt vmcnt(7)\n"
    "v_mfma_f32_16x16x32_bf16 v[8:11], a[0:3], v[64:67], 0\n"
    "v_mfma_f32_16x16x32_bf16 v[12:15], a[4:7], v[64:67], 0\n"
    "ds_write_b128 v1, v[64:67]\n"
    "s_waitcnt vmcnt(6)\n"
    "ds_write_b128 v1, v[68:71] offset:1024\n"
    "v_mfma_f32_16x16x32_bf16 v[8:11], a[12:15], v[68:71], v[8:11]\n"
    "s_waitcnt vmcnt(5)\n"
    "ds_write_b128 v1, v[72:75] offset:2048\n"
    "s_waitcnt vmcnt(4)\n"
    "ds_write_b128 v1, v[76:79] offset:3072\n"
    "v_mfma_f32_16x16x32_bf16 v[12:15], a[16:19], v[68:71], v[12:15]\n"
    "v_mfma_f32_16x16x32_bf16 v[8:11], a[24:27], v[72:75], v[8:11]\n"
    "v_mfma_f32_16x16x32_bf16 v[12:15], a[28:31], v[72:75], v[12:15]\n"
    "s_waitcnt vmcnt(3)\n"
    "ds_write_b128 v1, v[80:83] offset:4096\n"
    "s_waitcnt vmcnt(2)\n"
    "ds_write_b128 v1, v[84:87] offset:5120\n"
    "v_mfma_f32_16x16x32_bf16 v[8:11], a[36:39], v[76:79], v[8:11]\n"
    "v_mfma_f32_16x16x32_bf16 v[12:15], a[40:43], v[76:79], v[12:15]\n"
    "s_waitcnt vmcnt(1)\n"
    "ds_write_b128 v1, v[88:91] offset:6144\n"
    "s_waitcnt vmcnt(0)\n"
    "ds_write_b128 v1, v[92:95] offset:7168\n"
    "v_mfma_f32_16x16x32_bf16 v[8:11], a[48:51], v[80:83], v[8:11]\n"
    "v_xor_b32 v0, 0x2000, v1\n"
    "v_mfma_f32_16x16x32_bf16 v[12:15], a[52:55], v[80:83], v[12:15]\n"
    "s_waitcnt lgkmcnt(8)\n"
    "v_readfirstlane_b32 s49, v21\n"
    "v_mfma_f32_16x16x32_bf16 v[8:11], a[60:63], v[84:87], v[8:11]\n"
    "v_readfirstlane_b32 s62, v22\n"
    "v_add_u32 v20, v20, v6\n"
    "v_mfma_f32_16x16x32_bf16 v[12:15], a[64:67], v[84:87], v[12:15]\n"
    "s_cmp_eq_u32 s56, 0\n"
    "s_cbranch_scc1 .Lwc4_%=\n"
    "s_waitcnt vmcnt(0)\n"
    "v_mov_b32 v22, 1\n"
    "s_mov_b64 exec, 1\n"
    "ds_add_rtn_u32 v22, v7, v22 offset:16\n"
    "s_mov_b64 exec, -1\n"
    "s_waitcnt lgkmcnt(0)\n"
    "v_readfirstlane_b32 s50, v22\n"
    "s_cmp_eq_u32 s50, 3\n"
    "s_cselect_b64 exec, 1, 0\n"
    "global_store_dword v23, v22, %1 sc1\n"
    "s_mov_b64 exec, -1\n"
    "s_mov_b32 s56, 0\n"
    ".Lwc4_%=:\n"
    "s_branch .Lwtop_%=\n"
    ".Lwk2_%=:\n"
    "s_waitcnt vmcnt(8)\n"
    "v_cmp_eq_u32 vcc, 0, v21\n"
    "s_cmp_eq_u64 vcc, 0\n"
    "s_cselect_b32 s94, s42, 0\n"
    "global_load_dword v21, v20, %1 sc1\n"
    "buffer_load_dwordx4 v[64:67], v4, s[92:95], s49 offen sc1\n"
    "buffer_load_dwordx4 v[68:71], v4, s[92:95], s49 offen offset:1024 sc1\n"
    "buffer_load_dwordx4 v[72:75], v4, s[92:95], s49 offen offset:2048 sc1\n"
    "buffer_load_dwordx4 v[76:79], v4, s[92:95], s49 offen offset:3072 sc1\n"
    "buffer_load_dwordx4 v[80:83], v5, s[92:95], s49 offen sc1\n"
    "buffer_load_dwordx4 v[84:87], v5, s[92:95], s49 offen offset:1024 sc1\n"
    "buffer_load_dwordx4 v[88:91], v5, s[92:95], s49 offen offset:2048 sc1\n"
    "buffer_load_dwordx4 v[92:95], v5, s[92:95], s49 offen offset:3072 sc1\n"
    "s_branch .Lwk2_x%=\n"
    ".Lwend_%=:\n"
    "s_waitcnt vmcnt(0) lgkmcnt(0)\n"
    "s_endpgm\n"
    ".p2align 6\n"
    ".Lrole3_%=:\n"
    "s_mov_b64 s[40:41], %0\n"
    "s_and_b32 s41, s41, 0xffff\n"
    "s_mov_b32 s42, 0xbc00000\n"
    "s_mov_b32 s43, 0x20000\n"
    "s_mov_b64 s[92:93], %0\n"
    "s_and_b32 s93, s93, 0xffff\n"
    "s_mov_b32 s94, 0xbc00000\n"
    "s_mov_b32 s95, 0x20000\n"
    "s_mov_b32 s53, 131072\n"
    "s_mov_b64 s[56:57], 0\n"
    "v_mbcnt_lo_u32_b32 v92, -1, 0\n"
    "v_mbcnt_hi_u32_b32 v92, -1, v92\n"
    "s_lshl_b32 s50, %2, 8\n"
    "s_add_u32 s50, s50, 144384\n"
    "v_lshl_add_u32 v92, v92, 2, s50\n"
    "ds_read_b32 v0, v92\n"
    "ds_read_b32 v1, v92 offset:1024\n"
    "ds_read_b32 v2, v92 offset:2048\n"
    "ds_read_b32 v3, v92 offset:3072\n"
    "ds_read_b32 v4, v92 offset:4096\n"
    "ds_read_b32 v73, v92 offset:5120\n"
    "ds_read_b32 v6, v92 offset:6144\n"
    "ds_read_b32 v7, v92 offset:7168\n"
    "ds_read_b32 v8, v92 offset:8192\n"
    "ds_read_b32 v9, v92 offset:9216\n"
    "ds_read_b32 v10, v92 offset:10240\n"
    "ds_read_b32 v95, v92 offset:11264\n"
    "ds_read_b32 v90, v92 offset:12288\n"
    "ds_read_b32 v91, v92 offset:13312\n"
    "s_waitcnt lgkmcnt(0)\n"
    "s_barrier\n"
    "v_mbcnt_lo_u32_b32 v92, -1, 0\n"
    "v_mbcnt_hi_u32_b32 v92, -1, v92\n"
    "v_add_u32 v93, 0, v92\n"
    "v_mul_u32_u24 v94, 7282, v93\n"
    "v_lshrrev_b32 v94, 16, v94\n"
    "v_mul_u32_u24 v94, 3312, v94\n"
    "v_lshl_add_u32 v80, v93, 4, v94\n"
    "v_add_u32 v93, 64, v92\n"
    "v_mul_u32_u24 v94, 7282, v93\n"
    "v_lshrrev_b32 v94, 16, v94\n"
    "v_mul_u32_u24 v94, 3312, v94\n"
    "v_lshl_add_u32 v81, v93, 4, v94\n"
    "v_add_u32 v93, 128, v92\n"
    "v_mul_u32_u24 v94, 7282, v93\n"
    "v_lshrrev_b32 v94, 16, v94\n"
    "v_mul_u32_u24 v94, 3312, v94\n"
    "v_lshl_add_u32 v82, v93, 4, v94\n"
    "v_add_u32 v93, 192, v92\n"
    "v_mul_u32_u24 v94, 7282, v93\n"
    "v_lshrrev_b32 v94, 16, v94\n"
    "v_mul_u32_u24 v94, 3312, v94\n"
    "v_lshl_add_u32 v83, v93, 4, v94\n"
    "v_add_u32 v93, 256, v92\n"
    "v_mul_u32_u24 v94, 7282, v93\n"
    "v_lshrrev_b32 v94, 16, v94\n"
    "v_mul_u32_u24 v94, 3312, v94\n"
    "v_lshl_add_u32 v84, v93, 4, v94\n"
    "v_add_u32 v93, 320, v92\n"
    "v_mul_u32_u24 v94, 7282, v93\n"
    "v_lshrrev_b32 v94, 16, v94\n"
    "v_mul_u32_u24 v94, 3312, v94\n"
    "v_lshl_add_u32 v85, v93, 4, v94\n"
    "v_add_u32 v93, 384, v92\n"
    "v_mul_u32_u24 v94, 7282, v93\n"
    "v_lshrrev_b32 v94, 16, v94\n"
    "v_mul_u32_u24 v94, 3312, v94\n"
    "v_lshl_add_u32 v86, v93, 4, v94\n"
    "v_add_u32 v93, 448, v92\n"
    "v_mul_u32_u24 v94, 7282, v93\n"
    "v_lshrrev_b32 v94, 16, v94\n"
    "v_mul_u32_u24 v94, 3312, v94\n"
    "v_lshl_add_u32 v87, v93, 4, v94\n"
    "v_add_u32 v93, 512, v92\n"
    "v_mul_u32_u24 v94, 7282, v93\n"
    "v_lshrrev_b32 v94, 16, v94\n"
    "v_mul_u32_u24 v94, 3312, v94\n"
    "v_lshl_add_u32 v88, v93, 4, v94\n"
    "v_and_b32 v72, 48, v92\n"
    "v_add_u32 v5, 0x21c00, v73\n"
    "s_min_u32 s50, %2, 2\n"
    "s_mul_i32 s54, s50, 9216\n"
    "s_mul_i32 s50, s50, 1152\n"
    "s_add_u32 s51, s50, %6\n"
    "s_mov_b64 s[44:45], %8\n"
    "s_and_b32 s45, s45, 0xffff\n"
    "s_mov_b32 s46, 0x1e6000\n"
    "s_mov_b32 s47, 0x20000\n"
    "v_mov_b32 v56, 0x3f803f80\n"
    "v_mov_b32 v57, 0x3f803f80\n"
    "v_mov_b32 v58, 0x3f803f80\n"
    "v_mov_b32 v59, 0x3f803f80\n"
    "s_cmp_eq_u32 %2, 3\n"
    "s_cbranch_scc1 .Lac1_%=\n"
    "s_add_u32 s55, s51, 0\n"
    "s_add_u32 m0, s54, 0\n"
    "s_nop 0\n"
    "buffer_load_dwordx4 v80, s[44:47], s55 offen lds\n"
    "s_add_u32 m0, s54, 1024\n"
    "s_nop 0\n"
    "buffer_load_dwordx4 v81, s[44:47], s55 offen lds\n"
    "s_add_u32 m0, s54, 2048\n"
    "s_nop 0\n"
    "buffer_load_dwordx4 v82, s[44:47], s55 offen lds\n"
    "s_add_u32 m0, s54, 3072\n"
    "s_nop 0\n"
    "buffer_load_dwordx4 v83, s[44:47], s55 offen lds\n"
    "s_add_u32 m0, s54, 4096\n"
    "s_nop 0\n"
    "buffer_load_dwordx4 v84, s[44:47], s55 offen lds\n"
    "s_add_u32 m0, s54, 5120\n"
    "s_nop 0\n"
    "buffer_load_dwordx4 v85, s[44:47], s55 offen lds\n"
    "s_add_u32 m0, s54, 6144\n"
    "s_nop 0\n"
    "buffer_load_dwordx4 v86, s[44:47], s55 offen lds\n"
    "s_add_u32 m0, s54, 7168\n"
    "s_nop 0\n"
    "buffer_load_dwordx4 v87, s[44:47], s55 offen lds\n"
    "s_add_u32 m0, s54, 8192\n"
    "s_nop 0\n"
    "buffer_load_dwordx4 v88, s[44:47], s55 offen lds\n"
    ".Lac1_%=:\n"
    "s_cmp_eq_u32 %2, 3\n"
    "s_cbranch_scc1 .Lac2_%=\n"
    "s_add_u32 s55, s51, 221184\n"
    "s_add_u32 m0, s54, 27648\n"
    "s_nop 0\n"
    "buffer_load_dwordx4 v80, s[44:47], s55 offen lds\n"
    "s_add_u32 m0, s54, 28672\n"
    "s_nop 0\n"
    "buffer_load_dwordx4 v81, s[44:47], s55 offen lds\n"
    "s_add_u32 m0, s54, 29696\n"
    "s_nop 0\n"
    "buffer_load_dwordx4 v82, s[44:47], s55 offen lds\n"
    "s_add_u32 m0, s54, 30720\n"
    "s_nop 0\n"
    "buffer_load_dwordx4 v83, s[44:47], s55 offen lds\n"
    "s_add_u32 m0, s54, 31744\n"
    "s_nop 0\n"
    "buffer_load_dwordx4 v84, s[44:47], s55 offen lds\n"
    "s_add_u32 m0, s54, 32768\n"
    "s_nop 0\n"
    "buffer_load_dwordx4 v85, s[44:47], s55 offen lds\n"
    "s_add_u32 m0, s54, 33792\n"
    "s_nop 0\n"
    "buffer_load_dwordx4 v86, s[44:47], s55 offen lds\n"
    "s_add_u32 m0, s54, 34816\n"
    "s_nop 0\n"
    "buffer_load_dwordx4 v87, s[44:47], s55 offen lds\n"
    "s_add_u32 m0, s54, 35840\n"
    "s_nop 0\n"
    "buffer_load_dwordx4 v88, s[44:47], s55 offen lds\n"
    ".Lac2_%=:\n"
    "s_cmp_eq_u32 %2, 3\n"
    "s_cbranch_scc1 .Lac3_%=\n"
    "s_add_u32 s55, s51, 442368\n"
    "s_add_u32 m0, s54, 55296\n"
    "s_nop 0\n"
    "buffer_load_dwordx4 v80, s[44:47], s55 offen lds\n"
    "s_add_u32 m0, s54, 56320\n"
    "s_nop 0\n"
    "buffer_load_dwordx4 v81, s[44:47], s55 offen lds\n"
    "s_add_u32 m0, s54, 57344\n"
    "s_nop 0\n"
    "buffer_load_dwordx4 v82, s[44:47], s55 offen lds\n"
    "s_add_u32 m0, s54, 58368\n"
    "s_nop 0\n"
    "buffer_load_dwordx4 v83, s[44:47], s55 offen lds\n"
    "s_add_u32 m0, s54, 59392\n"
    "s_nop 0\n"
    "buffer_load_dwordx4 v84, s[44:47], s55 offen lds\n"
    "s_add_u32 m0, s54, 60416\n"
    "s_nop 0\n"
    "buffer_load_dwordx4 v85, s[44:47], s55 offen lds\n"
    "s_add_u32 m0, s54, 61440\n"
    "s_nop 0\n"
    "buffer_load_dwordx4 v86, s[44:47], s55 offen lds\n"
    "s_add_u32 m0, s54, 62464\n"
    "s_nop 0\n"
    "buffer_load_dwordx4 v87, s[44:47], s55 offen lds\n"
    "s_add_u32 m0, s54, 63488\n"
    "s_nop 0\n"
    "buffer_load_dwordx4 v88, s[44:47], s55 offen lds\n"
    ".Lac3_%=:\n"
    "s_cmp_eq_u32 %2, 3\n"
    "s_cbranch_scc1 .Lac4_%=\n"
    "s_add_u32 s55, s51, 663552\n"
    "s_add_u32 m0, s54, 82944\n"
    "s_nop 0\n"
    "buffer_load_dwordx4 v80, s[44:47], s55 offen lds\n"
    "s_add_u32 m0, s54, 83968\n"
    "s_nop 0\n"
    "buffer_load_dwordx4 v81, s[44:47], s55 offen lds\n"
    "s_add_u32 m0, s54, 84992\n"
    "s_nop 0\n"
    "buffer_load_dwordx4 v82, s[44:47], s55 offen lds\n"
    "s_add_u32 m0, s54, 86016\n"
    "s_nop 0\n"
    "buffer_load_dwordx4 v83, s[44:47], s55 offen lds\n"
    "s_add_u32 m0, s54, 87040\n"
    "s_nop 0\n"
    "buffer_load_dwordx4 v84, s[44:47], s55 offen lds\n"
    "s_add_u32 m0, s54, 88064\n"
    "s_nop 0\n"
    "buffer_load_dwordx4 v85, s[44:47], s55 offen lds\n"
    "s_add_u32 m0, s54, 89088\n"
    "s_nop 0\n"
    "buffer_load_dwordx4 v86, s[44:47], s55 offen lds\n"
    "s_add_u32 m0, s54, 90112\n"
    "s_nop 0\n"
    "buffer_load_dwordx4 v87, s[44:47], s55 offen lds\n"
    "s_add_u32 m0, s54, 91136\n"
    "s_nop 0\n"
    "buffer_load_dwordx4 v88, s[44:47], s55 offen lds\n"
    ".Lac4_%=:\n"
    "s_cmp_eq_u32 %2, 3\n"
    "s_cbranch_scc1 .Lac5_%=\n"
    "s_add_u32 s55, s51, 884736\n"
    "s_add_u32 m0, s54, 110592\n"
    "s_nop 0\n"
    "buffer_load_dwordx4 v80, s[44:47], s55 offen lds\n"
    "s_add_u32 m0, s54, 111616\n"
    "s_nop 0\n"
    "buffer_load_dwordx4 v81, s[44:47], s55 offen lds\n"
    "s_add_u32 m0, s54, 112640\n"
    "s_nop 0\n"
    "buffer_load_dwordx4 v82, s[44:47], s55 offen lds\n"
    "s_add_u32 m0, s54, 113664\n"
    "s_nop 0\n"
    "buffer_load_dwordx4 v83, s[44:47], s55 offen lds\n"
    "s_add_u32 m0, s54, 114688\n"
    "s_nop 0\n"
    "buffer_load_dwordx4 v84, s[44:47], s55 offen lds\n"
    "s_add_u32 m0, s54, 115712\n"
    "s_nop 0\n"
    "buffer_load_dwordx4 v85, s[44:47], s55 offen lds\n"
    "s_add_u32 m0, s54, 116736\n"
    "s_nop 0\n"
    "buffer_load_dwordx4 v86, s[44:47], s55 offen lds\n"
    "s_add_u32 m0, s54, 117760\n"
    "s_nop 0\n"
    "buffer_load_dwordx4 v87, s[44:47], s55 offen lds\n"
    "s_add_u32 m0, s54, 118784\n"
    "s_nop 0\n"
    "buffer_load_dwordx4 v88, s[44:47], s55 offen lds\n"
    ".Lac5_%=:\n"
    "s_waitcnt vmcnt(36) lgkmcnt(0)\n"
    "s_barrier\n"
    "ds_read_b64_tr_b16 a[0:1], v90\n"
    "ds_read_b64_tr_b16 a[2:3], v90 offset:576\n"
    "ds_read_b64_tr_b16 a[72:73], v90 offset:9216\n"
    "ds_read_b64_tr_b16 a[74:75], v90 offset:9792\n"
    "ds_read_b64_tr_b16 a[144:145], v90 offset:18432\n"
    "ds_read_b64_tr_b16 a[146:147], v90 offset:19008\n"
    "ds_read_b64_tr_b16 a[216:217], v91\n"
    "ds_read_b64_tr_b16 a[218:219], v91 offset:576\n"
    "ds_read_b64_tr_b16 a[4:5], v90 offset:4608\n"
    "ds_read_b64_tr_b16 a[6:7], v90 offset:5184\n"
    "ds_read_b64_tr_b16 a[76:77], v90 offset:13824\n"
    "ds_read_b64_tr_b16 a[78:79], v90 offset:14400\n"
    "ds_read_b64_tr_b16 a[148:149], v90 offset:23040\n"
    "ds_read_b64_tr_b16 a[150:151], v90 offset:23616\n"
    "ds_read_b64_tr_b16 a[220:221], v91 offset:4608\n"
    "ds_read_b64_tr_b16 a[222:223], v91 offset:5184\n"
    "s_waitcnt vmcnt(27) lgkmcnt(0)\n"
    "s_barrier\n"
    "s_cmp_eq_u32 %2, 3\n"
    "s_cbranch_scc1 .Lac6_%=\n"
    "s_add_u32 s55, s51, 1105920\n"
    "s_add_u32 m0, s54, 0\n"
    "s_nop 0\n"
    "buffer_load_dwordx4 v80, s[44:47], s55 offen lds\n"
    "s_add_u32 m0, s54, 1024\n"
    "s_nop 0\n"
    "buffer_load_dwordx4 v81, s[44:47], s55 offen lds\n"
    "s_add_u32 m0, s54, 2048\n"
    "s_nop 0\n"
    "buffer_load_dwordx4 v82, s[44:47], s55 offen lds\n"
    "s_add_u32 m0, s54, 3072\n"
    "s_nop 0\n"
    "buffer_load_dwordx4 v83, s[44:47], s55 offen lds\n"
    "s_add_u32 m0, s54, 4096\n"
    "s_nop 0\n"
    "buffer_load_dwordx4 v84, s[44:47], s55 offen lds\n"
    "s_add_u32 m0, s54, 5120\n"
    "s_nop 0\n"
    "buffer_load_dwordx4 v85, s[44:47], s55 offen lds\n"
    "s_add_u32 m0, s54, 6144\n"
    "s_nop 0\n"
    "buffer_load_dwordx4 v86, s[44:47], s55 offen lds\n"
    "s_add_u32 m0, s54, 7168\n"
    "s_nop 0\n"
    "buffer_load_dwordx4 v87, s[44:47], s55 offen lds\n"
    "s_add_u32 m0, s54, 8192\n"
    "s_nop 0\n"
    "buffer_load_dwordx4 v88, s[44:47], s55 offen lds\n"
    ".Lac6_%=:\n"
    "v_mfma_f32_16x16x32_bf16 v[12:15], v[56:59], a[0:3], 0\n"
    "v_mfma_f32_16x16x32_bf16 v[16:19], v[56:59], a[72:75], 0\n"
    "v_mfma_f32_16x16x32_bf16 v[20:23], v[56:59], a[144:147], 0\n"
    "v_mfma_f32_16x16x32_bf16 v[24:27], v[56:59], a[216:219], 0\n"
    "v_mfma_f32_16x16x32_bf16 v[12:15], v[56:59], a[4:7], v[12:15]\n"
    "v_mfma_f32_16x16x32_bf16 v[16:19], v[56:59], a[76:79], v[16:19]\n"
    "v_mfma_f32_16x16x32_bf16 v[20:23], v[56:59], a[148:151], v[20:23]\n"
    "v_mfma_f32_16x16x32_bf16 v[24:27], v[56:59], a[220:223], v[24:27]\n"
    "ds_read_b64_tr_b16 a[8:9], v90 offset:27648\n"
    "ds_read_b64_tr_b16 a[10:11], v90 offset:28224\n"
    "ds_read_b64_tr_b16 a[80:81], v90 offset:36864\n"
    "ds_read_b64_tr_b16 a[82:83], v90 offset:37440\n"
    "ds_read_b64_tr_b16 a[152:153], v90 offset:46080\n"
    "ds_read_b64_tr_b16 a[154:155], v90 offset:46656\n"
    "ds_read_b64_tr_b16 a[224:225], v91 offset:27648\n"
    "ds_read_b64_tr_b16 a[226:227], v91 offset:28224\n"
    "ds_read_b64_tr_b16 a[12:13], v90 offset:32256\n"
    "ds_read_b64_tr_b16 a[14:15], v90 offset:32832\n"
    "ds_read_b64_tr_b16 a[84:85], v90 offset:41472\n"
    "ds_read_b64_tr_b16 a[86:87], v90 offset:42048\n"
    "ds_read_b64_tr_b16 a[156:157], v90 offset:50688\n"
    "ds_read_b64_tr_b16 a[158:159], v90 offset:51264\n"
    "ds_read_b64_tr_b16 a[228:229], v91 offset:32256\n"
    "ds_read_b64_tr_b16 a[230:231], v91 offset:32832\n"
    "s_waitcnt vmcnt(27) lgkmcnt(0)\n"
    "s_barrier\n"
    "s_cmp_eq_u32 %2, 3\n"
    "s_cbranch_scc1 .Lac7_%=\n"
    "s_add_u32 s55, s51, 1327104\n"
    "s_add_u32 m0, s54, 27648\n"
    "s_nop 0\n"
    "buffer_load_dwordx4 v80, s[44:47], s55 offen lds\n"
    "s_add_u32 m0, s54, 28672\n"
    "s_nop 0\n"
    "buffer_load_dwordx4 v81, s[44:47], s55 offen lds\n"
    "s_add_u32 m0, s54, 29696\n"
    "s_nop 0\n"
    "buffer_load_dwordx4 v82, s[44:47], s55 offen lds\n"
    "s_add_u32 m0, s54, 30720\n"
    "s_nop 0\n"
    "buffer_load_dwordx4 v83, s[44:47], s55 offen lds\n"
    "s_add_u32 m0, s54, 31744\n"
    "s_nop 0\n"
    "buffer_load_dwordx4 v84, s[44:47], s55 offen lds\n"
    "s_add_u32 m0, s54, 32768\n"
    "s_nop 0\n"
    "buffer_load_dwordx4 v85, s[44:47], s55 offen lds\n"
    "s_add_u32 m0, s54, 33792\n"
    "s_nop 0\n"
    "buffer_load_dwordx4 v86, s[44:47], s55 offen lds\n"
    "s_add_u32 m0, s54, 34816\n"
    "s_nop 0\n"
    "buffer_load_dwordx4 v87, s[44:47], s55 offen lds\n"
    "s_add_u32 m0, s54, 35840\n"
    "s_nop 0\n"
    "buffer_load_dwordx4 v88, s[44:47], s55 offen lds\n"
    ".Lac7_%=:\n"
    "v_mfma_f32_16x16x32_bf16 v[12:15], v[56:59], a[8:11], v[12:15]\n"
    "v_mfma_f32_16x16x32_bf16 v[16:19], v[56:59], a[80:83], v[16:19]\n"
    "v_mfma_f32_16x16x32_bf16 v[20:23], v[56:59], a[152:155], v[20:23]\n"
    "v_mfma_f32_16x16x32_bf16 v[24:27], v[56:59], a[224:227], v[24:27]\n"
    "v_mfma_f32_16x16x32_bf16 v[12:15], v[56:59], a[12:15], v[12:15]\n"
    "v_mfma_f32_16x16x32_bf16 v[16:19], v[56:59], a[84:87], v[16:19]\n"
    "v_mfma_f32_16x16x32_bf16 v[20:23], v[56:59], a[156:159], v[20:23]\n"
    "v_mfma_f32_16x16x32_bf16 v[24:27], v[56:59], a[228:231], v[24:27]\n"
    "ds_read_b64_tr_b16 a[16:17], v90 offset:55296\n"
    "ds_read_b64_tr_b16 a[18:19], v90 offset:55872\n"
    "ds_read_b64_tr_b16 a[88:89], v90 offset:64512\n"
    "ds_read_b64_tr_b16 a[90:91], v90 offset:65088\n"
    "v_add_u32 v99, 0x10000, v90\n"
    "ds_read_b64_tr_b16 a[160:161], v99 offset:8192\n"
    "ds_read_b64_tr_b16 a[162:163], v99 offset:8768\n"
    "ds_read_b64_tr_b16 a[232:233], v91 offset:55296\n"
    "ds_read_b64_tr_b16 a[234:235], v91 offset:55872\n"
    "ds_read_b64_tr_b16 a[20:21], v90 offset:59904\n"
    "ds_read_b64_tr_b16 a[22:23], v90 offset:60480\n"
    "ds_read_b64_tr_b16 a[92:93], v99 offset:3584\n"
    "ds_read_b64_tr_b16 a[94:95], v99 offset:4160\n"
    "ds_read_b64_tr_b16 a[164:165], v99 offset:12800\n"
    "ds_read_b64_tr_b16 a[166:167], v99 offset:13376\n"
    "ds_read_b64_tr_b16 a[236:237], v91 offset:59904\n"
    "ds_read_b64_tr_b16 a[238:239], v91 offset:60480\n"
    "s_waitcnt vmcnt(27) lgkmcnt(0)\n"
    "s_barrier\n"
    "s_cmp_eq_u32 %2, 3\n"
    "s_cbranch_scc1 .Lac8_%=\n"
    "s_add_u32 s55, s51, 1548288\n"
    "s_add_u32 m0, s54, 55296\n"
    "s_nop 0\n"
    "buffer_load_dwordx4 v80, s[44:47], s55 offen lds\n"
    "s_add_u32 m0, s54, 56320\n"
    "s_nop 0\n"
    "buffer_load_dwordx4 v81, s[44:47], s55 offen lds\n"
    "s_add_u32 m0, s54, 57344\n"
    "s_nop 0\n"
    "buffer_load_dwordx4 v82, s[44:47], s55 offen lds\n"
    "s_add_u32 m0, s54, 58368\n"
    "s_nop 0\n"
    "buffer_load_dwordx4 v83, s[44:47], s55 offen lds\n"
    "s_add_u32 m0, s54, 59392\n"
    "s_nop 0\n"
    "buffer_load_dwordx4 v84, s[44:47], s55 offen lds\n"
    "s_add_u32 m0, s54, 60416\n"
    "s_nop 0\n"
    "buffer_load_dwordx4 v85, s[44:47], s55 offen lds\n"
    "s_add_u32 m0, s54, 61440\n"
    "s_nop 0\n"
    "buffer_load_dwordx4 v86, s[44:47], s55 offen lds\n"
    "s_add_u32 m0, s54, 62464\n"
    "s_nop 0\n"
    "buffer_load_dwordx4 v87, s[44:47], s55 offen lds\n"
    "s_add_u32 m0, s54, 63488\n"
    "s_nop 0\n"
    "buffer_load_dwordx4 v88, s[44:47], s55 offen lds\n"
    ".Lac8_%=:\n"
    "v_mfma_f32_16x16x32_bf16 v[12:15], v[56:59], a[16:19], v[12:15]\n"
    "v_mfma_f32_16x16x32_bf16 v[16:19], v[56:59], a[88:91], v[16:19]\n"
    "v_mfma_f32_16x16x32_bf16 v[20:23], v[56:59], a[160:163], v[20:23]\n"
    "v_mfma_f32_16x16x32_bf16 v[24:27], v[56:59], a[232:235], v[24:27]\n"
    "v_mfma_f32_16x16x32_bf16 v[12:15], v[56:59], a[20:23], v[12:15]\n"
    "v_mfma_f32_16x16x32_bf16 v[16:19], v[56:59], a[92:95], v[16:19]\n"
    "v_mfma_f32_16x16x32_bf16 v[20:23], v[56:59], a[164:167], v[20:23]\n"
    "v_mfma_f32_16x16x32_bf16 v[24:27], v[56:59], a[236:239], v[24:27]\n"
    "ds_read_b64_tr_b16 a[24:25], v99 offset:17408\n"
    "ds_read_b64_tr_b16 a[26:27], v99 offset:17984\n"
    "ds_read_b64_tr_b16 a[96:97], v99 offset:26624\n"
    "ds_read_b64_tr_b16 a[98:99], v99 offset:27200\n"
    "ds_read_b64_tr_b16 a[168:169], v99 offset:35840\n"
    "ds_read_b64_tr_b16 a[170:171], v99 offset:36416\n"
    "v_add_u32 v98, 0x10000, v91\n"
    "ds_read_b64_tr_b16 a[240:241], v98 offset:17408\n"
    "ds_read_b64_tr_b16 a[242:243], v98 offset:17984\n"
    "ds_read_b64_tr_b16 a[28:29], v99 offset:22016\n"
    "ds_read_b64_tr_b16 a[30:31], v99 offset:22592\n"
    "ds_read_b64_tr_b16 a[100:101], v99 offset:31232\n"
    "ds_read_b64_tr_b16 a[102:103], v99 offset:31808\n"
    "ds_read_b64_tr_b16 a[172:173], v99 offset:40448\n"
    "ds_read_b64_tr_b16 a[174:175], v99 offset:41024\n"
    "ds_read_b64_tr_b16 a[244:245], v98 offset:22016\n"
    "ds_read_b64_tr_b16 a[246:247], v98 offset:22592\n"
    "s_waitcnt vmcnt(27) lgkmcnt(0)\n"
    "s_barrier\n"
    "s_cmp_eq_u32 %2, 3\n"
    "s_cbranch_scc1 .Lac9_%=\n"
    "s_add_u32 s55, s51, 1769472\n"
    "s_add_u32 m0, s54, 82944\n"
    "s_nop 0\n"
    "buffer_load_dwordx4 v80, s[44:47], s55 offen lds\n"
    "s_add_u32 m0, s54, 83968\n"
    "s_nop 0\n"
    "buffer_load_dwordx4 v81, s[44:47], s55 offen lds\n"
    "s_add_u32 m0, s54, 84992\n"
    "s_nop 0\n"
    "buffer_load_dwordx4 v82, s[44:47], s55 offen lds\n"
    "s_add_u32 m0, s54, 86016\n"
    "s_nop 0\n"
    "buffer_load_dwordx4 v83, s[44:47], s55 offen lds\n"
    "s_add_u32 m0, s54, 87040\n"
    "s_nop 0\n"
    "buffer_load_dwordx4 v84, s[44:47], s55 offen lds\n"
    "s_add_u32 m0, s54, 88064\n"
    "s_nop 0\n"
    "buffer_load_dwordx4 v85, s[44:47], s55 offen lds\n"
    "s_add_u32 m0, s54, 89088\n"
    "s_nop 0\n"
    "buffer_load_dwordx4 v86, s[44:47], s55 offen lds\n"
    "s_add_u32 m0, s54, 90112\n"
    "s_nop 0\n"
    "buffer_load_dwordx4 v87, s[44:47], s55 offen lds\n"
    "s_add_u32 m0, s54, 91136\n"
    "s_nop 0\n"
    "buffer_load_dwordx4 v88, s[44:47], s55 offen lds\n"
    ".Lac9_%=:\n"
    "v_mfma_f32_16x16x32_bf16 v[12:15], v[56:59], a[24:27], v[12:15]\n"
    "v_mfma_f32_16x16x32_bf16 v[16:19], v[56:59], a[96:99], v[16:19]\n"
    "v_mfma_f32_16x16x32_bf16 v[20:23], v[56:59], a[168:171], v[20:23]\n"
    "v_mfma_f32_16x16x32_bf16 v[24:27], v[56:59], a[240:243], v[24:27]\n"
    "v_mfma_f32_16x16x32_bf16 v[12:15], v[56:59], a[28:31], v[12:15]\n"
    "v_mfma_f32_16x16x32_bf16 v[16:19], v[56:59], a[100:103], v[16:19]\n"
    "v_mfma_f32_16x16x32_bf16 v[20:23], v[56:59], a[172:175], v[20:23]\n"
    "v_mfma_f32_16x16x32_bf16 v[24:27], v[56:59], a[244:247], v[24:27]\n"
    "ds_read_b64_tr_b16 a[32:33], v99 offset:45056\n"
    "ds_read_b64_tr_b16 a[34:35], v99 offset:45632\n"
    "ds_read_b64_tr_b16 a[104:105], v99 offset:54272\n"
    "ds_read_b64_tr_b16 a[106:107], v99 offset:54848\n"
    "ds_read_b64_tr_b16 a[176:177], v99 offset:63488\n"
    "ds_read_b64_tr_b16 a[178:179], v99 offset:64064\n"
    "ds_read_b64_tr_b16 a[248:249], v98 offset:45056\n"
    "ds_read_b64_tr_b16 a[250:251], v98 offset:45632\n"
    "ds_read_b64_tr_b16 a[36:37], v99 offset:49664\n"
    "ds_read_b64_tr_b16 a[38:39], v99 offset:50240\n"
    "ds_read_b64_tr_b16 a[108:109], v99 offset:58880\n"
    "ds_read_b64_tr_b16 a[110:111], v99 offset:59456\n"
    "v_add_u32 v97, 0x20000, v90\n"
    "ds_read_b64_tr_b16 a[180:181], v97 offset:2560\n"
    "ds_read_b64_tr_b16 a[182:183], v97 offset:3136\n"
    "ds_read_b64_tr_b16 a[252:253], v98 offset:49664\n"
    "ds_read_b64_tr_b16 a[254:255], v98 offset:50240\n"
    "s_waitcnt vmcnt(27) lgkmcnt(0)\n"
    "s_barrier\n"
    "v_mfma_f32_16x16x32_bf16 v[12:15], v[56:59], a[32:35], v[12:15]\n"
    "v_mfma_f32_16x16x32_bf16 v[16:19], v[56:59], a[104:107], v[16:19]\n"
    "v_mfma_f32_16x16x32_bf16 v[20:23], v[56:59], a[176:179], v[20:23]\n"
    "v_mfma_f32_16x16x32_bf16 v[24:27], v[56:59], a[248:251], v[24:27]\n"
    "v_mfma_f32_16x16x32_bf16 v[12:15], v[56:59], a[36:39], v[12:15]\n"
    "v_mfma_f32_16x16x32_bf16 v[16:19], v[56:59], a[108:111], v[16:19]\n"
    "v_mfma_f32_16x16x32_bf16 v[20:23], v[56:59], a[180:183], v[20:23]\n"
    "v_mfma_f32_16x16x32_bf16 v[24:27], v[56:59], a[252:255], v[24:27]\n"
    "ds_read_b64_tr_b16 a[40:41], v90\n"
    "ds_read_b64_tr_b16 a[42:43], v90 offset:576\n"
    "ds_read_b64_tr_b16 a[112:113], v90 offset:9216\n"
    "ds_read_b64_tr_b16 a[114:115], v90 offset:9792\n"
    "ds_read_b64_tr_b16 a[184:185], v90 offset:18432\n"
    "ds_read_b64_tr_b16 a[186:187], v90 offset:19008\n"
    "ds_read_b64_tr_b16 v[224:225], v91\n"
    "ds_read_b64_tr_b16 v[226:227], v91 offset:576\n"
    "ds_read_b64_tr_b16 a[44:45], v90 offset:4608\n"
    "ds_read_b64_tr_b16 a[46:47], v90 offset:5184\n"
    "ds_read_b64_tr_b16 a[116:117], v90 offset:13824\n"
    "ds_read_b64_tr_b16 a[118:119], v90 offset:14400\n"
    "ds_read_b64_tr_b16 a[188:189], v90 offset:23040\n"
    "ds_read_b64_tr_b16 a[190:191], v90 offset:23616\n"
    "ds_read_b64_tr_b16 v[228:229], v91 offset:4608\n"
    "ds_read_b64_tr_b16 v[230:231], v91 offset:5184\n"
    "s_waitcnt vmcnt(18) lgkmcnt(0)\n"
    "s_barrier\n"
    "v_mfma_f32_16x16x32_bf16 v[12:15], v[56:59], a[40:43], v[12:15]\n"
    "v_mfma_f32_16x16x32_bf16 v[16:19], v[56:59], a[112:115], v[16:19]\n"
    "v_mfma_f32_16x16x32_bf16 v[20:23], v[56:59], a[184:187], v[20:23]\n"
    "v_mfma_f32_16x16x32_bf16 v[24:27], v[56:59], v[224:227], v[24:27]\n"
    "v_mfma_f32_16x16x32_bf16 v[12:15], v[56:59], a[44:47], v[12:15]\n"
    "v_mfma_f32_16x16x32_bf16 v[16:19], v[56:59], a[116:119], v[16:19]\n"
    "v_mfma_f32_16x16x32_bf16 v[20:23], v[56:59], a[188:191], v[20:23]\n"
    "v_mfma_f32_16x16x32_bf16 v[24:27], v[56:59], v[228:231], v[24:27]\n"
    "ds_read_b64_tr_b16 a[48:49], v90 offset:27648\n"
    "ds_read_b64_tr_b16 a[50:51], v90 offset:28224\n"
    "ds_read_b64_tr_b16 a[120:121], v90 offset:36864\n"
    "ds_read_b64_tr_b16 a[122:123], v90 offset:37440\n"
    "ds_read_b64_tr_b16 a[192:193], v90 offset:46080\n"
    "ds_read_b64_tr_b16 a[194:195], v90 offset:46656\n"
    "ds_read_b64_tr_b16 v[232:233], v91 offset:27648\n"
    "ds_read_b64_tr_b16 v[234:235], v91 offset:28224\n"
    "ds_read_b64_tr_b16 a[52:53], v90 offset:32256\n"
    "ds_read_b64_tr_b16 a[54:55], v90 offset:32832\n"
    "ds_read_b64_tr_b16 a[124:125], v90 offset:41472\n"
    "ds_read_b64_tr_b16 a[126:127], v90 offset:42048\n"
    "ds_read_b64_tr_b16 a[196:197], v90 offset:50688\n"
    "ds_read_b64_tr_b16 a[198:199], v90 offset:51264\n"
    "ds_read_b64_tr_b16 v[236:237], v91 offset:32256\n"
    "ds_read_b64_tr_b16 v[238:239], v91 offset:32832\n"
    "s_waitcnt vmcnt(9) lgkmcnt(0)\n"
    "s_barrier\n"
    "v_mfma_f32_16x16x32_bf16 v[12:15], v[56:59], a[48:51], v[12:15]\n"
    "v_mfma_f32_16x16x32_bf16 v[16:19], v[56:59], a[120:123], v[16:19]\n"
    "v_mfma_f32_16x16x32_bf16 v[20:23], v[56:59], a[192:195], v[20:23]\n"
    "v_mfma_f32_16x16x32_bf16 v[24:27], v[56:59], v[232:235], v[24:27]\n"
    "v_mfma_f32_16x16x32_bf16 v[12:15], v[56:59], a[52:55], v[12:15]\n"
    "v_mfma_f32_16x16x32_bf16 v[16:19], v[56:59], a[124:127], v[16:19]\n"
    "v_mfma_f32_16x16x32_bf16 v[20:23], v[56:59], a[196:199], v[20:23]\n"
    "v_mfma_f32_16x16x32_bf16 v[24:27], v[56:59], v[236:239], v[24:27]\n"
    "ds_read_b64_tr_b16 a[56:57], v90 offset:55296\n"
    "ds_read_b64_tr_b16 a[58:59], v90 offset:55872\n"
    "ds_read_b64_tr_b16 a[128:129], v90 offset:64512\n"
    "ds_read_b64_tr_b16 a[130:131], v90 offset:65088\n"
    "ds_read_b64_tr_b16 a[200:201], v99 offset:8192\n"
    "ds_read_b64_tr_b16 a[202:203], v99 offset:8768\n"
    "ds_read_b64_tr_b16 v[240:241], v91 offset:55296\n"
    "ds_read_b64_tr_b16 v[242:243], v91 offset:55872\n"
    "ds_read_b64_tr_b16 a[60:61], v90 offset:59904\n"
    "ds_read_b64_tr_b16 a[62:63], v90 offset:60480\n"
    "ds_read_b64_tr_b16 a[132:133], v99 offset:3584\n"
    "ds_read_b64_tr_b16 a[134:135], v99 offset:4160\n"
    "ds_read_b64_tr_b16 a[204:205], v99 offset:12800\n"
    "ds_read_b64_tr_b16 a[206:207], v99 offset:13376\n"
    "ds_read_b64_tr_b16 v[244:245], v91 offset:59904\n"
    "ds_read_b64_tr_b16 v[246:247], v91 offset:60480\n"
    "s_waitcnt vmcnt(0) lgkmcnt(0)\n"
    "s_barrier\n"
    "v_mfma_f32_16x16x32_bf16 v[12:15], v[56:59], a[56:59], v[12:15]\n"
    "v_mfma_f32_16x16x32_bf16 v[16:19], v[56:59], a[128:131], v[16:19]\n"
    "v_mfma_f32_16x16x32_bf16 v[20:23], v[56:59], a[200:203], v[20:23]\n"
    "v_mfma_f32_16x16x32_bf16 v[24:27], v[56:59], v[240:243], v[24:27]\n"
    "v_mfma_f32_16x16x32_bf16 v[12:15], v[56:59], a[60:63], v[12:15]\n"
    "v_mfma_f32_16x16x32_bf16 v[16:19], v[56:59], a[132:135], v[16:19]\n"
    "v_mfma_f32_16x16x32_bf16 v[20:23], v[56:59], a[204:207], v[20:23]\n"
    "v_mfma_f32_16x16x32_bf16 v[24:27], v[56:59], v[244:247], v[24:27]\n"
    "ds_read_b64_tr_b16 a[64:65], v99 offset:17408\n"
    "ds_read_b64_tr_b16 a[66:67], v99 offset:17984\n"
    "ds_read_b64_tr_b16 a[136:137], v99 offset:26624\n"
    "ds_read_b64_tr_b16 a[138:139], v99 offset:27200\n"
    "ds_read_b64_tr_b16 a[208:209], v99 offset:35840\n"
    "ds_read_b64_tr_b16 a[210:211], v99 offset:36416\n"
    "ds_read_b64_tr_b16 v[248:249], v98 offset:17408\n"
    "ds_read_b64_tr_b16 v[250:251], v98 offset:17984\n"
    "ds_read_b64_tr_b16 a[68:69], v99 offset:22016\n"
    "ds_read_b64_tr_b16 a[70:71], v99 offset:22592\n"
    "ds_read_b64_tr_b16 a[140:141], v99 offset:31232\n"
    "ds_read_b64_tr_b16 a[142:143], v99 offset:31808\n"
    "ds_read_b64_tr_b16 a[212:213], v99 offset:40448\n"
    "ds_read_b64_tr_b16 a[214:215], v99 offset:41024\n"
    "ds_read_b64_tr_b16 v[252:253], v98 offset:22016\n"
    "ds_read_b64_tr_b16 v[254:255], v98 offset:22592\n"
    "s_waitcnt lgkmcnt(0)\n"
    "s_barrier\n"
    "v_mfma_f32_16x16x32_bf16 v[12:15], v[56:59], a[64:67], v[12:15]\n"
    "v_mfma_f32_16x16x32_bf16 v[16:19], v[56:59], a[136:139], v[16:19]\n"
    "v_mfma_f32_16x16x32_bf16 v[20:23], v[56:59], a[208:211], v[20:23]\n"
    "v_mfma_f32_16x16x32_bf16 v[24:27], v[56:59], v[248:251], v[24:27]\n"
    "v_mfma_f32_16x16x32_bf16 v[12:15], v[56:59], a[68:71], v[12:15]\n"
    "v_mfma_f32_16x16x32_bf16 v[16:19], v[56:59], a[140:143], v[16:19]\n"
    "v_mfma_f32_16x16x32_bf16 v[20:23], v[56:59], a[212:215], v[20:23]\n"
    "v_mfma_f32_16x16x32_bf16 v[24:27], v[56:59], v[252:255], v[24:27]\n"
    "v_mov_b32 v28, 0\n"
    "v_mov_b32 v29, 0\n"
    "v_mov_b32 v30, 0\n"
    "v_mov_b32 v31, 0\n"
    "v_mov_b32 v32, 0\n"
    "v_mov_b32 v33, 0\n"
    "v_mov_b32 v34, 0\n"
    "v_mov_b32 v35, 0\n"
    "v_mov_b32 v36, 0\n"
    "v_mov_b32 v37, 0\n"
    "v_mov_b32 v38, 0\n"
    "v_mov_b32 v39, 0\n"
    "v_mov_b32 v172, 0\n"
    "v_mov_b32 v173, 0\n"
    "v_mov_b32 v174, 0\n"
    "v_mov_b32 v175, 0\n"
    "v_mov_b32 v176, 0\n"
    "v_mov_b32 v177, 0\n"
    "v_mov_b32 v178, 0\n"
    "v_mov_b32 v199, 0\n"
    "v_mov_b32 v215, 0\n"
    "v_mov_b32 v218, 0\n"
    "v_mov_b32 v219, 0\n"
    "v_mov_b32 v94, 0xf149f2ca\n"
    "v_add_u32 v93, 7, v95\n"
    "v_cmp_gt_u32 vcc, 8, v93\n"
    "s_nop 1\n"
    "v_cndmask_b32 v44, v94, v28, vcc\n"
    "v_cmp_le_i32 vcc, 9, v95\n"
    "s_nop 1\n"
    "v_cndmask_b32 v68, v94, v28, vcc\n"
    "v_add_u32 v93, 8, v95\n"
    "v_cmp_gt_u32 vcc, 8, v93\n"
    "s_nop 1\n"
    "v_cndmask_b32 v45, v94, v28, vcc\n"
    "v_cmp_le_i32 vcc, 8, v95\n"
    "s_nop 1\n"
    "v_cndmask_b32 v69, v94, v28, vcc\n"
    "v_add_u32 v93, 9, v95\n"
    "v_cmp_gt_u32 vcc, 8, v93\n"
    "s_nop 1\n"
    "v_cndmask_b32 v46, v94, v28, vcc\n"
    "v_cmp_le_i32 vcc, 7, v95\n"
    "s_nop 1\n"
    "v_cndmask_b32 v70, v94, v28, vcc\n"
    "v_add_u32 v93, 10, v95\n"
    "v_cmp_gt_u32 vcc, 8, v93\n"
    "s_nop 1\n"
    "v_cndmask_b32 v47, v94, v28, vcc\n"
    "v_cmp_le_i32 vcc, 6, v95\n"
    "s_nop 1\n"
    "v_cndmask_b32 v71, v94, v28, vcc\n"
    "s_mov_b32 s86, 0xcccccccc\n"
    "s_mov_b32 s87, 0xcccccccc\n"
    "v_cvt_pk_bf16_f32 v13, 0, v12\n"
    "v_sub_f32 v12, v12, v13\n"
    "v_cvt_pk_bf16_f32 v14, 0, v12\n"
    "v_sub_f32 v12, v12, v14\n"
    "v_cvt_pk_bf16_f32 v13, v13, v14\n"
    "v_cvt_pk_bf16_f32 v14, v12, 0\n"
    "s_mov_b64 vcc, 0xffff\n"
    "v_cndmask_b32 v60, 0, v13, vcc\n"
    "v_cndmask_b32 v61, 0, v14, vcc\n"
    "v_cvt_pk_bf16_f32 v17, 0, v16\n"
    "v_sub_f32 v16, v16, v17\n"
    "v_cvt_pk_bf16_f32 v18, 0, v16\n"
    "v_sub_f32 v16, v16, v18\n"
    "v_cvt_pk_bf16_f32 v17, v17, v18\n"
    "v_cvt_pk_bf16_f32 v18, v16, 0\n"
    "s_mov_b64 vcc, 0xffff\n"
    "v_cndmask_b32 v62, 0, v17, vcc\n"
    "v_cndmask_b32 v63, 0, v18, vcc\n"
    "v_cvt_pk_bf16_f32 v21, 0, v20\n"
    "v_sub_f32 v20, v20, v21\n"
    "v_cvt_pk_bf16_f32 v22, 0, v20\n"
    "v_sub_f32 v20, v20, v22\n"
    "v_cvt_pk_bf16_f32 v21, v21, v22\n"
    "v_cvt_pk_bf16_f32 v22, v20, 0\n"
    "s_mov_b64 vcc, 0xffff\n"
    "v_cndmask_b32 v64, 0, v21, vcc\n"
    "v_cndmask_b32 v65, 0, v22, vcc\n"
    "v_cvt_pk_bf16_f32 v25, 0, v24\n"
    "v_sub_f32 v24, v24, v25\n"
    "v_cvt_pk_bf16_f32 v26, 0, v24\n"
    "v_sub_f32 v24, v24, v26\n"
    "v_cvt_pk_bf16_f32 v25, v25, v26\n"
    "v_cvt_pk_bf16_f32 v26, v24, 0\n"
    "s_mov_b64 vcc, 0xffff\n"
    "v_cndmask_b32 v66, 0, v25, vcc\n"
    "v_cndmask_b32 v67, 0, v26, vcc\n"
    "v_mov_b32 v76, 64\n"
    "global_load_dword v77, v76, %1 sc1\n"
    "s_mov_b32 s52, 0\n"
    ".Lap10_%=:\n"
    "s_waitcnt vmcnt(0)\n"
    "v_cmp_eq_u32 vcc, 0, v77\n"
    "s_cmp_eq_u64 vcc, 0\n"
    "s_cbranch_scc1 .Lap10_x%=\n"
    "s_add_u32 s52, s52, 1\n"
    "s_sleep 2\n"
    "global_load_dword v77, v76, %1 sc1\n"
    "s_cmp_lt_u32 s52, s53\n"
    "s_cbranch_scc1 .Lap10_%=\n"
    "s_mov_b32 s53, 0\n"
    "v_mov_b32 v78, 0\n"
    "v_mov_b32 v79, 1\n"
    "global_store_dword v78, v79, %1 sc1\n"
    "s_waitcnt vmcnt(0)\n"
    ".Lap10_x%=:\n"
    "v_readfirstlane_b32 s50, v77\n"
    "v_mbcnt_lo_u32_b32 v76, -1, 0\n"
    "v_mbcnt_hi_u32_b32 v76, -1, v76\n"
    "v_min_u32 v220, 5, v76\n"
    "v_lshlrev_b32 v220, 7, v220\n"
    "s_lshl_b32 s51, %2, 6\n"
    "v_add_u32 v76, s51, v76\n"
    "v_min_u32 v76, 129, v76\n"
    "v_lshlrev_b32 v76, 5, v76\n"
    "v_add_u32 v11, 0x26c00, v76\n"
    "ds_read_b128 v[76:79], v11\n"
    "s_and_b32 s50, s50, 1\n"
    "s_waitcnt lgkmcnt(0)\n"
    "v_and_b32 v76, s50, v77\n"
    "v_lshlrev_b32 v76, 13, v76\n"
    "v_and_b32 v77, -2, v77\n"
    "v_xor_b32 v77, v76, v77\n"
    "v_and_b32 v76, s50, v78\n"
    "v_lshlrev_b32 v76, 13, v76\n"
    "v_and_b32 v78, -2, v78\n"
    "v_xor_b32 v78, v76, v78\n"
    "ds_write2_b32 v11, v77, v78 offset0:1 offset1:2\n"
    "s_waitcnt lgkmcnt(0)\n"
    "s_barrier\n"
    "v_mov_b32 v11, 0x26c00\n"
    "ds_read_b128 v[76:79], v11\n"
    "s_waitcnt lgkmcnt(0)\n"
    "v_readfirstlane_b32 s49, v77\n"
    "v_add_u32 v76, v76, v220\n"
    "s_branch .Lawait_%=\n"
    ".Lago_%=:\n"
    "s_waitcnt vmcnt(1)\n"
    "v_add_u32 v11, 32, v11\n"
    "ds_read_b128 v[76:79], v11\n"
    "ds_read2_b32 v[74:75], v11 offset0:5 offset1:6\n"
    "ds_write_b128 v1, v[152:155]\n"
    "v_mfma_f32_16x16x32_bf16 v[48:51], v[152:155], v[152:155], 0\n"
    "ds_write_b128 v1, v[156:159] offset:1024\n"
    "v_mfma_f32_16x16x32_bf16 v[48:51], v[156:159], v[156:159], v[48:51]\n"
    "ds_write_b128 v1, v[160:163] offset:2048\n"
    "v_mfma_f32_16x16x32_bf16 v[48:51], v[160:163], v[160:163], v[48:51]\n"
    "ds_write_b128 v1, v[164:167] offset:3072\n"
    "v_mfma_f32_16x16x32_bf16 v[48:51], v[164:167], v[164:167], v[48:51]\n"
    "ds_write_b128 v1, v[168:171] offset:4096\n"
    "v_mfma_f32_16x16x32_bf16 v[48:51], v[168:171], v[168:171], v[48:51]\n"
    "s_waitcnt lgkmcnt(5)\n"
    "v_readfirstlane_b32 s50, v75\n"
    "s_cmp_eq_u32 s50, 0\n"
    "s_cbranch_scc1 .Lac11_%=\n"
    "v_swap_b32 v28, v172\n"
    "v_swap_b32 v29, v173\n"
    "v_swap_b32 v30, v174\n"
    "v_swap_b32 v32, v175\n"
    "v_swap_b32 v33, v176\n"
    "v_swap_b32 v36, v177\n"
    "v_swap_b32 v37, v178\n"
    ".Lac11_%=:\n"
    "v_readfirstlane_b32 s49, v77\n"
    "v_readfirstlane_b32 s62, v78\n"
    "v_add_u32 v76, v76, v220\n"
    "s_waitcnt lgkmcnt(0)\n"
    ".Latop_%=:\n"
    "s_waitcnt lgkmcnt(0)\n"
    "s_barrier\n"
    "s_cmp_eq_u32 %2, 1\n"
    "s_cbranch_scc1 .Laq1_%=\n"
    "s_cmp_eq_u32 %2, 2\n"
    "s_cbranch_scc1 .Laq2_%=\n"
    "s_cmp_eq_u32 %2, 3\n"
    "s_cbranch_scc1 .Laq3_%=\n"
    "ds_read_b128 v[80:83], v0\n"
    "ds_read_b128 v[88:91], v0 offset:2048\n"
    "ds_read_b128 v[84:87], v0 offset:1024\n"
    "ds_read_b128 v[100:103], v0 offset:6144\n"
    "ds_read_b128 v[96:99], v0 offset:5120\n"
    "ds_read_b128 v[92:95], v0 offset:3072\n"
    "ds_read_b128 v[112:115], v0 offset:10240\n"
    "ds_read_b128 v[108:111], v0 offset:8192\n"
    "ds_read_b128 v[104:107], v0 offset:7168\n"
    "global_load_dword v77, v76, %1 sc1\n"
    "ds_write_b128 v73, v[48:51] offset:20480\n"
    "v_mfma_f32_16x16x32_bf16 v[52:55], v[56:59], v[152:155], 0\n"
    "v_mfma_f32_16x16x32_bf16 v[52:55], v[56:59], v[156:159], v[52:55]\n"
    "v_mfma_f32_16x16x32_bf16 v[52:55], v[56:59], v[160:163], v[52:55]\n"
    "v_mfma_f32_16x16x32_bf16 v[52:55], v[56:59], v[164:167], v[52:55]\n"
    "v_mfma_f32_16x16x32_bf16 v[52:55], v[56:59], v[168:171], v[52:55]\n"
    "s_waitcnt lgkmcnt(9)\n"
    "v_mfma_f32_16x16x32_bf16 v[24:27], a[216:219], v[80:83], 0\n"
    "ds_read_b128 v[116:119], v0 offset:11264\n"
    "ds_read_b128 v[120:123], v0 offset:12288\n"
    "s_waitcnt lgkmcnt(9)\n"
    "v_mfma_f32_16x16x32_bf16 v[24:27], a[220:223], v[84:87], v[24:27]\n"
    "ds_read_b128 v[124:127], v0 offset:13312\n"
    "ds_read_b128 v[128:131], v0 offset:15360\n"
    "v_mfma_f32_16x16x32_bf16 v[24:27], a[224:227], v[88:91], v[24:27]\n"
    "ds_write_b128 v73, v[52:55] offset:24576\n"
    "ds_read_b128 v[132:135], v0 offset:16384\n"
    "s_waitcnt lgkmcnt(10)\n"
    "v_mfma_f32_16x16x32_bf16 v[24:27], a[228:231], v[92:95], v[24:27]\n"
    "ds_read_b128 v[136:139], v0 offset:17408\n"
    "ds_read_b128 v[140:143], v0 offset:18432\n"
    "v_mfma_f32_16x16x32_bf16 v[24:27], a[232:235], v[96:99], v[24:27]\n"
    "ds_read_b128 v[144:147], v0 offset:4096\n"
    "ds_read_b128 v[148:151], v0 offset:9216\n"
    "v_mfma_f32_16x16x32_bf16 v[24:27], a[236:239], v[100:103], v[24:27]\n"
    "s_waitcnt lgkmcnt(11)\n"
    "v_mfma_f32_16x16x32_bf16 v[24:27], a[240:243], v[104:107], v[24:27]\n"
    "v_mfma_f32_16x16x32_bf16 v[24:27], a[244:247], v[108:111], v[24:27]\n"
    "s_waitcnt lgkmcnt(0)\n"
    "s_barrier\n"
    "v_mfma_f32_16x16x32_bf16 v[24:27], a[248:251], v[112:115], v[24:27]\n"
    "ds_read2st64_b32 v[48:49], v10 offset0:80 offset1:84\n"
    "ds_read2st64_b32 v[50:51], v10 offset0:88 offset1:92\n"
    "v_mfma_f32_16x16x32_bf16 v[12:15], a[0:3], v[80:83], 0\n"
    "ds_read2st64_b32 v[54:55], v10 offset0:104 offset1:108\n"
    "ds_read2st64_b32 v[52:53], v10 offset0:96 offset1:100\n"
    "v_mfma_f32_16x16x32_bf16 v[16:19], a[72:75], v[80:83], 0\n"
    "v_min_f32 v40, v74, v68\n"
    "v_min_f32 v41, v74, v69\n"
    "v_mfma_f32_16x16x32_bf16 v[12:15], a[4:7], v[84:87], v[12:15]\n"
    "v_min_f32 v42, v74, v70\n"
    "v_min_f32 v43, v74, v71\n"
    "v_mfma_f32_16x16x32_bf16 v[16:19], a[76:79], v[84:87], v[16:19]\n"
    "v_mfma_f32_16x16x32_bf16 v[12:15], a[8:11], v[88:91], v[12:15]\n"
    "s_waitcnt lgkmcnt(0)\n"
    "v_add_f32 v52, v52, v53\n"
    "v_add_f32 v54, v54, v55\n"
    "v_mfma_f32_16x16x32_bf16 v[16:19], a[80:83], v[88:91], v[16:19]\n"
    "v_add_f32 v48, v48, v49\n"
    "v_add_f32 v50, v50, v51\n"
    "v_mfma_f32_16x16x32_bf16 v[12:15], a[12:15], v[92:95], v[12:15]\n"
    "v_add_f32 v52, v52, v54\n"
    "v_add_f32 v50, v48, v50\n"
    "v_mfma_f32_16x16x32_bf16 v[16:19], a[84:87], v[92:95], v[16:19]\n"
    "v_mul_f32 v52, 0xbae38e39, v52\n"
    "v_mul_f32 v50, 0x3ae38e39, v50\n"
    "v_mfma_f32_16x16x32_bf16 v[12:15], a[16:19], v[96:99], v[12:15]\n"
    "v_cvt_pk_bf16_f32 v216, v52, v52\n"
    "v_fma_f32 v50, -v52, v52, v50\n"
    "v_mfma_f32_16x16x32_bf16 v[16:19], a[88:91], v[96:99], v[16:19]\n"
    "v_cvt_pk_bf16_f32 v217, v52, v52\n"
    "v_max_f32 v50, 0, v50\n"
    "v_mfma_f32_16x16x32_bf16 v[12:15], a[20:23], v[100:103], v[12:15]\n"
    "v_cvt_pk_bf16_f32 v50, 0, v50\n"
    "v_add_f32 v50, 0x3727c5ac, v50\n"
    "v_mfma_f32_16x16x32_bf16 v[16:19], a[92:95], v[100:103], v[16:19]\n"
    "v_mfma_f32_16x16x32_bf16 v[24:27], v[66:69], v[216:219], v[24:27]\n"
    "v_cvt_pk_bf16_f32 v50, 0, v50\n"
    "v_mfma_f32_16x16x32_bf16 v[12:15], a[24:27], v[104:107], v[12:15]\n"
    "v_sqrt_f32 v50, v50\n"
    "v_mfma_f32_16x16x32_bf16 v[16:19], a[96:99], v[104:107], v[16:19]\n"
    "v_cvt_pk_bf16_f32 v50, 0, v50\n"
    "v_rcp_f32 v50, v50\n"
    "v_mfma_f32_16x16x32_bf16 v[12:15], a[28:31], v[108:111], v[12:15]\n"
    "ds_write_b32 v9, v50\n"
    "v_mfma_f32_16x16x32_bf16 v[16:19], a[100:103], v[108:111], v[16:19]\n"
    "ds_read_b128 v[200:203], v72 offset:28672\n"
    "v_mfma_f32_16x16x32_bf16 v[12:15], v[60:63], v[216:219], v[12:15]\n"
    "v_mfma_f32_16x16x32_bf16 v[12:15], a[32:35], v[112:115], v[12:15]\n"
    "v_mfma_f32_16x16x32_bf16 v[16:19], v[62:65], v[216:219], v[16:19]\n"
    "v_mul_f32 v24, v24, v50\n"
    "v_mfma_f32_16x16x32_bf16 v[16:19], a[104:107], v[112:115], v[16:19]\n"
    "v_mul_f32 v25, v25, v50\n"
    "v_mul_f32 v26, v26, v50\n"
    "v_mfma_f32_16x16x32_bf16 v[12:15], a[36:39], v[116:119], v[12:15]\n"
    "v_mul_f32 v27, v27, v50\n"
    "ds_write_b128 v5, v[24:27]\n"
    "v_mfma_f32_16x16x32_bf16 v[16:19], a[108:111], v[116:119], v[16:19]\n"
    "v_mfma_f32_16x16x32_bf16 v[12:15], a[40:43], v[120:123], v[12:15]\n"
    "v_mfma_f32_16x16x32_bf16 v[16:19], a[112:115], v[120:123], v[16:19]\n"
    "v_mfma_f32_16x16x32_bf16 v[12:15], a[44:47], v[124:127], v[12:15]\n"
    "s_waitcnt vmcnt(0)\n"
    "v_mov_b32 v78, 1\n"
    "v_mfma_f32_16x16x32_bf16 v[16:19], a[116:119], v[124:127], v[16:19]\n"
    "s_mov_b64 exec, s[56:57]\n"
    "ds_add_rtn_u32 v78, v11, v78 offset:16\n"
    "s_mov_b64 exec, -1\n"
    "v_mfma_f32_16x16x32_bf16 v[12:15], a[48:51], v[128:131], v[12:15]\n"
    "s_waitcnt lgkmcnt(0)\n"
    "s_barrier\n"
    "v_mfma_f32_16x16x32_bf16 v[16:19], a[120:123], v[128:131], v[16:19]\n"
    "ds_read_b128 v[188:191], v8\n"
    "ds_read_b128 v[192:195], v8 offset:1024\n"
    "v_mfma_f32_16x16x32_bf16 v[12:15], a[52:55], v[132:135], v[12:15]\n"
    "ds_read2st64_b64 v[184:187], v7 offset1:2\n"
    "ds_read2st64_b64 v[180:183], v6 offset1:2\n"
    "v_cmp_eq_u32 vcc, 0, v77\n"
    "s_cmp_eq_u64 vcc, 0\n"
    "v_mfma_f32_16x16x32_bf16 v[16:19], a[124:127], v[132:135], v[16:19]\n"
    "s_cselect_b32 s94, s42, 0\n"
    "v_mfma_f32_16x16x32_bf16 v[12:15], a[56:59], v[136:139], v[12:15]\n"
    "global_load_dword v77, v76, %1 sc1\n"
    "v_mfma_f32_16x16x32_bf16 v[16:19], a[128:131], v[136:139], v[16:19]\n"
    "buffer_load_dwordx4 v[152:155], v3, s[92:95], s49 offen sc1\n"
    "v_mfma_f32_16x16x32_bf16 v[12:15], a[60:63], v[140:143], v[12:15]\n"
    "buffer_load_dwordx4 v[156:159], v3, s[92:95], s49 offen offset:1024 sc1\n"
    "v_mfma_f32_16x16x32_bf16 v[16:19], a[132:135], v[140:143], v[16:19]\n"
    "v_readfirstlane_b32 s50, v78\n"
    "v_mfma_f32_16x16x32_bf16 v[12:15], a[64:67], v[144:147], v[12:15]\n"
    "buffer_load_dwordx4 v[160:163], v3, s[92:95], s49 offen offset:2048 sc1\n"
    "s_cmp_eq_u32 s50, 3\n"
    "v_mfma_f32_16x16x32_bf16 v[16:19], a[136:139], v[144:147], v[16:19]\n"
    "s_waitcnt lgkmcnt(0)\n"
    "v_add_f32 v180, v180, v182\n"
    "v_add_f32 v181, v181, v183\n"
    "s_cselect_b64 exec, 1, 0\n"
    "global_store_dword v79, v78, %1 sc1\n"
    "s_mov_b64 exec, -1\n"
    "v_mfma_f32_16x16x32_bf16 v[12:15], a[68:71], v[148:151], v[12:15]\n"
    "v_cvt_pk_bf16_f32 v198, v180, v181\n"
    "v_add_f32 v184, v184, v186\n"
    "buffer_load_dwordx4 v[164:167], v3, s[92:95], s49 offen offset:3072 sc1\n"
    "v_mfma_f32_16x16x32_bf16 v[16:19], a[140:143], v[148:151], v[16:19]\n"
    "v_add_f32 v185, v185, v187\n"
    "v_cvt_pk_bf16_f32 v214, v184, v185\n"
    "buffer_load_dwordx4 v[168:171], v4, s[92:95], s49 offen sc1\n"
    "s_branch .Lak_%=\n"
    ".Laq1_%=:\n"
    "ds_read_b128 v[116:119], v0 offset:11264\n"
    "ds_read_b128 v[124:127], v0 offset:13312\n"
    "ds_read_b128 v[120:123], v0 offset:12288\n"
    "ds_read_b128 v[136:139], v0 offset:17408\n"
    "ds_read_b128 v[132:135], v0 offset:16384\n"
    "ds_read_b128 v[128:131], v0 offset:15360\n"
    "ds_read_b128 v[148:151], v0 offset:9216\n"
    "ds_read_b128 v[144:147], v0 offset:4096\n"
    "ds_read_b128 v[140:143], v0 offset:18432\n"
    "global_load_dword v77, v76, %1 sc1\n"
    "ds_write_b128 v73, v[48:51] offset:20480\n"
    "v_mfma_f32_16x16x32_bf16 v[52:55], v[56:59], v[152:155], 0\n"
    "v_mfma_f32_16x16x32_bf16 v[52:55], v[56:59], v[156:159], v[52:55]\n"
    "v_mfma_f32_16x16x32_bf16 v[52:55], v[56:59], v[160:163], v[52:55]\n"
    "v_mfma_f32_16x16x32_bf16 v[52:55], v[56:59], v[164:167], v[52:55]\n"
    "v_mfma_f32_16x16x32_bf16 v[52:55], v[56:59], v[168:171], v[52:55]\n"
    "s_waitcnt lgkmcnt(9)\n"
    "v_mfma_f32_16x16x32_bf16 v[24:27], a[252:255], v[116:119], 0\n"
    "ds_read_b128 v[80:83], v0\n"
    "ds_read_b128 v[84:87], v0 offset:1024\n"
    "s_waitcnt lgkmcnt(9)\n"
    "v_mfma_f32_16x16x32_bf16 v[24:27], v[224:227], v[120:123], v[24:27]\n"
    "ds_read_b128 v[88:91], v0 offset:2048\n"
    "ds_read_b128 v[92:95], v0 offset:3072\n"
    "v_mfma_f32_16x16x32_bf16 v[24:27], v[228:231], v[124:127], v[24:27]\n"
    "ds_write_b128 v73, v[52:55] offset:24576\n"
    "ds_read_b128 v[96:99], v0 offset:5120\n"
    "s_waitcnt lgkmcnt(10)\n"
    "v_mfma_f32_16x16x32_bf16 v[24:27], v[232:235], v[128:131], v[24:27]\n"
    "ds_read_b128 v[100:103], v0 offset:6144\n"
    "ds_read_b128 v[104:107], v0 offset:7168\n"
    "v_mfma_f32_16x16x32_bf16 v[24:27], v[236:239], v[132:135], v[24:27]\n"
    "ds_read_b128 v[108:111], v0 offset:8192\n"
    "ds_read_b128 v[112:115], v0 offset:10240\n"
    "v_mfma_f32_16x16x32_bf16 v[24:27], v[240:243], v[136:139], v[24:27]\n"
    "s_waitcnt lgkmcnt(11)\n"
    "v_mfma_f32_16x16x32_bf16 v[24:27], v[244:247], v[140:143], v[24:27]\n"
    "v_mfma_f32_16x16x32_bf16 v[24:27], v[248:251], v[144:147], v[24:27]\n"
    "s_waitcnt lgkmcnt(0)\n"
    "s_barrier\n"
    "v_mfma_f32_16x16x32_bf16 v[24:27], v[252:255], v[148:151], v[24:27]\n"
    "ds_read2st64_b32 v[48:49], v10 offset0:80 offset1:84\n"
    "ds_read2st64_b32 v[50:51], v10 offset0:88 offset1:92\n"
    "v_mfma_f32_16x16x32_bf16 v[12:15], a[36:39], v[116:119], 0\n"
    "ds_read2st64_b32 v[54:55], v10 offset0:104 offset1:108\n"
    "ds_read2st64_b32 v[52:53], v10 offset0:96 offset1:100\n"
    "v_mfma_f32_16x16x32_bf16 v[16:19], a[108:111], v[116:119], 0\n"
    "v_min_f32 v40, v74, v68\n"
    "v_min_f32 v41, v74, v69\n"
    "v_mfma_f32_16x16x32_bf16 v[12:15], a[40:43], v[120:123], v[12:15]\n"
    "v_min_f32 v42, v74, v70\n"
    "v_min_f32 v43, v74, v71\n"
    "v_mfma_f32_16x16x32_bf16 v[16:19], a[112:115], v[120:123], v[16:19]\n"
    "v_mfma_f32_16x16x32_bf16 v[12:15], a[44:47], v[124:127], v[12:15]\n"
    "s_waitcnt lgkmcnt(0)\n"
    "v_add_f32 v52, v52, v53\n"
    "v_add_f32 v54, v54, v55\n"
    "v_mfma_f32_16x16x32_bf16 v[16:19], a[116:119], v[124:127], v[16:19]\n"
    "v_add_f32 v48, v48, v49\n"
    "v_add_f32 v50, v50, v51\n"
    "v_mfma_f32_16x16x32_bf16 v[12:15], a[48:51], v[128:131], v[12:15]\n"
    "v_add_f32 v52, v52, v54\n"
    "v_add_f32 v50, v48, v50\n"
    "v_mfma_f32_16x16x32_bf16 v[16:19], a[120:123], v[128:131], v[16:19]\n"
    "v_mul_f32 v52, 0xbae38e39, v52\n"
    "v_mul_f32 v50, 0x3ae38e39, v50\n"
    "v_mfma_f32_16x16x32_bf16 v[12:15], a[52:55], v[132:135], v[12:15]\n"
    "v_cvt_pk_bf16_f32 v216, v52, v52\n"
    "v_fma_f32 v50, -v52, v52, v50\n"
    "v_mfma_f32_16x16x32_bf16 v[16:19], a[124:127], v[132:135], v[16:19]\n"
    "v_cvt_pk_bf16_f32 v217, v52, v52\n"
    "v_max_f32 v50, 0, v50\n"
    "v_mfma_f32_16x16x32_bf16 v[12:15], a[56:59], v[136:139], v[12:15]\n"
    "v_cvt_pk_bf16_f32 v50, 0, v50\n"
    "v_add_f32 v50, 0x3727c5ac, v50\n"
    "v_mfma_f32_16x16x32_bf16 v[16:19], a[128:131], v[136:139], v[16:19]\n"
    "v_cvt_pk_bf16_f32 v50, 0, v50\n"
    "v_sqrt_f32 v50, v50\n"
    "v_mfma_f32_16x16x32_bf16 v[12:15], a[60:63], v[140:143], v[12:15]\n"
    "v_cvt_pk_bf16_f32 v50, 0, v50\n"
    "v_mfma_f32_16x16x32_bf16 v[16:19], a[132:135], v[140:143], v[16:19]\n"
    "v_rcp_f32 v50, v50\n"
    "v_mfma_f32_16x16x32_bf16 v[12:15], a[64:67], v[144:147], v[12:15]\n"
    "ds_write_b32 v9, v50\n"
    "ds_read_b128 v[200:203], v72 offset:28672\n"
    "v_mfma_f32_16x16x32_bf16 v[16:19], a[136:139], v[144:147], v[16:19]\n"
    "v_mfma_f32_16x16x32_bf16 v[12:15], v[60:63], v[216:219], v[12:15]\n"
    "v_mfma_f32_16x16x32_bf16 v[16:19], v[62:65], v[216:219], v[16:19]\n"
    "v_mfma_f32_16x16x32_bf16 v[12:15], a[68:71], v[148:151], v[12:15]\n"
    "v_mul_f32 v24, v24, v50\n"
    "v_mul_f32 v25, v25, v50\n"
    "v_mfma_f32_16x16x32_bf16 v[16:19], a[140:143], v[148:151], v[16:19]\n"
    "v_mul_f32 v26, v26, v50\n"
    "v_mul_f32 v27, v27, v50\n"
    "v_mfma_f32_16x16x32_bf16 v[12:15], a[0:3], v[80:83], v[12:15]\n"
    "ds_write_b128 v5, v[24:27]\n"
    "v_mfma_f32_16x16x32_bf16 v[16:19], a[72:75], v[80:83], v[16:19]\n"
    "v_mfma_f32_16x16x32_bf16 v[12:15], a[4:7], v[84:87], v[12:15]\n"
    "v_mfma_f32_16x16x32_bf16 v[16:19], a[76:79], v[84:87], v[16:19]\n"
    "v_mfma_f32_16x16x32_bf16 v[12:15], a[8:11], v[88:91], v[12:15]\n"
    "s_waitcnt vmcnt(0)\n"
    "v_mov_b32 v78, 1\n"
    "v_mfma_f32_16x16x32_bf16 v[16:19], a[80:83], v[88:91], v[16:19]\n"
    "s_mov_b64 exec, s[56:57]\n"
    "ds_add_rtn_u32 v78, v11, v78 offset:16\n"
    "s_mov_b64 exec, -1\n"
    "v_mfma_f32_16x16x32_bf16 v[12:15], a[12:15], v[92:95], v[12:15]\n"
    "s_waitcnt lgkmcnt(0)\n"
    "s_barrier\n"
    "v_mfma_f32_16x16x32_bf16 v[16:19], a[84:87], v[92:95], v[16:19]\n"
    "ds_read_b128 v[188:191], v8\n"
    "ds_read_b128 v[192:195], v8 offset:1024\n"
    "v_mfma_f32_16x16x32_bf16 v[12:15], a[16:19], v[96:99], v[12:15]\n"
    "ds_read2st64_b64 v[184:187], v7 offset1:2\n"
    "ds_read2st64_b64 v[180:183], v6 offset1:2\n"
    "v_cmp_eq_u32 vcc, 0, v77\n"
    "s_cmp_eq_u64 vcc, 0\n"
    "v_mfma_f32_16x16x32_bf16 v[16:19], a[88:91], v[96:99], v[16:19]\n"
    "s_cselect_b32 s94, s42, 0\n"
    "v_mfma_f32_16x16x32_bf16 v[12:15], a[20:23], v[100:103], v[12:15]\n"
    "global_load_dword v77, v76, %1 sc1\n"
    "v_mfma_f32_16x16x32_bf16 v[16:19], a[92:95], v[100:103], v[16:19]\n"
    "buffer_load_dwordx4 v[152:155], v3, s[92:95], s49 offen sc1\n"
    "v_mfma_f32_16x16x32_bf16 v[12:15], a[24:27], v[104:107], v[12:15]\n"
    "buffer_load_dwordx4 v[156:159], v3, s[92:95], s49 offen offset:1024 sc1\n"
    "v_mfma_f32_16x16x32_bf16 v[16:19], a[96:99], v[104:107], v[16:19]\n"
    "v_readfirstlane_b32 s50, v78\n"
    "v_mfma_f32_16x16x32_bf16 v[12:15], a[28:31], v[108:111], v[12:15]\n"
    "buffer_load_dwordx4 v[160:163], v3, s[92:95], s49 offen offset:2048 sc1\n"
    "s_cmp_eq_u32 s50, 3\n"
    "v_mfma_f32_16x16x32_bf16 v[16:19], a[100:103], v[108:111], v[16:19]\n"
    "s_waitcnt lgkmcnt(0)\n"
    "v_add_f32 v180, v180, v182\n"
    "v_add_f32 v181, v181, v183\n"
    "s_cselect_b64 exec, 1, 0\n"
    "global_store_dword v79, v78, %1 sc1\n"
    "s_mov_b64 exec, -1\n"
    "v_mfma_f32_16x16x32_bf16 v[12:15], a[32:35], v[112:115], v[12:15]\n"
    "v_cvt_pk_bf16_f32 v198, v180, v181\n"
    "v_add_f32 v184, v184, v186\n"
    "buffer_load_dwordx4 v[164:167], v3, s[92:95], s49 offen offset:3072 sc1\n"
    "v_mfma_f32_16x16x32_bf16 v[16:19], a[104:107], v[112:115], v[16:19]\n"
    "v_add_f32 v185, v185, v187\n"
    "v_cvt_pk_bf16_f32 v214, v184, v185\n"
    "buffer_load_dwordx4 v[168:171], v4, s[92:95], s49 offen sc1\n"
    "s_branch .Lak_%=\n"
    ".Laq2_%=:\n"
    "ds_read_b128 v[80:83], v0\n"
    "ds_read_b128 v[88:91], v0 offset:2048\n"
    "ds_read_b128 v[84:87], v0 offset:1024\n"
    "ds_read_b128 v[100:103], v0 offset:6144\n"
    "ds_read_b128 v[96:99], v0 offset:5120\n"
    "ds_read_b128 v[92:95], v0 offset:3072\n"
    "ds_read_b128 v[112:115], v0 offset:10240\n"
    "ds_read_b128 v[108:111], v0 offset:8192\n"
    "ds_read_b128 v[104:107], v0 offset:7168\n"
    "global_load_dword v77, v76, %1 sc1\n"
    "ds_write_b128 v73, v[48:51] offset:20480\n"
    "v_mfma_f32_16x16x32_bf16 v[52:55], v[56:59], v[152:155], 0\n"
    "v_mfma_f32_16x16x32_bf16 v[52:55], v[56:59], v[156:159], v[52:55]\n"
    "v_mfma_f32_16x16x32_bf16 v[52:55], v[56:59], v[160:163], v[52:55]\n"
    "v_mfma_f32_16x16x32_bf16 v[52:55], v[56:59], v[164:167], v[52:55]\n"
    "v_mfma_f32_16x16x32_bf16 v[52:55], v[56:59], v[168:171], v[52:55]\n"
    "s_waitcnt lgkmcnt(9)\n"
    "v_mfma_f32_16x16x32_bf16 v[24:27], v[80:83], a[216:219], 0\n"
    "ds_read_b128 v[116:119], v0 offset:11264\n"
    "ds_read_b128 v[120:123], v0 offset:12288\n"
    "s_waitcnt lgkmcnt(9)\n"
    "v_mfma_f32_16x16x32_bf16 v[24:27], v[84:87], a[220:223], v[24:27]\n"
    "ds_read_b128 v[124:127], v0 offset:13312\n"
    "ds_read_b128 v[128:131], v0 offset:15360\n"
    "v_mfma_f32_16x16x32_bf16 v[24:27], v[88:91], a[224:227], v[24:27]\n"
    "ds_write_b128 v73, v[52:55] offset:24576\n"
    "ds_read_b128 v[132:135], v0 offset:16384\n"
    "s_waitcnt lgkmcnt(10)\n"
    "v_mfma_f32_16x16x32_bf16 v[24:27], v[92:95], a[228:231], v[24:27]\n"
    "ds_read_b128 v[136:139], v0 offset:17408\n"
    "ds_read_b128 v[140:143], v0 offset:18432\n"
    "v_mfma_f32_16x16x32_bf16 v[24:27], v[96:99], a[232:235], v[24:27]\n"
    "ds_read_b128 v[144:147], v0 offset:4096\n"
    "ds_read_b128 v[148:151], v0 offset:9216\n"
    "v_mfma_f32_16x16x32_bf16 v[24:27], v[100:103], a[236:239], v[24:27]\n"
    "s_waitcnt lgkmcnt(11)\n"
    "v_mfma_f32_16x16x32_bf16 v[24:27], v[104:107], a[240:243], v[24:27]\n"
    "v_mfma_f32_16x16x32_bf16 v[24:27], v[108:111], a[244:247], v[24:27]\n"
    "s_waitcnt lgkmcnt(0)\n"
    "s_barrier\n"
    "v_mfma_f32_16x16x32_bf16 v[24:27], v[112:115], a[248:251], v[24:27]\n"
    "ds_read2st64_b32 v[48:49], v10 offset0:80 offset1:84\n"
    "ds_read2st64_b32 v[50:51], v10 offset0:88 offset1:92\n"
    "v_mfma_f32_16x16x32_bf16 v[12:15], a[0:3], v[80:83], 0\n"
    "ds_read2st64_b32 v[54:55], v10 offset0:104 offset1:108\n"
    "ds_read2st64_b32 v[52:53], v10 offset0:96 offset1:100\n"
    "v_mfma_f32_16x16x32_bf16 v[16:19], a[72:75], v[80:83], 0\n"
    "v_min_f32 v40, v74, v68\n"
    "v_min_f32 v41, v74, v69\n"
    "v_mfma_f32_16x16x32_bf16 v[12:15], a[4:7], v[84:87], v[12:15]\n"
    "v_min_f32 v42, v74, v70\n"
    "v_min_f32 v43, v74, v71\n"
    "v_mfma_f32_16x16x32_bf16 v[16:19], a[76:79], v[84:87], v[16:19]\n"
    "v_mfma_f32_16x16x32_bf16 v[12:15], a[8:11], v[88:91], v[12:15]\n"
    "s_waitcnt lgkmcnt(0)\n"
    "v_add_f32 v52, v52, v53\n"
    "v_add_f32 v54, v54, v55\n"
    "v_mfma_f32_16x16x32_bf16 v[16:19], a[80:83], v[88:91], v[16:19]\n"
    "v_add_f32 v48, v48, v49\n"
    "v_add_f32 v50, v50, v51\n"
    "v_mfma_f32_16x16x32_bf16 v[12:15], a[12:15], v[92:95], v[12:15]\n"
    "v_add_f32 v52, v52, v54\n"
    "v_add_f32 v50, v48, v50\n"
    "v_mfma_f32_16x16x32_bf16 v[16:19], a[84:87], v[92:95], v[16:19]\n"
    "v_mul_f32 v52, 0xbae38e39, v52\n"
    "v_mul_f32 v50, 0x3ae38e39, v50\n"
    "v_mfma_f32_16x16x32_bf16 v[12:15], a[16:19], v[96:99], v[12:15]\n"
    "v_cvt_pk_bf16_f32 v216, v52, v52\n"
    "v_fma_f32 v50, -v52, v52, v50\n"
    "v_mfma_f32_16x16x32_bf16 v[16:19], a[88:91], v[96:99], v[16:19]\n"
    "v_cvt_pk_bf16_f32 v217, v52, v52\n"
    "v_max_f32 v50, 0, v50\n"
    "v_mfma_f32_16x16x32_bf16 v[12:15], a[20:23], v[100:103], v[12:15]\n"
    "v_cvt_pk_bf16_f32 v50, 0, v50\n"
    "v_add_f32 v50, 0x3727c5ac, v50\n"
    "v_mfma_f32_16x16x32_bf16 v[16:19], a[92:95], v[100:103], v[16:19]\n"
    "v_mfma_f32_16x16x32_bf16 v[24:27], v[216:219], v[66:69], v[24:27]\n"
    "v_cvt_pk_bf16_f32 v50, 0, v50\n"
    "v_mfma_f32_16x16x32_bf16 v[12:15], a[24:27], v[104:107], v[12:15]\n"
    "v_sqrt_f32 v50, v50\n"
    "v_mfma_f32_16x16x32_bf16 v[16:19], a[96:99], v[104:107], v[16:19]\n"
    "v_cvt_pk_bf16_f32 v50, 0, v50\n"
    "v_rcp_f32 v50, v50\n"
    "v_mfma_f32_16x16x32_bf16 v[12:15], a[28:31], v[108:111], v[12:15]\n"
    "ds_write_b32 v9, v50\n"
    "v_mfma_f32_16x16x32_bf16 v[16:19], a[100:103], v[108:111], v[16:19]\n"
    "ds_read_b128 v[200:203], v72 offset:28672\n"
    "v_mfma_f32_16x16x32_bf16 v[12:15], v[60:63], v[216:219], v[12:15]\n"
    "v_mfma_f32_16x16x32_bf16 v[12:15], a[32:35], v[112:115], v[12:15]\n"
    "v_mfma_f32_16x16x32_bf16 v[16:19], v[62:65], v[216:219], v[16:19]\n"
    "v_mfma_f32_16x16x32_bf16 v[16:19], a[104:107], v[112:115], v[16:19]\n"
    "v_mfma_f32_16x16x32_bf16 v[12:15], a[36:39], v[116:119], v[12:15]\n"
    "v_mfma_f32_16x16x32_bf16 v[16:19], a[108:111], v[116:119], v[16:19]\n"
    "s_waitcnt lgkmcnt(0)\n"
    "v_mul_f32 v24, v24, v200\n"
    "v_mul_f32 v25, v25, v201\n"
    "v_mfma_f32_16x16x32_bf16 v[12:15], a[40:43], v[120:123], v[12:15]\n"
    "v_mul_f32 v26, v26, v202\n"
    "v_mul_f32 v27, v27, v203\n"
    "v_mfma_f32_16x16x32_bf16 v[16:19], a[112:115], v[120:123], v[16:19]\n"
    "ds_write_b128 v5, v[24:27]\n"
    "v_mfma_f32_16x16x32_bf16 v[12:15], a[44:47], v[124:127], v[12:15]\n"
    "s_waitcnt vmcnt(0)\n"
    "v_mov_b32 v78, 1\n"
    "v_mfma_f32_16x16x32_bf16 v[16:19], a[116:119], v[124:127], v[16:19]\n"
    "s_mov_b64 exec, s[56:57]\n"
    "ds_add_rtn_u32 v78, v11, v78 offset:16\n"
    "s_mov_b64 exec, -1\n"
    "v_mfma_f32_16x16x32_bf16 v[12:15], a[48:51], v[128:131], v[12:15]\n"
    "s_waitcnt lgkmcnt(0)\n"
    "s_barrier\n"
    "v_mfma_f32_16x16x32_bf16 v[16:19], a[120:123], v[128:131], v[16:19]\n"
    "ds_read_b128 v[188:191], v8\n"
    "ds_read_b128 v[192:195], v8 offset:1024\n"
    "v_mfma_f32_16x16x32_bf16 v[12:15], a[52:55], v[132:135], v[12:15]\n"
    "ds_read2st64_b64 v[184:187], v7 offset1:2\n"
    "ds_read2st64_b64 v[180:183], v6 offset1:2\n"
    "v_cmp_eq_u32 vcc, 0, v77\n"
    "s_cmp_eq_u64 vcc, 0\n"
    "v_mfma_f32_16x16x32_bf16 v[16:19], a[124:127], v[132:135], v[16:19]\n"
    "s_cselect_b32 s94, s42, 0\n"
    "v_mfma_f32_16x16x32_bf16 v[12:15], a[56:59], v[136:139], v[12:15]\n"
    "global_load_dword v77, v76, %1 sc1\n"
    "v_mfma_f32_16x16x32_bf16 v[16:19], a[128:131], v[136:139], v[16:19]\n"
    "buffer_load_dwordx4 v[152:155], v3, s[92:95], s49 offen sc1\n"
    "v_mfma_f32_16x16x32_bf16 v[12:15], a[60:63], v[140:143], v[12:15]\n"
    "buffer_load_dwordx4 v[156:159], v3, s[92:95], s49 offen offset:1024 sc1\n"
    "v_mfma_f32_16x16x32_bf16 v[16:19], a[132:135], v[140:143], v[16:19]\n"
    "v_readfirstlane_b32 s50, v78\n"
    "v_mfma_f32_16x16x32_bf16 v[12:15], a[64:67], v[144:147], v[12:15]\n"
    "buffer_load_dwordx4 v[160:163], v3, s[92:95], s49 offen offset:2048 sc1\n"
    "s_cmp_eq_u32 s50, 3\n"
    "v_mfma_f32_16x16x32_bf16 v[16:19], a[136:139], v[144:147], v[16:19]\n"
    "s_waitcnt lgkmcnt(0)\n"
    "v_add_f32 v180, v180, v182\n"
    "v_add_f32 v181, v181, v183\n"
    "s_cselect_b64 exec, 1, 0\n"
    "global_store_dword v79, v78, %1 sc1\n"
    "s_mov_b64 exec, -1\n"
    "v_mfma_f32_16x16x32_bf16 v[12:15], a[68:71], v[148:151], v[12:15]\n"
    "v_cvt_pk_bf16_f32 v198, v180, v181\n"
    "v_add_f32 v184, v184, v186\n"
    "buffer_load_dwordx4 v[164:167], v3, s[92:95], s49 offen offset:3072 sc1\n"
    "v_mfma_f32_16x16x32_bf16 v[16:19], a[140:143], v[148:151], v[16:19]\n"
    "v_add_f32 v185, v185, v187\n"
    "v_cvt_pk_bf16_f32 v214, v184, v185\n"
    "buffer_load_dwordx4 v[168:171], v4, s[92:95], s49 offen sc1\n"
    "s_branch .Lak_%=\n"
    ".Laq3_%=:\n"
    "ds_read_b128 v[116:119], v0 offset:11264\n"
    "ds_read_b128 v[124:127], v0 offset:13312\n"
    "ds_read_b128 v[120:123], v0 offset:12288\n"
    "ds_read_b128 v[136:139], v0 offset:17408\n"
    "ds_read_b128 v[132:135], v0 offset:16384\n"
    "ds_read_b128 v[128:131], v0 offset:15360\n"
    "ds_read_b128 v[148:151], v0 offset:9216\n"
    "ds_read_b128 v[144:147], v0 offset:4096\n"
    "ds_read_b128 v[140:143], v0 offset:18432\n"
    "global_load_dword v77, v76, %1 sc1\n"
    "ds_write_b128 v73, v[48:51] offset:20480\n"
    "v_mfma_f32_16x16x32_bf16 v[52:55], v[56:59], v[152:155], 0\n"
    "v_mfma_f32_16x16x32_bf16 v[52:55], v[56:59], v[156:159], v[52:55]\n"
    "v_mfma_f32_16x16x32_bf16 v[52:55], v[56:59], v[160:163], v[52:55]\n"
    "v_mfma_f32_16x16x32_bf16 v[52:55], v[56:59], v[164:167], v[52:55]\n"
    "v_mfma_f32_16x16x32_bf16 v[52:55], v[56:59], v[168:171], v[52:55]\n"
    "s_waitcnt lgkmcnt(9)\n"
    "v_mfma_f32_16x16x32_bf16 v[24:27], v[116:119], a[252:255], 0\n"
    "ds_read_b128 v[80:83], v0\n"
    "ds_read_b128 v[84:87], v0 offset:1024\n"
    "s_waitcnt lgkmcnt(9)\n"
    "v_mfma_f32_16x16x32_bf16 v[24:27], v[120:123], v[224:227], v[24:27]\n"
    "ds_read_b128 v[88:91], v0 offset:2048\n"
    "ds_read_b128 v[92:95], v0 offset:3072\n"
    "v_mfma_f32_16x16x32_bf16 v[24:27], v[124:127], v[228:231], v[24:27]\n"
    "ds_write_b128 v73, v[52:55] offset:24576\n"
    "ds_read_b128 v[96:99], v0 offset:5120\n"
    "s_waitcnt lgkmcnt(10)\n"
    "v_mfma_f32_16x16x32_bf16 v[24:27], v[128:131], v[232:235], v[24:27]\n"
    "ds_read_b128 v[100:103], v0 offset:6144\n"
    "ds_read_b128 v[104:107], v0 offset:7168\n"
    "v_mfma_f32_16x16x32_bf16 v[24:27], v[132:135], v[236:239], v[24:27]\n"
    "ds_read_b128 v[108:111], v0 offset:8192\n"
    "ds_read_b128 v[112:115], v0 offset:10240\n"
    "v_mfma_f32_16x16x32_bf16 v[24:27], v[136:139], v[240:243], v[24:27]\n"
    "s_waitcnt lgkmcnt(11)\n"
    "v_mfma_f32_16x16x32_bf16 v[24:27], v[140:143], v[244:247], v[24:27]\n"
    "v_mfma_f32_16x16x32_bf16 v[24:27], v[144:147], v[248:251], v[24:27]\n"
    "s_waitcnt lgkmcnt(0)\n"
    "s_barrier\n"
    "v_mfma_f32_16x16x32_bf16 v[24:27], v[148:151], v[252:255], v[24:27]\n"
    "ds_read2st64_b32 v[48:49], v10 offset0:80 offset1:84\n"
    "ds_read2st64_b32 v[50:51], v10 offset0:88 offset1:92\n"
    "v_mfma_f32_16x16x32_bf16 v[12:15], a[36:39], v[116:119], 0\n"
    "ds_read2st64_b32 v[54:55], v10 offset0:104 offset1:108\n"
    "ds_read2st64_b32 v[52:53], v10 offset0:96 offset1:100\n"
    "v_mfma_f32_16x16x32_bf16 v[16:19], a[108:111], v[116:119], 0\n"
    "v_min_f32 v40, v74, v68\n"
    "v_min_f32 v41, v74, v69\n"
    "v_mfma_f32_16x16x32_bf16 v[12:15], a[40:43], v[120:123], v[12:15]\n"
    "v_min_f32 v42, v74, v70\n"
    "v_min_f32 v43, v74, v71\n"
    "v_mfma_f32_16x16x32_bf16 v[16:19], a[112:115], v[120:123], v[16:19]\n"
    "v_mfma_f32_16x16x32_bf16 v[12:15], a[44:47], v[124:127], v[12:15]\n"
    "s_waitcnt lgkmcnt(0)\n"
    "v_add_f32 v52, v52, v53\n"
    "v_add_f32 v54, v54, v55\n"
    "v_mfma_f32_16x16x32_bf16 v[16:19], a[116:119], v[124:127], v[16:19]\n"
    "v_add_f32 v48, v48, v49\n"
    "v_add_f32 v50, v50, v51\n"
    "v_mfma_f32_16x16x32_bf16 v[12:15], a[48:51], v[128:131], v[12:15]\n"
    "v_add_f32 v52, v52, v54\n"
    "v_add_f32 v50, v48, v50\n"
    "v_mfma_f32_16x16x32_bf16 v[16:19], a[120:123], v[128:131], v[16:19]\n"
    "v_mul_f32 v52, 0xbae38e39, v52\n"
    "v_mul_f32 v50, 0x3ae38e39, v50\n"
    "v_mfma_f32_16x16x32_bf16 v[12:15], a[52:55], v[132:135], v[12:15]\n"
    "v_cvt_pk_bf16_f32 v216, v52, v52\n"
    "v_fma_f32 v50, -v52, v52, v50\n"
    "v_mfma_f32_16x16x32_bf16 v[16:19], a[124:127], v[132:135], v[16:19]\n"
    "v_cvt_pk_bf16_f32 v217, v52, v52\n"
    "v_max_f32 v50, 0, v50\n"
    "v_mfma_f32_16x16x32_bf16 v[12:15], a[56:59], v[136:139], v[12:15]\n"
    "v_cvt_pk_bf16_f32 v50, 0, v50\n"
    "v_add_f32 v50, 0x3727c5ac, v50\n"
    "v_mfma_f32_16x16x32_bf16 v[16:19], a[128:131], v[136:139], v[16:19]\n"
    "v_cvt_pk_bf16_f32 v50, 0, v50\n"
    "v_sqrt_f32 v50, v50\n"
    "v_mfma_f32_16x16x32_bf16 v[12:15], a[60:63], v[140:143], v[12:15]\n"
    "v_cvt_pk_bf16_f32 v50, 0, v50\n"
    "v_mfma_f32_16x16x32_bf16 v[16:19], a[132:135], v[140:143], v[16:19]\n"
    "v_rcp_f32 v50, v50\n"
    "v_mfma_f32_16x16x32_bf16 v[12:15], a[64:67], v[144:147], v[12:15]\n"
    "ds_write_b32 v9, v50\n"
    "ds_read_b128 v[200:203], v72 offset:28672\n"
    "v_mfma_f32_16x16x32_bf16 v[16:19], a[136:139], v[144:147], v[16:19]\n"
    "v_mfma_f32_16x16x32_bf16 v[12:15], v[60:63], v[216:219], v[12:15]\n"
    "v_mfma_f32_16x16x32_bf16 v[16:19], v[62:65], v[216:219], v[16:19]\n"
    "v_mfma_f32_16x16x32_bf16 v[12:15], a[68:71], v[148:151], v[12:15]\n"
    "v_mfma_f32_16x16x32_bf16 v[16:19], a[140:143], v[148:151], v[16:19]\n"
    "v_mfma_f32_16x16x32_bf16 v[12:15], a[0:3], v[80:83], v[12:15]\n"
    "s_waitcnt lgkmcnt(0)\n"
    "v_mul_f32 v24, v24, v200\n"
    "v_mul_f32 v25, v25, v201\n"
    "v_mfma_f32_16x16x32_bf16 v[16:19], a[72:75], v[80:83], v[16:19]\n"
    "v_mul_f32 v26, v26, v202\n"
    "v_mul_f32 v27, v27, v203\n"
    "v_mfma_f32_16x16x32_bf16 v[12:15], a[4:7], v[84:87], v[12:15]\n"
    "ds_write_b128 v5, v[24:27]\n"
    "v_mfma_f32_16x16x32_bf16 v[16:19], a[76:79], v[84:87], v[16:19]\n"
    "v_mfma_f32_16x16x32_bf16 v[12:15], a[8:11], v[88:91], v[12:15]\n"
    "s_waitcnt vmcnt(0)\n"
    "v_mov_b32 v78, 1\n"
    "v_mfma_f32_16x16x32_bf16 v[16:19], a[80:83], v[88:91], v[16:19]\n"
    "s_mov_b64 exec, s[56:57]\n"
    "ds_add_rtn_u32 v78, v11, v78 offset:16\n"
    "s_mov_b64 exec, -1\n"
    "v_mfma_f32_16x16x32_bf16 v[12:15], a[12:15], v[92:95], v[12:15]\n"
    "s_waitcnt lgkmcnt(0)\n"
    "s_barrier\n"
    "v_mfma_f32_16x16x32_bf16 v[16:19], a[84:87], v[92:95], v[16:19]\n"
    "ds_read_b128 v[188:191], v8\n"
    "ds_read_b128 v[192:195], v8 offset:1024\n"
    "v_mfma_f32_16x16x32_bf16 v[12:15], a[16:19], v[96:99], v[12:15]\n"
    "ds_read2st64_b64 v[184:187], v7 offset1:2\n"
    "ds_read2st64_b64 v[180:183], v6 offset1:2\n"
    "v_cmp_eq_u32 vcc, 0, v77\n"
    "s_cmp_eq_u64 vcc, 0\n"
    "v_mfma_f32_16x16x32_bf16 v[16:19], a[88:91], v[96:99], v[16:19]\n"
    "s_cselect_b32 s94, s42, 0\n"
    "v_mfma_f32_16x16x32_bf16 v[12:15], a[20:23], v[100:103], v[12:15]\n"
    "global_load_dword v77, v76, %1 sc1\n"
    "v_mfma_f32_16x16x32_bf16 v[16:19], a[92:95], v[100:103], v[16:19]\n"
    "buffer_load_dwordx4 v[152:155], v3, s[92:95], s49 offen sc1\n"
    "v_mfma_f32_16x16x32_bf16 v[12:15], a[24:27], v[104:107], v[12:15]\n"
    "buffer_load_dwordx4 v[156:159], v3, s[92:95], s49 offen offset:1024 sc1\n"
    "v_mfma_f32_16x16x32_bf16 v[16:19], a[96:99], v[104:107], v[16:19]\n"
    "v_readfirstlane_b32 s50, v78\n"
    "v_mfma_f32_16x16x32_bf16 v[12:15], a[28:31], v[108:111], v[12:15]\n"
    "buffer_load_dwordx4 v[160:163], v3, s[92:95], s49 offen offset:2048 sc1\n"
    "s_cmp_eq_u32 s50, 3\n"
    "v_mfma_f32_16x16x32_bf16 v[16:19], a[100:103], v[108:111], v[16:19]\n"
    "s_waitcnt lgkmcnt(0)\n"
    "v_add_f32 v180, v180, v182\n"
    "v_add_f32 v181, v181, v183\n"
    "s_cselect_b64 exec, 1, 0\n"
    "global_store_dword v79, v78, %1 sc1\n"
    "s_mov_b64 exec, -1\n"
    "v_mfma_f32_16x16x32_bf16 v[12:15], a[32:35], v[112:115], v[12:15]\n"
    "v_cvt_pk_bf16_f32 v198, v180, v181\n"
    "v_add_f32 v184, v184, v186\n"
    "buffer_load_dwordx4 v[164:167], v3, s[92:95], s49 offen offset:3072 sc1\n"
    "v_mfma_f32_16x16x32_bf16 v[16:19], a[104:107], v[112:115], v[16:19]\n"
    "v_add_f32 v185, v185, v187\n"
    "v_cvt_pk_bf16_f32 v214, v184, v185\n"
    "buffer_load_dwordx4 v[168:171], v4, s[92:95], s49 offen sc1\n"
    ".Lak_%=:\n"
    "v_mfma_f32_16x16x32_bf16 v[20:23], v[216:219], v[64:67], 0\n"
    "v_add_f32 v188, v188, v192\n"
    "v_add_f32 v189, v189, v193\n"
    "v_mfma_f32_16x16x32_bf16 v[20:23], v[80:83], a[144:147], v[20:23]\n"
    "v_add_f32 v190, v190, v194\n"
    "v_add_f32 v191, v191, v195\n"
    "v_mfma_f32_16x16x32_bf16 v[20:23], v[84:87], a[148:151], v[20:23]\n"
    "v_mul_f32 v12, v12, v50\n"
    "v_mul_f32 v13, v13, v50\n"
    "v_mfma_f32_16x16x32_bf16 v[20:23], v[88:91], a[152:155], v[20:23]\n"
    "v_mul_f32 v14, v14, v50\n"
    "v_mul_f32 v15, v15, v50\n"
    "v_mfma_f32_16x16x32_bf16 v[20:23], v[92:95], a[156:159], v[20:23]\n"
    "v_cvt_pk_bf16_f32 v196, v12, v13\n"
    "v_cvt_pk_bf16_f32 v197, v14, v15\n"
    "v_mfma_f32_16x16x32_bf16 v[20:23], v[96:99], a[160:163], v[20:23]\n"
    "v_mul_f32 v16, v16, v50\n"
    "v_mul_f32 v17, v17, v50\n"
    "v_mfma_f32_16x16x32_bf16 v[20:23], v[100:103], a[164:167], v[20:23]\n"
    "v_mfma_f32_16x16x32_bf16 v[204:207], v[28:31], v[196:199], v[40:43]\n"
    "v_mul_f32 v18, v18, v50\n"
    "v_mfma_f32_16x16x32_bf16 v[20:23], v[104:107], a[168:171], v[20:23]\n"
    "v_mul_f32 v19, v19, v50\n"
    "v_cvt_pk_bf16_f32 v212, v16, v17\n"
    "v_mfma_f32_16x16x32_bf16 v[20:23], v[108:111], a[172:175], v[20:23]\n"
    "v_cvt_pk_bf16_f32 v213, v18, v19\n"
    "v_cvt_pk_bf16_f32 v38, v188, v189\n"
    "v_mfma_f32_16x16x32_bf16 v[20:23], v[112:115], a[176:179], v[20:23]\n"
    "v_cvt_pk_bf16_f32 v39, v190, v191\n"
    "v_mfma_f32_16x16x32_bf16 v[208:211], v[212:215], v[196:199], v[44:47]\n"
    "v_mfma_f32_16x16x32_bf16 v[20:23], v[116:119], a[180:183], v[20:23]\n"
    "v_cndmask_b32 v38, v38, v56, s[86:87]\n"
    "v_cndmask_b32 v39, v39, v56, s[86:87]\n"
    "v_mfma_f32_16x16x32_bf16 v[20:23], v[120:123], a[184:187], v[20:23]\n"
    "v_maximum3_f32 v188, v204, v205, v206\n"
    "v_mov_b32 v28, v212\n"
    "v_mfma_f32_16x16x32_bf16 v[20:23], v[124:127], a[188:191], v[20:23]\n"
    "v_mov_b32 v29, v213\n"
    "v_mov_b32 v30, v214\n"
    "v_mfma_f32_16x16x32_bf16 v[20:23], v[128:131], a[192:195], v[20:23]\n"
    "v_maximum3_f32 v188, v188, v207, v208\n"
    "v_maximum3_f32 v188, v188, v209, v210\n"
    "v_mfma_f32_16x16x32_bf16 v[20:23], v[132:135], a[196:199], v[20:23]\n"
    "v_maximum3_f32 v188, v188, v211, v211\n"
    "v_mov_b32 v189, v188\n"
    "v_mfma_f32_16x16x32_bf16 v[20:23], v[136:139], a[200:203], v[20:23]\n"
    "s_nop 0\n"
    "v_permlane16_swap_b32 v189, v188\n"
    "v_maximum3_f32 v188, v188, v189, v189\n"
    "v_mfma_f32_16x16x32_bf16 v[20:23], v[140:143], a[204:207], v[20:23]\n"
    "v_mov_b32 v189, v188\n"
    "v_mfma_f32_16x16x32_bf16 v[20:23], v[144:147], a[208:211], v[20:23]\n"
    "s_nop 0\n"
    "v_permlane32_swap_b32 v189, v188\n"
    "v_maximum3_f32 v188, v188, v189, v189\n"
    "v_mfma_f32_16x16x32_bf16 v[20:23], v[148:151], a[212:215], v[20:23]\n"
    "v_mul_f32 v188, 0xbeae1a93, v188\n"
    "v_fmamk_f32 v204, v204, 0x3eae1a93, v188\n"
    "v_fmamk_f32 v205, v205, 0x3eae1a93, v188\n"
    "v_fmamk_f32 v206, v206, 0x3eae1a93, v188\n"
    "v_fmamk_f32 v207, v207, 0x3eae1a93, v188\n"
    "v_fmamk_f32 v208, v208, 0x3eae1a93, v188\n"
    "v_fmamk_f32 v209, v209, 0x3eae1a93, v188\n"
    "v_fmamk_f32 v210, v210, 0x3eae1a93, v188\n"
    "v_fmamk_f32 v211, v211, 0x3eae1a93, v188\n"
    "v_exp_f32 v204, v204\n"
    "v_exp_f32 v205, v205\n"
    "v_exp_f32 v206, v206\n"
    "v_exp_f32 v207, v207\n"
    "v_exp_f32 v208, v208\n"
    "v_exp_f32 v209, v209\n"
    "v_exp_f32 v210, v210\n"
    "v_exp_f32 v211, v211\n"
    "v_mul_f32 v20, v20, v200\n"
    "v_mul_f32 v21, v21, v201\n"
    "v_mul_f32 v22, v22, v202\n"
    "v_mul_f32 v23, v23, v203\n"
    "v_cvt_pk_bf16_f32 v204, v204, v205\n"
    "v_cvt_pk_bf16_f32 v205, v206, v207\n"
    "v_cvt_pk_bf16_f32 v206, v208, v209\n"
    "v_cvt_pk_bf16_f32 v207, v210, v211\n"
    "v_cvt_pk_bf16_f32 v34, v20, v21\n"
    "v_cvt_pk_bf16_f32 v35, v22, v23\n"
    "v_mfma_f32_16x16x32_bf16 v[184:187], v[36:39], v[204:207], 0\n"
    "s_nop 0\n"
    "v_mfma_f32_16x16x32_bf16 v[180:183], v[32:35], v[204:207], 0\n"
    "v_mov_b64 v[36:37], v[38:39]\n"
    "v_mov_b64 v[32:33], v[34:35]\n"
    "s_nop 2\n"
    "v_rcp_f32 v188, v186\n"
    "s_nop 0\n"
    "v_mul_f32 v184, v184, v188\n"
    "v_mul_f32 v185, v185, v188\n"
    "v_cvt_pk_bf16_f32 v184, v184, v185\n"
    "v_pk_mul_f32 v[180:181], v[180:181], v[188:189] op_sel_hi:[1,0]\n"
    "v_pk_mul_f32 v[182:183], v[182:183], v[188:189] op_sel_hi:[1,0]\n"
    "v_cvt_pk_bf16_f32 v204, v180, v181\n"
    "v_cvt_pk_bf16_f32 v205, v182, v183\n"
    "v_cvt_pk_bf16_f32 v206, v180, v181\n"
    "v_cvt_pk_bf16_f32 v207, v182, v183\n"
    "s_nop 0\n"
    "v_permlane16_swap_b32 v204, v206\n"
    "v_permlane16_swap_b32 v205, v207\n"
    "s_nop 0\n"
    "v_mov_b32_dpp v204, v184 quad_perm:[0,0,0,0] row_mask:0x2 bank_mask:0xf\n"
    "v_mov_b32_dpp v205, v184 quad_perm:[1,1,1,1] row_mask:0x2 bank_mask:0xf\n"
    "v_mov_b32_dpp v206, v184 quad_perm:[2,2,2,2] row_mask:0x2 bank_mask:0xf\n"
    "v_mov_b32_dpp v207, v184 quad_perm:[3,3,3,3] row_mask:0x2 bank_mask:0xf\n"
    "buffer_store_dwordx4 v[204:207], v2, s[40:43], s62 offen sc1\n"
    "s_mov_b32 s56, 1\n"
    "s_cmp_lg_u32 s94, 0\n"
    "s_cbranch_scc1 .Lago_%=\n"
    "s_waitcnt vmcnt(1)\n"
    "v_cmp_eq_u32 vcc, 0, v77\n"
    "s_cmp_eq_u64 vcc, 0\n"
    "s_cbranch_scc1 .Lald_%=\n"
    "ds_read_b32 v79, v11 offset:44\n"
    "global_load_dword v77, v76, %1 sc1\n"
    "s_waitcnt vmcnt(0)\n"
    "v_mov_b32 v78, 1\n"
    "s_mov_b64 exec, 1\n"
    "ds_add_rtn_u32 v78, v11, v78 offset:48\n"
    "s_mov_b64 exec, -1\n"
    "s_waitcnt lgkmcnt(0)\n"
    "v_readfirstlane_b32 s50, v78\n"
    "s_cmp_eq_u32 s50, 3\n"
    "s_cselect_b64 exec, 1, 0\n"
    "global_store_dword v79, v78, %1 sc1\n"
    "s_mov_b64 exec, -1\n"
    "s_mov_b32 s56, 0\n"
    "v_cmp_eq_u32 vcc, 0, v77\n"
    "s_cmp_eq_u64 vcc, 0\n"
    "s_cbranch_scc1 .Lald_%=\n"
    "v_readfirstlane_b32 s50, v76\n"
    "s_cmp_eq_u32 s50, 192\n"
    "s_cbranch_scc1 .Laend_%=\n"
    ".Lawait_%=:\n"
    "global_load_dword v77, v76, %1 sc1\n"
    "s_mov_b32 s52, 0\n"
    ".Lap12_%=:\n"
    "s_waitcnt vmcnt(0)\n"
    "v_cmp_eq_u32 vcc, 0, v77\n"
    "s_cmp_eq_u64 vcc, 0\n"
    "s_cbranch_scc1 .Lap12_x%=\n"
    "s_add_u32 s52, s52, 1\n"
    "s_sleep 2\n"
    "global_load_dword v77, v76, %1 sc1\n"
    "s_cmp_lt_u32 s52, s53\n"
    "s_cbranch_scc1 .Lap12_%=\n"
    "s_mov_b32 s53, 0\n"
    "v_mov_b32 v78, 0\n"
    "v_mov_b32 v79, 1\n"
    "global_store_dword v78, v79, %1 sc1\n"
    "s_waitcnt vmcnt(0)\n"
    ".Lap12_x%=:\n"
    ".Lald_%=:\n"
    "s_mov_b32 s94, s42\n"
    "buffer_load_dwordx4 v[152:155], v3, s[92:95], s49 offen sc1\n"
    "buffer_load_dwordx4 v[156:159], v3, s[92:95], s49 offen offset:1024 sc1\n"
    "buffer_load_dwordx4 v[160:163], v3, s[92:95], s49 offen offset:2048 sc1\n"
    "buffer_load_dwordx4 v[164:167], v3, s[92:95], s49 offen offset:3072 sc1\n"
    "buffer_load_dwordx4 v[168:171], v4, s[92:95], s49 offen sc1\n"
    ".Lain_%=:\n"
    "v_add_u32 v11, 32, v11\n"
    "ds_read_b128 v[76:79], v11\n"
    "ds_read2_b32 v[74:75], v11 offset0:5 offset1:6\n"
    "s_waitcnt vmcnt(4)\n"
    "ds_write_b128 v1, v[152:155]\n"
    "v_mfma_f32_16x16x32_bf16 v[48:51], v[152:155], v[152:155], 0\n"
    "s_waitcnt vmcnt(3)\n"
    "ds_write_b128 v1, v[156:159] offset:1024\n"
    "v_mfma_f32_16x16x32_bf16 v[48:51], v[156:159], v[156:159], v[48:51]\n"
    "s_waitcnt vmcnt(2)\n"
    "ds_write_b128 v1, v[160:163] offset:2048\n"
    "v_mfma_f32_16x16x32_bf16 v[48:51], v[160:163], v[160:163], v[48:51]\n"
    "s_waitcnt vmcnt(1)\n"
    "ds_write_b128 v1, v[164:167] offset:3072\n"
    "v_mfma_f32_16x16x32_bf16 v[48:51], v[164:167], v[164:167], v[48:51]\n"
    "s_waitcnt vmcnt(0)\n"
    "ds_write_b128 v1, v[168:171] offset:4096\n"
    "v_mfma_f32_16x16x32_bf16 v[48:51], v[168:171], v[168:171], v[48:51]\n"
    "s_waitcnt lgkmcnt(5)\n"
    "v_readfirstlane_b32 s50, v75\n"
    "s_cmp_eq_u32 s50, 0\n"
    "s_cbranch_scc1 .Lac13_%=\n"
    "v_swap_b32 v28, v172\n"
    "v_swap_b32 v29, v173\n"
    "v_swap_b32 v30, v174\n"
    "v_swap_b32 v32, v175\n"
    "v_swap_b32 v33, v176\n"
    "v_swap_b32 v36, v177\n"
    "v_swap_b32 v37, v178\n"
    ".Lac13_%=:\n"
    "v_readfirstlane_b32 s49, v77\n"
    "v_readfirstlane_b32 s62, v78\n"
    "v_add_u32 v76, v76, v220\n"
    "s_waitcnt lgkmcnt(0)\n"
    "s_cmp_eq_u32 s56, 0\n"
    "s_cbranch_scc1 .Lac14_%=\n"
    "s_waitcnt vmcnt(0)\n"
    "v_mov_b32 v78, 1\n"
    "s_mov_b64 exec, 1\n"
    "ds_add_rtn_u32 v78, v11, v78 offset:16\n"
    "s_mov_b64 exec, -1\n"
    "s_waitcnt lgkmcnt(0)\n"
    "v_readfirstlane_b32 s50, v78\n"
    "s_cmp_eq_u32 s50, 3\n"
    "s_cselect_b64 exec, 1, 0\n"
    "global_store_dword v79, v78, %1 sc1\n"
    "s_mov_b64 exec, -1\n"
    "s_mov_b32 s56, 0\n"
    ".Lac14_%=:\n"
    "s_branch .Latop_%=\n"
    ".Laend_%=:\n"
    "s_waitcnt vmcnt(0) lgkmcnt(0)\n"
    "s_endpgm\n"
    ".p2align 6\n"
    ".Lrole4_%=:\n"
    "s_mov_b64 s[40:41], %0\n"
    "s_and_b32 s41, s41, 0xffff\n"
    "s_mov_b32 s42, 0xbc00000\n"
    "s_mov_b32 s43, 0x20000\n"
    "s_mov_b64 s[92:93], %0\n"
    "s_and_b32 s93, s93, 0xffff\n"
    "s_mov_b32 s94, 0xbc00000\n"
    "s_mov_b32 s95, 0x20000\n"
    "s_mov_b32 s53, 131072\n"
    "s_mov_b64 s[56:57], 0\n"
    "v_mbcnt_lo_u32_b32 v20, -1, 0\n"
    "v_mbcnt_hi_u32_b32 v20, -1, v20\n"
    "s_lshl_b32 s50, %2, 8\n"
    "s_add_u32 s50, s50, 147456\n"
    "v_lshl_add_u32 v20, v20, 2, s50\n"
    "ds_read_b32 v0, v20\n"
    "ds_read_b32 v1, v20 offset:1024\n"
    "ds_read_b32 v2, v20 offset:2048\n"
    "ds_read_b32 v3, v20 offset:3072\n"
    "ds_read_b32 v4, v20 offset:4096\n"
    "ds_read_b32 v5, v20 offset:5120\n"
    "ds_read_b32 v8, v20 offset:6144\n"
    "ds_read_b32 v9, v20 offset:7168\n"
    "ds_read_b32 v10, v20 offset:8192\n"
    "s_waitcnt lgkmcnt(0)\n"
    "s_barrier\n"
    "s_lshl_b32 s54, %2, 10\n"
    "s_mov_b64 s[44:45], %8\n"
    "s_and_b32 s45, s45, 0xffff\n"
    "s_mov_b32 s46, 0xa2000\n"
    "s_mov_b32 s47, 0x20000\n"
    "s_add_u32 m0, s54, 0\n"
    "s_add_u32 s55, %6, 0\n"
    "buffer_load_dwordx4 v8, s[44:47], s55 offen lds\n"
    "s_add_u32 m0, s54, 36864\n"
    "s_add_u32 s55, %6, 64\n"
    "buffer_load_dwordx4 v8, s[44:47], s55 offen lds\n"
    "s_add_u32 m0, s54, 73728\n"
    "s_add_u32 s55, %6, 128\n"
    "buffer_load_dwordx4 v8, s[44:47], s55 offen lds\n"
    "s_add_u32 m0, s54, 4096\n"
    "s_add_u32 s55, %6, 73728\n"
    "buffer_load_dwordx4 v8, s[44:47], s55 offen lds\n"
    "s_add_u32 m0, s54, 40960\n"
    "s_add_u32 s55, %6, 73792\n"
    "buffer_load_dwordx4 v8, s[44:47], s55 offen lds\n"
    "s_add_u32 m0, s54, 77824\n"
    "s_add_u32 s55, %6, 73856\n"
    "buffer_load_dwordx4 v8, s[44:47], s55 offen lds\n"
    "s_add_u32 m0, s54, 8192\n"
    "s_add_u32 s55, %6, 147456\n"
    "buffer_load_dwordx4 v8, s[44:47], s55 offen lds\n"
    "s_add_u32 m0, s54, 45056\n"
    "s_add_u32 s55, %6, 147520\n"
    "buffer_load_dwordx4 v8, s[44:47], s55 offen lds\n"
    "s_add_u32 m0, s54, 81920\n"
    "s_add_u32 s55, %6, 147584\n"
    "buffer_load_dwordx4 v8, s[44:47], s55 offen lds\n"
    "s_add_u32 m0, s54, 12288\n"
    "s_add_u32 s55, %6, 221184\n"
    "buffer_load_dwordx4 v8, s[44:47], s55 offen lds\n"
    "s_add_u32 m0, s54, 49152\n"
    "s_add_u32 s55, %6, 221248\n"
    "buffer_load_dwordx4 v8, s[44:47], s55 offen lds\n"
    "s_add_u32 m0, s54, 86016\n"
    "s_add_u32 s55, %6, 221312\n"
    "buffer_load_dwordx4 v8, s[44:47], s55 offen lds\n"
    "s_add_u32 m0, s54, 16384\n"
    "s_add_u32 s55, %6, 294912\n"
    "buffer_load_dwordx4 v8, s[44:47], s55 offen lds\n"
    "s_add_u32 m0, s54, 53248\n"
    "s_add_u32 s55, %6, 294976\n"
    "buffer_load_dwordx4 v8, s[44:47], s55 offen lds\n"
    "s_add_u32 m0, s54, 90112\n"
    "s_add_u32 s55, %6, 295040\n"
    "buffer_load_dwordx4 v8, s[44:47], s55 offen lds\n"
    "s_add_u32 m0, s54, 20480\n"
    "s_add_u32 s55, %6, 368640\n"
    "buffer_load_dwordx4 v8, s[44:47], s55 offen lds\n"
    "s_add_u32 m0, s54, 57344\n"
    "s_add_u32 s55, %6, 368704\n"
    "buffer_load_dwordx4 v8, s[44:47], s55 offen lds\n"
    "s_add_u32 m0, s54, 94208\n"
    "s_add_u32 s55, %6, 368768\n"
    "buffer_load_dwordx4 v8, s[44:47], s55 offen lds\n"
    "s_add_u32 m0, s54, 24576\n"
    "s_add_u32 s55, %6, 442368\n"
    "buffer_load_dwordx4 v8, s[44:47], s55 offen lds\n"
    "s_add_u32 m0, s54, 61440\n"
    "s_add_u32 s55, %6, 442432\n"
    "buffer_load_dwordx4 v8, s[44:47], s55 offen lds\n"
    "s_add_u32 m0, s54, 98304\n"
    "s_add_u32 s55, %6, 442496\n"
    "buffer_load_dwordx4 v8, s[44:47], s55 offen lds\n"
    "s_add_u32 m0, s54, 28672\n"
    "s_add_u32 s55, %6, 516096\n"
    "buffer_load_dwordx4 v8, s[44:47], s55 offen lds\n"
    "s_add_u32 m0, s54, 65536\n"
    "s_add_u32 s55, %6, 516160\n"
    "buffer_load_dwordx4 v8, s[44:47], s55 offen lds\n"
    "s_add_u32 m0, s54, 102400\n"
    "s_add_u32 s55, %6, 516224\n"
    "buffer_load_dwordx4 v8, s[44:47], s55 offen lds\n"
    "s_add_u32 m0, s54, 32768\n"
    "s_add_u32 s55, %6, 589824\n"
    "buffer_load_dwordx4 v8, s[44:47], s55 offen lds\n"
    "s_add_u32 m0, s54, 69632\n"
    "s_add_u32 s55, %6, 589888\n"
    "buffer_load_dwordx4 v8, s[44:47], s55 offen lds\n"
    "s_add_u32 m0, s54, 106496\n"
    "s_add_u32 s55, %6, 589952\n"
    "buffer_load_dwordx4 v8, s[44:47], s55 offen lds\n"
    "s_waitcnt vmcnt(0) lgkmcnt(0)\n"
    "s_barrier\n"
    "ds_read_b64_tr_b16 a[0:1], v9\n"
    "ds_read_b64_tr_b16 a[2:3], v9 offset:256\n"
    "ds_read_b64_tr_b16 a[4:5], v9 offset:8\n"
    "ds_read_b64_tr_b16 a[6:7], v9 offset:264\n"
    "ds_read_b64_tr_b16 a[8:9], v9 offset:2304\n"
    "ds_read_b64_tr_b16 a[10:11], v9 offset:2560\n"
    "ds_read_b64_tr_b16 a[12:13], v9 offset:2312\n"
    "ds_read_b64_tr_b16 a[14:15], v9 offset:2568\n"
    "ds_read_b64_tr_b16 a[16:17], v9 offset:4608\n"
    "ds_read_b64_tr_b16 a[18:19], v9 offset:4864\n"
    "ds_read_b64_tr_b16 a[20:21], v9 offset:4616\n"
    "ds_read_b64_tr_b16 a[22:23], v9 offset:4872\n"
    "ds_read_b64_tr_b16 a[24:25], v9 offset:6912\n"
    "ds_read_b64_tr_b16 a[26:27], v9 offset:7168\n"
    "ds_read_b64_tr_b16 a[28:29], v9 offset:6920\n"
    "ds_read_b64_tr_b16 a[30:31], v9 offset:7176\n"
    "ds_read_b64_tr_b16 a[32:33], v9 offset:9216\n"
    "ds_read_b64_tr_b16 a[34:35], v9 offset:9472\n"
    "ds_read_b64_tr_b16 a[36:37], v9 offset:9224\n"
    "ds_read_b64_tr_b16 a[38:39], v9 offset:9480\n"
    "ds_read_b64_tr_b16 a[40:41], v9 offset:11520\n"
    "ds_read_b64_tr_b16 a[42:43], v9 offset:11776\n"
    "ds_read_b64_tr_b16 a[44:45], v9 offset:11528\n"
    "ds_read_b64_tr_b16 a[46:47], v9 offset:11784\n"
    "ds_read_b64_tr_b16 a[48:49], v9 offset:13824\n"
    "ds_read_b64_tr_b16 a[50:51], v9 offset:14080\n"
    "ds_read_b64_tr_b16 a[52:53], v9 offset:13832\n"
    "ds_read_b64_tr_b16 a[54:55], v9 offset:14088\n"
    "ds_read_b64_tr_b16 a[56:57], v9 offset:16128\n"
    "ds_read_b64_tr_b16 a[58:59], v9 offset:16384\n"
    "ds_read_b64_tr_b16 a[60:61], v9 offset:16136\n"
    "ds_read_b64_tr_b16 a[62:63], v9 offset:16392\n"
    "ds_read_b64_tr_b16 a[64:65], v9 offset:18432\n"
    "ds_read_b64_tr_b16 a[66:67], v9 offset:18688\n"
    "ds_read_b64_tr_b16 a[68:69], v9 offset:18440\n"
    "ds_read_b64_tr_b16 a[70:71], v9 offset:18696\n"
    "ds_read_b64_tr_b16 a[72:73], v9 offset:20736\n"
    "ds_read_b64_tr_b16 a[74:75], v9 offset:20992\n"
    "ds_read_b64_tr_b16 a[76:77], v9 offset:20744\n"
    "ds_read_b64_tr_b16 a[78:79], v9 offset:21000\n"
    "ds_read_b64_tr_b16 a[80:81], v9 offset:23040\n"
    "ds_read_b64_tr_b16 a[82:83], v9 offset:23296\n"
    "ds_read_b64_tr_b16 a[84:85], v9 offset:23048\n"
    "ds_read_b64_tr_b16 a[86:87], v9 offset:23304\n"
    "ds_read_b64_tr_b16 a[88:89], v9 offset:25344\n"
    "ds_read_b64_tr_b16 a[90:91], v9 offset:25600\n"
    "ds_read_b64_tr_b16 a[92:93], v9 offset:25352\n"
    "ds_read_b64_tr_b16 a[94:95], v9 offset:25608\n"
    "ds_read_b64_tr_b16 a[96:97], v9 offset:27648\n"
    "ds_read_b64_tr_b16 a[98:99], v9 offset:27904\n"
    "ds_read_b64_tr_b16 a[100:101], v9 offset:27656\n"
    "ds_read_b64_tr_b16 a[102:103], v9 offset:27912\n"
    "ds_read_b64_tr_b16 a[104:105], v9 offset:29952\n"
    "ds_read_b64_tr_b16 a[106:107], v9 offset:30208\n"
    "ds_read_b64_tr_b16 a[108:109], v9 offset:29960\n"
    "ds_read_b64_tr_b16 a[110:111], v9 offset:30216\n"
    "ds_read_b64_tr_b16 a[112:113], v9 offset:32256\n"
    "ds_read_b64_tr_b16 a[114:115], v9 offset:32512\n"
    "ds_read_b64_tr_b16 a[116:117], v9 offset:32264\n"
    "ds_read_b64_tr_b16 a[118:119], v9 offset:32520\n"
    "ds_read_b64_tr_b16 a[120:121], v9 offset:34560\n"
    "ds_read_b64_tr_b16 a[122:123], v9 offset:34816\n"
    "ds_read_b64_tr_b16 a[124:125], v9 offset:34568\n"
    "ds_read_b64_tr_b16 a[126:127], v9 offset:34824\n"
    "ds_read_b64_tr_b16 a[128:129], v10\n"
    "ds_read_b64_tr_b16 a[130:131], v10 offset:2304\n"
    "ds_read_b64_tr_b16 a[132:133], v10 offset:8\n"
    "ds_read_b64_tr_b16 a[134:135], v10 offset:2312\n"
    "ds_read_b64_tr_b16 a[136:137], v10 offset:18432\n"
    "ds_read_b64_tr_b16 a[138:139], v10 offset:20736\n"
    "ds_read_b64_tr_b16 a[140:141], v10 offset:18440\n"
    "ds_read_b64_tr_b16 a[142:143], v10 offset:20744\n"
    "s_waitcnt lgkmcnt(0)\n"
    "s_barrier\n"
    "v_mov_b32 v20, 64\n"
    "global_load_dword v21, v20, %1 sc1\n"
    "s_mov_b32 s52, 0\n"
    ".Lbp1_%=:\n"
    "s_waitcnt vmcnt(0)\n"
    "v_cmp_eq_u32 vcc, 0, v21\n"
    "s_cmp_eq_u64 vcc, 0\n"
    "s_cbranch_scc1 .Lbp1_x%=\n"
    "s_add_u32 s52, s52, 1\n"
    "s_sleep 2\n"
    "global_load_dword v21, v20, %1 sc1\n"
    "s_cmp_lt_u32 s52, s53\n"
    "s_cbranch_scc1 .Lbp1_%=\n"
    "s_mov_b32 s53, 0\n"
    "v_mov_b32 v22, 0\n"
    "v_mov_b32 v23, 1\n"
    "global_store_dword v22, v23, %1 sc1\n"
    "s_waitcnt vmcnt(0)\n"
    ".Lbp1_x%=:\n"
    "v_readfirstlane_b32 s50, v21\n"
    "v_mbcnt_lo_u32_b32 v20, -1, 0\n"
    "v_mbcnt_hi_u32_b32 v20, -1, v20\n"
    "v_min_u32 v6, 7, v20\n"
    "v_lshlrev_b32 v6, 7, v6\n"
    "s_lshl_b32 s51, %2, 6\n"
    "v_add_u32 v20, s51, v20\n"
    "v_min_u32 v20, 129, v20\n"
    "v_lshlrev_b32 v20, 5, v20\n"
    "v_add_u32 v7, 0x26c00, v20\n"
    "ds_read_b128 v[20:23], v7\n"
    "s_and_b32 s50, s50, 1\n"
    "s_waitcnt lgkmcnt(0)\n"
    "v_and_b32 v20, s50, v21\n"
    "v_lshlrev_b32 v20, 13, v20\n"
    "v_and_b32 v21, -2, v21\n"
    "v_xor_b32 v21, v20, v21\n"
    "v_and_b32 v20, s50, v22\n"
    "v_lshlrev_b32 v20, 13, v20\n"
    "v_and_b32 v22, -2, v22\n"
    "v_xor_b32 v22, v20, v22\n"
    "ds_write2_b32 v7, v21, v22 offset0:1 offset1:2\n"
    "s_waitcnt lgkmcnt(0)\n"
    "s_barrier\n"
    "v_mov_b32 v7, 0x26c00\n"
    "ds_read_b128 v[20:23], v7\n"
    "s_waitcnt lgkmcnt(0)\n"
    "v_readfirstlane_b32 s49, v21\n"
    "v_add_u32 v20, v20, v6\n"
    "s_branch .Lbwait_%=\n"
    ".Lbgo_%=:\n"
    "s_waitcnt vmcnt(1)\n"
    "v_add_u32 v7, 32, v7\n"
    "ds_read_b128 v[20:23], v7\n"
    "ds_write_b128 v1, v[100:103]\n"
    "ds_write_b128 v1, v[104:107] offset:1024\n"
    "ds_write_b128 v1, v[108:111] offset:2048\n"
    "ds_write_b128 v1, v[112:115] offset:3072\n"
    "ds_write_b32 v4, v116\n"
    "ds_write_b32 v4, v117 offset:16\n"
    "ds_write_b32 v4, v118 offset:32\n"
    "ds_write_b32 v4, v119 offset:48\n"
    "v_lshlrev_b32 v8, 16, v120\n"
    "v_and_b32 v9, 0xffff0000, v120\n"
    "v_lshlrev_b32 v10, 16, v121\n"
    "v_and_b32 v11, 0xffff0000, v121\n"
    "v_lshlrev_b32 v12, 16, v122\n"
    "v_and_b32 v13, 0xffff0000, v122\n"
    "v_lshlrev_b32 v14, 16, v123\n"
    "v_and_b32 v15, 0xffff0000, v123\n"
    "v_xor_b32 v0, 0x8000, v0\n"
    "v_xor_b32 v1, 0x8000, v1\n"
    "v_xor_b32 v4, 0x8000, v4\n"
    "s_waitcnt lgkmcnt(8)\n"
    "v_readfirstlane_b32 s49, v21\n"
    "v_readfirstlane_b32 s62, v22\n"
    "v_add_u32 v20, v20, v6\n"
    "s_waitcnt lgkmcnt(0)\n"
    ".Lbtop_%=:\n"
    "s_waitcnt lgkmcnt(0)\n"
    "s_barrier\n"
    "ds_read_b128 v[24:27], v0\n"
    "ds_read_b128 v[28:31], v0 offset:1024\n"
    "ds_read_b128 v[36:39], v0 offset:3072\n"
    "ds_read_b128 v[32:35], v0 offset:2048\n"
    "ds_read_b128 v[44:47], v0 offset:5120\n"
    "ds_read_b128 v[40:43], v0 offset:4096\n"
    "global_load_dword v21, v20, %1 sc1\n"
    "s_waitcnt lgkmcnt(5)\n"
    "v_mfma_f32_16x16x32_bf16 v[8:11], a[0:3], v[24:27], v[8:11]\n"
    "ds_read_b128 v[52:55], v0 offset:7168\n"
    "ds_read_b128 v[48:51], v0 offset:6144\n"
    "v_mfma_f32_16x16x32_bf16 v[12:15], a[4:7], v[24:27], v[12:15]\n"
    "ds_read_b128 v[60:63], v0 offset:9216\n"
    "ds_read_b128 v[56:59], v0 offset:8192\n"
    "s_waitcnt lgkmcnt(8)\n"
    "v_mfma_f32_16x16x32_bf16 v[8:11], a[8:11], v[28:31], v[8:11]\n"
    "ds_read_b128 v[68:71], v0 offset:11264\n"
    "ds_read_b128 v[64:67], v0 offset:10240\n"
    "v_mfma_f32_16x16x32_bf16 v[12:15], a[12:15], v[28:31], v[12:15]\n"
    "ds_read_b128 v[76:79], v0 offset:13312\n"
    "ds_read_b128 v[72:75], v0 offset:12288\n"
    "s_waitcnt lgkmcnt(10)\n"
    "v_mfma_f32_16x16x32_bf16 v[8:11], a[16:19], v[32:35], v[8:11]\n"
    "ds_read_b128 v[84:87], v0 offset:15360\n"
    "ds_read_b128 v[80:83], v0 offset:14336\n"
    "v_mfma_f32_16x16x32_bf16 v[12:15], a[20:23], v[32:35], v[12:15]\n"
    "ds_read_b128 v[92:95], v0 offset:17408\n"
    "ds_read_b128 v[88:91], v0 offset:16384\n"
    "v_mfma_f32_16x16x32_bf16 v[8:11], a[24:27], v[36:39], v[8:11]\n"
    "v_mfma_f32_16x16x32_bf16 v[12:15], a[28:31], v[36:39], v[12:15]\n"
    "s_waitcnt lgkmcnt(12)\n"
    "v_mfma_f32_16x16x32_bf16 v[8:11], a[32:35], v[40:43], v[8:11]\n"
    "v_mfma_f32_16x16x32_bf16 v[12:15], a[36:39], v[40:43], v[12:15]\n"
    "v_mfma_f32_16x16x32_bf16 v[8:11], a[40:43], v[44:47], v[8:11]\n"
    "v_mfma_f32_16x16x32_bf16 v[12:15], a[44:47], v[44:47], v[12:15]\n"
    "s_waitcnt lgkmcnt(10)\n"
    "v_mfma_f32_16x16x32_bf16 v[8:11], a[48:51], v[48:51], v[8:11]\n"
    "v_mfma_f32_16x16x32_bf16 v[12:15], a[52:55], v[48:51], v[12:15]\n"
    "v_mfma_f32_16x16x32_bf16 v[8:11], a[56:59], v[52:55], v[8:11]\n"
    "v_mfma_f32_16x16x32_bf16 v[12:15], a[60:63], v[52:55], v[12:15]\n"
    "s_waitcnt lgkmcnt(8)\n"
    "v_mfma_f32_16x16x32_bf16 v[8:11], a[64:67], v[56:59], v[8:11]\n"
    "v_mfma_f32_16x16x32_bf16 v[12:15], a[68:71], v[56:59], v[12:15]\n"
    "v_mfma_f32_16x16x32_bf16 v[8:11], a[72:75], v[60:63], v[8:11]\n"
    "v_mfma_f32_16x16x32_bf16 v[12:15], a[76:79], v[60:63], v[12:15]\n"
    "s_waitcnt lgkmcnt(6)\n"
    "v_mfma_f32_16x16x32_bf16 v[8:11], a[80:83], v[64:67], v[8:11]\n"
    "v_mfma_f32_16x16x32_bf16 v[12:15], a[84:87], v[64:67], v[12:15]\n"
    "v_mfma_f32_16x16x32_bf16 v[8:11], a[88:91], v[68:71], v[8:11]\n"
    "v_mfma_f32_16x16x32_bf16 v[12:15], a[92:95], v[68:71], v[12:15]\n"
    "s_waitcnt lgkmcnt(4)\n"
    "v_mfma_f32_16x16x32_bf16 v[8:11], a[96:99], v[72:75], v[8:11]\n"
    "v_mfma_f32_16x16x32_bf16 v[12:15], a[100:103], v[72:75], v[12:15]\n"
    "v_mfma_f32_16x16x32_bf16 v[8:11], a[104:107], v[76:79], v[8:11]\n"
    "v_mfma_f32_16x16x32_bf16 v[12:15], a[108:111], v[76:79], v[12:15]\n"
    "s_waitcnt lgkmcnt(2)\n"
    "v_mfma_f32_16x16x32_bf16 v[8:11], a[112:115], v[80:83], v[8:11]\n"
    "v_mfma_f32_16x16x32_bf16 v[12:15], a[116:119], v[80:83], v[12:15]\n"
    "v_mfma_f32_16x16x32_bf16 v[8:11], a[120:123], v[84:87], v[8:11]\n"
    "v_mfma_f32_16x16x32_bf16 v[12:15], a[124:127], v[84:87], v[12:15]\n"
    "s_waitcnt lgkmcnt(0)\n"
    "v_mfma_f32_16x16x32_bf16 v[8:11], a[128:131], v[88:91], v[8:11]\n"
    "v_mfma_f32_16x16x32_bf16 v[12:15], a[132:135], v[88:91], v[12:15]\n"
    "v_mfma_f32_16x16x32_bf16 v[8:11], a[136:139], v[92:95], v[8:11]\n"
    "v_mfma_f32_16x16x32_bf16 v[12:15], a[140:143], v[92:95], v[12:15]\n"
    "s_nop 4\n"
    "v_cvt_pk_bf16_f32 v16, v8, v9\n"
    "v_cvt_pk_bf16_f32 v17, v10, v11\n"
    "s_nop 1\n"
    "v_cvt_pk_bf16_f32 v18, v12, v13\n"
    "v_cvt_pk_bf16_f32 v19, v14, v15\n"
    "buffer_store_dwordx4 v[16:19], v3, s[40:43], s62 offen sc1\n"
    "s_mov_b32 s56, 1\n"
    "s_waitcnt vmcnt(1)\n"
    "v_cmp_eq_u32 vcc, 0, v21\n"
    "s_cmp_eq_u64 vcc, 0\n"
    "ds_read_b32 v23, v7 offset:44\n"
    "s_cbranch_scc0 .Lbno_%=\n"
    "buffer_load_dwordx4 v[100:103], v2, s[92:95], s49 offen sc1\n"
    "buffer_load_dwordx4 v[104:107], v2, s[92:95], s49 offen offset:1024 sc1\n"
    "buffer_load_dwordx4 v[108:111], v2, s[92:95], s49 offen offset:2048 sc1\n"
    "buffer_load_dwordx4 v[112:115], v2, s[92:95], s49 offen offset:3072 sc1\n"
    "buffer_load_dwordx4 v[116:119], v5, s[92:95], s49 offen sc1\n"
    "s_sub_u32 s55, s49, 0x1000000\n"
    "buffer_load_dwordx4 v[120:123], v3, s[40:43], s55 offen sc1\n"
    "s_waitcnt vmcnt(6)\n"
    "v_mov_b32 v22, 1\n"
    "s_mov_b64 exec, 1\n"
    "ds_add_rtn_u32 v22, v7, v22 offset:48\n"
    "s_mov_b64 exec, -1\n"
    "s_waitcnt lgkmcnt(0)\n"
    "v_readfirstlane_b32 s50, v22\n"
    "s_cmp_eq_u32 s50, 3\n"
    "s_cselect_b64 exec, 1, 0\n"
    "global_store_dword v23, v22, %1 sc1\n"
    "s_mov_b64 exec, -1\n"
    "s_branch .Lbin_%=\n"
    ".Lbno_%=:\n"
    "global_load_dword v21, v20, %1 sc1\n"
    "s_waitcnt vmcnt(0)\n"
    "v_mov_b32 v22, 1\n"
    "s_mov_b64 exec, 1\n"
    "ds_add_rtn_u32 v22, v7, v22 offset:48\n"
    "s_mov_b64 exec, -1\n"
    "s_waitcnt lgkmcnt(0)\n"
    "v_readfirstlane_b32 s50, v22\n"
    "s_cmp_eq_u32 s50, 3\n"
    "s_cselect_b64 exec, 1, 0\n"
    "global_store_dword v23, v22, %1 sc1\n"
    "s_mov_b64 exec, -1\n"
    "s_mov_b32 s56, 0\n"
    "v_cmp_eq_u32 vcc, 0, v21\n"
    "s_cmp_eq_u64 vcc, 0\n"
    "s_cbranch_scc1 .Lbld_%=\n"
    "v_readfirstlane_b32 s50, v20\n"
    "s_cmp_eq_u32 s50, 192\n"
    "s_cbranch_scc1 .Lbend_%=\n"
    ".Lbwait_%=:\n"
    "global_load_dword v21, v20, %1 sc1\n"
    "s_mov_b32 s52, 0\n"
    ".Lbp2_%=:\n"
    "s_waitcnt vmcnt(0)\n"
    "v_cmp_eq_u32 vcc, 0, v21\n"
    "s_cmp_eq_u64 vcc, 0\n"
    "s_cbranch_scc1 .Lbp2_x%=\n"
    "s_add_u32 s52, s52, 1\n"
    "s_sleep 2\n"
    "global_load_dword v21, v20, %1 sc1\n"
    "s_cmp_lt_u32 s52, s53\n"
    "s_cbranch_scc1 .Lbp2_%=\n"
    "s_mov_b32 s53, 0\n"
    "v_mov_b32 v22, 0\n"
    "v_mov_b32 v23, 1\n"
    "global_store_dword v22, v23, %1 sc1\n"
    "s_waitcnt vmcnt(0)\n"
    ".Lbp2_x%=:\n"
    ".Lbld_%=:\n"
    "s_mov_b32 s94, s42\n"
    "buffer_load_dwordx4 v[100:103], v2, s[92:95], s49 offen sc1\n"
    "buffer_load_dwordx4 v[104:107], v2, s[92:95], s49 offen offset:1024 sc1\n"
    "buffer_load_dwordx4 v[108:111], v2, s[92:95], s49 offen offset:2048 sc1\n"
    "buffer_load_dwordx4 v[112:115], v2, s[92:95], s49 offen offset:3072 sc1\n"
    "buffer_load_dwordx4 v[116:119], v5, s[92:95], s49 offen sc1\n"
    "s_sub_u32 s55, s49, 0x1000000\n"
    "buffer_load_dwordx4 v[120:123], v3, s[40:43], s55 offen sc1\n"
    ".Lbin_%=:\n"
    "v_add_u32 v7, 32, v7\n"
    "ds_read_b128 v[20:23], v7\n"
    "s_waitcnt vmcnt(5)\n"
    "ds_write_b128 v1, v[100:103]\n"
    "s_waitcnt vmcnt(4)\n"
    "ds_write_b128 v1, v[104:107] offset:1024\n"
    "s_waitcnt vmcnt(3)\n"
    "ds_write_b128 v1, v[108:111] offset:2048\n"
    "s_waitcnt vmcnt(2)\n"
    "ds_write_b128 v1, v[112:115] offset:3072\n"
    "s_waitcnt vmcnt(1)\n"
    "ds_write_b32 v4, v116\n"
    "ds_write_b32 v4, v117 offset:16\n"
    "ds_write_b32 v4, v118 offset:32\n"
    "ds_write_b32 v4, v119 offset:48\n"
    "s_waitcnt vmcnt(0)\n"
    "v_lshlrev_b32 v8, 16, v120\n"
    "v_and_b32 v9, 0xffff0000, v120\n"
    "v_lshlrev_b32 v10, 16, v121\n"
    "v_and_b32 v11, 0xffff0000, v121\n"
    "v_lshlrev_b32 v12, 16, v122\n"
    "v_and_b32 v13, 0xffff0000, v122\n"
    "v_lshlrev_b32 v14, 16, v123\n"
    "v_and_b32 v15, 0xffff0000, v123\n"
    "v_xor_b32 v0, 0x8000, v0\n"
    "v_xor_b32 v1, 0x8000, v1\n"
    "v_xor_b32 v4, 0x8000, v4\n"
    "s_waitcnt lgkmcnt(8)\n"
    "v_readfirstlane_b32 s49, v21\n"
    "v_readfirstlane_b32 s62, v22\n"
    "v_add_u32 v20, v20, v6\n"
    "s_waitcnt lgkmcnt(0)\n"
    "s_branch .Lbtop_%=\n"
    ".Lbend_%=:\n"
    "s_waitcnt vmcnt(0) lgkmcnt(0)\n"
    ".Lrole5_%=:\n"
        :: "s"(wd), "s"(ws), "s"(wave), "s"(rid), "s"(P.h), "s"(P.p), "s"(P.woff), "s"(layer), "s"(w0), "s"(Wk), "s"(Wv), "s"(Wo),
           "s"(mem0), "s"(P.role) : CLOBBERS);
}

extern "C" void kernel_launch(void* const* d_in, const int* in_sizes, int n_in, void* d_out, int out_size, void* d_ws,
                              size_t ws_size, hipStream_t stream) {
    hipMemsetAsync(d_ws, 0, SYNC_BYTES, stream);
    mega_kernel<<<256, 256, 0, stream>>>((const u16*)d_in[0], (const u16*)d_in[1], (const u16*)d_in[2], (const u16*)d_in[4],
                                         (const u16*)d_in[5], (const u16*)d_in[6], (const u16*)d_in[7], (const u16*)d_in[8],
                                         (u16*)d_out, (char*)d_ws);
}
```

```cpp
#include <hip/hip_runtime.h>
#include <hip/hip_bf16.h>
#include <stdint.h>

typedef unsigned short u16;
typedef unsigned u32;
typedef unsigned long long u64;
using bf16x8 = __attribute__((ext_vector_type(8))) short;
using bf16x4 = __attribute__((ext_vector_type(4))) short;
using f32x4 = __attribute__((ext_vector_type(4))) float;
using f32x2 = __attribute__((ext_vector_type(2))) float;
using v4u = __attribute__((ext_vector_type(4))) unsigned;
using v2u = __attribute__((ext_vector_type(2))) unsigned;
typedef __bf16 bfv2 __attribute__((ext_vector_type(2)));
#define GAS __attribute__((address_space(1)))
typedef GAS unsigned gu32;
#define RLX_AGENT __ATOMIC_RELAXED, __HIP_MEMORY_SCOPE_AGENT
typedef __amdgpu_buffer_rsrc_t rsrc_t;

constexpr int S_ = 2048, D_ = 576, NSEG = 64, SEG = 32, NB = 4096, NQA = 1728;

constexpr u32 OFF_ABORT = 0;
constexpr u32 OFF_POL = 64;
constexpr u32 OFF_RDY = 128;
constexpr u32 OFF_NEVER = 192;
constexpr u32 OFF_ROLE = 256;
constexpr u32 OFF_FA = 4096;
constexpr u32 OFF_FO = OFF_FA + 8192;
constexpr u32 OFF_FB = OFF_FO + 8192;
constexpr u32 OFF_FP = OFF_FB + 8192;
constexpr u32 OFF_COB = OFF_FP + 24576;
constexpr u32 OFF_FKV = OFF_COB + 32768;
constexpr u32 SYNC_BYTES = OFF_FKV + 32768;
constexpr u32 MB2 = 2u << 20;
constexpr u32 OFF_XA = 0;
constexpr u32 OFF_XB = MB2;
constexpr u32 XB = S_ * D_ * 2;
constexpr u32 OFF_TA = 2 * MB2;
constexpr u32 OFF_TB = 6 * MB2;
constexpr u32 OFF_OA = 10 * MB2;
constexpr u32 OFF_PB = 14 * MB2;
constexpr u32 HB_ = 2 * 64 * 32 * 8192;
constexpr u32 OFF_QX = 30 * MB2;
constexpr u32 OFF_KX = OFF_QX + HB_;
constexpr u32 OFF_VX = OFF_KX + HB_;
constexpr u32 OFF_OX = OFF_VX + HB_;
constexpr u32 WS_BYTES = OFF_OX + HB_;
constexpr u32 L_TAB = 147456, L_IT = L_TAB + 11264, L_EX = 138240, L_W = 31936;

#define STAMP(i, k) do {} while (0)
#define PH(k) do {} while (0)
__device__ __forceinline__ float bf2f(u16 v) { return __uint_as_float(((u32)v) << 16); }
__device__ __forceinline__ float bflo(u32 v) { return __uint_as_float(v << 16); }
__device__ __forceinline__ float bfhi(u32 v) { return __uint_as_float(v & 0xffff0000u); }
__device__ __forceinline__ u32 pack2(float a, float b) { bfv2 v = {(__bf16)a, (__bf16)b}; return __builtin_bit_cast(u32, v); }
__device__ __forceinline__ float rbf(float f) { return (float)(__bf16)f; }
__device__ __forceinline__ v4u ld16(rsrc_t r, u32 off) { return __builtin_amdgcn_raw_buffer_load_b128(r, off, 0, 16); }
__device__ __forceinline__ v2u ld8(rsrc_t r, u32 off) { return __builtin_amdgcn_raw_buffer_load_b64(r, off, 0, 16); }
__device__ __forceinline__ void st16(rsrc_t r, u32 off, v4u v) { __builtin_amdgcn_raw_buffer_store_b128(v, r, off, 0, 16); }
__device__ __forceinline__ void st8(rsrc_t r, u32 off, v2u v) { __builtin_amdgcn_raw_buffer_store_b64(v, r, off, 0, 16); }
__device__ __forceinline__ void st4(rsrc_t r, u32 off, u32 v) { __builtin_amdgcn_raw_buffer_store_b32(v, r, off, 0, 16); }
#define DRAIN() asm volatile("s_waitcnt vmcnt(0)" ::: "memory")
#define TOUCH(p) asm volatile("global_load_dword v255, %0, off" :: "v"(p) : "v255")
#define CBAR() asm volatile("" ::: "memory")
__device__ __forceinline__ u32 ldw(char* ws, u32 off) { return __hip_atomic_load((gu32*)(ws + off), RLX_AGENT); }
__device__ __forceinline__ void setw(char* ws, u32 off) { __hip_atomic_store((gu32*)(ws + off), 1u, RLX_AGENT); }
__device__ __forceinline__ u32 fl(u32 i) { return (i >> 5) * 1024 + (i & 31) * 4; }
constexpr u32 SPIN_CAP = 1u << 20;
#define SLP_W 2
#define SLP_C 8
__device__ __forceinline__ void until(u64 t) { while (__builtin_amdgcn_s_memrealtime() < t) __builtin_amdgcn_s_sleep(8); }
#define TDEL 0
__device__ __forceinline__ bool spin_fail(char* ws, u32& spins) {
    ++spins;
    if ((spins & 63u) == 0u) {
        if (ldw(ws, OFF_ABORT)) return true;
        if (spins > SPIN_CAP) { __hip_atomic_store((gu32*)(ws + OFF_ABORT), 1u, RLX_AGENT); return true; }
    }
    return false;
}
__device__ __forceinline__ void wait_set(char* ws, u32 off) {
    u32 spins = 0;
    while (__builtin_amdgcn_ballot_w64(ldw(ws, off) == 0)) {
        if (spin_fail(ws, spins)) break;
        __builtin_amdgcn_s_sleep(SLP_W);
    }
    CBAR();
}
constexpr u32 OOB = 0x80000000u;
__device__ __forceinline__ u32 cgo(u32 cg) { return (cg >> 5) * 16384 + (cg & 31) * 256; }

template <int N, bool FIN>
__device__ __forceinline__ void t_item(int rid, u32 i, u32 j, u32 m, u32 pol, u32 w, rsrc_t ro, char* ws, rsrc_t rs) {
    const u32 lane = threadIdx.x & 63, s = i & 63, side = (((s >> 5) ^ pol ^ m) & 1) << 13;
    v4u x[N], p[N][4];
    u32 v[N];
    wait_set(ws, OFF_FB + m * 4096 + j * 128 + fl(i));
#pragma unroll
    for (int u = 0; u < N; ++u) { v[u] = cgo(12 * j + 4 * (w + u) + (lane >> 4)) + (lane & 15) * 16; x[u] = ld16(rs, OFF_TB + (i << 16) + (FIN ? side : m << 13) + v[u]); }
    wait_set(ws, OFF_FP + (m * 3 + (j >> 1)) * 4096 + (lane & 3) * 128 + fl(i));
    STAMP(i, 0);
#pragma unroll
    for (int u = 0; u < N; ++u)
#pragma unroll
        for (int k = 0; k < 4; ++k) p[u][k] = __builtin_amdgcn_raw_buffer_load_b128(rs, v[u], OFF_PB + (i << 18) + (k << 16) + ((((i >> 3) ^ pol ^ m) & 1) << 13), 16);
#pragma unroll
    for (int u = 0; u < N; ++u) {
#pragma unroll
        for (int e = 0; e < 4; ++e) {
            f32x2 a = {bflo(x[u][e]), bfhi(x[u][e])};
#pragma unroll
            for (int k = 0; k < 4; ++k) a += f32x2{bflo(p[u][k][e]), bfhi(p[u][k][e])};
            x[u][e] = pack2(a[0], a[1]);
        }
        if (FIN) __builtin_amdgcn_raw_buffer_store_b128(x[u], ro, (s * 32 + 2 * (lane & 15) + m) * 1152 + (12 * j + 4 * (w + u) + (lane >> 4)) * 16, 0, 0);
        else st16(rs, OFF_TA + ((64 + s) << 16) + side + v[u], x[u]);
    }
    STAMP(i, 1);
}
__device__ __forceinline__ bool t_last(u32* cnt) {
    DRAIN();
    u32 o = 0;
    if ((threadIdx.x & 63) == 0) o = __hip_atomic_fetch_add(cnt, 1u, __ATOMIC_RELAXED, __HIP_MEMORY_SCOPE_WORKGROUP);
    return __builtin_amdgcn_readfirstlane(o) == 2;
}
__device__ __forceinline__ void role_t(u64 t_in, int rid, u32 j, u32 m, const u16* x0, u16* out, char* ws, rsrc_t rs, char* smem) {
    const u32 lane = threadIdx.x & 63, w = __builtin_amdgcn_readfirstlane(threadIdx.x >> 6);
    u32* cnt = (u32*)smem + 64;
    cnt[threadIdx.x] = 0;
    __syncthreads();
    const rsrc_t ro = __builtin_amdgcn_make_buffer_rsrc(out, 0, XB, 0x00020000);
    if (w < 3) {
        const rsrc_t rx = __builtin_amdgcn_make_buffer_rsrc((void*)x0, 0, XB, 0x00020000);
        const u32 cg = 12 * j + 4 * w + (lane >> 4), vx = (2 * (lane & 15) + m) * 1152 + cg * 16, v = OFF_TA + (m << 13) + cgo(cg) + (lane & 15) * 16;
        for (u32 s0 = 0; s0 < 64; s0 += s0 < 4 ? 2 : 10) {
            const u32 n = s0 < 4 ? 2 : 10;
            if (s0 == 14) until(t_in + TDEL);
            v4u x[10];
#pragma unroll
            for (u32 u = 0; u < 10; ++u) x[u] = __builtin_amdgcn_raw_buffer_load_b128(rx, u < n ? vx + (s0 + u) * 36864 : OOB, 0, 0);
#pragma unroll
            for (u32 u = 0; u < 10; ++u) st16(rs, u < n ? v + ((s0 + u) << 16) : OOB, x[u]);
            if (t_last(cnt + s0) && lane < n) setw(ws, OFF_FA + m * 4096 + j * 128 + fl(s0 + lane));
        }
    }
    wait_set(ws, OFF_POL);
    const u32 pol = ldw(ws, OFF_POL);
    if (w == 3) {
        for (u32 i = 64; i < 127; ++i) t_item<3, true>(rid, i, j, m, pol, 0, ro, ws, rs);
        return;
    }
    for (u32 i = 0; i < 64; ++i) {
        t_item<1, false>(rid, i, j, m, pol, w, ro, ws, rs);
        if (t_last(cnt + 64 + i) && lane == 0) setw(ws, OFF_FA + m * 4096 + j * 128 + fl(64 + i));
    }
    t_item<1, true>(rid, 127, j, m, pol, w, ro, ws, rs);
}

#define CLOBBERS "memory", "vcc", "scc", \
    "v0", "v1", "v2", "v3", "v4", "v5", "v6", "v7", "v8", "v9", "v10", "v11", "v12", "v13", "v14", "v15", "v16", "v17", "v18", "v19", "v20", "v21", "v22", "v23", \
    "v24", "v25", "v26", "v27", "v28", "v29", "v30", "v31", "v32", "v33", "v34", "v35", "v36", "v37", "v38", "v39", "v40", "v41", "v42", "v43", "v44", "v45", "v46", "v47", \
    "v48", "v49", "v50", "v51", "v52", "v53", "v54", "v55", "v56", "v57", "v58", "v59", "v60", "v61", "v62", "v63", "v64", "v65", "v66", "v67", "v68", "v69", "v70", "v71", \
    "v72", "v73", "v74", "v75", "v76", "v77", "v78", "v79", "v80", "v81", "v82", "v83", "v84", "v85", "v86", "v87", "v88", "v89", "v90", "v91", "v92", "v93", "v94", "v95", \
    "v96", "v97", "v98", "v99", "v100", "v101", "v102", "v103", "v104", "v105", "v106", "v107", "v108", "v109", "v110", "v111", "v112", "v113", "v114", "v115", "v116", "v117", "v118", "v119", \
    "v120", "v121", "v122", "v123", "v124", "v125", "v126", "v127", "v128", "v129", "v130", "v131", "v132", "v133", "v134", "v135", "v136", "v137", "v138", "v139", "v140", "v141", "v142", "v143", \
    "v144", "v145", "v146", "v147", "v148", "v149", "v150", "v151", "v152", "v153", "v154", "v155", "v156", "v157", "v158", "v159", "v160", "v161", "v162", "v163", "v164", "v165", "v166", "v167", \
    "v168", "v169", "v170", "v171", "v172", "v173", "v174", "v175", "v176", "v177", "v178", "v179", "v180", "v181", "v182", "v183", "v184", "v185", "v186", "v187", "v188", "v189", "v190", "v191", \
    "v192", "v193", "v194", "v195", "v196", "v197", "v198", "v199", "v200", "v201", "v202", "v203", "v204", "v205", "v206", "v207", "v208", "v209", "v210", "v211", "v212", "v213", "v214", "v215", \
    "v216", "v217", "v218", "v219", "v220", "v221", "v222", "v223", "v224", "v225", "v226", "v227", "v228", "v229", "v230", "v231", "v232", "v233", "v234", "v235", "v236", "v237", "v238", "v239", \
    "v240", "v241", "v242", "v243", "v244", "v245", "v246", "v247", "v248", "v249", "v250", "v251", "v252", "v253", "v254", "v255", "a0", "a1", "a2", "a3", "a4", "a5", "a6", "a7", \
    "a8", "a9", "a10", "a11", "a12", "a13", "a14", "a15", "a16", "a17", "a18", "a19", "a20", "a21", "a22", "a23", "a24", "a25", "a26", "a27", "a28", "a29", "a30", "a31", \
    "a32", "a33", "a34", "a35", "a36", "a37", "a38", "a39", "a40", "a41", "a42", "a43", "a44", "a45", "a46", "a47", "a48", "a49", "a50", "a51", "a52", "a53", "a54", "a55", \
    "a56", "a57", "a58", "a59", "a60", "a61", "a62", "a63", "a64", "a65", "a66", "a67", "a68", "a69", "a70", "a71", "a72", "a73", "a74", "a75", "a76", "a77", "a78", "a79", \
    "a80", "a81", "a82", "a83", "a84", "a85", "a86", "a87", "a88", "a89", "a90", "a91", "a92", "a93", "a94", "a95", "a96", "a97", "a98", "a99", "a100", "a101", "a102", "a103", \
    "a104", "a105", "a106", "a107", "a108", "a109", "a110", "a111", "a112", "a113", "a114", "a115", "a116", "a117", "a118", "a119", "a120", "a121", "a122", "a123", "a124", "a125", "a126", "a127", \
    "a128", "a129", "a130", "a131", "a132", "a133", "a134", "a135", "a136", "a137", "a138", "a139", "a140", "a141", "a142", "a143", "a144", "a145", "a146", "a147", "a148", "a149", "a150", "a151", \
    "a152", "a153", "a154", "a155", "a156", "a157", "a158", "a159", "a160", "a161", "a162", "a163", "a164", "a165", "a166", "a167", "a168", "a169", "a170", "a171", "a172", "a173", "a174", "a175", \
    "a176", "a177", "a178", "a179", "a180", "a181", "a182", "a183", "a184", "a185", "a186", "a187", "a188", "a189", "a190", "a191", "a192", "a193", "a194", "a195", "a196", "a197", "a198", "a199", \
    "a200", "a201", "a202", "a203", "a204", "a205", "a206", "a207", "a208", "a209", "a210", "a211", "a212", "a213", "a214", "a215", "a216", "a217", "a218", "a219", "a220", "a221", "a222", "a223", \
    "a224", "a225", "a226", "a227", "a228", "a229", "a230", "a231", "a232", "a233", "a234", "a235", "a236", "a237", "a238", "a239", "a240", "a241", "a242", "a243", "a244", "a245", "a246", "a247", \
    "a248", "a249", "a250", "a251", "a252", "a253", "a254", "a255", "s38", "s39", "s40", "s41", "s42", "s43", "s44", "s45", "s46", "s47", "s48", "s49", "s50", "s51", "s52", "s53", \
    "s54", "s55", "s56", "s57", "s58", "s59", "s60", "s61", "s62", "s63", "s64", "s65", "s66", "s67", "s68", "s69", "s70", "s71", "s72", "s73", "s74", "s75", "s76", "s77", \
    "s78", "s79", "s80", "s81", "s82", "s83", "s84", "s85", "s86", "s87", "s88", "s89", "s90", "s91", "s92", "s93", "s94", "s95", "s96", "s97", "s98", "s99", "s100", "s101"
#define LANES() const int tid = threadIdx.x, lane = tid & 63, c = lane & 15, g = lane >> 4, q = c >> 2, p4 = c & 3; \
    const int wave = __builtin_amdgcn_readfirstlane(tid >> 6); u32* tab = (u32*)(smem + L_TAB) + tid; (void)q; (void)p4; (void)g
struct Par { u32 role, h, p, woff; };
__device__ __forceinline__ u32 rows(u32 i, u32 m) { return (i << 16) + (i < 64 ? m << 13 : (((i >> 5 ^ m) & 1) << 13) + 1); }
__device__ __forceinline__ u32 slot(u32 i) { return ((i & 63) << 19) + (((i >> 2 ^ i >> 6) & 1) << 13) + 1; }
__device__ __forceinline__ u32 part(u32 i, u32 m) { return (i << 18) + (((i >> 3 ^ m) & 1) << 13) + 1; }
template <class FS, class FD>
__device__ __forceinline__ u32 items(u32 K, u32 mk, u32 fin, u32 fout, FS src, FD dst, char* smem) {
    const u32 n = threadIdx.x - 1;
    auto idx = [&](u32 k) { const u32 t = k - K; return t < 2 * (64 - K) ? ((t & 1) ? K : 64) + (t >> 1) : k & 127; };
    if (threadIdx.x < 130) {
        v4u* row = (v4u*)(smem + L_IT) + 2 * threadIdx.x;
        row[0] = v4u{n + 1 == mk ? OFF_NEVER + 4 : n + 1 < 128 ? fin + fl(idx(n + 1)) : OFF_NEVER, src(idx(n + 1)), dst(idx(n)), fout + fl(idx(n - 1))};
        row[1] = v4u{0u, idx(n) & 63 ? 0u : 0xf149f2cau, (idx(n) ^ idx(n - 1)) & 64, idx(n)};
    }
    return fin + fl(mk ? 64 : 0);
}
#define KA 11
#define KB 10
#define KH 9
#define KW 9

__device__ __forceinline__ Par role_h(u32 m, u32 h, char* smem) {
    LANES();
    tab[0] = 0x10000 + wave * 8192 + lane * 16;
    tab[256] = tid * 16;
    tab[2 * 256] = cgo(16 * wave) + lane * 16;
    tab[3 * 256] = 0x6000 + c * 4;
    tab[4 * 256] = 0x10000 + (q * 16 + c) * 16 + p4 * 4;
    tab[5 * 256] = (8 * wave + g) * 8192 + (c ^ ((g | (wave & 1) << 2) << 1)) * 16;
    tab[6 * 256] = (4 * g + q) * 256 + ((2 * wave) ^ (q | (g & 1) << 2)) * 32 + p4 * 8;
    tab[7 * 256] = tab[6 * 256] ^ 32;
    const u32 f = items(KH, 0, OFF_FB + m * 4096, OFF_FKV + (h >> 2) * 4096 + (h & 3) * 256 + m * 128, [&](u32 i) { return OFF_TB + rows(i, m); }, [&](u32 i) { return OFF_QX + h * 16384 + m * 4096 + slot(i); }, smem);
    return Par{1, f, 0, h * 256};
}

__device__ __forceinline__ Par role_w(u32 m, u32 kb, u32 nb, char* smem) {
    LANES();
    tab[0] = wave * 8192 + lane * 16;
    tab[256] = wave * 32768 + lane * 16;
    tab[512] = cgo(24 * nb + 6 * wave + g) + c * 16;
    tab[768] = (256 * wave + 4 * g + (c >> 3)) * 1152 + ((lane & 7) ^ ((g & 1) << 2)) * 16;
#pragma unroll
    for (int t = 0; t < 3; ++t) {
        const int col = wave * 96 + (t < 2 ? p4 * 16 + t * 8 : 64 + p4 * 8);
        tab[(4 + t) * 256] = (col >> 7) * 2048 + (q & 1) * 1024 + (2 * g + (q >> 1)) * 128 + ((col & 127) ^ ((g & 1) << 6));
    }
    tab[7 * 256] = (g & 1) ? OOB : cgo(24 * nb + 6 * wave + 4 + (g >> 1)) + c * 16;
    const u32 f = items(KW, 0, OFF_COB + (m * 4 + kb) * 4096, OFF_FP + (m * 3 + nb) * 4096 + kb * 128, [&](u32 i) { return OFF_OX + kb * 8 * 16384 + m * 4096 + slot(i); },
          [&](u32 i) { return OFF_PB + kb * 65536 + part(i, m); }, smem);
    return Par{2, f, 0, kb * 1024 * 1152 + nb * 384};
}

__device__ __forceinline__ Par role_a2(u32 m, u32 j, char* smem) {
    LANES();
    const int hd = tid >> 2, w = wave < 3 ? wave : 2;
    tab[0] = 0x8000 + lane * 16;
    tab[256] = wave * 4096 + lane * 16;
    tab[512] = cgo(16 * wave) + lane * 16;
    tab[768] = wave < 3 ? cgo(12 * j + 4 * wave + g) + c * 16 : OOB;
    tab[4 * 256] = (16 + (hd >> 4)) * 1024 + (((hd >> 2) & 3) * 16 + 4 * (tid & 3)) * 16 + (hd & 3) * 4;
    tab[5 * 256] = wave < 2 ? 32768 + tid * 16 : OOB;
    tab[6 * 256] = (16 * wave + (lane >> 2)) * 1152 + (lane & 3) * 16;
    tab[7 * 256] = w * 36864 + p4 * 16 + (20 * (g >> 1) + 8 * (g & 1) + q) * 64;
    tab[8 * 256] = w * 36864 + p4 * 16 + (72 * g + 16 + q) * 64;
    const u32 f = items(KB, KB, OFF_FO + m * 4096, OFF_FB + m * 4096 + j * 128, [&](u32 i) { return OFF_OA + rows(i, m); }, [&](u32 i) { return OFF_TB + rows(i, m); }, smem);
    return Par{4, f, 0, j * 192};
}

__device__ __forceinline__ Par role_a1(u32 m, u32 hg, char* smem) {
    LANES();
    const int hd = 4 * hg + wave;
    tab -= 768;
    tab[0] = lane * 16;
    tab[256] = wave * 5120 + lane * 16;
    tab[2 * 256] = !(g & 1) ? cgo(2 * hd + (g >> 1)) + c * 16 : g == 1 && !p4 ? 32768 + hd * 64 + c * 4 : OOB;
    tab[3 * 256] = cgo(16 * wave) + lane * 16;
    tab[4 * 256] = wave < 2 ? 32768 + tid * 16 : OOB;
    tab[5 * 256] = tid * 16;
    tab[6 * 256] = L_EX + ((wave >> 1) * 16 + c) * 16 + (wave & 1) * 8;
    tab[7 * 256] = g ? L_EX + 4096 : tab[6 * 256] + 512;
    tab[8 * 256] = L_EX + 2048 + (g * 16 + 2 * wave + (c & 1)) * 16;
    tab[9 * 256] = 0x7000 + c * 4;
    tab[10 * 256] = (q * 16 + c) * 16 + p4 * 4;
    tab[11 * 256] = 4 * g - c;
    tab[12 * 256] = (8 * g + q) * 144 + 36 * wave + 4 * (wave & 1) + p4 * 8;
    tab[13 * 256] = (wave < 2 ? p4 >> 1 : 2) * 9216 + (8 * g + q) * 144 + 72 * (p4 & 1) + 32;
    if (tid < 2) *(v4u*)(smem + L_EX + 4096 + tid * 1024) = v4u{0u, 0u, 0u, 0u};
    const u32 f = items(KA, KA, OFF_FA + m * 4096, OFF_FO + m * 4096 + hg * 128, [&](u32 i) { return OFF_TA + rows(i, m); }, [&](u32 i) { return OFF_OA + rows(i, m); }, smem);
    return Par{3, f, 0, hg * 144};
}

#define SB() __builtin_amdgcn_sched_barrier(0)
__device__ __forceinline__ u32 now32() { return (u32)__builtin_amdgcn_s_memtime(); }
__device__ __forceinline__ Par role_chain(u32 h, u32 p, char* smem) {
    LANES();
    const int ro = 16 * (q & 1) + 4 * (q >> 1) + 8 * (g >> 1) + 2 * (g & 1);
    tab[0] = lane * 16;
    tab[256] = L_W + (wave * 15 + (wave == 3 ? 2 : 0)) * 2048 + lane * 16;
    tab[2 * 256] = (4 * wave + g) * 8192 + (c ^ (((g & 1) | wave << 1) << 1)) * 16;
    tab[3 * 256] = (16 * (g >> 1) + 4 * (g & 1) + 8 * (q >> 1) + (q & 1)) * 256 + ((2 * wave) ^ ((q & 1) | (g & 1) << 1 | (q >> 1) << 2)) * 32 + p4 * 8;
    tab[4 * 256] = tab[3 * 256] ^ 32;
    tab[5 * 256] = (2 * wave + (lane >> 5)) * 1152 + ((lane & 31) ^ (wave << 1)) * 16;
    tab[6 * 256] = ro * 512 + ((2 * wave) ^ ((g & 1) | (q >> 1) << 1)) * 32 + p4 * 8;
    tab[7 * 256] = tab[6 * 256] ^ 32;
    tab[8 * 256] = (32 * wave + (lane >> 2)) * 1152 + (lane & 3) * 16;
    tab[9 * 256] = ro * 64 + (wave & 1) * 32 + p4 * 8;
    return Par{0, h, p, 0};
}

#define MAP 3
constexpr int RW = 90, RA1 = 24, RA2 = MAP >= 2 ? 184 : 56, RTT = 58;
#define PFO 1
#define WDEL1 700
#define CDEL 1000
#define WDEL 700
#define NPOL 6
static_assert(OFF_POL == 64u);
static_assert(OFF_RDY == 128u);
static_assert(OFF_COB == 53248u);
static_assert(OFF_FKV == 86016u);
static_assert(OFF_XA == 0u);
static_assert(OFF_XB == 2097152u);
static_assert(OFF_TA == 4194304u);
static_assert(OFF_OA == 20971520u);
static_assert(OFF_NEVER == 192u);
static_assert(L_IT == 158720u);
static_assert(OFF_QX == 62914560u);
static_assert(OFF_KX == 96468992u);
static_assert(OFF_VX == 130023424u);
static_assert(OFF_OX == 163577856u);
static_assert(WS_BYTES == 197132288u);
static_assert(L_TAB == 147456u);
static_assert(L_EX == 138240u);
static_assert(L_W == 31936);
__device__ __forceinline__ void measure_pol(int side, char* ws, rsrc_t rs, char* smem) {
    const int wave = __builtin_amdgcn_readfirstlane(threadIdx.x >> 6);
    u32 best[2] = {~0u, ~0u};
    for (int n = 0; n < NPOL; ++n)
#pragma unroll
        for (int i = 0; i < 2; ++i) {
            SB();
            const u32 t0 = now32();
            SB();
            st4(rs, (wave & 1) * MB2 + (508 + 2 * i + (wave >> 1)) * 4096 + n * 128, t0);
            DRAIN();
            const u32 t = now32() - t0;
            best[i] = t < best[i] ? t : best[i];
        }
    int* d = (int*)smem;
    if ((threadIdx.x & 63) == 0) d[wave] = (int)(best[0] - best[1]);
    __syncthreads();
    if (threadIdx.x == 0) __hip_atomic_store((gu32*)(ws + OFF_POL), 4u | ((d[0] + d[2] >= 0) ^ side) | ((d[1] + d[3] >= 0) ^ side) << 1, RLX_AGENT);
}

__global__ __launch_bounds__(256, 1) void mega_kernel(const u16* __restrict__ x0, const u16* __restrict__ wqkv, const u16* __restrict__ woA,
                                                     const u16* __restrict__ mem0, const u16* __restrict__ Wq, const u16* __restrict__ Wk,
                                                     const u16* __restrict__ Wv, const u16* __restrict__ Wo, u16* out, char* ws) {
    __shared__ __attribute__((aligned(16))) char smem[163840];
    char* wd = (char*)(((uintptr_t)ws + SYNC_BYTES + MB2 - 1) & ~(uintptr_t)(MB2 - 1));
    const rsrc_t rs = __builtin_amdgcn_make_buffer_rsrc(wd, 0, WS_BYTES, 0x00020000);
    const u64 t_in = __builtin_amdgcn_s_memrealtime();
    u32 xcc;
    asm volatile("s_getreg_b32 %0, hwreg(HW_REG_XCC_ID)" : "=s"(xcc));
    xcc &= 7;
    if (threadIdx.x >= 64) {
        const char* pc; u32 n;
        asm volatile("s_getpc_b64 %0\n.Lbeg:\ns_mov_b32 %1, .Lfin-.Lbeg" : "=s"(pc), "=s"(n));
        const u32 o = ((blockIdx.x >> 3 & 3) * 192 + threadIdx.x - 64) * 128;
        if (o < n) TOUCH(pc + o);
    }
    if (threadIdx.x == 0) {
        u32 x = xcc, t;
        for (;;) {
            t = __hip_atomic_fetch_add((gu32*)(ws + OFF_ROLE + x * 64), 1u, RLX_AGENT);
            if (t < 32) break;
            x = (x + 1) & 7;
        }
        t += x * 32;
        t = t - 24 < 8 ? t + 120 : t - 144 < 8 ? t - 120 : t;
        *(u32*)smem = t;
    }
    __syncthreads();
    const int rid = __builtin_amdgcn_readfirstlane(*(const u32*)smem);
    __syncthreads();
    STAMP(159, 0);
    const int xq = (rid >> 5) & 3, t = rid & 31, layer = rid >> 7;
    const int u = t - 24;
    const int role = t < 16 ? 0 : t < 24 ? 1 : xq == 0 ? 3 : u < 2 ? 4 : xq == 1 ? 5 : 2;
    const int ix = role == 3 ? u : role == 4 ? 2 * xq - 2 + u : role == 5 ? u - 2 : (xq * 6 + u - 5) % 12;
    if (role == 0 && layer) {
        const u32 i = t * 256 + threadIdx.x;
        for (u32 k = xq; k < (xq ? xq + 1 : 4); ++k) if (i < 3888) TOUCH((const char*)wqkv + (k * 3888 + i) * 128);
        if (xq && i < 1728) TOUCH((const char*)woA + (i / 3) * 1152 + (xq - 1) * 384 + (i % 3) * 128);
        for (const u16* w : {Wk, Wv})
            for (u32 k = i; k < 9216; k += 4096) TOUCH((const char*)w + (k >> 4) * 8192 + xq * 2048 + (k & 15) * 128);
        if (PFO) for (u32 k = i; k < 9216; k += 4096) TOUCH((const char*)Wo + (xq * 9216 + k) * 128);
        for (u32 k = 0; k < 12; ++k) st16(rs, (k < 6 ? OFF_XA : OFF_XB - 6 * 262144) + k * 262144 + (xq * 4096 + i) * 16, v4u{~0u, ~0u, ~0u, ~0u});
        DRAIN();
        __syncthreads();
        if (threadIdx.x == 0) __hip_atomic_fetch_add((gu32*)(ws + OFF_RDY), 1u, RLX_AGENT);
    }
    if (rid == RW) measure_pol(xcc >> 2, ws, rs, smem);
    until(t_in + (role == 2 ? (layer ? WDEL1 : WDEL) : role == 0 && layer ? CDEL : 0));
    Par P{5, 0, 0, 0};
    const u16* w0 = role == 2 ? Wo : role == 3 ? wqkv : role == 4 ? woA : Wq;
    if (role == 0) P = role_chain(xq * 8 + (t >> 1), t & 1, smem);
    else if (role == 1) P = role_h(layer, xq * 8 + t - 16, smem);
    else if (role == 2) P = role_w(layer, ix / 3, ix % 3, smem);
    else if (role == 3) P = role_a1(layer, ix, smem);
    else if (role == 4) P = role_a2(layer ^ (MAP >= 2), ix, smem);
    else { role_t(t_in, rid, ix, layer, x0, out, ws, rs, smem); return; }
    const int wave = __builtin_amdgcn_readfirstlane(threadIdx.x >> 6);
    asm volatile(
    "s_cmp_eq_u32 %13, 1\n"
    "s_cbranch_scc1 .Lrole1_%=\n"
    "s_cmp_eq_u32 %13, 2\n"
    "s_cbranch_scc1 .Lrole2_%=\n"
    "s_cmp_eq_u32 %13, 3\n"
    "s_cbranch_scc1 .Lrole3_%=\n"
    "s_cmp_eq_u32 %13, 4\n"
    "s_cbranch_scc1 .Lrole4_%=\n"
    "s_cmp_eq_u32 %13, 5\n"
    "s_cbranch_scc1 .Lrole5_%=\n"
    "s_mov_b64 s[40:41], %0\n"
    "s_and_b32 s41, s41, 0xffff\n"
    "s_mov_b32 s42, 0xbc00000\n"
    "s_mov_b32 s43, 0x20000\n"
    "s_mov_b32 s57, 131072\n"
    "v_mbcnt_lo_u32_b32 v10, -1, 0\n"
    "v_mbcnt_hi_u32_b32 v10, -1, v10\n"
    "s_lshl_b32 s73, %2, 8\n"
    "s_add_u32 s73, s73, 147456\n"
    "v_lshl_add_u32 v10, v10, 2, s73\n"
    "ds_read_b32 v0, v10\n"
    "ds_read_b32 v2, v10 offset:1024\n"
    "ds_read_b32 v1, v10 offset:2048\n"
    "ds_read_b32 v3, v10 offset:3072\n"
    "ds_read_b32 v4, v10 offset:4096\n"
    "ds_read_b32 v5, v10 offset:5120\n"
    "ds_read_b32 v6, v10 offset:6144\n"
    "ds_read_b32 v7, v10 offset:7168\n"
    "ds_read_b32 v8, v10 offset:8192\n"
    "ds_read_b32 v9, v10 offset:9216\n"
    "s_waitcnt lgkmcnt(0)\n"
    "s_barrier\n"
    "s_lshl_b32 s60, %4, 8\n"
    "s_lshl_b32 s61, %2, 10\n"
    "s_mul_i32 s63, %4, 147456\n"
    "s_mul_i32 s62, %5, 576\n"
    "s_add_u32 s63, s63, s62\n"
    "s_mov_b64 s[44:45], %11\n"
    "s_and_b32 s45, s45, 0xffff\n"
    "s_mov_b32 s46, 0x480000\n"
    "s_mov_b32 s47, 0x20000\n"
    "s_lshl_b32 s73, s61, 1\n"
    "s_add_u32 m0, s73, 0\n"
    "s_add_u32 s62, s63, 512\n"
    "buffer_load_dwordx4 v8, s[44:47], s62 offen lds\n"
    "s_lshl_b32 s73, s61, 1\n"
    "s_add_u32 m0, s73, 1024\n"
    "s_add_u32 s62, s63, 18944\n"
    "buffer_load_dwordx4 v8, s[44:47], s62 offen lds\n"
    "s_mov_b64 vcc, 0\n"
    "s_mov_b32 vcc_lo, %7\n"
    "s_cbranch_vccz .Lsl0_%=\n"
    "s_sleep 10\n"
    ".Lsl0_%=:\n"
    "s_mov_b64 s[44:45], %8\n"
    "s_and_b32 s45, s45, 0xffff\n"
    "s_mov_b32 s46, 0x480000\n"
    "s_mov_b32 s47, 0x20000\n"
    "s_add_u32 m0, s61, 16384\n"
    "s_add_u32 s62, s60, 2097152\n"
    "buffer_load_dwordx4 v1, s[44:47], s62 offen lds\n"
    "s_add_u32 m0, s61, 20480\n"
    "s_add_u32 s62, s60, 2228224\n"
    "buffer_load_dwordx4 v1, s[44:47], s62 offen lds\n"
    "s_add_u32 m0, s61, 24576\n"
    "s_add_u32 s62, s60, 2359296\n"
    "buffer_load_dwordx4 v1, s[44:47], s62 offen lds\n"
    "s_add_u32 m0, s61, 28672\n"
    "s_add_u32 s62, s60, 2490368\n"
    "buffer_load_dwordx4 v1, s[44:47], s62 offen lds\n"
    "s_cbranch_vccz .Lsl1_%=\n"
    "s_sleep 10\n"
    ".Lsl1_%=:\n"
    "s_add_u32 m0, s61, 32768\n"
    "s_add_u32 s62, s60, 2621440\n"
    "buffer_load_dwordx4 v1, s[44:47], s62 offen lds\n"
    "s_add_u32 m0, s61, 36864\n"
    "s_add_u32 s62, s60, 2752512\n"
    "buffer_load_dwordx4 v1, s[44:47], s62 offen lds\n"
    "s_add_u32 m0, s61, 40960\n"
    "s_add_u32 s62, s60, 2883584\n"
    "buffer_load_dwordx4 v1, s[44:47], s62 offen lds\n"
    "s_add_u32 m0, s61, 45056\n"
    "s_add_u32 s62, s60, 3014656\n"
    "buffer_load_dwordx4 v1, s[44:47], s62 offen lds\n"
    "s_cbranch_vccz .Lsl2_%=\n"
    "s_sleep 10\n"
    ".Lsl2_%=:\n"
    "s_add_u32 m0, s61, 49152\n"
    "s_add_u32 s62, s60, 3145728\n"
    "buffer_load_dwordx4 v1, s[44:47], s62 offen lds\n"
    "s_add_u32 m0, s61, 53248\n"
    "s_add_u32 s62, s60, 3276800\n"
    "buffer_load_dwordx4 v1, s[44:47], s62 offen lds\n"
    "s_add_u32 m0, s61, 57344\n"
    "s_add_u32 s62, s60, 3407872\n"
    "buffer_load_dwordx4 v1, s[44:47], s62 offen lds\n"
    "s_add_u32 m0, s61, 61440\n"
    "s_add_u32 s62, s60, 3538944\n"
    "buffer_load_dwordx4 v1, s[44:47], s62 offen lds\n"
    "s_cbranch_vccz .Lsl3_%=\n"
    "s_sleep 10\n"
    ".Lsl3_%=:\n"
    "s_add_u32 m0, s61, 65536\n"
    "s_add_u32 s62, s60, 3670016\n"
    "buffer_load_dwordx4 v1, s[44:47], s62 offen lds\n"
    "s_add_u32 m0, s61, 69632\n"
    "s_add_u32 s62, s60, 3801088\n"
    "buffer_load_dwordx4 v1, s[44:47], s62 offen lds\n"
    "s_add_u32 m0, s61, 73728\n"
    "s_add_u32 s62, s60, 3932160\n"
    "buffer_load_dwordx4 v1, s[44:47], s62 offen lds\n"
    "s_add_u32 m0, s61, 77824\n"
    "s_add_u32 s62, s60, 4063232\n"
    "buffer_load_dwordx4 v1, s[44:47], s62 offen lds\n"
    "s_cbranch_vccz .Lsl4_%=\n"
    "s_sleep 10\n"
    ".Lsl4_%=:\n"
    "s_add_u32 m0, s61, 81920\n"
    "s_add_u32 s62, s60, 4194304\n"
    "buffer_load_dwordx4 v1, s[44:47], s62 offen lds\n"
    "s_add_u32 m0, s61, 86016\n"
    "s_add_u32 s62, s60, 4325376\n"
    "buffer_load_dwordx4 v1, s[44:47], s62 offen lds\n"
    "s_add_u32 m0, s61, 90112\n"
    "s_add_u32 s62, s60, 4456448\n"
    "buffer_load_dwordx4 v1, s[44:47], s62 offen lds\n"
    "s_add_u32 m0, s61, 94208\n"
    "s_add_u32 s62, s60, 4587520\n"
    "buffer_load_dwordx4 v1, s[44:47], s62 offen lds\n"
    "s_cbranch_vccz .Lsl5_%=\n"
    "s_sleep 10\n"
    ".Lsl5_%=:\n"
    "s_mov_b64 s[44:45], %9\n"
    "s_and_b32 s45, s45, 0xffff\n"
    "s_mov_b32 s46, 0x480000\n"
    "s_mov_b32 s47, 0x20000\n"
    "s_add_u32 m0, s61, 98304\n"
    "s_add_u32 s62, s60, 0\n"
    "buffer_load_dwordx4 v1, s[44:47], s62 offen lds\n"
    "s_add_u32 m0, s61, 102400\n"
    "s_add_u32 s62, s60, 131072\n"
    "buffer_load_dwordx4 v1, s[44:47], s62 offen lds\n"
    "s_add_u32 m0, s61, 106496\n"
    "s_add_u32 s62, s60, 262144\n"
    "buffer_load_dwordx4 v1, s[44:47], s62 offen lds\n"
    "s_add_u32 m0, s61, 110592\n"
    "s_add_u32 s62, s60, 393216\n"
    "buffer_load_dwordx4 v1, s[44:47], s62 offen lds\n"
    "s_cbranch_vccz .Lsl6_%=\n"
    "s_sleep 10\n"
    ".Lsl6_%=:\n"
    "s_add_u32 m0, s61, 114688\n"
    "s_add_u32 s62, s60, 524288\n"
    "buffer_load_dwordx4 v1, s[44:47], s62 offen lds\n"
    "s_add_u32 m0, s61, 118784\n"
    "s_add_u32 s62, s60, 655360\n"
    "buffer_load_dwordx4 v1, s[44:47], s62 offen lds\n"
    "s_add_u32 m0, s61, 122880\n"
    "s_add_u32 s62, s60, 786432\n"
    "buffer_load_dwordx4 v1, s[44:47], s62 offen lds\n"
    "s_add_u32 m0, s61, 126976\n"
    "s_add_u32 s62, s60, 917504\n"
    "buffer_load_dwordx4 v1, s[44:47], s62 offen lds\n"
    "s_cbranch_vccz .Lsl7_%=\n"
    "s_sleep 10\n"
    ".Lsl7_%=:\n"
    "s_add_u32 m0, s61, 131072\n"
    "s_add_u32 s62, s60, 1048576\n"
    "buffer_load_dwordx4 v1, s[44:47], s62 offen lds\n"
    "s_add_u32 m0, s61, 135168\n"
    "s_add_u32 s62, s60, 1179648\n"
    "buffer_load_dwordx4 v1, s[44:47], s62 offen lds\n"
    "s_add_u32 m0, s61, 139264\n"
    "s_add_u32 s62, s60, 1310720\n"
    "buffer_load_dwordx4 v1, s[44:47], s62 offen lds\n"
    "s_add_u32 m0, s61, 143360\n"
    "s_add_u32 s62, s60, 1441792\n"
    "buffer_load_dwordx4 v1, s[44:47], s62 offen lds\n"
    "s_cbranch_vccz .Lsl8_%=\n"
    "s_sleep 10\n"
    ".Lsl8_%=:\n"
    "s_add_u32 m0, s61, 147456\n"
    "s_add_u32 s62, s60, 1572864\n"
    "buffer_load_dwordx4 v1, s[44:47], s62 offen lds\n"
    "s_add_u32 m0, s61, 151552\n"
    "s_add_u32 s62, s60, 1703936\n"
    "buffer_load_dwordx4 v1, s[44:47], s62 offen lds\n"
    "s_add_u32 m0, s61, 155648\n"
    "s_add_u32 s62, s60, 1835008\n"
    "buffer_load_dwordx4 v1, s[44:47], s62 offen lds\n"
    "s_add_u32 m0, s61, 159744\n"
    "s_add_u32 s62, s60, 1966080\n"
    "buffer_load_dwordx4 v1, s[44:47], s62 offen lds\n"
    "s_waitcnt vmcnt(32) lgkmcnt(0)\n"
    "s_barrier\n"
    "ds_read_b64_tr_b16 v[144:145], v3 offset:16384\n"
    "ds_read_b64_tr_b16 v[146:147], v3 offset:16896\n"
    "ds_read_b64_tr_b16 v[148:149], v3 offset:24576\n"
    "ds_read_b64_tr_b16 v[150:151], v3 offset:25088\n"
    "ds_read_b64_tr_b16 v[152:153], v4 offset:16384\n"
    "ds_read_b64_tr_b16 v[154:155], v4 offset:16896\n"
    "ds_read_b64_tr_b16 v[156:157], v4 offset:24576\n"
    "ds_read_b64_tr_b16 v[158:159], v4 offset:25088\n"
    "s_waitcnt vmcnt(28) lgkmcnt(0)\n"
    "s_barrier\n"
    "s_cbranch_vccz .Lsl9_%=\n"
    "s_sleep 10\n"
    ".Lsl9_%=:\n"
    "s_add_u32 m0, s61, 16384\n"
    "s_add_u32 s62, s60, 2097152\n"
    "buffer_load_dwordx4 v1, s[44:47], s62 offen lds\n"
    "s_add_u32 m0, s61, 20480\n"
    "s_add_u32 s62, s60, 2228224\n"
    "buffer_load_dwordx4 v1, s[44:47], s62 offen lds\n"
    "s_add_u32 m0, s61, 24576\n"
    "s_add_u32 s62, s60, 2359296\n"
    "buffer_load_dwordx4 v1, s[44:47], s62 offen lds\n"
    "s_add_u32 m0, s61, 28672\n"
    "s_add_u32 s62, s60, 2490368\n"
    "buffer_load_dwordx4 v1, s[44:47], s62 offen lds\n"
    "ds_read_b64_tr_b16 v[160:161], v3 offset:32768\n"
    "ds_read_b64_tr_b16 v[162:163], v3 offset:33280\n"
    "ds_read_b64_tr_b16 v[164:165], v3 offset:40960\n"
    "ds_read_b64_tr_b16 v[166:167], v3 offset:41472\n"
    "ds_read_b64_tr_b16 v[168:169], v4 offset:32768\n"
    "ds_read_b64_tr_b16 v[170:171], v4 offset:33280\n"
    "ds_read_b64_tr_b16 v[172:173], v4 offset:40960\n"
    "ds_read_b64_tr_b16 v[174:175], v4 offset:41472\n"
    "s_waitcnt vmcnt(28) lgkmcnt(0)\n"
    "s_barrier\n"
    "s_cbranch_vccz .Lsl10_%=\n"
    "s_sleep 10\n"
    ".Lsl10_%=:\n"
    "s_add_u32 m0, s61, 32768\n"
    "s_add_u32 s62, s60, 2621440\n"
    "buffer_load_dwordx4 v1, s[44:47], s62 offen lds\n"
    "s_add_u32 m0, s61, 36864\n"
    "s_add_u32 s62, s60, 2752512\n"
    "buffer_load_dwordx4 v1, s[44:47], s62 offen lds\n"
    "s_add_u32 m0, s61, 40960\n"
    "s_add_u32 s62, s60, 2883584\n"
    "buffer_load_dwordx4 v1, s[44:47], s62 offen lds\n"
    "s_add_u32 m0, s61, 45056\n"
    "s_add_u32 s62, s60, 3014656\n"
    "buffer_load_dwordx4 v1, s[44:47], s62 offen lds\n"
    "ds_read_b64_tr_b16 v[176:177], v3 offset:49152\n"
    "ds_read_b64_tr_b16 v[178:179], v3 offset:49664\n"
    "ds_read_b64_tr_b16 v[180:181], v3 offset:57344\n"
    "ds_read_b64_tr_b16 v[182:183], v3 offset:57856\n"
    "ds_read_b64_tr_b16 v[184:185], v4 offset:49152\n"
    "ds_read_b64_tr_b16 v[186:187], v4 offset:49664\n"
    "ds_read_b64_tr_b16 v[188:189], v4 offset:57344\n"
    "ds_read_b64_tr_b16 v[190:191], v4 offset:57856\n"
    "s_waitcnt vmcnt(28) lgkmcnt(0)\n"
    "s_barrier\n"
    "s_cbranch_vccz .Lsl11_%=\n"
    "s_sleep 10\n"
    ".Lsl11_%=:\n"
    "s_add_u32 m0, s61, 49152\n"
    "s_add_u32 s62, s60, 3145728\n"
    "buffer_load_dwordx4 v1, s[44:47], s62 offen lds\n"
    "s_add_u32 m0, s61, 53248\n"
    "s_add_u32 s62, s60, 3276800\n"
    "buffer_load_dwordx4 v1, s[44:47], s62 offen lds\n"
    "s_add_u32 m0, s61, 57344\n"
    "s_add_u32 s62, s60, 3407872\n"
    "buffer_load_dwordx4 v1, s[44:47], s62 offen lds\n"
    "s_add_u32 m0, s61, 61440\n"
    "s_add_u32 s62, s60, 3538944\n"
    "buffer_load_dwordx4 v1, s[44:47], s62 offen lds\n"
    "v_add_u32 v31, 0x10000, v3\n"
    "ds_read_b64_tr_b16 v[192:193], v31\n"
    "ds_read_b64_tr_b16 v[194:195], v31 offset:512\n"
    "ds_read_b64_tr_b16 v[196:197], v31 offset:8192\n"
    "ds_read_b64_tr_b16 v[198:199], v31 offset:8704\n"
    "v_add_u32 v30, 0x10000, v4\n"
    "ds_read_b64_tr_b16 v[200:201], v30\n"
    "ds_read_b64_tr_b16 v[202:203], v30 offset:512\n"
    "ds_read_b64_tr_b16 v[204:205], v30 offset:8192\n"
    "ds_read_b64_tr_b16 v[206:207], v30 offset:8704\n"
    "s_waitcnt vmcnt(28) lgkmcnt(0)\n"
    "s_barrier\n"
    "s_cbranch_vccz .Lsl12_%=\n"
    "s_sleep 10\n"
    ".Lsl12_%=:\n"
    "s_add_u32 m0, s61, 65536\n"
    "s_add_u32 s62, s60, 3670016\n"
    "buffer_load_dwordx4 v1, s[44:47], s62 offen lds\n"
    "s_add_u32 m0, s61, 69632\n"
    "s_add_u32 s62, s60, 3801088\n"
    "buffer_load_dwordx4 v1, s[44:47], s62 offen lds\n"
    "s_add_u32 m0, s61, 73728\n"
    "s_add_u32 s62, s60, 3932160\n"
    "buffer_load_dwordx4 v1, s[44:47], s62 offen lds\n"
    "s_add_u32 m0, s61, 77824\n"
    "s_add_u32 s62, s60, 4063232\n"
    "buffer_load_dwordx4 v1, s[44:47], s62 offen lds\n"
    "ds_read_b64_tr_b16 v[208:209], v31 offset:16384\n"
    "ds_read_b64_tr_b16 v[210:211], v31 offset:16896\n"
    "ds_read_b64_tr_b16 v[212:213], v31 offset:24576\n"
    "ds_read_b64_tr_b16 v[214:215], v31 offset:25088\n"
    "ds_read_b64_tr_b16 v[216:217], v30 offset:16384\n"
    "ds_read_b64_tr_b16 v[218:219], v30 offset:16896\n"
    "ds_read_b64_tr_b16 v[220:221], v30 offset:24576\n"
    "ds_read_b64_tr_b16 v[222:223], v30 offset:25088\n"
    "s_waitcnt vmcnt(28) lgkmcnt(0)\n"
    "s_barrier\n"
    "s_cbranch_vccz .Lsl13_%=\n"
    "s_sleep 10\n"
    ".Lsl13_%=:\n"
    "s_add_u32 m0, s61, 81920\n"
    "s_add_u32 s62, s60, 4194304\n"
    "buffer_load_dwordx4 v1, s[44:47], s62 offen lds\n"
    "s_add_u32 m0, s61, 86016\n"
    "s_add_u32 s62, s60, 4325376\n"
    "buffer_load_dwordx4 v1, s[44:47], s62 offen lds\n"
    "s_add_u32 m0, s61, 90112\n"
    "s_add_u32 s62, s60, 4456448\n"
    "buffer_load_dwordx4 v1, s[44:47], s62 offen lds\n"
    "s_add_u32 m0, s61, 94208\n"
    "s_add_u32 s62, s60, 4587520\n"
    "buffer_load_dwordx4 v1, s[44:47], s62 offen lds\n"
    "ds_read_b64_tr_b16 v[224:225], v31 offset:32768\n"
    "ds_read_b64_tr_b16 v[226:227], v31 offset:33280\n"
    "ds_read_b64_tr_b16 v[228:229], v31 offset:40960\n"
    "ds_read_b64_tr_b16 v[230:231], v31 offset:41472\n"
    "ds_read_b64_tr_b16 v[232:233], v30 offset:32768\n"
    "ds_read_b64_tr_b16 v[234:235], v30 offset:33280\n"
    "ds_read_b64_tr_b16 v[236:237], v30 offset:40960\n"
    "ds_read_b64_tr_b16 v[238:239], v30 offset:41472\n"
    "s_waitcnt vmcnt(28) lgkmcnt(0)\n"
    "s_barrier\n"
    "s_cbranch_vccz .Lsl14_%=\n"
    "s_sleep 10\n"
    ".Lsl14_%=:\n"
    "s_mov_b64 s[44:45], %10\n"
    "s_and_b32 s45, s45, 0xffff\n"
    "s_mov_b32 s46, 0x480000\n"
    "s_mov_b32 s47, 0x20000\n"
    "s_add_u32 m0, s61, 98304\n"
    "s_add_u32 s62, s60, 0\n"
    "buffer_load_dwordx4 v1, s[44:47], s62 offen lds\n"
    "s_add_u32 m0, s61, 102400\n"
    "s_add_u32 s62, s60, 131072\n"
    "buffer_load_dwordx4 v1, s[44:47], s62 offen lds\n"
    "s_add_u32 m0, s61, 106496\n"
    "s_add_u32 s62, s60, 262144\n"
    "buffer_load_dwordx4 v1, s[44:47], s62 offen lds\n"
    "s_add_u32 m0, s61, 110592\n"
    "s_add_u32 s62, s60, 393216\n"
    "buffer_load_dwordx4 v1, s[44:47], s62 offen lds\n"
    "ds_read_b64_tr_b16 v[240:241], v31 offset:49152\n"
    "ds_read_b64_tr_b16 v[242:243], v31 offset:49664\n"
    "ds_read_b64_tr_b16 v[244:245], v31 offset:57344\n"
    "ds_read_b64_tr_b16 v[246:247], v31 offset:57856\n"
    "ds_read_b64_tr_b16 v[248:249], v30 offset:49152\n"
    "ds_read_b64_tr_b16 v[250:251], v30 offset:49664\n"
    "ds_read_b64_tr_b16 v[252:253], v30 offset:57344\n"
    "ds_read_b64_tr_b16 v[254:255], v30 offset:57856\n"
    "s_waitcnt vmcnt(28) lgkmcnt(0)\n"
    "s_barrier\n"
    "s_cbranch_vccz .Lsl15_%=\n"
    "s_sleep 10\n"
    ".Lsl15_%=:\n"
    "s_add_u32 m0, s61, 114688\n"
    "s_add_u32 s62, s60, 524288\n"
    "buffer_load_dwordx4 v1, s[44:47], s62 offen lds\n"
    "s_add_u32 m0, s61, 118784\n"
    "s_add_u32 s62, s60, 655360\n"
    "buffer_load_dwordx4 v1, s[44:47], s62 offen lds\n"
    "s_add_u32 m0, s61, 122880\n"
    "s_add_u32 s62, s60, 786432\n"
    "buffer_load_dwordx4 v1, s[44:47], s62 offen lds\n"
    "s_add_u32 m0, s61, 126976\n"
    "s_add_u32 s62, s60, 917504\n"
    "buffer_load_dwordx4 v1, s[44:47], s62 offen lds\n"
    "v_add_u32 v29, 0x20000, v3\n"
    "ds_read_b64_tr_b16 a[0:1], v29\n"
    "ds_read_b64_tr_b16 a[2:3], v29 offset:512\n"
    "ds_read_b64_tr_b16 a[4:5], v29 offset:8192\n"
    "ds_read_b64_tr_b16 a[6:7], v29 offset:8704\n"
    "v_add_u32 v28, 0x20000, v4\n"
    "ds_read_b64_tr_b16 a[8:9], v28\n"
    "ds_read_b64_tr_b16 a[10:11], v28 offset:512\n"
    "ds_read_b64_tr_b16 a[12:13], v28 offset:8192\n"
    "ds_read_b64_tr_b16 a[14:15], v28 offset:8704\n"
    "s_waitcnt vmcnt(28) lgkmcnt(0)\n"
    "s_barrier\n"
    "s_cbranch_vccz .Lsl16_%=\n"
    "s_sleep 10\n"
    ".Lsl16_%=:\n"
    "s_add_u32 m0, s61, 131072\n"
    "s_add_u32 s62, s60, 1048576\n"
    "buffer_load_dwordx4 v1, s[44:47], s62 offen lds\n"
    "s_add_u32 m0, s61, 135168\n"
    "s_add_u32 s62, s60, 1179648\n"
    "buffer_load_dwordx4 v1, s[44:47], s62 offen lds\n"
    "s_add_u32 m0, s61, 139264\n"
    "s_add_u32 s62, s60, 1310720\n"
    "buffer_load_dwordx4 v1, s[44:47], s62 offen lds\n"
    "s_add_u32 m0, s61, 143360\n"
    "s_add_u32 s62, s60, 1441792\n"
    "buffer_load_dwordx4 v1, s[44:47], s62 offen lds\n"
    "ds_read_b64_tr_b16 a[16:17], v29 offset:16384\n"
    "ds_read_b64_tr_b16 a[18:19], v29 offset:16896\n"
    "ds_read_b64_tr_b16 a[20:21], v29 offset:24576\n"
    "ds_read_b64_tr_b16 a[22:23], v29 offset:25088\n"
    "ds_read_b64_tr_b16 a[24:25], v28 offset:16384\n"
    "ds_read_b64_tr_b16 a[26:27], v28 offset:16896\n"
    "ds_read_b64_tr_b16 a[28:29], v28 offset:24576\n"
    "ds_read_b64_tr_b16 a[30:31], v28 offset:25088\n"
    "s_waitcnt vmcnt(28) lgkmcnt(0)\n"
    "s_barrier\n"
    "s_cbranch_vccz .Lsl17_%=\n"
    "s_sleep 10\n"
    ".Lsl17_%=:\n"
    "s_add_u32 m0, s61, 147456\n"
    "s_add_u32 s62, s60, 1572864\n"
    "buffer_load_dwordx4 v1, s[44:47], s62 offen lds\n"
    "s_add_u32 m0, s61, 151552\n"
    "s_add_u32 s62, s60, 1703936\n"
    "buffer_load_dwordx4 v1, s[44:47], s62 offen lds\n"
    "s_add_u32 m0, s61, 155648\n"
    "s_add_u32 s62, s60, 1835008\n"
    "buffer_load_dwordx4 v1, s[44:47], s62 offen lds\n"
    "s_add_u32 m0, s61, 159744\n"
    "s_add_u32 s62, s60, 1966080\n"
    "buffer_load_dwordx4 v1, s[44:47], s62 offen lds\n"
    "ds_read_b64_tr_b16 a[32:33], v3 offset:16384\n"
    "ds_read_b64_tr_b16 a[34:35], v3 offset:16896\n"
    "ds_read_b64_tr_b16 a[36:37], v3 offset:24576\n"
    "ds_read_b64_tr_b16 a[38:39], v3 offset:25088\n"
    "ds_read_b64_tr_b16 a[40:41], v4 offset:16384\n"
    "ds_read_b64_tr_b16 a[42:43], v4 offset:16896\n"
    "ds_read_b64_tr_b16 a[44:45], v4 offset:24576\n"
    "ds_read_b64_tr_b16 a[46:47], v4 offset:25088\n"
    "s_waitcnt vmcnt(28) lgkmcnt(0)\n"
    "s_barrier\n"
    "s_cbranch_vccz .Lsl18_%=\n"
    "s_sleep 10\n"
    ".Lsl18_%=:\n"
    "s_add_u32 m0, s61, 16384\n"
    "s_add_u32 s62, s60, 2097152\n"
    "buffer_load_dwordx4 v1, s[44:47], s62 offen lds\n"
    "s_add_u32 m0, s61, 20480\n"
    "s_add_u32 s62, s60, 2228224\n"
    "buffer_load_dwordx4 v1, s[44:47], s62 offen lds\n"
    "s_add_u32 m0, s61, 24576\n"
    "s_add_u32 s62, s60, 2359296\n"
    "buffer_load_dwordx4 v1, s[44:47], s62 offen lds\n"
    "s_add_u32 m0, s61, 28672\n"
    "s_add_u32 s62, s60, 2490368\n"
    "buffer_load_dwordx4 v1, s[44:47], s62 offen lds\n"
    "ds_read_b64_tr_b16 a[48:49], v3 offset:32768\n"
    "ds_read_b64_tr_b16 a[50:51], v3 offset:33280\n"
    "ds_read_b64_tr_b16 a[52:53], v3 offset:40960\n"
    "ds_read_b64_tr_b16 a[54:55], v3 offset:41472\n"
    "ds_read_b64_tr_b16 a[56:57], v4 offset:32768\n"
    "ds_read_b64_tr_b16 a[58:59], v4 offset:33280\n"
    "ds_read_b64_tr_b16 a[60:61], v4 offset:40960\n"
    "ds_read_b64_tr_b16 a[62:63], v4 offset:41472\n"
    "s_waitcnt vmcnt(28) lgkmcnt(0)\n"
    "s_barrier\n"
    "s_cbranch_vccz .Lsl19_%=\n"
    "s_sleep 10\n"
    ".Lsl19_%=:\n"
    "s_add_u32 m0, s61, 32768\n"
    "s_add_u32 s62, s60, 2621440\n"
    "buffer_load_dwordx4 v1, s[44:47], s62 offen lds\n"
    "s_add_u32 m0, s61, 36864\n"
    "s_add_u32 s62, s60, 2752512\n"
    "buffer_load_dwordx4 v1, s[44:47], s62 offen lds\n"
    "s_add_u32 m0, s61, 40960\n"
    "s_add_u32 s62, s60, 2883584\n"
    "buffer_load_dwordx4 v1, s[44:47], s62 offen lds\n"
    "s_add_u32 m0, s61, 45056\n"
    "s_add_u32 s62, s60, 3014656\n"
    "buffer_load_dwordx4 v1, s[44:47], s62 offen lds\n"
    "ds_read_b64_tr_b16 a[64:65], v3 offset:49152\n"
    "ds_read_b64_tr_b16 a[66:67], v3 offset:49664\n"
    "ds_read_b64_tr_b16 a[68:69], v3 offset:57344\n"
    "ds_read_b64_tr_b16 a[70:71], v3 offset:57856\n"
    "ds_read_b64_tr_b16 a[72:73], v4 offset:49152\n"
    "ds_read_b64_tr_b16 a[74:75], v4 offset:49664\n"
    "ds_read_b64_tr_b16 a[76:77], v4 offset:57344\n"
    "ds_read_b64_tr_b16 a[78:79], v4 offset:57856\n"
    "s_waitcnt vmcnt(28) lgkmcnt(0)\n"
    "s_barrier\n"
    "s_cbranch_vccz .Lsl20_%=\n"
    "s_sleep 10\n"
    ".Lsl20_%=:\n"
    "s_add_u32 m0, s61, 49152\n"
    "s_add_u32 s62, s60, 3145728\n"
    "buffer_load_dwordx4 v1, s[44:47], s62 offen lds\n"
    "s_add_u32 m0, s61, 53248\n"
    "s_add_u32 s62, s60, 3276800\n"
    "buffer_load_dwordx4 v1, s[44:47], s62 offen lds\n"
    "s_add_u32 m0, s61, 57344\n"
    "s_add_u32 s62, s60, 3407872\n"
    "buffer_load_dwordx4 v1, s[44:47], s62 offen lds\n"
    "s_add_u32 m0, s61, 61440\n"
    "s_add_u32 s62, s60, 3538944\n"
    "buffer_load_dwordx4 v1, s[44:47], s62 offen lds\n"
    "ds_read_b64_tr_b16 a[80:81], v31\n"
    "ds_read_b64_tr_b16 a[82:83], v31 offset:512\n"
    "ds_read_b64_tr_b16 a[84:85], v31 offset:8192\n"
    "ds_read_b64_tr_b16 a[86:87], v31 offset:8704\n"
    "ds_read_b64_tr_b16 a[88:89], v30\n"
    "ds_read_b64_tr_b16 a[90:91], v30 offset:512\n"
    "ds_read_b64_tr_b16 a[92:93], v30 offset:8192\n"
    "ds_read_b64_tr_b16 a[94:95], v30 offset:8704\n"
    "s_waitcnt vmcnt(28) lgkmcnt(0)\n"
    "s_barrier\n"
    "s_cbranch_vccz .Lsl21_%=\n"
    "s_sleep 10\n"
    ".Lsl21_%=:\n"
    "s_add_u32 m0, s61, 65536\n"
    "s_add_u32 s62, s60, 3670016\n"
    "buffer_load_dwordx4 v1, s[44:47], s62 offen lds\n"
    "s_add_u32 m0, s61, 69632\n"
    "s_add_u32 s62, s60, 3801088\n"
    "buffer_load_dwordx4 v1, s[44:47], s62 offen lds\n"
    "s_add_u32 m0, s61, 73728\n"
    "s_add_u32 s62, s60, 3932160\n"
    "buffer_load_dwordx4 v1, s[44:47], s62 offen lds\n"
    "s_add_u32 m0, s61, 77824\n"
    "s_add_u32 s62, s60, 4063232\n"
    "buffer_load_dwordx4 v1, s[44:47], s62 offen lds\n"
    "ds_read_b64_tr_b16 a[96:97], v31 offset:16384\n"
    "ds_read_b64_tr_b16 a[98:99], v31 offset:16896\n"
    "ds_read_b64_tr_b16 a[100:101], v31 offset:24576\n"
    "ds_read_b64_tr_b16 a[102:103], v31 offset:25088\n"
    "ds_read_b64_tr_b16 a[104:105], v30 offset:16384\n"
    "ds_read_b64_tr_b16 a[106:107], v30 offset:16896\n"
    "ds_read_b64_tr_b16 a[108:109], v30 offset:24576\n"
    "ds_read_b64_tr_b16 a[110:111], v30 offset:25088\n"
    "s_waitcnt vmcnt(28) lgkmcnt(0)\n"
    "s_barrier\n"
    "s_cbranch_vccz .Lsl22_%=\n"
    "s_sleep 10\n"
    ".Lsl22_%=:\n"
    "s_add_u32 m0, s61, 81920\n"
    "s_add_u32 s62, s60, 4194304\n"
    "buffer_load_dwordx4 v1, s[44:47], s62 offen lds\n"
    "s_add_u32 m0, s61, 86016\n"
    "s_add_u32 s62, s60, 4325376\n"
    "buffer_load_dwordx4 v1, s[44:47], s62 offen lds\n"
    "s_add_u32 m0, s61, 90112\n"
    "s_add_u32 s62, s60, 4456448\n"
    "buffer_load_dwordx4 v1, s[44:47], s62 offen lds\n"
    "s_add_u32 m0, s61, 94208\n"
    "s_add_u32 s62, s60, 4587520\n"
    "buffer_load_dwordx4 v1, s[44:47], s62 offen lds\n"
    "ds_read_b64_tr_b16 a[112:113], v31 offset:32768\n"
    "ds_read_b64_tr_b16 a[114:115], v31 offset:33280\n"
    "ds_read_b64_tr_b16 a[116:117], v31 offset:40960\n"
    "ds_read_b64_tr_b16 a[118:119], v31 offset:41472\n"
    "ds_read_b64_tr_b16 a[120:121], v30 offset:32768\n"
    "ds_read_b64_tr_b16 a[122:123], v30 offset:33280\n"
    "ds_read_b64_tr_b16 a[124:125], v30 offset:40960\n"
    "ds_read_b64_tr_b16 a[126:127], v30 offset:41472\n"
    "s_waitcnt vmcnt(28) lgkmcnt(0)\n"
    "s_barrier\n"
    "s_cbranch_vccz .Lsl23_%=\n"
    "s_sleep 10\n"
    ".Lsl23_%=:\n"
    "s_mov_b64 s[44:45], %8\n"
    "s_and_b32 s45, s45, 0xffff\n"
    "s_mov_b32 s46, 0x480000\n"
    "s_mov_b32 s47, 0x20000\n"
    "s_add_u32 m0, s61, 98304\n"
    "s_add_u32 s62, s60, 0\n"
    "buffer_load_dwordx4 v1, s[44:47], s62 offen lds\n"
    "s_add_u32 m0, s61, 102400\n"
    "s_add_u32 s62, s60, 131072\n"
    "buffer_load_dwordx4 v1, s[44:47], s62 offen lds\n"
    "s_add_u32 m0, s61, 106496\n"
    "s_add_u32 s62, s60, 262144\n"
    "buffer_load_dwordx4 v1, s[44:47], s62 offen lds\n"
    "s_add_u32 m0, s61, 110592\n"
    "s_add_u32 s62, s60, 393216\n"
    "buffer_load_dwordx4 v1, s[44:47], s62 offen lds\n"
    "ds_read_b64_tr_b16 a[128:129], v31 offset:49152\n"
    "ds_read_b64_tr_b16 a[130:131], v31 offset:49664\n"
    "ds_read_b64_tr_b16 a[132:133], v31 offset:57344\n"
    "ds_read_b64_tr_b16 a[134:135], v31 offset:57856\n"
    "ds_read_b64_tr_b16 a[136:137], v30 offset:49152\n"
    "ds_read_b64_tr_b16 a[138:139], v30 offset:49664\n"
    "ds_read_b64_tr_b16 a[140:141], v30 offset:57344\n"
    "ds_read_b64_tr_b16 a[142:143], v30 offset:57856\n"
    "s_waitcnt vmcnt(28) lgkmcnt(0)\n"
    "s_barrier\n"
    "s_cbranch_vccz .Lsl24_%=\n"
    "s_sleep 10\n"
    ".Lsl24_%=:\n"
    "s_add_u32 m0, s61, 114688\n"
    "s_add_u32 s62, s60, 524288\n"
    "buffer_load_dwordx4 v1, s[44:47], s62 offen lds\n"
    "s_add_u32 m0, s61, 118784\n"
    "s_add_u32 s62, s60, 655360\n"
    "buffer_load_dwordx4 v1, s[44:47], s62 offen lds\n"
    "s_add_u32 m0, s61, 122880\n"
    "s_add_u32 s62, s60, 786432\n"
    "buffer_load_dwordx4 v1, s[44:47], s62 offen lds\n"
    "s_add_u32 m0, s61, 126976\n"
    "s_add_u32 s62, s60, 917504\n"
    "buffer_load_dwordx4 v1, s[44:47], s62 offen lds\n"
    "ds_read_b64_tr_b16 a[144:145], v29\n"
    "ds_read_b64_tr_b16 a[146:147], v29 offset:512\n"
    "ds_read_b64_tr_b16 a[148:149], v29 offset:8192\n"
    "ds_read_b64_tr_b16 a[150:151], v29 offset:8704\n"
    "ds_read_b64_tr_b16 a[152:153], v28\n"
    "ds_read_b64_tr_b16 a[154:155], v28 offset:512\n"
    "ds_read_b64_tr_b16 a[156:157], v28 offset:8192\n"
    "ds_read_b64_tr_b16 a[158:159], v28 offset:8704\n"
    "s_waitcnt vmcnt(28) lgkmcnt(0)\n"
    "s_barrier\n"
    "s_cbranch_vccz .Lsl25_%=\n"
    "s_sleep 10\n"
    ".Lsl25_%=:\n"
    "s_add_u32 m0, s61, 131072\n"
    "s_add_u32 s62, s60, 1048576\n"
    "buffer_load_dwordx4 v1, s[44:47], s62 offen lds\n"
    "s_add_u32 m0, s61, 135168\n"
    "s_add_u32 s62, s60, 1179648\n"
    "buffer_load_dwordx4 v1, s[44:47], s62 offen lds\n"
    "s_add_u32 m0, s61, 139264\n"
    "s_add_u32 s62, s60, 1310720\n"
    "buffer_load_dwordx4 v1, s[44:47], s62 offen lds\n"
    "s_add_u32 m0, s61, 143360\n"
    "s_add_u32 s62, s60, 1441792\n"
    "buffer_load_dwordx4 v1, s[44:47], s62 offen lds\n"
    "ds_read_b64_tr_b16 a[160:161], v29 offset:16384\n"
    "ds_read_b64_tr_b16 a[162:163], v29 offset:16896\n"
    "ds_read_b64_tr_b16 a[164:165], v29 offset:24576\n"
    "ds_read_b64_tr_b16 a[166:167], v29 offset:25088\n"
    "ds_read_b64_tr_b16 a[168:169], v28 offset:16384\n"
    "ds_read_b64_tr_b16 a[170:171], v28 offset:16896\n"
    "ds_read_b64_tr_b16 a[172:173], v28 offset:24576\n"
    "ds_read_b64_tr_b16 a[174:175], v28 offset:25088\n"
    "s_waitcnt vmcnt(28) lgkmcnt(0)\n"
    "s_barrier\n"
    "s_cbranch_vccz .Lsl26_%=\n"
    "s_sleep 10\n"
    ".Lsl26_%=:\n"
    "s_add_u32 m0, s61, 147456\n"
    "s_add_u32 s62, s60, 1572864\n"
    "buffer_load_dwordx4 v1, s[44:47], s62 offen lds\n"
    "s_add_u32 m0, s61, 151552\n"
    "s_add_u32 s62, s60, 1703936\n"
    "buffer_load_dwordx4 v1, s[44:47], s62 offen lds\n"
    "s_add_u32 m0, s61, 155648\n"
    "s_add_u32 s62, s60, 1835008\n"
    "buffer_load_dwordx4 v1, s[44:47], s62 offen lds\n"
    "s_add_u32 m0, s61, 159744\n"
    "s_add_u32 s62, s60, 1966080\n"
    "buffer_load_dwordx4 v1, s[44:47], s62 offen lds\n"
    "ds_read_b64_tr_b16 a[176:177], v3 offset:16384\n"
    "ds_read_b64_tr_b16 a[178:179], v3 offset:16896\n"
    "ds_read_b64_tr_b16 a[180:181], v3 offset:24576\n"
    "ds_read_b64_tr_b16 a[182:183], v3 offset:25088\n"
    "ds_read_b64_tr_b16 a[184:185], v4 offset:16384\n"
    "ds_read_b64_tr_b16 a[186:187], v4 offset:16896\n"
    "ds_read_b64_tr_b16 a[188:189], v4 offset:24576\n"
    "ds_read_b64_tr_b16 a[190:191], v4 offset:25088\n"
    "s_waitcnt vmcnt(28) lgkmcnt(0)\n"
    "s_barrier\n"
    "s_cbranch_vccz .Lsl27_%=\n"
    "s_sleep 10\n"
    ".Lsl27_%=:\n"
    "s_mov_b64 s[44:45], %11\n"
    "s_and_b32 s45, s45, 0xffff\n"
    "s_mov_b32 s46, 0x480000\n"
    "s_mov_b32 s47, 0x20000\n"
    "s_add_u32 m0, s61, 16384\n"
    "s_add_u32 s62, s63, 0\n"
    "buffer_load_dwordx4 v5, s[44:47], s62 offen lds\n"
    "s_add_u32 m0, s61, 20480\n"
    "s_add_u32 s62, s63, 9216\n"
    "buffer_load_dwordx4 v5, s[44:47], s62 offen lds\n"
    "s_add_u32 m0, s61, 24576\n"
    "s_add_u32 s62, s63, 18432\n"
    "buffer_load_dwordx4 v5, s[44:47], s62 offen lds\n"
    "s_add_u32 m0, s61, 28672\n"
    "s_add_u32 s62, s63, 27648\n"
    "buffer_load_dwordx4 v5, s[44:47], s62 offen lds\n"
    "ds_read_b64_tr_b16 a[192:193], v3 offset:32768\n"
    "ds_read_b64_tr_b16 a[194:195], v3 offset:33280\n"
    "ds_read_b64_tr_b16 a[196:197], v3 offset:40960\n"
    "ds_read_b64_tr_b16 a[198:199], v3 offset:41472\n"
    "ds_read_b64_tr_b16 a[200:201], v4 offset:32768\n"
    "ds_read_b64_tr_b16 a[202:203], v4 offset:33280\n"
    "ds_read_b64_tr_b16 a[204:205], v4 offset:40960\n"
    "ds_read_b64_tr_b16 a[206:207], v4 offset:41472\n"
    "s_waitcnt vmcnt(28) lgkmcnt(0)\n"
    "s_barrier\n"
    "s_cbranch_vccz .Lsl28_%=\n"
    "s_sleep 10\n"
    ".Lsl28_%=:\n"
    "s_add_u32 m0, s61, 32768\n"
    "s_add_u32 s62, s63, 36864\n"
    "buffer_load_dwordx4 v5, s[44:47], s62 offen lds\n"
    "s_add_u32 m0, s61, 36864\n"
    "s_add_u32 s62, s63, 46080\n"
    "buffer_load_dwordx4 v5, s[44:47], s62 offen lds\n"
    "s_add_u32 m0, s61, 40960\n"
    "s_add_u32 s62, s63, 55296\n"
    "buffer_load_dwordx4 v5, s[44:47], s62 offen lds\n"
    "s_add_u32 m0, s61, 45056\n"
    "s_add_u32 s62, s63, 64512\n"
    "buffer_load_dwordx4 v5, s[44:47], s62 offen lds\n"
    "ds_read_b64_tr_b16 a[208:209], v3 offset:49152\n"
    "ds_read_b64_tr_b16 a[210:211], v3 offset:49664\n"
    "ds_read_b64_tr_b16 a[212:213], v3 offset:57344\n"
    "ds_read_b64_tr_b16 a[214:215], v3 offset:57856\n"
    "ds_read_b64_tr_b16 a[216:217], v4 offset:49152\n"
    "ds_read_b64_tr_b16 a[218:219], v4 offset:49664\n"
    "ds_read_b64_tr_b16 a[220:221], v4 offset:57344\n"
    "ds_read_b64_tr_b16 a[222:223], v4 offset:57856\n"
    "s_waitcnt vmcnt(28) lgkmcnt(0)\n"
    "s_barrier\n"
    "s_cbranch_vccz .Lsl29_%=\n"
    "s_sleep 10\n"
    ".Lsl29_%=:\n"
    "s_add_u32 m0, s61, 49152\n"
    "s_add_u32 s62, s63, 73728\n"
    "buffer_load_dwordx4 v5, s[44:47], s62 offen lds\n"
    "s_add_u32 m0, s61, 53248\n"
    "s_add_u32 s62, s63, 82944\n"
    "buffer_load_dwordx4 v5, s[44:47], s62 offen lds\n"
    "s_add_u32 m0, s61, 57344\n"
    "s_add_u32 s62, s63, 92160\n"
    "buffer_load_dwordx4 v5, s[44:47], s62 offen lds\n"
    "s_add_u32 m0, s61, 61440\n"
    "s_add_u32 s62, s63, 101376\n"
    "buffer_load_dwordx4 v5, s[44:47], s62 offen lds\n"
    "ds_read_b64_tr_b16 a[224:225], v31\n"
    "ds_read_b64_tr_b16 a[226:227], v31 offset:512\n"
    "ds_read_b64_tr_b16 a[228:229], v31 offset:8192\n"
    "ds_read_b64_tr_b16 a[230:231], v31 offset:8704\n"
    "ds_read_b64_tr_b16 a[232:233], v30\n"
    "ds_read_b64_tr_b16 a[234:235], v30 offset:512\n"
    "ds_read_b64_tr_b16 a[236:237], v30 offset:8192\n"
    "ds_read_b64_tr_b16 a[238:239], v30 offset:8704\n"
    "s_waitcnt vmcnt(28) lgkmcnt(0)\n"
    "s_barrier\n"
    "s_cbranch_vccz .Lsl30_%=\n"
    "s_sleep 10\n"
    ".Lsl30_%=:\n"
    "s_add_u32 m0, s61, 65536\n"
    "s_add_u32 s62, s63, 110592\n"
    "buffer_load_dwordx4 v5, s[44:47], s62 offen lds\n"
    "s_add_u32 m0, s61, 69632\n"
    "s_add_u32 s62, s63, 119808\n"
    "buffer_load_dwordx4 v5, s[44:47], s62 offen lds\n"
    "s_add_u32 m0, s61, 73728\n"
    "s_add_u32 s62, s63, 129024\n"
    "buffer_load_dwordx4 v5, s[44:47], s62 offen lds\n"
    "s_add_u32 m0, s61, 77824\n"
    "s_add_u32 s62, s63, 138240\n"
    "buffer_load_dwordx4 v5, s[44:47], s62 offen lds\n"
    "ds_read_b64_tr_b16 a[240:241], v31 offset:16384\n"
    "ds_read_b64_tr_b16 a[242:243], v31 offset:16896\n"
    "ds_read_b64_tr_b16 a[244:245], v31 offset:24576\n"
    "ds_read_b64_tr_b16 a[246:247], v31 offset:25088\n"
    "ds_read_b64_tr_b16 a[248:249], v30 offset:16384\n"
    "ds_read_b64_tr_b16 a[250:251], v30 offset:16896\n"
    "ds_read_b64_tr_b16 a[252:253], v30 offset:24576\n"
    "ds_read_b64_tr_b16 a[254:255], v30 offset:25088\n"
    "s_waitcnt vmcnt(28) lgkmcnt(0)\n"
    "s_barrier\n"
    "ds_read_b64_tr_b16 v[32:33], v31 offset:32768\n"
    "ds_read_b64_tr_b16 v[34:35], v31 offset:33280\n"
    "ds_read_b64_tr_b16 v[36:37], v31 offset:40960\n"
    "ds_read_b64_tr_b16 v[38:39], v31 offset:41472\n"
    "ds_read_b64_tr_b16 v[40:41], v30 offset:32768\n"
    "ds_read_b64_tr_b16 v[42:43], v30 offset:33280\n"
    "ds_read_b64_tr_b16 v[44:45], v30 offset:40960\n"
    "ds_read_b64_tr_b16 v[46:47], v30 offset:41472\n"
    "s_waitcnt vmcnt(24) lgkmcnt(0)\n"
    "s_barrier\n"
    "ds_read_b64_tr_b16 v[48:49], v31 offset:49152\n"
    "ds_read_b64_tr_b16 v[50:51], v31 offset:49664\n"
    "ds_read_b64_tr_b16 v[52:53], v31 offset:57344\n"
    "ds_read_b64_tr_b16 v[54:55], v31 offset:57856\n"
    "ds_read_b64_tr_b16 v[56:57], v30 offset:49152\n"
    "ds_read_b64_tr_b16 v[58:59], v30 offset:49664\n"
    "ds_read_b64_tr_b16 v[60:61], v30 offset:57344\n"
    "ds_read_b64_tr_b16 v[62:63], v30 offset:57856\n"
    "s_waitcnt vmcnt(20) lgkmcnt(0)\n"
    "s_barrier\n"
    "ds_read_b64_tr_b16 v[64:65], v29\n"
    "ds_read_b64_tr_b16 v[66:67], v29 offset:512\n"
    "ds_read_b64_tr_b16 v[68:69], v29 offset:8192\n"
    "ds_read_b64_tr_b16 v[70:71], v29 offset:8704\n"
    "ds_read_b64_tr_b16 v[72:73], v28\n"
    "ds_read_b64_tr_b16 v[74:75], v28 offset:512\n"
    "ds_read_b64_tr_b16 v[76:77], v28 offset:8192\n"
    "ds_read_b64_tr_b16 v[78:79], v28 offset:8704\n"
    "s_waitcnt vmcnt(16) lgkmcnt(0)\n"
    "s_barrier\n"
    "ds_read_b64_tr_b16 v[80:81], v29 offset:16384\n"
    "ds_read_b64_tr_b16 v[82:83], v29 offset:16896\n"
    "ds_read_b64_tr_b16 v[84:85], v29 offset:24576\n"
    "ds_read_b64_tr_b16 v[86:87], v29 offset:25088\n"
    "ds_read_b64_tr_b16 v[136:137], v28 offset:16384\n"
    "ds_read_b64_tr_b16 v[138:139], v28 offset:16896\n"
    "ds_read_b64_tr_b16 v[140:141], v28 offset:24576\n"
    "ds_read_b64_tr_b16 v[142:143], v28 offset:25088\n"
    "s_waitcnt vmcnt(12) lgkmcnt(0)\n"
    "s_barrier\n"
    "ds_read_b64_tr_b16 v[88:89], v6 offset:16384\n"
    "ds_read_b64_tr_b16 v[90:91], v6 offset:16896\n"
    "ds_read_b64_tr_b16 v[120:121], v6 offset:16640\n"
    "ds_read_b64_tr_b16 v[122:123], v6 offset:17152\n"
    "ds_read_b64_tr_b16 v[96:97], v7 offset:16384\n"
    "ds_read_b64_tr_b16 v[98:99], v7 offset:16896\n"
    "ds_read_b64_tr_b16 v[128:129], v7 offset:16640\n"
    "ds_read_b64_tr_b16 v[130:131], v7 offset:17152\n"
    "s_waitcnt vmcnt(8) lgkmcnt(0)\n"
    "s_barrier\n"
    "ds_read_b64_tr_b16 v[92:93], v6 offset:32768\n"
    "ds_read_b64_tr_b16 v[94:95], v6 offset:33280\n"
    "ds_read_b64_tr_b16 v[124:125], v6 offset:33024\n"
    "ds_read_b64_tr_b16 v[126:127], v6 offset:33536\n"
    "ds_read_b64_tr_b16 v[100:101], v7 offset:32768\n"
    "ds_read_b64_tr_b16 v[102:103], v7 offset:33280\n"
    "ds_read_b64_tr_b16 v[132:133], v7 offset:33024\n"
    "ds_read_b64_tr_b16 v[134:135], v7 offset:33536\n"
    "s_waitcnt vmcnt(4) lgkmcnt(0)\n"
    "s_barrier\n"
    "ds_read_b64_tr_b16 v[104:105], v6 offset:49152\n"
    "ds_read_b64_tr_b16 v[106:107], v6 offset:49664\n"
    "ds_read_b64_tr_b16 v[12:13], v6 offset:49408\n"
    "ds_read_b64_tr_b16 v[14:15], v6 offset:49920\n"
    "ds_read_b64_tr_b16 v[112:113], v7 offset:49152\n"
    "ds_read_b64_tr_b16 v[114:115], v7 offset:49664\n"
    "ds_read_b64_tr_b16 v[20:21], v7 offset:49408\n"
    "ds_read_b64_tr_b16 v[22:23], v7 offset:49920\n"
    "s_waitcnt vmcnt(0) lgkmcnt(0)\n"
    "s_barrier\n"
    "v_add_u32 v11, 0x10000, v6\n"
    "ds_read_b64_tr_b16 v[108:109], v11\n"
    "ds_read_b64_tr_b16 v[110:111], v11 offset:512\n"
    "ds_read_b64_tr_b16 v[16:17], v11 offset:256\n"
    "ds_read_b64_tr_b16 v[18:19], v11 offset:768\n"
    "v_add_u32 v10, 0x10000, v7\n"
    "ds_read_b64_tr_b16 v[116:117], v10\n"
    "ds_read_b64_tr_b16 v[118:119], v10 offset:512\n"
    "ds_read_b64_tr_b16 v[24:25], v10 offset:256\n"
    "ds_read_b64_tr_b16 v[26:27], v10 offset:768\n"
    "s_waitcnt lgkmcnt(0)\n"
    "s_barrier\n"
    "ds_write_b128 v2, v[88:91] offset:14336\n"
    "ds_write_b128 v2, v[92:95] offset:15360\n"
    "ds_write_b128 v2, v[96:99] offset:16384\n"
    "ds_write_b128 v2, v[100:103] offset:17408\n"
    "ds_write_b128 v2, v[104:107] offset:18432\n"
    "ds_write_b128 v2, v[108:111] offset:19456\n"
    "ds_write_b128 v2, v[112:115] offset:20480\n"
    "ds_write_b128 v2, v[116:119] offset:21504\n"
    "ds_write_b128 v2, v[120:123] offset:22528\n"
    "ds_write_b128 v2, v[124:127] offset:23552\n"
    "ds_write_b128 v2, v[128:131] offset:24576\n"
    "ds_write_b128 v2, v[132:135] offset:25600\n"
    "ds_write_b128 v2, v[12:15] offset:26624\n"
    "ds_write_b128 v2, v[16:19] offset:27648\n"
    "ds_write_b128 v2, v[20:23] offset:28672\n"
    "ds_write_b128 v2, v[24:27] offset:29696\n"
    "ds_read_b64_tr_b16 v[88:89], v9\n"
    "ds_read_b64_tr_b16 v[90:91], v9 offset:64\n"
    "ds_read_b64_tr_b16 v[92:93], v9 offset:2048\n"
    "ds_read_b64_tr_b16 v[94:95], v9 offset:2112\n"
    "ds_read_b64_tr_b16 v[96:97], v9 offset:4096\n"
    "ds_read_b64_tr_b16 v[98:99], v9 offset:4160\n"
    "ds_read_b64_tr_b16 v[100:101], v9 offset:6144\n"
    "ds_read_b64_tr_b16 v[102:103], v9 offset:6208\n"
    "ds_write_b128 v2, v[32:35]\n"
    "ds_write_b128 v2, v[36:39] offset:1024\n"
    "ds_write_b128 v2, v[40:43] offset:2048\n"
    "ds_write_b128 v2, v[44:47] offset:3072\n"
    "ds_write_b128 v2, v[48:51] offset:4096\n"
    "ds_write_b128 v2, v[52:55] offset:5120\n"
    "ds_write_b128 v2, v[56:59] offset:6144\n"
    "ds_write_b128 v2, v[60:63] offset:7168\n"
    "ds_write_b128 v2, v[64:67] offset:8192\n"
    "ds_write_b128 v2, v[68:71] offset:9216\n"
    "ds_write_b128 v2, v[72:75] offset:10240\n"
    "ds_write_b128 v2, v[76:79] offset:11264\n"
    "ds_write_b128 v2, v[80:83] offset:12288\n"
    "ds_write_b128 v2, v[84:87] offset:13312\n"
    "v_lshrrev_b32 v112, 4, v0\n"
    "v_and_b32 v113, 15, v112\n"
    "v_lshrrev_b32 v114, 4, v112\n"
    "v_mov_b32 v11, 0\n"
    "v_and_b32 v115, 7, v112\n"
    "v_mul_u32_u24 v115, 1152, v115\n"
    "v_lshrrev_b32 v116, 3, v112\n"
    "v_lshl_add_u32 v117, v116, 4, v115\n"
    "v_and_b32 v115, 1, v114\n"
    "v_lshrrev_b32 v116, 1, v114\n"
    "v_mov_b32 v120, 0\n"
    "v_mov_b32 v121, 0\n"
    "v_mov_b32 v122, 0\n"
    "v_mov_b32 v123, 0\n"
    "ds_write_b128 v11, v[120:123] offset:31872\n"
    "v_mov_b32 v124, 0x3f803f80\n"
    "v_mov_b32 v125, 0x3f803f80\n"
    "v_mov_b32 v126, 0x3f803f80\n"
    "v_mov_b32 v127, 0x3f803f80\n"
    "ds_write_b128 v11, v[124:127] offset:31888\n"
    "v_mov_b32 v128, 0x3f003f00\n"
    "v_mov_b32 v129, 0x3f003f00\n"
    "v_mov_b32 v130, 0x3f003f00\n"
    "v_mov_b32 v131, 0x3f003f00\n"
    "ds_write_b128 v11, v[128:131] offset:31904\n"
    "s_waitcnt lgkmcnt(0)\n"
    "s_cmp_lt_u32 %2, 2\n"
    "s_cbranch_scc1 .Lsk_%=\n"
    "ds_write_b128 v2, v[88:91] offset:30720\n"
    "ds_write_b128 v2, v[92:95] offset:31744\n"
    "ds_write_b128 v2, v[96:99] offset:32768\n"
    "ds_write_b128 v2, v[100:103] offset:33792\n"
    ".Lsk_%=:\n"
    "s_cmp_eq_u32 %2, 3\n"
    "s_cbranch_scc1 .Lsm_%=\n"
    "s_mov_b64 s[44:45], %12\n"
    "s_and_b32 s45, s45, 0xffff\n"
    "s_mov_b32 s46, 0x2400\n"
    "s_mov_b32 s47, 0x20000\n"
    "s_mul_i32 s62, %2, 384\n"
    "s_mul_i32 s61, %2, 3072\n"
    "s_add_u32 m0, s61, 20480\n"
    "s_add_u32 s73, s62, 0\n"
    "buffer_load_dwordx4 v117, s[44:47], s73 offen lds\n"
    "s_add_u32 m0, s61, 21504\n"
    "s_add_u32 s73, s62, 128\n"
    "buffer_load_dwordx4 v117, s[44:47], s73 offen lds\n"
    "s_add_u32 m0, s61, 22528\n"
    "s_add_u32 s73, s62, 256\n"
    "buffer_load_dwordx4 v117, s[44:47], s73 offen lds\n"
    ".Lsm_%=:\n"
    "s_waitcnt vmcnt(0) lgkmcnt(0)\n"
    "s_barrier\n"
    "v_mov_b32 v1, 0x4444\n"
    "v_mov_b32 v118, 0xeeee\n"
    "v_cmp_gt_u32 vcc, 8, v113\n"
    "s_nop 1\n"
    "v_cndmask_b32 v1, v118, v1, vcc\n"
    "s_lshl_b32 s73, %2, 10\n"
    "v_add_u32 v5, s73, v0\n"
    "v_and_b32 v119, 7, v113\n"
    "v_lshrrev_b32 v120, 3, v113\n"
    "v_lshl_add_u32 v3, v115, 1, v116\n"
    "v_mul_u32_u24 v3, 544, v3\n"
    "v_lshl_add_u32 v3, v113, 2, v3\n"
    "s_lshl_b32 s73, %2, 6\n"
    "s_add_u32 s73, s73, 20480\n"
    "v_add_u32 v3, s73, v3\n"
    "v_mul_u32_u24 v4, 272, v119\n"
    "v_lshl_add_u32 v4, v114, 4, v4\n"
    "v_lshlrev_b32 v6, 7, v114\n"
    "v_lshl_add_u32 v6, v119, 4, v6\n"
    "s_lshl_b32 s73, %2, 9\n"
    "v_add_u32 v6, s73, v6\n"
    "s_mov_b32 s56, 0\n"
    "v_mov_b32 v134, 64\n"
    ".Lpoll1_%=:\n"
    "global_load_dword v135, v134, %1 sc1\n"
    "s_waitcnt vmcnt(0)\n"
    "v_readfirstlane_b32 s73, v135\n"
    "s_cmp_ge_u32 s73, 4\n"
    "s_cbranch_scc1 .Lpoll1_x%=\n"
    "s_add_u32 s56, s56, 1\n"
    "s_cmp_lt_u32 s56, 3\n"
    "s_cbranch_scc1 .Lpoll1_%=\n"
    "s_sleep 8\n"
    "s_cmp_lt_u32 s56, s57\n"
    "s_cbranch_scc1 .Lpoll1_%=\n"
    "s_mov_b32 s57, 0\n"
    "v_mov_b32 v135, 1\n"
    "global_store_dword v11, v135, %1 sc1\n"
    ".Lpoll1_x%=:\n"
    "s_xor_b32 s69, s73, %7\n"
    "s_lshr_b32 s51, s73, 1\n"
    "s_xor_b32 s51, s51, %7\n"
    "s_and_b32 s51, s51, 1\n"
    "s_lshl_b32 s51, s51, 13\n"
    "s_add_u32 s68, s51, 0x200000\n"
    "s_and_b32 s60, s69, 1\n"
    "s_lshl_b32 s60, s60, 13\n"
    "v_lshl_add_u32 v8, v120, 2, v115\n"
    "v_lshlrev_b32 v8, 12, v8\n"
    "v_lshl_add_u32 v8, v119, 4, v8\n"
    "v_lshl_add_u32 v7, v116, 15, v8\n"
    "v_lshl_add_u32 v8, v116, 3, v8\n"
    "s_mul_i32 s73, %5, 589824\n"
    "s_lshl_b32 s51, %4, 7\n"
    "s_add_u32 s73, s73, s51\n"
    "s_add_u32 s73, s73, s60\n"
    "s_lshl_b32 s51, %2, 16\n"
    "s_add_u32 s51, s51, s73\n"
    "v_add_u32 v7, s51, v7\n"
    "s_and_b32 s51, %2, 1\n"
    "s_lshl_b32 s51, s51, 15\n"
    "s_add_u32 s51, s51, 524288\n"
    "s_add_u32 s51, s51, s73\n"
    "v_add_u32 v8, s51, v8\n"
    "s_mov_b32 s74, 262144\n"
    "s_lshl_b32 s61, %4, 1\n"
    "s_add_u32 s61, s61, %5\n"
    "s_lshr_b32 s73, s61, 2\n"
    "s_add_u32 s73, s73, 128\n"
    "s_and_b32 s51, s61, 3\n"
    "s_cmp_eq_u32 s51, 0\n"
    "s_cselect_b32 s71, s73, -1\n"
    "s_lshl_b32 s73, s61, 1\n"
    "s_add_u32 s73, s73, %2\n"
    "s_cmp_lt_u32 %2, 2\n"
    "s_cselect_b32 s71, s73, s71\n"
    "s_max_i32 s61, s71, 0\n"
    "s_lshr_b32 s73, s61, 1\n"
    "s_lshl_b32 s73, s73, 14\n"
    "s_and_b32 s51, s61, 1\n"
    "s_lshl_b32 s51, s51, 12\n"
    "s_add_u32 s65, s73, s51\n"
    "s_add_u32 s65, s65, s60\n"
    "s_cmp_ge_u32 s61, 48\n"
    "s_cselect_b32 s94, 1, 0\n"
    "s_cmp_ge_u32 s61, 96\n"
    "s_cselect_b32 s94, 2, s94\n"
    "s_mul_i32 s73, s94, 48\n"
    "s_sub_u32 s73, s61, s73\n"
    "s_lshl_b32 s73, s73, 6\n"
    "v_lshlrev_b32 v9, 4, v114\n"
    "v_add_u32 v9, s73, v9\n"
    "v_bfrev_b32 v118, 1\n"
    "v_cmp_eq_u32 vcc, 0, v113\n"
    "s_nop 1\n"
    "v_cndmask_b32 v9, v118, v9, vcc\n"
    "s_mul_i32 s73, %2, 3072\n"
    "v_add_u32 v10, s73, v0\n"
    "s_cmp_eq_u32 %2, 3\n"
    "s_cbranch_scc0 .Lnw3_%=\n"
    "v_lshl_add_u32 v10, v113, 1, %5\n"
    "v_lshlrev_b32 v118, 3, v114\n"
    "v_sub_u32 v10, v10, v118\n"
    "v_cmp_le_i32 s[76:77], 0, v10\n"
    "v_cmp_le_i32 s[78:79], 2, v10\n"
    "v_cmp_le_i32 s[80:81], 4, v10\n"
    "v_cmp_le_i32 s[82:83], 6, v10\n"
    "v_cmp_le_i32 s[84:85], 1, v10\n"
    "v_cmp_le_i32 s[86:87], 3, v10\n"
    "v_cmp_le_i32 s[88:89], 5, v10\n"
    "v_cmp_le_i32 s[90:91], 7, v10\n"
    ".Lnw3_%=:\n"
    "s_lshr_b32 s73, %4, 2\n"
    "s_lshl1_add_u32 s73, s73, %7\n"
    "s_and_b32 s51, %4, 3\n"
    "s_lshl3_add_u32 s73, s73, s51\n"
    "s_lshl_b32 s73, s73, 8\n"
    "s_add_u32 s92, s73, 86016\n"
    "s_lshr_b32 s51, %4, 3\n"
    "s_lshl2_add_u32 s73, %5, s51\n"
    "s_lshl1_add_u32 s73, s73, %7\n"
    "s_and_b32 s51, %4, 7\n"
    "s_lshl4_add_u32 s73, s73, s51\n"
    "s_lshl_b32 s73, s73, 7\n"
    "s_add_u32 s93, s73, 53248\n"
    "s_lshl_b32 s73, %5, 12\n"
    "s_lshl_b32 s51, %4, 14\n"
    "s_add_u32 s73, s73, s51\n"
    "s_add_u32 s75, s73, 0x9c00000\n"
    "s_mov_b32 s48, 0\n"
    "s_mov_b32 s54, 68\n"
    "s_mov_b32 s55, 114\n"
    "s_mov_b32 s96, 1280\n"
    "s_mov_b32 s56, 0\n"
    "v_mov_b32 v134, 128\n"
    ".Lpoll2_%=:\n"
    "global_load_dword v135, v134, %1 sc1\n"
    "s_waitcnt vmcnt(0)\n"
    "v_readfirstlane_b32 s73, v135\n"
    "s_cmp_ge_u32 s73, 64\n"
    "s_cbranch_scc1 .Lpoll2_x%=\n"
    "s_add_u32 s56, s56, 1\n"
    "s_cmp_lt_u32 s56, 3\n"
    "s_cbranch_scc1 .Lpoll2_%=\n"
    "s_sleep 8\n"
    "s_cmp_lt_u32 s56, s57\n"
    "s_cbranch_scc1 .Lpoll2_%=\n"
    "s_mov_b32 s57, 0\n"
    "v_mov_b32 v135, 1\n"
    "global_store_dword v11, v135, %1 sc1\n"
    ".Lpoll2_x%=:\n"
    "ds_read_b128 v[32:35], v2\n"
    "ds_read_b128 v[36:39], v2 offset:1024\n"
    "ds_read_b128 v[40:43], v2 offset:2048\n"
    "ds_read_b128 v[44:47], v2 offset:3072\n"
    "ds_read_b128 v[48:51], v2 offset:4096\n"
    "ds_read_b128 v[52:55], v2 offset:5120\n"
    "ds_read_b128 v[56:59], v2 offset:6144\n"
    "ds_read_b128 v[60:63], v2 offset:7168\n"
    "ds_read_b128 v[64:67], v2 offset:8192\n"
    "ds_read_b128 v[68:71], v2 offset:9216\n"
    "ds_read_b128 v[72:75], v2 offset:10240\n"
    "ds_read_b128 v[76:79], v2 offset:11264\n"
    "ds_read_b128 v[80:83], v2 offset:12288\n"
    "ds_read_b128 v[84:87], v2 offset:13312\n"
    "v_mov_b64 v[124:125], 0\n"
    "v_mov_b64 v[126:127], 0\n"
    "v_mov_b64 v[128:129], 0\n"
    "v_mov_b64 v[130:131], 0\n"
    "s_add_u32 s73, s48, 0\n"
    "s_and_b32 s73, s73, 63\n"
    "s_lshr_b32 s51, s73, 2\n"
    "s_xor_b32 s51, s51, s69\n"
    "s_and_b32 s51, s51, 1\n"
    "s_lshl_b32 s51, s51, 13\n"
    "s_lshl_b32 s73, s73, 5\n"
    "s_add_u32 s73, s73, %4\n"
    "s_lshl_b32 s73, s73, 14\n"
    "s_add_u32 s73, s73, s51\n"
    "s_add_u32 s60, s73, 0x5c00000\n"
    "s_add_u32 s61, s73, 0x5c01000\n"
    "s_add_u32 s62, s73, 0x7c00000\n"
    "s_add_u32 s63, s73, 0x7c01000\n"
    "s_lshl_b32 s51, %5, 12\n"
    "s_add_u32 s73, s73, s51\n"
    "s_add_u32 s64, s73, 0x3c00000\n"
    "s_waitcnt lgkmcnt(0)\n"
    "s_mov_b32 s56, 0\n"
    "v_mov_b32 v134, s92\n"
    ".Lpoll3_%=:\n"
    "global_load_dword v132, v134, %1 sc1\n"
    "global_load_dword v133, v134, %1 offset:128 sc1\n"
    "s_waitcnt vmcnt(0)\n"
    "v_and_b32 v133, v132, v133\n"
    "s_nop 0\n"
    "v_readfirstlane_b32 s73, v133\n"
    "s_cmp_ge_u32 s73, 1\n"
    "s_cbranch_scc1 .Lpoll3_x%=\n"
    "s_add_u32 s56, s56, 1\n"
    "s_cmp_lt_u32 s56, 3\n"
    "s_cbranch_scc1 .Lpoll3_%=\n"
    "s_sleep 8\n"
    "s_cmp_lt_u32 s56, s57\n"
    "s_cbranch_scc1 .Lpoll3_%=\n"
    "s_mov_b32 s57, 0\n"
    "v_mov_b32 v135, 1\n"
    "global_store_dword v11, v135, %1 sc1\n"
    ".Lpoll3_x%=:\n"
    "buffer_load_dwordx4 v[12:15], v5, s[40:43], s60 offen sc1\n"
    "buffer_load_dwordx4 v[16:19], v5, s[40:43], s61 offen sc1\n"
    "buffer_load_dwordx4 v[20:23], v5, s[40:43], s62 offen sc1\n"
    "buffer_load_dwordx4 v[24:27], v5, s[40:43], s63 offen sc1\n"
    "buffer_load_dwordx4 v[28:31], v5, s[40:43], s64 offen sc1\n"
    "s_add_u32 s73, s48, 1\n"
    "s_and_b32 s51, s73, 32\n"
    "s_and_b32 s73, s73, 31\n"
    "s_lshl3_add_u32 s73, s51, s73\n"
    "s_lshl2_add_u32 s73, s73, s92\n"
    "v_mov_b32 v134, s73\n"
    "global_load_dword v132, v134, %1 sc1\n"
    "global_load_dword v133, v134, %1 offset:128 sc1\n"
    "s_add_u32 s73, s48, 1\n"
    "s_and_b32 s73, s73, 63\n"
    "s_lshr_b32 s51, s73, 2\n"
    "s_xor_b32 s51, s51, s69\n"
    "s_and_b32 s51, s51, 1\n"
    "s_lshl_b32 s51, s51, 13\n"
    "s_lshl_b32 s73, s73, 5\n"
    "s_add_u32 s73, s73, %4\n"
    "s_lshl_b32 s73, s73, 14\n"
    "s_add_u32 s73, s73, s51\n"
    "s_add_u32 s60, s73, 0x5c00000\n"
    "s_add_u32 s61, s73, 0x5c01000\n"
    "s_add_u32 s62, s73, 0x7c00000\n"
    "s_add_u32 s63, s73, 0x7c01000\n"
    "s_lshl_b32 s51, %5, 12\n"
    "s_add_u32 s73, s73, s51\n"
    "s_add_u32 s64, s73, 0x3c00000\n"
    "s_waitcnt vmcnt(6)\n"
    "ds_write_b128 v5, v[12:15]\n"
    "s_waitcnt vmcnt(5)\n"
    "ds_write_b128 v5, v[16:19] offset:4096\n"
    "s_waitcnt vmcnt(2)\n"
    "ds_write_b128 v5, v[28:31] offset:16384\n"
    "v_swap_b32 v22, v24\n"
    "v_swap_b32 v23, v25\n"
    "ds_write_b128 v5, v[20:23] offset:8192\n"
    "ds_write_b128 v5, v[24:27] offset:12288\n"
    "s_waitcnt vmcnt(0)\n"
    "v_and_b32 v133, v132, v133\n"
    "s_nop 0\n"
    "v_readfirstlane_b32 s59, v133\n"
    "s_memrealtime s[50:51]\n"
    "s_waitcnt lgkmcnt(0)\n"
    "v_mov_b32 v134, s50\n"
    "ds_write_b32 v11, v134 offset:31920\n"
    "s_mov_b32 s58, s50\n"
    "s_waitcnt lgkmcnt(0)\n"
    "s_barrier\n"
    ".Lstep_%=:\n"
    "ds_read_b128 v[88:91], v0 offset:20480\n"
    "ds_read_b128 v[92:95], v0 offset:21504\n"
    "ds_read_b128 v[96:99], v0 offset:22528\n"
    "ds_read_b128 v[100:103], v0 offset:23552\n"
    "ds_read_b128 v[104:107], v0 offset:24576\n"
    "ds_read_b128 v[108:111], v0 offset:25600\n"
    "ds_read_b128 v[112:115], v0 offset:26624\n"
    "ds_read_b128 v[116:119], v0 offset:27648\n"
    "ds_read_b128 v[120:123], v0 offset:28672\n"
    "buffer_load_dwordx4 v[12:15], v5, s[40:43], s60 offen sc1\n"
    "buffer_load_dwordx4 v[16:19], v5, s[40:43], s61 offen sc1\n"
    "buffer_load_dwordx4 v[20:23], v5, s[40:43], s62 offen sc1\n"
    "buffer_load_dwordx4 v[24:27], v5, s[40:43], s63 offen sc1\n"
    "buffer_load_dwordx4 v[28:31], v5, s[40:43], s64 offen sc1\n"
    "s_waitcnt lgkmcnt(8)\n"
    "v_smfmac_f32_16x16x64_bf16 v[124:127], v[88:91], v[32:39], v1\n"
    "v_smfmac_f32_16x16x64_bf16 v[128:131], v[88:91], v[40:47], v1\n"
    "s_waitcnt lgkmcnt(7)\n"
    "v_smfmac_f32_16x16x64_bf16 v[124:127], v[92:95], v[48:55], v1\n"
    "v_mov_b64 v[32:33], 0\n"
    "v_mov_b64 v[34:35], 0\n"
    "v_smfmac_f32_16x16x64_bf16 v[128:131], v[92:95], v[56:63], v1\n"
    "v_mov_b64 v[36:37], 0\n"
    "v_mov_b64 v[38:39], 0\n"
    "s_waitcnt lgkmcnt(6)\n"
    "v_smfmac_f32_16x16x64_bf16 v[124:127], v[96:99], v[64:71], v1\n"
    "v_smfmac_f32_16x16x64_bf16 v[128:131], v[96:99], v[72:79], v1\n"
    "s_waitcnt lgkmcnt(5)\n"
    "v_smfmac_f32_16x16x64_bf16 v[124:127], v[100:103], v[80:87], v1\n"
    "v_smfmac_f32_16x16x64_bf16 v[128:131], v[100:103], v[136:143], v1\n"
    "s_waitcnt lgkmcnt(4)\n"
    "v_smfmac_f32_16x16x64_bf16 v[124:127], v[104:107], v[144:151], v1\n"
    "ds_read_b128 v[40:43], v0\n"
    "v_smfmac_f32_16x16x64_bf16 v[128:131], v[104:107], v[152:159], v1\n"
    "ds_read_b128 v[44:47], v0 offset:4096\n"
    "s_waitcnt lgkmcnt(5)\n"
    "v_smfmac_f32_16x16x64_bf16 v[124:127], v[108:111], v[160:167], v1\n"
    "ds_read_b128 v[48:51], v0 offset:1024\n"
    "v_smfmac_f32_16x16x64_bf16 v[128:131], v[108:111], v[168:175], v1\n"
    "ds_read_b128 v[52:55], v0 offset:5120\n"
    "s_waitcnt lgkmcnt(6)\n"
    "v_smfmac_f32_16x16x64_bf16 v[124:127], v[112:115], v[176:183], v1\n"
    "ds_read_b128 v[56:59], v0 offset:2048\n"
    "v_smfmac_f32_16x16x64_bf16 v[128:131], v[112:115], v[184:191], v1\n"
    "ds_read_b128 v[60:63], v0 offset:6144\n"
    "s_waitcnt lgkmcnt(7)\n"
    "v_smfmac_f32_16x16x64_bf16 v[124:127], v[116:119], v[192:199], v1\n"
    "ds_read_b128 v[64:67], v0 offset:3072\n"
    "v_smfmac_f32_16x16x64_bf16 v[128:131], v[116:119], v[200:207], v1\n"
    "ds_read_b128 v[68:71], v0 offset:7168\n"
    "s_waitcnt lgkmcnt(8)\n"
    "v_smfmac_f32_16x16x64_bf16 v[124:127], v[120:123], v[208:215], v1\n"
    "v_smfmac_f32_16x16x64_bf16 v[128:131], v[120:123], v[216:223], v1\n"
    "v_smfmac_f32_16x16x64_bf16 v[32:35], v[88:91], v[224:231], v1\n"
    "v_smfmac_f32_16x16x64_bf16 v[36:39], v[88:91], v[232:239], v1\n"
    "v_smfmac_f32_16x16x64_bf16 v[32:35], v[92:95], v[240:247], v1\n"
    "v_permlane32_swap_b32 v124, v126\n"
    "v_permlane32_swap_b32 v128, v130\n"
    "v_smfmac_f32_16x16x64_bf16 v[36:39], v[92:95], v[248:255], v1\n"
    "v_permlane32_swap_b32 v125, v127\n"
    "v_permlane32_swap_b32 v129, v131\n"
    "v_smfmac_f32_16x16x64_bf16 v[32:35], v[96:99], a[0:7], v1\n"
    "v_add_f32 v124, v124, v126\n"
    "v_add_f32 v128, v128, v130\n"
    "v_smfmac_f32_16x16x64_bf16 v[36:39], v[96:99], a[8:15], v1\n"
    "v_add_f32 v125, v125, v127\n"
    "v_add_f32 v129, v129, v131\n"
    "v_smfmac_f32_16x16x64_bf16 v[32:35], v[100:103], a[16:23], v1\n"
    "v_cvt_pk_bf16_f32 v76, v124, v128\n"
    "v_cvt_pk_bf16_f32 v77, v125, v129\n"
    "ds_write_b32 v3, v76 offset:9216\n"
    "ds_write_b32 v3, v77 offset:9488\n"
    "v_smfmac_f32_16x16x64_bf16 v[36:39], v[100:103], a[24:31], v1\n"
    "v_smfmac_f32_16x16x64_bf16 v[32:35], v[104:107], a[32:39], v1\n"
    "v_smfmac_f32_16x16x64_bf16 v[36:39], v[104:107], a[40:47], v1\n"
    "v_smfmac_f32_16x16x64_bf16 v[32:35], v[108:111], a[48:55], v1\n"
    "s_waitcnt lgkmcnt(0)\n"
    "s_barrier\n"
    "ds_read_b128 v[124:127], v4 offset:29696\n"
    "v_smfmac_f32_16x16x64_bf16 v[36:39], v[108:111], a[56:63], v1\n"
    "ds_read_b128 v[128:131], v4 offset:29760\n"
    "v_smfmac_f32_16x16x64_bf16 v[32:35], v[112:115], a[64:71], v1\n"
    "ds_read_b128 v[132:135], v4 offset:29824\n"
    "v_smfmac_f32_16x16x64_bf16 v[36:39], v[112:115], a[72:79], v1\n"
    "ds_read_b128 v[72:75], v4 offset:29888\n"
    "v_smfmac_f32_16x16x64_bf16 v[32:35], v[116:119], a[80:87], v1\n"
    "v_smfmac_f32_16x16x64_bf16 v[36:39], v[116:119], a[88:95], v1\n"
    "v_smfmac_f32_16x16x64_bf16 v[32:35], v[120:123], a[96:103], v1\n"
    "v_smfmac_f32_16x16x64_bf16 v[36:39], v[120:123], a[104:111], v1\n"
    "s_waitcnt lgkmcnt(3)\n"
    "v_mfma_f32_16x16x32_bf16 v[80:83], v[40:43], v[124:127], 0\n"
    "v_mfma_f32_16x16x32_bf16 v[84:87], v[44:47], v[124:127], 0\n"
    "ds_read_b128 v[40:43], v11 offset:31872\n"
    "ds_read_b128 v[44:47], v11 offset:31872\n"
    "s_waitcnt lgkmcnt(4)\n"
    "v_mfma_f32_16x16x32_bf16 v[80:83], v[48:51], v[128:131], v[80:83]\n"
    "v_permlane32_swap_b32 v32, v34\n"
    "v_permlane32_swap_b32 v36, v38\n"
    "v_mfma_f32_16x16x32_bf16 v[84:87], v[52:55], v[128:131], v[84:87]\n"
    "v_permlane32_swap_b32 v33, v35\n"
    "v_permlane32_swap_b32 v37, v39\n"
    "s_waitcnt lgkmcnt(3)\n"
    "v_mfma_f32_16x16x32_bf16 v[80:83], v[56:59], v[132:135], v[80:83]\n"
    "v_add_f32 v32, v32, v34\n"
    "v_add_f32 v36, v36, v38\n"
    "v_mfma_f32_16x16x32_bf16 v[84:87], v[60:63], v[132:135], v[84:87]\n"
    "v_add_f32 v33, v33, v35\n"
    "v_add_f32 v37, v37, v39\n"
    "s_waitcnt lgkmcnt(2)\n"
    "v_mfma_f32_16x16x32_bf16 v[80:83], v[64:67], v[72:75], v[80:83]\n"
    "v_cvt_pk_bf16_f32 v76, v32, v36\n"
    "v_cvt_pk_bf16_f32 v77, v33, v37\n"
    "ds_write2_b32 v3, v76, v77 offset1:68\n"
    "v_mfma_f32_16x16x32_bf16 v[84:87], v[68:71], v[72:75], v[84:87]\n"
    "s_waitcnt lgkmcnt(2)\n"
    "v_smfmac_f32_16x16x64_bf16 v[40:43], v[88:91], a[112:119], v1\n"
    "s_waitcnt lgkmcnt(1)\n"
    "v_smfmac_f32_16x16x64_bf16 v[44:47], v[88:91], a[120:127], v1\n"
    "s_waitcnt lgkmcnt(0)\n"
    "s_barrier\n"
    "ds_read_b128 v[48:51], v4 offset:20480\n"
    "ds_read_b128 v[52:55], v4 offset:20544\n"
    "ds_read_b128 v[56:59], v4 offset:20608\n"
    "ds_read_b128 v[60:63], v4 offset:20672\n"
    "v_smfmac_f32_16x16x64_bf16 v[40:43], v[92:95], a[128:135], v1\n"
    "v_maximum3_f32 v78, v80, v81, v82\n"
    "v_maximum3_f32 v78, v78, v83, v84\n"
    "v_smfmac_f32_16x16x64_bf16 v[44:47], v[92:95], a[136:143], v1\n"
    "v_maximum3_f32 v78, v78, v85, v86\n"
    "v_maximum3_f32 v78, v78, v87, v87\n"
    "ds_read_b128 v[32:35], v5 offset:8192\n"
    "ds_read_b128 v[36:39], v5 offset:12288\n"
    "v_smfmac_f32_16x16x64_bf16 v[40:43], v[96:99], a[144:151], v1\n"
    "v_mov_b32 v79, v78\n"
    "ds_read_b128 v[68:71], v11 offset:31888\n"
    "ds_read_b64 v[76:77], v11 offset:31904\n"
    "v_smfmac_f32_16x16x64_bf16 v[44:47], v[96:99], a[152:159], v1\n"
    "v_permlane32_swap_b32 v79, v78\n"
    "v_maximum3_f32 v78, v78, v79, v79\n"
    "s_waitcnt lgkmcnt(7)\n"
    "v_mfma_f32_16x16x32_bf16 v[64:67], v[48:51], v[124:127], 0\n"
    "ds_read_b128 v[88:91], v2 offset:14336\n"
    "ds_read_b128 v[92:95], v2 offset:15360\n"
    "s_waitcnt lgkmcnt(8)\n"
    "v_mfma_f32_16x16x32_bf16 v[64:67], v[52:55], v[128:131], v[64:67]\n"
    "s_waitcnt lgkmcnt(7)\n"
    "v_mfma_f32_16x16x32_bf16 v[64:67], v[56:59], v[132:135], v[64:67]\n"
    "s_waitcnt lgkmcnt(6)\n"
    "v_mfma_f32_16x16x32_bf16 v[64:67], v[60:63], v[72:75], v[64:67]\n"
    "v_smfmac_f32_16x16x64_bf16 v[40:43], v[100:103], a[160:167], v1\n"
    "ds_read_b128 v[48:51], v2 offset:18432\n"
    "ds_read_b128 v[52:55], v2 offset:19456\n"
    "v_smfmac_f32_16x16x64_bf16 v[44:47], v[100:103], a[168:175], v1\n"
    "ds_read_b128 v[56:59], v2 offset:20480\n"
    "ds_read_b128 v[60:63], v2 offset:21504\n"
    "v_maximum3_f32 v79, v64, v65, v66\n"
    "v_maximum3_f32 v78, v79, v67, v78\n"
    "v_mov_b32 v79, v78\n"
    "v_smfmac_f32_16x16x64_bf16 v[40:43], v[104:107], a[176:183], v1\n"
    "ds_read_b128 v[96:99], v2 offset:16384\n"
    "ds_read_b128 v[100:103], v2 offset:17408\n"
    "v_permlane16_swap_b32 v79, v78\n"
    "v_maximum3_f32 v78, v78, v79, v79\n"
    "v_mul_f32 v78, 0xbe0293ee, v78\n"
    "v_smfmac_f32_16x16x64_bf16 v[44:47], v[104:107], a[184:191], v1\n"
    "v_fmamk_f32 v80, v80, 0x3e0293ee, v78\n"
    "v_fmamk_f32 v81, v81, 0x3e0293ee, v78\n"
    "v_exp_f32 v80, v80\n"
    "v_exp_f32 v81, v81\n"
    "ds_read_b128 v[124:127], v2 offset:22528\n"
    "ds_read_b128 v[128:131], v2 offset:23552\n"
    "v_smfmac_f32_16x16x64_bf16 v[40:43], v[108:111], a[192:199], v1\n"
    "v_fmamk_f32 v82, v82, 0x3e0293ee, v78\n"
    "v_fmamk_f32 v83, v83, 0x3e0293ee, v78\n"
    "v_exp_f32 v82, v82\n"
    "v_exp_f32 v83, v83\n"
    "v_cvt_pk_bf16_f32 v80, v80, v81\n"
    "v_smfmac_f32_16x16x64_bf16 v[44:47], v[108:111], a[200:207], v1\n"
    "v_fmamk_f32 v84, v84, 0x3e0293ee, v78\n"
    "v_fmamk_f32 v85, v85, 0x3e0293ee, v78\n"
    "v_exp_f32 v84, v84\n"
    "v_exp_f32 v85, v85\n"
    "v_cvt_pk_bf16_f32 v81, v82, v83\n"
    "v_smfmac_f32_16x16x64_bf16 v[40:43], v[112:115], a[208:215], v1\n"
    "v_fmamk_f32 v86, v86, 0x3e0293ee, v78\n"
    "v_fmamk_f32 v87, v87, 0x3e0293ee, v78\n"
    "v_exp_f32 v86, v86\n"
    "v_exp_f32 v87, v87\n"
    "v_cvt_pk_bf16_f32 v82, v84, v85\n"
    "ds_read_b128 v[104:107], v2 offset:24576\n"
    "ds_read_b128 v[108:111], v2 offset:25600\n"
    "v_smfmac_f32_16x16x64_bf16 v[44:47], v[112:115], a[216:223], v1\n"
    "v_fmamk_f32 v64, v64, 0x3e0293ee, v78\n"
    "v_fmamk_f32 v65, v65, 0x3e0293ee, v78\n"
    "v_exp_f32 v64, v64\n"
    "v_exp_f32 v65, v65\n"
    "v_cvt_pk_bf16_f32 v83, v86, v87\n"
    "v_smfmac_f32_16x16x64_bf16 v[40:43], v[116:119], a[224:231], v1\n"
    "v_fmamk_f32 v66, v66, 0x3e0293ee, v78\n"
    "v_fmamk_f32 v67, v67, 0x3e0293ee, v78\n"
    "v_exp_f32 v66, v66\n"
    "v_exp_f32 v67, v67\n"
    "v_cvt_pk_bf16_f32 v64, v64, v65\n"
    "v_smfmac_f32_16x16x64_bf16 v[44:47], v[116:119], a[232:239], v1\n"
    "v_cvt_pk_bf16_f32 v65, v66, v67\n"
    "v_smfmac_f32_16x16x64_bf16 v[40:43], v[120:123], a[240:247], v1\n"
    "ds_read_b128 v[112:115], v2 offset:26624\n"
    "ds_read_b128 v[116:119], v2 offset:27648\n"
    "v_smfmac_f32_16x16x64_bf16 v[44:47], v[120:123], a[248:255], v1\n"
    "s_waitcnt lgkmcnt(14)\n"
    "v_mfma_f32_16x16x32_bf16 v[72:75], v[68:71], v[80:83], 0\n"
    "v_mfma_f32_16x16x32_bf16 v[84:87], v[32:35], v[80:83], 0\n"
    "ds_read_b128 v[120:123], v11 offset:31872\n"
    "ds_read_b128 v[68:71], v11 offset:31872\n"
    "v_mfma_f32_16x16x32_bf16 v[132:135], v[36:39], v[80:83], 0\n"
    "ds_read_b128 v[32:35], v2 offset:28672\n"
    "ds_read_b128 v[36:39], v2 offset:29696\n"
    "v_cvt_pk_bf16_f32 v40, v40, v41\n"
    "v_cvt_pk_bf16_f32 v41, v42, v43\n"
    "s_waitcnt lgkmcnt(14)\n"
    "v_mfma_f32_16x16x16_bf16 v[72:75], v[76:77], v[64:65], v[72:75]\n"
    "v_cvt_pk_bf16_f32 v42, v44, v45\n"
    "v_cvt_pk_bf16_f32 v43, v46, v47\n"
    "v_mfma_f32_16x16x16_bf16 v[84:87], v[40:41], v[64:65], v[84:87]\n"
    "ds_read_b128 v[80:83], v11 offset:31872\n"
    "ds_read_b128 v[44:47], v11 offset:31872\n"
    "v_mfma_f32_16x16x16_bf16 v[132:135], v[42:43], v[64:65], v[132:135]\n"
    "ds_write_b128 v5, v[40:43] offset:22784\n"
    "ds_read_b128 v[64:67], v11 offset:31872\n"
    "s_and_b32 s49, s48, 1\n"
    "s_lshl_b32 s70, s49, 3\n"
    "v_rcp_f32 v78, v72\n"
    "s_nop 0\n"
    "v_pk_mul_f32 v[84:85], v[84:85], v[78:79] op_sel_hi:[1,0]\n"
    "v_pk_mul_f32 v[86:87], v[86:87], v[78:79] op_sel_hi:[1,0]\n"
    "s_nop 0\n"
    "v_pk_mul_f32 v[132:133], v[132:133], v[78:79] op_sel_hi:[1,0]\n"
    "v_pk_mul_f32 v[134:135], v[134:135], v[78:79] op_sel_hi:[1,0]\n"
    "v_cvt_pk_bf16_f32 v84, v84, v132\n"
    "v_cvt_pk_bf16_f32 v85, v85, v133\n"
    "v_cvt_pk_bf16_f32 v86, v86, v134\n"
    "v_cvt_pk_bf16_f32 v87, v87, v135\n"
    "ds_write_b128 v6, v[84:87] offset:26880\n"
    "s_cmp_eq_u32 s48, 63\n"
    "s_waitcnt lgkmcnt(0)\n"
    "s_barrier\n"
    "s_cbranch_scc1 .Lend_%=\n"
    "ds_read_b128 v[132:135], v0 offset:26880\n"
    "ds_read_b128 v[72:75], v0 offset:27904\n"
    "s_cmp_lt_u32 %2, 2\n"
    "s_waitcnt lgkmcnt(1)\n"
    "v_smfmac_f32_16x16x64_bf16 v[68:71], v[132:135], v[88:95], v1\n"
    "v_smfmac_f32_16x16x64_bf16 v[80:83], v[132:135], v[96:103], v1\n"
    "ds_read_b128 v[88:91], v2 offset:30720\n"
    "ds_read_b128 v[92:95], v2 offset:31744\n"
    "s_waitcnt lgkmcnt(2)\n"
    "v_smfmac_f32_16x16x64_bf16 v[68:71], v[72:75], v[48:55], v1\n"
    "ds_read_b128 v[96:99], v2 offset:32768\n"
    "ds_read_b128 v[100:103], v2 offset:33792\n"
    "v_smfmac_f32_16x16x64_bf16 v[80:83], v[72:75], v[56:63], v1\n"
    "v_smfmac_f32_16x16x64_bf16 v[120:123], v[132:135], v[124:131], v1\n"
    "v_smfmac_f32_16x16x64_bf16 v[44:47], v[132:135], v[104:111], v1\n"
    "v_smfmac_f32_16x16x64_bf16 v[120:123], v[72:75], v[112:119], v1\n"
    "v_permlane32_swap_b32 v68, v80\n"
    "v_permlane32_swap_b32 v69, v81\n"
    "v_smfmac_f32_16x16x64_bf16 v[44:47], v[72:75], v[32:39], v1\n"
    "s_waitcnt lgkmcnt(0)\n"
    "s_cbranch_scc1 .Lx1_%=\n"
    "v_smfmac_f32_16x16x64_bf16 v[64:67], v[132:135], v[88:95], v1\n"
    "v_smfmac_f32_16x16x64_bf16 v[64:67], v[72:75], v[96:103], v1\n"
    ".Lx1_%=:\n"
    "v_permlane32_swap_b32 v70, v82\n"
    "v_permlane32_swap_b32 v71, v83\n"
    "v_pk_add_f32 v[68:69], v[68:69], v[80:81]\n"
    "v_pk_add_f32 v[70:71], v[70:71], v[82:83]\n"
    "v_bfi_b32 v68, 15, s49, v68\n"
    "v_bfi_b32 v70, 15, s49, v70\n"
    "buffer_store_dwordx4 v[68:71], v7, s[40:43], 0 offen sc1\n"
    "v_permlane32_swap_b32 v120, v44\n"
    "v_permlane32_swap_b32 v121, v45\n"
    "v_permlane32_swap_b32 v122, v46\n"
    "v_permlane32_swap_b32 v123, v47\n"
    "v_pk_add_f32 v[120:121], v[120:121], v[44:45]\n"
    "v_pk_add_f32 v[122:123], v[122:123], v[46:47]\n"
    "v_bfi_b32 v120, 15, s49, v120\n"
    "v_bfi_b32 v122, 15, s49, v122\n"
    "buffer_store_dwordx4 v[120:123], v7, s[40:43], s74 offen sc1\n"
    "s_cbranch_scc1 .Lx2_%=\n"
    "v_permlane32_swap_b32 v64, v66\n"
    "v_permlane32_swap_b32 v65, v67\n"
    "v_pk_add_f32 v[64:65], v[64:65], v[66:67]\n"
    "v_bfi_b32 v64, 15, s49, v64\n"
    "buffer_store_dwordx2 v[64:65], v8, s[40:43], 0 offen sc1\n"
    ".Lx2_%=:\n"
    "ds_read_b32 v135, v11 offset:31920\n"
    "s_memrealtime s[38:39]\n"
    "s_waitcnt lgkmcnt(0)\n"
    "v_readfirstlane_b32 s95, v135\n"
    "s_add_u32 s53, s54, 16\n"
    "s_sub_u32 s73, s38, s58\n"
    "s_lshl_b32 s73, s73, 3\n"
    "s_sub_u32 s73, s73, s96\n"
    "s_ashr_i32 s73, s73, 2\n"
    "s_max_i32 s73, s73, -32\n"
    "s_min_i32 s73, s73, 16\n"
    "s_add_u32 s96, s96, s73\n"
    "s_add_u32 s73, s38, s53\n"
    ".Lsh4_%=:\n"
    "s_sub_u32 s51, s95, s73\n"
    "s_cmp_lt_u32 s51, 1024\n"
    "s_cbranch_scc1 .Lsh4_x%=\n"
    "s_add_u32 s95, s95, 48\n"
    "s_cmp_lt_u32 s51, -1024\n"
    "s_cselect_b32 s95, s73, s95\n"
    "s_branch .Lsh4_%=\n"
    ".Lsh4_x%=:\n"
    "s_cmp_eq_u32 %2, 3\n"
    "s_cbranch_scc1 .Lw3_%=\n"
    "ds_read_b128 v[32:35], v2\n"
    "ds_read_b128 v[36:39], v2 offset:1024\n"
    "ds_read_b128 v[40:43], v2 offset:2048\n"
    "ds_read_b128 v[44:47], v2 offset:3072\n"
    "ds_read_b128 v[48:51], v2 offset:4096\n"
    "ds_read_b128 v[52:55], v2 offset:5120\n"
    "ds_read_b128 v[56:59], v2 offset:6144\n"
    "ds_read_b128 v[60:63], v2 offset:7168\n"
    "ds_read_b128 v[64:67], v2 offset:8192\n"
    "ds_read_b128 v[68:71], v2 offset:9216\n"
    "ds_read_b128 v[72:75], v2 offset:10240\n"
    "ds_read_b128 v[76:79], v2 offset:11264\n"
    "ds_read_b128 v[80:83], v2 offset:12288\n"
    "ds_read_b128 v[84:87], v2 offset:13312\n"
    "v_mov_b64 v[124:125], 0\n"
    "v_mov_b64 v[126:127], 0\n"
    "v_mov_b64 v[128:129], 0\n"
    "v_mov_b64 v[130:131], 0\n"
    "s_mul_i32 s73, s48, 3\n"
    "s_add_u32 s73, s73, s94\n"
    "s_and_b32 s51, s73, 1\n"
    "s_lshl_b32 s51, s51, 12\n"
    "s_lshr_b32 s73, s73, 1\n"
    "s_lshl_b32 s73, s73, 14\n"
    "s_add_u32 s73, s73, s51\n"
    "s_add_u32 s66, s73, s68\n"
    "s_mul_i32 s73, s48, 3\n"
    "s_add_u32 s73, s73, %2\n"
    "s_and_b32 s51, s73, 1\n"
    "s_lshl_b32 s51, s51, 12\n"
    "s_lshr_b32 s73, s73, 1\n"
    "s_lshl_b32 s73, s73, 14\n"
    "s_add_u32 s73, s73, s51\n"
    "s_add_u32 s67, s73, s68\n"
    "s_waitcnt lgkmcnt(0)\n"
    "s_cmp_lt_i32 s71, 0\n"
    "s_cbranch_scc1 .Lnored_%=\n"
    "s_mov_b32 s56, 0\n"
    ".Lred_%=:\n"
    "s_sub_u32 s53, s95, s54\n"
    "s_waitcnt lgkmcnt(0)\n"
    ".Lgl5_%=:\n"
    "s_memrealtime s[50:51]\n"
    "s_waitcnt lgkmcnt(0)\n"
    "s_sub_u32 s50, s50, s53\n"
    "s_cmp_lt_i32 s50, 0\n"
    "s_cbranch_scc1 .Lgl5_%=\n"
    "buffer_load_dwordx4 v[88:91], v0, s[40:43], s65 offen sc1\n"
    "buffer_load_dwordx4 v[92:95], v0, s[40:43], s65 offen offset:1024 sc1\n"
    "buffer_load_dwordx4 v[96:99], v0, s[40:43], s65 offen offset:2048 sc1\n"
    "buffer_load_dwordx4 v[100:103], v0, s[40:43], s65 offen offset:3072 sc1\n"
    "s_lshr_b32 s73, s96, 3\n"
    "s_add_u32 s73, s73, s95\n"
    "s_add_u32 s73, s73, s55\n"
    "s_and_b32 s73, s73, -2\n"
    "v_mov_b32 v105, s73\n"
    "s_waitcnt vmcnt(2)\n"
    "v_add3_u32 v104, v88, v90, v92\n"
    "v_pk_add_f32 v[108:109], v[88:89], v[92:93]\n"
    "v_pk_add_f32 v[110:111], v[90:91], v[94:95]\n"
    "s_waitcnt vmcnt(1)\n"
    "v_add3_u32 v104, v104, v94, v96\n"
    "v_pk_add_f32 v[108:109], v[108:109], v[96:97]\n"
    "v_pk_add_f32 v[110:111], v[110:111], v[98:99]\n"
    "s_waitcnt vmcnt(0)\n"
    "v_add3_u32 v104, v104, v98, v100\n"
    "v_pk_add_f32 v[108:109], v[108:109], v[100:101]\n"
    "v_pk_add_f32 v[110:111], v[110:111], v[102:103]\n"
    "v_add_u32 v104, v104, v102\n"
    "v_and_b32 v104, 15, v104\n"
    "v_permlane32_swap_b32 v108, v110\n"
    "v_permlane32_swap_b32 v109, v111\n"
    "v_cmp_ne_u32 vcc, s70, v104\n"
    "v_pk_add_f32 v[108:109], v[108:109], v[110:111]\n"
    "s_cbranch_vccz .Lredok_%=\n"
    "s_memrealtime s[38:39]\n"
    "s_waitcnt lgkmcnt(0)\n"
    "s_add_u32 s73, s38, 44\n"
    ".Lsh6_%=:\n"
    "s_sub_u32 s51, s95, s73\n"
    "s_cmp_lt_u32 s51, 1024\n"
    "s_cbranch_scc1 .Lsh6_x%=\n"
    "s_add_u32 s95, s95, 48\n"
    "s_cmp_lt_u32 s51, -1024\n"
    "s_cselect_b32 s95, s73, s95\n"
    "s_branch .Lsh6_%=\n"
    ".Lsh6_x%=:\n"
    "s_add_u32 s56, s56, 1\n"
    "s_cmp_lt_u32 s56, s57\n"
    "s_cbranch_scc1 .Lred_%=\n"
    "s_mov_b32 s57, 0\n"
    "v_mov_b32 v135, 1\n"
    "global_store_dword v11, v135, %1 sc1\n"
    ".Lredok_%=:\n"
    "s_nop 0\n"
    "v_permlane16_swap_b32 v108, v109\n"
    "v_add_f32 v108, v108, v109\n"
    "s_nop 1\n"
    "v_add_f32_dpp v108, v108, v108 row_ror:8 row_mask:0xf bank_mask:0xf\n"
    "s_nop 1\n"
    "v_mov_b32_dpp v109, v108 quad_perm:[1,0,3,2] row_mask:0xf bank_mask:0xf\n"
    "v_cvt_pk_bf16_f32 v108, v108, v109\n"
    "v_min_u32 v108, -2, v108\n"
    "s_nop 1\n"
    "v_mov_b32_dpp v109, v108 row_shl:2 row_mask:0xf bank_mask:0xf\n"
    "v_mov_b32_dpp v110, v108 row_shl:4 row_mask:0xf bank_mask:0xf\n"
    "v_mov_b32_dpp v111, v108 row_shl:6 row_mask:0xf bank_mask:0xf\n"
    "buffer_store_dwordx4 v[108:111], v9, s[40:43], s66 offen sc1\n"
    "s_cmp_eq_u32 s71, 0\n"
    "s_cbranch_scc0 .Lnl_%=\n"
    "s_mov_b64 exec, 1\n"
    "buffer_store_dword v105, v11, s[40:43], s66 offen offset:3072 sc1\n"
    "s_mov_b64 exec, -1\n"
    ".Lnl_%=:\n"
    ".Lnored_%=:\n"
    "s_waitcnt vmcnt(2)\n"
    "s_cmp_eq_u32 %2, 0\n"
    "s_cselect_b32 s73, 0, 0x80000000\n"
    "v_mov_b32 v106, s73\n"
    "s_lshl_b32 s73, %2, 2\n"
    "v_mov_b32 v105, s73\n"
    "s_cmp_lg_u32 s59, 0\n"
    "s_cbranch_scc1 .Lkn7_%=\n"
    "s_waitcnt lgkmcnt(0)\n"
    "s_add_u32 s73, s48, 1\n"
    "s_and_b32 s51, s73, 32\n"
    "s_and_b32 s73, s73, 31\n"
    "s_lshl3_add_u32 s73, s51, s73\n"
    "s_lshl2_add_u32 s73, s73, s92\n"
    "s_mov_b32 s53, s73\n"
    "s_mov_b32 s56, 0\n"
    "v_mov_b32 v134, s53\n"
    ".Lpoll8_%=:\n"
    "global_load_dword v132, v134, %1 sc1\n"
    "global_load_dword v133, v134, %1 offset:128 sc1\n"
    "s_waitcnt vmcnt(0)\n"
    "v_and_b32 v133, v132, v133\n"
    "s_nop 0\n"
    "v_readfirstlane_b32 s73, v133\n"
    "s_cmp_ge_u32 s73, 1\n"
    "s_cbranch_scc1 .Lpoll8_x%=\n"
    "s_add_u32 s56, s56, 1\n"
    "s_cmp_lt_u32 s56, 3\n"
    "s_cbranch_scc1 .Lpoll8_%=\n"
    "s_sleep 8\n"
    "s_cmp_lt_u32 s56, s57\n"
    "s_cbranch_scc1 .Lpoll8_%=\n"
    "s_mov_b32 s57, 0\n"
    "v_mov_b32 v135, 1\n"
    "global_store_dword v11, v135, %1 sc1\n"
    ".Lpoll8_x%=:\n"
    "buffer_load_dwordx4 v[12:15], v5, s[40:43], s60 offen sc1\n"
    "buffer_load_dwordx4 v[16:19], v5, s[40:43], s61 offen sc1\n"
    "buffer_load_dwordx4 v[20:23], v5, s[40:43], s62 offen sc1\n"
    "buffer_load_dwordx4 v[24:27], v5, s[40:43], s63 offen sc1\n"
    "buffer_load_dwordx4 v[28:31], v5, s[40:43], s64 offen sc1\n"
    "s_waitcnt vmcnt(0)\n"
    ".Lkn7_%=:\n"
    "s_add_u32 s73, s48, 2\n"
    "s_and_b32 s51, s73, 32\n"
    "s_and_b32 s73, s73, 31\n"
    "s_lshl3_add_u32 s73, s51, s73\n"
    "s_lshl2_add_u32 s73, s73, s92\n"
    "v_mov_b32 v134, s73\n"
    "global_load_dword v132, v134, %1 sc1\n"
    "global_load_dword v133, v134, %1 offset:128 sc1\n"
    "s_add_u32 s73, s48, 2\n"
    "s_and_b32 s73, s73, 63\n"
    "s_lshr_b32 s51, s73, 2\n"
    "s_xor_b32 s51, s51, s69\n"
    "s_and_b32 s51, s51, 1\n"
    "s_lshl_b32 s51, s51, 13\n"
    "s_lshl_b32 s73, s73, 5\n"
    "s_add_u32 s73, s73, %4\n"
    "s_lshl_b32 s73, s73, 14\n"
    "s_add_u32 s73, s73, s51\n"
    "s_add_u32 s60, s73, 0x5c00000\n"
    "s_add_u32 s61, s73, 0x5c01000\n"
    "s_add_u32 s62, s73, 0x7c00000\n"
    "s_add_u32 s63, s73, 0x7c01000\n"
    "s_lshl_b32 s51, %5, 12\n"
    "s_add_u32 s73, s73, s51\n"
    "s_add_u32 s64, s73, 0x3c00000\n"
    "s_mov_b32 s56, 0\n"
    "s_sub_u32 s53, s95, 0\n"
    "s_waitcnt lgkmcnt(0)\n"
    ".Lgl9_%=:\n"
    "s_memrealtime s[50:51]\n"
    "s_waitcnt lgkmcnt(0)\n"
    "s_sub_u32 s50, s50, s53\n"
    "s_cmp_lt_i32 s50, 0\n"
    "s_cbranch_scc1 .Lgl9_%=\n"
    "buffer_load_dwordx4 v[88:91], v0, s[40:43], s67 offen sc1\n"
    "buffer_load_dwordx4 v[92:95], v0, s[40:43], s67 offen offset:1024 sc1\n"
    "buffer_load_dwordx4 v[96:99], v0, s[40:43], s67 offen offset:2048 sc1\n"
    "buffer_load_dword v101, v106, s[40:43], s67 offen offset:3072 sc1\n"
    "s_waitcnt lgkmcnt(0)\n"
    "s_barrier\n"
    "ds_write_b128 v5, v[12:15]\n"
    "ds_write_b128 v5, v[16:19] offset:4096\n"
    "ds_write_b128 v5, v[28:31] offset:16384\n"
    "v_swap_b32 v22, v24\n"
    "v_swap_b32 v23, v25\n"
    "ds_write_b128 v5, v[20:23] offset:8192\n"
    "ds_write_b128 v5, v[24:27] offset:12288\n"
    "s_waitcnt vmcnt(4)\n"
    "v_and_b32 v133, v132, v133\n"
    "s_nop 0\n"
    "v_readfirstlane_b32 s59, v133\n"
    ".Lgchk_%=:\n"
    "s_waitcnt vmcnt(2)\n"
    "v_max3_u32 v100, v88, v90, v92\n"
    "ds_write_b128 v10, v[88:91] offset:20480\n"
    "ds_write_b128 v10, v[92:95] offset:21504\n"
    "s_waitcnt vmcnt(1)\n"
    "v_max3_u32 v100, v100, v94, v96\n"
    "ds_write_b128 v10, v[96:99] offset:22528\n"
    "s_waitcnt vmcnt(0)\n"
    "v_max3_u32 v100, v100, v98, v101\n"
    "ds_write_b32 v105, v101 offset:31920\n"
    "v_cmp_eq_u32 vcc, -1, v100\n"
    "s_cbranch_vccz .Lgok_%=\n"
    "s_add_u32 s95, s95, 48\n"
    "s_memrealtime s[38:39]\n"
    "s_waitcnt lgkmcnt(0)\n"
    "s_add_u32 s73, s38, -28\n"
    ".Lsh10_%=:\n"
    "s_sub_u32 s51, s95, s73\n"
    "s_cmp_lt_u32 s51, 1024\n"
    "s_cbranch_scc1 .Lsh10_x%=\n"
    "s_add_u32 s95, s95, 48\n"
    "s_cmp_lt_u32 s51, -1024\n"
    "s_cselect_b32 s95, s73, s95\n"
    "s_branch .Lsh10_%=\n"
    ".Lsh10_x%=:\n"
    "s_sub_u32 s53, s95, 0\n"
    "s_waitcnt lgkmcnt(0)\n"
    ".Lgl11_%=:\n"
    "s_memrealtime s[50:51]\n"
    "s_waitcnt lgkmcnt(0)\n"
    "s_sub_u32 s50, s50, s53\n"
    "s_cmp_lt_i32 s50, 0\n"
    "s_cbranch_scc1 .Lgl11_%=\n"
    "buffer_load_dwordx4 v[88:91], v0, s[40:43], s67 offen sc1\n"
    "buffer_load_dwordx4 v[92:95], v0, s[40:43], s67 offen offset:1024 sc1\n"
    "buffer_load_dwordx4 v[96:99], v0, s[40:43], s67 offen offset:2048 sc1\n"
    "buffer_load_dword v101, v106, s[40:43], s67 offen offset:3072 sc1\n"
    "s_add_u32 s56, s56, 1\n"
    "s_cmp_lt_u32 s56, s57\n"
    "s_cbranch_scc1 .Lgchk_%=\n"
    "s_mov_b32 s57, 0\n"
    "v_mov_b32 v135, 1\n"
    "global_store_dword v11, v135, %1 sc1\n"
    "s_waitcnt vmcnt(0)\n"
    ".Lgok_%=:\n"
    "s_mov_b32 s58, s95\n"
    "s_add_u32 s48, s48, 1\n"
    "s_waitcnt lgkmcnt(0)\n"
    "s_barrier\n"
    "s_branch .Lstep_%=\n"
    ".Lw3_%=:\n"
    "ds_read_b128 v[32:35], v0 offset:16384\n"
    "ds_read_b128 v[48:51], v0\n"
    "ds_read_b128 v[64:67], v0 offset:4096\n"
    "ds_read_b128 v[80:83], v4 offset:20480\n"
    "ds_read_b128 v[36:39], v0 offset:17408\n"
    "ds_read_b128 v[52:55], v0 offset:1024\n"
    "ds_read_b128 v[68:71], v0 offset:5120\n"
    "ds_read_b128 v[84:87], v4 offset:20544\n"
    "ds_read_b128 v[40:43], v0 offset:18432\n"
    "ds_read_b128 v[56:59], v0 offset:2048\n"
    "ds_read_b128 v[72:75], v0 offset:6144\n"
    "ds_read_b128 v[88:91], v4 offset:20608\n"
    "ds_read_b128 v[44:47], v0 offset:19456\n"
    "ds_read_b128 v[60:63], v0 offset:3072\n"
    "ds_read_b128 v[76:79], v0 offset:7168\n"
    "ds_read_b128 v[92:95], v4 offset:20672\n"
    "s_waitcnt lgkmcnt(14)\n"
    "v_mfma_f32_16x16x32_bf16 v[96:99], v[48:51], v[32:35], 0\n"
    "s_waitcnt lgkmcnt(13)\n"
    "v_mfma_f32_16x16x32_bf16 v[100:103], v[64:67], v[32:35], 0\n"
    "s_waitcnt lgkmcnt(12)\n"
    "v_mfma_f32_16x16x32_bf16 v[104:107], v[80:83], v[32:35], 0\n"
    "s_waitcnt lgkmcnt(10)\n"
    "v_mfma_f32_16x16x32_bf16 v[96:99], v[52:55], v[36:39], v[96:99]\n"
    "s_waitcnt lgkmcnt(9)\n"
    "v_mfma_f32_16x16x32_bf16 v[100:103], v[68:71], v[36:39], v[100:103]\n"
    "s_waitcnt lgkmcnt(8)\n"
    "v_mfma_f32_16x16x32_bf16 v[104:107], v[84:87], v[36:39], v[104:107]\n"
    "s_waitcnt lgkmcnt(6)\n"
    "v_mfma_f32_16x16x32_bf16 v[96:99], v[56:59], v[40:43], v[96:99]\n"
    "s_waitcnt lgkmcnt(5)\n"
    "v_mfma_f32_16x16x32_bf16 v[100:103], v[72:75], v[40:43], v[100:103]\n"
    "s_waitcnt lgkmcnt(4)\n"
    "v_mfma_f32_16x16x32_bf16 v[104:107], v[88:91], v[40:43], v[104:107]\n"
    "s_waitcnt lgkmcnt(2)\n"
    "v_mfma_f32_16x16x32_bf16 v[96:99], v[60:63], v[44:47], v[96:99]\n"
    "s_waitcnt lgkmcnt(1)\n"
    "v_mfma_f32_16x16x32_bf16 v[100:103], v[76:79], v[44:47], v[100:103]\n"
    "s_waitcnt lgkmcnt(0)\n"
    "v_mfma_f32_16x16x32_bf16 v[104:107], v[92:95], v[44:47], v[104:107]\n"
    "ds_read_b128 v[32:35], v0 offset:8192\n"
    "ds_read_b128 v[36:39], v0 offset:12288\n"
    "ds_read_b128 v[40:43], v0 offset:9216\n"
    "ds_read_b128 v[44:47], v0 offset:13312\n"
    "ds_read_b128 v[48:51], v0 offset:10240\n"
    "ds_read_b128 v[52:55], v0 offset:14336\n"
    "ds_read_b128 v[56:59], v0 offset:11264\n"
    "ds_read_b128 v[60:63], v0 offset:15360\n"
    "ds_read_b128 v[64:67], v0 offset:22784\n"
    "ds_read_b128 v[68:71], v0 offset:23808\n"
    "ds_read_b128 v[72:75], v0 offset:24832\n"
    "ds_read_b128 v[76:79], v0 offset:25856\n"
    "ds_read_b128 v[80:83], v11 offset:31888\n"
    "ds_read_b64 v[84:85], v11 offset:31904\n"
    "v_mov_b32 v86, 0xd01502f9\n"
    "v_cndmask_b32 v96, v86, v96, s[76:77]\n"
    "v_cndmask_b32 v97, v86, v97, s[78:79]\n"
    "v_cndmask_b32 v98, v86, v98, s[80:81]\n"
    "v_cndmask_b32 v99, v86, v99, s[82:83]\n"
    "v_cndmask_b32 v100, v86, v100, s[84:85]\n"
    "v_cndmask_b32 v101, v86, v101, s[86:87]\n"
    "v_cndmask_b32 v102, v86, v102, s[88:89]\n"
    "v_cndmask_b32 v103, v86, v103, s[90:91]\n"
    "v_maximum3_f32 v87, v104, v105, v106\n"
    "v_maximum3_f32 v87, v87, v107, v96\n"
    "v_maximum3_f32 v87, v87, v97, v98\n"
    "v_maximum3_f32 v87, v87, v99, v100\n"
    "v_maximum3_f32 v87, v87, v101, v102\n"
    "v_maximum3_f32 v87, v87, v103, v103\n"
    "v_mov_b32 v86, v87\n"
    "s_nop 1\n"
    "v_permlane16_swap_b32 v86, v87\n"
    "v_maximum3_f32 v87, v87, v86, v86\n"
    "v_mov_b32 v86, v87\n"
    "s_nop 1\n"
    "v_permlane32_swap_b32 v86, v87\n"
    "v_maximum3_f32 v87, v87, v86, v86\n"
    "v_mul_f32 v87, 0xbe0293ee, v87\n"
    "v_fmamk_f32 v96, v96, 0x3e0293ee, v87\n"
    "v_fmamk_f32 v97, v97, 0x3e0293ee, v87\n"
    "v_fmamk_f32 v98, v98, 0x3e0293ee, v87\n"
    "v_fmamk_f32 v99, v99, 0x3e0293ee, v87\n"
    "v_fmamk_f32 v100, v100, 0x3e0293ee, v87\n"
    "v_fmamk_f32 v101, v101, 0x3e0293ee, v87\n"
    "v_fmamk_f32 v102, v102, 0x3e0293ee, v87\n"
    "v_fmamk_f32 v103, v103, 0x3e0293ee, v87\n"
    "v_fmamk_f32 v104, v104, 0x3e0293ee, v87\n"
    "v_fmamk_f32 v105, v105, 0x3e0293ee, v87\n"
    "v_fmamk_f32 v106, v106, 0x3e0293ee, v87\n"
    "v_fmamk_f32 v107, v107, 0x3e0293ee, v87\n"
    "v_exp_f32 v96, v96\n"
    "v_exp_f32 v97, v97\n"
    "v_exp_f32 v98, v98\n"
    "v_exp_f32 v99, v99\n"
    "v_exp_f32 v100, v100\n"
    "v_exp_f32 v101, v101\n"
    "v_exp_f32 v102, v102\n"
    "v_exp_f32 v103, v103\n"
    "v_exp_f32 v104, v104\n"
    "v_exp_f32 v105, v105\n"
    "v_exp_f32 v106, v106\n"
    "v_exp_f32 v107, v107\n"
    "v_cvt_pk_bf16_f32 v96, v96, v97\n"
    "v_cvt_pk_bf16_f32 v97, v98, v99\n"
    "v_cvt_pk_bf16_f32 v98, v100, v101\n"
    "v_cvt_pk_bf16_f32 v99, v102, v103\n"
    "v_cvt_pk_bf16_f32 v104, v104, v105\n"
    "v_cvt_pk_bf16_f32 v105, v106, v107\n"
    "s_waitcnt lgkmcnt(1)\n"
    "v_mfma_f32_16x16x32_bf16 v[108:111], v[80:83], v[96:99], 0\n"
    "v_mfma_f32_16x16x32_bf16 v[88:91], v[32:35], v[96:99], 0\n"
    "v_mfma_f32_16x16x32_bf16 v[120:123], v[36:39], v[96:99], 0\n"
    "v_mfma_f32_16x16x32_bf16 v[92:95], v[40:43], v[96:99], 0\n"
    "s_waitcnt lgkmcnt(0)\n"
    "v_mfma_f32_16x16x16_bf16 v[108:111], v[84:85], v[104:105], v[108:111]\n"
    "v_mfma_f32_16x16x32_bf16 v[124:127], v[44:47], v[96:99], 0\n"
    "v_mfma_f32_16x16x32_bf16 v[112:115], v[48:51], v[96:99], 0\n"
    "v_mfma_f32_16x16x32_bf16 v[128:131], v[52:55], v[96:99], 0\n"
    "v_mfma_f32_16x16x32_bf16 v[116:119], v[56:59], v[96:99], 0\n"
    "v_mfma_f32_16x16x32_bf16 v[132:135], v[60:63], v[96:99], 0\n"
    "v_rcp_f32 v86, v108\n"
    "v_mfma_f32_16x16x16_bf16 v[88:91], v[64:65], v[104:105], v[88:91]\n"
    "v_mfma_f32_16x16x16_bf16 v[120:123], v[66:67], v[104:105], v[120:123]\n"
    "v_mfma_f32_16x16x16_bf16 v[92:95], v[68:69], v[104:105], v[92:95]\n"
    "v_mfma_f32_16x16x16_bf16 v[124:127], v[70:71], v[104:105], v[124:127]\n"
    "v_mfma_f32_16x16x16_bf16 v[112:115], v[72:73], v[104:105], v[112:115]\n"
    "v_mfma_f32_16x16x16_bf16 v[128:131], v[74:75], v[104:105], v[128:131]\n"
    "v_mfma_f32_16x16x16_bf16 v[116:119], v[76:77], v[104:105], v[116:119]\n"
    "v_mfma_f32_16x16x16_bf16 v[132:135], v[78:79], v[104:105], v[132:135]\n"
    "v_pk_mul_f32 v[88:89], v[88:89], v[86:87] op_sel_hi:[1,0]\n"
    "v_pk_mul_f32 v[90:91], v[90:91], v[86:87] op_sel_hi:[1,0]\n"
    "v_pk_mul_f32 v[120:121], v[120:121], v[86:87] op_sel_hi:[1,0]\n"
    "v_pk_mul_f32 v[122:123], v[122:123], v[86:87] op_sel_hi:[1,0]\n"
    "v_cvt_pk_bf16_f32 v88, v88, v120\n"
    "v_cvt_pk_bf16_f32 v89, v89, v121\n"
    "v_cvt_pk_bf16_f32 v90, v90, v122\n"
    "v_cvt_pk_bf16_f32 v91, v91, v123\n"
    "v_pk_mul_f32 v[92:93], v[92:93], v[86:87] op_sel_hi:[1,0]\n"
    "v_pk_mul_f32 v[94:95], v[94:95], v[86:87] op_sel_hi:[1,0]\n"
    "v_pk_mul_f32 v[124:125], v[124:125], v[86:87] op_sel_hi:[1,0]\n"
    "v_pk_mul_f32 v[126:127], v[126:127], v[86:87] op_sel_hi:[1,0]\n"
    "v_cvt_pk_bf16_f32 v92, v92, v124\n"
    "v_cvt_pk_bf16_f32 v93, v93, v125\n"
    "v_cvt_pk_bf16_f32 v94, v94, v126\n"
    "v_cvt_pk_bf16_f32 v95, v95, v127\n"
    "v_pk_mul_f32 v[112:113], v[112:113], v[86:87] op_sel_hi:[1,0]\n"
    "v_pk_mul_f32 v[114:115], v[114:115], v[86:87] op_sel_hi:[1,0]\n"
    "v_pk_mul_f32 v[128:129], v[128:129], v[86:87] op_sel_hi:[1,0]\n"
    "v_pk_mul_f32 v[130:131], v[130:131], v[86:87] op_sel_hi:[1,0]\n"
    "v_cvt_pk_bf16_f32 v112, v112, v128\n"
    "v_cvt_pk_bf16_f32 v113, v113, v129\n"
    "v_cvt_pk_bf16_f32 v114, v114, v130\n"
    "v_cvt_pk_bf16_f32 v115, v115, v131\n"
    "v_pk_mul_f32 v[116:117], v[116:117], v[86:87] op_sel_hi:[1,0]\n"
    "v_pk_mul_f32 v[118:119], v[118:119], v[86:87] op_sel_hi:[1,0]\n"
    "v_pk_mul_f32 v[132:133], v[132:133], v[86:87] op_sel_hi:[1,0]\n"
    "v_pk_mul_f32 v[134:135], v[134:135], v[86:87] op_sel_hi:[1,0]\n"
    "v_cvt_pk_bf16_f32 v116, v116, v132\n"
    "v_cvt_pk_bf16_f32 v117, v117, v133\n"
    "v_cvt_pk_bf16_f32 v118, v118, v134\n"
    "v_cvt_pk_bf16_f32 v119, v119, v135\n"
    "ds_read_b128 v[32:35], v2\n"
    "ds_read_b128 v[36:39], v2 offset:1024\n"
    "ds_read_b128 v[40:43], v2 offset:2048\n"
    "ds_read_b128 v[44:47], v2 offset:3072\n"
    "ds_read_b128 v[48:51], v2 offset:4096\n"
    "ds_read_b128 v[52:55], v2 offset:5120\n"
    "ds_read_b128 v[56:59], v2 offset:6144\n"
    "ds_read_b128 v[60:63], v2 offset:7168\n"
    "ds_read_b128 v[64:67], v2 offset:8192\n"
    "ds_read_b128 v[68:71], v2 offset:9216\n"
    "ds_read_b128 v[72:75], v2 offset:10240\n"
    "ds_read_b128 v[76:79], v2 offset:11264\n"
    "ds_read_b128 v[80:83], v2 offset:12288\n"
    "ds_read_b128 v[84:87], v2 offset:13312\n"
    "v_mov_b64 v[124:125], 0\n"
    "v_mov_b64 v[126:127], 0\n"
    "v_mov_b64 v[128:129], 0\n"
    "v_mov_b64 v[130:131], 0\n"
    "s_waitcnt vmcnt(0)\n"
    "s_cmp_lg_u32 s59, 0\n"
    "s_cbranch_scc1 .Lkn12_%=\n"
    "s_waitcnt lgkmcnt(0)\n"
    "s_add_u32 s73, s48, 1\n"
    "s_and_b32 s51, s73, 32\n"
    "s_and_b32 s73, s73, 31\n"
    "s_lshl3_add_u32 s73, s51, s73\n"
    "s_lshl2_add_u32 s73, s73, s92\n"
    "s_mov_b32 s53, s73\n"
    "s_mov_b32 s56, 0\n"
    "v_mov_b32 v134, s53\n"
    ".Lpoll13_%=:\n"
    "global_load_dword v132, v134, %1 sc1\n"
    "global_load_dword v133, v134, %1 offset:128 sc1\n"
    "s_waitcnt vmcnt(0)\n"
    "v_and_b32 v133, v132, v133\n"
    "s_nop 0\n"
    "v_readfirstlane_b32 s73, v133\n"
    "s_cmp_ge_u32 s73, 1\n"
    "s_cbranch_scc1 .Lpoll13_x%=\n"
    "s_add_u32 s56, s56, 1\n"
    "s_cmp_lt_u32 s56, 3\n"
    "s_cbranch_scc1 .Lpoll13_%=\n"
    "s_sleep 8\n"
    "s_cmp_lt_u32 s56, s57\n"
    "s_cbranch_scc1 .Lpoll13_%=\n"
    "s_mov_b32 s57, 0\n"
    "v_mov_b32 v135, 1\n"
    "global_store_dword v11, v135, %1 sc1\n"
    ".Lpoll13_x%=:\n"
    "buffer_load_dwordx4 v[12:15], v5, s[40:43], s60 offen sc1\n"
    "buffer_load_dwordx4 v[16:19], v5, s[40:43], s61 offen sc1\n"
    "buffer_load_dwordx4 v[20:23], v5, s[40:43], s62 offen sc1\n"
    "buffer_load_dwordx4 v[24:27], v5, s[40:43], s63 offen sc1\n"
    "buffer_load_dwordx4 v[28:31], v5, s[40:43], s64 offen sc1\n"
    "s_waitcnt vmcnt(0)\n"
    ".Lkn12_%=:\n"
    "s_add_u32 s73, s48, 2\n"
    "s_and_b32 s51, s73, 32\n"
    "s_and_b32 s73, s73, 31\n"
    "s_lshl3_add_u32 s73, s51, s73\n"
    "s_lshl2_add_u32 s73, s73, s92\n"
    "v_mov_b32 v134, s73\n"
    "global_load_dword v132, v134, %1 sc1\n"
    "global_load_dword v133, v134, %1 offset:128 sc1\n"
    "s_add_u32 s73, s48, 2\n"
    "s_and_b32 s73, s73, 63\n"
    "s_lshr_b32 s51, s73, 2\n"
    "s_xor_b32 s51, s51, s69\n"
    "s_and_b32 s51, s51, 1\n"
    "s_lshl_b32 s51, s51, 13\n"
    "s_lshl_b32 s73, s73, 5\n"
    "s_add_u32 s73, s73, %4\n"
    "s_lshl_b32 s73, s73, 14\n"
    "s_add_u32 s73, s73, s51\n"
    "s_add_u32 s60, s73, 0x5c00000\n"
    "s_add_u32 s61, s73, 0x5c01000\n"
    "s_add_u32 s62, s73, 0x7c00000\n"
    "s_add_u32 s63, s73, 0x7c01000\n"
    "s_lshl_b32 s51, %5, 12\n"
    "s_add_u32 s73, s73, s51\n"
    "s_add_u32 s64, s73, 0x3c00000\n"
    "s_sub_u32 s53, s95, 16\n"
    "s_waitcnt lgkmcnt(0)\n"
    ".Lgl14_%=:\n"
    "s_memrealtime s[50:51]\n"
    "s_waitcnt lgkmcnt(0)\n"
    "s_sub_u32 s50, s50, s53\n"
    "s_cmp_lt_i32 s50, 0\n"
    "s_cbranch_scc1 .Lgl14_%=\n"
    "s_lshr_b32 s51, s48, 2\n"
    "s_xor_b32 s51, s51, s69\n"
    "s_and_b32 s51, s51, 1\n"
    "s_lshl_b32 s51, s51, 13\n"
    "s_lshl_b32 s73, s48, 19\n"
    "s_add_u32 s73, s73, s75\n"
    "s_add_u32 s73, s73, s51\n"
    "buffer_store_dwordx4 v[88:91], v0, s[40:43], s73 offen sc1\n"
    "buffer_store_dwordx4 v[92:95], v0, s[40:43], s73 offen offset:1024 sc1\n"
    "buffer_store_dwordx4 v[112:115], v0, s[40:43], s73 offen offset:2048 sc1\n"
    "buffer_store_dwordx4 v[116:119], v0, s[40:43], s73 offen offset:3072 sc1\n"
    "s_waitcnt lgkmcnt(0)\n"
    "s_barrier\n"
    "ds_write_b128 v5, v[12:15]\n"
    "ds_write_b128 v5, v[16:19] offset:4096\n"
    "ds_write_b128 v5, v[28:31] offset:16384\n"
    "v_swap_b32 v22, v24\n"
    "v_swap_b32 v23, v25\n"
    "ds_write_b128 v5, v[20:23] offset:8192\n"
    "ds_write_b128 v5, v[24:27] offset:12288\n"
    "s_waitcnt vmcnt(0)\n"
    "v_and_b32 v133, v132, v133\n"
    "s_nop 0\n"
    "v_readfirstlane_b32 s59, v133\n"
    "s_add_u32 s73, s48, 0\n"
    "s_and_b32 s51, s73, 32\n"
    "s_and_b32 s73, s73, 31\n"
    "s_lshl3_add_u32 s73, s51, s73\n"
    "s_lshl2_add_u32 s73, s73, s93\n"
    "v_mov_b32 v134, s73\n"
    "v_mov_b32 v135, 1\n"
    "s_mov_b64 exec, 1\n"
    "global_store_dword v134, v135, %1 sc1\n"
    "s_mov_b64 exec, -1\n"
    "s_add_u32 s48, s48, 1\n"
    "s_waitcnt lgkmcnt(0)\n"
    "s_barrier\n"
    "s_branch .Lstep_%=\n"
    ".Lend_%=:\n"
    "s_cmp_eq_u32 %2, 3\n"
    "s_cbranch_scc0 .Lout_%=\n"
    "ds_read_b128 v[32:35], v0 offset:16384\n"
    "ds_read_b128 v[48:51], v0\n"
    "ds_read_b128 v[64:67], v0 offset:4096\n"
    "ds_read_b128 v[80:83], v4 offset:20480\n"
    "ds_read_b128 v[36:39], v0 offset:17408\n"
    "ds_read_b128 v[52:55], v0 offset:1024\n"
    "ds_read_b128 v[68:71], v0 offset:5120\n"
    "ds_read_b128 v[84:87], v4 offset:20544\n"
    "ds_read_b128 v[40:43], v0 offset:18432\n"
    "ds_read_b128 v[56:59], v0 offset:2048\n"
    "ds_read_b128 v[72:75], v0 offset:6144\n"
    "ds_read_b128 v[88:91], v4 offset:20608\n"
    "ds_read_b128 v[44:47], v0 offset:19456\n"
    "ds_read_b128 v[60:63], v0 offset:3072\n"
    "ds_read_b128 v[76:79], v0 offset:7168\n"
    "ds_read_b128 v[92:95], v4 offset:20672\n"
    "s_waitcnt lgkmcnt(14)\n"
    "v_mfma_f32_16x16x32_bf16 v[96:99], v[48:51], v[32:35], 0\n"
    "s_waitcnt lgkmcnt(13)\n"
    "v_mfma_f32_16x16x32_bf16 v[100:103], v[64:67], v[32:35], 0\n"
    "s_waitcnt lgkmcnt(12)\n"
    "v_mfma_f32_16x16x32_bf16 v[104:107], v[80:83], v[32:35], 0\n"
    "s_waitcnt lgkmcnt(10)\n"
    "v_mfma_f32_16x16x32_bf16 v[96:99], v[52:55], v[36:39], v[96:99]\n"
    "s_waitcnt lgkmcnt(9)\n"
    "v_mfma_f32_16x16x32_bf16 v[100:103], v[68:71], v[36:39], v[100:103]\n"
    "s_waitcnt lgkmcnt(8)\n"
    "v_mfma_f32_16x16x32_bf16 v[104:107], v[84:87], v[36:39], v[104:107]\n"
    "s_waitcnt lgkmcnt(6)\n"
    "v_mfma_f32_16x16x32_bf16 v[96:99], v[56:59], v[40:43], v[96:99]\n"
    "s_waitcnt lgkmcnt(5)\n"
    "v_mfma_f32_16x16x32_bf16 v[100:103], v[72:75], v[40:43], v[100:103]\n"
    "s_waitcnt lgkmcnt(4)\n"
    "v_mfma_f32_16x16x32_bf16 v[104:107], v[88:91], v[40:43], v[104:107]\n"
    "s_waitcnt lgkmcnt(2)\n"
    "v_mfma_f32_16x16x32_bf16 v[96:99], v[60:63], v[44:47], v[96:99]\n"
    "s_waitcnt lgkmcnt(1)\n"
    "v_mfma_f32_16x16x32_bf16 v[100:103], v[76:79], v[44:47], v[100:103]\n"
    "s_waitcnt lgkmcnt(0)\n"
    "v_mfma_f32_16x16x32_bf16 v[104:107], v[92:95], v[44:47], v[104:107]\n"
    "ds_read_b128 v[32:35], v0 offset:8192\n"
    "ds_read_b128 v[36:39], v0 offset:12288\n"
    "ds_read_b128 v[40:43], v0 offset:9216\n"
    "ds_read_b128 v[44:47], v0 offset:13312\n"
    "ds_read_b128 v[48:51], v0 offset:10240\n"
    "ds_read_b128 v[52:55], v0 offset:14336\n"
    "ds_read_b128 v[56:59], v0 offset:11264\n"
    "ds_read_b128 v[60:63], v0 offset:15360\n"
    "ds_read_b128 v[64:67], v0 offset:22784\n"
    "ds_read_b128 v[68:71], v0 offset:23808\n"
    "ds_read_b128 v[72:75], v0 offset:24832\n"
    "ds_read_b128 v[76:79], v0 offset:25856\n"
    "ds_read_b128 v[80:83], v11 offset:31888\n"
    "ds_read_b64 v[84:85], v11 offset:31904\n"
    "v_mov_b32 v86, 0xd01502f9\n"
    "v_cndmask_b32 v96, v86, v96, s[76:77]\n"
    "v_cndmask_b32 v97, v86, v97, s[78:79]\n"
    "v_cndmask_b32 v98, v86, v98, s[80:81]\n"
    "v_cndmask_b32 v99, v86, v99, s[82:83]\n"
    "v_cndmask_b32 v100, v86, v100, s[84:85]\n"
    "v_cndmask_b32 v101, v86, v101, s[86:87]\n"
    "v_cndmask_b32 v102, v86, v102, s[88:89]\n"
    "v_cndmask_b32 v103, v86, v103, s[90:91]\n"
    "v_maximum3_f32 v87, v104, v105, v106\n"
    "v_maximum3_f32 v87, v87, v107, v96\n"
    "v_maximum3_f32 v87, v87, v97, v98\n"
    "v_maximum3_f32 v87, v87, v99, v100\n"
    "v_maximum3_f32 v87, v87, v101, v102\n"
    "v_maximum3_f32 v87, v87, v103, v103\n"
    "v_mov_b32 v86, v87\n"
    "s_nop 1\n"
    "v_permlane16_swap_b32 v86, v87\n"
    "v_maximum3_f32 v87, v87, v86, v86\n"
    "v_mov_b32 v86, v87\n"
    "s_nop 1\n"
    "v_permlane32_swap_b32 v86, v87\n"
    "v_maximum3_f32 v87, v87, v86, v86\n"
    "v_mul_f32 v87, 0xbe0293ee, v87\n"
    "v_fmamk_f32 v96, v96, 0x3e0293ee, v87\n"
    "v_fmamk_f32 v97, v97, 0x3e0293ee, v87\n"
    "v_fmamk_f32 v98, v98, 0x3e0293ee, v87\n"
    "v_fmamk_f32 v99, v99, 0x3e0293ee, v87\n"
    "v_fmamk_f32 v100, v100, 0x3e0293ee, v87\n"
    "v_fmamk_f32 v101, v101, 0x3e0293ee, v87\n"
    "v_fmamk_f32 v102, v102, 0x3e0293ee, v87\n"
    "v_fmamk_f32 v103, v103, 0x3e0293ee, v87\n"
    "v_fmamk_f32 v104, v104, 0x3e0293ee, v87\n"
    "v_fmamk_f32 v105, v105, 0x3e0293ee, v87\n"
    "v_fmamk_f32 v106, v106, 0x3e0293ee, v87\n"
    "v_fmamk_f32 v107, v107, 0x3e0293ee, v87\n"
    "v_exp_f32 v96, v96\n"
    "v_exp_f32 v97, v97\n"
    "v_exp_f32 v98, v98\n"
    "v_exp_f32 v99, v99\n"
    "v_exp_f32 v100, v100\n"
    "v_exp_f32 v101, v101\n"
    "v_exp_f32 v102, v102\n"
    "v_exp_f32 v103, v103\n"
    "v_exp_f32 v104, v104\n"
    "v_exp_f32 v105, v105\n"
    "v_exp_f32 v106, v106\n"
    "v_exp_f32 v107, v107\n"
    "v_cvt_pk_bf16_f32 v96, v96, v97\n"
    "v_cvt_pk_bf16_f32 v97, v98, v99\n"
    "v_cvt_pk_bf16_f32 v98, v100, v101\n"
    "v_cvt_pk_bf16_f32 v99, v102, v103\n"
    "v_cvt_pk_bf16_f32 v104, v104, v105\n"
    "v_cvt_pk_bf16_f32 v105, v106, v107\n"
    "s_waitcnt lgkmcnt(1)\n"
    "v_mfma_f32_16x16x32_bf16 v[108:111], v[80:83], v[96:99], 0\n"
    "v_mfma_f32_16x16x32_bf16 v[88:91], v[32:35], v[96:99], 0\n"
    "v_mfma_f32_16x16x32_bf16 v[120:123], v[36:39], v[96:99], 0\n"
    "v_mfma_f32_16x16x32_bf16 v[92:95], v[40:43], v[96:99], 0\n"
    "s_waitcnt lgkmcnt(0)\n"
    "v_mfma_f32_16x16x16_bf16 v[108:111], v[84:85], v[104:105], v[108:111]\n"
    "v_mfma_f32_16x16x32_bf16 v[124:127], v[44:47], v[96:99], 0\n"
    "v_mfma_f32_16x16x32_bf16 v[112:115], v[48:51], v[96:99], 0\n"
    "v_mfma_f32_16x16x32_bf16 v[128:131], v[52:55], v[96:99], 0\n"
    "v_mfma_f32_16x16x32_bf16 v[116:119], v[56:59], v[96:99], 0\n"
    "v_mfma_f32_16x16x32_bf16 v[132:135], v[60:63], v[96:99], 0\n"
    "v_rcp_f32 v86, v108\n"
    "v_mfma_f32_16x16x16_bf16 v[88:91], v[64:65], v[104:105], v[88:91]\n"
    "v_mfma_f32_16x16x16_bf16 v[120:123], v[66:67], v[104:105], v[120:123]\n"
    "v_mfma_f32_16x16x16_bf16 v[92:95], v[68:69], v[104:105], v[92:95]\n"
    "v_mfma_f32_16x16x16_bf16 v[124:127], v[70:71], v[104:105], v[124:127]\n"
    "v_mfma_f32_16x16x16_bf16 v[112:115], v[72:73], v[104:105], v[112:115]\n"
    "v_mfma_f32_16x16x16_bf16 v[128:131], v[74:75], v[104:105], v[128:131]\n"
    "v_mfma_f32_16x16x16_bf16 v[116:119], v[76:77], v[104:105], v[116:119]\n"
    "v_mfma_f32_16x16x16_bf16 v[132:135], v[78:79], v[104:105], v[132:135]\n"
    "v_pk_mul_f32 v[88:89], v[88:89], v[86:87] op_sel_hi:[1,0]\n"
    "v_pk_mul_f32 v[90:91], v[90:91], v[86:87] op_sel_hi:[1,0]\n"
    "v_pk_mul_f32 v[120:121], v[120:121], v[86:87] op_sel_hi:[1,0]\n"
    "v_pk_mul_f32 v[122:123], v[122:123], v[86:87] op_sel_hi:[1,0]\n"
    "v_cvt_pk_bf16_f32 v88, v88, v120\n"
    "v_cvt_pk_bf16_f32 v89, v89, v121\n"
    "v_cvt_pk_bf16_f32 v90, v90, v122\n"
    "v_cvt_pk_bf16_f32 v91, v91, v123\n"
    "v_pk_mul_f32 v[92:93], v[92:93], v[86:87] op_sel_hi:[1,0]\n"
    "v_pk_mul_f32 v[94:95], v[94:95], v[86:87] op_sel_hi:[1,0]\n"
    "v_pk_mul_f32 v[124:125], v[124:125], v[86:87] op_sel_hi:[1,0]\n"
    "v_pk_mul_f32 v[126:127], v[126:127], v[86:87] op_sel_hi:[1,0]\n"
    "v_cvt_pk_bf16_f32 v92, v92, v124\n"
    "v_cvt_pk_bf16_f32 v93, v93, v125\n"
    "v_cvt_pk_bf16_f32 v94, v94, v126\n"
    "v_cvt_pk_bf16_f32 v95, v95, v127\n"
    "v_pk_mul_f32 v[112:113], v[112:113], v[86:87] op_sel_hi:[1,0]\n"
    "v_pk_mul_f32 v[114:115], v[114:115], v[86:87] op_sel_hi:[1,0]\n"
    "v_pk_mul_f32 v[128:129], v[128:129], v[86:87] op_sel_hi:[1,0]\n"
    "v_pk_mul_f32 v[130:131], v[130:131], v[86:87] op_sel_hi:[1,0]\n"
    "v_cvt_pk_bf16_f32 v112, v112, v128\n"
    "v_cvt_pk_bf16_f32 v113, v113, v129\n"
    "v_cvt_pk_bf16_f32 v114, v114, v130\n"
    "v_cvt_pk_bf16_f32 v115, v115, v131\n"
    "v_pk_mul_f32 v[116:117], v[116:117], v[86:87] op_sel_hi:[1,0]\n"
    "v_pk_mul_f32 v[118:119], v[118:119], v[86:87] op_sel_hi:[1,0]\n"
    "v_pk_mul_f32 v[132:133], v[132:133], v[86:87] op_sel_hi:[1,0]\n"
    "v_pk_mul_f32 v[134:135], v[134:135], v[86:87] op_sel_hi:[1,0]\n"
    "v_cvt_pk_bf16_f32 v116, v116, v132\n"
    "v_cvt_pk_bf16_f32 v117, v117, v133\n"
    "v_cvt_pk_bf16_f32 v118, v118, v134\n"
    "v_cvt_pk_bf16_f32 v119, v119, v135\n"
    "s_lshr_b32 s51, s48, 2\n"
    "s_xor_b32 s51, s51, s69\n"
    "s_and_b32 s51, s51, 1\n"
    "s_lshl_b32 s51, s51, 13\n"
    "s_lshl_b32 s73, s48, 19\n"
    "s_add_u32 s73, s73, s75\n"
    "s_add_u32 s73, s73, s51\n"
    "buffer_store_dwordx4 v[88:91], v0, s[40:43], s73 offen sc1\n"
    "buffer_store_dwordx4 v[92:95], v0, s[40:43], s73 offen offset:1024 sc1\n"
    "buffer_store_dwordx4 v[112:115], v0, s[40:43], s73 offen offset:2048 sc1\n"
    "buffer_store_dwordx4 v[116:119], v0, s[40:43], s73 offen offset:3072 sc1\n"
    "s_waitcnt vmcnt(0)\n"
    "s_add_u32 s73, s48, 0\n"
    "s_and_b32 s51, s73, 32\n"
    "s_and_b32 s73, s73, 31\n"
    "s_lshl3_add_u32 s73, s51, s73\n"
    "s_lshl2_add_u32 s73, s73, s93\n"
    "v_mov_b32 v134, s73\n"
    "v_mov_b32 v135, 1\n"
    "s_mov_b64 exec, 1\n"
    "global_store_dword v134, v135, %1 sc1\n"
    "s_mov_b64 exec, -1\n"
    ".Lout_%=:\n"
    "s_waitcnt vmcnt(0)\n"
    "s_endpgm\n"
    ".p2align 6\n"
    ".Lrole1_%=:\n"
    "s_mov_b64 s[40:41], %0\n"
    "s_and_b32 s41, s41, 0xffff\n"
    "s_mov_b32 s42, 0xbc00000\n"
    "s_mov_b32 s43, 0x20000\n"
    "s_mov_b64 s[92:93], %0\n"
    "s_and_b32 s93, s93, 0xffff\n"
    "s_mov_b32 s94, 0xbc00000\n"
    "s_mov_b32 s95, 0x20000\n"
    "s_mov_b32 s53, 131072\n"
    "s_mov_b64 s[56:57], 0\n"
    "v_mbcnt_lo_u32_b32 v7, -1, 0\n"
    "v_mbcnt_hi_u32_b32 v7, -1, v7\n"
    "s_lshl_b32 s50, %2, 8\n"
    "s_add_u32 s50, s50, 147456\n"
    "v_lshl_add_u32 v7, v7, 2, s50\n"
    "ds_read_b32 v1, v7\n"
    "ds_read_b32 v2, v7 offset:1024\n"
    "ds_read_b32 v3, v7 offset:2048\n"
    "ds_read_b32 v5, v7 offset:3072\n"
    "ds_read_b32 v6, v7 offset:4096\n"
    "ds_read_b32 v48, v7 offset:5120\n"
    "ds_read_b32 v49, v7 offset:6144\n"
    "ds_read_b32 v50, v7 offset:7168\n"
    "s_waitcnt lgkmcnt(0)\n"
    "s_barrier\n"
    "s_lshl_b32 s54, %2, 10\n"
    "s_add_u32 s88, s40, 32768\n"
    "s_mov_b32 s89, s41\n"
    "s_cmp_lt_u32 %2, 2\n"
    "s_cselect_b32 s96, s42, 0\n"
    "s_mov_b32 s91, s43\n"
    "s_mov_b32 s86, 0x3f803f80\n"
    "s_mov_b32 s87, 0x3f803f80\n"
    "v_mov_b64 v[76:77], s[86:87]\n"
    "v_mov_b64 v[78:79], s[86:87]\n"
    "s_mov_b64 s[44:45], %8\n"
    "s_and_b32 s45, s45, 0xffff\n"
    "s_mov_b32 s46, 0x480000\n"
    "s_mov_b32 s47, 0x20000\n"
    "s_add_u32 m0, s54, 0\n"
    "s_add_u32 s55, %6, 0\n"
    "buffer_load_dwordx4 v48, s[44:47], s55 offen lds\n"
    "s_add_u32 m0, s54, 4096\n"
    "s_add_u32 s55, %6, 1048576\n"
    "buffer_load_dwordx4 v48, s[44:47], s55 offen lds\n"
    "s_add_u32 m0, s54, 8192\n"
    "s_add_u32 s55, %6, 2097152\n"
    "buffer_load_dwordx4 v48, s[44:47], s55 offen lds\n"
    "s_add_u32 m0, s54, 12288\n"
    "s_add_u32 s55, %6, 3145728\n"
    "buffer_load_dwordx4 v48, s[44:47], s55 offen lds\n"
    "s_add_u32 m0, s54, 16384\n"
    "s_add_u32 s55, %6, 32768\n"
    "buffer_load_dwordx4 v48, s[44:47], s55 offen lds\n"
    "s_add_u32 m0, s54, 20480\n"
    "s_add_u32 s55, %6, 1081344\n"
    "buffer_load_dwordx4 v48, s[44:47], s55 offen lds\n"
    "s_add_u32 m0, s54, 24576\n"
    "s_add_u32 s55, %6, 2129920\n"
    "buffer_load_dwordx4 v48, s[44:47], s55 offen lds\n"
    "s_add_u32 m0, s54, 28672\n"
    "s_add_u32 s55, %6, 3178496\n"
    "buffer_load_dwordx4 v48, s[44:47], s55 offen lds\n"
    "s_add_u32 m0, s54, 32768\n"
    "s_add_u32 s55, %6, 262144\n"
    "buffer_load_dwordx4 v48, s[44:47], s55 offen lds\n"
    "s_add_u32 m0, s54, 36864\n"
    "s_add_u32 s55, %6, 1310720\n"
    "buffer_load_dwordx4 v48, s[44:47], s55 offen lds\n"
    "s_add_u32 m0, s54, 40960\n"
    "s_add_u32 s55, %6, 2359296\n"
    "buffer_load_dwordx4 v48, s[44:47], s55 offen lds\n"
    "s_add_u32 m0, s54, 45056\n"
    "s_add_u32 s55, %6, 3407872\n"
    "buffer_load_dwordx4 v48, s[44:47], s55 offen lds\n"
    "s_add_u32 m0, s54, 49152\n"
    "s_add_u32 s55, %6, 294912\n"
    "buffer_load_dwordx4 v48, s[44:47], s55 offen lds\n"
    "s_add_u32 m0, s54, 53248\n"
    "s_add_u32 s55, %6, 1343488\n"
    "buffer_load_dwordx4 v48, s[44:47], s55 offen lds\n"
    "s_add_u32 m0, s54, 57344\n"
    "s_add_u32 s55, %6, 2392064\n"
    "buffer_load_dwordx4 v48, s[44:47], s55 offen lds\n"
    "s_add_u32 m0, s54, 61440\n"
    "s_add_u32 s55, %6, 3440640\n"
    "buffer_load_dwordx4 v48, s[44:47], s55 offen lds\n"
    "s_add_u32 m0, s54, 65536\n"
    "s_add_u32 s55, %6, 524288\n"
    "buffer_load_dwordx4 v48, s[44:47], s55 offen lds\n"
    "s_add_u32 m0, s54, 69632\n"
    "s_add_u32 s55, %6, 1572864\n"
    "buffer_load_dwordx4 v48, s[44:47], s55 offen lds\n"
    "s_add_u32 m0, s54, 73728\n"
    "s_add_u32 s55, %6, 2621440\n"
    "buffer_load_dwordx4 v48, s[44:47], s55 offen lds\n"
    "s_add_u32 m0, s54, 77824\n"
    "s_add_u32 s55, %6, 3670016\n"
    "buffer_load_dwordx4 v48, s[44:47], s55 offen lds\n"
    "s_add_u32 m0, s54, 81920\n"
    "s_add_u32 s55, %6, 557056\n"
    "buffer_load_dwordx4 v48, s[44:47], s55 offen lds\n"
    "s_add_u32 m0, s54, 86016\n"
    "s_add_u32 s55, %6, 1605632\n"
    "buffer_load_dwordx4 v48, s[44:47], s55 offen lds\n"
    "s_add_u32 m0, s54, 90112\n"
    "s_add_u32 s55, %6, 2654208\n"
    "buffer_load_dwordx4 v48, s[44:47], s55 offen lds\n"
    "s_add_u32 m0, s54, 94208\n"
    "s_add_u32 s55, %6, 3702784\n"
    "buffer_load_dwordx4 v48, s[44:47], s55 offen lds\n"
    "s_add_u32 m0, s54, 98304\n"
    "s_add_u32 s55, %6, 786432\n"
    "buffer_load_dwordx4 v48, s[44:47], s55 offen lds\n"
    "s_add_u32 m0, s54, 102400\n"
    "s_add_u32 s55, %6, 1835008\n"
    "buffer_load_dwordx4 v48, s[44:47], s55 offen lds\n"
    "s_add_u32 m0, s54, 106496\n"
    "s_add_u32 s55, %6, 2883584\n"
    "buffer_load_dwordx4 v48, s[44:47], s55 offen lds\n"
    "s_add_u32 m0, s54, 110592\n"
    "s_add_u32 s55, %6, 3932160\n"
    "buffer_load_dwordx4 v48, s[44:47], s55 offen lds\n"
    "s_add_u32 m0, s54, 114688\n"
    "s_add_u32 s55, %6, 819200\n"
    "buffer_load_dwordx4 v48, s[44:47], s55 offen lds\n"
    "s_add_u32 m0, s54, 118784\n"
    "s_add_u32 s55, %6, 1867776\n"
    "buffer_load_dwordx4 v48, s[44:47], s55 offen lds\n"
    "s_add_u32 m0, s54, 122880\n"
    "s_add_u32 s55, %6, 2916352\n"
    "buffer_load_dwordx4 v48, s[44:47], s55 offen lds\n"
    "s_add_u32 m0, s54, 126976\n"
    "s_add_u32 s55, %6, 3964928\n"
    "buffer_load_dwordx4 v48, s[44:47], s55 offen lds\n"
    "s_add_u32 m0, s54, 131072\n"
    "s_add_u32 s55, %6, 4194304\n"
    "buffer_load_dwordx4 v48, s[44:47], s55 offen lds\n"
    "s_add_u32 m0, s54, 135168\n"
    "s_add_u32 s55, %6, 4227072\n"
    "buffer_load_dwordx4 v48, s[44:47], s55 offen lds\n"
    "s_add_u32 m0, s54, 139264\n"
    "s_add_u32 s55, %6, 4456448\n"
    "buffer_load_dwordx4 v48, s[44:47], s55 offen lds\n"
    "s_add_u32 m0, s54, 143360\n"
    "s_add_u32 s55, %6, 4489216\n"
    "buffer_load_dwordx4 v48, s[44:47], s55 offen lds\n"
    "s_xor_b32 s50, %2, 0\n"
    "s_lshl_b32 s50, s50, 12\n"
    "v_add_u32 v32, s50, v49\n"
    "v_add_u32 v33, s50, v50\n"
    "s_xor_b32 s50, %2, 1\n"
    "s_lshl_b32 s50, s50, 12\n"
    "v_add_u32 v34, s50, v49\n"
    "v_add_u32 v35, s50, v50\n"
    "s_xor_b32 s50, %2, 2\n"
    "s_lshl_b32 s50, s50, 12\n"
    "v_add_u32 v36, s50, v49\n"
    "v_add_u32 v37, s50, v50\n"
    "s_xor_b32 s50, %2, 3\n"
    "s_lshl_b32 s50, s50, 12\n"
    "v_add_u32 v38, s50, v49\n"
    "v_add_u32 v39, s50, v50\n"
    "s_waitcnt vmcnt(32) lgkmcnt(0)\n"
    "s_barrier\n"
    "ds_read_b64_tr_b16 a[0:1], v32\n"
    "ds_read_b64_tr_b16 a[4:5], v33\n"
    "ds_read_b64_tr_b16 a[32:33], v34\n"
    "ds_read_b64_tr_b16 a[36:37], v35\n"
    "ds_read_b64_tr_b16 a[64:65], v36\n"
    "ds_read_b64_tr_b16 a[68:69], v37\n"
    "ds_read_b64_tr_b16 a[96:97], v38\n"
    "ds_read_b64_tr_b16 a[100:101], v39\n"
    "s_waitcnt vmcnt(28) lgkmcnt(0)\n"
    "s_barrier\n"
    "s_mov_b64 s[44:45], %9\n"
    "s_and_b32 s45, s45, 0xffff\n"
    "s_mov_b32 s46, 0x480000\n"
    "s_mov_b32 s47, 0x20000\n"
    "s_add_u32 m0, s54, 0\n"
    "s_add_u32 s55, %6, 0\n"
    "buffer_load_dwordx4 v48, s[44:47], s55 offen lds\n"
    "s_add_u32 m0, s54, 4096\n"
    "s_add_u32 s55, %6, 1048576\n"
    "buffer_load_dwordx4 v48, s[44:47], s55 offen lds\n"
    "s_add_u32 m0, s54, 8192\n"
    "s_add_u32 s55, %6, 2097152\n"
    "buffer_load_dwordx4 v48, s[44:47], s55 offen lds\n"
    "s_add_u32 m0, s54, 12288\n"
    "s_add_u32 s55, %6, 3145728\n"
    "buffer_load_dwordx4 v48, s[44:47], s55 offen lds\n"
    "ds_read_b64_tr_b16 a[2:3], v32 offset:16384\n"
    "ds_read_b64_tr_b16 a[6:7], v33 offset:16384\n"
    "ds_read_b64_tr_b16 a[34:35], v34 offset:16384\n"
    "ds_read_b64_tr_b16 a[38:39], v35 offset:16384\n"
    "ds_read_b64_tr_b16 a[66:67], v36 offset:16384\n"
    "ds_read_b64_tr_b16 a[70:71], v37 offset:16384\n"
    "ds_read_b64_tr_b16 a[98:99], v38 offset:16384\n"
    "ds_read_b64_tr_b16 a[102:103], v39 offset:16384\n"
    "s_waitcnt vmcnt(28) lgkmcnt(0)\n"
    "s_barrier\n"
    "s_add_u32 m0, s54, 16384\n"
    "s_add_u32 s55, %6, 32768\n"
    "buffer_load_dwordx4 v48, s[44:47], s55 offen lds\n"
    "s_add_u32 m0, s54, 20480\n"
    "s_add_u32 s55, %6, 1081344\n"
    "buffer_load_dwordx4 v48, s[44:47], s55 offen lds\n"
    "s_add_u32 m0, s54, 24576\n"
    "s_add_u32 s55, %6, 2129920\n"
    "buffer_load_dwordx4 v48, s[44:47], s55 offen lds\n"
    "s_add_u32 m0, s54, 28672\n"
    "s_add_u32 s55, %6, 3178496\n"
    "buffer_load_dwordx4 v48, s[44:47], s55 offen lds\n"
    "v_mfma_f32_16x16x32_bf16 v[8:11], v[76:79], a[0:3], 0\n"
    "v_mfma_f32_16x16x32_bf16 v[12:15], v[76:79], a[4:7], 0\n"
    "v_mfma_f32_16x16x32_bf16 v[8:11], v[76:79], a[32:35], v[8:11]\n"
    "v_mfma_f32_16x16x32_bf16 v[12:15], v[76:79], a[36:39], v[12:15]\n"
    "v_mfma_f32_16x16x32_bf16 v[8:11], v[76:79], a[64:67], v[8:11]\n"
    "v_mfma_f32_16x16x32_bf16 v[12:15], v[76:79], a[68:71], v[12:15]\n"
    "v_mfma_f32_16x16x32_bf16 v[8:11], v[76:79], a[96:99], v[8:11]\n"
    "v_mfma_f32_16x16x32_bf16 v[12:15], v[76:79], a[100:103], v[12:15]\n"
    "ds_read_b64_tr_b16 a[8:9], v32 offset:32768\n"
    "ds_read_b64_tr_b16 a[12:13], v33 offset:32768\n"
    "ds_read_b64_tr_b16 a[40:41], v34 offset:32768\n"
    "ds_read_b64_tr_b16 a[44:45], v35 offset:32768\n"
    "ds_read_b64_tr_b16 a[72:73], v36 offset:32768\n"
    "ds_read_b64_tr_b16 a[76:77], v37 offset:32768\n"
    "ds_read_b64_tr_b16 a[104:105], v38 offset:32768\n"
    "ds_read_b64_tr_b16 a[108:109], v39 offset:32768\n"
    "s_waitcnt vmcnt(28) lgkmcnt(0)\n"
    "s_barrier\n"
    "s_add_u32 m0, s54, 32768\n"
    "s_add_u32 s55, %6, 262144\n"
    "buffer_load_dwordx4 v48, s[44:47], s55 offen lds\n"
    "s_add_u32 m0, s54, 36864\n"
    "s_add_u32 s55, %6, 1310720\n"
    "buffer_load_dwordx4 v48, s[44:47], s55 offen lds\n"
    "s_add_u32 m0, s54, 40960\n"
    "s_add_u32 s55, %6, 2359296\n"
    "buffer_load_dwordx4 v48, s[44:47], s55 offen lds\n"
    "s_add_u32 m0, s54, 45056\n"
    "s_add_u32 s55, %6, 3407872\n"
    "buffer_load_dwordx4 v48, s[44:47], s55 offen lds\n"
    "ds_read_b64_tr_b16 a[10:11], v32 offset:49152\n"
    "ds_read_b64_tr_b16 a[14:15], v33 offset:49152\n"
    "ds_read_b64_tr_b16 a[42:43], v34 offset:49152\n"
    "ds_read_b64_tr_b16 a[46:47], v35 offset:49152\n"
    "ds_read_b64_tr_b16 a[74:75], v36 offset:49152\n"
    "ds_read_b64_tr_b16 a[78:79], v37 offset:49152\n"
    "ds_read_b64_tr_b16 a[106:107], v38 offset:49152\n"
    "ds_read_b64_tr_b16 a[110:111], v39 offset:49152\n"
    "s_waitcnt vmcnt(28) lgkmcnt(0)\n"
    "s_barrier\n"
    "s_add_u32 m0, s54, 49152\n"
    "s_add_u32 s55, %6, 294912\n"
    "buffer_load_dwordx4 v48, s[44:47], s55 offen lds\n"
    "s_add_u32 m0, s54, 53248\n"
    "s_add_u32 s55, %6, 1343488\n"
    "buffer_load_dwordx4 v48, s[44:47], s55 offen lds\n"
    "s_add_u32 m0, s54, 57344\n"
    "s_add_u32 s55, %6, 2392064\n"
    "buffer_load_dwordx4 v48, s[44:47], s55 offen lds\n"
    "s_add_u32 m0, s54, 61440\n"
    "s_add_u32 s55, %6, 3440640\n"
    "buffer_load_dwordx4 v48, s[44:47], s55 offen lds\n"
    "v_mfma_f32_16x16x32_bf16 v[8:11], v[76:79], a[8:11], v[8:11]\n"
    "v_mfma_f32_16x16x32_bf16 v[12:15], v[76:79], a[12:15], v[12:15]\n"
    "v_mfma_f32_16x16x32_bf16 v[8:11], v[76:79], a[40:43], v[8:11]\n"
    "v_mfma_f32_16x16x32_bf16 v[12:15], v[76:79], a[44:47], v[12:15]\n"
    "v_mfma_f32_16x16x32_bf16 v[8:11], v[76:79], a[72:75], v[8:11]\n"
    "v_mfma_f32_16x16x32_bf16 v[12:15], v[76:79], a[76:79], v[12:15]\n"
    "v_mfma_f32_16x16x32_bf16 v[8:11], v[76:79], a[104:107], v[8:11]\n"
    "v_mfma_f32_16x16x32_bf16 v[12:15], v[76:79], a[108:111], v[12:15]\n"
    "v_add_u32 v71, 0x10000, v32\n"
    "ds_read_b64_tr_b16 a[16:17], v71\n"
    "v_add_u32 v70, 0x10000, v33\n"
    "ds_read_b64_tr_b16 a[20:21], v70\n"
    "v_add_u32 v69, 0x10000, v34\n"
    "ds_read_b64_tr_b16 a[48:49], v69\n"
    "v_add_u32 v68, 0x10000, v35\n"
    "ds_read_b64_tr_b16 a[52:53], v68\n"
    "v_add_u32 v67, 0x10000, v36\n"
    "ds_read_b64_tr_b16 a[80:81], v67\n"
    "v_add_u32 v66, 0x10000, v37\n"
    "ds_read_b64_tr_b16 a[84:85], v66\n"
    "v_add_u32 v65, 0x10000, v38\n"
    "ds_read_b64_tr_b16 a[112:113], v65\n"
    "v_add_u32 v64, 0x10000, v39\n"
    "ds_read_b64_tr_b16 a[116:117], v64\n"
    "s_waitcnt vmcnt(28) lgkmcnt(0)\n"
    "s_barrier\n"
    "s_add_u32 m0, s54, 65536\n"
    "s_add_u32 s55, %6, 524288\n"
    "buffer_load_dwordx4 v48, s[44:47], s55 offen lds\n"
    "s_add_u32 m0, s54, 69632\n"
    "s_add_u32 s55, %6, 1572864\n"
    "buffer_load_dwordx4 v48, s[44:47], s55 offen lds\n"
    "s_add_u32 m0, s54, 73728\n"
    "s_add_u32 s55, %6, 2621440\n"
    "buffer_load_dwordx4 v48, s[44:47], s55 offen lds\n"
    "s_add_u32 m0, s54, 77824\n"
    "s_add_u32 s55, %6, 3670016\n"
    "buffer_load_dwordx4 v48, s[44:47], s55 offen lds\n"
    "ds_read_b64_tr_b16 a[18:19], v71 offset:16384\n"
    "ds_read_b64_tr_b16 a[22:23], v70 offset:16384\n"
    "ds_read_b64_tr_b16 a[50:51], v69 offset:16384\n"
    "ds_read_b64_tr_b16 a[54:55], v68 offset:16384\n"
    "ds_read_b64_tr_b16 a[82:83], v67 offset:16384\n"
    "ds_read_b64_tr_b16 a[86:87], v66 offset:16384\n"
    "ds_read_b64_tr_b16 a[114:115], v65 offset:16384\n"
    "ds_read_b64_tr_b16 a[118:119], v64 offset:16384\n"
    "s_waitcnt vmcnt(28) lgkmcnt(0)\n"
    "s_barrier\n"
    "s_add_u32 m0, s54, 81920\n"
    "s_add_u32 s55, %6, 557056\n"
    "buffer_load_dwordx4 v48, s[44:47], s55 offen lds\n"
    "s_add_u32 m0, s54, 86016\n"
    "s_add_u32 s55, %6, 1605632\n"
    "buffer_load_dwordx4 v48, s[44:47], s55 offen lds\n"
    "s_add_u32 m0, s54, 90112\n"
    "s_add_u32 s55, %6, 2654208\n"
    "buffer_load_dwordx4 v48, s[44:47], s55 offen lds\n"
    "s_add_u32 m0, s54, 94208\n"
    "s_add_u32 s55, %6, 3702784\n"
    "buffer_load_dwordx4 v48, s[44:47], s55 offen lds\n"
    "v_mfma_f32_16x16x32_bf16 v[8:11], v[76:79], a[16:19], v[8:11]\n"
    "v_mfma_f32_16x16x32_bf16 v[12:15], v[76:79], a[20:23], v[12:15]\n"
    "v_mfma_f32_16x16x32_bf16 v[8:11], v[76:79], a[48:51], v[8:11]\n"
    "v_mfma_f32_16x16x32_bf16 v[12:15], v[76:79], a[52:55], v[12:15]\n"
    "v_mfma_f32_16x16x32_bf16 v[8:11], v[76:79], a[80:83], v[8:11]\n"
    "v_mfma_f32_16x16x32_bf16 v[12:15], v[76:79], a[84:87], v[12:15]\n"
    "v_mfma_f32_16x16x32_bf16 v[8:11], v[76:79], a[112:115], v[8:11]\n"
    "v_mfma_f32_16x16x32_bf16 v[12:15], v[76:79], a[116:119], v[12:15]\n"
    "ds_read_b64_tr_b16 a[24:25], v71 offset:32768\n"
    "ds_read_b64_tr_b16 a[28:29], v70 offset:32768\n"
    "ds_read_b64_tr_b16 a[56:57], v69 offset:32768\n"
    "ds_read_b64_tr_b16 a[60:61], v68 offset:32768\n"
    "ds_read_b64_tr_b16 a[88:89], v67 offset:32768\n"
    "ds_read_b64_tr_b16 a[92:93], v66 offset:32768\n"
    "ds_read_b64_tr_b16 a[120:121], v65 offset:32768\n"
    "ds_read_b64_tr_b16 a[124:125], v64 offset:32768\n"
    "s_waitcnt vmcnt(28) lgkmcnt(0)\n"
    "s_barrier\n"
    "s_add_u32 m0, s54, 98304\n"
    "s_add_u32 s55, %6, 786432\n"
    "buffer_load_dwordx4 v48, s[44:47], s55 offen lds\n"
    "s_add_u32 m0, s54, 102400\n"
    "s_add_u32 s55, %6, 1835008\n"
    "buffer_load_dwordx4 v48, s[44:47], s55 offen lds\n"
    "s_add_u32 m0, s54, 106496\n"
    "s_add_u32 s55, %6, 2883584\n"
    "buffer_load_dwordx4 v48, s[44:47], s55 offen lds\n"
    "s_add_u32 m0, s54, 110592\n"
    "s_add_u32 s55, %6, 3932160\n"
    "buffer_load_dwordx4 v48, s[44:47], s55 offen lds\n"
    "ds_read_b64_tr_b16 a[26:27], v71 offset:49152\n"
    "ds_read_b64_tr_b16 a[30:31], v70 offset:49152\n"
    "ds_read_b64_tr_b16 a[58:59], v69 offset:49152\n"
    "ds_read_b64_tr_b16 a[62:63], v68 offset:49152\n"
    "ds_read_b64_tr_b16 a[90:91], v67 offset:49152\n"
    "ds_read_b64_tr_b16 a[94:95], v66 offset:49152\n"
    "ds_read_b64_tr_b16 a[122:123], v65 offset:49152\n"
    "ds_read_b64_tr_b16 a[126:127], v64 offset:49152\n"
    "s_waitcnt vmcnt(28) lgkmcnt(0)\n"
    "s_barrier\n"
    "s_add_u32 m0, s54, 114688\n"
    "s_add_u32 s55, %6, 819200\n"
    "buffer_load_dwordx4 v48, s[44:47], s55 offen lds\n"
    "s_add_u32 m0, s54, 118784\n"
    "s_add_u32 s55, %6, 1867776\n"
    "buffer_load_dwordx4 v48, s[44:47], s55 offen lds\n"
    "s_add_u32 m0, s54, 122880\n"
    "s_add_u32 s55, %6, 2916352\n"
    "buffer_load_dwordx4 v48, s[44:47], s55 offen lds\n"
    "s_add_u32 m0, s54, 126976\n"
    "s_add_u32 s55, %6, 3964928\n"
    "buffer_load_dwordx4 v48, s[44:47], s55 offen lds\n"
    "v_mfma_f32_16x16x32_bf16 v[8:11], v[76:79], a[24:27], v[8:11]\n"
    "v_mfma_f32_16x16x32_bf16 v[12:15], v[76:79], a[28:31], v[12:15]\n"
    "v_mfma_f32_16x16x32_bf16 v[8:11], v[76:79], a[56:59], v[8:11]\n"
    "v_mfma_f32_16x16x32_bf16 v[12:15], v[76:79], a[60:63], v[12:15]\n"
    "v_mfma_f32_16x16x32_bf16 v[8:11], v[76:79], a[88:91], v[8:11]\n"
    "v_mfma_f32_16x16x32_bf16 v[12:15], v[76:79], a[92:95], v[12:15]\n"
    "v_mfma_f32_16x16x32_bf16 v[8:11], v[76:79], a[120:123], v[8:11]\n"
    "v_mfma_f32_16x16x32_bf16 v[12:15], v[76:79], a[124:127], v[12:15]\n"
    "v_add_u32 v63, 0x20000, v49\n"
    "ds_read_b64_tr_b16 a[128:129], v63\n"
    "v_add_u32 v62, 0x20000, v50\n"
    "ds_read_b64_tr_b16 a[132:133], v62\n"
    "ds_read_b64_tr_b16 a[130:131], v63 offset:4096\n"
    "ds_read_b64_tr_b16 a[134:135], v62 offset:4096\n"
    "ds_read_b64_tr_b16 a[136:137], v63 offset:8192\n"
    "ds_read_b64_tr_b16 a[140:141], v62 offset:8192\n"
    "ds_read_b64_tr_b16 a[138:139], v63 offset:12288\n"
    "ds_read_b64_tr_b16 a[142:143], v62 offset:12288\n"
    "s_waitcnt vmcnt(28) lgkmcnt(0)\n"
    "s_barrier\n"
    "s_add_u32 m0, s54, 131072\n"
    "s_add_u32 s55, %6, 4194304\n"
    "buffer_load_dwordx4 v48, s[44:47], s55 offen lds\n"
    "s_add_u32 m0, s54, 135168\n"
    "s_add_u32 s55, %6, 4227072\n"
    "buffer_load_dwordx4 v48, s[44:47], s55 offen lds\n"
    "s_add_u32 m0, s54, 139264\n"
    "s_add_u32 s55, %6, 4456448\n"
    "buffer_load_dwordx4 v48, s[44:47], s55 offen lds\n"
    "s_add_u32 m0, s54, 143360\n"
    "s_add_u32 s55, %6, 4489216\n"
    "buffer_load_dwordx4 v48, s[44:47], s55 offen lds\n"
    "v_mfma_f32_16x16x32_bf16 v[8:11], v[76:79], a[128:131], v[8:11]\n"
    "v_mfma_f32_16x16x32_bf16 v[12:15], v[76:79], a[132:135], v[12:15]\n"
    "v_mfma_f32_16x16x32_bf16 v[8:11], v[76:79], a[136:139], v[8:11]\n"
    "v_mfma_f32_16x16x32_bf16 v[12:15], v[76:79], a[140:143], v[12:15]\n"
    "ds_read_b64_tr_b16 a[144:145], v32\n"
    "ds_read_b64_tr_b16 a[148:149], v33\n"
    "ds_read_b64_tr_b16 a[176:177], v34\n"
    "ds_read_b64_tr_b16 a[180:181], v35\n"
    "ds_read_b64_tr_b16 a[208:209], v36\n"
    "ds_read_b64_tr_b16 a[212:213], v37\n"
    "ds_read_b64_tr_b16 a[240:241], v38\n"
    "ds_read_b64_tr_b16 a[244:245], v39\n"
    "s_waitcnt vmcnt(28) lgkmcnt(0)\n"
    "s_barrier\n"
    "s_mov_b64 s[44:45], %10\n"
    "s_and_b32 s45, s45, 0xffff\n"
    "s_mov_b32 s46, 0x480000\n"
    "s_mov_b32 s47, 0x20000\n"
    "s_add_u32 m0, s54, 0\n"
    "s_add_u32 s55, %6, 0\n"
    "buffer_load_dwordx4 v48, s[44:47], s55 offen lds\n"
    "s_add_u32 m0, s54, 4096\n"
    "s_add_u32 s55, %6, 1048576\n"
    "buffer_load_dwordx4 v48, s[44:47], s55 offen lds\n"
    "s_add_u32 m0, s54, 8192\n"
    "s_add_u32 s55, %6, 2097152\n"
    "buffer_load_dwordx4 v48, s[44:47], s55 offen lds\n"
    "s_add_u32 m0, s54, 12288\n"
    "s_add_u32 s55, %6, 3145728\n"
    "buffer_load_dwordx4 v48, s[44:47], s55 offen lds\n"
    "ds_read_b64_tr_b16 a[146:147], v32 offset:16384\n"
    "ds_read_b64_tr_b16 a[150:151], v33 offset:16384\n"
    "ds_read_b64_tr_b16 a[178:179], v34 offset:16384\n"
    "ds_read_b64_tr_b16 a[182:183], v35 offset:16384\n"
    "ds_read_b64_tr_b16 a[210:211], v36 offset:16384\n"
    "ds_read_b64_tr_b16 a[214:215], v37 offset:16384\n"
    "ds_read_b64_tr_b16 a[242:243], v38 offset:16384\n"
    "ds_read_b64_tr_b16 a[246:247], v39 offset:16384\n"
    "s_waitcnt vmcnt(28) lgkmcnt(0)\n"
    "s_barrier\n"
    "s_add_u32 m0, s54, 16384\n"
    "s_add_u32 s55, %6, 32768\n"
    "buffer_load_dwordx4 v48, s[44:47], s55 offen lds\n"
    "s_add_u32 m0, s54, 20480\n"
    "s_add_u32 s55, %6, 1081344\n"
    "buffer_load_dwordx4 v48, s[44:47], s55 offen lds\n"
    "s_add_u32 m0, s54, 24576\n"
    "s_add_u32 s55, %6, 2129920\n"
    "buffer_load_dwordx4 v48, s[44:47], s55 offen lds\n"
    "s_add_u32 m0, s54, 28672\n"
    "s_add_u32 s55, %6, 3178496\n"
    "buffer_load_dwordx4 v48, s[44:47], s55 offen lds\n"
    "v_mfma_f32_16x16x32_bf16 v[16:19], v[76:79], a[144:147], 0\n"
    "v_mfma_f32_16x16x32_bf16 v[20:23], v[76:79], a[148:151], 0\n"
    "v_mfma_f32_16x16x32_bf16 v[16:19], v[76:79], a[176:179], v[16:19]\n"
    "v_mfma_f32_16x16x32_bf16 v[20:23], v[76:79], a[180:183], v[20:23]\n"
    "v_mfma_f32_16x16x32_bf16 v[16:19], v[76:79], a[208:211], v[16:19]\n"
    "v_mfma_f32_16x16x32_bf16 v[20:23], v[76:79], a[212:215], v[20:23]\n"
    "v_mfma_f32_16x16x32_bf16 v[16:19], v[76:79], a[240:243], v[16:19]\n"
    "v_mfma_f32_16x16x32_bf16 v[20:23], v[76:79], a[244:247], v[20:23]\n"
    "ds_read_b64_tr_b16 a[152:153], v32 offset:32768\n"
    "ds_read_b64_tr_b16 a[156:157], v33 offset:32768\n"
    "ds_read_b64_tr_b16 a[184:185], v34 offset:32768\n"
    "ds_read_b64_tr_b16 a[188:189], v35 offset:32768\n"
    "ds_read_b64_tr_b16 a[216:217], v36 offset:32768\n"
    "ds_read_b64_tr_b16 a[220:221], v37 offset:32768\n"
    "ds_read_b64_tr_b16 a[248:249], v38 offset:32768\n"
    "ds_read_b64_tr_b16 a[252:253], v39 offset:32768\n"
    "s_waitcnt vmcnt(28) lgkmcnt(0)\n"
    "s_barrier\n"
    "s_add_u32 m0, s54, 32768\n"
    "s_add_u32 s55, %6, 262144\n"
    "buffer_load_dwordx4 v48, s[44:47], s55 offen lds\n"
    "s_add_u32 m0, s54, 36864\n"
    "s_add_u32 s55, %6, 1310720\n"
    "buffer_load_dwordx4 v48, s[44:47], s55 offen lds\n"
    "s_add_u32 m0, s54, 40960\n"
    "s_add_u32 s55, %6, 2359296\n"
    "buffer_load_dwordx4 v48, s[44:47], s55 offen lds\n"
    "s_add_u32 m0, s54, 45056\n"
    "s_add_u32 s55, %6, 3407872\n"
    "buffer_load_dwordx4 v48, s[44:47], s55 offen lds\n"
    "ds_read_b64_tr_b16 a[154:155], v32 offset:49152\n"
    "ds_read_b64_tr_b16 a[158:159], v33 offset:49152\n"
    "ds_read_b64_tr_b16 a[186:187], v34 offset:49152\n"
    "ds_read_b64_tr_b16 a[190:191], v35 offset:49152\n"
    "ds_read_b64_tr_b16 a[218:219], v36 offset:49152\n"
    "ds_read_b64_tr_b16 a[222:223], v37 offset:49152\n"
    "ds_read_b64_tr_b16 a[250:251], v38 offset:49152\n"
    "ds_read_b64_tr_b16 a[254:255], v39 offset:49152\n"
    "s_waitcnt vmcnt(28) lgkmcnt(0)\n"
    "s_barrier\n"
    "s_add_u32 m0, s54, 49152\n"
    "s_add_u32 s55, %6, 294912\n"
    "buffer_load_dwordx4 v48, s[44:47], s55 offen lds\n"
    "s_add_u32 m0, s54, 53248\n"
    "s_add_u32 s55, %6, 1343488\n"
    "buffer_load_dwordx4 v48, s[44:47], s55 offen lds\n"
    "s_add_u32 m0, s54, 57344\n"
    "s_add_u32 s55, %6, 2392064\n"
    "buffer_load_dwordx4 v48, s[44:47], s55 offen lds\n"
    "s_add_u32 m0, s54, 61440\n"
    "s_add_u32 s55, %6, 3440640\n"
    "buffer_load_dwordx4 v48, s[44:47], s55 offen lds\n"
    "v_mfma_f32_16x16x32_bf16 v[16:19], v[76:79], a[152:155], v[16:19]\n"
    "v_mfma_f32_16x16x32_bf16 v[20:23], v[76:79], a[156:159], v[20:23]\n"
    "v_mfma_f32_16x16x32_bf16 v[16:19], v[76:79], a[184:187], v[16:19]\n"
    "v_mfma_f32_16x16x32_bf16 v[20:23], v[76:79], a[188:191], v[20:23]\n"
    "v_mfma_f32_16x16x32_bf16 v[16:19], v[76:79], a[216:219], v[16:19]\n"
    "v_mfma_f32_16x16x32_bf16 v[20:23], v[76:79], a[220:223], v[20:23]\n"
    "v_mfma_f32_16x16x32_bf16 v[16:19], v[76:79], a[248:251], v[16:19]\n"
    "v_mfma_f32_16x16x32_bf16 v[20:23], v[76:79], a[252:255], v[20:23]\n"
    "ds_read_b64_tr_b16 a[160:161], v71\n"
    "ds_read_b64_tr_b16 a[164:165], v70\n"
    "ds_read_b64_tr_b16 a[192:193], v69\n"
    "ds_read_b64_tr_b16 a[196:197], v68\n"
    "ds_read_b64_tr_b16 a[224:225], v67\n"
    "ds_read_b64_tr_b16 a[228:229], v66\n"
    "ds_read_b64_tr_b16 v[84:85], v65\n"
    "ds_read_b64_tr_b16 v[88:89], v64\n"
    "s_waitcnt vmcnt(28) lgkmcnt(0)\n"
    "s_barrier\n"
    "s_add_u32 m0, s54, 65536\n"
    "s_add_u32 s55, %6, 524288\n"
    "buffer_load_dwordx4 v48, s[44:47], s55 offen lds\n"
    "s_add_u32 m0, s54, 69632\n"
    "s_add_u32 s55, %6, 1572864\n"
    "buffer_load_dwordx4 v48, s[44:47], s55 offen lds\n"
    "s_add_u32 m0, s54, 73728\n"
    "s_add_u32 s55, %6, 2621440\n"
    "buffer_load_dwordx4 v48, s[44:47], s55 offen lds\n"
    "s_add_u32 m0, s54, 77824\n"
    "s_add_u32 s55, %6, 3670016\n"
    "buffer_load_dwordx4 v48, s[44:47], s55 offen lds\n"
    "ds_read_b64_tr_b16 a[162:163], v71 offset:16384\n"
    "ds_read_b64_tr_b16 a[166:167], v70 offset:16384\n"
    "ds_read_b64_tr_b16 a[194:195], v69 offset:16384\n"
    "ds_read_b64_tr_b16 a[198:199], v68 offset:16384\n"
    "ds_read_b64_tr_b16 a[226:227], v67 offset:16384\n"
    "ds_read_b64_tr_b16 a[230:231], v66 offset:16384\n"
    "ds_read_b64_tr_b16 v[86:87], v65 offset:16384\n"
    "ds_read_b64_tr_b16 v[90:91], v64 offset:16384\n"
    "s_waitcnt vmcnt(28) lgkmcnt(0)\n"
    "s_barrier\n"
    "s_add_u32 m0, s54, 81920\n"
    "s_add_u32 s55, %6, 557056\n"
    "buffer_load_dwordx4 v48, s[44:47], s55 offen lds\n"
    "s_add_u32 m0, s54, 86016\n"
    "s_add_u32 s55, %6, 1605632\n"
    "buffer_load_dwordx4 v48, s[44:47], s55 offen lds\n"
    "s_add_u32 m0, s54, 90112\n"
    "s_add_u32 s55, %6, 2654208\n"
    "buffer_load_dwordx4 v48, s[44:47], s55 offen lds\n"
    "s_add_u32 m0, s54, 94208\n"
    "s_add_u32 s55, %6, 3702784\n"
    "buffer_load_dwordx4 v48, s[44:47], s55 offen lds\n"
    "v_mfma_f32_16x16x32_bf16 v[16:19], v[76:79], a[160:163], v[16:19]\n"
    "v_mfma_f32_16x16x32_bf16 v[20:23], v[76:79], a[164:167], v[20:23]\n"
    "v_mfma_f32_16x16x32_bf16 v[16:19], v[76:79], a[192:195], v[16:19]\n"
    "v_mfma_f32_16x16x32_bf16 v[20:23], v[76:79], a[196:199], v[20:23]\n"
    "v_mfma_f32_16x16x32_bf16 v[16:19], v[76:79], a[224:227], v[16:19]\n"
    "v_mfma_f32_16x16x32_bf16 v[20:23], v[76:79], a[228:231], v[20:23]\n"
    "v_mfma_f32_16x16x32_bf16 v[16:19], v[76:79], v[84:87], v[16:19]\n"
    "v_mfma_f32_16x16x32_bf16 v[20:23], v[76:79], v[88:91], v[20:23]\n"
    "ds_read_b64_tr_b16 a[168:169], v71 offset:32768\n"
    "ds_read_b64_tr_b16 a[172:173], v70 offset:32768\n"
    "ds_read_b64_tr_b16 a[200:201], v69 offset:32768\n"
    "ds_read_b64_tr_b16 a[204:205], v68 offset:32768\n"
    "ds_read_b64_tr_b16 a[232:233], v67 offset:32768\n"
    "ds_read_b64_tr_b16 a[236:237], v66 offset:32768\n"
    "ds_read_b64_tr_b16 v[92:93], v65 offset:32768\n"
    "ds_read_b64_tr_b16 v[96:97], v64 offset:32768\n"
    "s_waitcnt vmcnt(28) lgkmcnt(0)\n"
    "s_barrier\n"
    "s_add_u32 m0, s54, 98304\n"
    "s_add_u32 s55, %6, 786432\n"
    "buffer_load_dwordx4 v48, s[44:47], s55 offen lds\n"
    "s_add_u32 m0, s54, 102400\n"
    "s_add_u32 s55, %6, 1835008\n"
    "buffer_load_dwordx4 v48, s[44:47], s55 offen lds\n"
    "s_add_u32 m0, s54, 106496\n"
    "s_add_u32 s55, %6, 2883584\n"
    "buffer_load_dwordx4 v48, s[44:47], s55 offen lds\n"
    "s_add_u32 m0, s54, 110592\n"
    "s_add_u32 s55, %6, 3932160\n"
    "buffer_load_dwordx4 v48, s[44:47], s55 offen lds\n"
    "ds_read_b64_tr_b16 a[170:171], v71 offset:49152\n"
    "ds_read_b64_tr_b16 a[174:175], v70 offset:49152\n"
    "ds_read_b64_tr_b16 a[202:203], v69 offset:49152\n"
    "ds_read_b64_tr_b16 a[206:207], v68 offset:49152\n"
    "ds_read_b64_tr_b16 a[234:235], v67 offset:49152\n"
    "ds_read_b64_tr_b16 a[238:239], v66 offset:49152\n"
    "ds_read_b64_tr_b16 v[94:95], v65 offset:49152\n"
    "ds_read_b64_tr_b16 v[98:99], v64 offset:49152\n"
    "s_waitcnt vmcnt(28) lgkmcnt(0)\n"
    "s_barrier\n"
    "s_add_u32 m0, s54, 114688\n"
    "s_add_u32 s55, %6, 819200\n"
    "buffer_load_dwordx4 v48, s[44:47], s55 offen lds\n"
    "s_add_u32 m0, s54, 118784\n"
    "s_add_u32 s55, %6, 1867776\n"
    "buffer_load_dwordx4 v48, s[44:47], s55 offen lds\n"
    "s_add_u32 m0, s54, 122880\n"
    "s_add_u32 s55, %6, 2916352\n"
    "buffer_load_dwordx4 v48, s[44:47], s55 offen lds\n"
    "s_add_u32 m0, s54, 126976\n"
    "s_add_u32 s55, %6, 3964928\n"
    "buffer_load_dwordx4 v48, s[44:47], s55 offen lds\n"
    "v_mfma_f32_16x16x32_bf16 v[16:19], v[76:79], a[168:171], v[16:19]\n"
    "v_mfma_f32_16x16x32_bf16 v[20:23], v[76:79], a[172:175], v[20:23]\n"
    "v_mfma_f32_16x16x32_bf16 v[16:19], v[76:79], a[200:203], v[16:19]\n"
    "v_mfma_f32_16x16x32_bf16 v[20:23], v[76:79], a[204:207], v[20:23]\n"
    "v_mfma_f32_16x16x32_bf16 v[16:19], v[76:79], a[232:235], v[16:19]\n"
    "v_mfma_f32_16x16x32_bf16 v[20:23], v[76:79], a[236:239], v[20:23]\n"
    "v_mfma_f32_16x16x32_bf16 v[16:19], v[76:79], v[92:95], v[16:19]\n"
    "v_mfma_f32_16x16x32_bf16 v[20:23], v[76:79], v[96:99], v[20:23]\n"
    "ds_read_b64_tr_b16 v[100:101], v63\n"
    "ds_read_b64_tr_b16 v[104:105], v62\n"
    "ds_read_b64_tr_b16 v[102:103], v63 offset:4096\n"
    "ds_read_b64_tr_b16 v[106:107], v62 offset:4096\n"
    "ds_read_b64_tr_b16 v[108:109], v63 offset:8192\n"
    "ds_read_b64_tr_b16 v[112:113], v62 offset:8192\n"
    "ds_read_b64_tr_b16 v[110:111], v63 offset:12288\n"
    "ds_read_b64_tr_b16 v[114:115], v62 offset:12288\n"
    "s_waitcnt vmcnt(28) lgkmcnt(0)\n"
    "s_barrier\n"
    "s_add_u32 m0, s54, 131072\n"
    "s_add_u32 s55, %6, 4194304\n"
    "buffer_load_dwordx4 v48, s[44:47], s55 offen lds\n"
    "s_add_u32 m0, s54, 135168\n"
    "s_add_u32 s55, %6, 4227072\n"
    "buffer_load_dwordx4 v48, s[44:47], s55 offen lds\n"
    "s_add_u32 m0, s54, 139264\n"
    "s_add_u32 s55, %6, 4456448\n"
    "buffer_load_dwordx4 v48, s[44:47], s55 offen lds\n"
    "s_add_u32 m0, s54, 143360\n"
    "s_add_u32 s55, %6, 4489216\n"
    "buffer_load_dwordx4 v48, s[44:47], s55 offen lds\n"
    "v_mfma_f32_16x16x32_bf16 v[16:19], v[76:79], v[100:103], v[16:19]\n"
    "v_mfma_f32_16x16x32_bf16 v[20:23], v[76:79], v[104:107], v[20:23]\n"
    "v_mfma_f32_16x16x32_bf16 v[16:19], v[76:79], v[108:111], v[16:19]\n"
    "v_mfma_f32_16x16x32_bf16 v[20:23], v[76:79], v[112:115], v[20:23]\n"
    "ds_read_b64_tr_b16 v[116:117], v32\n"
    "ds_read_b64_tr_b16 v[120:121], v33\n"
    "ds_read_b64_tr_b16 v[148:149], v34\n"
    "ds_read_b64_tr_b16 v[152:153], v35\n"
    "ds_read_b64_tr_b16 v[180:181], v36\n"
    "ds_read_b64_tr_b16 v[184:185], v37\n"
    "ds_read_b64_tr_b16 v[212:213], v38\n"
    "ds_read_b64_tr_b16 v[216:217], v39\n"
    "s_waitcnt vmcnt(28) lgkmcnt(0)\n"
    "s_barrier\n"
    "ds_read_b64_tr_b16 v[118:119], v32 offset:16384\n"
    "ds_read_b64_tr_b16 v[122:123], v33 offset:16384\n"
    "ds_read_b64_tr_b16 v[150:151], v34 offset:16384\n"
    "ds_read_b64_tr_b16 v[154:155], v35 offset:16384\n"
    "ds_read_b64_tr_b16 v[182:183], v36 offset:16384\n"
    "ds_read_b64_tr_b16 v[186:187], v37 offset:16384\n"
    "ds_read_b64_tr_b16 v[214:215], v38 offset:16384\n"
    "ds_read_b64_tr_b16 v[218:219], v39 offset:16384\n"
    "s_waitcnt vmcnt(24) lgkmcnt(0)\n"
    "s_barrier\n"
    "v_mfma_f32_16x16x32_bf16 v[24:27], v[76:79], v[116:119], 0\n"
    "v_mfma_f32_16x16x32_bf16 v[28:31], v[76:79], v[120:123], 0\n"
    "v_mfma_f32_16x16x32_bf16 v[24:27], v[76:79], v[148:151], v[24:27]\n"
    "v_mfma_f32_16x16x32_bf16 v[28:31], v[76:79], v[152:155], v[28:31]\n"
    "v_mfma_f32_16x16x32_bf16 v[24:27], v[76:79], v[180:183], v[24:27]\n"
    "v_mfma_f32_16x16x32_bf16 v[28:31], v[76:79], v[184:187], v[28:31]\n"
    "v_mfma_f32_16x16x32_bf16 v[24:27], v[76:79], v[212:215], v[24:27]\n"
    "v_mfma_f32_16x16x32_bf16 v[28:31], v[76:79], v[216:219], v[28:31]\n"
    "ds_read_b64_tr_b16 v[124:125], v32 offset:32768\n"
    "ds_read_b64_tr_b16 v[128:129], v33 offset:32768\n"
    "ds_read_b64_tr_b16 v[156:157], v34 offset:32768\n"
    "ds_read_b64_tr_b16 v[160:161], v35 offset:32768\n"
    "ds_read_b64_tr_b16 v[188:189], v36 offset:32768\n"
    "ds_read_b64_tr_b16 v[192:193], v37 offset:32768\n"
    "ds_read_b64_tr_b16 v[220:221], v38 offset:32768\n"
    "ds_read_b64_tr_b16 v[224:225], v39 offset:32768\n"
    "s_waitcnt vmcnt(20) lgkmcnt(0)\n"
    "s_barrier\n"
    "ds_read_b64_tr_b16 v[126:127], v32 offset:49152\n"
    "ds_read_b64_tr_b16 v[130:131], v33 offset:49152\n"
    "ds_read_b64_tr_b16 v[158:159], v34 offset:49152\n"
    "ds_read_b64_tr_b16 v[162:163], v35 offset:49152\n"
    "ds_read_b64_tr_b16 v[190:191], v36 offset:49152\n"
    "ds_read_b64_tr_b16 v[194:195], v37 offset:49152\n"
    "ds_read_b64_tr_b16 v[222:223], v38 offset:49152\n"
    "ds_read_b64_tr_b16 v[226:227], v39 offset:49152\n"
    "s_waitcnt vmcnt(16) lgkmcnt(0)\n"
    "s_barrier\n"
    "v_mfma_f32_16x16x32_bf16 v[24:27], v[76:79], v[124:127], v[24:27]\n"
    "v_mfma_f32_16x16x32_bf16 v[28:31], v[76:79], v[128:131], v[28:31]\n"
    "v_mfma_f32_16x16x32_bf16 v[24:27], v[76:79], v[156:159], v[24:27]\n"
    "v_mfma_f32_16x16x32_bf16 v[28:31], v[76:79], v[160:163], v[28:31]\n"
    "v_mfma_f32_16x16x32_bf16 v[24:27], v[76:79], v[188:191], v[24:27]\n"
    "v_mfma_f32_16x16x32_bf16 v[28:31], v[76:79], v[192:195], v[28:31]\n"
    "v_mfma_f32_16x16x32_bf16 v[24:27], v[76:79], v[220:223], v[24:27]\n"
    "v_mfma_f32_16x16x32_bf16 v[28:31], v[76:79], v[224:227], v[28:31]\n"
    "ds_read_b64_tr_b16 v[132:133], v71\n"
    "ds_read_b64_tr_b16 v[136:137], v70\n"
    "ds_read_b64_tr_b16 v[164:165], v69\n"
    "ds_read_b64_tr_b16 v[168:169], v68\n"
    "ds_read_b64_tr_b16 v[196:197], v67\n"
    "ds_read_b64_tr_b16 v[200:201], v66\n"
    "ds_read_b64_tr_b16 v[228:229], v65\n"
    "ds_read_b64_tr_b16 v[232:233], v64\n"
    "s_waitcnt vmcnt(12) lgkmcnt(0)\n"
    "s_barrier\n"
    "ds_read_b64_tr_b16 v[134:135], v71 offset:16384\n"
    "ds_read_b64_tr_b16 v[138:139], v70 offset:16384\n"
    "ds_read_b64_tr_b16 v[166:167], v69 offset:16384\n"
    "ds_read_b64_tr_b16 v[170:171], v68 offset:16384\n"
    "ds_read_b64_tr_b16 v[198:199], v67 offset:16384\n"
    "ds_read_b64_tr_b16 v[202:203], v66 offset:16384\n"
    "ds_read_b64_tr_b16 v[230:231], v65 offset:16384\n"
    "ds_read_b64_tr_b16 v[234:235], v64 offset:16384\n"
    "s_waitcnt vmcnt(8) lgkmcnt(0)\n"
    "s_barrier\n"
    "v_mfma_f32_16x16x32_bf16 v[24:27], v[76:79], v[132:135], v[24:27]\n"
    "v_mfma_f32_16x16x32_bf16 v[28:31], v[76:79], v[136:139], v[28:31]\n"
    "v_mfma_f32_16x16x32_bf16 v[24:27], v[76:79], v[164:167], v[24:27]\n"
    "v_mfma_f32_16x16x32_bf16 v[28:31], v[76:79], v[168:171], v[28:31]\n"
    "v_mfma_f32_16x16x32_bf16 v[24:27], v[76:79], v[196:199], v[24:27]\n"
    "v_mfma_f32_16x16x32_bf16 v[28:31], v[76:79], v[200:203], v[28:31]\n"
    "v_mfma_f32_16x16x32_bf16 v[24:27], v[76:79], v[228:231], v[24:27]\n"
    "v_mfma_f32_16x16x32_bf16 v[28:31], v[76:79], v[232:235], v[28:31]\n"
    "ds_read_b64_tr_b16 v[140:141], v71 offset:32768\n"
    "ds_read_b64_tr_b16 v[144:145], v70 offset:32768\n"
    "ds_read_b64_tr_b16 v[172:173], v69 offset:32768\n"
    "ds_read_b64_tr_b16 v[176:177], v68 offset:32768\n"
    "ds_read_b64_tr_b16 v[204:205], v67 offset:32768\n"
    "ds_read_b64_tr_b16 v[208:209], v66 offset:32768\n"
    "ds_read_b64_tr_b16 v[236:237], v65 offset:32768\n"
    "ds_read_b64_tr_b16 v[240:241], v64 offset:32768\n"
    "s_waitcnt vmcnt(4) lgkmcnt(0)\n"
    "s_barrier\n"
    "ds_read_b64_tr_b16 v[142:143], v71 offset:49152\n"
    "ds_read_b64_tr_b16 v[146:147], v70 offset:49152\n"
    "ds_read_b64_tr_b16 v[174:175], v69 offset:49152\n"
    "ds_read_b64_tr_b16 v[178:179], v68 offset:49152\n"
    "ds_read_b64_tr_b16 v[206:207], v67 offset:49152\n"
    "ds_read_b64_tr_b16 v[210:211], v66 offset:49152\n"
    "ds_read_b64_tr_b16 v[238:239], v65 offset:49152\n"
    "ds_read_b64_tr_b16 v[242:243], v64 offset:49152\n"
    "s_waitcnt vmcnt(0) lgkmcnt(0)\n"
    "s_barrier\n"
    "v_mfma_f32_16x16x32_bf16 v[24:27], v[76:79], v[140:143], v[24:27]\n"
    "v_mfma_f32_16x16x32_bf16 v[28:31], v[76:79], v[144:147], v[28:31]\n"
    "v_mfma_f32_16x16x32_bf16 v[24:27], v[76:79], v[172:175], v[24:27]\n"
    "v_mfma_f32_16x16x32_bf16 v[28:31], v[76:79], v[176:179], v[28:31]\n"
    "v_mfma_f32_16x16x32_bf16 v[24:27], v[76:79], v[204:207], v[24:27]\n"
    "v_mfma_f32_16x16x32_bf16 v[28:31], v[76:79], v[208:211], v[28:31]\n"
    "v_mfma_f32_16x16x32_bf16 v[24:27], v[76:79], v[236:239], v[24:27]\n"
    "v_mfma_f32_16x16x32_bf16 v[28:31], v[76:79], v[240:243], v[28:31]\n"
    "ds_read_b64_tr_b16 v[244:245], v63\n"
    "ds_read_b64_tr_b16 v[248:249], v62\n"
    "ds_read_b64_tr_b16 v[246:247], v63 offset:4096\n"
    "ds_read_b64_tr_b16 v[250:251], v62 offset:4096\n"
    "ds_read_b64_tr_b16 v[252:253], v63 offset:8192\n"
    "ds_read_b64_tr_b16 v[72:73], v62 offset:8192\n"
    "ds_read_b64_tr_b16 v[254:255], v63 offset:12288\n"
    "ds_read_b64_tr_b16 v[74:75], v62 offset:12288\n"
    "s_waitcnt lgkmcnt(0)\n"
    "s_barrier\n"
    "v_mfma_f32_16x16x32_bf16 v[24:27], v[76:79], v[244:247], v[24:27]\n"
    "v_mfma_f32_16x16x32_bf16 v[28:31], v[76:79], v[248:251], v[28:31]\n"
    "v_mfma_f32_16x16x32_bf16 v[24:27], v[76:79], v[252:255], v[24:27]\n"
    "v_mfma_f32_16x16x32_bf16 v[28:31], v[76:79], v[72:75], v[28:31]\n"
    "ds_write_b128 v2, v[72:75] offset:20480\n"
    "v_cvt_pk_bf16_f32 v9, 0, v8\n"
    "v_sub_f32 v8, v8, v9\n"
    "v_cvt_pk_bf16_f32 v10, 0, v8\n"
    "v_sub_f32 v8, v8, v10\n"
    "v_cvt_pk_bf16_f32 v9, v9, v10\n"
    "v_cvt_pk_bf16_f32 v10, v8, 0\n"
    "s_mov_b64 vcc, 0xffff\n"
    "v_cndmask_b32 v32, 0, v9, vcc\n"
    "v_cndmask_b32 v33, 0, v10, vcc\n"
    "v_cvt_pk_bf16_f32 v13, 0, v12\n"
    "v_sub_f32 v12, v12, v13\n"
    "v_cvt_pk_bf16_f32 v14, 0, v12\n"
    "v_sub_f32 v12, v12, v14\n"
    "v_cvt_pk_bf16_f32 v13, v13, v14\n"
    "v_cvt_pk_bf16_f32 v14, v12, 0\n"
    "s_mov_b64 vcc, 0xffff\n"
    "v_cndmask_b32 v34, 0, v13, vcc\n"
    "v_cndmask_b32 v35, 0, v14, vcc\n"
    "ds_write_b128 v2, v[32:35] offset:8192\n"
    "v_cvt_pk_bf16_f32 v17, 0, v16\n"
    "v_sub_f32 v16, v16, v17\n"
    "v_cvt_pk_bf16_f32 v18, 0, v16\n"
    "v_sub_f32 v16, v16, v18\n"
    "v_cvt_pk_bf16_f32 v17, v17, v18\n"
    "v_cvt_pk_bf16_f32 v18, v16, 0\n"
    "s_mov_b64 vcc, 0xffff\n"
    "v_cndmask_b32 v36, 0, v17, vcc\n"
    "v_cndmask_b32 v37, 0, v18, vcc\n"
    "v_cvt_pk_bf16_f32 v21, 0, v20\n"
    "v_sub_f32 v20, v20, v21\n"
    "v_cvt_pk_bf16_f32 v22, 0, v20\n"
    "v_sub_f32 v20, v20, v22\n"
    "v_cvt_pk_bf16_f32 v21, v21, v22\n"
    "v_cvt_pk_bf16_f32 v22, v20, 0\n"
    "s_mov_b64 vcc, 0xffff\n"
    "v_cndmask_b32 v38, 0, v21, vcc\n"
    "v_cndmask_b32 v39, 0, v22, vcc\n"
    "ds_write_b128 v2, v[36:39] offset:12288\n"
    "v_cvt_pk_bf16_f32 v25, 0, v24\n"
    "v_sub_f32 v24, v24, v25\n"
    "v_cvt_pk_bf16_f32 v26, 0, v24\n"
    "v_sub_f32 v24, v24, v26\n"
    "v_cvt_pk_bf16_f32 v25, v25, v26\n"
    "v_cvt_pk_bf16_f32 v26, v24, 0\n"
    "s_mov_b64 vcc, 0xffff\n"
    "v_cndmask_b32 v40, 0, v25, vcc\n"
    "v_cndmask_b32 v41, 0, v26, vcc\n"
    "v_cvt_pk_bf16_f32 v29, 0, v28\n"
    "v_sub_f32 v28, v28, v29\n"
    "v_cvt_pk_bf16_f32 v30, 0, v28\n"
    "v_sub_f32 v28, v28, v30\n"
    "v_cvt_pk_bf16_f32 v29, v29, v30\n"
    "v_cvt_pk_bf16_f32 v30, v28, 0\n"
    "s_mov_b64 vcc, 0xffff\n"
    "v_cndmask_b32 v42, 0, v29, vcc\n"
    "v_cndmask_b32 v43, 0, v30, vcc\n"
    "ds_write_b128 v2, v[40:43] offset:16384\n"
    "v_mbcnt_lo_u32_b32 v80, -1, 0\n"
    "v_mbcnt_hi_u32_b32 v80, -1, v80\n"
    "v_min_u32 v4, 5, v80\n"
    "v_lshlrev_b32 v4, 7, v4\n"
    "v_mov_b32 v7, 0x26c00\n"
    "ds_read_b128 v[80:83], v7\n"
    "s_waitcnt lgkmcnt(0)\n"
    "v_readfirstlane_b32 s49, v81\n"
    "v_add_u32 v80, v80, v4\n"
    "s_branch .Lhchk_%=\n"
    ".Lhgo_%=:\n"
    "s_waitcnt vmcnt(2)\n"
    "v_mfma_f32_16x16x32_bf16 v[68:71], v[48:51], v[48:51], 0\n"
    "v_add_u32 v7, 32, v7\n"
    "ds_read_b128 v[80:83], v7\n"
    "v_mfma_f32_16x16x32_bf16 v[68:71], v[52:55], v[52:55], v[68:71]\n"
    "ds_write_b128 v1, v[48:51]\n"
    "ds_write_b128 v1, v[52:55] offset:1024\n"
    "v_mfma_f32_16x16x32_bf16 v[68:71], v[56:59], v[56:59], v[68:71]\n"
    "ds_write_b128 v1, v[56:59] offset:2048\n"
    "ds_write_b128 v1, v[60:63] offset:3072\n"
    "v_mfma_f32_16x16x32_bf16 v[68:71], v[60:63], v[60:63], v[68:71]\n"
    "v_mul_f32 v24, v24, v76\n"
    "v_mul_f32 v25, v25, v77\n"
    "v_mfma_f32_16x16x32_bf16 v[68:71], v[64:67], v[64:67], v[68:71]\n"
    "v_mul_f32 v26, v26, v78\n"
    "v_mul_f32 v27, v27, v79\n"
    "v_mfma_f32_16x16x32_bf16 v[8:11], a[0:3], v[48:51], 0\n"
    "v_mul_f32 v28, v28, v76\n"
    "v_mul_f32 v29, v29, v77\n"
    "v_mfma_f32_16x16x32_bf16 v[12:15], a[4:7], v[48:51], 0\n"
    "v_mul_f32 v30, v30, v78\n"
    "v_mul_f32 v31, v31, v79\n"
    "v_mfma_f32_16x16x32_bf16 v[16:19], a[144:147], v[48:51], 0\n"
    "v_mov_b64 v[76:77], s[86:87]\n"
    "v_mov_b64 v[78:79], s[86:87]\n"
    "v_mfma_f32_16x16x32_bf16 v[20:23], a[148:151], v[48:51], 0\n"
    "ds_write_b128 v1, v[64:67] offset:4096\n"
    "v_cvt_pk_bf16_f32 v24, v24, v25\n"
    "v_mfma_f32_16x16x32_bf16 v[72:75], v[76:79], v[48:51], 0\n"
    "v_cvt_pk_bf16_f32 v25, v26, v27\n"
    "v_cvt_pk_bf16_f32 v26, v28, v29\n"
    "v_mfma_f32_16x16x32_bf16 v[72:75], v[76:79], v[52:55], v[72:75]\n"
    "v_cvt_pk_bf16_f32 v27, v30, v31\n"
    "buffer_store_dwordx4 v[24:27], v2, s[40:43], s64 offen sc1\n"
    "v_mfma_f32_16x16x32_bf16 v[72:75], v[76:79], v[56:59], v[72:75]\n"
    "ds_write_b128 v1, v[68:71] offset:5120\n"
    "s_waitcnt lgkmcnt(6)\n"
    "v_readfirstlane_b32 s49, v81\n"
    "v_mfma_f32_16x16x32_bf16 v[72:75], v[76:79], v[60:63], v[72:75]\n"
    "v_readfirstlane_b32 s62, v82\n"
    "v_add_u32 v80, v80, v4\n"
    "v_mfma_f32_16x16x32_bf16 v[72:75], v[76:79], v[64:67], v[72:75]\n"
    ".Lhtop_%=:\n"
    "v_mfma_f32_16x16x32_bf16 v[24:27], v[48:51], v[116:119], 0\n"
    "global_load_dword v81, v80, %1 sc1\n"
    "s_add_u32 s63, s62, 0x2000000\n"
    "v_mfma_f32_16x16x32_bf16 v[28:31], v[48:51], v[120:123], 0\n"
    "s_add_u32 s64, s62, 0x4000000\n"
    "v_mfma_f32_16x16x32_bf16 v[8:11], a[8:11], v[52:55], v[8:11]\n"
    "ds_write_b128 v1, v[72:75] offset:6144\n"
    "v_xor_b32 v0, 0x2000, v1\n"
    "v_mfma_f32_16x16x32_bf16 v[12:15], a[12:15], v[52:55], v[12:15]\n"
    "v_mfma_f32_16x16x32_bf16 v[16:19], a[152:155], v[52:55], v[16:19]\n"
    "v_mfma_f32_16x16x32_bf16 v[20:23], a[156:159], v[52:55], v[20:23]\n"
    "v_mfma_f32_16x16x32_bf16 v[24:27], v[52:55], v[124:127], v[24:27]\n"
    "s_waitcnt lgkmcnt(0)\n"
    "s_barrier\n"
    "v_mfma_f32_16x16x32_bf16 v[28:31], v[52:55], v[128:131], v[28:31]\n"
    "ds_read_b128 v[32:35], v0\n"
    "ds_read_b128 v[36:39], v0 offset:1024\n"
    "v_mfma_f32_16x16x32_bf16 v[8:11], a[16:19], v[56:59], v[8:11]\n"
    "ds_read2st64_b32 v[68:69], v6 offset0:20 offset1:52\n"
    "ds_read2st64_b32 v[70:71], v6 offset0:84 offset1:116\n"
    "v_mfma_f32_16x16x32_bf16 v[12:15], a[20:23], v[56:59], v[12:15]\n"
    "ds_read2st64_b32 v[74:75], v6 offset0:88 offset1:120\n"
    "ds_read2st64_b32 v[72:73], v6 offset0:24 offset1:56\n"
    "v_mfma_f32_16x16x32_bf16 v[16:19], a[160:163], v[56:59], v[16:19]\n"
    "v_mov_b64 v[64:65], 0\n"
    "v_mfma_f32_16x16x32_bf16 v[20:23], a[164:167], v[56:59], v[20:23]\n"
    "v_mfma_f32_16x16x32_bf16 v[24:27], v[56:59], v[132:135], v[24:27]\n"
    "v_mfma_f32_16x16x32_bf16 v[28:31], v[56:59], v[136:139], v[28:31]\n"
    "ds_read_b128 v[48:51], v2 offset:8192\n"
    "ds_read_b128 v[52:55], v2 offset:12288\n"
    "v_mfma_f32_16x16x32_bf16 v[8:11], a[24:27], v[60:63], v[8:11]\n"
    "ds_read_b128 v[56:59], v2 offset:16384\n"
    "ds_read_b128 v[44:47], v0 offset:3072\n"
    "v_mfma_f32_16x16x32_bf16 v[12:15], a[28:31], v[60:63], v[12:15]\n"
    "ds_read_b128 v[40:43], v0 offset:2048\n"
    "v_mfma_f32_16x16x32_bf16 v[16:19], a[168:171], v[60:63], v[16:19]\n"
    "s_waitcnt lgkmcnt(5)\n"
    "v_add_f32 v72, v72, v73\n"
    "v_add_f32 v74, v74, v75\n"
    "v_mfma_f32_16x16x32_bf16 v[20:23], a[172:175], v[60:63], v[20:23]\n"
    "v_add_f32 v68, v68, v69\n"
    "v_add_f32 v70, v70, v71\n"
    "v_mfma_f32_16x16x32_bf16 v[24:27], v[60:63], v[140:143], v[24:27]\n"
    "v_add_f32 v72, v72, v74\n"
    "v_add_f32 v70, v68, v70\n"
    "v_mfma_f32_16x16x32_bf16 v[28:31], v[60:63], v[144:147], v[28:31]\n"
    "v_mul_f32 v72, 0xbae38e39, v72\n"
    "v_mul_f32 v70, 0x3ae38e39, v70\n"
    "v_mfma_f32_16x16x32_bf16 v[8:11], a[32:35], v[32:35], v[8:11]\n"
    "v_cvt_pk_bf16_f32 v62, v72, v72\n"
    "v_fma_f32 v70, -v72, v72, v70\n"
    "v_mfma_f32_16x16x32_bf16 v[12:15], a[36:39], v[32:35], v[12:15]\n"
    "v_cvt_pk_bf16_f32 v63, v72, v72\n"
    "v_max_f32 v70, 0, v70\n"
    "v_mfma_f32_16x16x32_bf16 v[16:19], a[176:179], v[32:35], v[16:19]\n"
    "v_cvt_pk_bf16_f32 v70, 0, v70\n"
    "v_add_f32 v70, 0x3727c5ac, v70\n"
    "v_mfma_f32_16x16x32_bf16 v[20:23], a[180:183], v[32:35], v[20:23]\n"
    "v_cvt_pk_bf16_f32 v70, 0, v70\n"
    "v_sqrt_f32 v70, v70\n"
    "v_mfma_f32_16x16x32_bf16 v[24:27], v[32:35], v[148:151], v[24:27]\n"
    "v_cvt_pk_bf16_f32 v70, 0, v70\n"
    "v_mfma_f32_16x16x32_bf16 v[28:31], v[32:35], v[152:155], v[28:31]\n"
    "v_rcp_f32 v70, v70\n"
    "v_mfma_f32_16x16x32_bf16 v[8:11], a[40:43], v[36:39], v[8:11]\n"
    "v_mov_b64 v[60:61], 0\n"
    "v_mfma_f32_16x16x32_bf16 v[12:15], a[44:47], v[36:39], v[12:15]\n"
    "ds_write_b32 v5, v70\n"
    "v_lshrrev_b32 v71, 4, v2\n"
    "v_mfma_f32_16x16x32_bf16 v[16:19], a[184:187], v[36:39], v[16:19]\n"
    "v_and_b32 v71, 48, v71\n"
    "ds_read_b128 v[76:79], v71 offset:24576\n"
    "v_mfma_f32_16x16x32_bf16 v[20:23], a[188:191], v[36:39], v[20:23]\n"
    "ds_read_b128 v[72:75], v2 offset:20480\n"
    "v_mfma_f32_16x16x32_bf16 v[24:27], v[36:39], v[156:159], v[24:27]\n"
    "v_xor_b32 v0, 0x4000, v1\n"
    "v_mfma_f32_16x16x32_bf16 v[28:31], v[36:39], v[160:163], v[28:31]\n"
    "s_waitcnt lgkmcnt(3)\n"
    "v_mfma_f32_16x16x32_bf16 v[8:11], a[48:51], v[40:43], v[8:11]\n"
    "ds_read_b128 v[36:39], v0 offset:1024\n"
    "ds_read_b128 v[32:35], v0\n"
    "v_mfma_f32_16x16x32_bf16 v[12:15], a[52:55], v[40:43], v[12:15]\n"
    "v_mfma_f32_16x16x32_bf16 v[16:19], a[192:195], v[40:43], v[16:19]\n"
    "v_mfma_f32_16x16x32_bf16 v[20:23], a[196:199], v[40:43], v[20:23]\n"
    "v_mfma_f32_16x16x32_bf16 v[24:27], v[40:43], v[164:167], v[24:27]\n"
    "v_mfma_f32_16x16x32_bf16 v[28:31], v[40:43], v[168:171], v[28:31]\n"
    "v_mfma_f32_16x16x32_bf16 v[8:11], v[48:51], v[62:65], v[8:11]\n"
    "v_mfma_f32_16x16x32_bf16 v[12:15], v[48:51], v[60:63], v[12:15]\n"
    "v_mfma_f32_16x16x32_bf16 v[16:19], v[52:55], v[62:65], v[16:19]\n"
    "s_waitcnt vmcnt(0)\n"
    "v_mov_b32 v82, 1\n"
    "v_mfma_f32_16x16x32_bf16 v[20:23], v[52:55], v[60:63], v[20:23]\n"
    "s_mov_b64 exec, s[56:57]\n"
    "ds_add_rtn_u32 v82, v7, v82 offset:16\n"
    "s_mov_b64 exec, -1\n"
    "v_mfma_f32_16x16x32_bf16 v[24:27], v[62:65], v[56:59], v[24:27]\n"
    "v_cmp_eq_u32 vcc, 0, v81\n"
    "s_cmp_eq_u64 vcc, 0\n"
    "v_mfma_f32_16x16x32_bf16 v[28:31], v[60:63], v[56:59], v[28:31]\n"
    "s_cselect_b32 s94, s42, 0\n"
    "s_cselect_b32 s90, s96, 0\n"
    "v_mfma_f32_16x16x32_bf16 v[8:11], a[56:59], v[44:47], v[8:11]\n"
    "global_load_dword v81, v80, %1 sc1\n"
    "v_mfma_f32_16x16x32_bf16 v[12:15], a[60:63], v[44:47], v[12:15]\n"
    "buffer_load_dwordx4 v[48:51], v3, s[92:95], s49 offen sc1\n"
    "v_mfma_f32_16x16x32_bf16 v[16:19], a[200:203], v[44:47], v[16:19]\n"
    "buffer_load_dwordx4 v[52:55], v3, s[92:95], s49 offen offset:1024 sc1\n"
    "v_mfma_f32_16x16x32_bf16 v[20:23], a[204:207], v[44:47], v[20:23]\n"
    "s_waitcnt lgkmcnt(0)\n"
    "v_readfirstlane_b32 s50, v82\n"
    "v_mfma_f32_16x16x32_bf16 v[24:27], v[44:47], v[172:175], v[24:27]\n"
    "buffer_load_dwordx4 v[56:59], v3, s[92:95], s49 offen offset:2048 sc1\n"
    "s_cmp_eq_u32 s50, 3\n"
    "v_mfma_f32_16x16x32_bf16 v[28:31], v[44:47], v[176:179], v[28:31]\n"
    "v_mfma_f32_16x16x32_bf16 v[8:11], a[64:67], v[32:35], v[8:11]\n"
    "ds_read_b128 v[44:47], v0 offset:3072\n"
    "ds_read_b128 v[40:43], v0 offset:2048\n"
    "v_mfma_f32_16x16x32_bf16 v[12:15], a[68:71], v[32:35], v[12:15]\n"
    "s_cselect_b64 exec, 1, 0\n"
    "global_store_dword v83, v82, %1 sc1\n"
    "s_mov_b64 exec, -1\n"
    "v_mfma_f32_16x16x32_bf16 v[16:19], a[208:211], v[32:35], v[16:19]\n"
    "buffer_load_dwordx4 v[60:63], v3, s[92:95], s49 offen offset:3072 sc1\n"
    "v_mfma_f32_16x16x32_bf16 v[20:23], a[212:215], v[32:35], v[20:23]\n"
    "buffer_load_dwordx4 v[64:67], v2, s[88:91], s49 offen sc1\n"
    "v_mfma_f32_16x16x32_bf16 v[24:27], v[32:35], v[180:183], v[24:27]\n"
    "v_mfma_f32_16x16x32_bf16 v[28:31], v[32:35], v[184:187], v[28:31]\n"
    "v_mfma_f32_16x16x32_bf16 v[8:11], a[72:75], v[36:39], v[8:11]\n"
    "v_mfma_f32_16x16x32_bf16 v[12:15], a[76:79], v[36:39], v[12:15]\n"
    "v_mfma_f32_16x16x32_bf16 v[16:19], a[216:219], v[36:39], v[16:19]\n"
    "v_mfma_f32_16x16x32_bf16 v[20:23], a[220:223], v[36:39], v[20:23]\n"
    "v_mfma_f32_16x16x32_bf16 v[24:27], v[36:39], v[188:191], v[24:27]\n"
    "v_xor_b32 v0, 0x6000, v1\n"
    "v_mfma_f32_16x16x32_bf16 v[28:31], v[36:39], v[192:195], v[28:31]\n"
    "s_waitcnt lgkmcnt(0)\n"
    "v_mfma_f32_16x16x32_bf16 v[8:11], a[80:83], v[40:43], v[8:11]\n"
    "ds_read_b128 v[36:39], v0 offset:1024\n"
    "ds_read_b128 v[32:35], v0\n"
    "v_mfma_f32_16x16x32_bf16 v[12:15], a[84:87], v[40:43], v[12:15]\n"
    "v_mfma_f32_16x16x32_bf16 v[16:19], a[224:227], v[40:43], v[16:19]\n"
    "v_mfma_f32_16x16x32_bf16 v[20:23], a[228:231], v[40:43], v[20:23]\n"
    "v_mfma_f32_16x16x32_bf16 v[24:27], v[40:43], v[196:199], v[24:27]\n"
    "v_mfma_f32_16x16x32_bf16 v[28:31], v[40:43], v[200:203], v[28:31]\n"
    "v_mfma_f32_16x16x32_bf16 v[8:11], a[88:91], v[44:47], v[8:11]\n"
    "v_mfma_f32_16x16x32_bf16 v[12:15], a[92:95], v[44:47], v[12:15]\n"
    "v_mfma_f32_16x16x32_bf16 v[16:19], a[232:235], v[44:47], v[16:19]\n"
    "v_mfma_f32_16x16x32_bf16 v[20:23], a[236:239], v[44:47], v[20:23]\n"
    "v_mfma_f32_16x16x32_bf16 v[24:27], v[44:47], v[204:207], v[24:27]\n"
    "v_mfma_f32_16x16x32_bf16 v[28:31], v[44:47], v[208:211], v[28:31]\n"
    "s_waitcnt lgkmcnt(0)\n"
    "v_mfma_f32_16x16x32_bf16 v[8:11], a[96:99], v[32:35], v[8:11]\n"
    "ds_read_b128 v[44:47], v0 offset:3072\n"
    "ds_read_b128 v[40:43], v0 offset:2048\n"
    "v_mfma_f32_16x16x32_bf16 v[12:15], a[100:103], v[32:35], v[12:15]\n"
    "v_mfma_f32_16x16x32_bf16 v[16:19], a[240:243], v[32:35], v[16:19]\n"
    "s_cmp_eq_u32 s94, 0\n"
    "s_cbranch_scc1 .Lhk1_%=\n"
    ".Lhk1_x%=:\n"
    "v_mfma_f32_16x16x32_bf16 v[20:23], a[244:247], v[32:35], v[20:23]\n"
    "v_mfma_f32_16x16x32_bf16 v[24:27], v[32:35], v[212:215], v[24:27]\n"
    "v_and_b32 v0, 0xffff9fff, v1\n"
    "v_mfma_f32_16x16x32_bf16 v[28:31], v[32:35], v[216:219], v[28:31]\n"
    "ds_read_b128 v[32:35], v0 offset:4096\n"
    "v_mfma_f32_16x16x32_bf16 v[8:11], a[104:107], v[36:39], v[8:11]\n"
    "v_mfma_f32_16x16x32_bf16 v[12:15], a[108:111], v[36:39], v[12:15]\n"
    "v_mfma_f32_16x16x32_bf16 v[16:19], a[248:251], v[36:39], v[16:19]\n"
    "v_mfma_f32_16x16x32_bf16 v[20:23], a[252:255], v[36:39], v[20:23]\n"
    "v_mfma_f32_16x16x32_bf16 v[24:27], v[36:39], v[220:223], v[24:27]\n"
    "v_mfma_f32_16x16x32_bf16 v[28:31], v[36:39], v[224:227], v[28:31]\n"
    "ds_read_b128 v[36:39], v0 offset:12288\n"
    "s_waitcnt lgkmcnt(2)\n"
    "v_mfma_f32_16x16x32_bf16 v[8:11], a[112:115], v[40:43], v[8:11]\n"
    "v_mfma_f32_16x16x32_bf16 v[12:15], a[116:119], v[40:43], v[12:15]\n"
    "v_xor_b32 v1, 0x8000, v1\n"
    "v_xor_b32 v6, 0x8000, v6\n"
    "v_mfma_f32_16x16x32_bf16 v[8:11], a[120:123], v[44:47], v[8:11]\n"
    "v_mfma_f32_16x16x32_bf16 v[12:15], a[124:127], v[44:47], v[12:15]\n"
    "s_waitcnt lgkmcnt(1)\n"
    "v_mfma_f32_16x16x32_bf16 v[8:11], a[128:131], v[32:35], v[8:11]\n"
    "v_mfma_f32_16x16x32_bf16 v[12:15], a[132:135], v[32:35], v[12:15]\n"
    "s_waitcnt lgkmcnt(0)\n"
    "v_mfma_f32_16x16x32_bf16 v[8:11], a[136:139], v[36:39], v[8:11]\n"
    "v_mfma_f32_16x16x32_bf16 v[12:15], a[140:143], v[36:39], v[12:15]\n"
    "v_mfma_f32_16x16x32_bf16 v[16:19], v[84:87], v[40:43], v[16:19]\n"
    "v_mfma_f32_16x16x32_bf16 v[20:23], v[88:91], v[40:43], v[20:23]\n"
    "v_mul_f32 v8, v8, v70\n"
    "v_mul_f32 v12, v12, v70\n"
    "v_mfma_f32_16x16x32_bf16 v[16:19], v[92:95], v[44:47], v[16:19]\n"
    "v_cvt_pk_bf16_f32 v8, v8, v12\n"
    "v_mul_f32 v9, v9, v70\n"
    "v_mfma_f32_16x16x32_bf16 v[20:23], v[96:99], v[44:47], v[20:23]\n"
    "v_mul_f32 v13, v13, v70\n"
    "v_cvt_pk_bf16_f32 v9, v9, v13\n"
    "v_mfma_f32_16x16x32_bf16 v[16:19], v[100:103], v[32:35], v[16:19]\n"
    "v_mul_f32 v10, v10, v70\n"
    "v_mul_f32 v14, v14, v70\n"
    "v_mfma_f32_16x16x32_bf16 v[20:23], v[104:107], v[32:35], v[20:23]\n"
    "v_cvt_pk_bf16_f32 v10, v10, v14\n"
    "v_mul_f32 v11, v11, v70\n"
    "v_mfma_f32_16x16x32_bf16 v[16:19], v[108:111], v[36:39], v[16:19]\n"
    "v_mul_f32 v15, v15, v70\n"
    "v_cvt_pk_bf16_f32 v11, v11, v15\n"
    "v_mfma_f32_16x16x32_bf16 v[20:23], v[112:115], v[36:39], v[20:23]\n"
    "buffer_store_dwordx4 v[8:11], v2, s[40:43], s62 offen sc1\n"
    "v_mfma_f32_16x16x32_bf16 v[24:27], v[40:43], v[228:231], v[24:27]\n"
    "v_mfma_f32_16x16x32_bf16 v[28:31], v[40:43], v[232:235], v[28:31]\n"
    "v_mul_f32 v16, v16, v70\n"
    "v_mul_f32 v20, v20, v70\n"
    "v_mfma_f32_16x16x32_bf16 v[24:27], v[44:47], v[236:239], v[24:27]\n"
    "v_cvt_pk_bf16_f32 v16, v16, v20\n"
    "v_mul_f32 v17, v17, v70\n"
    "v_mfma_f32_16x16x32_bf16 v[28:31], v[44:47], v[240:243], v[28:31]\n"
    "v_mul_f32 v21, v21, v70\n"
    "v_cvt_pk_bf16_f32 v17, v17, v21\n"
    "v_mfma_f32_16x16x32_bf16 v[24:27], v[32:35], v[244:247], v[24:27]\n"
    "v_mul_f32 v18, v18, v70\n"
    "v_mul_f32 v22, v22, v70\n"
    "v_mfma_f32_16x16x32_bf16 v[28:31], v[32:35], v[248:251], v[28:31]\n"
    "v_cvt_pk_bf16_f32 v18, v18, v22\n"
    "v_mul_f32 v19, v19, v70\n"
    "v_mfma_f32_16x16x32_bf16 v[24:27], v[36:39], v[252:255], v[24:27]\n"
    "v_mul_f32 v23, v23, v70\n"
    "v_cvt_pk_bf16_f32 v19, v19, v23\n"
    "v_mfma_f32_16x16x32_bf16 v[28:31], v[36:39], v[72:75], v[28:31]\n"
    "buffer_store_dwordx4 v[16:19], v2, s[40:43], s63 offen sc1\n"
    "s_mov_b32 s56, 1\n"
    "s_cmp_lg_u32 s94, 0\n"
    "s_cbranch_scc1 .Lhgo_%=\n"
    "s_nop 0\n"
    "v_mul_f32 v24, v24, v76\n"
    "v_mul_f32 v25, v25, v77\n"
    "v_cvt_pk_bf16_f32 v24, v24, v25\n"
    "v_mul_f32 v26, v26, v78\n"
    "v_mul_f32 v27, v27, v79\n"
    "v_cvt_pk_bf16_f32 v25, v26, v27\n"
    "v_mul_f32 v28, v28, v76\n"
    "v_mul_f32 v29, v29, v77\n"
    "v_cvt_pk_bf16_f32 v26, v28, v29\n"
    "v_mul_f32 v30, v30, v78\n"
    "v_mul_f32 v31, v31, v79\n"
    "v_cvt_pk_bf16_f32 v27, v30, v31\n"
    "buffer_store_dwordx4 v[24:27], v2, s[40:43], s64 offen sc1\n"
    "s_waitcnt vmcnt(3)\n"
    "v_cmp_eq_u32 vcc, 0, v81\n"
    "s_cmp_eq_u64 vcc, 0\n"
    "s_cbranch_scc1 .Lhld_%=\n"
    "ds_read_b32 v83, v7 offset:44\n"
    "global_load_dword v81, v80, %1 sc1\n"
    "s_waitcnt vmcnt(0)\n"
    "v_mov_b32 v82, 1\n"
    "s_mov_b64 exec, 1\n"
    "ds_add_rtn_u32 v82, v7, v82 offset:48\n"
    "s_mov_b64 exec, -1\n"
    "s_waitcnt lgkmcnt(0)\n"
    "v_readfirstlane_b32 s50, v82\n"
    "s_cmp_eq_u32 s50, 3\n"
    "s_cselect_b64 exec, 1, 0\n"
    "global_store_dword v83, v82, %1 sc1\n"
    "s_mov_b64 exec, -1\n"
    "s_mov_b32 s56, 0\n"
    "v_cmp_eq_u32 vcc, 0, v81\n"
    "s_cmp_eq_u64 vcc, 0\n"
    "s_cbranch_scc1 .Lhld_%=\n"
    ".Lhchk_%=:\n"
    "v_readfirstlane_b32 s50, v80\n"
    "s_cmp_lt_u32 s50, 200\n"
    "s_cbranch_scc1 .Lhend_%=\n"
    ".Lhwait_%=:\n"
    "global_load_dword v81, v80, %1 sc1\n"
    "s_mov_b32 s52, 0\n"
    ".Lhp2_%=:\n"
    "s_waitcnt vmcnt(0)\n"
    "v_cmp_eq_u32 vcc, 0, v81\n"
    "s_cmp_eq_u64 vcc, 0\n"
    "s_cbranch_scc1 .Lhp2_x%=\n"
    "s_add_u32 s52, s52, 1\n"
    "s_sleep 2\n"
    "global_load_dword v81, v80, %1 sc1\n"
    "s_cmp_lt_u32 s52, s53\n"
    "s_cbranch_scc1 .Lhp2_%=\n"
    "s_mov_b32 s53, 0\n"
    "v_mov_b32 v82, 0\n"
    "v_mov_b32 v83, 1\n"
    "global_store_dword v82, v83, %1 sc1\n"
    "s_waitcnt vmcnt(0)\n"
    ".Lhp2_x%=:\n"
    ".Lhld_%=:\n"
    "s_mov_b32 s94, s42\n"
    "s_mov_b32 s90, s96\n"
    "buffer_load_dwordx4 v[48:51], v3, s[92:95], s49 offen sc1\n"
    "buffer_load_dwordx4 v[52:55], v3, s[92:95], s49 offen offset:1024 sc1\n"
    "buffer_load_dwordx4 v[56:59], v3, s[92:95], s49 offen offset:2048 sc1\n"
    "buffer_load_dwordx4 v[60:63], v3, s[92:95], s49 offen offset:3072 sc1\n"
    "buffer_load_dwordx4 v[64:67], v2, s[88:91], s49 offen sc1\n"
    ".Lhin_%=:\n"
    "v_add_u32 v7, 32, v7\n"
    "ds_read_b128 v[80:83], v7\n"
    "s_waitcnt vmcnt(4)\n"
    "v_mfma_f32_16x16x32_bf16 v[68:71], v[48:51], v[48:51], 0\n"
    "s_waitcnt vmcnt(3)\n"
    "v_mfma_f32_16x16x32_bf16 v[68:71], v[52:55], v[52:55], v[68:71]\n"
    "ds_write_b128 v1, v[48:51]\n"
    "ds_write_b128 v1, v[52:55] offset:1024\n"
    "s_waitcnt vmcnt(2)\n"
    "v_mfma_f32_16x16x32_bf16 v[68:71], v[56:59], v[56:59], v[68:71]\n"
    "ds_write_b128 v1, v[56:59] offset:2048\n"
    "s_waitcnt vmcnt(1)\n"
    "ds_write_b128 v1, v[60:63] offset:3072\n"
    "v_mfma_f32_16x16x32_bf16 v[68:71], v[60:63], v[60:63], v[68:71]\n"
    "s_waitcnt vmcnt(0)\n"
    "v_mfma_f32_16x16x32_bf16 v[68:71], v[64:67], v[64:67], v[68:71]\n"
    "v_mfma_f32_16x16x32_bf16 v[8:11], a[0:3], v[48:51], 0\n"
    "v_mfma_f32_16x16x32_bf16 v[12:15], a[4:7], v[48:51], 0\n"
    "v_mfma_f32_16x16x32_bf16 v[16:19], a[144:147], v[48:51], 0\n"
    "v_mov_b64 v[76:77], s[86:87]\n"
    "v_mov_b64 v[78:79], s[86:87]\n"
    "v_mfma_f32_16x16x32_bf16 v[20:23], a[148:151], v[48:51], 0\n"
    "ds_write_b128 v1, v[64:67] offset:4096\n"
    "v_mfma_f32_16x16x32_bf16 v[72:75], v[76:79], v[48:51], 0\n"
    "v_mfma_f32_16x16x32_bf16 v[72:75], v[76:79], v[52:55], v[72:75]\n"
    "v_mfma_f32_16x16x32_bf16 v[72:75], v[76:79], v[56:59], v[72:75]\n"
    "ds_write_b128 v1, v[68:71] offset:5120\n"
    "s_waitcnt lgkmcnt(6)\n"
    "v_readfirstlane_b32 s49, v81\n"
    "v_mfma_f32_16x16x32_bf16 v[72:75], v[76:79], v[60:63], v[72:75]\n"
    "v_readfirstlane_b32 s62, v82\n"
    "v_add_u32 v80, v80, v4\n"
    "v_mfma_f32_16x16x32_bf16 v[72:75], v[76:79], v[64:67], v[72:75]\n"
    "s_cmp_eq_u32 s56, 0\n"
    "s_cbranch_scc1 .Lhc3_%=\n"
    "s_waitcnt vmcnt(0)\n"
    "v_mov_b32 v82, 1\n"
    "s_mov_b64 exec, 1\n"
    "ds_add_rtn_u32 v82, v7, v82 offset:16\n"
    "s_mov_b64 exec, -1\n"
    "s_waitcnt lgkmcnt(0)\n"
    "v_readfirstlane_b32 s50, v82\n"
    "s_cmp_eq_u32 s50, 3\n"
    "s_cselect_b64 exec, 1, 0\n"
    "global_store_dword v83, v82, %1 sc1\n"
    "s_mov_b64 exec, -1\n"
    "s_mov_b32 s56, 0\n"
    ".Lhc3_%=:\n"
    "s_branch .Lhtop_%=\n"
    ".Lhk1_%=:\n"
    "s_waitcnt vmcnt(5)\n"
    "v_cmp_eq_u32 vcc, 0, v81\n"
    "s_cmp_eq_u64 vcc, 0\n"
    "s_cselect_b32 s94, s42, 0\n"
    "s_cselect_b32 s90, s96, 0\n"
    "global_load_dword v81, v80, %1 sc1\n"
    "buffer_load_dwordx4 v[48:51], v3, s[92:95], s49 offen sc1\n"
    "buffer_load_dwordx4 v[52:55], v3, s[92:95], s49 offen offset:1024 sc1\n"
    "buffer_load_dwordx4 v[56:59], v3, s[92:95], s49 offen offset:2048 sc1\n"
    "buffer_load_dwordx4 v[60:63], v3, s[92:95], s49 offen offset:3072 sc1\n"
    "buffer_load_dwordx4 v[64:67], v2, s[88:91], s49 offen sc1\n"
    "s_branch .Lhk1_x%=\n"
    ".Lhpol_%=:\n"
    "v_mov_b32 v80, 64\n"
    "global_load_dword v81, v80, %1 sc1\n"
    "s_mov_b32 s52, 0\n"
    ".Lhp4_%=:\n"
    "s_waitcnt vmcnt(0)\n"
    "v_cmp_eq_u32 vcc, 0, v81\n"
    "s_cmp_eq_u64 vcc, 0\n"
    "s_cbranch_scc1 .Lhp4_x%=\n"
    "s_add_u32 s52, s52, 1\n"
    "s_sleep 2\n"
    "global_load_dword v81, v80, %1 sc1\n"
    "s_cmp_lt_u32 s52, s53\n"
    "s_cbranch_scc1 .Lhp4_%=\n"
    "s_mov_b32 s53, 0\n"
    "v_mov_b32 v82, 0\n"
    "v_mov_b32 v83, 1\n"
    "global_store_dword v82, v83, %1 sc1\n"
    "s_waitcnt vmcnt(0)\n"
    ".Lhp4_x%=:\n"
    "v_readfirstlane_b32 s50, v81\n"
    "v_mbcnt_lo_u32_b32 v80, -1, 0\n"
    "v_mbcnt_hi_u32_b32 v80, -1, v80\n"
    "s_lshl_b32 s51, %2, 6\n"
    "v_add_u32 v80, s51, v80\n"
    "v_min_u32 v80, 129, v80\n"
    "v_lshlrev_b32 v80, 5, v80\n"
    "v_add_u32 v80, 0x26c00, v80\n"
    "ds_read2_b32 v[82:83], v80 offset0:1 offset1:2\n"
    "s_and_b32 s50, s50, 1\n"
    "s_waitcnt lgkmcnt(0)\n"
    "v_and_b32 v81, s50, v82\n"
    "v_lshlrev_b32 v81, 13, v81\n"
    "v_and_b32 v82, -2, v82\n"
    "v_xor_b32 v82, v81, v82\n"
    "v_and_b32 v81, s50, v83\n"
    "v_lshlrev_b32 v81, 13, v81\n"
    "v_and_b32 v83, -2, v83\n"
    "v_xor_b32 v83, v81, v83\n"
    "ds_write2_b32 v80, v82, v83 offset0:1 offset1:2\n"
    "s_waitcnt lgkmcnt(0)\n"
    "s_barrier\n"
    "ds_read_b32 v81, v7 offset:4\n"
    "v_add_u32 v80, %4, v4\n"
    "s_waitcnt lgkmcnt(0)\n"
    "v_readfirstlane_b32 s49, v81\n"
    "s_branch .Lhwait_%=\n"
    ".Lhend_%=:\n"
    "s_cmp_eq_u32 s50, 192\n"
    "s_cbranch_scc0 .Lhpol_%=\n"
    "s_waitcnt vmcnt(0) lgkmcnt(0)\n"
    "s_endpgm\n"
    ".p2align 6\n"
    ".Lrole2_%=:\n"
    "s_mov_b64 s[40:41], %0\n"
    "s_and_b32 s41, s41, 0xffff\n"
    "s_mov_b32 s42, 0xbc00000\n"
    "s_mov_b32 s43, 0x20000\n"
    "s_mov_b64 s[92:93], %0\n"
    "s_and_b32 s93, s93, 0xffff\n"
    "s_mov_b32 s94, 0xbc00000\n"
    "s_mov_b32 s95, 0x20000\n"
    "s_mov_b32 s53, 131072\n"
    "s_mov_b64 s[56:57], 0\n"
    "v_mbcnt_lo_u32_b32 v7, -1, 0\n"
    "v_mbcnt_hi_u32_b32 v7, -1, v7\n"
    "s_lshl_b32 s50, %2, 8\n"
    "s_add_u32 s50, s50, 147456\n"
    "v_lshl_add_u32 v7, v7, 2, s50\n"
    "ds_read_b32 v1, v7\n"
    "ds_read_b32 v4, v7 offset:1024\n"
    "ds_read_b32 v2, v7 offset:2048\n"
    "ds_read_b32 v8, v7 offset:3072\n"
    "ds_read_b32 v9, v7 offset:4096\n"
    "ds_read_b32 v10, v7 offset:5120\n"
    "ds_read_b32 v11, v7 offset:6144\n"
    "ds_read_b32 v3, v7 offset:7168\n"
    "s_waitcnt lgkmcnt(0)\n"
    "s_barrier\n"
    "s_mul_i32 s54, %2, 6144\n"
    "v_add_u32 v5, 16384, v4\n"
    "s_xor_b32 s50, %2, 0\n"
    "s_mul_i32 s50, s50, 6144\n"
    "v_add_u32 v32, s50, v9\n"
    "v_add_u32 v33, s50, v10\n"
    "v_add_u32 v34, s50, v11\n"
    "s_xor_b32 s50, %2, 1\n"
    "s_mul_i32 s50, s50, 6144\n"
    "v_add_u32 v35, s50, v9\n"
    "v_add_u32 v36, s50, v10\n"
    "v_add_u32 v37, s50, v11\n"
    "s_xor_b32 s50, %2, 2\n"
    "s_mul_i32 s50, s50, 6144\n"
    "v_add_u32 v38, s50, v9\n"
    "v_add_u32 v39, s50, v10\n"
    "v_add_u32 v40, s50, v11\n"
    "s_xor_b32 s50, %2, 3\n"
    "s_mul_i32 s50, s50, 6144\n"
    "v_add_u32 v41, s50, v9\n"
    "v_add_u32 v42, s50, v10\n"
    "v_add_u32 v43, s50, v11\n"
    "s_mov_b64 s[44:45], %8\n"
    "s_and_b32 s45, s45, 0xffff\n"
    "s_mov_b32 s46, 0x480000\n"
    "s_mov_b32 s47, 0x20000\n"
    "s_add_u32 m0, s54, 0\n"
    "s_add_u32 s55, %6, 0\n"
    "buffer_load_dwordx4 v8, s[44:47], s55 offen lds\n"
    "s_add_u32 m0, s54, 1024\n"
    "s_add_u32 s55, %6, 18432\n"
    "buffer_load_dwordx4 v8, s[44:47], s55 offen lds\n"
    "s_add_u32 m0, s54, 2048\n"
    "s_add_u32 s55, %6, 128\n"
    "buffer_load_dwordx4 v8, s[44:47], s55 offen lds\n"
    "s_add_u32 m0, s54, 3072\n"
    "s_add_u32 s55, %6, 18560\n"
    "buffer_load_dwordx4 v8, s[44:47], s55 offen lds\n"
    "s_add_u32 m0, s54, 4096\n"
    "s_add_u32 s55, %6, 256\n"
    "buffer_load_dwordx4 v8, s[44:47], s55 offen lds\n"
    "s_add_u32 m0, s54, 5120\n"
    "s_add_u32 s55, %6, 18688\n"
    "buffer_load_dwordx4 v8, s[44:47], s55 offen lds\n"
    "s_add_u32 m0, s54, 24576\n"
    "s_add_u32 s55, %6, 2304\n"
    "buffer_load_dwordx4 v8, s[44:47], s55 offen lds\n"
    "s_add_u32 m0, s54, 25600\n"
    "s_add_u32 s55, %6, 20736\n"
    "buffer_load_dwordx4 v8, s[44:47], s55 offen lds\n"
    "s_add_u32 m0, s54, 26624\n"
    "s_add_u32 s55, %6, 2432\n"
    "buffer_load_dwordx4 v8, s[44:47], s55 offen lds\n"
    "s_add_u32 m0, s54, 27648\n"
    "s_add_u32 s55, %6, 20864\n"
    "buffer_load_dwordx4 v8, s[44:47], s55 offen lds\n"
    "s_add_u32 m0, s54, 28672\n"
    "s_add_u32 s55, %6, 2560\n"
    "buffer_load_dwordx4 v8, s[44:47], s55 offen lds\n"
    "s_add_u32 m0, s54, 29696\n"
    "s_add_u32 s55, %6, 20992\n"
    "buffer_load_dwordx4 v8, s[44:47], s55 offen lds\n"
    "s_add_u32 m0, s54, 49152\n"
    "s_add_u32 s55, %6, 36864\n"
    "buffer_load_dwordx4 v8, s[44:47], s55 offen lds\n"
    "s_add_u32 m0, s54, 50176\n"
    "s_add_u32 s55, %6, 55296\n"
    "buffer_load_dwordx4 v8, s[44:47], s55 offen lds\n"
    "s_add_u32 m0, s54, 51200\n"
    "s_add_u32 s55, %6, 36992\n"
    "buffer_load_dwordx4 v8, s[44:47], s55 offen lds\n"
    "s_add_u32 m0, s54, 52224\n"
    "s_add_u32 s55, %6, 55424\n"
    "buffer_load_dwordx4 v8, s[44:47], s55 offen lds\n"
    "s_add_u32 m0, s54, 53248\n"
    "s_add_u32 s55, %6, 37120\n"
    "buffer_load_dwordx4 v8, s[44:47], s55 offen lds\n"
    "s_add_u32 m0, s54, 54272\n"
    "s_add_u32 s55, %6, 55552\n"
    "buffer_load_dwordx4 v8, s[44:47], s55 offen lds\n"
    "s_add_u32 m0, s54, 73728\n"
    "s_add_u32 s55, %6, 39168\n"
    "buffer_load_dwordx4 v8, s[44:47], s55 offen lds\n"
    "s_add_u32 m0, s54, 74752\n"
    "s_add_u32 s55, %6, 57600\n"
    "buffer_load_dwordx4 v8, s[44:47], s55 offen lds\n"
    "s_add_u32 m0, s54, 75776\n"
    "s_add_u32 s55, %6, 39296\n"
    "buffer_load_dwordx4 v8, s[44:47], s55 offen lds\n"
    "s_add_u32 m0, s54, 76800\n"
    "s_add_u32 s55, %6, 57728\n"
    "buffer_load_dwordx4 v8, s[44:47], s55 offen lds\n"
    "s_add_u32 m0, s54, 77824\n"
    "s_add_u32 s55, %6, 39424\n"
    "buffer_load_dwordx4 v8, s[44:47], s55 offen lds\n"
    "s_add_u32 m0, s54, 78848\n"
    "s_add_u32 s55, %6, 57856\n"
    "buffer_load_dwordx4 v8, s[44:47], s55 offen lds\n"
    "s_add_u32 m0, s54, 98304\n"
    "s_add_u32 s55, %6, 73728\n"
    "buffer_load_dwordx4 v8, s[44:47], s55 offen lds\n"
    "s_add_u32 m0, s54, 99328\n"
    "s_add_u32 s55, %6, 92160\n"
    "buffer_load_dwordx4 v8, s[44:47], s55 offen lds\n"
    "s_add_u32 m0, s54, 100352\n"
    "s_add_u32 s55, %6, 73856\n"
    "buffer_load_dwordx4 v8, s[44:47], s55 offen lds\n"
    "s_add_u32 m0, s54, 101376\n"
    "s_add_u32 s55, %6, 92288\n"
    "buffer_load_dwordx4 v8, s[44:47], s55 offen lds\n"
    "s_add_u32 m0, s54, 102400\n"
    "s_add_u32 s55, %6, 73984\n"
    "buffer_load_dwordx4 v8, s[44:47], s55 offen lds\n"
    "s_add_u32 m0, s54, 103424\n"
    "s_add_u32 s55, %6, 92416\n"
    "buffer_load_dwordx4 v8, s[44:47], s55 offen lds\n"
    "s_add_u32 m0, s54, 122880\n"
    "s_add_u32 s55, %6, 76032\n"
    "buffer_load_dwordx4 v8, s[44:47], s55 offen lds\n"
    "s_add_u32 m0, s54, 123904\n"
    "s_add_u32 s55, %6, 94464\n"
    "buffer_load_dwordx4 v8, s[44:47], s55 offen lds\n"
    "s_add_u32 m0, s54, 124928\n"
    "s_add_u32 s55, %6, 76160\n"
    "buffer_load_dwordx4 v8, s[44:47], s55 offen lds\n"
    "s_add_u32 m0, s54, 125952\n"
    "s_add_u32 s55, %6, 94592\n"
    "buffer_load_dwordx4 v8, s[44:47], s55 offen lds\n"
    "s_add_u32 m0, s54, 126976\n"
    "s_add_u32 s55, %6, 76288\n"
    "buffer_load_dwordx4 v8, s[44:47], s55 offen lds\n"
    "s_add_u32 m0, s54, 128000\n"
    "s_add_u32 s55, %6, 94720\n"
    "buffer_load_dwordx4 v8, s[44:47], s55 offen lds\n"
    "s_waitcnt vmcnt(30) lgkmcnt(0)\n"
    "s_barrier\n"
    "ds_read_b64_tr_b16 a[0:1], v32\n"
    "ds_read_b64_tr_b16 a[4:5], v33\n"
    "ds_read_b64_tr_b16 a[8:9], v34\n"
    "ds_read_b64_tr_b16 a[96:97], v35\n"
    "ds_read_b64_tr_b16 a[100:101], v36\n"
    "ds_read_b64_tr_b16 a[104:105], v37\n"
    "ds_read_b64_tr_b16 a[192:193], v38\n"
    "ds_read_b64_tr_b16 a[196:197], v39\n"
    "ds_read_b64_tr_b16 a[200:201], v40\n"
    "ds_read_b64_tr_b16 v[160:161], v41\n"
    "ds_read_b64_tr_b16 v[164:165], v42\n"
    "ds_read_b64_tr_b16 v[168:169], v43\n"
    "s_waitcnt vmcnt(24) lgkmcnt(0)\n"
    "s_barrier\n"
    "s_add_u32 m0, s54, 0\n"
    "s_add_u32 s55, %6, 110592\n"
    "buffer_load_dwordx4 v8, s[44:47], s55 offen lds\n"
    "s_add_u32 m0, s54, 1024\n"
    "s_add_u32 s55, %6, 129024\n"
    "buffer_load_dwordx4 v8, s[44:47], s55 offen lds\n"
    "s_add_u32 m0, s54, 2048\n"
    "s_add_u32 s55, %6, 110720\n"
    "buffer_load_dwordx4 v8, s[44:47], s55 offen lds\n"
    "s_add_u32 m0, s54, 3072\n"
    "s_add_u32 s55, %6, 129152\n"
    "buffer_load_dwordx4 v8, s[44:47], s55 offen lds\n"
    "s_add_u32 m0, s54, 4096\n"
    "s_add_u32 s55, %6, 110848\n"
    "buffer_load_dwordx4 v8, s[44:47], s55 offen lds\n"
    "s_add_u32 m0, s54, 5120\n"
    "s_add_u32 s55, %6, 129280\n"
    "buffer_load_dwordx4 v8, s[44:47], s55 offen lds\n"
    "ds_read_b64_tr_b16 a[2:3], v32 offset:24576\n"
    "ds_read_b64_tr_b16 a[6:7], v33 offset:24576\n"
    "ds_read_b64_tr_b16 a[10:11], v34 offset:24576\n"
    "ds_read_b64_tr_b16 a[98:99], v35 offset:24576\n"
    "ds_read_b64_tr_b16 a[102:103], v36 offset:24576\n"
    "ds_read_b64_tr_b16 a[106:107], v37 offset:24576\n"
    "ds_read_b64_tr_b16 a[194:195], v38 offset:24576\n"
    "ds_read_b64_tr_b16 a[198:199], v39 offset:24576\n"
    "ds_read_b64_tr_b16 a[202:203], v40 offset:24576\n"
    "ds_read_b64_tr_b16 v[162:163], v41 offset:24576\n"
    "ds_read_b64_tr_b16 v[166:167], v42 offset:24576\n"
    "ds_read_b64_tr_b16 v[170:171], v43 offset:24576\n"
    "s_waitcnt vmcnt(24) lgkmcnt(0)\n"
    "s_barrier\n"
    "s_add_u32 m0, s54, 24576\n"
    "s_add_u32 s55, %6, 112896\n"
    "buffer_load_dwordx4 v8, s[44:47], s55 offen lds\n"
    "s_add_u32 m0, s54, 25600\n"
    "s_add_u32 s55, %6, 131328\n"
    "buffer_load_dwordx4 v8, s[44:47], s55 offen lds\n"
    "s_add_u32 m0, s54, 26624\n"
    "s_add_u32 s55, %6, 113024\n"
    "buffer_load_dwordx4 v8, s[44:47], s55 offen lds\n"
    "s_add_u32 m0, s54, 27648\n"
    "s_add_u32 s55, %6, 131456\n"
    "buffer_load_dwordx4 v8, s[44:47], s55 offen lds\n"
    "s_add_u32 m0, s54, 28672\n"
    "s_add_u32 s55, %6, 113152\n"
    "buffer_load_dwordx4 v8, s[44:47], s55 offen lds\n"
    "s_add_u32 m0, s54, 29696\n"
    "s_add_u32 s55, %6, 131584\n"
    "buffer_load_dwordx4 v8, s[44:47], s55 offen lds\n"
    "ds_read_b64_tr_b16 a[12:13], v32 offset:49152\n"
    "ds_read_b64_tr_b16 a[16:17], v33 offset:49152\n"
    "ds_read_b64_tr_b16 a[20:21], v34 offset:49152\n"
    "ds_read_b64_tr_b16 a[108:109], v35 offset:49152\n"
    "ds_read_b64_tr_b16 a[112:113], v36 offset:49152\n"
    "ds_read_b64_tr_b16 a[116:117], v37 offset:49152\n"
    "ds_read_b64_tr_b16 a[204:205], v38 offset:49152\n"
    "ds_read_b64_tr_b16 a[208:209], v39 offset:49152\n"
    "ds_read_b64_tr_b16 a[212:213], v40 offset:49152\n"
    "ds_read_b64_tr_b16 v[172:173], v41 offset:49152\n"
    "ds_read_b64_tr_b16 v[176:177], v42 offset:49152\n"
    "ds_read_b64_tr_b16 v[180:181], v43 offset:49152\n"
    "s_waitcnt vmcnt(24) lgkmcnt(0)\n"
    "s_barrier\n"
    "s_add_u32 m0, s54, 49152\n"
    "s_add_u32 s55, %6, 147456\n"
    "buffer_load_dwordx4 v8, s[44:47], s55 offen lds\n"
    "s_add_u32 m0, s54, 50176\n"
    "s_add_u32 s55, %6, 165888\n"
    "buffer_load_dwordx4 v8, s[44:47], s55 offen lds\n"
    "s_add_u32 m0, s54, 51200\n"
    "s_add_u32 s55, %6, 147584\n"
    "buffer_load_dwordx4 v8, s[44:47], s55 offen lds\n"
    "s_add_u32 m0, s54, 52224\n"
    "s_add_u32 s55, %6, 166016\n"
    "buffer_load_dwordx4 v8, s[44:47], s55 offen lds\n"
    "s_add_u32 m0, s54, 53248\n"
    "s_add_u32 s55, %6, 147712\n"
    "buffer_load_dwordx4 v8, s[44:47], s55 offen lds\n"
    "s_add_u32 m0, s54, 54272\n"
    "s_add_u32 s55, %6, 166144\n"
    "buffer_load_dwordx4 v8, s[44:47], s55 offen lds\n"
    "v_add_u32 v71, 0x10000, v32\n"
    "ds_read_b64_tr_b16 a[14:15], v71 offset:8192\n"
    "v_add_u32 v70, 0x10000, v33\n"
    "ds_read_b64_tr_b16 a[18:19], v70 offset:8192\n"
    "v_add_u32 v69, 0x10000, v34\n"
    "ds_read_b64_tr_b16 a[22:23], v69 offset:8192\n"
    "v_add_u32 v68, 0x10000, v35\n"
    "ds_read_b64_tr_b16 a[110:111], v68 offset:8192\n"
    "v_add_u32 v67, 0x10000, v36\n"
    "ds_read_b64_tr_b16 a[114:115], v67 offset:8192\n"
    "v_add_u32 v66, 0x10000, v37\n"
    "ds_read_b64_tr_b16 a[118:119], v66 offset:8192\n"
    "v_add_u32 v65, 0x10000, v38\n"
    "ds_read_b64_tr_b16 a[206:207], v65 offset:8192\n"
    "v_add_u32 v64, 0x10000, v39\n"
    "ds_read_b64_tr_b16 a[210:211], v64 offset:8192\n"
    "v_add_u32 v63, 0x10000, v40\n"
    "ds_read_b64_tr_b16 a[214:215], v63 offset:8192\n"
    "v_add_u32 v62, 0x10000, v41\n"
    "ds_read_b64_tr_b16 v[174:175], v62 offset:8192\n"
    "v_add_u32 v61, 0x10000, v42\n"
    "ds_read_b64_tr_b16 v[178:179], v61 offset:8192\n"
    "v_add_u32 v60, 0x10000, v43\n"
    "ds_read_b64_tr_b16 v[182:183], v60 offset:8192\n"
    "s_waitcnt vmcnt(24) lgkmcnt(0)\n"
    "s_barrier\n"
    "s_add_u32 m0, s54, 73728\n"
    "s_add_u32 s55, %6, 149760\n"
    "buffer_load_dwordx4 v8, s[44:47], s55 offen lds\n"
    "s_add_u32 m0, s54, 74752\n"
    "s_add_u32 s55, %6, 168192\n"
    "buffer_load_dwordx4 v8, s[44:47], s55 offen lds\n"
    "s_add_u32 m0, s54, 75776\n"
    "s_add_u32 s55, %6, 149888\n"
    "buffer_load_dwordx4 v8, s[44:47], s55 offen lds\n"
    "s_add_u32 m0, s54, 76800\n"
    "s_add_u32 s55, %6, 168320\n"
    "buffer_load_dwordx4 v8, s[44:47], s55 offen lds\n"
    "s_add_u32 m0, s54, 77824\n"
    "s_add_u32 s55, %6, 150016\n"
    "buffer_load_dwordx4 v8, s[44:47], s55 offen lds\n"
    "s_add_u32 m0, s54, 78848\n"
    "s_add_u32 s55, %6, 168448\n"
    "buffer_load_dwordx4 v8, s[44:47], s55 offen lds\n"
    "ds_read_b64_tr_b16 a[24:25], v71 offset:32768\n"
    "ds_read_b64_tr_b16 a[28:29], v70 offset:32768\n"
    "ds_read_b64_tr_b16 a[32:33], v69 offset:32768\n"
    "ds_read_b64_tr_b16 a[120:121], v68 offset:32768\n"
    "ds_read_b64_tr_b16 a[124:125], v67 offset:32768\n"
    "ds_read_b64_tr_b16 a[128:129], v66 offset:32768\n"
    "ds_read_b64_tr_b16 a[216:217], v65 offset:32768\n"
    "ds_read_b64_tr_b16 a[220:221], v64 offset:32768\n"
    "ds_read_b64_tr_b16 a[224:225], v63 offset:32768\n"
    "ds_read_b64_tr_b16 v[184:185], v62 offset:32768\n"
    "ds_read_b64_tr_b16 v[188:189], v61 offset:32768\n"
    "ds_read_b64_tr_b16 v[192:193], v60 offset:32768\n"
    "s_waitcnt vmcnt(24) lgkmcnt(0)\n"
    "s_barrier\n"
    "s_add_u32 m0, s54, 98304\n"
    "s_add_u32 s55, %6, 184320\n"
    "buffer_load_dwordx4 v8, s[44:47], s55 offen lds\n"
    "s_add_u32 m0, s54, 99328\n"
    "s_add_u32 s55, %6, 202752\n"
    "buffer_load_dwordx4 v8, s[44:47], s55 offen lds\n"
    "s_add_u32 m0, s54, 100352\n"
    "s_add_u32 s55, %6, 184448\n"
    "buffer_load_dwordx4 v8, s[44:47], s55 offen lds\n"
    "s_add_u32 m0, s54, 101376\n"
    "s_add_u32 s55, %6, 202880\n"
    "buffer_load_dwordx4 v8, s[44:47], s55 offen lds\n"
    "s_add_u32 m0, s54, 102400\n"
    "s_add_u32 s55, %6, 184576\n"
    "buffer_load_dwordx4 v8, s[44:47], s55 offen lds\n"
    "s_add_u32 m0, s54, 103424\n"
    "s_add_u32 s55, %6, 203008\n"
    "buffer_load_dwordx4 v8, s[44:47], s55 offen lds\n"
    "ds_read_b64_tr_b16 a[26:27], v71 offset:57344\n"
    "ds_read_b64_tr_b16 a[30:31], v70 offset:57344\n"
    "ds_read_b64_tr_b16 a[34:35], v69 offset:57344\n"
    "ds_read_b64_tr_b16 a[122:123], v68 offset:57344\n"
    "ds_read_b64_tr_b16 a[126:127], v67 offset:57344\n"
    "ds_read_b64_tr_b16 a[130:131], v66 offset:57344\n"
    "ds_read_b64_tr_b16 a[218:219], v65 offset:57344\n"
    "ds_read_b64_tr_b16 a[222:223], v64 offset:57344\n"
    "ds_read_b64_tr_b16 a[226:227], v63 offset:57344\n"
    "ds_read_b64_tr_b16 v[186:187], v62 offset:57344\n"
    "ds_read_b64_tr_b16 v[190:191], v61 offset:57344\n"
    "ds_read_b64_tr_b16 v[194:195], v60 offset:57344\n"
    "s_waitcnt vmcnt(24) lgkmcnt(0)\n"
    "s_barrier\n"
    "s_add_u32 m0, s54, 122880\n"
    "s_add_u32 s55, %6, 186624\n"
    "buffer_load_dwordx4 v8, s[44:47], s55 offen lds\n"
    "s_add_u32 m0, s54, 123904\n"
    "s_add_u32 s55, %6, 205056\n"
    "buffer_load_dwordx4 v8, s[44:47], s55 offen lds\n"
    "s_add_u32 m0, s54, 124928\n"
    "s_add_u32 s55, %6, 186752\n"
    "buffer_load_dwordx4 v8, s[44:47], s55 offen lds\n"
    "s_add_u32 m0, s54, 125952\n"
    "s_add_u32 s55, %6, 205184\n"
    "buffer_load_dwordx4 v8, s[44:47], s55 offen lds\n"
    "s_add_u32 m0, s54, 126976\n"
    "s_add_u32 s55, %6, 186880\n"
    "buffer_load_dwordx4 v8, s[44:47], s55 offen lds\n"
    "s_add_u32 m0, s54, 128000\n"
    "s_add_u32 s55, %6, 205312\n"
    "buffer_load_dwordx4 v8, s[44:47], s55 offen lds\n"
    "ds_read_b64_tr_b16 a[36:37], v32\n"
    "ds_read_b64_tr_b16 a[40:41], v33\n"
    "ds_read_b64_tr_b16 a[44:45], v34\n"
    "ds_read_b64_tr_b16 a[132:133], v35\n"
    "ds_read_b64_tr_b16 a[136:137], v36\n"
    "ds_read_b64_tr_b16 a[140:141], v37\n"
    "ds_read_b64_tr_b16 a[228:229], v38\n"
    "ds_read_b64_tr_b16 a[232:233], v39\n"
    "ds_read_b64_tr_b16 a[236:237], v40\n"
    "ds_read_b64_tr_b16 v[196:197], v41\n"
    "ds_read_b64_tr_b16 v[200:201], v42\n"
    "ds_read_b64_tr_b16 v[204:205], v43\n"
    "s_waitcnt vmcnt(24) lgkmcnt(0)\n"
    "s_barrier\n"
    "s_add_u32 m0, s54, 0\n"
    "s_add_u32 s55, %6, 221184\n"
    "buffer_load_dwordx4 v8, s[44:47], s55 offen lds\n"
    "s_add_u32 m0, s54, 1024\n"
    "s_add_u32 s55, %6, 239616\n"
    "buffer_load_dwordx4 v8, s[44:47], s55 offen lds\n"
    "s_add_u32 m0, s54, 2048\n"
    "s_add_u32 s55, %6, 221312\n"
    "buffer_load_dwordx4 v8, s[44:47], s55 offen lds\n"
    "s_add_u32 m0, s54, 3072\n"
    "s_add_u32 s55, %6, 239744\n"
    "buffer_load_dwordx4 v8, s[44:47], s55 offen lds\n"
    "s_add_u32 m0, s54, 4096\n"
    "s_add_u32 s55, %6, 221440\n"
    "buffer_load_dwordx4 v8, s[44:47], s55 offen lds\n"
    "s_add_u32 m0, s54, 5120\n"
    "s_add_u32 s55, %6, 239872\n"
    "buffer_load_dwordx4 v8, s[44:47], s55 offen lds\n"
    "ds_read_b64_tr_b16 a[38:39], v32 offset:24576\n"
    "ds_read_b64_tr_b16 a[42:43], v33 offset:24576\n"
    "ds_read_b64_tr_b16 a[46:47], v34 offset:24576\n"
    "ds_read_b64_tr_b16 a[134:135], v35 offset:24576\n"
    "ds_read_b64_tr_b16 a[138:139], v36 offset:24576\n"
    "ds_read_b64_tr_b16 a[142:143], v37 offset:24576\n"
    "ds_read_b64_tr_b16 a[230:231], v38 offset:24576\n"
    "ds_read_b64_tr_b16 a[234:235], v39 offset:24576\n"
    "ds_read_b64_tr_b16 a[238:239], v40 offset:24576\n"
    "ds_read_b64_tr_b16 v[198:199], v41 offset:24576\n"
    "ds_read_b64_tr_b16 v[202:203], v42 offset:24576\n"
    "ds_read_b64_tr_b16 v[206:207], v43 offset:24576\n"
    "s_waitcnt vmcnt(24) lgkmcnt(0)\n"
    "s_barrier\n"
    "s_add_u32 m0, s54, 24576\n"
    "s_add_u32 s55, %6, 223488\n"
    "buffer_load_dwordx4 v8, s[44:47], s55 offen lds\n"
    "s_add_u32 m0, s54, 25600\n"
    "s_add_u32 s55, %6, 241920\n"
    "buffer_load_dwordx4 v8, s[44:47], s55 offen lds\n"
    "s_add_u32 m0, s54, 26624\n"
    "s_add_u32 s55, %6, 223616\n"
    "buffer_load_dwordx4 v8, s[44:47], s55 offen lds\n"
    "s_add_u32 m0, s54, 27648\n"
    "s_add_u32 s55, %6, 242048\n"
    "buffer_load_dwordx4 v8, s[44:47], s55 offen lds\n"
    "s_add_u32 m0, s54, 28672\n"
    "s_add_u32 s55, %6, 223744\n"
    "buffer_load_dwordx4 v8, s[44:47], s55 offen lds\n"
    "s_add_u32 m0, s54, 29696\n"
    "s_add_u32 s55, %6, 242176\n"
    "buffer_load_dwordx4 v8, s[44:47], s55 offen lds\n"
    "ds_read_b64_tr_b16 a[48:49], v32 offset:49152\n"
    "ds_read_b64_tr_b16 a[52:53], v33 offset:49152\n"
    "ds_read_b64_tr_b16 a[56:57], v34 offset:49152\n"
    "ds_read_b64_tr_b16 a[144:145], v35 offset:49152\n"
    "ds_read_b64_tr_b16 a[148:149], v36 offset:49152\n"
    "ds_read_b64_tr_b16 a[152:153], v37 offset:49152\n"
    "ds_read_b64_tr_b16 a[240:241], v38 offset:49152\n"
    "ds_read_b64_tr_b16 a[244:245], v39 offset:49152\n"
    "ds_read_b64_tr_b16 a[248:249], v40 offset:49152\n"
    "ds_read_b64_tr_b16 v[208:209], v41 offset:49152\n"
    "ds_read_b64_tr_b16 v[212:213], v42 offset:49152\n"
    "ds_read_b64_tr_b16 v[216:217], v43 offset:49152\n"
    "s_waitcnt vmcnt(24) lgkmcnt(0)\n"
    "s_barrier\n"
    "s_add_u32 m0, s54, 49152\n"
    "s_add_u32 s55, %6, 258048\n"
    "buffer_load_dwordx4 v8, s[44:47], s55 offen lds\n"
    "s_add_u32 m0, s54, 50176\n"
    "s_add_u32 s55, %6, 276480\n"
    "buffer_load_dwordx4 v8, s[44:47], s55 offen lds\n"
    "s_add_u32 m0, s54, 51200\n"
    "s_add_u32 s55, %6, 258176\n"
    "buffer_load_dwordx4 v8, s[44:47], s55 offen lds\n"
    "s_add_u32 m0, s54, 52224\n"
    "s_add_u32 s55, %6, 276608\n"
    "buffer_load_dwordx4 v8, s[44:47], s55 offen lds\n"
    "s_add_u32 m0, s54, 53248\n"
    "s_add_u32 s55, %6, 258304\n"
    "buffer_load_dwordx4 v8, s[44:47], s55 offen lds\n"
    "s_add_u32 m0, s54, 54272\n"
    "s_add_u32 s55, %6, 276736\n"
    "buffer_load_dwordx4 v8, s[44:47], s55 offen lds\n"
    "ds_read_b64_tr_b16 a[50:51], v71 offset:8192\n"
    "ds_read_b64_tr_b16 a[54:55], v70 offset:8192\n"
    "ds_read_b64_tr_b16 a[58:59], v69 offset:8192\n"
    "ds_read_b64_tr_b16 a[146:147], v68 offset:8192\n"
    "ds_read_b64_tr_b16 a[150:151], v67 offset:8192\n"
    "ds_read_b64_tr_b16 a[154:155], v66 offset:8192\n"
    "ds_read_b64_tr_b16 a[242:243], v65 offset:8192\n"
    "ds_read_b64_tr_b16 a[246:247], v64 offset:8192\n"
    "ds_read_b64_tr_b16 a[250:251], v63 offset:8192\n"
    "ds_read_b64_tr_b16 v[210:211], v62 offset:8192\n"
    "ds_read_b64_tr_b16 v[214:215], v61 offset:8192\n"
    "ds_read_b64_tr_b16 v[218:219], v60 offset:8192\n"
    "s_waitcnt vmcnt(24) lgkmcnt(0)\n"
    "s_barrier\n"
    "s_add_u32 m0, s54, 73728\n"
    "s_add_u32 s55, %6, 260352\n"
    "buffer_load_dwordx4 v8, s[44:47], s55 offen lds\n"
    "s_add_u32 m0, s54, 74752\n"
    "s_add_u32 s55, %6, 278784\n"
    "buffer_load_dwordx4 v8, s[44:47], s55 offen lds\n"
    "s_add_u32 m0, s54, 75776\n"
    "s_add_u32 s55, %6, 260480\n"
    "buffer_load_dwordx4 v8, s[44:47], s55 offen lds\n"
    "s_add_u32 m0, s54, 76800\n"
    "s_add_u32 s55, %6, 278912\n"
    "buffer_load_dwordx4 v8, s[44:47], s55 offen lds\n"
    "s_add_u32 m0, s54, 77824\n"
    "s_add_u32 s55, %6, 260608\n"
    "buffer_load_dwordx4 v8, s[44:47], s55 offen lds\n"
    "s_add_u32 m0, s54, 78848\n"
    "s_add_u32 s55, %6, 279040\n"
    "buffer_load_dwordx4 v8, s[44:47], s55 offen lds\n"
    "ds_read_b64_tr_b16 a[60:61], v71 offset:32768\n"
    "ds_read_b64_tr_b16 a[64:65], v70 offset:32768\n"
    "ds_read_b64_tr_b16 a[68:69], v69 offset:32768\n"
    "ds_read_b64_tr_b16 a[156:157], v68 offset:32768\n"
    "ds_read_b64_tr_b16 a[160:161], v67 offset:32768\n"
    "ds_read_b64_tr_b16 a[164:165], v66 offset:32768\n"
    "ds_read_b64_tr_b16 a[252:253], v65 offset:32768\n"
    "ds_read_b64_tr_b16 v[128:129], v64 offset:32768\n"
    "ds_read_b64_tr_b16 v[132:133], v63 offset:32768\n"
    "ds_read_b64_tr_b16 v[220:221], v62 offset:32768\n"
    "ds_read_b64_tr_b16 v[224:225], v61 offset:32768\n"
    "ds_read_b64_tr_b16 v[228:229], v60 offset:32768\n"
    "s_waitcnt vmcnt(24) lgkmcnt(0)\n"
    "s_barrier\n"
    "ds_read_b64_tr_b16 a[62:63], v71 offset:57344\n"
    "ds_read_b64_tr_b16 a[66:67], v70 offset:57344\n"
    "ds_read_b64_tr_b16 a[70:71], v69 offset:57344\n"
    "ds_read_b64_tr_b16 a[158:159], v68 offset:57344\n"
    "ds_read_b64_tr_b16 a[162:163], v67 offset:57344\n"
    "ds_read_b64_tr_b16 a[166:167], v66 offset:57344\n"
    "ds_read_b64_tr_b16 a[254:255], v65 offset:57344\n"
    "ds_read_b64_tr_b16 v[130:131], v64 offset:57344\n"
    "ds_read_b64_tr_b16 v[134:135], v63 offset:57344\n"
    "ds_read_b64_tr_b16 v[222:223], v62 offset:57344\n"
    "ds_read_b64_tr_b16 v[226:227], v61 offset:57344\n"
    "ds_read_b64_tr_b16 v[230:231], v60 offset:57344\n"
    "s_waitcnt vmcnt(18) lgkmcnt(0)\n"
    "s_barrier\n"
    "ds_read_b64_tr_b16 a[72:73], v32\n"
    "ds_read_b64_tr_b16 a[76:77], v33\n"
    "ds_read_b64_tr_b16 a[80:81], v34\n"
    "ds_read_b64_tr_b16 a[168:169], v35\n"
    "ds_read_b64_tr_b16 a[172:173], v36\n"
    "ds_read_b64_tr_b16 a[176:177], v37\n"
    "ds_read_b64_tr_b16 v[136:137], v38\n"
    "ds_read_b64_tr_b16 v[140:141], v39\n"
    "ds_read_b64_tr_b16 v[144:145], v40\n"
    "ds_read_b64_tr_b16 v[232:233], v41\n"
    "ds_read_b64_tr_b16 v[236:237], v42\n"
    "ds_read_b64_tr_b16 v[240:241], v43\n"
    "s_waitcnt vmcnt(12) lgkmcnt(0)\n"
    "s_barrier\n"
    "ds_read_b64_tr_b16 a[74:75], v32 offset:24576\n"
    "ds_read_b64_tr_b16 a[78:79], v33 offset:24576\n"
    "ds_read_b64_tr_b16 a[82:83], v34 offset:24576\n"
    "ds_read_b64_tr_b16 a[170:171], v35 offset:24576\n"
    "ds_read_b64_tr_b16 a[174:175], v36 offset:24576\n"
    "ds_read_b64_tr_b16 a[178:179], v37 offset:24576\n"
    "ds_read_b64_tr_b16 v[138:139], v38 offset:24576\n"
    "ds_read_b64_tr_b16 v[142:143], v39 offset:24576\n"
    "ds_read_b64_tr_b16 v[146:147], v40 offset:24576\n"
    "ds_read_b64_tr_b16 v[234:235], v41 offset:24576\n"
    "ds_read_b64_tr_b16 v[238:239], v42 offset:24576\n"
    "ds_read_b64_tr_b16 v[242:243], v43 offset:24576\n"
    "s_waitcnt vmcnt(6) lgkmcnt(0)\n"
    "s_barrier\n"
    "ds_read_b64_tr_b16 a[84:85], v32 offset:49152\n"
    "ds_read_b64_tr_b16 a[88:89], v33 offset:49152\n"
    "ds_read_b64_tr_b16 a[92:93], v34 offset:49152\n"
    "ds_read_b64_tr_b16 a[180:181], v35 offset:49152\n"
    "ds_read_b64_tr_b16 a[184:185], v36 offset:49152\n"
    "ds_read_b64_tr_b16 a[188:189], v37 offset:49152\n"
    "ds_read_b64_tr_b16 v[148:149], v38 offset:49152\n"
    "ds_read_b64_tr_b16 v[152:153], v39 offset:49152\n"
    "ds_read_b64_tr_b16 v[156:157], v40 offset:49152\n"
    "ds_read_b64_tr_b16 v[244:245], v41 offset:49152\n"
    "ds_read_b64_tr_b16 v[248:249], v42 offset:49152\n"
    "ds_read_b64_tr_b16 v[252:253], v43 offset:49152\n"
    "s_waitcnt vmcnt(0) lgkmcnt(0)\n"
    "s_barrier\n"
    "ds_read_b64_tr_b16 a[86:87], v71 offset:8192\n"
    "ds_read_b64_tr_b16 a[90:91], v70 offset:8192\n"
    "ds_read_b64_tr_b16 a[94:95], v69 offset:8192\n"
    "ds_read_b64_tr_b16 a[182:183], v68 offset:8192\n"
    "ds_read_b64_tr_b16 a[186:187], v67 offset:8192\n"
    "ds_read_b64_tr_b16 a[190:191], v66 offset:8192\n"
    "ds_read_b64_tr_b16 v[150:151], v65 offset:8192\n"
    "ds_read_b64_tr_b16 v[154:155], v64 offset:8192\n"
    "ds_read_b64_tr_b16 v[158:159], v63 offset:8192\n"
    "ds_read_b64_tr_b16 v[246:247], v62 offset:8192\n"
    "ds_read_b64_tr_b16 v[250:251], v61 offset:8192\n"
    "ds_read_b64_tr_b16 v[254:255], v60 offset:8192\n"
    "s_waitcnt lgkmcnt(0)\n"
    "s_barrier\n"
    "v_mbcnt_lo_u32_b32 v20, -1, 0\n"
    "v_mbcnt_hi_u32_b32 v20, -1, v20\n"
    "v_min_u32 v6, 7, v20\n"
    "v_lshlrev_b32 v6, 7, v6\n"
    "v_mov_b32 v7, 0x26c00\n"
    "ds_read_b128 v[20:23], v7\n"
    "s_waitcnt lgkmcnt(0)\n"
    "v_readfirstlane_b32 s49, v21\n"
    "v_add_u32 v20, v20, v6\n"
    "s_branch .Lwchk_%=\n"
    ".Lwgo_%=:\n"
    "s_waitcnt vmcnt(1)\n"
    "v_mfma_f32_16x16x32_bf16 v[8:11], a[0:3], v[64:67], 0\n"
    "v_add_u32 v7, 32, v7\n"
    "ds_read_b128 v[20:23], v7\n"
    "v_mfma_f32_16x16x32_bf16 v[12:15], a[4:7], v[64:67], 0\n"
    "ds_write_b128 v1, v[64:67]\n"
    "ds_write_b128 v1, v[68:71] offset:1024\n"
    "v_mfma_f32_16x16x32_bf16 v[8:11], a[12:15], v[68:71], v[8:11]\n"
    "ds_write_b128 v1, v[72:75] offset:2048\n"
    "ds_write_b128 v1, v[76:79] offset:3072\n"
    "v_mfma_f32_16x16x32_bf16 v[12:15], a[16:19], v[68:71], v[12:15]\n"
    "v_cvt_pk_bf16_f32 v28, v16, v17\n"
    "v_cvt_pk_bf16_f32 v30, v16, v17\n"
    "v_mfma_f32_16x16x32_bf16 v[8:11], a[24:27], v[72:75], v[8:11]\n"
    "v_cvt_pk_bf16_f32 v29, v18, v19\n"
    "v_cvt_pk_bf16_f32 v31, v18, v19\n"
    "v_mfma_f32_16x16x32_bf16 v[12:15], a[28:31], v[72:75], v[12:15]\n"
    "ds_write_b128 v1, v[80:83] offset:4096\n"
    "ds_write_b128 v1, v[84:87] offset:5120\n"
    "v_mfma_f32_16x16x32_bf16 v[8:11], a[36:39], v[76:79], v[8:11]\n"
    "v_permlane16_swap_b32 v28, v30\n"
    "v_permlane16_swap_b32 v29, v31\n"
    "v_mfma_f32_16x16x32_bf16 v[12:15], a[40:43], v[76:79], v[12:15]\n"
    "ds_write_b128 v1, v[88:91] offset:6144\n"
    "ds_write_b128 v1, v[92:95] offset:7168\n"
    "v_mfma_f32_16x16x32_bf16 v[8:11], a[48:51], v[80:83], v[8:11]\n"
    "buffer_store_dwordx4 v[28:31], v3, s[40:43], s62 offen sc1\n"
    "v_xor_b32 v0, 0x2000, v1\n"
    "v_mfma_f32_16x16x32_bf16 v[12:15], a[52:55], v[80:83], v[12:15]\n"
    "s_waitcnt lgkmcnt(8)\n"
    "v_readfirstlane_b32 s49, v21\n"
    "v_mfma_f32_16x16x32_bf16 v[8:11], a[60:63], v[84:87], v[8:11]\n"
    "v_readfirstlane_b32 s62, v22\n"
    "v_add_u32 v20, v20, v6\n"
    "v_mfma_f32_16x16x32_bf16 v[12:15], a[64:67], v[84:87], v[12:15]\n"
    ".Lwtop_%=:\n"
    "v_mfma_f32_16x16x32_bf16 v[16:19], a[8:11], v[64:67], 0\n"
    "s_waitcnt lgkmcnt(0)\n"
    "s_barrier\n"
    "v_mfma_f32_16x16x32_bf16 v[16:19], a[20:23], v[68:71], v[16:19]\n"
    "ds_read_b128 v[32:35], v0\n"
    "ds_read_b128 v[36:39], v0 offset:1024\n"
    "v_mfma_f32_16x16x32_bf16 v[16:19], a[32:35], v[72:75], v[16:19]\n"
    "ds_read_b128 v[44:47], v0 offset:3072\n"
    "ds_read_b128 v[40:43], v0 offset:2048\n"
    "v_mfma_f32_16x16x32_bf16 v[16:19], a[44:47], v[76:79], v[16:19]\n"
    "ds_read_b128 v[52:55], v0 offset:5120\n"
    "ds_read_b128 v[48:51], v0 offset:4096\n"
    "v_mfma_f32_16x16x32_bf16 v[16:19], a[56:59], v[80:83], v[16:19]\n"
    "ds_read_b128 v[60:63], v0 offset:7168\n"
    "ds_read_b128 v[56:59], v0 offset:6144\n"
    "v_mfma_f32_16x16x32_bf16 v[16:19], a[68:71], v[84:87], v[16:19]\n"
    "global_load_dword v21, v20, %1 sc1\n"
    "v_mfma_f32_16x16x32_bf16 v[8:11], a[72:75], v[88:91], v[8:11]\n"
    "v_mfma_f32_16x16x32_bf16 v[12:15], a[76:79], v[88:91], v[12:15]\n"
    "v_mfma_f32_16x16x32_bf16 v[16:19], a[80:83], v[88:91], v[16:19]\n"
    "v_mfma_f32_16x16x32_bf16 v[8:11], a[84:87], v[92:95], v[8:11]\n"
    "v_mfma_f32_16x16x32_bf16 v[12:15], a[88:91], v[92:95], v[12:15]\n"
    "v_mfma_f32_16x16x32_bf16 v[16:19], a[92:95], v[92:95], v[16:19]\n"
    "s_waitcnt lgkmcnt(7)\n"
    "v_mfma_f32_16x16x32_bf16 v[8:11], a[96:99], v[32:35], v[8:11]\n"
    "v_mfma_f32_16x16x32_bf16 v[12:15], a[100:103], v[32:35], v[12:15]\n"
    "v_mfma_f32_16x16x32_bf16 v[16:19], a[104:107], v[32:35], v[16:19]\n"
    "s_waitcnt lgkmcnt(6)\n"
    "v_mfma_f32_16x16x32_bf16 v[8:11], a[108:111], v[36:39], v[8:11]\n"
    "v_mfma_f32_16x16x32_bf16 v[12:15], a[112:115], v[36:39], v[12:15]\n"
    "v_xor_b32 v0, 0x4000, v1\n"
    "v_mfma_f32_16x16x32_bf16 v[16:19], a[116:119], v[36:39], v[16:19]\n"
    "s_waitcnt lgkmcnt(4)\n"
    "v_mfma_f32_16x16x32_bf16 v[8:11], a[120:123], v[40:43], v[8:11]\n"
    "ds_read_b128 v[36:39], v0 offset:1024\n"
    "ds_read_b128 v[32:35], v0\n"
    "v_mfma_f32_16x16x32_bf16 v[12:15], a[124:127], v[40:43], v[12:15]\n"
    "v_mfma_f32_16x16x32_bf16 v[16:19], a[128:131], v[40:43], v[16:19]\n"
    "v_mfma_f32_16x16x32_bf16 v[8:11], a[132:135], v[44:47], v[8:11]\n"
    "v_mfma_f32_16x16x32_bf16 v[12:15], a[136:139], v[44:47], v[12:15]\n"
    "v_mfma_f32_16x16x32_bf16 v[16:19], a[140:143], v[44:47], v[16:19]\n"
    "s_waitcnt lgkmcnt(4)\n"
    "v_mfma_f32_16x16x32_bf16 v[8:11], a[144:147], v[48:51], v[8:11]\n"
    "ds_read_b128 v[44:47], v0 offset:3072\n"
    "ds_read_b128 v[40:43], v0 offset:2048\n"
    "v_mfma_f32_16x16x32_bf16 v[12:15], a[148:151], v[48:51], v[12:15]\n"
    "v_mfma_f32_16x16x32_bf16 v[16:19], a[152:155], v[48:51], v[16:19]\n"
    "v_mfma_f32_16x16x32_bf16 v[8:11], a[156:159], v[52:55], v[8:11]\n"
    "v_mfma_f32_16x16x32_bf16 v[12:15], a[160:163], v[52:55], v[12:15]\n"
    "v_mfma_f32_16x16x32_bf16 v[16:19], a[164:167], v[52:55], v[16:19]\n"
    "s_waitcnt lgkmcnt(4)\n"
    "v_mfma_f32_16x16x32_bf16 v[8:11], a[168:171], v[56:59], v[8:11]\n"
    "ds_read_b128 v[52:55], v0 offset:5120\n"
    "ds_read_b128 v[48:51], v0 offset:4096\n"
    "v_mfma_f32_16x16x32_bf16 v[12:15], a[172:175], v[56:59], v[12:15]\n"
    "v_mfma_f32_16x16x32_bf16 v[16:19], a[176:179], v[56:59], v[16:19]\n"
    "s_waitcnt vmcnt(0)\n"
    "v_mov_b32 v22, 1\n"
    "v_mfma_f32_16x16x32_bf16 v[8:11], a[180:183], v[60:63], v[8:11]\n"
    "s_mov_b64 exec, s[56:57]\n"
    "ds_add_rtn_u32 v22, v7, v22 offset:16\n"
    "s_mov_b64 exec, -1\n"
    "v_mfma_f32_16x16x32_bf16 v[12:15], a[184:187], v[60:63], v[12:15]\n"
    "v_cmp_eq_u32 vcc, 0, v21\n"
    "s_cmp_eq_u64 vcc, 0\n"
    "v_mfma_f32_16x16x32_bf16 v[16:19], a[188:191], v[60:63], v[16:19]\n"
    "s_waitcnt lgkmcnt(4)\n"
    "v_mfma_f32_16x16x32_bf16 v[8:11], a[192:195], v[32:35], v[8:11]\n"
    "ds_read_b128 v[60:63], v0 offset:7168\n"
    "ds_read_b128 v[56:59], v0 offset:6144\n"
    "v_mfma_f32_16x16x32_bf16 v[12:15], a[196:199], v[32:35], v[12:15]\n"
    "s_cselect_b32 s94, s42, 0\n"
    "v_mfma_f32_16x16x32_bf16 v[16:19], a[200:203], v[32:35], v[16:19]\n"
    "global_load_dword v21, v20, %1 sc1\n"
    "v_mfma_f32_16x16x32_bf16 v[8:11], a[204:207], v[36:39], v[8:11]\n"
    "buffer_load_dwordx4 v[64:67], v4, s[92:95], s49 offen sc1\n"
    "v_mfma_f32_16x16x32_bf16 v[12:15], a[208:211], v[36:39], v[12:15]\n"
    "v_xor_b32 v0, 0x6000, v1\n"
    "buffer_load_dwordx4 v[68:71], v4, s[92:95], s49 offen offset:1024 sc1\n"
    "v_mfma_f32_16x16x32_bf16 v[16:19], a[212:215], v[36:39], v[16:19]\n"
    "s_waitcnt lgkmcnt(4)\n"
    "v_mfma_f32_16x16x32_bf16 v[8:11], a[216:219], v[40:43], v[8:11]\n"
    "ds_read_b128 v[36:39], v0 offset:1024\n"
    "ds_read_b128 v[32:35], v0\n"
    "v_mfma_f32_16x16x32_bf16 v[12:15], a[220:223], v[40:43], v[12:15]\n"
    "s_waitcnt lgkmcnt(4)\n"
    "v_readfirstlane_b32 s50, v22\n"
    "v_mfma_f32_16x16x32_bf16 v[16:19], a[224:227], v[40:43], v[16:19]\n"
    "buffer_load_dwordx4 v[72:75], v4, s[92:95], s49 offen offset:2048 sc1\n"
    "s_cmp_eq_u32 s50, 3\n"
    "v_mfma_f32_16x16x32_bf16 v[8:11], a[228:231], v[44:47], v[8:11]\n"
    "s_cselect_b64 exec, 1, 0\n"
    "global_store_dword v23, v22, %1 sc1\n"
    "s_mov_b64 exec, -1\n"
    "v_mfma_f32_16x16x32_bf16 v[12:15], a[232:235], v[44:47], v[12:15]\n"
    "buffer_load_dwordx4 v[76:79], v4, s[92:95], s49 offen offset:3072 sc1\n"
    "v_mfma_f32_16x16x32_bf16 v[16:19], a[236:239], v[44:47], v[16:19]\n"
    "buffer_load_dwordx4 v[80:83], v5, s[92:95], s49 offen sc1\n"
    "v_mfma_f32_16x16x32_bf16 v[8:11], a[240:243], v[48:51], v[8:11]\n"
    "ds_read_b128 v[44:47], v0 offset:3072\n"
    "ds_read_b128 v[40:43], v0 offset:2048\n"
    "v_mfma_f32_16x16x32_bf16 v[12:15], a[244:247], v[48:51], v[12:15]\n"
    "buffer_load_dwordx4 v[84:87], v5, s[92:95], s49 offen offset:1024 sc1\n"
    "v_mfma_f32_16x16x32_bf16 v[16:19], a[248:251], v[48:51], v[16:19]\n"
    "buffer_load_dwordx4 v[88:91], v5, s[92:95], s49 offen offset:2048 sc1\n"
    "v_mfma_f32_16x16x32_bf16 v[8:11], a[252:255], v[52:55], v[8:11]\n"
    "buffer_load_dwordx4 v[92:95], v5, s[92:95], s49 offen offset:3072 sc1\n"
    "v_mfma_f32_16x16x32_bf16 v[12:15], v[128:131], v[52:55], v[12:15]\n"
    "v_mfma_f32_16x16x32_bf16 v[16:19], v[132:135], v[52:55], v[16:19]\n"
    "s_waitcnt lgkmcnt(4)\n"
    "v_mfma_f32_16x16x32_bf16 v[8:11], v[136:139], v[56:59], v[8:11]\n"
    "ds_read_b128 v[52:55], v0 offset:5120\n"
    "ds_read_b128 v[48:51], v0 offset:4096\n"
    "v_mfma_f32_16x16x32_bf16 v[12:15], v[140:143], v[56:59], v[12:15]\n"
    "v_mfma_f32_16x16x32_bf16 v[16:19], v[144:147], v[56:59], v[16:19]\n"
    "v_mfma_f32_16x16x32_bf16 v[8:11], v[148:151], v[60:63], v[8:11]\n"
    "v_mfma_f32_16x16x32_bf16 v[12:15], v[152:155], v[60:63], v[12:15]\n"
    "v_mfma_f32_16x16x32_bf16 v[16:19], v[156:159], v[60:63], v[16:19]\n"
    "s_waitcnt lgkmcnt(4)\n"
    "v_mfma_f32_16x16x32_bf16 v[8:11], v[160:163], v[32:35], v[8:11]\n"
    "ds_read_b128 v[60:63], v0 offset:7168\n"
    "ds_read_b128 v[56:59], v0 offset:6144\n"
    "v_mfma_f32_16x16x32_bf16 v[12:15], v[164:167], v[32:35], v[12:15]\n"
    "s_cmp_eq_u32 s94, 0\n"
    "s_cbranch_scc1 .Lwk1_%=\n"
    ".Lwk1_x%=:\n"
    "v_mfma_f32_16x16x32_bf16 v[16:19], v[168:171], v[32:35], v[16:19]\n"
    "v_mfma_f32_16x16x32_bf16 v[8:11], v[172:175], v[36:39], v[8:11]\n"
    "v_mfma_f32_16x16x32_bf16 v[12:15], v[176:179], v[36:39], v[12:15]\n"
    "v_mfma_f32_16x16x32_bf16 v[16:19], v[180:183], v[36:39], v[16:19]\n"
    "s_waitcnt lgkmcnt(4)\n"
    "v_mfma_f32_16x16x32_bf16 v[8:11], v[184:187], v[40:43], v[8:11]\n"
    "v_mfma_f32_16x16x32_bf16 v[12:15], v[188:191], v[40:43], v[12:15]\n"
    "v_mfma_f32_16x16x32_bf16 v[16:19], v[192:195], v[40:43], v[16:19]\n"
    "v_mfma_f32_16x16x32_bf16 v[8:11], v[196:199], v[44:47], v[8:11]\n"
    "v_xor_b32 v1, 0x8000, v1\n"
    "v_mfma_f32_16x16x32_bf16 v[12:15], v[200:203], v[44:47], v[12:15]\n"
    "s_waitcnt lgkmcnt(2)\n"
    "v_mfma_f32_16x16x32_bf16 v[8:11], v[208:211], v[48:51], v[8:11]\n"
    "v_mfma_f32_16x16x32_bf16 v[12:15], v[212:215], v[48:51], v[12:15]\n"
    "v_mfma_f32_16x16x32_bf16 v[8:11], v[220:223], v[52:55], v[8:11]\n"
    "v_mfma_f32_16x16x32_bf16 v[12:15], v[224:227], v[52:55], v[12:15]\n"
    "s_waitcnt lgkmcnt(0)\n"
    "v_mfma_f32_16x16x32_bf16 v[8:11], v[232:235], v[56:59], v[8:11]\n"
    "v_mfma_f32_16x16x32_bf16 v[12:15], v[236:239], v[56:59], v[12:15]\n"
    "v_mfma_f32_16x16x32_bf16 v[8:11], v[244:247], v[60:63], v[8:11]\n"
    "v_mfma_f32_16x16x32_bf16 v[12:15], v[248:251], v[60:63], v[12:15]\n"
    "v_mfma_f32_16x16x32_bf16 v[16:19], v[204:207], v[44:47], v[16:19]\n"
    "v_mfma_f32_16x16x32_bf16 v[16:19], v[216:219], v[48:51], v[16:19]\n"
    "v_cvt_pk_bf16_f32 v24, v8, v9\n"
    "v_cvt_pk_bf16_f32 v25, v10, v11\n"
    "v_mfma_f32_16x16x32_bf16 v[16:19], v[228:231], v[52:55], v[16:19]\n"
    "v_cvt_pk_bf16_f32 v26, v12, v13\n"
    "v_cvt_pk_bf16_f32 v27, v14, v15\n"
    "v_mfma_f32_16x16x32_bf16 v[16:19], v[240:243], v[56:59], v[16:19]\n"
    "buffer_store_dwordx4 v[24:27], v2, s[40:43], s62 offen sc1\n"
    "v_mfma_f32_16x16x32_bf16 v[16:19], v[252:255], v[60:63], v[16:19]\n"
    "s_mov_b32 s56, 1\n"
    "s_cmp_lg_u32 s94, 0\n"
    "s_cbranch_scc1 .Lwgo_%=\n"
    "s_nop 5\n"
    "v_cvt_pk_bf16_f32 v28, v16, v17\n"
    "v_cvt_pk_bf16_f32 v30, v16, v17\n"
    "v_cvt_pk_bf16_f32 v29, v18, v19\n"
    "v_cvt_pk_bf16_f32 v31, v18, v19\n"
    "v_permlane16_swap_b32 v28, v30\n"
    "s_nop 0\n"
    "v_permlane16_swap_b32 v29, v31\n"
    "buffer_store_dwordx4 v[28:31], v3, s[40:43], s62 offen sc1\n"
    "s_waitcnt vmcnt(2)\n"
    "v_cmp_eq_u32 vcc, 0, v21\n"
    "s_cmp_eq_u64 vcc, 0\n"
    "s_cbranch_scc1 .Lwld_%=\n"
    "ds_read_b32 v23, v7 offset:44\n"
    "global_load_dword v21, v20, %1 sc1\n"
    "s_waitcnt vmcnt(0)\n"
    "v_mov_b32 v22, 1\n"
    "s_mov_b64 exec, 1\n"
    "ds_add_rtn_u32 v22, v7, v22 offset:48\n"
    "s_mov_b64 exec, -1\n"
    "s_waitcnt lgkmcnt(0)\n"
    "v_readfirstlane_b32 s50, v22\n"
    "s_cmp_eq_u32 s50, 3\n"
    "s_cselect_b64 exec, 1, 0\n"
    "global_store_dword v23, v22, %1 sc1\n"
    "s_mov_b64 exec, -1\n"
    "s_mov_b32 s56, 0\n"
    "v_cmp_eq_u32 vcc, 0, v21\n"
    "s_cmp_eq_u64 vcc, 0\n"
    "s_cbranch_scc1 .Lwld_%=\n"
    ".Lwchk_%=:\n"
    "v_readfirstlane_b32 s50, v20\n"
    "s_cmp_lt_u32 s50, 200\n"
    "s_cbranch_scc1 .Lwend_%=\n"
    ".Lwwait_%=:\n"
    "global_load_dword v21, v20, %1 sc1\n"
    "s_mov_b32 s52, 0\n"
    ".Lwp2_%=:\n"
    "s_waitcnt vmcnt(0)\n"
    "v_cmp_eq_u32 vcc, 0, v21\n"
    "s_cmp_eq_u64 vcc, 0\n"
    "s_cbranch_scc1 .Lwp2_x%=\n"
    "s_add_u32 s52, s52, 1\n"
    "s_sleep 2\n"
    "global_load_dword v21, v20, %1 sc1\n"
    "s_cmp_lt_u32 s52, s53\n"
    "s_cbranch_scc1 .Lwp2_%=\n"
    "s_mov_b32 s53, 0\n"
    "v_mov_b32 v22, 0\n"
    "v_mov_b32 v23, 1\n"
    "global_store_dword v22, v23, %1 sc1\n"
    "s_waitcnt vmcnt(0)\n"
    ".Lwp2_x%=:\n"
    ".Lwld_%=:\n"
    "s_mov_b32 s94, s42\n"
    "buffer_load_dwordx4 v[64:67], v4, s[92:95], s49 offen sc1\n"
    "buffer_load_dwordx4 v[68:71], v4, s[92:95], s49 offen offset:1024 sc1\n"
    "buffer_load_dwordx4 v[72:75], v4, s[92:95], s49 offen offset:2048 sc1\n"
    "buffer_load_dwordx4 v[76:79], v4, s[92:95], s49 offen offset:3072 sc1\n"
    "buffer_load_dwordx4 v[80:83], v5, s[92:95], s49 offen sc1\n"
    "buffer_load_dwordx4 v[84:87], v5, s[92:95], s49 offen offset:1024 sc1\n"
    "buffer_load_dwordx4 v[88:91], v5, s[92:95], s49 offen offset:2048 sc1\n"
    "buffer_load_dwordx4 v[92:95], v5, s[92:95], s49 offen offset:3072 sc1\n"
    ".Lwin_%=:\n"
    "v_add_u32 v7, 32, v7\n"
    "ds_read_b128 v[20:23], v7\n"
    "s_waitcnt vmcnt(7)\n"
    "v_mfma_f32_16x16x32_bf16 v[8:11], a[0:3], v[64:67], 0\n"
    "v_mfma_f32_16x16x32_bf16 v[12:15], a[4:7], v[64:67], 0\n"
    "ds_write_b128 v1, v[64:67]\n"
    "s_waitcnt vmcnt(6)\n"
    "ds_write_b128 v1, v[68:71] offset:1024\n"
    "v_mfma_f32_16x16x32_bf16 v[8:11], a[12:15], v[68:71], v[8:11]\n"
    "s_waitcnt vmcnt(5)\n"
    "ds_write_b128 v1, v[72:75] offset:2048\n"
    "s_waitcnt vmcnt(4)\n"
    "ds_write_b128 v1, v[76:79] offset:3072\n"
    "v_mfma_f32_16x16x32_bf16 v[12:15], a[16:19], v[68:71], v[12:15]\n"
    "v_mfma_f32_16x16x32_bf16 v[8:11], a[24:27], v[72:75], v[8:11]\n"
    "v_mfma_f32_16x16x32_bf16 v[12:15], a[28:31], v[72:75], v[12:15]\n"
    "s_waitcnt vmcnt(3)\n"
    "ds_write_b128 v1, v[80:83] offset:4096\n"
    "s_waitcnt vmcnt(2)\n"
    "ds_write_b128 v1, v[84:87] offset:5120\n"
    "v_mfma_f32_16x16x32_bf16 v[8:11], a[36:39], v[76:79], v[8:11]\n"
    "v_mfma_f32_16x16x32_bf16 v[12:15], a[40:43], v[76:79], v[12:15]\n"
    "s_waitcnt vmcnt(1)\n"
    "ds_write_b128 v1, v[88:91] offset:6144\n"
    "s_waitcnt vmcnt(0)\n"
    "ds_write_b128 v1, v[92:95] offset:7168\n"
    "v_mfma_f32_16x16x32_bf16 v[8:11], a[48:51], v[80:83], v[8:11]\n"
    "v_xor_b32 v0, 0x2000, v1\n"
    "v_mfma_f32_16x16x32_bf16 v[12:15], a[52:55], v[80:83], v[12:15]\n"
    "s_waitcnt lgkmcnt(8)\n"
    "v_readfirstlane_b32 s49, v21\n"
    "v_mfma_f32_16x16x32_bf16 v[8:11], a[60:63], v[84:87], v[8:11]\n"
    "v_readfirstlane_b32 s62, v22\n"
    "v_add_u32 v20, v20, v6\n"
    "v_mfma_f32_16x16x32_bf16 v[12:15], a[64:67], v[84:87], v[12:15]\n"
    "s_cmp_eq_u32 s56, 0\n"
    "s_cbranch_scc1 .Lwc3_%=\n"
    "s_waitcnt vmcnt(0)\n"
    "v_mov_b32 v22, 1\n"
    "s_mov_b64 exec, 1\n"
    "ds_add_rtn_u32 v22, v7, v22 offset:16\n"
    "s_mov_b64 exec, -1\n"
    "s_waitcnt lgkmcnt(0)\n"
    "v_readfirstlane_b32 s50, v22\n"
    "s_cmp_eq_u32 s50, 3\n"
    "s_cselect_b64 exec, 1, 0\n"
    "global_store_dword v23, v22, %1 sc1\n"
    "s_mov_b64 exec, -1\n"
    "s_mov_b32 s56, 0\n"
    ".Lwc3_%=:\n"
    "s_branch .Lwtop_%=\n"
    ".Lwk1_%=:\n"
    "s_waitcnt vmcnt(8)\n"
    "v_cmp_eq_u32 vcc, 0, v21\n"
    "s_cmp_eq_u64 vcc, 0\n"
    "s_cselect_b32 s94, s42, 0\n"
    "global_load_dword v21, v20, %1 sc1\n"
    "buffer_load_dwordx4 v[64:67], v4, s[92:95], s49 offen sc1\n"
    "buffer_load_dwordx4 v[68:71], v4, s[92:95], s49 offen offset:1024 sc1\n"
    "buffer_load_dwordx4 v[72:75], v4, s[92:95], s49 offen offset:2048 sc1\n"
    "buffer_load_dwordx4 v[76:79], v4, s[92:95], s49 offen offset:3072 sc1\n"
    "buffer_load_dwordx4 v[80:83], v5, s[92:95], s49 offen sc1\n"
    "buffer_load_dwordx4 v[84:87], v5, s[92:95], s49 offen offset:1024 sc1\n"
    "buffer_load_dwordx4 v[88:91], v5, s[92:95], s49 offen offset:2048 sc1\n"
    "buffer_load_dwordx4 v[92:95], v5, s[92:95], s49 offen offset:3072 sc1\n"
    "s_branch .Lwk1_x%=\n"
    ".Lwpol_%=:\n"
    "v_mov_b32 v20, 64\n"
    "global_load_dword v21, v20, %1 sc1\n"
    "s_mov_b32 s52, 0\n"
    ".Lwp4_%=:\n"
    "s_waitcnt vmcnt(0)\n"
    "v_cmp_eq_u32 vcc, 0, v21\n"
    "s_cmp_eq_u64 vcc, 0\n"
    "s_cbranch_scc1 .Lwp4_x%=\n"
    "s_add_u32 s52, s52, 1\n"
    "s_sleep 2\n"
    "global_load_dword v21, v20, %1 sc1\n"
    "s_cmp_lt_u32 s52, s53\n"
    "s_cbranch_scc1 .Lwp4_%=\n"
    "s_mov_b32 s53, 0\n"
    "v_mov_b32 v22, 0\n"
    "v_mov_b32 v23, 1\n"
    "global_store_dword v22, v23, %1 sc1\n"
    "s_waitcnt vmcnt(0)\n"
    ".Lwp4_x%=:\n"
    "v_readfirstlane_b32 s50, v21\n"
    "v_mbcnt_lo_u32_b32 v20, -1, 0\n"
    "v_mbcnt_hi_u32_b32 v20, -1, v20\n"
    "s_lshl_b32 s51, %2, 6\n"
    "v_add_u32 v20, s51, v20\n"
    "v_min_u32 v20, 129, v20\n"
    "v_lshlrev_b32 v20, 5, v20\n"
    "v_add_u32 v20, 0x26c00, v20\n"
    "ds_read2_b32 v[22:23], v20 offset0:1 offset1:2\n"
    "s_and_b32 s50, s50, 1\n"
    "s_waitcnt lgkmcnt(0)\n"
    "v_and_b32 v21, s50, v22\n"
    "v_lshlrev_b32 v21, 13, v21\n"
    "v_and_b32 v22, -2, v22\n"
    "v_xor_b32 v22, v21, v22\n"
    "v_and_b32 v21, s50, v23\n"
    "v_lshlrev_b32 v21, 13, v21\n"
    "v_and_b32 v23, -2, v23\n"
    "v_xor_b32 v23, v21, v23\n"
    "ds_write2_b32 v20, v22, v23 offset0:1 offset1:2\n"
    "s_waitcnt lgkmcnt(0)\n"
    "s_barrier\n"
    "ds_read_b32 v21, v7 offset:4\n"
    "v_add_u32 v20, %4, v6\n"
    "s_waitcnt lgkmcnt(0)\n"
    "v_readfirstlane_b32 s49, v21\n"
    "s_branch .Lwwait_%=\n"
    ".Lwend_%=:\n"
    "s_cmp_eq_u32 s50, 192\n"
    "s_cbranch_scc0 .Lwpol_%=\n"
    "s_waitcnt vmcnt(0) lgkmcnt(0)\n"
    "s_endpgm\n"
    ".p2align 6\n"
    ".Lrole3_%=:\n"
    "s_mov_b64 s[40:41], %0\n"
    "s_and_b32 s41, s41, 0xffff\n"
    "s_mov_b32 s42, 0xbc00000\n"
    "s_mov_b32 s43, 0x20000\n"
    "s_mov_b64 s[92:93], %0\n"
    "s_and_b32 s93, s93, 0xffff\n"
    "s_mov_b32 s94, 0xbc00000\n"
    "s_mov_b32 s95, 0x20000\n"
    "s_mov_b32 s53, 131072\n"
    "s_mov_b64 s[56:57], 0\n"
    "v_mbcnt_lo_u32_b32 v92, -1, 0\n"
    "v_mbcnt_hi_u32_b32 v92, -1, v92\n"
    "s_lshl_b32 s50, %2, 8\n"
    "s_add_u32 s50, s50, 144384\n"
    "v_lshl_add_u32 v92, v92, 2, s50\n"
    "ds_read_b32 v0, v92\n"
    "ds_read_b32 v1, v92 offset:1024\n"
    "ds_read_b32 v2, v92 offset:2048\n"
    "ds_read_b32 v3, v92 offset:3072\n"
    "ds_read_b32 v4, v92 offset:4096\n"
    "ds_read_b32 v73, v92 offset:5120\n"
    "ds_read_b32 v6, v92 offset:6144\n"
    "ds_read_b32 v7, v92 offset:7168\n"
    "ds_read_b32 v8, v92 offset:8192\n"
    "ds_read_b32 v9, v92 offset:9216\n"
    "ds_read_b32 v10, v92 offset:10240\n"
    "ds_read_b32 v95, v92 offset:11264\n"
    "ds_read_b32 v90, v92 offset:12288\n"
    "ds_read_b32 v91, v92 offset:13312\n"
    "s_waitcnt lgkmcnt(0)\n"
    "s_barrier\n"
    "v_mbcnt_lo_u32_b32 v92, -1, 0\n"
    "v_mbcnt_hi_u32_b32 v92, -1, v92\n"
    "v_add_u32 v93, 0, v92\n"
    "v_mul_u32_u24 v94, 7282, v93\n"
    "v_lshrrev_b32 v94, 16, v94\n"
    "v_mul_u32_u24 v94, 3312, v94\n"
    "v_lshl_add_u32 v80, v93, 4, v94\n"
    "v_add_u32 v93, 64, v92\n"
    "v_mul_u32_u24 v94, 7282, v93\n"
    "v_lshrrev_b32 v94, 16, v94\n"
    "v_mul_u32_u24 v94, 3312, v94\n"
    "v_lshl_add_u32 v81, v93, 4, v94\n"
    "v_add_u32 v93, 128, v92\n"
    "v_mul_u32_u24 v94, 7282, v93\n"
    "v_lshrrev_b32 v94, 16, v94\n"
    "v_mul_u32_u24 v94, 3312, v94\n"
    "v_lshl_add_u32 v82, v93, 4, v94\n"
    "v_add_u32 v93, 192, v92\n"
    "v_mul_u32_u24 v94, 7282, v93\n"
    "v_lshrrev_b32 v94, 16, v94\n"
    "v_mul_u32_u24 v94, 3312, v94\n"
    "v_lshl_add_u32 v83, v93, 4, v94\n"
    "v_add_u32 v93, 256, v92\n"
    "v_mul_u32_u24 v94, 7282, v93\n"
    "v_lshrrev_b32 v94, 16, v94\n"
    "v_mul_u32_u24 v94, 3312, v94\n"
    "v_lshl_add_u32 v84, v93, 4, v94\n"
    "v_add_u32 v93, 320, v92\n"
    "v_mul_u32_u24 v94, 7282, v93\n"
    "v_lshrrev_b32 v94, 16, v94\n"
    "v_mul_u32_u24 v94, 3312, v94\n"
    "v_lshl_add_u32 v85, v93, 4, v94\n"
    "v_add_u32 v93, 384, v92\n"
    "v_mul_u32_u24 v94, 7282, v93\n"
    "v_lshrrev_b32 v94, 16, v94\n"
    "v_mul_u32_u24 v94, 3312, v94\n"
    "v_lshl_add_u32 v86, v93, 4, v94\n"
    "v_add_u32 v93, 448, v92\n"
    "v_mul_u32_u24 v94, 7282, v93\n"
    "v_lshrrev_b32 v94, 16, v94\n"
    "v_mul_u32_u24 v94, 3312, v94\n"
    "v_lshl_add_u32 v87, v93, 4, v94\n"
    "v_add_u32 v93, 512, v92\n"
    "v_mul_u32_u24 v94, 7282, v93\n"
    "v_lshrrev_b32 v94, 16, v94\n"
    "v_mul_u32_u24 v94, 3312, v94\n"
    "v_lshl_add_u32 v88, v93, 4, v94\n"
    "v_and_b32 v72, 48, v92\n"
    "v_add_u32 v5, 0x21c00, v73\n"
    "s_min_u32 s50, %2, 2\n"
    "s_mul_i32 s54, s50, 9216\n"
    "s_mul_i32 s50, s50, 1152\n"
    "s_add_u32 s51, s50, %6\n"
    "s_mov_b64 s[44:45], %8\n"
    "s_and_b32 s45, s45, 0xffff\n"
    "s_mov_b32 s46, 0x1e6000\n"
    "s_mov_b32 s47, 0x20000\n"
    "v_mov_b32 v56, 0x3f803f80\n"
    "v_mov_b32 v57, 0x3f803f80\n"
    "v_mov_b32 v58, 0x3f803f80\n"
    "v_mov_b32 v59, 0x3f803f80\n"
    "s_cmp_eq_u32 %2, 3\n"
    "s_cbranch_scc1 .Lac1_%=\n"
    "s_add_u32 s55, s51, 0\n"
    "s_add_u32 m0, s54, 0\n"
    "s_nop 0\n"
    "buffer_load_dwordx4 v80, s[44:47], s55 offen lds\n"
    "s_add_u32 m0, s54, 1024\n"
    "s_nop 0\n"
    "buffer_load_dwordx4 v81, s[44:47], s55 offen lds\n"
    "s_add_u32 m0, s54, 2048\n"
    "s_nop 0\n"
    "buffer_load_dwordx4 v82, s[44:47], s55 offen lds\n"
    "s_add_u32 m0, s54, 3072\n"
    "s_nop 0\n"
    "buffer_load_dwordx4 v83, s[44:47], s55 offen lds\n"
    "s_add_u32 m0, s54, 4096\n"
    "s_nop 0\n"
    "buffer_load_dwordx4 v84, s[44:47], s55 offen lds\n"
    "s_add_u32 m0, s54, 5120\n"
    "s_nop 0\n"
    "buffer_load_dwordx4 v85, s[44:47], s55 offen lds\n"
    "s_add_u32 m0, s54, 6144\n"
    "s_nop 0\n"
    "buffer_load_dwordx4 v86, s[44:47], s55 offen lds\n"
    "s_add_u32 m0, s54, 7168\n"
    "s_nop 0\n"
    "buffer_load_dwordx4 v87, s[44:47], s55 offen lds\n"
    "s_add_u32 m0, s54, 8192\n"
    "s_nop 0\n"
    "buffer_load_dwordx4 v88, s[44:47], s55 offen lds\n"
    ".Lac1_%=:\n"
    "s_cmp_eq_u32 %2, 3\n"
    "s_cbranch_scc1 .Lac2_%=\n"
    "s_add_u32 s55, s51, 221184\n"
    "s_add_u32 m0, s54, 27648\n"
    "s_nop 0\n"
    "buffer_load_dwordx4 v80, s[44:47], s55 offen lds\n"
    "s_add_u32 m0, s54, 28672\n"
    "s_nop 0\n"
    "buffer_load_dwordx4 v81, s[44:47], s55 offen lds\n"
    "s_add_u32 m0, s54, 29696\n"
    "s_nop 0\n"
    "buffer_load_dwordx4 v82, s[44:47], s55 offen lds\n"
    "s_add_u32 m0, s54, 30720\n"
    "s_nop 0\n"
    "buffer_load_dwordx4 v83, s[44:47], s55 offen lds\n"
    "s_add_u32 m0, s54, 31744\n"
    "s_nop 0\n"
    "buffer_load_dwordx4 v84, s[44:47], s55 offen lds\n"
    "s_add_u32 m0, s54, 32768\n"
    "s_nop 0\n"
    "buffer_load_dwordx4 v85, s[44:47], s55 offen lds\n"
    "s_add_u32 m0, s54, 33792\n"
    "s_nop 0\n"
    "buffer_load_dwordx4 v86, s[44:47], s55 offen lds\n"
    "s_add_u32 m0, s54, 34816\n"
    "s_nop 0\n"
    "buffer_load_dwordx4 v87, s[44:47], s55 offen lds\n"
    "s_add_u32 m0, s54, 35840\n"
    "s_nop 0\n"
    "buffer_load_dwordx4 v88, s[44:47], s55 offen lds\n"
    ".Lac2_%=:\n"
    "s_cmp_eq_u32 %2, 3\n"
    "s_cbranch_scc1 .Lac3_%=\n"
    "s_add_u32 s55, s51, 442368\n"
    "s_add_u32 m0, s54, 55296\n"
    "s_nop 0\n"
    "buffer_load_dwordx4 v80, s[44:47], s55 offen lds\n"
    "s_add_u32 m0, s54, 56320\n"
    "s_nop 0\n"
    "buffer_load_dwordx4 v81, s[44:47], s55 offen lds\n"
    "s_add_u32 m0, s54, 57344\n"
    "s_nop 0\n"
    "buffer_load_dwordx4 v82, s[44:47], s55 offen lds\n"
    "s_add_u32 m0, s54, 58368\n"
    "s_nop 0\n"
    "buffer_load_dwordx4 v83, s[44:47], s55 offen lds\n"
    "s_add_u32 m0, s54, 59392\n"
    "s_nop 0\n"
    "buffer_load_dwordx4 v84, s[44:47], s55 offen lds\n"
    "s_add_u32 m0, s54, 60416\n"
    "s_nop 0\n"
    "buffer_load_dwordx4 v85, s[44:47], s55 offen lds\n"
    "s_add_u32 m0, s54, 61440\n"
    "s_nop 0\n"
    "buffer_load_dwordx4 v86, s[44:47], s55 offen lds\n"
    "s_add_u32 m0, s54, 62464\n"
    "s_nop 0\n"
    "buffer_load_dwordx4 v87, s[44:47], s55 offen lds\n"
    "s_add_u32 m0, s54, 63488\n"
    "s_nop 0\n"
    "buffer_load_dwordx4 v88, s[44:47], s55 offen lds\n"
    ".Lac3_%=:\n"
    "s_cmp_eq_u32 %2, 3\n"
    "s_cbranch_scc1 .Lac4_%=\n"
    "s_add_u32 s55, s51, 663552\n"
    "s_add_u32 m0, s54, 82944\n"
    "s_nop 0\n"
    "buffer_load_dwordx4 v80, s[44:47], s55 offen lds\n"
    "s_add_u32 m0, s54, 83968\n"
    "s_nop 0\n"
    "buffer_load_dwordx4 v81, s[44:47], s55 offen lds\n"
    "s_add_u32 m0, s54, 84992\n"
    "s_nop 0\n"
    "buffer_load_dwordx4 v82, s[44:47], s55 offen lds\n"
    "s_add_u32 m0, s54, 86016\n"
    "s_nop 0\n"
    "buffer_load_dwordx4 v83, s[44:47], s55 offen lds\n"
    "s_add_u32 m0, s54, 87040\n"
    "s_nop 0\n"
    "buffer_load_dwordx4 v84, s[44:47], s55 offen lds\n"
    "s_add_u32 m0, s54, 88064\n"
    "s_nop 0\n"
    "buffer_load_dwordx4 v85, s[44:47], s55 offen lds\n"
    "s_add_u32 m0, s54, 89088\n"
    "s_nop 0\n"
    "buffer_load_dwordx4 v86, s[44:47], s55 offen lds\n"
    "s_add_u32 m0, s54, 90112\n"
    "s_nop 0\n"
    "buffer_load_dwordx4 v87, s[44:47], s55 offen lds\n"
    "s_add_u32 m0, s54, 91136\n"
    "s_nop 0\n"
    "buffer_load_dwordx4 v88, s[44:47], s55 offen lds\n"
    ".Lac4_%=:\n"
    "s_cmp_eq_u32 %2, 3\n"
    "s_cbranch_scc1 .Lac5_%=\n"
    "s_add_u32 s55, s51, 884736\n"
    "s_add_u32 m0, s54, 110592\n"
    "s_nop 0\n"
    "buffer_load_dwordx4 v80, s[44:47], s55 offen lds\n"
    "s_add_u32 m0, s54, 111616\n"
    "s_nop 0\n"
    "buffer_load_dwordx4 v81, s[44:47], s55 offen lds\n"
    "s_add_u32 m0, s54, 112640\n"
    "s_nop 0\n"
    "buffer_load_dwordx4 v82, s[44:47], s55 offen lds\n"
    "s_add_u32 m0, s54, 113664\n"
    "s_nop 0\n"
    "buffer_load_dwordx4 v83, s[44:47], s55 offen lds\n"
    "s_add_u32 m0, s54, 114688\n"
    "s_nop 0\n"
    "buffer_load_dwordx4 v84, s[44:47], s55 offen lds\n"
    "s_add_u32 m0, s54, 115712\n"
    "s_nop 0\n"
    "buffer_load_dwordx4 v85, s[44:47], s55 offen lds\n"
    "s_add_u32 m0, s54, 116736\n"
    "s_nop 0\n"
    "buffer_load_dwordx4 v86, s[44:47], s55 offen lds\n"
    "s_add_u32 m0, s54, 117760\n"
    "s_nop 0\n"
    "buffer_load_dwordx4 v87, s[44:47], s55 offen lds\n"
    "s_add_u32 m0, s54, 118784\n"
    "s_nop 0\n"
    "buffer_load_dwordx4 v88, s[44:47], s55 offen lds\n"
    ".Lac5_%=:\n"
    "s_waitcnt vmcnt(36) lgkmcnt(0)\n"
    "s_barrier\n"
    "ds_read_b64_tr_b16 a[0:1], v90\n"
    "ds_read_b64_tr_b16 a[2:3], v90 offset:576\n"
    "ds_read_b64_tr_b16 a[72:73], v90 offset:9216\n"
    "ds_read_b64_tr_b16 a[74:75], v90 offset:9792\n"
    "ds_read_b64_tr_b16 a[144:145], v90 offset:18432\n"
    "ds_read_b64_tr_b16 a[146:147], v90 offset:19008\n"
    "ds_read_b64_tr_b16 a[216:217], v91\n"
    "ds_read_b64_tr_b16 a[218:219], v91 offset:576\n"
    "ds_read_b64_tr_b16 a[4:5], v90 offset:4608\n"
    "ds_read_b64_tr_b16 a[6:7], v90 offset:5184\n"
    "ds_read_b64_tr_b16 a[76:77], v90 offset:13824\n"
    "ds_read_b64_tr_b16 a[78:79], v90 offset:14400\n"
    "ds_read_b64_tr_b16 a[148:149], v90 offset:23040\n"
    "ds_read_b64_tr_b16 a[150:151], v90 offset:23616\n"
    "ds_read_b64_tr_b16 a[220:221], v91 offset:4608\n"
    "ds_read_b64_tr_b16 a[222:223], v91 offset:5184\n"
    "s_waitcnt vmcnt(27) lgkmcnt(0)\n"
    "s_barrier\n"
    "s_cmp_eq_u32 %2, 3\n"
    "s_cbranch_scc1 .Lac6_%=\n"
    "s_add_u32 s55, s51, 1105920\n"
    "s_add_u32 m0, s54, 0\n"
    "s_nop 0\n"
    "buffer_load_dwordx4 v80, s[44:47], s55 offen lds\n"
    "s_add_u32 m0, s54, 1024\n"
    "s_nop 0\n"
    "buffer_load_dwordx4 v81, s[44:47], s55 offen lds\n"
    "s_add_u32 m0, s54, 2048\n"
    "s_nop 0\n"
    "buffer_load_dwordx4 v82, s[44:47], s55 offen lds\n"
    "s_add_u32 m0, s54, 3072\n"
    "s_nop 0\n"
    "buffer_load_dwordx4 v83, s[44:47], s55 offen lds\n"
    "s_add_u32 m0, s54, 4096\n"
    "s_nop 0\n"
    "buffer_load_dwordx4 v84, s[44:47], s55 offen lds\n"
    "s_add_u32 m0, s54, 5120\n"
    "s_nop 0\n"
    "buffer_load_dwordx4 v85, s[44:47], s55 offen lds\n"
    "s_add_u32 m0, s54, 6144\n"
    "s_nop 0\n"
    "buffer_load_dwordx4 v86, s[44:47], s55 offen lds\n"
    "s_add_u32 m0, s54, 7168\n"
    "s_nop 0\n"
    "buffer_load_dwordx4 v87, s[44:47], s55 offen lds\n"
    "s_add_u32 m0, s54, 8192\n"
    "s_nop 0\n"
    "buffer_load_dwordx4 v88, s[44:47], s55 offen lds\n"
    ".Lac6_%=:\n"
    "v_mfma_f32_16x16x32_bf16 v[12:15], v[56:59], a[0:3], 0\n"
    "v_mfma_f32_16x16x32_bf16 v[16:19], v[56:59], a[72:75], 0\n"
    "v_mfma_f32_16x16x32_bf16 v[20:23], v[56:59], a[144:147], 0\n"
    "v_mfma_f32_16x16x32_bf16 v[24:27], v[56:59], a[216:219], 0\n"
    "v_mfma_f32_16x16x32_bf16 v[12:15], v[56:59], a[4:7], v[12:15]\n"
    "v_mfma_f32_16x16x32_bf16 v[16:19], v[56:59], a[76:79], v[16:19]\n"
    "v_mfma_f32_16x16x32_bf16 v[20:23], v[56:59], a[148:151], v[20:23]\n"
    "v_mfma_f32_16x16x32_bf16 v[24:27], v[56:59], a[220:223], v[24:27]\n"
    "ds_read_b64_tr_b16 a[8:9], v90 offset:27648\n"
    "ds_read_b64_tr_b16 a[10:11], v90 offset:28224\n"
    "ds_read_b64_tr_b16 a[80:81], v90 offset:36864\n"
    "ds_read_b64_tr_b16 a[82:83], v90 offset:37440\n"
    "ds_read_b64_tr_b16 a[152:153], v90 offset:46080\n"
    "ds_read_b64_tr_b16 a[154:155], v90 offset:46656\n"
    "ds_read_b64_tr_b16 a[224:225], v91 offset:27648\n"
    "ds_read_b64_tr_b16 a[226:227], v91 offset:28224\n"
    "ds_read_b64_tr_b16 a[12:13], v90 offset:32256\n"
    "ds_read_b64_tr_b16 a[14:15], v90 offset:32832\n"
    "ds_read_b64_tr_b16 a[84:85], v90 offset:41472\n"
    "ds_read_b64_tr_b16 a[86:87], v90 offset:42048\n"
    "ds_read_b64_tr_b16 a[156:157], v90 offset:50688\n"
    "ds_read_b64_tr_b16 a[158:159], v90 offset:51264\n"
    "ds_read_b64_tr_b16 a[228:229], v91 offset:32256\n"
    "ds_read_b64_tr_b16 a[230:231], v91 offset:32832\n"
    "s_waitcnt vmcnt(27) lgkmcnt(0)\n"
    "s_barrier\n"
    "s_cmp_eq_u32 %2, 3\n"
    "s_cbranch_scc1 .Lac7_%=\n"
    "s_add_u32 s55, s51, 1327104\n"
    "s_add_u32 m0, s54, 27648\n"
    "s_nop 0\n"
    "buffer_load_dwordx4 v80, s[44:47], s55 offen lds\n"
    "s_add_u32 m0, s54, 28672\n"
    "s_nop 0\n"
    "buffer_load_dwordx4 v81, s[44:47], s55 offen lds\n"
    "s_add_u32 m0, s54, 29696\n"
    "s_nop 0\n"
    "buffer_load_dwordx4 v82, s[44:47], s55 offen lds\n"
    "s_add_u32 m0, s54, 30720\n"
    "s_nop 0\n"
    "buffer_load_dwordx4 v83, s[44:47], s55 offen lds\n"
    "s_add_u32 m0, s54, 31744\n"
    "s_nop 0\n"
    "buffer_load_dwordx4 v84, s[44:47], s55 offen lds\n"
    "s_add_u32 m0, s54, 32768\n"
    "s_nop 0\n"
    "buffer_load_dwordx4 v85, s[44:47], s55 offen lds\n"
    "s_add_u32 m0, s54, 33792\n"
    "s_nop 0\n"
    "buffer_load_dwordx4 v86, s[44:47], s55 offen lds\n"
    "s_add_u32 m0, s54, 34816\n"
    "s_nop 0\n"
    "buffer_load_dwordx4 v87, s[44:47], s55 offen lds\n"
    "s_add_u32 m0, s54, 35840\n"
    "s_nop 0\n"
    "buffer_load_dwordx4 v88, s[44:47], s55 offen lds\n"
    ".Lac7_%=:\n"
    "v_mfma_f32_16x16x32_bf16 v[12:15], v[56:59], a[8:11], v[12:15]\n"
    "v_mfma_f32_16x16x32_bf16 v[16:19], v[56:59], a[80:83], v[16:19]\n"
    "v_mfma_f32_16x16x32_bf16 v[20:23], v[56:59], a[152:155], v[20:23]\n"
    "v_mfma_f32_16x16x32_bf16 v[24:27], v[56:59], a[224:227], v[24:27]\n"
    "v_mfma_f32_16x16x32_bf16 v[12:15], v[56:59], a[12:15], v[12:15]\n"
    "v_mfma_f32_16x16x32_bf16 v[16:19], v[56:59], a[84:87], v[16:19]\n"
    "v_mfma_f32_16x16x32_bf16 v[20:23], v[56:59], a[156:159], v[20:23]\n"
    "v_mfma_f32_16x16x32_bf16 v[24:27], v[56:59], a[228:231], v[24:27]\n"
    "ds_read_b64_tr_b16 a[16:17], v90 offset:55296\n"
    "ds_read_b64_tr_b16 a[18:19], v90 offset:55872\n"
    "ds_read_b64_tr_b16 a[88:89], v90 offset:64512\n"
    "ds_read_b64_tr_b16 a[90:91], v90 offset:65088\n"
    "v_add_u32 v99, 0x10000, v90\n"
    "ds_read_b64_tr_b16 a[160:161], v99 offset:8192\n"
    "ds_read_b64_tr_b16 a[162:163], v99 offset:8768\n"
    "ds_read_b64_tr_b16 a[232:233], v91 offset:55296\n"
    "ds_read_b64_tr_b16 a[234:235], v91 offset:55872\n"
    "ds_read_b64_tr_b16 a[20:21], v90 offset:59904\n"
    "ds_read_b64_tr_b16 a[22:23], v90 offset:60480\n"
    "ds_read_b64_tr_b16 a[92:93], v99 offset:3584\n"
    "ds_read_b64_tr_b16 a[94:95], v99 offset:4160\n"
    "ds_read_b64_tr_b16 a[164:165], v99 offset:12800\n"
    "ds_read_b64_tr_b16 a[166:167], v99 offset:13376\n"
    "ds_read_b64_tr_b16 a[236:237], v91 offset:59904\n"
    "ds_read_b64_tr_b16 a[238:239], v91 offset:60480\n"
    "s_waitcnt vmcnt(27) lgkmcnt(0)\n"
    "s_barrier\n"
    "s_cmp_eq_u32 %2, 3\n"
    "s_cbranch_scc1 .Lac8_%=\n"
    "s_add_u32 s55, s51, 1548288\n"
    "s_add_u32 m0, s54, 55296\n"
    "s_nop 0\n"
    "buffer_load_dwordx4 v80, s[44:47], s55 offen lds\n"
    "s_add_u32 m0, s54, 56320\n"
    "s_nop 0\n"
    "buffer_load_dwordx4 v81, s[44:47], s55 offen lds\n"
    "s_add_u32 m0, s54, 57344\n"
    "s_nop 0\n"
    "buffer_load_dwordx4 v82, s[44:47], s55 offen lds\n"
    "s_add_u32 m0, s54, 58368\n"
    "s_nop 0\n"
    "buffer_load_dwordx4 v83, s[44:47], s55 offen lds\n"
    "s_add_u32 m0, s54, 59392\n"
    "s_nop 0\n"
    "buffer_load_dwordx4 v84, s[44:47], s55 offen lds\n"
    "s_add_u32 m0, s54, 60416\n"
    "s_nop 0\n"
    "buffer_load_dwordx4 v85, s[44:47], s55 offen lds\n"
    "s_add_u32 m0, s54, 61440\n"
    "s_nop 0\n"
    "buffer_load_dwordx4 v86, s[44:47], s55 offen lds\n"
    "s_add_u32 m0, s54, 62464\n"
    "s_nop 0\n"
    "buffer_load_dwordx4 v87, s[44:47], s55 offen lds\n"
    "s_add_u32 m0, s54, 63488\n"
    "s_nop 0\n"
    "buffer_load_dwordx4 v88, s[44:47], s55 offen lds\n"
    ".Lac8_%=:\n"
    "v_mfma_f32_16x16x32_bf16 v[12:15], v[56:59], a[16:19], v[12:15]\n"
    "v_mfma_f32_16x16x32_bf16 v[16:19], v[56:59], a[88:91], v[16:19]\n"
    "v_mfma_f32_16x16x32_bf16 v[20:23], v[56:59], a[160:163], v[20:23]\n"
    "v_mfma_f32_16x16x32_bf16 v[24:27], v[56:59], a[232:235], v[24:27]\n"
    "v_mfma_f32_16x16x32_bf16 v[12:15], v[56:59], a[20:23], v[12:15]\n"
    "v_mfma_f32_16x16x32_bf16 v[16:19], v[56:59], a[92:95], v[16:19]\n"
    "v_mfma_f32_16x16x32_bf16 v[20:23], v[56:59], a[164:167], v[20:23]\n"
    "v_mfma_f32_16x16x32_bf16 v[24:27], v[56:59], a[236:239], v[24:27]\n"
    "ds_read_b64_tr_b16 a[24:25], v99 offset:17408\n"
    "ds_read_b64_tr_b16 a[26:27], v99 offset:17984\n"
    "ds_read_b64_tr_b16 a[96:97], v99 offset:26624\n"
    "ds_read_b64_tr_b16 a[98:99], v99 offset:27200\n"
    "ds_read_b64_tr_b16 a[168:169], v99 offset:35840\n"
    "ds_read_b64_tr_b16 a[170:171], v99 offset:36416\n"
    "v_add_u32 v98, 0x10000, v91\n"
    "ds_read_b64_tr_b16 a[240:241], v98 offset:17408\n"
    "ds_read_b64_tr_b16 a[242:243], v98 offset:17984\n"
    "ds_read_b64_tr_b16 a[28:29], v99 offset:22016\n"
    "ds_read_b64_tr_b16 a[30:31], v99 offset:22592\n"
    "ds_read_b64_tr_b16 a[100:101], v99 offset:31232\n"
    "ds_read_b64_tr_b16 a[102:103], v99 offset:31808\n"
    "ds_read_b64_tr_b16 a[172:173], v99 offset:40448\n"
    "ds_read_b64_tr_b16 a[174:175], v99 offset:41024\n"
    "ds_read_b64_tr_b16 a[244:245], v98 offset:22016\n"
    "ds_read_b64_tr_b16 a[246:247], v98 offset:22592\n"
    "s_waitcnt vmcnt(27) lgkmcnt(0)\n"
    "s_barrier\n"
    "s_cmp_eq_u32 %2, 3\n"
    "s_cbranch_scc1 .Lac9_%=\n"
    "s_add_u32 s55, s51, 1769472\n"
    "s_add_u32 m0, s54, 82944\n"
    "s_nop 0\n"
    "buffer_load_dwordx4 v80, s[44:47], s55 offen lds\n"
    "s_add_u32 m0, s54, 83968\n"
    "s_nop 0\n"
    "buffer_load_dwordx4 v81, s[44:47], s55 offen lds\n"
    "s_add_u32 m0, s54, 84992\n"
    "s_nop 0\n"
    "buffer_load_dwordx4 v82, s[44:47], s55 offen lds\n"
    "s_add_u32 m0, s54, 86016\n"
    "s_nop 0\n"
    "buffer_load_dwordx4 v83, s[44:47], s55 offen lds\n"
    "s_add_u32 m0, s54, 87040\n"
    "s_nop 0\n"
    "buffer_load_dwordx4 v84, s[44:47], s55 offen lds\n"
    "s_add_u32 m0, s54, 88064\n"
    "s_nop 0\n"
    "buffer_load_dwordx4 v85, s[44:47], s55 offen lds\n"
    "s_add_u32 m0, s54, 89088\n"
    "s_nop 0\n"
    "buffer_load_dwordx4 v86, s[44:47], s55 offen lds\n"
    "s_add_u32 m0, s54, 90112\n"
    "s_nop 0\n"
    "buffer_load_dwordx4 v87, s[44:47], s55 offen lds\n"
    "s_add_u32 m0, s54, 91136\n"
    "s_nop 0\n"
    "buffer_load_dwordx4 v88, s[44:47], s55 offen lds\n"
    ".Lac9_%=:\n"
    "v_mfma_f32_16x16x32_bf16 v[12:15], v[56:59], a[24:27], v[12:15]\n"
    "v_mfma_f32_16x16x32_bf16 v[16:19], v[56:59], a[96:99], v[16:19]\n"
    "v_mfma_f32_16x16x32_bf16 v[20:23], v[56:59], a[168:171], v[20:23]\n"
    "v_mfma_f32_16x16x32_bf16 v[24:27], v[56:59], a[240:243], v[24:27]\n"
    "v_mfma_f32_16x16x32_bf16 v[12:15], v[56:59], a[28:31], v[12:15]\n"
    "v_mfma_f32_16x16x32_bf16 v[16:19], v[56:59], a[100:103], v[16:19]\n"
    "v_mfma_f32_16x16x32_bf16 v[20:23], v[56:59], a[172:175], v[20:23]\n"
    "v_mfma_f32_16x16x32_bf16 v[24:27], v[56:59], a[244:247], v[24:27]\n"
    "ds_read_b64_tr_b16 a[32:33], v99 offset:45056\n"
    "ds_read_b64_tr_b16 a[34:35], v99 offset:45632\n"
    "ds_read_b64_tr_b16 a[104:105], v99 offset:54272\n"
    "ds_read_b64_tr_b16 a[106:107], v99 offset:54848\n"
    "ds_read_b64_tr_b16 a[176:177], v99 offset:63488\n"
    "ds_read_b64_tr_b16 a[178:179], v99 offset:64064\n"
    "ds_read_b64_tr_b16 a[248:249], v98 offset:45056\n"
    "ds_read_b64_tr_b16 a[250:251], v98 offset:45632\n"
    "ds_read_b64_tr_b16 a[36:37], v99 offset:49664\n"
    "ds_read_b64_tr_b16 a[38:39], v99 offset:50240\n"
    "ds_read_b64_tr_b16 a[108:109], v99 offset:58880\n"
    "ds_read_b64_tr_b16 a[110:111], v99 offset:59456\n"
    "v_add_u32 v97, 0x20000, v90\n"
    "ds_read_b64_tr_b16 a[180:181], v97 offset:2560\n"
    "ds_read_b64_tr_b16 a[182:183], v97 offset:3136\n"
    "ds_read_b64_tr_b16 a[252:253], v98 offset:49664\n"
    "ds_read_b64_tr_b16 a[254:255], v98 offset:50240\n"
    "s_waitcnt vmcnt(27) lgkmcnt(0)\n"
    "s_barrier\n"
    "v_mfma_f32_16x16x32_bf16 v[12:15], v[56:59], a[32:35], v[12:15]\n"
    "v_mfma_f32_16x16x32_bf16 v[16:19], v[56:59], a[104:107], v[16:19]\n"
    "v_mfma_f32_16x16x32_bf16 v[20:23], v[56:59], a[176:179], v[20:23]\n"
    "v_mfma_f32_16x16x32_bf16 v[24:27], v[56:59], a[248:251], v[24:27]\n"
    "v_mfma_f32_16x16x32_bf16 v[12:15], v[56:59], a[36:39], v[12:15]\n"
    "v_mfma_f32_16x16x32_bf16 v[16:19], v[56:59], a[108:111], v[16:19]\n"
    "v_mfma_f32_16x16x32_bf16 v[20:23], v[56:59], a[180:183], v[20:23]\n"
    "v_mfma_f32_16x16x32_bf16 v[24:27], v[56:59], a[252:255], v[24:27]\n"
    "ds_read_b64_tr_b16 a[40:41], v90\n"
    "ds_read_b64_tr_b16 a[42:43], v90 offset:576\n"
    "ds_read_b64_tr_b16 a[112:113], v90 offset:9216\n"
    "ds_read_b64_tr_b16 a[114:115], v90 offset:9792\n"
    "ds_read_b64_tr_b16 a[184:185], v90 offset:18432\n"
    "ds_read_b64_tr_b16 a[186:187], v90 offset:19008\n"
    "ds_read_b64_tr_b16 v[224:225], v91\n"
    "ds_read_b64_tr_b16 v[226:227], v91 offset:576\n"
    "ds_read_b64_tr_b16 a[44:45], v90 offset:4608\n"
    "ds_read_b64_tr_b16 a[46:47], v90 offset:5184\n"
    "ds_read_b64_tr_b16 a[116:117], v90 offset:13824\n"
    "ds_read_b64_tr_b16 a[118:119], v90 offset:14400\n"
    "ds_read_b64_tr_b16 a[188:189], v90 offset:23040\n"
    "ds_read_b64_tr_b16 a[190:191], v90 offset:23616\n"
    "ds_read_b64_tr_b16 v[228:229], v91 offset:4608\n"
    "ds_read_b64_tr_b16 v[230:231], v91 offset:5184\n"
    "s_waitcnt vmcnt(18) lgkmcnt(0)\n"
    "s_barrier\n"
    "v_mfma_f32_16x16x32_bf16 v[12:15], v[56:59], a[40:43], v[12:15]\n"
    "v_mfma_f32_16x16x32_bf16 v[16:19], v[56:59], a[112:115], v[16:19]\n"
    "v_mfma_f32_16x16x32_bf16 v[20:23], v[56:59], a[184:187], v[20:23]\n"
    "v_mfma_f32_16x16x32_bf16 v[24:27], v[56:59], v[224:227], v[24:27]\n"
    "v_mfma_f32_16x16x32_bf16 v[12:15], v[56:59], a[44:47], v[12:15]\n"
    "v_mfma_f32_16x16x32_bf16 v[16:19], v[56:59], a[116:119], v[16:19]\n"
    "v_mfma_f32_16x16x32_bf16 v[20:23], v[56:59], a[188:191], v[20:23]\n"
    "v_mfma_f32_16x16x32_bf16 v[24:27], v[56:59], v[228:231], v[24:27]\n"
    "ds_read_b64_tr_b16 a[48:49], v90 offset:27648\n"
    "ds_read_b64_tr_b16 a[50:51], v90 offset:28224\n"
    "ds_read_b64_tr_b16 a[120:121], v90 offset:36864\n"
    "ds_read_b64_tr_b16 a[122:123], v90 offset:37440\n"
    "ds_read_b64_tr_b16 a[192:193], v90 offset:46080\n"
    "ds_read_b64_tr_b16 a[194:195], v90 offset:46656\n"
    "ds_read_b64_tr_b16 v[232:233], v91 offset:27648\n"
    "ds_read_b64_tr_b16 v[234:235], v91 offset:28224\n"
    "ds_read_b64_tr_b16 a[52:53], v90 offset:32256\n"
    "ds_read_b64_tr_b16 a[54:55], v90 offset:32832\n"
    "ds_read_b64_tr_b16 a[124:125], v90 offset:41472\n"
    "ds_read_b64_tr_b16 a[126:127], v90 offset:42048\n"
    "ds_read_b64_tr_b16 a[196:197], v90 offset:50688\n"
    "ds_read_b64_tr_b16 a[198:199], v90 offset:51264\n"
    "ds_read_b64_tr_b16 v[236:237], v91 offset:32256\n"
    "ds_read_b64_tr_b16 v[238:239], v91 offset:32832\n"
    "s_waitcnt vmcnt(9) lgkmcnt(0)\n"
    "s_barrier\n"
    "v_mfma_f32_16x16x32_bf16 v[12:15], v[56:59], a[48:51], v[12:15]\n"
    "v_mfma_f32_16x16x32_bf16 v[16:19], v[56:59], a[120:123], v[16:19]\n"
    "v_mfma_f32_16x16x32_bf16 v[20:23], v[56:59], a[192:195], v[20:23]\n"
    "v_mfma_f32_16x16x32_bf16 v[24:27], v[56:59], v[232:235], v[24:27]\n"
    "v_mfma_f32_16x16x32_bf16 v[12:15], v[56:59], a[52:55], v[12:15]\n"
    "v_mfma_f32_16x16x32_bf16 v[16:19], v[56:59], a[124:127], v[16:19]\n"
    "v_mfma_f32_16x16x32_bf16 v[20:23], v[56:59], a[196:199], v[20:23]\n"
    "v_mfma_f32_16x16x32_bf16 v[24:27], v[56:59], v[236:239], v[24:27]\n"
    "ds_read_b64_tr_b16 a[56:57], v90 offset:55296\n"
    "ds_read_b64_tr_b16 a[58:59], v90 offset:55872\n"
    "ds_read_b64_tr_b16 a[128:129], v90 offset:64512\n"
    "ds_read_b64_tr_b16 a[130:131], v90 offset:65088\n"
    "ds_read_b64_tr_b16 a[200:201], v99 offset:8192\n"
    "ds_read_b64_tr_b16 a[202:203], v99 offset:8768\n"
    "ds_read_b64_tr_b16 v[240:241], v91 offset:55296\n"
    "ds_read_b64_tr_b16 v[242:243], v91 offset:55872\n"
    "ds_read_b64_tr_b16 a[60:61], v90 offset:59904\n"
    "ds_read_b64_tr_b16 a[62:63], v90 offset:60480\n"
    "ds_read_b64_tr_b16 a[132:133], v99 offset:3584\n"
    "ds_read_b64_tr_b16 a[134:135], v99 offset:4160\n"
    "ds_read_b64_tr_b16 a[204:205], v99 offset:12800\n"
    "ds_read_b64_tr_b16 a[206:207], v99 offset:13376\n"
    "ds_read_b64_tr_b16 v[244:245], v91 offset:59904\n"
    "ds_read_b64_tr_b16 v[246:247], v91 offset:60480\n"
    "s_waitcnt vmcnt(0) lgkmcnt(0)\n"
    "s_barrier\n"
    "v_mfma_f32_16x16x32_bf16 v[12:15], v[56:59], a[56:59], v[12:15]\n"
    "v_mfma_f32_16x16x32_bf16 v[16:19], v[56:59], a[128:131], v[16:19]\n"
    "v_mfma_f32_16x16x32_bf16 v[20:23], v[56:59], a[200:203], v[20:23]\n"
    "v_mfma_f32_16x16x32_bf16 v[24:27], v[56:59], v[240:243], v[24:27]\n"
    "v_mfma_f32_16x16x32_bf16 v[12:15], v[56:59], a[60:63], v[12:15]\n"
    "v_mfma_f32_16x16x32_bf16 v[16:19], v[56:59], a[132:135], v[16:19]\n"
    "v_mfma_f32_16x16x32_bf16 v[20:23], v[56:59], a[204:207], v[20:23]\n"
    "v_mfma_f32_16x16x32_bf16 v[24:27], v[56:59], v[244:247], v[24:27]\n"
    "ds_read_b64_tr_b16 a[64:65], v99 offset:17408\n"
    "ds_read_b64_tr_b16 a[66:67], v99 offset:17984\n"
    "ds_read_b64_tr_b16 a[136:137], v99 offset:26624\n"
    "ds_read_b64_tr_b16 a[138:139], v99 offset:27200\n"
    "ds_read_b64_tr_b16 a[208:209], v99 offset:35840\n"
    "ds_read_b64_tr_b16 a[210:211], v99 offset:36416\n"
    "ds_read_b64_tr_b16 v[248:249], v98 offset:17408\n"
    "ds_read_b64_tr_b16 v[250:251], v98 offset:17984\n"
    "ds_read_b64_tr_b16 a[68:69], v99 offset:22016\n"
    "ds_read_b64_tr_b16 a[70:71], v99 offset:22592\n"
    "ds_read_b64_tr_b16 a[140:141], v99 offset:31232\n"
    "ds_read_b64_tr_b16 a[142:143], v99 offset:31808\n"
    "ds_read_b64_tr_b16 a[212:213], v99 offset:40448\n"
    "ds_read_b64_tr_b16 a[214:215], v99 offset:41024\n"
    "ds_read_b64_tr_b16 v[252:253], v98 offset:22016\n"
    "ds_read_b64_tr_b16 v[254:255], v98 offset:22592\n"
    "s_waitcnt lgkmcnt(0)\n"
    "s_barrier\n"
    "v_mfma_f32_16x16x32_bf16 v[12:15], v[56:59], a[64:67], v[12:15]\n"
    "v_mfma_f32_16x16x32_bf16 v[16:19], v[56:59], a[136:139], v[16:19]\n"
    "v_mfma_f32_16x16x32_bf16 v[20:23], v[56:59], a[208:211], v[20:23]\n"
    "v_mfma_f32_16x16x32_bf16 v[24:27], v[56:59], v[248:251], v[24:27]\n"
    "v_mfma_f32_16x16x32_bf16 v[12:15], v[56:59], a[68:71], v[12:15]\n"
    "v_mfma_f32_16x16x32_bf16 v[16:19], v[56:59], a[140:143], v[16:19]\n"
    "v_mfma_f32_16x16x32_bf16 v[20:23], v[56:59], a[212:215], v[20:23]\n"
    "v_mfma_f32_16x16x32_bf16 v[24:27], v[56:59], v[252:255], v[24:27]\n"
    "v_mov_b32 v28, 0\n"
    "v_mov_b32 v29, 0\n"
    "v_mov_b32 v30, 0\n"
    "v_mov_b32 v31, 0\n"
    "v_mov_b32 v32, 0\n"
    "v_mov_b32 v33, 0\n"
    "v_mov_b32 v34, 0\n"
    "v_mov_b32 v35, 0\n"
    "v_mov_b32 v36, 0\n"
    "v_mov_b32 v37, 0\n"
    "v_mov_b32 v38, 0\n"
    "v_mov_b32 v39, 0\n"
    "v_mov_b32 v172, 0\n"
    "v_mov_b32 v173, 0\n"
    "v_mov_b32 v174, 0\n"
    "v_mov_b32 v175, 0\n"
    "v_mov_b32 v176, 0\n"
    "v_mov_b32 v177, 0\n"
    "v_mov_b32 v178, 0\n"
    "v_mov_b32 v199, 0\n"
    "v_mov_b32 v215, 0\n"
    "v_mov_b32 v218, 0\n"
    "v_mov_b32 v219, 0\n"
    "v_mov_b32 v94, 0xf149f2ca\n"
    "v_add_u32 v93, 7, v95\n"
    "v_cmp_gt_u32 vcc, 8, v93\n"
    "s_nop 1\n"
    "v_cndmask_b32 v44, v94, v28, vcc\n"
    "v_cmp_le_i32 vcc, 9, v95\n"
    "s_nop 1\n"
    "v_cndmask_b32 v68, v94, v28, vcc\n"
    "v_add_u32 v93, 8, v95\n"
    "v_cmp_gt_u32 vcc, 8, v93\n"
    "s_nop 1\n"
    "v_cndmask_b32 v45, v94, v28, vcc\n"
    "v_cmp_le_i32 vcc, 8, v95\n"
    "s_nop 1\n"
    "v_cndmask_b32 v69, v94, v28, vcc\n"
    "v_add_u32 v93, 9, v95\n"
    "v_cmp_gt_u32 vcc, 8, v93\n"
    "s_nop 1\n"
    "v_cndmask_b32 v46, v94, v28, vcc\n"
    "v_cmp_le_i32 vcc, 7, v95\n"
    "s_nop 1\n"
    "v_cndmask_b32 v70, v94, v28, vcc\n"
    "v_add_u32 v93, 10, v95\n"
    "v_cmp_gt_u32 vcc, 8, v93\n"
    "s_nop 1\n"
    "v_cndmask_b32 v47, v94, v28, vcc\n"
    "v_cmp_le_i32 vcc, 6, v95\n"
    "s_nop 1\n"
    "v_cndmask_b32 v71, v94, v28, vcc\n"
    "s_mov_b32 s86, 0xcccccccc\n"
    "s_mov_b32 s87, 0xcccccccc\n"
    "v_cvt_pk_bf16_f32 v13, 0, v12\n"
    "v_sub_f32 v12, v12, v13\n"
    "v_cvt_pk_bf16_f32 v14, 0, v12\n"
    "v_sub_f32 v12, v12, v14\n"
    "v_cvt_pk_bf16_f32 v13, v13, v14\n"
    "v_cvt_pk_bf16_f32 v14, v12, 0\n"
    "s_mov_b64 vcc, 0xffff\n"
    "v_cndmask_b32 v60, 0, v13, vcc\n"
    "v_cndmask_b32 v61, 0, v14, vcc\n"
    "v_cvt_pk_bf16_f32 v17, 0, v16\n"
    "v_sub_f32 v16, v16, v17\n"
    "v_cvt_pk_bf16_f32 v18, 0, v16\n"
    "v_sub_f32 v16, v16, v18\n"
    "v_cvt_pk_bf16_f32 v17, v17, v18\n"
    "v_cvt_pk_bf16_f32 v18, v16, 0\n"
    "s_mov_b64 vcc, 0xffff\n"
    "v_cndmask_b32 v62, 0, v17, vcc\n"
    "v_cndmask_b32 v63, 0, v18, vcc\n"
    "v_cvt_pk_bf16_f32 v21, 0, v20\n"
    "v_sub_f32 v20, v20, v21\n"
    "v_cvt_pk_bf16_f32 v22, 0, v20\n"
    "v_sub_f32 v20, v20, v22\n"
    "v_cvt_pk_bf16_f32 v21, v21, v22\n"
    "v_cvt_pk_bf16_f32 v22, v20, 0\n"
    "s_mov_b64 vcc, 0xffff\n"
    "v_cndmask_b32 v64, 0, v21, vcc\n"
    "v_cndmask_b32 v65, 0, v22, vcc\n"
    "v_cvt_pk_bf16_f32 v25, 0, v24\n"
    "v_sub_f32 v24, v24, v25\n"
    "v_cvt_pk_bf16_f32 v26, 0, v24\n"
    "v_sub_f32 v24, v24, v26\n"
    "v_cvt_pk_bf16_f32 v25, v25, v26\n"
    "v_cvt_pk_bf16_f32 v26, v24, 0\n"
    "s_mov_b64 vcc, 0xffff\n"
    "v_cndmask_b32 v66, 0, v25, vcc\n"
    "v_cndmask_b32 v67, 0, v26, vcc\n"
    "v_mbcnt_lo_u32_b32 v76, -1, 0\n"
    "v_mbcnt_hi_u32_b32 v76, -1, v76\n"
    "v_min_u32 v220, 5, v76\n"
    "v_lshlrev_b32 v220, 7, v220\n"
    "v_mov_b32 v11, 0x26c00\n"
    "ds_read_b128 v[76:79], v11\n"
    "s_waitcnt lgkmcnt(0)\n"
    "v_readfirstlane_b32 s49, v77\n"
    "v_add_u32 v76, v76, v220\n"
    "s_branch .Lachk_%=\n"
    ".Lago_%=:\n"
    "s_waitcnt vmcnt(1)\n"
    "v_add_u32 v11, 32, v11\n"
    "ds_read_b128 v[76:79], v11\n"
    "ds_read2_b32 v[74:75], v11 offset0:5 offset1:6\n"
    "ds_write_b128 v1, v[152:155]\n"
    "v_mfma_f32_16x16x32_bf16 v[48:51], v[152:155], v[152:155], 0\n"
    "ds_write_b128 v1, v[156:159] offset:1024\n"
    "v_mfma_f32_16x16x32_bf16 v[48:51], v[156:159], v[156:159], v[48:51]\n"
    "ds_write_b128 v1, v[160:163] offset:2048\n"
    "v_mfma_f32_16x16x32_bf16 v[48:51], v[160:163], v[160:163], v[48:51]\n"
    "ds_write_b128 v1, v[164:167] offset:3072\n"
    "v_mfma_f32_16x16x32_bf16 v[48:51], v[164:167], v[164:167], v[48:51]\n"
    "ds_write_b128 v1, v[168:171] offset:4096\n"
    "v_mfma_f32_16x16x32_bf16 v[48:51], v[168:171], v[168:171], v[48:51]\n"
    "s_waitcnt lgkmcnt(5)\n"
    "v_readfirstlane_b32 s50, v75\n"
    "s_cmp_eq_u32 s50, 0\n"
    "s_cbranch_scc1 .Lac10_%=\n"
    "v_swap_b32 v28, v172\n"
    "v_swap_b32 v29, v173\n"
    "v_swap_b32 v30, v174\n"
    "v_swap_b32 v32, v175\n"
    "v_swap_b32 v33, v176\n"
    "v_swap_b32 v36, v177\n"
    "v_swap_b32 v37, v178\n"
    ".Lac10_%=:\n"
    "v_readfirstlane_b32 s49, v77\n"
    "v_readfirstlane_b32 s62, v78\n"
    "v_add_u32 v76, v76, v220\n"
    "s_waitcnt lgkmcnt(0)\n"
    ".Latop_%=:\n"
    "s_waitcnt lgkmcnt(0)\n"
    "s_barrier\n"
    "s_cmp_eq_u32 %2, 1\n"
    "s_cbranch_scc1 .Laq1_%=\n"
    "s_cmp_eq_u32 %2, 2\n"
    "s_cbranch_scc1 .Laq2_%=\n"
    "s_cmp_eq_u32 %2, 3\n"
    "s_cbranch_scc1 .Laq3_%=\n"
    "ds_read_b128 v[80:83], v0\n"
    "ds_read_b128 v[88:91], v0 offset:2048\n"
    "ds_read_b128 v[84:87], v0 offset:1024\n"
    "ds_read_b128 v[100:103], v0 offset:6144\n"
    "ds_read_b128 v[96:99], v0 offset:5120\n"
    "ds_read_b128 v[92:95], v0 offset:3072\n"
    "ds_read_b128 v[112:115], v0 offset:10240\n"
    "ds_read_b128 v[108:111], v0 offset:8192\n"
    "ds_read_b128 v[104:107], v0 offset:7168\n"
    "global_load_dword v77, v76, %1 sc1\n"
    "ds_write_b128 v73, v[48:51] offset:20480\n"
    "v_mfma_f32_16x16x32_bf16 v[52:55], v[56:59], v[152:155], 0\n"
    "v_mfma_f32_16x16x32_bf16 v[52:55], v[56:59], v[156:159], v[52:55]\n"
    "v_mfma_f32_16x16x32_bf16 v[52:55], v[56:59], v[160:163], v[52:55]\n"
    "v_mfma_f32_16x16x32_bf16 v[52:55], v[56:59], v[164:167], v[52:55]\n"
    "v_mfma_f32_16x16x32_bf16 v[52:55], v[56:59], v[168:171], v[52:55]\n"
    "s_waitcnt lgkmcnt(9)\n"
    "v_mfma_f32_16x16x32_bf16 v[24:27], a[216:219], v[80:83], 0\n"
    "ds_read_b128 v[116:119], v0 offset:11264\n"
    "ds_read_b128 v[120:123], v0 offset:12288\n"
    "s_waitcnt lgkmcnt(9)\n"
    "v_mfma_f32_16x16x32_bf16 v[24:27], a[220:223], v[84:87], v[24:27]\n"
    "ds_read_b128 v[124:127], v0 offset:13312\n"
    "ds_read_b128 v[128:131], v0 offset:15360\n"
    "v_mfma_f32_16x16x32_bf16 v[24:27], a[224:227], v[88:91], v[24:27]\n"
    "ds_write_b128 v73, v[52:55] offset:24576\n"
    "ds_read_b128 v[132:135], v0 offset:16384\n"
    "s_waitcnt lgkmcnt(10)\n"
    "v_mfma_f32_16x16x32_bf16 v[24:27], a[228:231], v[92:95], v[24:27]\n"
    "ds_read_b128 v[136:139], v0 offset:17408\n"
    "ds_read_b128 v[140:143], v0 offset:18432\n"
    "v_mfma_f32_16x16x32_bf16 v[24:27], a[232:235], v[96:99], v[24:27]\n"
    "ds_read_b128 v[144:147], v0 offset:4096\n"
    "ds_read_b128 v[148:151], v0 offset:9216\n"
    "v_mfma_f32_16x16x32_bf16 v[24:27], a[236:239], v[100:103], v[24:27]\n"
    "s_waitcnt lgkmcnt(11)\n"
    "v_mfma_f32_16x16x32_bf16 v[24:27], a[240:243], v[104:107], v[24:27]\n"
    "v_mfma_f32_16x16x32_bf16 v[24:27], a[244:247], v[108:111], v[24:27]\n"
    "s_waitcnt lgkmcnt(0)\n"
    "s_barrier\n"
    "v_mfma_f32_16x16x32_bf16 v[24:27], a[248:251], v[112:115], v[24:27]\n"
    "ds_read2st64_b32 v[48:49], v10 offset0:80 offset1:84\n"
    "ds_read2st64_b32 v[50:51], v10 offset0:88 offset1:92\n"
    "v_mfma_f32_16x16x32_bf16 v[12:15], a[0:3], v[80:83], 0\n"
    "ds_read2st64_b32 v[54:55], v10 offset0:104 offset1:108\n"
    "ds_read2st64_b32 v[52:53], v10 offset0:96 offset1:100\n"
    "v_mfma_f32_16x16x32_bf16 v[16:19], a[72:75], v[80:83], 0\n"
    "v_min_f32 v40, v74, v68\n"
    "v_min_f32 v41, v74, v69\n"
    "v_mfma_f32_16x16x32_bf16 v[12:15], a[4:7], v[84:87], v[12:15]\n"
    "v_min_f32 v42, v74, v70\n"
    "v_min_f32 v43, v74, v71\n"
    "v_mfma_f32_16x16x32_bf16 v[16:19], a[76:79], v[84:87], v[16:19]\n"
    "v_mfma_f32_16x16x32_bf16 v[12:15], a[8:11], v[88:91], v[12:15]\n"
    "s_waitcnt lgkmcnt(0)\n"
    "v_add_f32 v52, v52, v53\n"
    "v_add_f32 v54, v54, v55\n"
    "v_mfma_f32_16x16x32_bf16 v[16:19], a[80:83], v[88:91], v[16:19]\n"
    "v_add_f32 v48, v48, v49\n"
    "v_add_f32 v50, v50, v51\n"
    "v_mfma_f32_16x16x32_bf16 v[12:15], a[12:15], v[92:95], v[12:15]\n"
    "v_add_f32 v52, v52, v54\n"
    "v_add_f32 v50, v48, v50\n"
    "v_mfma_f32_16x16x32_bf16 v[16:19], a[84:87], v[92:95], v[16:19]\n"
    "v_mul_f32 v52, 0xbae38e39, v52\n"
    "v_mul_f32 v50, 0x3ae38e39, v50\n"
    "v_mfma_f32_16x16x32_bf16 v[12:15], a[16:19], v[96:99], v[12:15]\n"
    "v_cvt_pk_bf16_f32 v216, v52, v52\n"
    "v_fma_f32 v50, -v52, v52, v50\n"
    "v_mfma_f32_16x16x32_bf16 v[16:19], a[88:91], v[96:99], v[16:19]\n"
    "v_cvt_pk_bf16_f32 v217, v52, v52\n"
    "v_max_f32 v50, 0, v50\n"
    "v_mfma_f32_16x16x32_bf16 v[12:15], a[20:23], v[100:103], v[12:15]\n"
    "v_cvt_pk_bf16_f32 v50, 0, v50\n"
    "v_add_f32 v50, 0x3727c5ac, v50\n"
    "v_mfma_f32_16x16x32_bf16 v[16:19], a[92:95], v[100:103], v[16:19]\n"
    "v_mfma_f32_16x16x32_bf16 v[24:27], v[66:69], v[216:219], v[24:27]\n"
    "v_cvt_pk_bf16_f32 v50, 0, v50\n"
    "v_mfma_f32_16x16x32_bf16 v[12:15], a[24:27], v[104:107], v[12:15]\n"
    "v_sqrt_f32 v50, v50\n"
    "v_mfma_f32_16x16x32_bf16 v[16:19], a[96:99], v[104:107], v[16:19]\n"
    "v_cvt_pk_bf16_f32 v50, 0, v50\n"
    "v_rcp_f32 v50, v50\n"
    "v_mfma_f32_16x16x32_bf16 v[12:15], a[28:31], v[108:111], v[12:15]\n"
    "ds_write_b32 v9, v50\n"
    "v_mfma_f32_16x16x32_bf16 v[16:19], a[100:103], v[108:111], v[16:19]\n"
    "ds_read_b128 v[200:203], v72 offset:28672\n"
    "v_mfma_f32_16x16x32_bf16 v[12:15], v[60:63], v[216:219], v[12:15]\n"
    "v_mfma_f32_16x16x32_bf16 v[12:15], a[32:35], v[112:115], v[12:15]\n"
    "v_mfma_f32_16x16x32_bf16 v[16:19], v[62:65], v[216:219], v[16:19]\n"
    "v_mul_f32 v24, v24, v50\n"
    "v_mfma_f32_16x16x32_bf16 v[16:19], a[104:107], v[112:115], v[16:19]\n"
    "v_mul_f32 v25, v25, v50\n"
    "v_mul_f32 v26, v26, v50\n"
    "v_mfma_f32_16x16x32_bf16 v[12:15], a[36:39], v[116:119], v[12:15]\n"
    "v_mul_f32 v27, v27, v50\n"
    "ds_write_b128 v5, v[24:27]\n"
    "v_mfma_f32_16x16x32_bf16 v[16:19], a[108:111], v[116:119], v[16:19]\n"
    "v_mfma_f32_16x16x32_bf16 v[12:15], a[40:43], v[120:123], v[12:15]\n"
    "v_mfma_f32_16x16x32_bf16 v[16:19], a[112:115], v[120:123], v[16:19]\n"
    "v_mfma_f32_16x16x32_bf16 v[12:15], a[44:47], v[124:127], v[12:15]\n"
    "s_waitcnt vmcnt(0)\n"
    "v_mov_b32 v78, 1\n"
    "v_mfma_f32_16x16x32_bf16 v[16:19], a[116:119], v[124:127], v[16:19]\n"
    "s_mov_b64 exec, s[56:57]\n"
    "ds_add_rtn_u32 v78, v11, v78 offset:16\n"
    "s_mov_b64 exec, -1\n"
    "v_mfma_f32_16x16x32_bf16 v[12:15], a[48:51], v[128:131], v[12:15]\n"
    "s_waitcnt lgkmcnt(0)\n"
    "s_barrier\n"
    "v_mfma_f32_16x16x32_bf16 v[16:19], a[120:123], v[128:131], v[16:19]\n"
    "ds_read_b128 v[188:191], v8\n"
    "ds_read_b128 v[192:195], v8 offset:1024\n"
    "v_mfma_f32_16x16x32_bf16 v[12:15], a[52:55], v[132:135], v[12:15]\n"
    "ds_read2st64_b64 v[184:187], v7 offset1:2\n"
    "ds_read2st64_b64 v[180:183], v6 offset1:2\n"
    "v_cmp_eq_u32 vcc, 0, v77\n"
    "s_cmp_eq_u64 vcc, 0\n"
    "v_mfma_f32_16x16x32_bf16 v[16:19], a[124:127], v[132:135], v[16:19]\n"
    "s_cselect_b32 s94, s42, 0\n"
    "v_mfma_f32_16x16x32_bf16 v[12:15], a[56:59], v[136:139], v[12:15]\n"
    "global_load_dword v77, v76, %1 sc1\n"
    "v_mfma_f32_16x16x32_bf16 v[16:19], a[128:131], v[136:139], v[16:19]\n"
    "buffer_load_dwordx4 v[152:155], v3, s[92:95], s49 offen sc1\n"
    "v_mfma_f32_16x16x32_bf16 v[12:15], a[60:63], v[140:143], v[12:15]\n"
    "buffer_load_dwordx4 v[156:159], v3, s[92:95], s49 offen offset:1024 sc1\n"
    "v_mfma_f32_16x16x32_bf16 v[16:19], a[132:135], v[140:143], v[16:19]\n"
    "v_readfirstlane_b32 s50, v78\n"
    "v_mfma_f32_16x16x32_bf16 v[12:15], a[64:67], v[144:147], v[12:15]\n"
    "buffer_load_dwordx4 v[160:163], v3, s[92:95], s49 offen offset:2048 sc1\n"
    "s_cmp_eq_u32 s50, 3\n"
    "v_mfma_f32_16x16x32_bf16 v[16:19], a[136:139], v[144:147], v[16:19]\n"
    "s_waitcnt lgkmcnt(0)\n"
    "v_add_f32 v180, v180, v182\n"
    "v_add_f32 v181, v181, v183\n"
    "s_cselect_b64 exec, 1, 0\n"
    "global_store_dword v79, v78, %1 sc1\n"
    "s_mov_b64 exec, -1\n"
    "v_mfma_f32_16x16x32_bf16 v[12:15], a[68:71], v[148:151], v[12:15]\n"
    "v_cvt_pk_bf16_f32 v198, v180, v181\n"
    "v_add_f32 v184, v184, v186\n"
    "buffer_load_dwordx4 v[164:167], v3, s[92:95], s49 offen offset:3072 sc1\n"
    "v_mfma_f32_16x16x32_bf16 v[16:19], a[140:143], v[148:151], v[16:19]\n"
    "v_add_f32 v185, v185, v187\n"
    "v_cvt_pk_bf16_f32 v214, v184, v185\n"
    "buffer_load_dwordx4 v[168:171], v4, s[92:95], s49 offen sc1\n"
    "s_branch .Lak_%=\n"
    ".Laq1_%=:\n"
    "ds_read_b128 v[116:119], v0 offset:11264\n"
    "ds_read_b128 v[124:127], v0 offset:13312\n"
    "ds_read_b128 v[120:123], v0 offset:12288\n"
    "ds_read_b128 v[136:139], v0 offset:17408\n"
    "ds_read_b128 v[132:135], v0 offset:16384\n"
    "ds_read_b128 v[128:131], v0 offset:15360\n"
    "ds_read_b128 v[148:151], v0 offset:9216\n"
    "ds_read_b128 v[144:147], v0 offset:4096\n"
    "ds_read_b128 v[140:143], v0 offset:18432\n"
    "global_load_dword v77, v76, %1 sc1\n"
    "ds_write_b128 v73, v[48:51] offset:20480\n"
    "v_mfma_f32_16x16x32_bf16 v[52:55], v[56:59], v[152:155], 0\n"
    "v_mfma_f32_16x16x32_bf16 v[52:55], v[56:59], v[156:159], v[52:55]\n"
    "v_mfma_f32_16x16x32_bf16 v[52:55], v[56:59], v[160:163], v[52:55]\n"
    "v_mfma_f32_16x16x32_bf16 v[52:55], v[56:59], v[164:167], v[52:55]\n"
    "v_mfma_f32_16x16x32_bf16 v[52:55], v[56:59], v[168:171], v[52:55]\n"
    "s_waitcnt lgkmcnt(9)\n"
    "v_mfma_f32_16x16x32_bf16 v[24:27], a[252:255], v[116:119], 0\n"
    "ds_read_b128 v[80:83], v0\n"
    "ds_read_b128 v[84:87], v0 offset:1024\n"
    "s_waitcnt lgkmcnt(9)\n"
    "v_mfma_f32_16x16x32_bf16 v[24:27], v[224:227], v[120:123], v[24:27]\n"
    "ds_read_b128 v[88:91], v0 offset:2048\n"
    "ds_read_b128 v[92:95], v0 offset:3072\n"
    "v_mfma_f32_16x16x32_bf16 v[24:27], v[228:231], v[124:127], v[24:27]\n"
    "ds_write_b128 v73, v[52:55] offset:24576\n"
    "ds_read_b128 v[96:99], v0 offset:5120\n"
    "s_waitcnt lgkmcnt(10)\n"
    "v_mfma_f32_16x16x32_bf16 v[24:27], v[232:235], v[128:131], v[24:27]\n"
    "ds_read_b128 v[100:103], v0 offset:6144\n"
    "ds_read_b128 v[104:107], v0 offset:7168\n"
    "v_mfma_f32_16x16x32_bf16 v[24:27], v[236:239], v[132:135], v[24:27]\n"
    "ds_read_b128 v[108:111], v0 offset:8192\n"
    "ds_read_b128 v[112:115], v0 offset:10240\n"
    "v_mfma_f32_16x16x32_bf16 v[24:27], v[240:243], v[136:139], v[24:27]\n"
    "s_waitcnt lgkmcnt(11)\n"
    "v_mfma_f32_16x16x32_bf16 v[24:27], v[244:247], v[140:143], v[24:27]\n"
    "v_mfma_f32_16x16x32_bf16 v[24:27], v[248:251], v[144:147], v[24:27]\n"
    "s_waitcnt lgkmcnt(0)\n"
    "s_barrier\n"
    "v_mfma_f32_16x16x32_bf16 v[24:27], v[252:255], v[148:151], v[24:27]\n"
    "ds_read2st64_b32 v[48:49], v10 offset0:80 offset1:84\n"
    "ds_read2st64_b32 v[50:51], v10 offset0:88 offset1:92\n"
    "v_mfma_f32_16x16x32_bf16 v[12:15], a[36:39], v[116:119], 0\n"
    "ds_read2st64_b32 v[54:55], v10 offset0:104 offset1:108\n"
    "ds_read2st64_b32 v[52:53], v10 offset0:96 offset1:100\n"
    "v_mfma_f32_16x16x32_bf16 v[16:19], a[108:111], v[116:119], 0\n"
    "v_min_f32 v40, v74, v68\n"
    "v_min_f32 v41, v74, v69\n"
    "v_mfma_f32_16x16x32_bf16 v[12:15], a[40:43], v[120:123], v[12:15]\n"
    "v_min_f32 v42, v74, v70\n"
    "v_min_f32 v43, v74, v71\n"
    "v_mfma_f32_16x16x32_bf16 v[16:19], a[112:115], v[120:123], v[16:19]\n"
    "v_mfma_f32_16x16x32_bf16 v[12:15], a[44:47], v[124:127], v[12:15]\n"
    "s_waitcnt lgkmcnt(0)\n"
    "v_add_f32 v52, v52, v53\n"
    "v_add_f32 v54, v54, v55\n"
    "v_mfma_f32_16x16x32_bf16 v[16:19], a[116:119], v[124:127], v[16:19]\n"
    "v_add_f32 v48, v48, v49\n"
    "v_add_f32 v50, v50, v51\n"
    "v_mfma_f32_16x16x32_bf16 v[12:15], a[48:51], v[128:131], v[12:15]\n"
    "v_add_f32 v52, v52, v54\n"
    "v_add_f32 v50, v48, v50\n"
    "v_mfma_f32_16x16x32_bf16 v[16:19], a[120:123], v[128:131], v[16:19]\n"
    "v_mul_f32 v52, 0xbae38e39, v52\n"
    "v_mul_f32 v50, 0x3ae38e39, v50\n"
    "v_mfma_f32_16x16x32_bf16 v[12:15], a[52:55], v[132:135], v[12:15]\n"
    "v_cvt_pk_bf16_f32 v216, v52, v52\n"
    "v_fma_f32 v50, -v52, v52, v50\n"
    "v_mfma_f32_16x16x32_bf16 v[16:19], a[124:127], v[132:135], v[16:19]\n"
    "v_cvt_pk_bf16_f32 v217, v52, v52\n"
    "v_max_f32 v50, 0, v50\n"
    "v_mfma_f32_16x16x32_bf16 v[12:15], a[56:59], v[136:139], v[12:15]\n"
    "v_cvt_pk_bf16_f32 v50, 0, v50\n"
    "v_add_f32 v50, 0x3727c5ac, v50\n"
    "v_mfma_f32_16x16x32_bf16 v[16:19], a[128:131], v[136:139], v[16:19]\n"
    "v_cvt_pk_bf16_f32 v50, 0, v50\n"
    "v_sqrt_f32 v50, v50\n"
    "v_mfma_f32_16x16x32_bf16 v[12:15], a[60:63], v[140:143], v[12:15]\n"
    "v_cvt_pk_bf16_f32 v50, 0, v50\n"
    "v_mfma_f32_16x16x32_bf16 v[16:19], a[132:135], v[140:143], v[16:19]\n"
    "v_rcp_f32 v50, v50\n"
    "v_mfma_f32_16x16x32_bf16 v[12:15], a[64:67], v[144:147], v[12:15]\n"
    "ds_write_b32 v9, v50\n"
    "ds_read_b128 v[200:203], v72 offset:28672\n"
    "v_mfma_f32_16x16x32_bf16 v[16:19], a[136:139], v[144:147], v[16:19]\n"
    "v_mfma_f32_16x16x32_bf16 v[12:15], v[60:63], v[216:219], v[12:15]\n"
    "v_mfma_f32_16x16x32_bf16 v[16:19], v[62:65], v[216:219], v[16:19]\n"
    "v_mfma_f32_16x16x32_bf16 v[12:15], a[68:71], v[148:151], v[12:15]\n"
    "v_mul_f32 v24, v24, v50\n"
    "v_mul_f32 v25, v25, v50\n"
    "v_mfma_f32_16x16x32_bf16 v[16:19], a[140:143], v[148:151], v[16:19]\n"
    "v_mul_f32 v26, v26, v50\n"
    "v_mul_f32 v27, v27, v50\n"
    "v_mfma_f32_16x16x32_bf16 v[12:15], a[0:3], v[80:83], v[12:15]\n"
    "ds_write_b128 v5, v[24:27]\n"
    "v_mfma_f32_16x16x32_bf16 v[16:19], a[72:75], v[80:83], v[16:19]\n"
    "v_mfma_f32_16x16x32_bf16 v[12:15], a[4:7], v[84:87], v[12:15]\n"
    "v_mfma_f32_16x16x32_bf16 v[16:19], a[76:79], v[84:87], v[16:19]\n"
    "v_mfma_f32_16x16x32_bf16 v[12:15], a[8:11], v[88:91], v[12:15]\n"
    "s_waitcnt vmcnt(0)\n"
    "v_mov_b32 v78, 1\n"
    "v_mfma_f32_16x16x32_bf16 v[16:19], a[80:83], v[88:91], v[16:19]\n"
    "s_mov_b64 exec, s[56:57]\n"
    "ds_add_rtn_u32 v78, v11, v78 offset:16\n"
    "s_mov_b64 exec, -1\n"
    "v_mfma_f32_16x16x32_bf16 v[12:15], a[12:15], v[92:95], v[12:15]\n"
    "s_waitcnt lgkmcnt(0)\n"
    "s_barrier\n"
    "v_mfma_f32_16x16x32_bf16 v[16:19], a[84:87], v[92:95], v[16:19]\n"
    "ds_read_b128 v[188:191], v8\n"
    "ds_read_b128 v[192:195], v8 offset:1024\n"
    "v_mfma_f32_16x16x32_bf16 v[12:15], a[16:19], v[96:99], v[12:15]\n"
    "ds_read2st64_b64 v[184:187], v7 offset1:2\n"
    "ds_read2st64_b64 v[180:183], v6 offset1:2\n"
    "v_cmp_eq_u32 vcc, 0, v77\n"
    "s_cmp_eq_u64 vcc, 0\n"
    "v_mfma_f32_16x16x32_bf16 v[16:19], a[88:91], v[96:99], v[16:19]\n"
    "s_cselect_b32 s94, s42, 0\n"
    "v_mfma_f32_16x16x32_bf16 v[12:15], a[20:23], v[100:103], v[12:15]\n"
    "global_load_dword v77, v76, %1 sc1\n"
    "v_mfma_f32_16x16x32_bf16 v[16:19], a[92:95], v[100:103], v[16:19]\n"
    "buffer_load_dwordx4 v[152:155], v3, s[92:95], s49 offen sc1\n"
    "v_mfma_f32_16x16x32_bf16 v[12:15], a[24:27], v[104:107], v[12:15]\n"
    "buffer_load_dwordx4 v[156:159], v3, s[92:95], s49 offen offset:1024 sc1\n"
    "v_mfma_f32_16x16x32_bf16 v[16:19], a[96:99], v[104:107], v[16:19]\n"
    "v_readfirstlane_b32 s50, v78\n"
    "v_mfma_f32_16x16x32_bf16 v[12:15], a[28:31], v[108:111], v[12:15]\n"
    "buffer_load_dwordx4 v[160:163], v3, s[92:95], s49 offen offset:2048 sc1\n"
    "s_cmp_eq_u32 s50, 3\n"
    "v_mfma_f32_16x16x32_bf16 v[16:19], a[100:103], v[108:111], v[16:19]\n"
    "s_waitcnt lgkmcnt(0)\n"
    "v_add_f32 v180, v180, v182\n"
    "v_add_f32 v181, v181, v183\n"
    "s_cselect_b64 exec, 1, 0\n"
    "global_store_dword v79, v78, %1 sc1\n"
    "s_mov_b64 exec, -1\n"
    "v_mfma_f32_16x16x32_bf16 v[12:15], a[32:35], v[112:115], v[12:15]\n"
    "v_cvt_pk_bf16_f32 v198, v180, v181\n"
    "v_add_f32 v184, v184, v186\n"
    "buffer_load_dwordx4 v[164:167], v3, s[92:95], s49 offen offset:3072 sc1\n"
    "v_mfma_f32_16x16x32_bf16 v[16:19], a[104:107], v[112:115], v[16:19]\n"
    "v_add_f32 v185, v185, v187\n"
    "v_cvt_pk_bf16_f32 v214, v184, v185\n"
    "buffer_load_dwordx4 v[168:171], v4, s[92:95], s49 offen sc1\n"
    "s_branch .Lak_%=\n"
    ".Laq2_%=:\n"
    "ds_read_b128 v[80:83], v0\n"
    "ds_read_b128 v[88:91], v0 offset:2048\n"
    "ds_read_b128 v[84:87], v0 offset:1024\n"
    "ds_read_b128 v[100:103], v0 offset:6144\n"
    "ds_read_b128 v[96:99], v0 offset:5120\n"
    "ds_read_b128 v[92:95], v0 offset:3072\n"
    "ds_read_b128 v[112:115], v0 offset:10240\n"
    "ds_read_b128 v[108:111], v0 offset:8192\n"
    "ds_read_b128 v[104:107], v0 offset:7168\n"
    "global_load_dword v77, v76, %1 sc1\n"
    "ds_write_b128 v73, v[48:51] offset:20480\n"
    "v_mfma_f32_16x16x32_bf16 v[52:55], v[56:59], v[152:155], 0\n"
    "v_mfma_f32_16x16x32_bf16 v[52:55], v[56:59], v[156:159], v[52:55]\n"
    "v_mfma_f32_16x16x32_bf16 v[52:55], v[56:59], v[160:163], v[52:55]\n"
    "v_mfma_f32_16x16x32_bf16 v[52:55], v[56:59], v[164:167], v[52:55]\n"
    "v_mfma_f32_16x16x32_bf16 v[52:55], v[56:59], v[168:171], v[52:55]\n"
    "s_waitcnt lgkmcnt(9)\n"
    "v_mfma_f32_16x16x32_bf16 v[24:27], v[80:83], a[216:219], 0\n"
    "ds_read_b128 v[116:119], v0 offset:11264\n"
    "ds_read_b128 v[120:123], v0 offset:12288\n"
    "s_waitcnt lgkmcnt(9)\n"
    "v_mfma_f32_16x16x32_bf16 v[24:27], v[84:87], a[220:223], v[24:27]\n"
    "ds_read_b128 v[124:127], v0 offset:13312\n"
    "ds_read_b128 v[128:131], v0 offset:15360\n"
    "v_mfma_f32_16x16x32_bf16 v[24:27], v[88:91], a[224:227], v[24:27]\n"
    "ds_write_b128 v73, v[52:55] offset:24576\n"
    "ds_read_b128 v[132:135], v0 offset:16384\n"
    "s_waitcnt lgkmcnt(10)\n"
    "v_mfma_f32_16x16x32_bf16 v[24:27], v[92:95], a[228:231], v[24:27]\n"
    "ds_read_b128 v[136:139], v0 offset:17408\n"
    "ds_read_b128 v[140:143], v0 offset:18432\n"
    "v_mfma_f32_16x16x32_bf16 v[24:27], v[96:99], a[232:235], v[24:27]\n"
    "ds_read_b128 v[144:147], v0 offset:4096\n"
    "ds_read_b128 v[148:151], v0 offset:9216\n"
    "v_mfma_f32_16x16x32_bf16 v[24:27], v[100:103], a[236:239], v[24:27]\n"
    "s_waitcnt lgkmcnt(11)\n"
    "v_mfma_f32_16x16x32_bf16 v[24:27], v[104:107], a[240:243], v[24:27]\n"
    "v_mfma_f32_16x16x32_bf16 v[24:27], v[108:111], a[244:247], v[24:27]\n"
    "s_waitcnt lgkmcnt(0)\n"
    "s_barrier\n"
    "v_mfma_f32_16x16x32_bf16 v[24:27], v[112:115], a[248:251], v[24:27]\n"
    "ds_read2st64_b32 v[48:49], v10 offset0:80 offset1:84\n"
    "ds_read2st64_b32 v[50:51], v10 offset0:88 offset1:92\n"
    "v_mfma_f32_16x16x32_bf16 v[12:15], a[0:3], v[80:83], 0\n"
    "ds_read2st64_b32 v[54:55], v10 offset0:104 offset1:108\n"
    "ds_read2st64_b32 v[52:53], v10 offset0:96 offset1:100\n"
    "v_mfma_f32_16x16x32_bf16 v[16:19], a[72:75], v[80:83], 0\n"
    "v_min_f32 v40, v74, v68\n"
    "v_min_f32 v41, v74, v69\n"
    "v_mfma_f32_16x16x32_bf16 v[12:15], a[4:7], v[84:87], v[12:15]\n"
    "v_min_f32 v42, v74, v70\n"
    "v_min_f32 v43, v74, v71\n"
    "v_mfma_f32_16x16x32_bf16 v[16:19], a[76:79], v[84:87], v[16:19]\n"
    "v_mfma_f32_16x16x32_bf16 v[12:15], a[8:11], v[88:91], v[12:15]\n"
    "s_waitcnt lgkmcnt(0)\n"
    "v_add_f32 v52, v52, v53\n"
    "v_add_f32 v54, v54, v55\n"
    "v_mfma_f32_16x16x32_bf16 v[16:19], a[80:83], v[88:91], v[16:19]\n"
    "v_add_f32 v48, v48, v49\n"
    "v_add_f32 v50, v50, v51\n"
    "v_mfma_f32_16x16x32_bf16 v[12:15], a[12:15], v[92:95], v[12:15]\n"
    "v_add_f32 v52, v52, v54\n"
    "v_add_f32 v50, v48, v50\n"
    "v_mfma_f32_16x16x32_bf16 v[16:19], a[84:87], v[92:95], v[16:19]\n"
    "v_mul_f32 v52, 0xbae38e39, v52\n"
    "v_mul_f32 v50, 0x3ae38e39, v50\n"
    "v_mfma_f32_16x16x32_bf16 v[12:15], a[16:19], v[96:99], v[12:15]\n"
    "v_cvt_pk_bf16_f32 v216, v52, v52\n"
    "v_fma_f32 v50, -v52, v52, v50\n"
    "v_mfma_f32_16x16x32_bf16 v[16:19], a[88:91], v[96:99], v[16:19]\n"
    "v_cvt_pk_bf16_f32 v217, v52, v52\n"
    "v_max_f32 v50, 0, v50\n"
    "v_mfma_f32_16x16x32_bf16 v[12:15], a[20:23], v[100:103], v[12:15]\n"
    "v_cvt_pk_bf16_f32 v50, 0, v50\n"
    "v_add_f32 v50, 0x3727c5ac, v50\n"
    "v_mfma_f32_16x16x32_bf16 v[16:19], a[92:95], v[100:103], v[16:19]\n"
    "v_mfma_f32_16x16x32_bf16 v[24:27], v[216:219], v[66:69], v[24:27]\n"
    "v_cvt_pk_bf16_f32 v50, 0, v50\n"
    "v_mfma_f32_16x16x32_bf16 v[12:15], a[24:27], v[104:107], v[12:15]\n"
    "v_sqrt_f32 v50, v50\n"
    "v_mfma_f32_16x16x32_bf16 v[16:19], a[96:99], v[104:107], v[16:19]\n"
    "v_cvt_pk_bf16_f32 v50, 0, v50\n"
    "v_rcp_f32 v50, v50\n"
    "v_mfma_f32_16x16x32_bf16 v[12:15], a[28:31], v[108:111], v[12:15]\n"
    "ds_write_b32 v9, v50\n"
    "v_mfma_f32_16x16x32_bf16 v[16:19], a[100:103], v[108:111], v[16:19]\n"
    "ds_read_b128 v[200:203], v72 offset:28672\n"
    "v_mfma_f32_16x16x32_bf16 v[12:15], v[60:63], v[216:219], v[12:15]\n"
    "v_mfma_f32_16x16x32_bf16 v[12:15], a[32:35], v[112:115], v[12:15]\n"
    "v_mfma_f32_16x16x32_bf16 v[16:19], v[62:65], v[216:219], v[16:19]\n"
    "v_mfma_f32_16x16x32_bf16 v[16:19], a[104:107], v[112:115], v[16:19]\n"
    "v_mfma_f32_16x16x32_bf16 v[12:15], a[36:39], v[116:119], v[12:15]\n"
    "v_mfma_f32_16x16x32_bf16 v[16:19], a[108:111], v[116:119], v[16:19]\n"
    "s_waitcnt lgkmcnt(0)\n"
    "v_mul_f32 v24, v24, v200\n"
    "v_mul_f32 v25, v25, v201\n"
    "v_mfma_f32_16x16x32_bf16 v[12:15], a[40:43], v[120:123], v[12:15]\n"
    "v_mul_f32 v26, v26, v202\n"
    "v_mul_f32 v27, v27, v203\n"
    "v_mfma_f32_16x16x32_bf16 v[16:19], a[112:115], v[120:123], v[16:19]\n"
    "ds_write_b128 v5, v[24:27]\n"
    "v_mfma_f32_16x16x32_bf16 v[12:15], a[44:47], v[124:127], v[12:15]\n"
    "s_waitcnt vmcnt(0)\n"
    "v_mov_b32 v78, 1\n"
    "v_mfma_f32_16x16x32_bf16 v[16:19], a[116:119], v[124:127], v[16:19]\n"
    "s_mov_b64 exec, s[56:57]\n"
    "ds_add_rtn_u32 v78, v11, v78 offset:16\n"
    "s_mov_b64 exec, -1\n"
    "v_mfma_f32_16x16x32_bf16 v[12:15], a[48:51], v[128:131], v[12:15]\n"
    "s_waitcnt lgkmcnt(0)\n"
    "s_barrier\n"
    "v_mfma_f32_16x16x32_bf16 v[16:19], a[120:123], v[128:131], v[16:19]\n"
    "ds_read_b128 v[188:191], v8\n"
    "ds_read_b128 v[192:195], v8 offset:1024\n"
    "v_mfma_f32_16x16x32_bf16 v[12:15], a[52:55], v[132:135], v[12:15]\n"
    "ds_read2st64_b64 v[184:187], v7 offset1:2\n"
    "ds_read2st64_b64 v[180:183], v6 offset1:2\n"
    "v_cmp_eq_u32 vcc, 0, v77\n"
    "s_cmp_eq_u64 vcc, 0\n"
    "v_mfma_f32_16x16x32_bf16 v[16:19], a[124:127], v[132:135], v[16:19]\n"
    "s_cselect_b32 s94, s42, 0\n"
    "v_mfma_f32_16x16x32_bf16 v[12:15], a[56:59], v[136:139], v[12:15]\n"
    "global_load_dword v77, v76, %1 sc1\n"
    "v_mfma_f32_16x16x32_bf16 v[16:19], a[128:131], v[136:139], v[16:19]\n"
    "buffer_load_dwordx4 v[152:155], v3, s[92:95], s49 offen sc1\n"
    "v_mfma_f32_16x16x32_bf16 v[12:15], a[60:63], v[140:143], v[12:15]\n"
    "buffer_load_dwordx4 v[156:159], v3, s[92:95], s49 offen offset:1024 sc1\n"
    "v_mfma_f32_16x16x32_bf16 v[16:19], a[132:135], v[140:143], v[16:19]\n"
    "v_readfirstlane_b32 s50, v78\n"
    "v_mfma_f32_16x16x32_bf16 v[12:15], a[64:67], v[144:147], v[12:15]\n"
    "buffer_load_dwordx4 v[160:163], v3, s[92:95], s49 offen offset:2048 sc1\n"
    "s_cmp_eq_u32 s50, 3\n"
    "v_mfma_f32_16x16x32_bf16 v[16:19], a[136:139], v[144:147], v[16:19]\n"
    "s_waitcnt lgkmcnt(0)\n"
    "v_add_f32 v180, v180, v182\n"
    "v_add_f32 v181, v181, v183\n"
    "s_cselect_b64 exec, 1, 0\n"
    "global_store_dword v79, v78, %1 sc1\n"
    "s_mov_b64 exec, -1\n"
    "v_mfma_f32_16x16x32_bf16 v[12:15], a[68:71], v[148:151], v[12:15]\n"
    "v_cvt_pk_bf16_f32 v198, v180, v181\n"
    "v_add_f32 v184, v184, v186\n"
    "buffer_load_dwordx4 v[164:167], v3, s[92:95], s49 offen offset:3072 sc1\n"
    "v_mfma_f32_16x16x32_bf16 v[16:19], a[140:143], v[148:151], v[16:19]\n"
    "v_add_f32 v185, v185, v187\n"
    "v_cvt_pk_bf16_f32 v214, v184, v185\n"
    "buffer_load_dwordx4 v[168:171], v4, s[92:95], s49 offen sc1\n"
    "s_branch .Lak_%=\n"
    ".Laq3_%=:\n"
    "ds_read_b128 v[116:119], v0 offset:11264\n"
    "ds_read_b128 v[124:127], v0 offset:13312\n"
    "ds_read_b128 v[120:123], v0 offset:12288\n"
    "ds_read_b128 v[136:139], v0 offset:17408\n"
    "ds_read_b128 v[132:135], v0 offset:16384\n"
    "ds_read_b128 v[128:131], v0 offset:15360\n"
    "ds_read_b128 v[148:151], v0 offset:9216\n"
    "ds_read_b128 v[144:147], v0 offset:4096\n"
    "ds_read_b128 v[140:143], v0 offset:18432\n"
    "global_load_dword v77, v76, %1 sc1\n"
    "ds_write_b128 v73, v[48:51] offset:20480\n"
    "v_mfma_f32_16x16x32_bf16 v[52:55], v[56:59], v[152:155], 0\n"
    "v_mfma_f32_16x16x32_bf16 v[52:55], v[56:59], v[156:159], v[52:55]\n"
    "v_mfma_f32_16x16x32_bf16 v[52:55], v[56:59], v[160:163], v[52:55]\n"
    "v_mfma_f32_16x16x32_bf16 v[52:55], v[56:59], v[164:167], v[52:55]\n"
    "v_mfma_f32_16x16x32_bf16 v[52:55], v[56:59], v[168:171], v[52:55]\n"
    "s_waitcnt lgkmcnt(9)\n"
    "v_mfma_f32_16x16x32_bf16 v[24:27], v[116:119], a[252:255], 0\n"
    "ds_read_b128 v[80:83], v0\n"
    "ds_read_b128 v[84:87], v0 offset:1024\n"
    "s_waitcnt lgkmcnt(9)\n"
    "v_mfma_f32_16x16x32_bf16 v[24:27], v[120:123], v[224:227], v[24:27]\n"
    "ds_read_b128 v[88:91], v0 offset:2048\n"
    "ds_read_b128 v[92:95], v0 offset:3072\n"
    "v_mfma_f32_16x16x32_bf16 v[24:27], v[124:127], v[228:231], v[24:27]\n"
    "ds_write_b128 v73, v[52:55] offset:24576\n"
    "ds_read_b128 v[96:99], v0 offset:5120\n"
    "s_waitcnt lgkmcnt(10)\n"
    "v_mfma_f32_16x16x32_bf16 v[24:27], v[128:131], v[232:235], v[24:27]\n"
    "ds_read_b128 v[100:103], v0 offset:6144\n"
    "ds_read_b128 v[104:107], v0 offset:7168\n"
    "v_mfma_f32_16x16x32_bf16 v[24:27], v[132:135], v[236:239], v[24:27]\n"
    "ds_read_b128 v[108:111], v0 offset:8192\n"
    "ds_read_b128 v[112:115], v0 offset:10240\n"
    "v_mfma_f32_16x16x32_bf16 v[24:27], v[136:139], v[240:243], v[24:27]\n"
    "s_waitcnt lgkmcnt(11)\n"
    "v_mfma_f32_16x16x32_bf16 v[24:27], v[140:143], v[244:247], v[24:27]\n"
    "v_mfma_f32_16x16x32_bf16 v[24:27], v[144:147], v[248:251], v[24:27]\n"
    "s_waitcnt lgkmcnt(0)\n"
    "s_barrier\n"
    "v_mfma_f32_16x16x32_bf16 v[24:27], v[148:151], v[252:255], v[24:27]\n"
    "ds_read2st64_b32 v[48:49], v10 offset0:80 offset1:84\n"
    "ds_read2st64_b32 v[50:51], v10 offset0:88 offset1:92\n"
    "v_mfma_f32_16x16x32_bf16 v[12:15], a[36:39], v[116:119], 0\n"
    "ds_read2st64_b32 v[54:55], v10 offset0:104 offset1:108\n"
    "ds_read2st64_b32 v[52:53], v10 offset0:96 offset1:100\n"
    "v_mfma_f32_16x16x32_bf16 v[16:19], a[108:111], v[116:119], 0\n"
    "v_min_f32 v40, v74, v68\n"
    "v_min_f32 v41, v74, v69\n"
    "v_mfma_f32_16x16x32_bf16 v[12:15], a[40:43], v[120:123], v[12:15]\n"
    "v_min_f32 v42, v74, v70\n"
    "v_min_f32 v43, v74, v71\n"
    "v_mfma_f32_16x16x32_bf16 v[16:19], a[112:115], v[120:123], v[16:19]\n"
    "v_mfma_f32_16x16x32_bf16 v[12:15], a[44:47], v[124:127], v[12:15]\n"
    "s_waitcnt lgkmcnt(0)\n"
    "v_add_f32 v52, v52, v53\n"
    "v_add_f32 v54, v54, v55\n"
    "v_mfma_f32_16x16x32_bf16 v[16:19], a[116:119], v[124:127], v[16:19]\n"
    "v_add_f32 v48, v48, v49\n"
    "v_add_f32 v50, v50, v51\n"
    "v_mfma_f32_16x16x32_bf16 v[12:15], a[48:51], v[128:131], v[12:15]\n"
    "v_add_f32 v52, v52, v54\n"
    "v_add_f32 v50, v48, v50\n"
    "v_mfma_f32_16x16x32_bf16 v[16:19], a[120:123], v[128:131], v[16:19]\n"
    "v_mul_f32 v52, 0xbae38e39, v52\n"
    "v_mul_f32 v50, 0x3ae38e39, v50\n"
    "v_mfma_f32_16x16x32_bf16 v[12:15], a[52:55], v[132:135], v[12:15]\n"
    "v_cvt_pk_bf16_f32 v216, v52, v52\n"
    "v_fma_f32 v50, -v52, v52, v50\n"
    "v_mfma_f32_16x16x32_bf16 v[16:19], a[124:127], v[132:135], v[16:19]\n"
    "v_cvt_pk_bf16_f32 v217, v52, v52\n"
    "v_max_f32 v50, 0, v50\n"
    "v_mfma_f32_16x16x32_bf16 v[12:15], a[56:59], v[136:139], v[12:15]\n"
    "v_cvt_pk_bf16_f32 v50, 0, v50\n"
    "v_add_f32 v50, 0x3727c5ac, v50\n"
    "v_mfma_f32_16x16x32_bf16 v[16:19], a[128:131], v[136:139], v[16:19]\n"
    "v_cvt_pk_bf16_f32 v50, 0, v50\n"
    "v_sqrt_f32 v50, v50\n"
    "v_mfma_f32_16x16x32_bf16 v[12:15], a[60:63], v[140:143], v[12:15]\n"
    "v_cvt_pk_bf16_f32 v50, 0, v50\n"
    "v_mfma_f32_16x16x32_bf16 v[16:19], a[132:135], v[140:143], v[16:19]\n"
    "v_rcp_f32 v50, v50\n"
    "v_mfma_f32_16x16x32_bf16 v[12:15], a[64:67], v[144:147], v[12:15]\n"
    "ds_write_b32 v9, v50\n"
    "ds_read_b128 v[200:203], v72 offset:28672\n"
    "v_mfma_f32_16x16x32_bf16 v[16:19], a[136:139], v[144:147], v[16:19]\n"
    "v_mfma_f32_16x16x32_bf16 v[12:15], v[60:63], v[216:219], v[12:15]\n"
    "v_mfma_f32_16x16x32_bf16 v[16:19], v[62:65], v[216:219], v[16:19]\n"
    "v_mfma_f32_16x16x32_bf16 v[12:15], a[68:71], v[148:151], v[12:15]\n"
    "v_mfma_f32_16x16x32_bf16 v[16:19], a[140:143], v[148:151], v[16:19]\n"
    "v_mfma_f32_16x16x32_bf16 v[12:15], a[0:3], v[80:83], v[12:15]\n"
    "s_waitcnt lgkmcnt(0)\n"
    "v_mul_f32 v24, v24, v200\n"
    "v_mul_f32 v25, v25, v201\n"
    "v_mfma_f32_16x16x32_bf16 v[16:19], a[72:75], v[80:83], v[16:19]\n"
    "v_mul_f32 v26, v26, v202\n"
    "v_mul_f32 v27, v27, v203\n"
    "v_mfma_f32_16x16x32_bf16 v[12:15], a[4:7], v[84:87], v[12:15]\n"
    "ds_write_b128 v5, v[24:27]\n"
    "v_mfma_f32_16x16x32_bf16 v[16:19], a[76:79], v[84:87], v[16:19]\n"
    "v_mfma_f32_16x16x32_bf16 v[12:15], a[8:11], v[88:91], v[12:15]\n"
    "s_waitcnt vmcnt(0)\n"
    "v_mov_b32 v78, 1\n"
    "v_mfma_f32_16x16x32_bf16 v[16:19], a[80:83], v[88:91], v[16:19]\n"
    "s_mov_b64 exec, s[56:57]\n"
    "ds_add_rtn_u32 v78, v11, v78 offset:16\n"
    "s_mov_b64 exec, -1\n"
    "v_mfma_f32_16x16x32_bf16 v[12:15], a[12:15], v[92:95], v[12:15]\n"
    "s_waitcnt lgkmcnt(0)\n"
    "s_barrier\n"
    "v_mfma_f32_16x16x32_bf16 v[16:19], a[84:87], v[92:95], v[16:19]\n"
    "ds_read_b128 v[188:191], v8\n"
    "ds_read_b128 v[192:195], v8 offset:1024\n"
    "v_mfma_f32_16x16x32_bf16 v[12:15], a[16:19], v[96:99], v[12:15]\n"
    "ds_read2st64_b64 v[184:187], v7 offset1:2\n"
    "ds_read2st64_b64 v[180:183], v6 offset1:2\n"
    "v_cmp_eq_u32 vcc, 0, v77\n"
    "s_cmp_eq_u64 vcc, 0\n"
    "v_mfma_f32_16x16x32_bf16 v[16:19], a[88:91], v[96:99], v[16:19]\n"
    "s_cselect_b32 s94, s42, 0\n"
    "v_mfma_f32_16x16x32_bf16 v[12:15], a[20:23], v[100:103], v[12:15]\n"
    "global_load_dword v77, v76, %1 sc1\n"
    "v_mfma_f32_16x16x32_bf16 v[16:19], a[92:95], v[100:103], v[16:19]\n"
    "buffer_load_dwordx4 v[152:155], v3, s[92:95], s49 offen sc1\n"
    "v_mfma_f32_16x16x32_bf16 v[12:15], a[24:27], v[104:107], v[12:15]\n"
    "buffer_load_dwordx4 v[156:159], v3, s[92:95], s49 offen offset:1024 sc1\n"
    "v_mfma_f32_16x16x32_bf16 v[16:19], a[96:99], v[104:107], v[16:19]\n"
    "v_readfirstlane_b32 s50, v78\n"
    "v_mfma_f32_16x16x32_bf16 v[12:15], a[28:31], v[108:111], v[12:15]\n"
    "buffer_load_dwordx4 v[160:163], v3, s[92:95], s49 offen offset:2048 sc1\n"
    "s_cmp_eq_u32 s50, 3\n"
    "v_mfma_f32_16x16x32_bf16 v[16:19], a[100:103], v[108:111], v[16:19]\n"
    "s_waitcnt lgkmcnt(0)\n"
    "v_add_f32 v180, v180, v182\n"
    "v_add_f32 v181, v181, v183\n"
    "s_cselect_b64 exec, 1, 0\n"
    "global_store_dword v79, v78, %1 sc1\n"
    "s_mov_b64 exec, -1\n"
    "v_mfma_f32_16x16x32_bf16 v[12:15], a[32:35], v[112:115], v[12:15]\n"
    "v_cvt_pk_bf16_f32 v198, v180, v181\n"
    "v_add_f32 v184, v184, v186\n"
    "buffer_load_dwordx4 v[164:167], v3, s[92:95], s49 offen offset:3072 sc1\n"
    "v_mfma_f32_16x16x32_bf16 v[16:19], a[104:107], v[112:115], v[16:19]\n"
    "v_add_f32 v185, v185, v187\n"
    "v_cvt_pk_bf16_f32 v214, v184, v185\n"
    "buffer_load_dwordx4 v[168:171], v4, s[92:95], s49 offen sc1\n"
    ".Lak_%=:\n"
    "v_mfma_f32_16x16x32_bf16 v[20:23], v[216:219], v[64:67], 0\n"
    "v_add_f32 v188, v188, v192\n"
    "v_add_f32 v189, v189, v193\n"
    "v_mfma_f32_16x16x32_bf16 v[20:23], v[80:83], a[144:147], v[20:23]\n"
    "v_add_f32 v190, v190, v194\n"
    "v_add_f32 v191, v191, v195\n"
    "v_mfma_f32_16x16x32_bf16 v[20:23], v[84:87], a[148:151], v[20:23]\n"
    "v_mul_f32 v12, v12, v50\n"
    "v_mul_f32 v13, v13, v50\n"
    "v_mfma_f32_16x16x32_bf16 v[20:23], v[88:91], a[152:155], v[20:23]\n"
    "v_mul_f32 v14, v14, v50\n"
    "v_mul_f32 v15, v15, v50\n"
    "v_mfma_f32_16x16x32_bf16 v[20:23], v[92:95], a[156:159], v[20:23]\n"
    "v_cvt_pk_bf16_f32 v196, v12, v13\n"
    "v_cvt_pk_bf16_f32 v197, v14, v15\n"
    "v_mfma_f32_16x16x32_bf16 v[20:23], v[96:99], a[160:163], v[20:23]\n"
    "v_mul_f32 v16, v16, v50\n"
    "v_mul_f32 v17, v17, v50\n"
    "v_mfma_f32_16x16x32_bf16 v[20:23], v[100:103], a[164:167], v[20:23]\n"
    "v_mfma_f32_16x16x32_bf16 v[204:207], v[28:31], v[196:199], v[40:43]\n"
    "v_mul_f32 v18, v18, v50\n"
    "v_mfma_f32_16x16x32_bf16 v[20:23], v[104:107], a[168:171], v[20:23]\n"
    "v_mul_f32 v19, v19, v50\n"
    "v_cvt_pk_bf16_f32 v212, v16, v17\n"
    "v_mfma_f32_16x16x32_bf16 v[20:23], v[108:111], a[172:175], v[20:23]\n"
    "v_cvt_pk_bf16_f32 v213, v18, v19\n"
    "v_cvt_pk_bf16_f32 v38, v188, v189\n"
    "v_mfma_f32_16x16x32_bf16 v[20:23], v[112:115], a[176:179], v[20:23]\n"
    "v_cvt_pk_bf16_f32 v39, v190, v191\n"
    "v_mfma_f32_16x16x32_bf16 v[208:211], v[212:215], v[196:199], v[44:47]\n"
    "v_mfma_f32_16x16x32_bf16 v[20:23], v[116:119], a[180:183], v[20:23]\n"
    "v_cndmask_b32 v38, v38, v56, s[86:87]\n"
    "v_cndmask_b32 v39, v39, v56, s[86:87]\n"
    "v_mfma_f32_16x16x32_bf16 v[20:23], v[120:123], a[184:187], v[20:23]\n"
    "v_maximum3_f32 v188, v204, v205, v206\n"
    "v_mov_b32 v28, v212\n"
    "v_mfma_f32_16x16x32_bf16 v[20:23], v[124:127], a[188:191], v[20:23]\n"
    "v_mov_b32 v29, v213\n"
    "v_mov_b32 v30, v214\n"
    "v_mfma_f32_16x16x32_bf16 v[20:23], v[128:131], a[192:195], v[20:23]\n"
    "v_maximum3_f32 v188, v188, v207, v208\n"
    "v_maximum3_f32 v188, v188, v209, v210\n"
    "v_mfma_f32_16x16x32_bf16 v[20:23], v[132:135], a[196:199], v[20:23]\n"
    "v_maximum3_f32 v188, v188, v211, v211\n"
    "v_mov_b32 v189, v188\n"
    "v_mfma_f32_16x16x32_bf16 v[20:23], v[136:139], a[200:203], v[20:23]\n"
    "s_nop 0\n"
    "v_permlane16_swap_b32 v189, v188\n"
    "v_maximum3_f32 v188, v188, v189, v189\n"
    "v_mfma_f32_16x16x32_bf16 v[20:23], v[140:143], a[204:207], v[20:23]\n"
    "v_mov_b32 v189, v188\n"
    "v_mfma_f32_16x16x32_bf16 v[20:23], v[144:147], a[208:211], v[20:23]\n"
    "s_nop 0\n"
    "v_permlane32_swap_b32 v189, v188\n"
    "v_maximum3_f32 v188, v188, v189, v189\n"
    "v_mfma_f32_16x16x32_bf16 v[20:23], v[148:151], a[212:215], v[20:23]\n"
    "v_mul_f32 v188, 0xbeae1a93, v188\n"
    "v_fmamk_f32 v204, v204, 0x3eae1a93, v188\n"
    "v_fmamk_f32 v205, v205, 0x3eae1a93, v188\n"
    "v_fmamk_f32 v206, v206, 0x3eae1a93, v188\n"
    "v_fmamk_f32 v207, v207, 0x3eae1a93, v188\n"
    "v_fmamk_f32 v208, v208, 0x3eae1a93, v188\n"
    "v_fmamk_f32 v209, v209, 0x3eae1a93, v188\n"
    "v_fmamk_f32 v210, v210, 0x3eae1a93, v188\n"
    "v_fmamk_f32 v211, v211, 0x3eae1a93, v188\n"
    "v_exp_f32 v204, v204\n"
    "v_exp_f32 v205, v205\n"
    "v_exp_f32 v206, v206\n"
    "v_exp_f32 v207, v207\n"
    "v_exp_f32 v208, v208\n"
    "v_exp_f32 v209, v209\n"
    "v_exp_f32 v210, v210\n"
    "v_exp_f32 v211, v211\n"
    "v_mul_f32 v20, v20, v200\n"
    "v_mul_f32 v21, v21, v201\n"
    "v_mul_f32 v22, v22, v202\n"
    "v_mul_f32 v23, v23, v203\n"
    "v_cvt_pk_bf16_f32 v204, v204, v205\n"
    "v_cvt_pk_bf16_f32 v205, v206, v207\n"
    "v_cvt_pk_bf16_f32 v206, v208, v209\n"
    "v_cvt_pk_bf16_f32 v207, v210, v211\n"
    "v_cvt_pk_bf16_f32 v34, v20, v21\n"
    "v_cvt_pk_bf16_f32 v35, v22, v23\n"
    "v_mfma_f32_16x16x32_bf16 v[184:187], v[36:39], v[204:207], 0\n"
    "s_nop 0\n"
    "v_mfma_f32_16x16x32_bf16 v[180:183], v[32:35], v[204:207], 0\n"
    "v_mov_b64 v[36:37], v[38:39]\n"
    "v_mov_b64 v[32:33], v[34:35]\n"
    "s_nop 2\n"
    "v_rcp_f32 v188, v186\n"
    "s_nop 0\n"
    "v_mul_f32 v184, v184, v188\n"
    "v_mul_f32 v185, v185, v188\n"
    "v_cvt_pk_bf16_f32 v184, v184, v185\n"
    "v_pk_mul_f32 v[180:181], v[180:181], v[188:189] op_sel_hi:[1,0]\n"
    "v_pk_mul_f32 v[182:183], v[182:183], v[188:189] op_sel_hi:[1,0]\n"
    "v_cvt_pk_bf16_f32 v204, v180, v181\n"
    "v_cvt_pk_bf16_f32 v205, v182, v183\n"
    "v_cvt_pk_bf16_f32 v206, v180, v181\n"
    "v_cvt_pk_bf16_f32 v207, v182, v183\n"
    "s_nop 0\n"
    "v_permlane16_swap_b32 v204, v206\n"
    "v_permlane16_swap_b32 v205, v207\n"
    "s_nop 0\n"
    "v_mov_b32_dpp v204, v184 quad_perm:[0,0,0,0] row_mask:0x2 bank_mask:0xf\n"
    "v_mov_b32_dpp v205, v184 quad_perm:[1,1,1,1] row_mask:0x2 bank_mask:0xf\n"
    "v_mov_b32_dpp v206, v184 quad_perm:[2,2,2,2] row_mask:0x2 bank_mask:0xf\n"
    "v_mov_b32_dpp v207, v184 quad_perm:[3,3,3,3] row_mask:0x2 bank_mask:0xf\n"
    "buffer_store_dwordx4 v[204:207], v2, s[40:43], s62 offen sc1\n"
    "s_mov_b32 s56, 1\n"
    "s_cmp_lg_u32 s94, 0\n"
    "s_cbranch_scc1 .Lago_%=\n"
    "s_waitcnt vmcnt(1)\n"
    "v_cmp_eq_u32 vcc, 0, v77\n"
    "s_cmp_eq_u64 vcc, 0\n"
    "s_cbranch_scc1 .Lald_%=\n"
    "ds_read_b32 v79, v11 offset:44\n"
    "global_load_dword v77, v76, %1 sc1\n"
    "s_waitcnt vmcnt(0)\n"
    "v_mov_b32 v78, 1\n"
    "s_mov_b64 exec, 1\n"
    "ds_add_rtn_u32 v78, v11, v78 offset:48\n"
    "s_mov_b64 exec, -1\n"
    "s_waitcnt lgkmcnt(0)\n"
    "v_readfirstlane_b32 s50, v78\n"
    "s_cmp_eq_u32 s50, 3\n"
    "s_cselect_b64 exec, 1, 0\n"
    "global_store_dword v79, v78, %1 sc1\n"
    "s_mov_b64 exec, -1\n"
    "s_mov_b32 s56, 0\n"
    "v_cmp_eq_u32 vcc, 0, v77\n"
    "s_cmp_eq_u64 vcc, 0\n"
    "s_cbranch_scc1 .Lald_%=\n"
    ".Lachk_%=:\n"
    "v_readfirstlane_b32 s50, v76\n"
    "s_cmp_lt_u32 s50, 200\n"
    "s_cbranch_scc1 .Laend_%=\n"
    ".Lawait_%=:\n"
    "global_load_dword v77, v76, %1 sc1\n"
    "s_mov_b32 s52, 0\n"
    ".Lap11_%=:\n"
    "s_waitcnt vmcnt(0)\n"
    "v_cmp_eq_u32 vcc, 0, v77\n"
    "s_cmp_eq_u64 vcc, 0\n"
    "s_cbranch_scc1 .Lap11_x%=\n"
    "s_add_u32 s52, s52, 1\n"
    "s_sleep 2\n"
    "global_load_dword v77, v76, %1 sc1\n"
    "s_cmp_lt_u32 s52, s53\n"
    "s_cbranch_scc1 .Lap11_%=\n"
    "s_mov_b32 s53, 0\n"
    "v_mov_b32 v78, 0\n"
    "v_mov_b32 v79, 1\n"
    "global_store_dword v78, v79, %1 sc1\n"
    "s_waitcnt vmcnt(0)\n"
    ".Lap11_x%=:\n"
    ".Lald_%=:\n"
    "s_mov_b32 s94, s42\n"
    "buffer_load_dwordx4 v[152:155], v3, s[92:95], s49 offen sc1\n"
    "buffer_load_dwordx4 v[156:159], v3, s[92:95], s49 offen offset:1024 sc1\n"
    "buffer_load_dwordx4 v[160:163], v3, s[92:95], s49 offen offset:2048 sc1\n"
    "buffer_load_dwordx4 v[164:167], v3, s[92:95], s49 offen offset:3072 sc1\n"
    "buffer_load_dwordx4 v[168:171], v4, s[92:95], s49 offen sc1\n"
    ".Lain_%=:\n"
    "v_add_u32 v11, 32, v11\n"
    "ds_read_b128 v[76:79], v11\n"
    "ds_read2_b32 v[74:75], v11 offset0:5 offset1:6\n"
    "s_waitcnt vmcnt(4)\n"
    "ds_write_b128 v1, v[152:155]\n"
    "v_mfma_f32_16x16x32_bf16 v[48:51], v[152:155], v[152:155], 0\n"
    "s_waitcnt vmcnt(3)\n"
    "ds_write_b128 v1, v[156:159] offset:1024\n"
    "v_mfma_f32_16x16x32_bf16 v[48:51], v[156:159], v[156:159], v[48:51]\n"
    "s_waitcnt vmcnt(2)\n"
    "ds_write_b128 v1, v[160:163] offset:2048\n"
    "v_mfma_f32_16x16x32_bf16 v[48:51], v[160:163], v[160:163], v[48:51]\n"
    "s_waitcnt vmcnt(1)\n"
    "ds_write_b128 v1, v[164:167] offset:3072\n"
    "v_mfma_f32_16x16x32_bf16 v[48:51], v[164:167], v[164:167], v[48:51]\n"
    "s_waitcnt vmcnt(0)\n"
    "ds_write_b128 v1, v[168:171] offset:4096\n"
    "v_mfma_f32_16x16x32_bf16 v[48:51], v[168:171], v[168:171], v[48:51]\n"
    "s_waitcnt lgkmcnt(5)\n"
    "v_readfirstlane_b32 s50, v75\n"
    "s_cmp_eq_u32 s50, 0\n"
    "s_cbranch_scc1 .Lac12_%=\n"
    "v_swap_b32 v28, v172\n"
    "v_swap_b32 v29, v173\n"
    "v_swap_b32 v30, v174\n"
    "v_swap_b32 v32, v175\n"
    "v_swap_b32 v33, v176\n"
    "v_swap_b32 v36, v177\n"
    "v_swap_b32 v37, v178\n"
    ".Lac12_%=:\n"
    "v_readfirstlane_b32 s49, v77\n"
    "v_readfirstlane_b32 s62, v78\n"
    "v_add_u32 v76, v76, v220\n"
    "s_waitcnt lgkmcnt(0)\n"
    "s_cmp_eq_u32 s56, 0\n"
    "s_cbranch_scc1 .Lac13_%=\n"
    "s_waitcnt vmcnt(0)\n"
    "v_mov_b32 v78, 1\n"
    "s_mov_b64 exec, 1\n"
    "ds_add_rtn_u32 v78, v11, v78 offset:16\n"
    "s_mov_b64 exec, -1\n"
    "s_waitcnt lgkmcnt(0)\n"
    "v_readfirstlane_b32 s50, v78\n"
    "s_cmp_eq_u32 s50, 3\n"
    "s_cselect_b64 exec, 1, 0\n"
    "global_store_dword v79, v78, %1 sc1\n"
    "s_mov_b64 exec, -1\n"
    "s_mov_b32 s56, 0\n"
    ".Lac13_%=:\n"
    "s_branch .Latop_%=\n"
    ".Lapol_%=:\n"
    "v_mov_b32 v76, 64\n"
    "global_load_dword v77, v76, %1 sc1\n"
    "s_mov_b32 s52, 0\n"
    ".Lap14_%=:\n"
    "s_waitcnt vmcnt(0)\n"
    "v_cmp_eq_u32 vcc, 0, v77\n"
    "s_cmp_eq_u64 vcc, 0\n"
    "s_cbranch_scc1 .Lap14_x%=\n"
    "s_add_u32 s52, s52, 1\n"
    "s_sleep 2\n"
    "global_load_dword v77, v76, %1 sc1\n"
    "s_cmp_lt_u32 s52, s53\n"
    "s_cbranch_scc1 .Lap14_%=\n"
    "s_mov_b32 s53, 0\n"
    "v_mov_b32 v78, 0\n"
    "v_mov_b32 v79, 1\n"
    "global_store_dword v78, v79, %1 sc1\n"
    "s_waitcnt vmcnt(0)\n"
    ".Lap14_x%=:\n"
    "v_readfirstlane_b32 s50, v77\n"
    "v_mbcnt_lo_u32_b32 v76, -1, 0\n"
    "v_mbcnt_hi_u32_b32 v76, -1, v76\n"
    "s_lshl_b32 s51, %2, 6\n"
    "v_add_u32 v76, s51, v76\n"
    "v_min_u32 v76, 129, v76\n"
    "v_lshlrev_b32 v76, 5, v76\n"
    "v_add_u32 v76, 0x26c00, v76\n"
    "ds_read2_b32 v[78:79], v76 offset0:1 offset1:2\n"
    "s_and_b32 s50, s50, 1\n"
    "s_waitcnt lgkmcnt(0)\n"
    "v_and_b32 v77, s50, v78\n"
    "v_lshlrev_b32 v77, 13, v77\n"
    "v_and_b32 v78, -2, v78\n"
    "v_xor_b32 v78, v77, v78\n"
    "v_and_b32 v77, s50, v79\n"
    "v_lshlrev_b32 v77, 13, v77\n"
    "v_and_b32 v79, -2, v79\n"
    "v_xor_b32 v79, v77, v79\n"
    "ds_write2_b32 v76, v78, v79 offset0:1 offset1:2\n"
    "s_waitcnt lgkmcnt(0)\n"
    "s_barrier\n"
    "ds_read_b32 v77, v11 offset:4\n"
    "v_add_u32 v76, %4, v220\n"
    "s_waitcnt lgkmcnt(0)\n"
    "v_readfirstlane_b32 s49, v77\n"
    "s_branch .Lawait_%=\n"
    ".Laend_%=:\n"
    "s_cmp_eq_u32 s50, 192\n"
    "s_cbranch_scc0 .Lapol_%=\n"
    "s_waitcnt vmcnt(0) lgkmcnt(0)\n"
    "s_endpgm\n"
    ".p2align 6\n"
    ".Lrole4_%=:\n"
    "s_mov_b64 s[40:41], %0\n"
    "s_and_b32 s41, s41, 0xffff\n"
    "s_mov_b32 s42, 0xbc00000\n"
    "s_mov_b32 s43, 0x20000\n"
    "s_mov_b64 s[92:93], %0\n"
    "s_and_b32 s93, s93, 0xffff\n"
    "s_mov_b32 s94, 0xbc00000\n"
    "s_mov_b32 s95, 0x20000\n"
    "s_mov_b32 s53, 131072\n"
    "s_mov_b64 s[56:57], 0\n"
    "v_mbcnt_lo_u32_b32 v20, -1, 0\n"
    "v_mbcnt_hi_u32_b32 v20, -1, v20\n"
    "s_lshl_b32 s50, %2, 8\n"
    "s_add_u32 s50, s50, 147456\n"
    "v_lshl_add_u32 v20, v20, 2, s50\n"
    "ds_read_b32 v0, v20\n"
    "ds_read_b32 v1, v20 offset:1024\n"
    "ds_read_b32 v2, v20 offset:2048\n"
    "ds_read_b32 v3, v20 offset:3072\n"
    "ds_read_b32 v4, v20 offset:4096\n"
    "ds_read_b32 v5, v20 offset:5120\n"
    "ds_read_b32 v8, v20 offset:6144\n"
    "ds_read_b32 v9, v20 offset:7168\n"
    "ds_read_b32 v10, v20 offset:8192\n"
    "s_waitcnt lgkmcnt(0)\n"
    "s_barrier\n"
    "s_lshl_b32 s54, %2, 10\n"
    "s_mov_b64 s[44:45], %8\n"
    "s_and_b32 s45, s45, 0xffff\n"
    "s_mov_b32 s46, 0xa2000\n"
    "s_mov_b32 s47, 0x20000\n"
    "s_add_u32 m0, s54, 0\n"
    "s_add_u32 s55, %6, 0\n"
    "buffer_load_dwordx4 v8, s[44:47], s55 offen lds\n"
    "s_add_u32 m0, s54, 36864\n"
    "s_add_u32 s55, %6, 64\n"
    "buffer_load_dwordx4 v8, s[44:47], s55 offen lds\n"
    "s_add_u32 m0, s54, 73728\n"
    "s_add_u32 s55, %6, 128\n"
    "buffer_load_dwordx4 v8, s[44:47], s55 offen lds\n"
    "s_add_u32 m0, s54, 4096\n"
    "s_add_u32 s55, %6, 73728\n"
    "buffer_load_dwordx4 v8, s[44:47], s55 offen lds\n"
    "s_add_u32 m0, s54, 40960\n"
    "s_add_u32 s55, %6, 73792\n"
    "buffer_load_dwordx4 v8, s[44:47], s55 offen lds\n"
    "s_add_u32 m0, s54, 77824\n"
    "s_add_u32 s55, %6, 73856\n"
    "buffer_load_dwordx4 v8, s[44:47], s55 offen lds\n"
    "s_add_u32 m0, s54, 8192\n"
    "s_add_u32 s55, %6, 147456\n"
    "buffer_load_dwordx4 v8, s[44:47], s55 offen lds\n"
    "s_add_u32 m0, s54, 45056\n"
    "s_add_u32 s55, %6, 147520\n"
    "buffer_load_dwordx4 v8, s[44:47], s55 offen lds\n"
    "s_add_u32 m0, s54, 81920\n"
    "s_add_u32 s55, %6, 147584\n"
    "buffer_load_dwordx4 v8, s[44:47], s55 offen lds\n"
    "s_add_u32 m0, s54, 12288\n"
    "s_add_u32 s55, %6, 221184\n"
    "buffer_load_dwordx4 v8, s[44:47], s55 offen lds\n"
    "s_add_u32 m0, s54, 49152\n"
    "s_add_u32 s55, %6, 221248\n"
    "buffer_load_dwordx4 v8, s[44:47], s55 offen lds\n"
    "s_add_u32 m0, s54, 86016\n"
    "s_add_u32 s55, %6, 221312\n"
    "buffer_load_dwordx4 v8, s[44:47], s55 offen lds\n"
    "s_add_u32 m0, s54, 16384\n"
    "s_add_u32 s55, %6, 294912\n"
    "buffer_load_dwordx4 v8, s[44:47], s55 offen lds\n"
    "s_add_u32 m0, s54, 53248\n"
    "s_add_u32 s55, %6, 294976\n"
    "buffer_load_dwordx4 v8, s[44:47], s55 offen lds\n"
    "s_add_u32 m0, s54, 90112\n"
    "s_add_u32 s55, %6, 295040\n"
    "buffer_load_dwordx4 v8, s[44:47], s55 offen lds\n"
    "s_add_u32 m0, s54, 20480\n"
    "s_add_u32 s55, %6, 368640\n"
    "buffer_load_dwordx4 v8, s[44:47], s55 offen lds\n"
    "s_add_u32 m0, s54, 57344\n"
    "s_add_u32 s55, %6, 368704\n"
    "buffer_load_dwordx4 v8, s[44:47], s55 offen lds\n"
    "s_add_u32 m0, s54, 94208\n"
    "s_add_u32 s55, %6, 368768\n"
    "buffer_load_dwordx4 v8, s[44:47], s55 offen lds\n"
    "s_add_u32 m0, s54, 24576\n"
    "s_add_u32 s55, %6, 442368\n"
    "buffer_load_dwordx4 v8, s[44:47], s55 offen lds\n"
    "s_add_u32 m0, s54, 61440\n"
    "s_add_u32 s55, %6, 442432\n"
    "buffer_load_dwordx4 v8, s[44:47], s55 offen lds\n"
    "s_add_u32 m0, s54, 98304\n"
    "s_add_u32 s55, %6, 442496\n"
    "buffer_load_dwordx4 v8, s[44:47], s55 offen lds\n"
    "s_add_u32 m0, s54, 28672\n"
    "s_add_u32 s55, %6, 516096\n"
    "buffer_load_dwordx4 v8, s[44:47], s55 offen lds\n"
    "s_add_u32 m0, s54, 65536\n"
    "s_add_u32 s55, %6, 516160\n"
    "buffer_load_dwordx4 v8, s[44:47], s55 offen lds\n"
    "s_add_u32 m0, s54, 102400\n"
    "s_add_u32 s55, %6, 516224\n"
    "buffer_load_dwordx4 v8, s[44:47], s55 offen lds\n"
    "s_add_u32 m0, s54, 32768\n"
    "s_add_u32 s55, %6, 589824\n"
    "buffer_load_dwordx4 v8, s[44:47], s55 offen lds\n"
    "s_add_u32 m0, s54, 69632\n"
    "s_add_u32 s55, %6, 589888\n"
    "buffer_load_dwordx4 v8, s[44:47], s55 offen lds\n"
    "s_add_u32 m0, s54, 106496\n"
    "s_add_u32 s55, %6, 589952\n"
    "buffer_load_dwordx4 v8, s[44:47], s55 offen lds\n"
    "s_waitcnt vmcnt(0) lgkmcnt(0)\n"
    "s_barrier\n"
    "ds_read_b64_tr_b16 a[0:1], v9\n"
    "ds_read_b64_tr_b16 a[2:3], v9 offset:256\n"
    "ds_read_b64_tr_b16 a[4:5], v9 offset:8\n"
    "ds_read_b64_tr_b16 a[6:7], v9 offset:264\n"
    "ds_read_b64_tr_b16 a[8:9], v9 offset:2304\n"
    "ds_read_b64_tr_b16 a[10:11], v9 offset:2560\n"
    "ds_read_b64_tr_b16 a[12:13], v9 offset:2312\n"
    "ds_read_b64_tr_b16 a[14:15], v9 offset:2568\n"
    "ds_read_b64_tr_b16 a[16:17], v9 offset:4608\n"
    "ds_read_b64_tr_b16 a[18:19], v9 offset:4864\n"
    "ds_read_b64_tr_b16 a[20:21], v9 offset:4616\n"
    "ds_read_b64_tr_b16 a[22:23], v9 offset:4872\n"
    "ds_read_b64_tr_b16 a[24:25], v9 offset:6912\n"
    "ds_read_b64_tr_b16 a[26:27], v9 offset:7168\n"
    "ds_read_b64_tr_b16 a[28:29], v9 offset:6920\n"
    "ds_read_b64_tr_b16 a[30:31], v9 offset:7176\n"
    "ds_read_b64_tr_b16 a[32:33], v9 offset:9216\n"
    "ds_read_b64_tr_b16 a[34:35], v9 offset:9472\n"
    "ds_read_b64_tr_b16 a[36:37], v9 offset:9224\n"
    "ds_read_b64_tr_b16 a[38:39], v9 offset:9480\n"
    "ds_read_b64_tr_b16 a[40:41], v9 offset:11520\n"
    "ds_read_b64_tr_b16 a[42:43], v9 offset:11776\n"
    "ds_read_b64_tr_b16 a[44:45], v9 offset:11528\n"
    "ds_read_b64_tr_b16 a[46:47], v9 offset:11784\n"
    "ds_read_b64_tr_b16 a[48:49], v9 offset:13824\n"
    "ds_read_b64_tr_b16 a[50:51], v9 offset:14080\n"
    "ds_read_b64_tr_b16 a[52:53], v9 offset:13832\n"
    "ds_read_b64_tr_b16 a[54:55], v9 offset:14088\n"
    "ds_read_b64_tr_b16 a[56:57], v9 offset:16128\n"
    "ds_read_b64_tr_b16 a[58:59], v9 offset:16384\n"
    "ds_read_b64_tr_b16 a[60:61], v9 offset:16136\n"
    "ds_read_b64_tr_b16 a[62:63], v9 offset:16392\n"
    "ds_read_b64_tr_b16 a[64:65], v9 offset:18432\n"
    "ds_read_b64_tr_b16 a[66:67], v9 offset:18688\n"
    "ds_read_b64_tr_b16 a[68:69], v9 offset:18440\n"
    "ds_read_b64_tr_b16 a[70:71], v9 offset:18696\n"
    "ds_read_b64_tr_b16 a[72:73], v9 offset:20736\n"
    "ds_read_b64_tr_b16 a[74:75], v9 offset:20992\n"
    "ds_read_b64_tr_b16 a[76:77], v9 offset:20744\n"
    "ds_read_b64_tr_b16 a[78:79], v9 offset:21000\n"
    "ds_read_b64_tr_b16 a[80:81], v9 offset:23040\n"
    "ds_read_b64_tr_b16 a[82:83], v9 offset:23296\n"
    "ds_read_b64_tr_b16 a[84:85], v9 offset:23048\n"
    "ds_read_b64_tr_b16 a[86:87], v9 offset:23304\n"
    "ds_read_b64_tr_b16 a[88:89], v9 offset:25344\n"
    "ds_read_b64_tr_b16 a[90:91], v9 offset:25600\n"
    "ds_read_b64_tr_b16 a[92:93], v9 offset:25352\n"
    "ds_read_b64_tr_b16 a[94:95], v9 offset:25608\n"
    "ds_read_b64_tr_b16 a[96:97], v9 offset:27648\n"
    "ds_read_b64_tr_b16 a[98:99], v9 offset:27904\n"
    "ds_read_b64_tr_b16 a[100:101], v9 offset:27656\n"
    "ds_read_b64_tr_b16 a[102:103], v9 offset:27912\n"
    "ds_read_b64_tr_b16 a[104:105], v9 offset:29952\n"
    "ds_read_b64_tr_b16 a[106:107], v9 offset:30208\n"
    "ds_read_b64_tr_b16 a[108:109], v9 offset:29960\n"
    "ds_read_b64_tr_b16 a[110:111], v9 offset:30216\n"
    "ds_read_b64_tr_b16 a[112:113], v9 offset:32256\n"
    "ds_read_b64_tr_b16 a[114:115], v9 offset:32512\n"
    "ds_read_b64_tr_b16 a[116:117], v9 offset:32264\n"
    "ds_read_b64_tr_b16 a[118:119], v9 offset:32520\n"
    "ds_read_b64_tr_b16 a[120:121], v9 offset:34560\n"
    "ds_read_b64_tr_b16 a[122:123], v9 offset:34816\n"
    "ds_read_b64_tr_b16 a[124:125], v9 offset:34568\n"
    "ds_read_b64_tr_b16 a[126:127], v9 offset:34824\n"
    "ds_read_b64_tr_b16 a[128:129], v10\n"
    "ds_read_b64_tr_b16 a[130:131], v10 offset:2304\n"
    "ds_read_b64_tr_b16 a[132:133], v10 offset:8\n"
    "ds_read_b64_tr_b16 a[134:135], v10 offset:2312\n"
    "ds_read_b64_tr_b16 a[136:137], v10 offset:18432\n"
    "ds_read_b64_tr_b16 a[138:139], v10 offset:20736\n"
    "ds_read_b64_tr_b16 a[140:141], v10 offset:18440\n"
    "ds_read_b64_tr_b16 a[142:143], v10 offset:20744\n"
    "s_waitcnt lgkmcnt(0)\n"
    "s_barrier\n"
    "v_mbcnt_lo_u32_b32 v20, -1, 0\n"
    "v_mbcnt_hi_u32_b32 v20, -1, v20\n"
    "v_min_u32 v6, 7, v20\n"
    "v_lshlrev_b32 v6, 7, v6\n"
    "v_mov_b32 v7, 0x26c00\n"
    "ds_read_b128 v[20:23], v7\n"
    "s_waitcnt lgkmcnt(0)\n"
    "v_readfirstlane_b32 s49, v21\n"
    "v_add_u32 v20, v20, v6\n"
    "s_branch .Lbchk_%=\n"
    ".Lbgo_%=:\n"
    "s_waitcnt vmcnt(1)\n"
    "v_add_u32 v7, 32, v7\n"
    "ds_read_b128 v[20:23], v7\n"
    "ds_write_b128 v1, v[100:103]\n"
    "ds_write_b128 v1, v[104:107] offset:1024\n"
    "ds_write_b128 v1, v[108:111] offset:2048\n"
    "ds_write_b128 v1, v[112:115] offset:3072\n"
    "ds_write_b32 v4, v116\n"
    "ds_write_b32 v4, v117 offset:16\n"
    "ds_write_b32 v4, v118 offset:32\n"
    "ds_write_b32 v4, v119 offset:48\n"
    "v_lshlrev_b32 v8, 16, v120\n"
    "v_and_b32 v9, 0xffff0000, v120\n"
    "v_lshlrev_b32 v10, 16, v121\n"
    "v_and_b32 v11, 0xffff0000, v121\n"
    "v_lshlrev_b32 v12, 16, v122\n"
    "v_and_b32 v13, 0xffff0000, v122\n"
    "v_lshlrev_b32 v14, 16, v123\n"
    "v_and_b32 v15, 0xffff0000, v123\n"
    "v_xor_b32 v0, 0x8000, v0\n"
    "v_xor_b32 v1, 0x8000, v1\n"
    "v_xor_b32 v4, 0x8000, v4\n"
    "s_waitcnt lgkmcnt(8)\n"
    "v_readfirstlane_b32 s49, v21\n"
    "v_readfirstlane_b32 s62, v22\n"
    "v_add_u32 v20, v20, v6\n"
    "s_waitcnt lgkmcnt(0)\n"
    ".Lbtop_%=:\n"
    "s_waitcnt lgkmcnt(0)\n"
    "s_barrier\n"
    "ds_read_b128 v[24:27], v0\n"
    "ds_read_b128 v[28:31], v0 offset:1024\n"
    "ds_read_b128 v[36:39], v0 offset:3072\n"
    "ds_read_b128 v[32:35], v0 offset:2048\n"
    "ds_read_b128 v[44:47], v0 offset:5120\n"
    "ds_read_b128 v[40:43], v0 offset:4096\n"
    "global_load_dword v21, v20, %1 sc1\n"
    "s_waitcnt lgkmcnt(5)\n"
    "v_mfma_f32_16x16x32_bf16 v[8:11], a[0:3], v[24:27], v[8:11]\n"
    "ds_read_b128 v[52:55], v0 offset:7168\n"
    "ds_read_b128 v[48:51], v0 offset:6144\n"
    "v_mfma_f32_16x16x32_bf16 v[12:15], a[4:7], v[24:27], v[12:15]\n"
    "ds_read_b128 v[60:63], v0 offset:9216\n"
    "ds_read_b128 v[56:59], v0 offset:8192\n"
    "s_waitcnt lgkmcnt(8)\n"
    "v_mfma_f32_16x16x32_bf16 v[8:11], a[8:11], v[28:31], v[8:11]\n"
    "ds_read_b128 v[68:71], v0 offset:11264\n"
    "ds_read_b128 v[64:67], v0 offset:10240\n"
    "v_mfma_f32_16x16x32_bf16 v[12:15], a[12:15], v[28:31], v[12:15]\n"
    "ds_read_b128 v[76:79], v0 offset:13312\n"
    "ds_read_b128 v[72:75], v0 offset:12288\n"
    "s_waitcnt lgkmcnt(10)\n"
    "v_mfma_f32_16x16x32_bf16 v[8:11], a[16:19], v[32:35], v[8:11]\n"
    "ds_read_b128 v[84:87], v0 offset:15360\n"
    "ds_read_b128 v[80:83], v0 offset:14336\n"
    "v_mfma_f32_16x16x32_bf16 v[12:15], a[20:23], v[32:35], v[12:15]\n"
    "ds_read_b128 v[92:95], v0 offset:17408\n"
    "ds_read_b128 v[88:91], v0 offset:16384\n"
    "v_mfma_f32_16x16x32_bf16 v[8:11], a[24:27], v[36:39], v[8:11]\n"
    "v_mfma_f32_16x16x32_bf16 v[12:15], a[28:31], v[36:39], v[12:15]\n"
    "s_waitcnt lgkmcnt(12)\n"
    "v_mfma_f32_16x16x32_bf16 v[8:11], a[32:35], v[40:43], v[8:11]\n"
    "v_mfma_f32_16x16x32_bf16 v[12:15], a[36:39], v[40:43], v[12:15]\n"
    "v_mfma_f32_16x16x32_bf16 v[8:11], a[40:43], v[44:47], v[8:11]\n"
    "v_mfma_f32_16x16x32_bf16 v[12:15], a[44:47], v[44:47], v[12:15]\n"
    "s_waitcnt lgkmcnt(10)\n"
    "v_mfma_f32_16x16x32_bf16 v[8:11], a[48:51], v[48:51], v[8:11]\n"
    "v_mfma_f32_16x16x32_bf16 v[12:15], a[52:55], v[48:51], v[12:15]\n"
    "v_mfma_f32_16x16x32_bf16 v[8:11], a[56:59], v[52:55], v[8:11]\n"
    "v_mfma_f32_16x16x32_bf16 v[12:15], a[60:63], v[52:55], v[12:15]\n"
    "s_waitcnt lgkmcnt(8)\n"
    "v_mfma_f32_16x16x32_bf16 v[8:11], a[64:67], v[56:59], v[8:11]\n"
    "v_mfma_f32_16x16x32_bf16 v[12:15], a[68:71], v[56:59], v[12:15]\n"
    "v_mfma_f32_16x16x32_bf16 v[8:11], a[72:75], v[60:63], v[8:11]\n"
    "v_mfma_f32_16x16x32_bf16 v[12:15], a[76:79], v[60:63], v[12:15]\n"
    "s_waitcnt lgkmcnt(6)\n"
    "v_mfma_f32_16x16x32_bf16 v[8:11], a[80:83], v[64:67], v[8:11]\n"
    "v_mfma_f32_16x16x32_bf16 v[12:15], a[84:87], v[64:67], v[12:15]\n"
    "v_mfma_f32_16x16x32_bf16 v[8:11], a[88:91], v[68:71], v[8:11]\n"
    "v_mfma_f32_16x16x32_bf16 v[12:15], a[92:95], v[68:71], v[12:15]\n"
    "s_waitcnt lgkmcnt(4)\n"
    "v_mfma_f32_16x16x32_bf16 v[8:11], a[96:99], v[72:75], v[8:11]\n"
    "v_mfma_f32_16x16x32_bf16 v[12:15], a[100:103], v[72:75], v[12:15]\n"
    "v_mfma_f32_16x16x32_bf16 v[8:11], a[104:107], v[76:79], v[8:11]\n"
    "v_mfma_f32_16x16x32_bf16 v[12:15], a[108:111], v[76:79], v[12:15]\n"
    "s_waitcnt lgkmcnt(2)\n"
    "v_mfma_f32_16x16x32_bf16 v[8:11], a[112:115], v[80:83], v[8:11]\n"
    "v_mfma_f32_16x16x32_bf16 v[12:15], a[116:119], v[80:83], v[12:15]\n"
    "v_mfma_f32_16x16x32_bf16 v[8:11], a[120:123], v[84:87], v[8:11]\n"
    "v_mfma_f32_16x16x32_bf16 v[12:15], a[124:127], v[84:87], v[12:15]\n"
    "s_waitcnt lgkmcnt(0)\n"
    "v_mfma_f32_16x16x32_bf16 v[8:11], a[128:131], v[88:91], v[8:11]\n"
    "v_mfma_f32_16x16x32_bf16 v[12:15], a[132:135], v[88:91], v[12:15]\n"
    "v_mfma_f32_16x16x32_bf16 v[8:11], a[136:139], v[92:95], v[8:11]\n"
    "v_mfma_f32_16x16x32_bf16 v[12:15], a[140:143], v[92:95], v[12:15]\n"
    "s_nop 4\n"
    "v_cvt_pk_bf16_f32 v16, v8, v9\n"
    "v_cvt_pk_bf16_f32 v17, v10, v11\n"
    "s_nop 1\n"
    "v_cvt_pk_bf16_f32 v18, v12, v13\n"
    "v_cvt_pk_bf16_f32 v19, v14, v15\n"
    "buffer_store_dwordx4 v[16:19], v3, s[40:43], s62 offen sc1\n"
    "s_mov_b32 s56, 1\n"
    "s_waitcnt vmcnt(1)\n"
    "v_cmp_eq_u32 vcc, 0, v21\n"
    "s_cmp_eq_u64 vcc, 0\n"
    "ds_read_b32 v23, v7 offset:44\n"
    "s_cbranch_scc0 .Lbno_%=\n"
    "buffer_load_dwordx4 v[100:103], v2, s[92:95], s49 offen sc1\n"
    "buffer_load_dwordx4 v[104:107], v2, s[92:95], s49 offen offset:1024 sc1\n"
    "buffer_load_dwordx4 v[108:111], v2, s[92:95], s49 offen offset:2048 sc1\n"
    "buffer_load_dwordx4 v[112:115], v2, s[92:95], s49 offen offset:3072 sc1\n"
    "buffer_load_dwordx4 v[116:119], v5, s[92:95], s49 offen sc1\n"
    "s_sub_u32 s55, s49, 0x1000000\n"
    "buffer_load_dwordx4 v[120:123], v3, s[40:43], s55 offen sc1\n"
    "s_waitcnt vmcnt(6)\n"
    "v_mov_b32 v22, 1\n"
    "s_mov_b64 exec, 1\n"
    "ds_add_rtn_u32 v22, v7, v22 offset:48\n"
    "s_mov_b64 exec, -1\n"
    "s_waitcnt lgkmcnt(0)\n"
    "v_readfirstlane_b32 s50, v22\n"
    "s_cmp_eq_u32 s50, 3\n"
    "s_cselect_b64 exec, 1, 0\n"
    "global_store_dword v23, v22, %1 sc1\n"
    "s_mov_b64 exec, -1\n"
    "s_branch .Lbin_%=\n"
    ".Lbno_%=:\n"
    "global_load_dword v21, v20, %1 sc1\n"
    "s_waitcnt vmcnt(0)\n"
    "v_mov_b32 v22, 1\n"
    "s_mov_b64 exec, 1\n"
    "ds_add_rtn_u32 v22, v7, v22 offset:48\n"
    "s_mov_b64 exec, -1\n"
    "s_waitcnt lgkmcnt(0)\n"
    "v_readfirstlane_b32 s50, v22\n"
    "s_cmp_eq_u32 s50, 3\n"
    "s_cselect_b64 exec, 1, 0\n"
    "global_store_dword v23, v22, %1 sc1\n"
    "s_mov_b64 exec, -1\n"
    "s_mov_b32 s56, 0\n"
    "v_cmp_eq_u32 vcc, 0, v21\n"
    "s_cmp_eq_u64 vcc, 0\n"
    "s_cbranch_scc1 .Lbld_%=\n"
    ".Lbchk_%=:\n"
    "v_readfirstlane_b32 s50, v20\n"
    "s_cmp_lt_u32 s50, 200\n"
    "s_cbranch_scc1 .Lbend_%=\n"
    ".Lbwait_%=:\n"
    "global_load_dword v21, v20, %1 sc1\n"
    "s_mov_b32 s52, 0\n"
    ".Lbp1_%=:\n"
    "s_waitcnt vmcnt(0)\n"
    "v_cmp_eq_u32 vcc, 0, v21\n"
    "s_cmp_eq_u64 vcc, 0\n"
    "s_cbranch_scc1 .Lbp1_x%=\n"
    "s_add_u32 s52, s52, 1\n"
    "s_sleep 2\n"
    "global_load_dword v21, v20, %1 sc1\n"
    "s_cmp_lt_u32 s52, s53\n"
    "s_cbranch_scc1 .Lbp1_%=\n"
    "s_mov_b32 s53, 0\n"
    "v_mov_b32 v22, 0\n"
    "v_mov_b32 v23, 1\n"
    "global_store_dword v22, v23, %1 sc1\n"
    "s_waitcnt vmcnt(0)\n"
    ".Lbp1_x%=:\n"
    ".Lbld_%=:\n"
    "s_mov_b32 s94, s42\n"
    "buffer_load_dwordx4 v[100:103], v2, s[92:95], s49 offen sc1\n"
    "buffer_load_dwordx4 v[104:107], v2, s[92:95], s49 offen offset:1024 sc1\n"
    "buffer_load_dwordx4 v[108:111], v2, s[92:95], s49 offen offset:2048 sc1\n"
    "buffer_load_dwordx4 v[112:115], v2, s[92:95], s49 offen offset:3072 sc1\n"
    "buffer_load_dwordx4 v[116:119], v5, s[92:95], s49 offen sc1\n"
    "s_sub_u32 s55, s49, 0x1000000\n"
    "buffer_load_dwordx4 v[120:123], v3, s[40:43], s55 offen sc1\n"
    ".Lbin_%=:\n"
    "v_add_u32 v7, 32, v7\n"
    "ds_read_b128 v[20:23], v7\n"
    "s_waitcnt vmcnt(5)\n"
    "ds_write_b128 v1, v[100:103]\n"
    "s_waitcnt vmcnt(4)\n"
    "ds_write_b128 v1, v[104:107] offset:1024\n"
    "s_waitcnt vmcnt(3)\n"
    "ds_write_b128 v1, v[108:111] offset:2048\n"
    "s_waitcnt vmcnt(2)\n"
    "ds_write_b128 v1, v[112:115] offset:3072\n"
    "s_waitcnt vmcnt(1)\n"
    "ds_write_b32 v4, v116\n"
    "ds_write_b32 v4, v117 offset:16\n"
    "ds_write_b32 v4, v118 offset:32\n"
    "ds_write_b32 v4, v119 offset:48\n"
    "s_waitcnt vmcnt(0)\n"
    "v_lshlrev_b32 v8, 16, v120\n"
    "v_and_b32 v9, 0xffff0000, v120\n"
    "v_lshlrev_b32 v10, 16, v121\n"
    "v_and_b32 v11, 0xffff0000, v121\n"
    "v_lshlrev_b32 v12, 16, v122\n"
    "v_and_b32 v13, 0xffff0000, v122\n"
    "v_lshlrev_b32 v14, 16, v123\n"
    "v_and_b32 v15, 0xffff0000, v123\n"
    "v_xor_b32 v0, 0x8000, v0\n"
    "v_xor_b32 v1, 0x8000, v1\n"
    "v_xor_b32 v4, 0x8000, v4\n"
    "s_waitcnt lgkmcnt(8)\n"
    "v_readfirstlane_b32 s49, v21\n"
    "v_readfirstlane_b32 s62, v22\n"
    "v_add_u32 v20, v20, v6\n"
    "s_waitcnt lgkmcnt(0)\n"
    "s_branch .Lbtop_%=\n"
    ".Lbpol_%=:\n"
    "v_mov_b32 v20, 64\n"
    "global_load_dword v21, v20, %1 sc1\n"
    "s_mov_b32 s52, 0\n"
    ".Lbp2_%=:\n"
    "s_waitcnt vmcnt(0)\n"
    "v_cmp_eq_u32 vcc, 0, v21\n"
    "s_cmp_eq_u64 vcc, 0\n"
    "s_cbranch_scc1 .Lbp2_x%=\n"
    "s_add_u32 s52, s52, 1\n"
    "s_sleep 2\n"
    "global_load_dword v21, v20, %1 sc1\n"
    "s_cmp_lt_u32 s52, s53\n"
    "s_cbranch_scc1 .Lbp2_%=\n"
    "s_mov_b32 s53, 0\n"
    "v_mov_b32 v22, 0\n"
    "v_mov_b32 v23, 1\n"
    "global_store_dword v22, v23, %1 sc1\n"
    "s_waitcnt vmcnt(0)\n"
    ".Lbp2_x%=:\n"
    "v_readfirstlane_b32 s50, v21\n"
    "v_mbcnt_lo_u32_b32 v20, -1, 0\n"
    "v_mbcnt_hi_u32_b32 v20, -1, v20\n"
    "s_lshl_b32 s51, %2, 6\n"
    "v_add_u32 v20, s51, v20\n"
    "v_min_u32 v20, 129, v20\n"
    "v_lshlrev_b32 v20, 5, v20\n"
    "v_add_u32 v20, 0x26c00, v20\n"
    "ds_read2_b32 v[22:23], v20 offset0:1 offset1:2\n"
    "s_and_b32 s50, s50, 1\n"
    "s_waitcnt lgkmcnt(0)\n"
    "v_and_b32 v21, s50, v22\n"
    "v_lshlrev_b32 v21, 13, v21\n"
    "v_and_b32 v22, -2, v22\n"
    "v_xor_b32 v22, v21, v22\n"
    "v_and_b32 v21, s50, v23\n"
    "v_lshlrev_b32 v21, 13, v21\n"
    "v_and_b32 v23, -2, v23\n"
    "v_xor_b32 v23, v21, v23\n"
    "ds_write2_b32 v20, v22, v23 offset0:1 offset1:2\n"
    "s_waitcnt lgkmcnt(0)\n"
    "s_barrier\n"
    "ds_read_b32 v21, v7 offset:4\n"
    "v_add_u32 v20, %4, v6\n"
    "s_waitcnt lgkmcnt(0)\n"
    "v_readfirstlane_b32 s49, v21\n"
    "s_branch .Lbwait_%=\n"
    ".Lbend_%=:\n"
    "s_cmp_eq_u32 s50, 192\n"
    "s_cbranch_scc0 .Lbpol_%=\n"
    "s_waitcnt vmcnt(0) lgkmcnt(0)\n"
    ".Lrole5_%=:\n"
    ".Lfin:\n"
        :: "s"(wd), "s"(ws), "s"(wave), "s"(rid), "s"(P.h), "s"(P.p), "s"(P.woff), "s"(layer), "s"(w0), "s"(Wk), "s"(Wv), "s"(Wo),
           "s"(mem0), "s"(P.role) : CLOBBERS);
}

extern "C" void kernel_launch(void* const* d_in, const int* in_sizes, int n_in, void* d_out, int out_size, void* d_ws,
                              size_t ws_size, hipStream_t stream) {
    hipMemsetAsync(d_ws, 0, SYNC_BYTES, stream);
    mega_kernel<<<256, 256, 0, stream>>>((const u16*)d_in[0], (const u16*)d_in[1], (const u16*)d_in[2], (const u16*)d_in[4],
                                         (const u16*)d_in[5], (const u16*)d_in[6], (const u16*)d_in[7], (const u16*)d_in[8],
                                         (u16*)d_out, (char*)d_ws);
}
```

```cpp
#include <hip/hip_runtime.h>
#include <hip/hip_bf16.h>
#include <stdint.h>

typedef unsigned short u16;
typedef unsigned u32;
typedef unsigned long long u64;
using bf16x8 = __attribute__((ext_vector_type(8))) short;
using bf16x4 = __attribute__((ext_vector_type(4))) short;
using f32x4 = __attribute__((ext_vector_type(4))) float;
using f32x2 = __attribute__((ext_vector_type(2))) float;
using v4u = __attribute__((ext_vector_type(4))) unsigned;
using v2u = __attribute__((ext_vector_type(2))) unsigned;
typedef __bf16 bfv2 __attribute__((ext_vector_type(2)));
#define GAS __attribute__((address_space(1)))
typedef GAS unsigned gu32;
typedef GAS u64 gu64;
#define RLX_AGENT __ATOMIC_RELAXED, __HIP_MEMORY_SCOPE_AGENT
typedef __amdgpu_buffer_rsrc_t rsrc_t;

constexpr int S_ = 2048, D_ = 576, NSEG = 64, SEG = 32, NB = 4096, NQA = 1728;

constexpr u32 OFF_ABORT = 0;
constexpr u32 OFF_POL = 64;
constexpr u32 OFF_RDY = 128;
constexpr u32 OFF_NEVER = 192;
constexpr u32 OFF_FA = 4096;
constexpr u32 OFF_FO = OFF_FA + 8192;
constexpr u32 OFF_FB = OFF_FO + 8192;
constexpr u32 OFF_FP = OFF_FB + 8192;
constexpr u32 OFF_COB = OFF_FP + 24576;
constexpr u32 OFF_FKV = OFF_COB + 32768;
constexpr u32 OFF_GO = 131072;
constexpr u32 OFF_EP = OFF_GO + 1024;
constexpr u32 SYNC_BYTES = OFF_EP + 128;
constexpr u32 MB2 = 2u << 20;
constexpr u32 OFF_XA = 0;
constexpr u32 OFF_XB = MB2;
constexpr u32 XB = S_ * D_ * 2;
constexpr u32 OFF_TA = 2 * MB2;
constexpr u32 OFF_TB = 6 * MB2;
constexpr u32 OFF_OA = 10 * MB2;
constexpr u32 OFF_PB = 14 * MB2;
constexpr u32 HB_ = 2 * 64 * 32 * 8192;
constexpr u32 OFF_QX = 30 * MB2;
constexpr u32 OFF_KX = OFF_QX + HB_;
constexpr u32 OFF_VX = OFF_KX + HB_;
constexpr u32 OFF_OX = OFF_VX + HB_;
constexpr u32 WS_BYTES = OFF_OX + HB_;
constexpr u32 L_TAB = 147456, L_IT = L_TAB + 11264, L_EX = 138240, L_W = 31936;

#define STAMP(i, k) do {} while (0)
#define PH(k) do {} while (0)
__device__ __forceinline__ float bf2f(u16 v) { return __uint_as_float(((u32)v) << 16); }
__device__ __forceinline__ float bflo(u32 v) { return __uint_as_float(v << 16); }
__device__ __forceinline__ float bfhi(u32 v) { return __uint_as_float(v & 0xffff0000u); }
__device__ __forceinline__ u32 pack2(float a, float b) { bfv2 v = {(__bf16)a, (__bf16)b}; return __builtin_bit_cast(u32, v); }
__device__ __forceinline__ float rbf(float f) { return (float)(__bf16)f; }
__device__ __forceinline__ v4u ld16(rsrc_t r, u32 off) { return __builtin_amdgcn_raw_buffer_load_b128(r, off, 0, 16); }
__device__ __forceinline__ v2u ld8(rsrc_t r, u32 off) { return __builtin_amdgcn_raw_buffer_load_b64(r, off, 0, 16); }
__device__ __forceinline__ void st16(rsrc_t r, u32 off, v4u v) { __builtin_amdgcn_raw_buffer_store_b128(v, r, off, 0, 16); }
__device__ __forceinline__ void st8(rsrc_t r, u32 off, v2u v) { __builtin_amdgcn_raw_buffer_store_b64(v, r, off, 0, 16); }
__device__ __forceinline__ void st4(rsrc_t r, u32 off, u32 v) { __builtin_amdgcn_raw_buffer_store_b32(v, r, off, 0, 16); }
#define DRAIN() asm volatile("s_waitcnt vmcnt(0)" ::: "memory")
#define TOUCH(p) asm volatile("global_load_dword v255, %0, off" :: "v"(p) : "v255")
#define CBAR() asm volatile("" ::: "memory")
__device__ __forceinline__ u32 ldw(char* ws, u32 off) { return __hip_atomic_load((gu32*)(ws + off), RLX_AGENT); }
__device__ __forceinline__ void setw(char* ws, u32 off) { __hip_atomic_store((gu32*)(ws + off), 1u, RLX_AGENT); }
__device__ __forceinline__ u32 fl(u32 i) { return (i >> 5) * 1024 + (i & 31) * 4; }
constexpr u32 SPIN_CAP = 1u << 20;
#define SLP_W 2
#define SLP_C 8
__device__ __forceinline__ void until(u64 t) { while (__builtin_amdgcn_s_memrealtime() < t) __builtin_amdgcn_s_sleep(8); }
#define TDEL 0
__device__ __forceinline__ u64 succ(char* ws) {
    const u64 e = __hip_atomic_load((gu64*)(ws + OFF_EP), RLX_AGENT);
    return (u64)((u32)(e >> 32) + 1) << 32 | ((u32)e + 1);
}
__device__ __forceinline__ void wait_go(char* ws) {
    const u64 e = succ(ws);
    for (u32 n = 0; n < SPIN_CAP && __builtin_amdgcn_ballot_w64(__hip_atomic_load((gu64*)(ws + OFF_GO + (threadIdx.x & 7) * 128), RLX_AGENT) != e); ++n) __builtin_amdgcn_s_sleep(2);
    CBAR();
}
__device__ __forceinline__ bool spin_fail(char* ws, u32& spins) {
    ++spins;
    if ((spins & 63u) == 0u) {
        if (ldw(ws, OFF_ABORT)) return true;
        if (spins > SPIN_CAP) { __hip_atomic_store((gu32*)(ws + OFF_ABORT), 1u, RLX_AGENT); return true; }
    }
    return false;
}
__device__ __forceinline__ void wait_set(char* ws, u32 off) {
    u32 spins = 0;
    while (__builtin_amdgcn_ballot_w64(ldw(ws, off) == 0)) {
        if (spin_fail(ws, spins)) break;
        __builtin_amdgcn_s_sleep(SLP_W);
    }
    CBAR();
}
constexpr u32 OOB = 0x80000000u;
__device__ __forceinline__ u32 cgo(u32 cg) { return (cg >> 5) * 16384 + (cg & 31) * 256; }

template <int N, bool FIN>
__device__ __forceinline__ void t_item(int rid, u32 i, u32 j, u32 m, u32 pol, u32 w, rsrc_t ro, char* ws, rsrc_t rs) {
    const u32 lane = threadIdx.x & 63, s = i & 63, side = (((s >> 5) ^ pol ^ m) & 1) << 13;
    v4u x[N], p[N][4];
    u32 v[N];
    wait_set(ws, OFF_FB + m * 4096 + j * 128 + fl(i));
#pragma unroll
    for (int u = 0; u < N; ++u) { v[u] = cgo(12 * j + 4 * (w + u) + (lane >> 4)) + (lane & 15) * 16; x[u] = ld16(rs, OFF_TB + (i << 16) + (FIN ? side : m << 13) + v[u]); }
    wait_set(ws, OFF_FP + (m * 3 + (j >> 1)) * 4096 + (lane & 3) * 128 + fl(i));
    STAMP(i, 0);
#pragma unroll
    for (int u = 0; u < N; ++u)
#pragma unroll
        for (int k = 0; k < 4; ++k) p[u][k] = __builtin_amdgcn_raw_buffer_load_b128(rs, v[u], OFF_PB + (i << 18) + (k << 16) + ((((i >> 3) ^ pol ^ m) & 1) << 13), 16);
#pragma unroll
    for (int u = 0; u < N; ++u) {
#pragma unroll
        for (int e = 0; e < 4; ++e) {
            f32x2 a = {bflo(x[u][e]), bfhi(x[u][e])};
#pragma unroll
            for (int k = 0; k < 4; ++k) a += f32x2{bflo(p[u][k][e]), bfhi(p[u][k][e])};
            x[u][e] = pack2(a[0], a[1]);
        }
        if (FIN) __builtin_amdgcn_raw_buffer_store_b128(x[u], ro, (s * 32 + 2 * (lane & 15) + m) * 1152 + (12 * j + 4 * (w + u) + (lane >> 4)) * 16, 0, 0);
        else st16(rs, OFF_TA + ((64 + s) << 16) + side + v[u], x[u]);
    }
    STAMP(i, 1);
}
__device__ __forceinline__ bool t_last(u32* cnt) {
    DRAIN();
    u32 o = 0;
    if ((threadIdx.x & 63) == 0) o = __hip_atomic_fetch_add(cnt, 1u, __ATOMIC_RELAXED, __HIP_MEMORY_SCOPE_WORKGROUP);
    return __builtin_amdgcn_readfirstlane(o) == 2;
}
__device__ __forceinline__ void role_t(u64 t_in, int rid, u32 j, u32 m, const u16* x0, u16* out, char* ws, rsrc_t rs, char* smem) {
    const u32 lane = threadIdx.x & 63, w = __builtin_amdgcn_readfirstlane(threadIdx.x >> 6);
    u32* cnt = (u32*)smem + 64;
    cnt[threadIdx.x] = 0;
    __syncthreads();
    const rsrc_t ro = __builtin_amdgcn_make_buffer_rsrc(out, 0, XB, 0x00020000);
    if (w < 3) {
        const rsrc_t rx = __builtin_amdgcn_make_buffer_rsrc((void*)x0, 0, XB, 0x00020000);
        const u32 cg = 12 * j + 4 * w + (lane >> 4), vx = (2 * (lane & 15) + m) * 1152 + cg * 16, v = OFF_TA + (m << 13) + cgo(cg) + (lane & 15) * 16;
#pragma unroll 1
        for (u32 s0 = 0; s0 < 64; s0 += s0 < 4 ? 2 : 10) {
            const u32 n = s0 < 4 ? 2 : 10;
            if (s0 == 14) until(t_in + TDEL);
            v4u x[10];
#pragma unroll
            for (u32 u = 0; u < 10; ++u) x[u] = __builtin_amdgcn_raw_buffer_load_b128(rx, u < n ? vx + (s0 + u) * 36864 : OOB, 0, 0);
#pragma unroll
            for (u32 u = 0; u < 10; ++u) st16(rs, u < n ? v + ((s0 + u) << 16) : OOB, x[u]);
            if (s0 == 0) { wait_go(ws); __builtin_amdgcn_s_barrier(); }
            if (t_last(cnt + s0) && lane < n) setw(ws, OFF_FA + m * 4096 + j * 128 + fl(s0 + lane));
        }
    }
    else __builtin_amdgcn_s_barrier();
    CBAR();
    wait_set(ws, OFF_POL);
    const u32 pol = ldw(ws, OFF_POL);
    if (w == 3) {
        for (u32 i = 64; i < 127; ++i) t_item<3, true>(rid, i, j, m, pol, 0, ro, ws, rs);
        return;
    }
    for (u32 i = 0; i < 64; ++i) {
        t_item<1, false>(rid, i, j, m, pol, w, ro, ws, rs);
        if (t_last(cnt + 64 + i) && lane == 0) setw(ws, OFF_FA + m * 4096 + j * 128 + fl(64 + i));
    }
    t_item<1, true>(rid, 127, j, m, pol, w, ro, ws, rs);
}

#define CLOBBERS "memory", "vcc", "scc", \
    "v0", "v1", "v2", "v3", "v4", "v5", "v6", "v7", "v8", "v9", "v10", "v11", "v12", "v13", "v14", "v15", "v16", "v17", "v18", "v19", "v20", "v21", "v22", "v23", \
    "v24", "v25", "v26", "v27", "v28", "v29", "v30", "v31", "v32", "v33", "v34", "v35", "v36", "v37", "v38", "v39", "v40", "v41", "v42", "v43", "v44", "v45", "v46", "v47", \
    "v48", "v49", "v50", "v51", "v52", "v53", "v54", "v55", "v56", "v57", "v58", "v59", "v60", "v61", "v62", "v63", "v64", "v65", "v66", "v67", "v68", "v69", "v70", "v71", \
    "v72", "v73", "v74", "v75", "v76", "v77", "v78", "v79", "v80", "v81", "v82", "v83", "v84", "v85", "v86", "v87", "v88", "v89", "v90", "v91", "v92", "v93", "v94", "v95", \
    "v96", "v97", "v98", "v99", "v100", "v101", "v102", "v103", "v104", "v105", "v106", "v107", "v108", "v109", "v110", "v111", "v112", "v113", "v114", "v115", "v116", "v117", "v118", "v119", \
    "v120", "v121", "v122", "v123", "v124", "v125", "v126", "v127", "v128", "v129", "v130", "v131", "v132", "v133", "v134", "v135", "v136", "v137", "v138", "v139", "v140", "v141", "v142", "v143", \
    "v144", "v145", "v146", "v147", "v148", "v149", "v150", "v151", "v152", "v153", "v154", "v155", "v156", "v157", "v158", "v159", "v160", "v161", "v162", "v163", "v164", "v165", "v166", "v167", \
    "v168", "v169", "v170", "v171", "v172", "v173", "v174", "v175", "v176", "v177", "v178", "v179", "v180", "v181", "v182", "v183", "v184", "v185", "v186", "v187", "v188", "v189", "v190", "v191", \
    "v192", "v193", "v194", "v195", "v196", "v197", "v198", "v199", "v200", "v201", "v202", "v203", "v204", "v205", "v206", "v207", "v208", "v209", "v210", "v211", "v212", "v213", "v214", "v215", \
    "v216", "v217", "v218", "v219", "v220", "v221", "v222", "v223", "v224", "v225", "v226", "v227", "v228", "v229", "v230", "v231", "v232", "v233", "v234", "v235", "v236", "v237", "v238", "v239", \
    "v240", "v241", "v242", "v243", "v244", "v245", "v246", "v247", "v248", "v249", "v250", "v251", "v252", "v253", "v254", "v255", "a0", "a1", "a2", "a3", "a4", "a5", "a6", "a7", \
    "a8", "a9", "a10", "a11", "a12", "a13", "a14", "a15", "a16", "a17", "a18", "a19", "a20", "a21", "a22", "a23", "a24", "a25", "a26", "a27", "a28", "a29", "a30", "a31", \
    "a32", "a33", "a34", "a35", "a36", "a37", "a38", "a39", "a40", "a41", "a42", "a43", "a44", "a45", "a46", "a47", "a48", "a49", "a50", "a51", "a52", "a53", "a54", "a55", \
    "a56", "a57", "a58", "a59", "a60", "a61", "a62", "a63", "a64", "a65", "a66", "a67", "a68", "a69", "a70", "a71", "a72", "a73", "a74", "a75", "a76", "a77", "a78", "a79", \
    "a80", "a81", "a82", "a83", "a84", "a85", "a86", "a87", "a88", "a89", "a90", "a91", "a92", "a93", "a94", "a95", "a96", "a97", "a98", "a99", "a100", "a101", "a102", "a103", \
    "a104", "a105", "a106", "a107", "a108", "a109", "a110", "a111", "a112", "a113", "a114", "a115", "a116", "a117", "a118", "a119", "a120", "a121", "a122", "a123", "a124", "a125", "a126", "a127", \
    "a128", "a129", "a130", "a131", "a132", "a133", "a134", "a135", "a136", "a137", "a138", "a139", "a140", "a141", "a142", "a143", "a144", "a145", "a146", "a147", "a148", "a149", "a150", "a151", \
    "a152", "a153", "a154", "a155", "a156", "a157", "a158", "a159", "a160", "a161", "a162", "a163", "a164", "a165", "a166", "a167", "a168", "a169", "a170", "a171", "a172", "a173", "a174", "a175", \
    "a176", "a177", "a178", "a179", "a180", "a181", "a182", "a183", "a184", "a185", "a186", "a187", "a188", "a189", "a190", "a191", "a192", "a193", "a194", "a195", "a196", "a197", "a198", "a199", \
    "a200", "a201", "a202", "a203", "a204", "a205", "a206", "a207", "a208", "a209", "a210", "a211", "a212", "a213", "a214", "a215", "a216", "a217", "a218", "a219", "a220", "a221", "a222", "a223", \
    "a224", "a225", "a226", "a227", "a228", "a229", "a230", "a231", "a232", "a233", "a234", "a235", "a236", "a237", "a238", "a239", "a240", "a241", "a242", "a243", "a244", "a245", "a246", "a247", \
    "a248", "a249", "a250", "a251", "a252", "a253", "a254", "a255", "s38", "s39", "s40", "s41", "s42", "s43", "s44", "s45", "s46", "s47", "s48", "s49", "s50", "s51", "s52", "s53", \
    "s54", "s55", "s56", "s57", "s58", "s59", "s60", "s61", "s62", "s63", "s64", "s65", "s66", "s67", "s68", "s69", "s70", "s71", "s72", "s73", "s74", "s75", "s76", "s77", \
    "s78", "s79", "s80", "s81", "s82", "s83", "s84", "s85", "s86", "s87", "s88", "s89", "s90", "s91", "s92", "s93", "s94", "s95", "s96", "s97", "s98", "s99", "s100", "s101"
#define LANES() const int tid = threadIdx.x, lane = tid & 63, c = lane & 15, g = lane >> 4, q = c >> 2, p4 = c & 3; \
    const int wave = __builtin_amdgcn_readfirstlane(tid >> 6); u32* tab = (u32*)(smem + L_TAB) + tid; (void)q; (void)p4; (void)g
struct Par { u32 role, h, p, woff; };
__device__ __forceinline__ u32 rows(u32 i, u32 m) { return (i << 16) + (i < 64 ? m << 13 : (((i >> 5 ^ m) & 1) << 13) + 1); }
__device__ __forceinline__ u32 slot(u32 i) { return ((i & 63) << 19) + (((i >> 2 ^ i >> 6) & 1) << 13) + 1; }
__device__ __forceinline__ u32 part(u32 i, u32 m) { return (i << 18) + (((i >> 3 ^ m) & 1) << 13) + 1; }
template <class FS, class FD>
__device__ __forceinline__ u32 items(u32 K, u32 mk, u32 fin, u32 fout, FS src, FD dst, char* smem) {
    const u32 n = threadIdx.x - 1;
    auto idx = [&](u32 k) { const u32 t = k - K; return t < 2 * (64 - K) ? ((t & 1) ? K : 64) + (t >> 1) : k & 127; };
    if (threadIdx.x < 130) {
        v4u* row = (v4u*)(smem + L_IT) + 2 * threadIdx.x;
        row[0] = v4u{n + 1 == mk ? OFF_NEVER + 4 : n + 1 < 128 ? fin + fl(idx(n + 1)) : OFF_NEVER, src(idx(n + 1)), dst(idx(n)), fout + fl(idx(n - 1))};
        row[1] = v4u{0u, idx(n) & 63 ? 0u : 0xf149f2cau, (idx(n) ^ idx(n - 1)) & 64, idx(n)};
    }
    return fin + fl(mk ? 64 : 0);
}
#define KA 11
#define KB 10
#define KH 9
#define KW 9

__device__ __forceinline__ Par role_h(u32 m, u32 h, char* smem) {
    LANES();
    tab[0] = 0x10000 + wave * 8192 + lane * 16;
    tab[256] = tid * 16;
    tab[2 * 256] = cgo(16 * wave) + lane * 16;
    tab[3 * 256] = 0x6000 + c * 4;
    tab[4 * 256] = 0x10000 + (q * 16 + c) * 16 + p4 * 4;
    tab[5 * 256] = (8 * wave + g) * 8192 + (c ^ ((g | (wave & 1) << 2) << 1)) * 16;
    tab[6 * 256] = (4 * g + q) * 256 + ((2 * wave) ^ (q | (g & 1) << 2)) * 32 + p4 * 8;
    tab[7 * 256] = tab[6 * 256] ^ 32;
    const u32 f = items(KH, 0, OFF_FB + m * 4096, OFF_FKV + (h >> 2) * 4096 + (h & 3) * 256 + m * 128, [&](u32 i) { return OFF_TB + rows(i, m); }, [&](u32 i) { return OFF_QX + h * 16384 + m * 4096 + slot(i); }, smem);
    return Par{1, f, 0, h * 256};
}

__device__ __forceinline__ Par role_w(u32 m, u32 kb, u32 nb, char* smem) {
    LANES();
    tab[0] = wave * 8192 + lane * 16;
    tab[256] = wave * 32768 + lane * 16;
    tab[512] = cgo(24 * nb + 6 * wave + g) + c * 16;
    tab[768] = (256 * wave + 4 * g + (c >> 3)) * 1152 + ((lane & 7) ^ ((g & 1) << 2)) * 16;
#pragma unroll
    for (int t = 0; t < 3; ++t) {
        const int col = wave * 96 + (t < 2 ? p4 * 16 + t * 8 : 64 + p4 * 8);
        tab[(4 + t) * 256] = (col >> 7) * 2048 + (q & 1) * 1024 + (2 * g + (q >> 1)) * 128 + ((col & 127) ^ ((g & 1) << 6));
    }
    tab[7 * 256] = (g & 1) ? OOB : cgo(24 * nb + 6 * wave + 4 + (g >> 1)) + c * 16;
    const u32 f = items(KW, 0, OFF_COB + (m * 4 + kb) * 4096, OFF_FP + (m * 3 + nb) * 4096 + kb * 128, [&](u32 i) { return OFF_OX + kb * 8 * 16384 + m * 4096 + slot(i); },
          [&](u32 i) { return OFF_PB + kb * 65536 + part(i, m); }, smem);
    return Par{2, f, 0, kb * 1024 * 1152 + nb * 384};
}

__device__ __forceinline__ Par role_a2(u32 m, u32 j, char* smem) {
    LANES();
    const int hd = tid >> 2, w = wave < 3 ? wave : 2;
    tab[0] = 0x8000 + lane * 16;
    tab[256] = wave * 4096 + lane * 16;
    tab[512] = cgo(16 * wave) + lane * 16;
    tab[768] = wave < 3 ? cgo(12 * j + 4 * wave + g) + c * 16 : OOB;
    tab[4 * 256] = (16 + (hd >> 4)) * 1024 + (((hd >> 2) & 3) * 16 + 4 * (tid & 3)) * 16 + (hd & 3) * 4;
    tab[5 * 256] = wave < 2 ? 32768 + tid * 16 : OOB;
    tab[6 * 256] = (16 * wave + (lane >> 2)) * 1152 + (lane & 3) * 16;
    tab[7 * 256] = w * 36864 + p4 * 16 + (20 * (g >> 1) + 8 * (g & 1) + q) * 64;
    tab[8 * 256] = w * 36864 + p4 * 16 + (72 * g + 16 + q) * 64;
    const u32 f = items(KB, KB, OFF_FO + m * 4096, OFF_FB + m * 4096 + j * 128, [&](u32 i) { return OFF_OA + rows(i, m); }, [&](u32 i) { return OFF_TB + rows(i, m); }, smem);
    return Par{4, f, 0, j * 192};
}

__device__ __forceinline__ Par role_a1(u32 m, u32 hg, char* smem) {
    LANES();
    const int hd = 4 * hg + wave;
    tab -= 768;
    tab[0] = lane * 16;
    tab[256] = wave * 5120 + lane * 16;
    tab[2 * 256] = !(g & 1) ? cgo(2 * hd + (g >> 1)) + c * 16 : g == 1 && !p4 ? 32768 + hd * 64 + c * 4 : OOB;
    tab[3 * 256] = cgo(16 * wave) + lane * 16;
    tab[4 * 256] = wave < 2 ? 32768 + tid * 16 : OOB;
    tab[5 * 256] = tid * 16;
    tab[6 * 256] = L_EX + ((wave >> 1) * 16 + c) * 16 + (wave & 1) * 8;
    tab[7 * 256] = g ? L_EX + 4096 : tab[6 * 256] + 512;
    tab[8 * 256] = L_EX + 2048 + (g * 16 + 2 * wave + (c & 1)) * 16;
    tab[9 * 256] = 0x7000 + c * 4;
    tab[10 * 256] = (q * 16 + c) * 16 + p4 * 4;
    tab[11 * 256] = 4 * g - c;
    tab[12 * 256] = (8 * g + q) * 144 + 36 * wave + 4 * (wave & 1) + p4 * 8;
    tab[13 * 256] = (wave < 2 ? p4 >> 1 : 2) * 9216 + (8 * g + q) * 144 + 72 * (p4 & 1) + 32;
    if (tid < 2) *(v4u*)(smem + L_EX + 4096 + tid * 1024) = v4u{0u, 0u, 0u, 0u};
    const u32 f = items(KA, KA, OFF_FA + m * 4096, OFF_FO + m * 4096 + hg * 128, [&](u32 i) { return OFF_TA + rows(i, m); }, [&](u32 i) { return OFF_OA + rows(i, m); }, smem);
    return Par{3, f, 0, hg * 144};
}

#define SB() __builtin_amdgcn_sched_barrier(0)
__device__ __forceinline__ u32 now32() { return (u32)__builtin_amdgcn_s_memtime(); }
__device__ __forceinline__ Par role_chain(u32 h, u32 p, char* smem) {
    LANES();
    const int ro = 16 * (q & 1) + 4 * (q >> 1) + 8 * (g >> 1) + 2 * (g & 1);
    tab[0] = lane * 16;
    tab[256] = L_W + (wave * 15 + (wave == 3 ? 2 : 0)) * 2048 + lane * 16;
    tab[2 * 256] = (4 * wave + g) * 8192 + (c ^ (((g & 1) | wave << 1) << 1)) * 16;
    tab[3 * 256] = (16 * (g >> 1) + 4 * (g & 1) + 8 * (q >> 1) + (q & 1)) * 256 + ((2 * wave) ^ ((q & 1) | (g & 1) << 1 | (q >> 1) << 2)) * 32 + p4 * 8;
    tab[4 * 256] = tab[3 * 256] ^ 32;
    tab[5 * 256] = (2 * wave + (lane >> 5)) * 1152 + ((lane & 31) ^ (wave << 1)) * 16;
    tab[6 * 256] = ro * 512 + ((2 * wave) ^ ((g & 1) | (q >> 1) << 1)) * 32 + p4 * 8;
    tab[7 * 256] = tab[6 * 256] ^ 32;
    tab[8 * 256] = (32 * wave + (lane >> 2)) * 1152 + (lane & 3) * 16;
    tab[9 * 256] = ro * 64 + (wave & 1) * 32 + p4 * 8;
    return Par{0, h, p, 0};
}

#define MAP 3
constexpr int RW = 90, RA1 = 24, RA2 = MAP >= 2 ? 184 : 56, RTT = 58;
#define PFO 1
#define WDEL1 700
#define CDEL 1000
#define WDEL 700
#define NPOL 6
static_assert(OFF_GO == 131072u);
static_assert(OFF_EP == 132096u);
static_assert(OFF_POL == 64u);
static_assert(OFF_RDY == 128u);
static_assert(OFF_COB == 53248u);
static_assert(OFF_FKV == 86016u);
static_assert(OFF_XA == 0u);
static_assert(OFF_XB == 2097152u);
static_assert(OFF_TA == 4194304u);
static_assert(OFF_OA == 20971520u);
static_assert(OFF_NEVER == 192u);
static_assert(L_IT == 158720u);
static_assert(OFF_QX == 62914560u);
static_assert(OFF_KX == 96468992u);
static_assert(OFF_VX == 130023424u);
static_assert(OFF_OX == 163577856u);
static_assert(WS_BYTES == 197132288u);
static_assert(L_TAB == 147456u);
static_assert(L_EX == 138240u);
static_assert(L_W == 31936);
__device__ __forceinline__ void measure_pol(char* ws, rsrc_t rs, char* smem) {
    const int wave = __builtin_amdgcn_readfirstlane(threadIdx.x >> 6);
    u32 best[2] = {~0u, ~0u};
    for (int n = 0; n < NPOL; ++n)
#pragma unroll
        for (int i = 0; i < 2; ++i) {
            SB();
            const u32 t0 = now32();
            SB();
            st4(rs, (wave & 1) * MB2 + (508 + 2 * i + (wave >> 1)) * 4096 + n * 128, t0);
            DRAIN();
            const u32 t = now32() - t0;
            best[i] = t < best[i] ? t : best[i];
        }
    int* d = (int*)smem;
    if ((threadIdx.x & 63) == 0) d[wave] = (int)(best[0] - best[1]);
    wait_go(ws);
    __syncthreads();
    if (threadIdx.x == 0) __hip_atomic_store((gu32*)(ws + OFF_POL), 4u | (d[0] + d[2] >= 0) | (d[1] + d[3] >= 0) << 1, RLX_AGENT);
}

__global__ __launch_bounds__(256, 1) void mega_kernel(const u16* __restrict__ x0, const u16* __restrict__ wqkv, const u16* __restrict__ woA,
                                                     const u16* __restrict__ mem0, const u16* __restrict__ Wq, const u16* __restrict__ Wk,
                                                     const u16* __restrict__ Wv, const u16* __restrict__ Wo, u16* out, char* ws) {
    __shared__ __attribute__((aligned(16))) char smem[163840];
    char* wd = (char*)(((uintptr_t)ws + SYNC_BYTES + MB2 - 1) & ~(uintptr_t)(MB2 - 1));
    const rsrc_t rs = __builtin_amdgcn_make_buffer_rsrc(wd, 0, WS_BYTES, 0x00020000);
    const u64 t_in = __builtin_amdgcn_s_memrealtime();
    {
        const char* pc; u32 n;
        asm volatile("s_getpc_b64 %0\n.Lbeg:\ns_mov_b32 %1, .Lfin-.Lbeg" : "=s"(pc), "=s"(n));
        const u32 o = ((blockIdx.x >> 3 & 3) * 256 + threadIdx.x) * 128;
        if (o < n) TOUCH(pc + o);
    }
    u32 rid = (blockIdx.x & 7) * 32 + (blockIdx.x >> 3);
    rid = rid - 24 < 8 ? rid + 120 : rid - 144 < 8 ? rid - 120 : rid;
    asm volatile("" : "+s"(rid));
    STAMP(159, 0);
    const u32 xq = (rid >> 5) & 3, t = rid & 31, layer = rid >> 7;
    const int u = t - 24;
    const int role = t < 16 ? 0 : t < 24 ? 1 : xq == 0 ? 3 : u < 2 ? 4 : xq == 1 ? 5 : 2;
    const int ix = role == 3 ? u : role == 4 ? 2 * xq - 2 + u : role == 5 ? u - 2 : (xq * 6 + u - 5) % 12;
    if (role == 5) { role_t(t_in, rid, ix, layer, x0, out, ws, rs, smem); asm volatile("s_endpgm" ::: "memory"); }
    if (role == 0 && layer) {
        const u32 i = t * 256 + threadIdx.x;
        for (u32 k = xq; k < (xq ? xq + 1 : 4); ++k) if (i < 3888) TOUCH((const char*)wqkv + (k * 3888 + i) * 128);
        if (xq && i < 1728) TOUCH((const char*)woA + (i / 3) * 1152 + (xq - 1) * 384 + (i % 3) * 128);
        for (const u16* w : {Wk, Wv})
            for (u32 k = i; k < 9216; k += 4096) TOUCH((const char*)w + (k >> 4) * 8192 + xq * 2048 + (k & 15) * 128);
        if (PFO) for (u32 k = i; k < 9216; k += 4096) TOUCH((const char*)Wo + (xq * 9216 + k) * 128);
        for (u32 k = 0; k < 12; ++k) st16(rs, (k < 6 ? OFF_XA : OFF_XB - 6 * 262144) + k * 262144 + (xq * 4096 + i) * 16, v4u{~0u, ~0u, ~0u, ~0u});
        DRAIN();
        wait_go(ws);
        __syncthreads();
        if (threadIdx.x == 0) __hip_atomic_fetch_add((gu32*)(ws + OFF_RDY), 1u, RLX_AGENT);
    }
    if (role == 2 && layer && ix < 8) {
        const u64 e = succ(ws);
        const rsrc_t rc = __builtin_amdgcn_make_buffer_rsrc(ws, 0, OFF_GO, 0x00020000);
        for (u32 k = 0; k < 4; ++k) st16(rc, ix * 16384 + k * 4096 + threadIdx.x * 16, v4u{0u, 0u, 0u, 0u});
        DRAIN();
        __syncthreads();
        if (threadIdx.x == 0) __hip_atomic_store((gu64*)(ws + OFF_GO + ix * 128), e, RLX_AGENT);
    }
    if (rid == RW) measure_pol(ws, rs, smem);
    until(t_in + (role == 2 ? (layer ? WDEL1 : WDEL) : role == 0 && layer ? CDEL : 0));
    Par P{5, 0, 0, 0};
    const u16* w0 = role == 2 ? Wo : role == 3 ? wqkv : role == 4 ? woA : Wq;
    if (role == 0) P = role_chain(xq * 8 + (t >> 1), t & 1, smem);
    else if (role == 1) P = role_h(layer, xq * 8 + t - 16, smem);
    else if (role == 2) P = role_w(layer, ix / 3, ix % 3, smem);
    else if (role == 3) P = role_a1(layer, ix, smem);
    else if (role == 4) P = role_a2(layer ^ (MAP >= 2), ix, smem);
    if (role == 0) P.woff = (u32)t_in;
    const int wave = __builtin_amdgcn_readfirstlane(threadIdx.x >> 6);
    asm volatile(
    "s_cmp_eq_u32 %13, 1\n"
    "s_cbranch_scc1 .Lrole1_%=\n"
    "s_cmp_eq_u32 %13, 2\n"
    "s_cbranch_scc1 .Lrole2_%=\n"
    "s_cmp_eq_u32 %13, 3\n"
    "s_cbranch_scc1 .Lrole3_%=\n"
    "s_cmp_eq_u32 %13, 4\n"
    "s_cbranch_scc1 .Lrole4_%=\n"
    "s_cmp_eq_u32 %13, 5\n"
    "s_cbranch_scc1 .Lrole5_%=\n"
    "s_mov_b64 s[40:41], %0\n"
    "s_and_b32 s41, s41, 0xffff\n"
    "s_mov_b32 s42, 0xbc00000\n"
    "s_mov_b32 s43, 0x20000\n"
    "s_mov_b32 s57, 131072\n"
    "v_mbcnt_lo_u32_b32 v10, -1, 0\n"
    "v_mbcnt_hi_u32_b32 v10, -1, v10\n"
    "s_lshl_b32 s73, %2, 8\n"
    "s_add_u32 s73, s73, 147456\n"
    "v_lshl_add_u32 v10, v10, 2, s73\n"
    "ds_read_b32 v0, v10\n"
    "ds_read_b32 v2, v10 offset:1024\n"
    "ds_read_b32 v1, v10 offset:2048\n"
    "ds_read_b32 v3, v10 offset:3072\n"
    "ds_read_b32 v4, v10 offset:4096\n"
    "ds_read_b32 v5, v10 offset:5120\n"
    "ds_read_b32 v6, v10 offset:6144\n"
    "ds_read_b32 v7, v10 offset:7168\n"
    "ds_read_b32 v8, v10 offset:8192\n"
    "ds_read_b32 v9, v10 offset:9216\n"
    "s_waitcnt lgkmcnt(0)\n"
    "s_barrier\n"
    "s_lshl_b32 s60, %4, 8\n"
    "s_lshl_b32 s61, %2, 10\n"
    "s_mul_i32 s63, %4, 147456\n"
    "s_mul_i32 s62, %5, 576\n"
    "s_add_u32 s63, s63, s62\n"
    "s_mov_b64 s[44:45], %11\n"
    "s_and_b32 s45, s45, 0xffff\n"
    "s_mov_b32 s46, 0x480000\n"
    "s_mov_b32 s47, 0x20000\n"
    "s_lshl_b32 s73, s61, 1\n"
    "s_add_u32 m0, s73, 0\n"
    "s_add_u32 s62, s63, 512\n"
    "buffer_load_dwordx4 v8, s[44:47], s62 offen lds\n"
    "s_lshl_b32 s73, s61, 1\n"
    "s_add_u32 m0, s73, 1024\n"
    "s_add_u32 s62, s63, 18944\n"
    "buffer_load_dwordx4 v8, s[44:47], s62 offen lds\n"
    "s_mov_b64 vcc, 0\n"
    "s_mov_b32 vcc_lo, %7\n"
    "s_cbranch_vccz .Lsl0_%=\n"
    "s_sleep 10\n"
    ".Lsl0_%=:\n"
    "s_mov_b64 s[44:45], %8\n"
    "s_and_b32 s45, s45, 0xffff\n"
    "s_mov_b32 s46, 0x480000\n"
    "s_mov_b32 s47, 0x20000\n"
    "s_add_u32 m0, s61, 16384\n"
    "s_add_u32 s62, s60, 2097152\n"
    "buffer_load_dwordx4 v1, s[44:47], s62 offen lds\n"
    "s_add_u32 m0, s61, 20480\n"
    "s_add_u32 s62, s60, 2228224\n"
    "buffer_load_dwordx4 v1, s[44:47], s62 offen lds\n"
    "s_add_u32 m0, s61, 24576\n"
    "s_add_u32 s62, s60, 2359296\n"
    "buffer_load_dwordx4 v1, s[44:47], s62 offen lds\n"
    "s_add_u32 m0, s61, 28672\n"
    "s_add_u32 s62, s60, 2490368\n"
    "buffer_load_dwordx4 v1, s[44:47], s62 offen lds\n"
    "s_cbranch_vccz .Lsl1_%=\n"
    "s_sleep 10\n"
    ".Lsl1_%=:\n"
    "s_add_u32 m0, s61, 32768\n"
    "s_add_u32 s62, s60, 2621440\n"
    "buffer_load_dwordx4 v1, s[44:47], s62 offen lds\n"
    "s_add_u32 m0, s61, 36864\n"
    "s_add_u32 s62, s60, 2752512\n"
    "buffer_load_dwordx4 v1, s[44:47], s62 offen lds\n"
    "s_add_u32 m0, s61, 40960\n"
    "s_add_u32 s62, s60, 2883584\n"
    "buffer_load_dwordx4 v1, s[44:47], s62 offen lds\n"
    "s_add_u32 m0, s61, 45056\n"
    "s_add_u32 s62, s60, 3014656\n"
    "buffer_load_dwordx4 v1, s[44:47], s62 offen lds\n"
    "s_cbranch_vccz .Lsl2_%=\n"
    "s_sleep 10\n"
    ".Lsl2_%=:\n"
    "s_add_u32 m0, s61, 49152\n"
    "s_add_u32 s62, s60, 3145728\n"
    "buffer_load_dwordx4 v1, s[44:47], s62 offen lds\n"
    "s_add_u32 m0, s61, 53248\n"
    "s_add_u32 s62, s60, 3276800\n"
    "buffer_load_dwordx4 v1, s[44:47], s62 offen lds\n"
    "s_add_u32 m0, s61, 57344\n"
    "s_add_u32 s62, s60, 3407872\n"
    "buffer_load_dwordx4 v1, s[44:47], s62 offen lds\n"
    "s_add_u32 m0, s61, 61440\n"
    "s_add_u32 s62, s60, 3538944\n"
    "buffer_load_dwordx4 v1, s[44:47], s62 offen lds\n"
    "s_cbranch_vccz .Lsl3_%=\n"
    "s_sleep 10\n"
    ".Lsl3_%=:\n"
    "s_add_u32 m0, s61, 65536\n"
    "s_add_u32 s62, s60, 3670016\n"
    "buffer_load_dwordx4 v1, s[44:47], s62 offen lds\n"
    "s_add_u32 m0, s61, 69632\n"
    "s_add_u32 s62, s60, 3801088\n"
    "buffer_load_dwordx4 v1, s[44:47], s62 offen lds\n"
    "s_add_u32 m0, s61, 73728\n"
    "s_add_u32 s62, s60, 3932160\n"
    "buffer_load_dwordx4 v1, s[44:47], s62 offen lds\n"
    "s_add_u32 m0, s61, 77824\n"
    "s_add_u32 s62, s60, 4063232\n"
    "buffer_load_dwordx4 v1, s[44:47], s62 offen lds\n"
    "s_cbranch_vccz .Lsl4_%=\n"
    "s_sleep 10\n"
    ".Lsl4_%=:\n"
    "s_add_u32 m0, s61, 81920\n"
    "s_add_u32 s62, s60, 4194304\n"
    "buffer_load_dwordx4 v1, s[44:47], s62 offen lds\n"
    "s_add_u32 m0, s61, 86016\n"
    "s_add_u32 s62, s60, 4325376\n"
    "buffer_load_dwordx4 v1, s[44:47], s62 offen lds\n"
    "s_add_u32 m0, s61, 90112\n"
    "s_add_u32 s62, s60, 4456448\n"
    "buffer_load_dwordx4 v1, s[44:47], s62 offen lds\n"
    "s_add_u32 m0, s61, 94208\n"
    "s_add_u32 s62, s60, 4587520\n"
    "buffer_load_dwordx4 v1, s[44:47], s62 offen lds\n"
    "s_cbranch_vccz .Lsl5_%=\n"
    "s_sleep 10\n"
    ".Lsl5_%=:\n"
    "s_mov_b64 s[44:45], %9\n"
    "s_and_b32 s45, s45, 0xffff\n"
    "s_mov_b32 s46, 0x480000\n"
    "s_mov_b32 s47, 0x20000\n"
    "s_add_u32 m0, s61, 98304\n"
    "s_add_u32 s62, s60, 0\n"
    "buffer_load_dwordx4 v1, s[44:47], s62 offen lds\n"
    "s_add_u32 m0, s61, 102400\n"
    "s_add_u32 s62, s60, 131072\n"
    "buffer_load_dwordx4 v1, s[44:47], s62 offen lds\n"
    "s_add_u32 m0, s61, 106496\n"
    "s_add_u32 s62, s60, 262144\n"
    "buffer_load_dwordx4 v1, s[44:47], s62 offen lds\n"
    "s_add_u32 m0, s61, 110592\n"
    "s_add_u32 s62, s60, 393216\n"
    "buffer_load_dwordx4 v1, s[44:47], s62 offen lds\n"
    "s_cbranch_vccz .Lsl6_%=\n"
    "s_sleep 10\n"
    ".Lsl6_%=:\n"
    "s_add_u32 m0, s61, 114688\n"
    "s_add_u32 s62, s60, 524288\n"
    "buffer_load_dwordx4 v1, s[44:47], s62 offen lds\n"
    "s_add_u32 m0, s61, 118784\n"
    "s_add_u32 s62, s60, 655360\n"
    "buffer_load_dwordx4 v1, s[44:47], s62 offen lds\n"
    "s_add_u32 m0, s61, 122880\n"
    "s_add_u32 s62, s60, 786432\n"
    "buffer_load_dwordx4 v1, s[44:47], s62 offen lds\n"
    "s_add_u32 m0, s61, 126976\n"
    "s_add_u32 s62, s60, 917504\n"
    "buffer_load_dwordx4 v1, s[44:47], s62 offen lds\n"
    "s_cbranch_vccz .Lsl7_%=\n"
    "s_sleep 10\n"
    ".Lsl7_%=:\n"
    "s_add_u32 m0, s61, 131072\n"
    "s_add_u32 s62, s60, 1048576\n"
    "buffer_load_dwordx4 v1, s[44:47], s62 offen lds\n"
    "s_add_u32 m0, s61, 135168\n"
    "s_add_u32 s62, s60, 1179648\n"
    "buffer_load_dwordx4 v1, s[44:47], s62 offen lds\n"
    "s_add_u32 m0, s61, 139264\n"
    "s_add_u32 s62, s60, 1310720\n"
    "buffer_load_dwordx4 v1, s[44:47], s62 offen lds\n"
    "s_add_u32 m0, s61, 143360\n"
    "s_add_u32 s62, s60, 1441792\n"
    "buffer_load_dwordx4 v1, s[44:47], s62 offen lds\n"
    "s_cbranch_vccz .Lsl8_%=\n"
    "s_sleep 10\n"
    ".Lsl8_%=:\n"
    "s_add_u32 m0, s61, 147456\n"
    "s_add_u32 s62, s60, 1572864\n"
    "buffer_load_dwordx4 v1, s[44:47], s62 offen lds\n"
    "s_add_u32 m0, s61, 151552\n"
    "s_add_u32 s62, s60, 1703936\n"
    "buffer_load_dwordx4 v1, s[44:47], s62 offen lds\n"
    "s_add_u32 m0, s61, 155648\n"
    "s_add_u32 s62, s60, 1835008\n"
    "buffer_load_dwordx4 v1, s[44:47], s62 offen lds\n"
    "s_add_u32 m0, s61, 159744\n"
    "s_add_u32 s62, s60, 1966080\n"
    "buffer_load_dwordx4 v1, s[44:47], s62 offen lds\n"
    "s_waitcnt vmcnt(32) lgkmcnt(0)\n"
    "s_barrier\n"
    "ds_read_b64_tr_b16 v[144:145], v3 offset:16384\n"
    "ds_read_b64_tr_b16 v[146:147], v3 offset:16896\n"
    "ds_read_b64_tr_b16 v[148:149], v3 offset:24576\n"
    "ds_read_b64_tr_b16 v[150:151], v3 offset:25088\n"
    "ds_read_b64_tr_b16 v[152:153], v4 offset:16384\n"
    "ds_read_b64_tr_b16 v[154:155], v4 offset:16896\n"
    "ds_read_b64_tr_b16 v[156:157], v4 offset:24576\n"
    "ds_read_b64_tr_b16 v[158:159], v4 offset:25088\n"
    "s_waitcnt vmcnt(28) lgkmcnt(0)\n"
    "s_barrier\n"
    "s_cbranch_vccz .Lsl9_%=\n"
    "s_sleep 10\n"
    ".Lsl9_%=:\n"
    "s_add_u32 m0, s61, 16384\n"
    "s_add_u32 s62, s60, 2097152\n"
    "buffer_load_dwordx4 v1, s[44:47], s62 offen lds\n"
    "s_add_u32 m0, s61, 20480\n"
    "s_add_u32 s62, s60, 2228224\n"
    "buffer_load_dwordx4 v1, s[44:47], s62 offen lds\n"
    "s_add_u32 m0, s61, 24576\n"
    "s_add_u32 s62, s60, 2359296\n"
    "buffer_load_dwordx4 v1, s[44:47], s62 offen lds\n"
    "s_add_u32 m0, s61, 28672\n"
    "s_add_u32 s62, s60, 2490368\n"
    "buffer_load_dwordx4 v1, s[44:47], s62 offen lds\n"
    "ds_read_b64_tr_b16 v[160:161], v3 offset:32768\n"
    "ds_read_b64_tr_b16 v[162:163], v3 offset:33280\n"
    "ds_read_b64_tr_b16 v[164:165], v3 offset:40960\n"
    "ds_read_b64_tr_b16 v[166:167], v3 offset:41472\n"
    "ds_read_b64_tr_b16 v[168:169], v4 offset:32768\n"
    "ds_read_b64_tr_b16 v[170:171], v4 offset:33280\n"
    "ds_read_b64_tr_b16 v[172:173], v4 offset:40960\n"
    "ds_read_b64_tr_b16 v[174:175], v4 offset:41472\n"
    "s_waitcnt vmcnt(28) lgkmcnt(0)\n"
    "s_barrier\n"
    "s_cbranch_vccz .Lsl10_%=\n"
    "s_sleep 10\n"
    ".Lsl10_%=:\n"
    "s_add_u32 m0, s61, 32768\n"
    "s_add_u32 s62, s60, 2621440\n"
    "buffer_load_dwordx4 v1, s[44:47], s62 offen lds\n"
    "s_add_u32 m0, s61, 36864\n"
    "s_add_u32 s62, s60, 2752512\n"
    "buffer_load_dwordx4 v1, s[44:47], s62 offen lds\n"
    "s_add_u32 m0, s61, 40960\n"
    "s_add_u32 s62, s60, 2883584\n"
    "buffer_load_dwordx4 v1, s[44:47], s62 offen lds\n"
    "s_add_u32 m0, s61, 45056\n"
    "s_add_u32 s62, s60, 3014656\n"
    "buffer_load_dwordx4 v1, s[44:47], s62 offen lds\n"
    "ds_read_b64_tr_b16 v[176:177], v3 offset:49152\n"
    "ds_read_b64_tr_b16 v[178:179], v3 offset:49664\n"
    "ds_read_b64_tr_b16 v[180:181], v3 offset:57344\n"
    "ds_read_b64_tr_b16 v[182:183], v3 offset:57856\n"
    "ds_read_b64_tr_b16 v[184:185], v4 offset:49152\n"
    "ds_read_b64_tr_b16 v[186:187], v4 offset:49664\n"
    "ds_read_b64_tr_b16 v[188:189], v4 offset:57344\n"
    "ds_read_b64_tr_b16 v[190:191], v4 offset:57856\n"
    "s_waitcnt vmcnt(28) lgkmcnt(0)\n"
    "s_barrier\n"
    "s_cbranch_vccz .Lsl11_%=\n"
    "s_sleep 10\n"
    ".Lsl11_%=:\n"
    "s_add_u32 m0, s61, 49152\n"
    "s_add_u32 s62, s60, 3145728\n"
    "buffer_load_dwordx4 v1, s[44:47], s62 offen lds\n"
    "s_add_u32 m0, s61, 53248\n"
    "s_add_u32 s62, s60, 3276800\n"
    "buffer_load_dwordx4 v1, s[44:47], s62 offen lds\n"
    "s_add_u32 m0, s61, 57344\n"
    "s_add_u32 s62, s60, 3407872\n"
    "buffer_load_dwordx4 v1, s[44:47], s62 offen lds\n"
    "s_add_u32 m0, s61, 61440\n"
    "s_add_u32 s62, s60, 3538944\n"
    "buffer_load_dwordx4 v1, s[44:47], s62 offen lds\n"
    "v_add_u32 v31, 0x10000, v3\n"
    "ds_read_b64_tr_b16 v[192:193], v31\n"
    "ds_read_b64_tr_b16 v[194:195], v31 offset:512\n"
    "ds_read_b64_tr_b16 v[196:197], v31 offset:8192\n"
    "ds_read_b64_tr_b16 v[198:199], v31 offset:8704\n"
    "v_add_u32 v30, 0x10000, v4\n"
    "ds_read_b64_tr_b16 v[200:201], v30\n"
    "ds_read_b64_tr_b16 v[202:203], v30 offset:512\n"
    "ds_read_b64_tr_b16 v[204:205], v30 offset:8192\n"
    "ds_read_b64_tr_b16 v[206:207], v30 offset:8704\n"
    "s_waitcnt vmcnt(28) lgkmcnt(0)\n"
    "s_barrier\n"
    "s_cbranch_vccz .Lsl12_%=\n"
    "s_sleep 10\n"
    ".Lsl12_%=:\n"
    "s_add_u32 m0, s61, 65536\n"
    "s_add_u32 s62, s60, 3670016\n"
    "buffer_load_dwordx4 v1, s[44:47], s62 offen lds\n"
    "s_add_u32 m0, s61, 69632\n"
    "s_add_u32 s62, s60, 3801088\n"
    "buffer_load_dwordx4 v1, s[44:47], s62 offen lds\n"
    "s_add_u32 m0, s61, 73728\n"
    "s_add_u32 s62, s60, 3932160\n"
    "buffer_load_dwordx4 v1, s[44:47], s62 offen lds\n"
    "s_add_u32 m0, s61, 77824\n"
    "s_add_u32 s62, s60, 4063232\n"
    "buffer_load_dwordx4 v1, s[44:47], s62 offen lds\n"
    "ds_read_b64_tr_b16 v[208:209], v31 offset:16384\n"
    "ds_read_b64_tr_b16 v[210:211], v31 offset:16896\n"
    "ds_read_b64_tr_b16 v[212:213], v31 offset:24576\n"
    "ds_read_b64_tr_b16 v[214:215], v31 offset:25088\n"
    "ds_read_b64_tr_b16 v[216:217], v30 offset:16384\n"
    "ds_read_b64_tr_b16 v[218:219], v30 offset:16896\n"
    "ds_read_b64_tr_b16 v[220:221], v30 offset:24576\n"
    "ds_read_b64_tr_b16 v[222:223], v30 offset:25088\n"
    "s_waitcnt vmcnt(28) lgkmcnt(0)\n"
    "s_barrier\n"
    "s_cbranch_vccz .Lsl13_%=\n"
    "s_sleep 10\n"
    ".Lsl13_%=:\n"
    "s_add_u32 m0, s61, 81920\n"
    "s_add_u32 s62, s60, 4194304\n"
    "buffer_load_dwordx4 v1, s[44:47], s62 offen lds\n"
    "s_add_u32 m0, s61, 86016\n"
    "s_add_u32 s62, s60, 4325376\n"
    "buffer_load_dwordx4 v1, s[44:47], s62 offen lds\n"
    "s_add_u32 m0, s61, 90112\n"
    "s_add_u32 s62, s60, 4456448\n"
    "buffer_load_dwordx4 v1, s[44:47], s62 offen lds\n"
    "s_add_u32 m0, s61, 94208\n"
    "s_add_u32 s62, s60, 4587520\n"
    "buffer_load_dwordx4 v1, s[44:47], s62 offen lds\n"
    "ds_read_b64_tr_b16 v[224:225], v31 offset:32768\n"
    "ds_read_b64_tr_b16 v[226:227], v31 offset:33280\n"
    "ds_read_b64_tr_b16 v[228:229], v31 offset:40960\n"
    "ds_read_b64_tr_b16 v[230:231], v31 offset:41472\n"
    "ds_read_b64_tr_b16 v[232:233], v30 offset:32768\n"
    "ds_read_b64_tr_b16 v[234:235], v30 offset:33280\n"
    "ds_read_b64_tr_b16 v[236:237], v30 offset:40960\n"
    "ds_read_b64_tr_b16 v[238:239], v30 offset:41472\n"
    "s_waitcnt vmcnt(28) lgkmcnt(0)\n"
    "s_barrier\n"
    "s_cbranch_vccz .Lsl14_%=\n"
    "s_sleep 10\n"
    ".Lsl14_%=:\n"
    "s_mov_b64 s[44:45], %10\n"
    "s_and_b32 s45, s45, 0xffff\n"
    "s_mov_b32 s46, 0x480000\n"
    "s_mov_b32 s47, 0x20000\n"
    "s_add_u32 m0, s61, 98304\n"
    "s_add_u32 s62, s60, 0\n"
    "buffer_load_dwordx4 v1, s[44:47], s62 offen lds\n"
    "s_add_u32 m0, s61, 102400\n"
    "s_add_u32 s62, s60, 131072\n"
    "buffer_load_dwordx4 v1, s[44:47], s62 offen lds\n"
    "s_add_u32 m0, s61, 106496\n"
    "s_add_u32 s62, s60, 262144\n"
    "buffer_load_dwordx4 v1, s[44:47], s62 offen lds\n"
    "s_add_u32 m0, s61, 110592\n"
    "s_add_u32 s62, s60, 393216\n"
    "buffer_load_dwordx4 v1, s[44:47], s62 offen lds\n"
    "ds_read_b64_tr_b16 v[240:241], v31 offset:49152\n"
    "ds_read_b64_tr_b16 v[242:243], v31 offset:49664\n"
    "ds_read_b64_tr_b16 v[244:245], v31 offset:57344\n"
    "ds_read_b64_tr_b16 v[246:247], v31 offset:57856\n"
    "ds_read_b64_tr_b16 v[248:249], v30 offset:49152\n"
    "ds_read_b64_tr_b16 v[250:251], v30 offset:49664\n"
    "ds_read_b64_tr_b16 v[252:253], v30 offset:57344\n"
    "ds_read_b64_tr_b16 v[254:255], v30 offset:57856\n"
    "s_waitcnt vmcnt(28) lgkmcnt(0)\n"
    "s_barrier\n"
    "s_cbranch_vccz .Lsl15_%=\n"
    "s_sleep 10\n"
    ".Lsl15_%=:\n"
    "s_add_u32 m0, s61, 114688\n"
    "s_add_u32 s62, s60, 524288\n"
    "buffer_load_dwordx4 v1, s[44:47], s62 offen lds\n"
    "s_add_u32 m0, s61, 118784\n"
    "s_add_u32 s62, s60, 655360\n"
    "buffer_load_dwordx4 v1, s[44:47], s62 offen lds\n"
    "s_add_u32 m0, s61, 122880\n"
    "s_add_u32 s62, s60, 786432\n"
    "buffer_load_dwordx4 v1, s[44:47], s62 offen lds\n"
    "s_add_u32 m0, s61, 126976\n"
    "s_add_u32 s62, s60, 917504\n"
    "buffer_load_dwordx4 v1, s[44:47], s62 offen lds\n"
    "v_add_u32 v29, 0x20000, v3\n"
    "ds_read_b64_tr_b16 a[0:1], v29\n"
    "ds_read_b64_tr_b16 a[2:3], v29 offset:512\n"
    "ds_read_b64_tr_b16 a[4:5], v29 offset:8192\n"
    "ds_read_b64_tr_b16 a[6:7], v29 offset:8704\n"
    "v_add_u32 v28, 0x20000, v4\n"
    "ds_read_b64_tr_b16 a[8:9], v28\n"
    "ds_read_b64_tr_b16 a[10:11], v28 offset:512\n"
    "ds_read_b64_tr_b16 a[12:13], v28 offset:8192\n"
    "ds_read_b64_tr_b16 a[14:15], v28 offset:8704\n"
    "s_waitcnt vmcnt(28) lgkmcnt(0)\n"
    "s_barrier\n"
    "s_cbranch_vccz .Lsl16_%=\n"
    "s_sleep 10\n"
    ".Lsl16_%=:\n"
    "s_add_u32 m0, s61, 131072\n"
    "s_add_u32 s62, s60, 1048576\n"
    "buffer_load_dwordx4 v1, s[44:47], s62 offen lds\n"
    "s_add_u32 m0, s61, 135168\n"
    "s_add_u32 s62, s60, 1179648\n"
    "buffer_load_dwordx4 v1, s[44:47], s62 offen lds\n"
    "s_add_u32 m0, s61, 139264\n"
    "s_add_u32 s62, s60, 1310720\n"
    "buffer_load_dwordx4 v1, s[44:47], s62 offen lds\n"
    "s_add_u32 m0, s61, 143360\n"
    "s_add_u32 s62, s60, 1441792\n"
    "buffer_load_dwordx4 v1, s[44:47], s62 offen lds\n"
    "ds_read_b64_tr_b16 a[16:17], v29 offset:16384\n"
    "ds_read_b64_tr_b16 a[18:19], v29 offset:16896\n"
    "ds_read_b64_tr_b16 a[20:21], v29 offset:24576\n"
    "ds_read_b64_tr_b16 a[22:23], v29 offset:25088\n"
    "ds_read_b64_tr_b16 a[24:25], v28 offset:16384\n"
    "ds_read_b64_tr_b16 a[26:27], v28 offset:16896\n"
    "ds_read_b64_tr_b16 a[28:29], v28 offset:24576\n"
    "ds_read_b64_tr_b16 a[30:31], v28 offset:25088\n"
    "s_waitcnt vmcnt(28) lgkmcnt(0)\n"
    "s_barrier\n"
    "s_cbranch_vccz .Lsl17_%=\n"
    "s_sleep 10\n"
    ".Lsl17_%=:\n"
    "s_add_u32 m0, s61, 147456\n"
    "s_add_u32 s62, s60, 1572864\n"
    "buffer_load_dwordx4 v1, s[44:47], s62 offen lds\n"
    "s_add_u32 m0, s61, 151552\n"
    "s_add_u32 s62, s60, 1703936\n"
    "buffer_load_dwordx4 v1, s[44:47], s62 offen lds\n"
    "s_add_u32 m0, s61, 155648\n"
    "s_add_u32 s62, s60, 1835008\n"
    "buffer_load_dwordx4 v1, s[44:47], s62 offen lds\n"
    "s_add_u32 m0, s61, 159744\n"
    "s_add_u32 s62, s60, 1966080\n"
    "buffer_load_dwordx4 v1, s[44:47], s62 offen lds\n"
    "ds_read_b64_tr_b16 a[32:33], v3 offset:16384\n"
    "ds_read_b64_tr_b16 a[34:35], v3 offset:16896\n"
    "ds_read_b64_tr_b16 a[36:37], v3 offset:24576\n"
    "ds_read_b64_tr_b16 a[38:39], v3 offset:25088\n"
    "ds_read_b64_tr_b16 a[40:41], v4 offset:16384\n"
    "ds_read_b64_tr_b16 a[42:43], v4 offset:16896\n"
    "ds_read_b64_tr_b16 a[44:45], v4 offset:24576\n"
    "ds_read_b64_tr_b16 a[46:47], v4 offset:25088\n"
    "s_waitcnt vmcnt(28) lgkmcnt(0)\n"
    "s_barrier\n"
    "s_cbranch_vccz .Lsl18_%=\n"
    "s_sleep 10\n"
    ".Lsl18_%=:\n"
    "s_add_u32 m0, s61, 16384\n"
    "s_add_u32 s62, s60, 2097152\n"
    "buffer_load_dwordx4 v1, s[44:47], s62 offen lds\n"
    "s_add_u32 m0, s61, 20480\n"
    "s_add_u32 s62, s60, 2228224\n"
    "buffer_load_dwordx4 v1, s[44:47], s62 offen lds\n"
    "s_add_u32 m0, s61, 24576\n"
    "s_add_u32 s62, s60, 2359296\n"
    "buffer_load_dwordx4 v1, s[44:47], s62 offen lds\n"
    "s_add_u32 m0, s61, 28672\n"
    "s_add_u32 s62, s60, 2490368\n"
    "buffer_load_dwordx4 v1, s[44:47], s62 offen lds\n"
    "ds_read_b64_tr_b16 a[48:49], v3 offset:32768\n"
    "ds_read_b64_tr_b16 a[50:51], v3 offset:33280\n"
    "ds_read_b64_tr_b16 a[52:53], v3 offset:40960\n"
    "ds_read_b64_tr_b16 a[54:55], v3 offset:41472\n"
    "ds_read_b64_tr_b16 a[56:57], v4 offset:32768\n"
    "ds_read_b64_tr_b16 a[58:59], v4 offset:33280\n"
    "ds_read_b64_tr_b16 a[60:61], v4 offset:40960\n"
    "ds_read_b64_tr_b16 a[62:63], v4 offset:41472\n"
    "s_waitcnt vmcnt(28) lgkmcnt(0)\n"
    "s_barrier\n"
    "s_cbranch_vccz .Lsl19_%=\n"
    "s_sleep 10\n"
    ".Lsl19_%=:\n"
    "s_add_u32 m0, s61, 32768\n"
    "s_add_u32 s62, s60, 2621440\n"
    "buffer_load_dwordx4 v1, s[44:47], s62 offen lds\n"
    "s_add_u32 m0, s61, 36864\n"
    "s_add_u32 s62, s60, 2752512\n"
    "buffer_load_dwordx4 v1, s[44:47], s62 offen lds\n"
    "s_add_u32 m0, s61, 40960\n"
    "s_add_u32 s62, s60, 2883584\n"
    "buffer_load_dwordx4 v1, s[44:47], s62 offen lds\n"
    "s_add_u32 m0, s61, 45056\n"
    "s_add_u32 s62, s60, 3014656\n"
    "buffer_load_dwordx4 v1, s[44:47], s62 offen lds\n"
    "ds_read_b64_tr_b16 a[64:65], v3 offset:49152\n"
    "ds_read_b64_tr_b16 a[66:67], v3 offset:49664\n"
    "ds_read_b64_tr_b16 a[68:69], v3 offset:57344\n"
    "ds_read_b64_tr_b16 a[70:71], v3 offset:57856\n"
    "ds_read_b64_tr_b16 a[72:73], v4 offset:49152\n"
    "ds_read_b64_tr_b16 a[74:75], v4 offset:49664\n"
    "ds_read_b64_tr_b16 a[76:77], v4 offset:57344\n"
    "ds_read_b64_tr_b16 a[78:79], v4 offset:57856\n"
    "s_waitcnt vmcnt(28) lgkmcnt(0)\n"
    "s_barrier\n"
    "s_cbranch_vccz .Lsl20_%=\n"
    "s_sleep 10\n"
    ".Lsl20_%=:\n"
    "s_add_u32 m0, s61, 49152\n"
    "s_add_u32 s62, s60, 3145728\n"
    "buffer_load_dwordx4 v1, s[44:47], s62 offen lds\n"
    "s_add_u32 m0, s61, 53248\n"
    "s_add_u32 s62, s60, 3276800\n"
    "buffer_load_dwordx4 v1, s[44:47], s62 offen lds\n"
    "s_add_u32 m0, s61, 57344\n"
    "s_add_u32 s62, s60, 3407872\n"
    "buffer_load_dwordx4 v1, s[44:47], s62 offen lds\n"
    "s_add_u32 m0, s61, 61440\n"
    "s_add_u32 s62, s60, 3538944\n"
    "buffer_load_dwordx4 v1, s[44:47], s62 offen lds\n"
    "ds_read_b64_tr_b16 a[80:81], v31\n"
    "ds_read_b64_tr_b16 a[82:83], v31 offset:512\n"
    "ds_read_b64_tr_b16 a[84:85], v31 offset:8192\n"
    "ds_read_b64_tr_b16 a[86:87], v31 offset:8704\n"
    "ds_read_b64_tr_b16 a[88:89], v30\n"
    "ds_read_b64_tr_b16 a[90:91], v30 offset:512\n"
    "ds_read_b64_tr_b16 a[92:93], v30 offset:8192\n"
    "ds_read_b64_tr_b16 a[94:95], v30 offset:8704\n"
    "s_waitcnt vmcnt(28) lgkmcnt(0)\n"
    "s_barrier\n"
    "s_cbranch_vccz .Lsl21_%=\n"
    "s_sleep 10\n"
    ".Lsl21_%=:\n"
    "s_add_u32 m0, s61, 65536\n"
    "s_add_u32 s62, s60, 3670016\n"
    "buffer_load_dwordx4 v1, s[44:47], s62 offen lds\n"
    "s_add_u32 m0, s61, 69632\n"
    "s_add_u32 s62, s60, 3801088\n"
    "buffer_load_dwordx4 v1, s[44:47], s62 offen lds\n"
    "s_add_u32 m0, s61, 73728\n"
    "s_add_u32 s62, s60, 3932160\n"
    "buffer_load_dwordx4 v1, s[44:47], s62 offen lds\n"
    "s_add_u32 m0, s61, 77824\n"
    "s_add_u32 s62, s60, 4063232\n"
    "buffer_load_dwordx4 v1, s[44:47], s62 offen lds\n"
    "ds_read_b64_tr_b16 a[96:97], v31 offset:16384\n"
    "ds_read_b64_tr_b16 a[98:99], v31 offset:16896\n"
    "ds_read_b64_tr_b16 a[100:101], v31 offset:24576\n"
    "ds_read_b64_tr_b16 a[102:103], v31 offset:25088\n"
    "ds_read_b64_tr_b16 a[104:105], v30 offset:16384\n"
    "ds_read_b64_tr_b16 a[106:107], v30 offset:16896\n"
    "ds_read_b64_tr_b16 a[108:109], v30 offset:24576\n"
    "ds_read_b64_tr_b16 a[110:111], v30 offset:25088\n"
    "s_waitcnt vmcnt(28) lgkmcnt(0)\n"
    "s_barrier\n"
    "s_cbranch_vccz .Lsl22_%=\n"
    "s_sleep 10\n"
    ".Lsl22_%=:\n"
    "s_add_u32 m0, s61, 81920\n"
    "s_add_u32 s62, s60, 4194304\n"
    "buffer_load_dwordx4 v1, s[44:47], s62 offen lds\n"
    "s_add_u32 m0, s61, 86016\n"
    "s_add_u32 s62, s60, 4325376\n"
    "buffer_load_dwordx4 v1, s[44:47], s62 offen lds\n"
    "s_add_u32 m0, s61, 90112\n"
    "s_add_u32 s62, s60, 4456448\n"
    "buffer_load_dwordx4 v1, s[44:47], s62 offen lds\n"
    "s_add_u32 m0, s61, 94208\n"
    "s_add_u32 s62, s60, 4587520\n"
    "buffer_load_dwordx4 v1, s[44:47], s62 offen lds\n"
    "ds_read_b64_tr_b16 a[112:113], v31 offset:32768\n"
    "ds_read_b64_tr_b16 a[114:115], v31 offset:33280\n"
    "ds_read_b64_tr_b16 a[116:117], v31 offset:40960\n"
    "ds_read_b64_tr_b16 a[118:119], v31 offset:41472\n"
    "ds_read_b64_tr_b16 a[120:121], v30 offset:32768\n"
    "ds_read_b64_tr_b16 a[122:123], v30 offset:33280\n"
    "ds_read_b64_tr_b16 a[124:125], v30 offset:40960\n"
    "ds_read_b64_tr_b16 a[126:127], v30 offset:41472\n"
    "s_waitcnt vmcnt(28) lgkmcnt(0)\n"
    "s_barrier\n"
    "s_cbranch_vccz .Lsl23_%=\n"
    "s_sleep 10\n"
    ".Lsl23_%=:\n"
    "s_mov_b64 s[44:45], %8\n"
    "s_and_b32 s45, s45, 0xffff\n"
    "s_mov_b32 s46, 0x480000\n"
    "s_mov_b32 s47, 0x20000\n"
    "s_add_u32 m0, s61, 98304\n"
    "s_add_u32 s62, s60, 0\n"
    "buffer_load_dwordx4 v1, s[44:47], s62 offen lds\n"
    "s_add_u32 m0, s61, 102400\n"
    "s_add_u32 s62, s60, 131072\n"
    "buffer_load_dwordx4 v1, s[44:47], s62 offen lds\n"
    "s_add_u32 m0, s61, 106496\n"
    "s_add_u32 s62, s60, 262144\n"
    "buffer_load_dwordx4 v1, s[44:47], s62 offen lds\n"
    "s_add_u32 m0, s61, 110592\n"
    "s_add_u32 s62, s60, 393216\n"
    "buffer_load_dwordx4 v1, s[44:47], s62 offen lds\n"
    "ds_read_b64_tr_b16 a[128:129], v31 offset:49152\n"
    "ds_read_b64_tr_b16 a[130:131], v31 offset:49664\n"
    "ds_read_b64_tr_b16 a[132:133], v31 offset:57344\n"
    "ds_read_b64_tr_b16 a[134:135], v31 offset:57856\n"
    "ds_read_b64_tr_b16 a[136:137], v30 offset:49152\n"
    "ds_read_b64_tr_b16 a[138:139], v30 offset:49664\n"
    "ds_read_b64_tr_b16 a[140:141], v30 offset:57344\n"
    "ds_read_b64_tr_b16 a[142:143], v30 offset:57856\n"
    "s_waitcnt vmcnt(28) lgkmcnt(0)\n"
    "s_barrier\n"
    "s_cbranch_vccz .Lsl24_%=\n"
    "s_sleep 10\n"
    ".Lsl24_%=:\n"
    "s_add_u32 m0, s61, 114688\n"
    "s_add_u32 s62, s60, 524288\n"
    "buffer_load_dwordx4 v1, s[44:47], s62 offen lds\n"
    "s_add_u32 m0, s61, 118784\n"
    "s_add_u32 s62, s60, 655360\n"
    "buffer_load_dwordx4 v1, s[44:47], s62 offen lds\n"
    "s_add_u32 m0, s61, 122880\n"
    "s_add_u32 s62, s60, 786432\n"
    "buffer_load_dwordx4 v1, s[44:47], s62 offen lds\n"
    "s_add_u32 m0, s61, 126976\n"
    "s_add_u32 s62, s60, 917504\n"
    "buffer_load_dwordx4 v1, s[44:47], s62 offen lds\n"
    "ds_read_b64_tr_b16 a[144:145], v29\n"
    "ds_read_b64_tr_b16 a[146:147], v29 offset:512\n"
    "ds_read_b64_tr_b16 a[148:149], v29 offset:8192\n"
    "ds_read_b64_tr_b16 a[150:151], v29 offset:8704\n"
    "ds_read_b64_tr_b16 a[152:153], v28\n"
    "ds_read_b64_tr_b16 a[154:155], v28 offset:512\n"
    "ds_read_b64_tr_b16 a[156:157], v28 offset:8192\n"
    "ds_read_b64_tr_b16 a[158:159], v28 offset:8704\n"
    "s_waitcnt vmcnt(28) lgkmcnt(0)\n"
    "s_barrier\n"
    "s_cbranch_vccz .Lsl25_%=\n"
    "s_sleep 10\n"
    ".Lsl25_%=:\n"
    "s_add_u32 m0, s61, 131072\n"
    "s_add_u32 s62, s60, 1048576\n"
    "buffer_load_dwordx4 v1, s[44:47], s62 offen lds\n"
    "s_add_u32 m0, s61, 135168\n"
    "s_add_u32 s62, s60, 1179648\n"
    "buffer_load_dwordx4 v1, s[44:47], s62 offen lds\n"
    "s_add_u32 m0, s61, 139264\n"
    "s_add_u32 s62, s60, 1310720\n"
    "buffer_load_dwordx4 v1, s[44:47], s62 offen lds\n"
    "s_add_u32 m0, s61, 143360\n"
    "s_add_u32 s62, s60, 1441792\n"
    "buffer_load_dwordx4 v1, s[44:47], s62 offen lds\n"
    "ds_read_b64_tr_b16 a[160:161], v29 offset:16384\n"
    "ds_read_b64_tr_b16 a[162:163], v29 offset:16896\n"
    "ds_read_b64_tr_b16 a[164:165], v29 offset:24576\n"
    "ds_read_b64_tr_b16 a[166:167], v29 offset:25088\n"
    "ds_read_b64_tr_b16 a[168:169], v28 offset:16384\n"
    "ds_read_b64_tr_b16 a[170:171], v28 offset:16896\n"
    "ds_read_b64_tr_b16 a[172:173], v28 offset:24576\n"
    "ds_read_b64_tr_b16 a[174:175], v28 offset:25088\n"
    "s_waitcnt vmcnt(28) lgkmcnt(0)\n"
    "s_barrier\n"
    "s_cbranch_vccz .Lsl26_%=\n"
    "s_sleep 10\n"
    ".Lsl26_%=:\n"
    "s_add_u32 m0, s61, 147456\n"
    "s_add_u32 s62, s60, 1572864\n"
    "buffer_load_dwordx4 v1, s[44:47], s62 offen lds\n"
    "s_add_u32 m0, s61, 151552\n"
    "s_add_u32 s62, s60, 1703936\n"
    "buffer_load_dwordx4 v1, s[44:47], s62 offen lds\n"
    "s_add_u32 m0, s61, 155648\n"
    "s_add_u32 s62, s60, 1835008\n"
    "buffer_load_dwordx4 v1, s[44:47], s62 offen lds\n"
    "s_add_u32 m0, s61, 159744\n"
    "s_add_u32 s62, s60, 1966080\n"
    "buffer_load_dwordx4 v1, s[44:47], s62 offen lds\n"
    "ds_read_b64_tr_b16 a[176:177], v3 offset:16384\n"
    "ds_read_b64_tr_b16 a[178:179], v3 offset:16896\n"
    "ds_read_b64_tr_b16 a[180:181], v3 offset:24576\n"
    "ds_read_b64_tr_b16 a[182:183], v3 offset:25088\n"
    "ds_read_b64_tr_b16 a[184:185], v4 offset:16384\n"
    "ds_read_b64_tr_b16 a[186:187], v4 offset:16896\n"
    "ds_read_b64_tr_b16 a[188:189], v4 offset:24576\n"
    "ds_read_b64_tr_b16 a[190:191], v4 offset:25088\n"
    "s_waitcnt vmcnt(28) lgkmcnt(0)\n"
    "s_barrier\n"
    "s_cbranch_vccz .Lsl27_%=\n"
    "s_sleep 10\n"
    ".Lsl27_%=:\n"
    "s_mov_b64 s[44:45], %11\n"
    "s_and_b32 s45, s45, 0xffff\n"
    "s_mov_b32 s46, 0x480000\n"
    "s_mov_b32 s47, 0x20000\n"
    "s_add_u32 m0, s61, 16384\n"
    "s_add_u32 s62, s63, 0\n"
    "buffer_load_dwordx4 v5, s[44:47], s62 offen lds\n"
    "s_add_u32 m0, s61, 20480\n"
    "s_add_u32 s62, s63, 9216\n"
    "buffer_load_dwordx4 v5, s[44:47], s62 offen lds\n"
    "s_add_u32 m0, s61, 24576\n"
    "s_add_u32 s62, s63, 18432\n"
    "buffer_load_dwordx4 v5, s[44:47], s62 offen lds\n"
    "s_add_u32 m0, s61, 28672\n"
    "s_add_u32 s62, s63, 27648\n"
    "buffer_load_dwordx4 v5, s[44:47], s62 offen lds\n"
    "ds_read_b64_tr_b16 a[192:193], v3 offset:32768\n"
    "ds_read_b64_tr_b16 a[194:195], v3 offset:33280\n"
    "ds_read_b64_tr_b16 a[196:197], v3 offset:40960\n"
    "ds_read_b64_tr_b16 a[198:199], v3 offset:41472\n"
    "ds_read_b64_tr_b16 a[200:201], v4 offset:32768\n"
    "ds_read_b64_tr_b16 a[202:203], v4 offset:33280\n"
    "ds_read_b64_tr_b16 a[204:205], v4 offset:40960\n"
    "ds_read_b64_tr_b16 a[206:207], v4 offset:41472\n"
    "s_waitcnt vmcnt(28) lgkmcnt(0)\n"
    "s_barrier\n"
    "s_cbranch_vccz .Lsl28_%=\n"
    "s_sleep 10\n"
    ".Lsl28_%=:\n"
    "s_add_u32 m0, s61, 32768\n"
    "s_add_u32 s62, s63, 36864\n"
    "buffer_load_dwordx4 v5, s[44:47], s62 offen lds\n"
    "s_add_u32 m0, s61, 36864\n"
    "s_add_u32 s62, s63, 46080\n"
    "buffer_load_dwordx4 v5, s[44:47], s62 offen lds\n"
    "s_add_u32 m0, s61, 40960\n"
    "s_add_u32 s62, s63, 55296\n"
    "buffer_load_dwordx4 v5, s[44:47], s62 offen lds\n"
    "s_add_u32 m0, s61, 45056\n"
    "s_add_u32 s62, s63, 64512\n"
    "buffer_load_dwordx4 v5, s[44:47], s62 offen lds\n"
    "ds_read_b64_tr_b16 a[208:209], v3 offset:49152\n"
    "ds_read_b64_tr_b16 a[210:211], v3 offset:49664\n"
    "ds_read_b64_tr_b16 a[212:213], v3 offset:57344\n"
    "ds_read_b64_tr_b16 a[214:215], v3 offset:57856\n"
    "ds_read_b64_tr_b16 a[216:217], v4 offset:49152\n"
    "ds_read_b64_tr_b16 a[218:219], v4 offset:49664\n"
    "ds_read_b64_tr_b16 a[220:221], v4 offset:57344\n"
    "ds_read_b64_tr_b16 a[222:223], v4 offset:57856\n"
    "s_waitcnt vmcnt(28) lgkmcnt(0)\n"
    "s_barrier\n"
    "s_cbranch_vccz .Lsl29_%=\n"
    "s_sleep 10\n"
    ".Lsl29_%=:\n"
    "s_add_u32 m0, s61, 49152\n"
    "s_add_u32 s62, s63, 73728\n"
    "buffer_load_dwordx4 v5, s[44:47], s62 offen lds\n"
    "s_add_u32 m0, s61, 53248\n"
    "s_add_u32 s62, s63, 82944\n"
    "buffer_load_dwordx4 v5, s[44:47], s62 offen lds\n"
    "s_add_u32 m0, s61, 57344\n"
    "s_add_u32 s62, s63, 92160\n"
    "buffer_load_dwordx4 v5, s[44:47], s62 offen lds\n"
    "s_add_u32 m0, s61, 61440\n"
    "s_add_u32 s62, s63, 101376\n"
    "buffer_load_dwordx4 v5, s[44:47], s62 offen lds\n"
    "ds_read_b64_tr_b16 a[224:225], v31\n"
    "ds_read_b64_tr_b16 a[226:227], v31 offset:512\n"
    "ds_read_b64_tr_b16 a[228:229], v31 offset:8192\n"
    "ds_read_b64_tr_b16 a[230:231], v31 offset:8704\n"
    "ds_read_b64_tr_b16 a[232:233], v30\n"
    "ds_read_b64_tr_b16 a[234:235], v30 offset:512\n"
    "ds_read_b64_tr_b16 a[236:237], v30 offset:8192\n"
    "ds_read_b64_tr_b16 a[238:239], v30 offset:8704\n"
    "s_waitcnt vmcnt(28) lgkmcnt(0)\n"
    "s_barrier\n"
    "s_cbranch_vccz .Lsl30_%=\n"
    "s_sleep 10\n"
    ".Lsl30_%=:\n"
    "s_add_u32 m0, s61, 65536\n"
    "s_add_u32 s62, s63, 110592\n"
    "buffer_load_dwordx4 v5, s[44:47], s62 offen lds\n"
    "s_add_u32 m0, s61, 69632\n"
    "s_add_u32 s62, s63, 119808\n"
    "buffer_load_dwordx4 v5, s[44:47], s62 offen lds\n"
    "s_add_u32 m0, s61, 73728\n"
    "s_add_u32 s62, s63, 129024\n"
    "buffer_load_dwordx4 v5, s[44:47], s62 offen lds\n"
    "s_add_u32 m0, s61, 77824\n"
    "s_add_u32 s62, s63, 138240\n"
    "buffer_load_dwordx4 v5, s[44:47], s62 offen lds\n"
    "ds_read_b64_tr_b16 a[240:241], v31 offset:16384\n"
    "ds_read_b64_tr_b16 a[242:243], v31 offset:16896\n"
    "ds_read_b64_tr_b16 a[244:245], v31 offset:24576\n"
    "ds_read_b64_tr_b16 a[246:247], v31 offset:25088\n"
    "ds_read_b64_tr_b16 a[248:249], v30 offset:16384\n"
    "ds_read_b64_tr_b16 a[250:251], v30 offset:16896\n"
    "ds_read_b64_tr_b16 a[252:253], v30 offset:24576\n"
    "ds_read_b64_tr_b16 a[254:255], v30 offset:25088\n"
    "s_waitcnt vmcnt(28) lgkmcnt(0)\n"
    "s_barrier\n"
    "ds_read_b64_tr_b16 v[32:33], v31 offset:32768\n"
    "ds_read_b64_tr_b16 v[34:35], v31 offset:33280\n"
    "ds_read_b64_tr_b16 v[36:37], v31 offset:40960\n"
    "ds_read_b64_tr_b16 v[38:39], v31 offset:41472\n"
    "ds_read_b64_tr_b16 v[40:41], v30 offset:32768\n"
    "ds_read_b64_tr_b16 v[42:43], v30 offset:33280\n"
    "ds_read_b64_tr_b16 v[44:45], v30 offset:40960\n"
    "ds_read_b64_tr_b16 v[46:47], v30 offset:41472\n"
    "s_waitcnt vmcnt(24) lgkmcnt(0)\n"
    "s_barrier\n"
    "ds_read_b64_tr_b16 v[48:49], v31 offset:49152\n"
    "ds_read_b64_tr_b16 v[50:51], v31 offset:49664\n"
    "ds_read_b64_tr_b16 v[52:53], v31 offset:57344\n"
    "ds_read_b64_tr_b16 v[54:55], v31 offset:57856\n"
    "ds_read_b64_tr_b16 v[56:57], v30 offset:49152\n"
    "ds_read_b64_tr_b16 v[58:59], v30 offset:49664\n"
    "ds_read_b64_tr_b16 v[60:61], v30 offset:57344\n"
    "ds_read_b64_tr_b16 v[62:63], v30 offset:57856\n"
    "s_waitcnt vmcnt(20) lgkmcnt(0)\n"
    "s_barrier\n"
    "ds_read_b64_tr_b16 v[64:65], v29\n"
    "ds_read_b64_tr_b16 v[66:67], v29 offset:512\n"
    "ds_read_b64_tr_b16 v[68:69], v29 offset:8192\n"
    "ds_read_b64_tr_b16 v[70:71], v29 offset:8704\n"
    "ds_read_b64_tr_b16 v[72:73], v28\n"
    "ds_read_b64_tr_b16 v[74:75], v28 offset:512\n"
    "ds_read_b64_tr_b16 v[76:77], v28 offset:8192\n"
    "ds_read_b64_tr_b16 v[78:79], v28 offset:8704\n"
    "s_waitcnt vmcnt(16) lgkmcnt(0)\n"
    "s_barrier\n"
    "ds_read_b64_tr_b16 v[80:81], v29 offset:16384\n"
    "ds_read_b64_tr_b16 v[82:83], v29 offset:16896\n"
    "ds_read_b64_tr_b16 v[84:85], v29 offset:24576\n"
    "ds_read_b64_tr_b16 v[86:87], v29 offset:25088\n"
    "ds_read_b64_tr_b16 v[136:137], v28 offset:16384\n"
    "ds_read_b64_tr_b16 v[138:139], v28 offset:16896\n"
    "ds_read_b64_tr_b16 v[140:141], v28 offset:24576\n"
    "ds_read_b64_tr_b16 v[142:143], v28 offset:25088\n"
    "s_waitcnt vmcnt(12) lgkmcnt(0)\n"
    "s_barrier\n"
    "ds_read_b64_tr_b16 v[88:89], v6 offset:16384\n"
    "ds_read_b64_tr_b16 v[90:91], v6 offset:16896\n"
    "ds_read_b64_tr_b16 v[120:121], v6 offset:16640\n"
    "ds_read_b64_tr_b16 v[122:123], v6 offset:17152\n"
    "ds_read_b64_tr_b16 v[96:97], v7 offset:16384\n"
    "ds_read_b64_tr_b16 v[98:99], v7 offset:16896\n"
    "ds_read_b64_tr_b16 v[128:129], v7 offset:16640\n"
    "ds_read_b64_tr_b16 v[130:131], v7 offset:17152\n"
    "s_waitcnt vmcnt(8) lgkmcnt(0)\n"
    "s_barrier\n"
    "ds_read_b64_tr_b16 v[92:93], v6 offset:32768\n"
    "ds_read_b64_tr_b16 v[94:95], v6 offset:33280\n"
    "ds_read_b64_tr_b16 v[124:125], v6 offset:33024\n"
    "ds_read_b64_tr_b16 v[126:127], v6 offset:33536\n"
    "ds_read_b64_tr_b16 v[100:101], v7 offset:32768\n"
    "ds_read_b64_tr_b16 v[102:103], v7 offset:33280\n"
    "ds_read_b64_tr_b16 v[132:133], v7 offset:33024\n"
    "ds_read_b64_tr_b16 v[134:135], v7 offset:33536\n"
    "s_waitcnt vmcnt(4) lgkmcnt(0)\n"
    "s_barrier\n"
    "ds_read_b64_tr_b16 v[104:105], v6 offset:49152\n"
    "ds_read_b64_tr_b16 v[106:107], v6 offset:49664\n"
    "ds_read_b64_tr_b16 v[12:13], v6 offset:49408\n"
    "ds_read_b64_tr_b16 v[14:15], v6 offset:49920\n"
    "ds_read_b64_tr_b16 v[112:113], v7 offset:49152\n"
    "ds_read_b64_tr_b16 v[114:115], v7 offset:49664\n"
    "ds_read_b64_tr_b16 v[20:21], v7 offset:49408\n"
    "ds_read_b64_tr_b16 v[22:23], v7 offset:49920\n"
    "s_waitcnt vmcnt(0) lgkmcnt(0)\n"
    "s_barrier\n"
    "v_add_u32 v11, 0x10000, v6\n"
    "ds_read_b64_tr_b16 v[108:109], v11\n"
    "ds_read_b64_tr_b16 v[110:111], v11 offset:512\n"
    "ds_read_b64_tr_b16 v[16:17], v11 offset:256\n"
    "ds_read_b64_tr_b16 v[18:19], v11 offset:768\n"
    "v_add_u32 v10, 0x10000, v7\n"
    "ds_read_b64_tr_b16 v[116:117], v10\n"
    "ds_read_b64_tr_b16 v[118:119], v10 offset:512\n"
    "ds_read_b64_tr_b16 v[24:25], v10 offset:256\n"
    "ds_read_b64_tr_b16 v[26:27], v10 offset:768\n"
    "s_waitcnt lgkmcnt(0)\n"
    "s_barrier\n"
    "ds_write_b128 v2, v[88:91] offset:14336\n"
    "ds_write_b128 v2, v[92:95] offset:15360\n"
    "ds_write_b128 v2, v[96:99] offset:16384\n"
    "ds_write_b128 v2, v[100:103] offset:17408\n"
    "ds_write_b128 v2, v[104:107] offset:18432\n"
    "ds_write_b128 v2, v[108:111] offset:19456\n"
    "ds_write_b128 v2, v[112:115] offset:20480\n"
    "ds_write_b128 v2, v[116:119] offset:21504\n"
    "ds_write_b128 v2, v[120:123] offset:22528\n"
    "ds_write_b128 v2, v[124:127] offset:23552\n"
    "ds_write_b128 v2, v[128:131] offset:24576\n"
    "ds_write_b128 v2, v[132:135] offset:25600\n"
    "ds_write_b128 v2, v[12:15] offset:26624\n"
    "ds_write_b128 v2, v[16:19] offset:27648\n"
    "ds_write_b128 v2, v[20:23] offset:28672\n"
    "ds_write_b128 v2, v[24:27] offset:29696\n"
    "ds_read_b64_tr_b16 v[88:89], v9\n"
    "ds_read_b64_tr_b16 v[90:91], v9 offset:64\n"
    "ds_read_b64_tr_b16 v[92:93], v9 offset:2048\n"
    "ds_read_b64_tr_b16 v[94:95], v9 offset:2112\n"
    "ds_read_b64_tr_b16 v[96:97], v9 offset:4096\n"
    "ds_read_b64_tr_b16 v[98:99], v9 offset:4160\n"
    "ds_read_b64_tr_b16 v[100:101], v9 offset:6144\n"
    "ds_read_b64_tr_b16 v[102:103], v9 offset:6208\n"
    "ds_write_b128 v2, v[32:35]\n"
    "ds_write_b128 v2, v[36:39] offset:1024\n"
    "ds_write_b128 v2, v[40:43] offset:2048\n"
    "ds_write_b128 v2, v[44:47] offset:3072\n"
    "ds_write_b128 v2, v[48:51] offset:4096\n"
    "ds_write_b128 v2, v[52:55] offset:5120\n"
    "ds_write_b128 v2, v[56:59] offset:6144\n"
    "ds_write_b128 v2, v[60:63] offset:7168\n"
    "ds_write_b128 v2, v[64:67] offset:8192\n"
    "ds_write_b128 v2, v[68:71] offset:9216\n"
    "ds_write_b128 v2, v[72:75] offset:10240\n"
    "ds_write_b128 v2, v[76:79] offset:11264\n"
    "ds_write_b128 v2, v[80:83] offset:12288\n"
    "ds_write_b128 v2, v[84:87] offset:13312\n"
    "v_lshrrev_b32 v112, 4, v0\n"
    "v_and_b32 v113, 15, v112\n"
    "v_lshrrev_b32 v114, 4, v112\n"
    "v_mov_b32 v11, 0\n"
    "v_and_b32 v115, 7, v112\n"
    "v_mul_u32_u24 v115, 1152, v115\n"
    "v_lshrrev_b32 v116, 3, v112\n"
    "v_lshl_add_u32 v117, v116, 4, v115\n"
    "v_and_b32 v115, 1, v114\n"
    "v_lshrrev_b32 v116, 1, v114\n"
    "v_mov_b32 v120, 0\n"
    "v_mov_b32 v121, 0\n"
    "v_mov_b32 v122, 0\n"
    "v_mov_b32 v123, 0\n"
    "ds_write_b128 v11, v[120:123] offset:31872\n"
    "v_mov_b32 v124, 0x3f803f80\n"
    "v_mov_b32 v125, 0x3f803f80\n"
    "v_mov_b32 v126, 0x3f803f80\n"
    "v_mov_b32 v127, 0x3f803f80\n"
    "ds_write_b128 v11, v[124:127] offset:31888\n"
    "v_mov_b32 v128, 0x3f003f00\n"
    "v_mov_b32 v129, 0x3f003f00\n"
    "v_mov_b32 v130, 0x3f003f00\n"
    "v_mov_b32 v131, 0x3f003f00\n"
    "ds_write_b128 v11, v[128:131] offset:31904\n"
    "s_waitcnt lgkmcnt(0)\n"
    "s_cmp_lt_u32 %2, 2\n"
    "s_cbranch_scc1 .Lsk_%=\n"
    "ds_write_b128 v2, v[88:91] offset:30720\n"
    "ds_write_b128 v2, v[92:95] offset:31744\n"
    "ds_write_b128 v2, v[96:99] offset:32768\n"
    "ds_write_b128 v2, v[100:103] offset:33792\n"
    ".Lsk_%=:\n"
    "s_cmp_eq_u32 %2, 3\n"
    "s_cbranch_scc1 .Lsm_%=\n"
    "s_mov_b64 s[44:45], %12\n"
    "s_and_b32 s45, s45, 0xffff\n"
    "s_mov_b32 s46, 0x2400\n"
    "s_mov_b32 s47, 0x20000\n"
    "s_mul_i32 s62, %2, 384\n"
    "s_mul_i32 s61, %2, 3072\n"
    "s_add_u32 m0, s61, 20480\n"
    "s_add_u32 s73, s62, 0\n"
    "buffer_load_dwordx4 v117, s[44:47], s73 offen lds\n"
    "s_add_u32 m0, s61, 21504\n"
    "s_add_u32 s73, s62, 128\n"
    "buffer_load_dwordx4 v117, s[44:47], s73 offen lds\n"
    "s_add_u32 m0, s61, 22528\n"
    "s_add_u32 s73, s62, 256\n"
    "buffer_load_dwordx4 v117, s[44:47], s73 offen lds\n"
    ".Lsm_%=:\n"
    "v_mov_b32 v130, 0x20400\n"
    "global_load_dwordx2 v[132:133], v130, %1 sc1\n"
    "v_mbcnt_lo_u32_b32 v131, -1, 0\n"
    "v_min_u32 v131, 7, v131\n"
    "v_lshlrev_b32 v131, 7, v131\n"
    "v_add_u32 v131, 0x20000, v131\n"
    "global_load_dwordx2 v[134:135], v131, %1 sc1\n"
    "s_waitcnt vmcnt(0) lgkmcnt(0)\n"
    "s_barrier\n"
    "v_mov_b32 v1, 0x4444\n"
    "v_mov_b32 v118, 0xeeee\n"
    "v_cmp_gt_u32 vcc, 8, v113\n"
    "s_nop 1\n"
    "v_cndmask_b32 v1, v118, v1, vcc\n"
    "s_lshl_b32 s73, %2, 10\n"
    "v_add_u32 v5, s73, v0\n"
    "v_and_b32 v119, 7, v113\n"
    "v_lshrrev_b32 v120, 3, v113\n"
    "v_lshl_add_u32 v3, v115, 1, v116\n"
    "v_mul_u32_u24 v3, 544, v3\n"
    "v_lshl_add_u32 v3, v113, 2, v3\n"
    "s_lshl_b32 s73, %2, 6\n"
    "s_add_u32 s73, s73, 20480\n"
    "v_add_u32 v3, s73, v3\n"
    "v_mul_u32_u24 v4, 272, v119\n"
    "v_lshl_add_u32 v4, v114, 4, v4\n"
    "v_lshlrev_b32 v6, 7, v114\n"
    "v_lshl_add_u32 v6, v119, 4, v6\n"
    "s_lshl_b32 s73, %2, 9\n"
    "v_add_u32 v6, s73, v6\n"
    "s_waitcnt vmcnt(0)\n"
    "v_add_u32 v132, 1, v132\n"
    "v_add_u32 v133, 1, v133\n"
    "s_mov_b32 s56, 1048576\n"
    ".Lcgo_a%=:\n"
    "v_cmp_ne_u64 vcc, v[132:133], v[134:135]\n"
    "s_cbranch_vccz .Lcgo_x%=\n"
    "global_load_dwordx2 v[134:135], v131, %1 sc1\n"
    "s_sub_u32 s56, s56, 1\n"
    "s_waitcnt vmcnt(0)\n"
    "s_cbranch_scc0 .Lcgo_a%=\n"
    ".Lcgo_x%=:\n"
    "s_mov_b32 s56, 0\n"
    "v_mov_b32 v134, 64\n"
    ".Lpoll1_%=:\n"
    "global_load_dword v135, v134, %1 sc1\n"
    "s_waitcnt vmcnt(0)\n"
    "v_readfirstlane_b32 s73, v135\n"
    "s_cmp_ge_u32 s73, 4\n"
    "s_cbranch_scc1 .Lpoll1_x%=\n"
    "s_add_u32 s56, s56, 1\n"
    "s_cmp_lt_u32 s56, 3\n"
    "s_cbranch_scc1 .Lpoll1_%=\n"
    "s_sleep 8\n"
    "s_cmp_lt_u32 s56, s57\n"
    "s_cbranch_scc1 .Lpoll1_%=\n"
    "s_mov_b32 s57, 0\n"
    "v_mov_b32 v135, 1\n"
    "global_store_dword v11, v135, %1 sc1\n"
    ".Lpoll1_x%=:\n"
    "s_xor_b32 s69, s73, %7\n"
    "s_lshr_b32 s51, s73, 1\n"
    "s_xor_b32 s51, s51, %7\n"
    "s_and_b32 s51, s51, 1\n"
    "s_lshl_b32 s51, s51, 13\n"
    "s_add_u32 s68, s51, 0x200000\n"
    "s_and_b32 s60, s69, 1\n"
    "s_lshl_b32 s60, s60, 13\n"
    "v_lshl_add_u32 v8, v120, 2, v115\n"
    "v_lshlrev_b32 v8, 12, v8\n"
    "v_lshl_add_u32 v8, v119, 4, v8\n"
    "v_lshl_add_u32 v7, v116, 15, v8\n"
    "v_lshl_add_u32 v8, v116, 3, v8\n"
    "s_mul_i32 s73, %5, 589824\n"
    "s_lshl_b32 s51, %4, 7\n"
    "s_add_u32 s73, s73, s51\n"
    "s_add_u32 s73, s73, s60\n"
    "s_lshl_b32 s51, %2, 16\n"
    "s_add_u32 s51, s51, s73\n"
    "v_add_u32 v7, s51, v7\n"
    "s_and_b32 s51, %2, 1\n"
    "s_lshl_b32 s51, s51, 15\n"
    "s_add_u32 s51, s51, 524288\n"
    "s_add_u32 s51, s51, s73\n"
    "v_add_u32 v8, s51, v8\n"
    "s_mov_b32 s74, 262144\n"
    "s_lshl_b32 s61, %4, 1\n"
    "s_add_u32 s61, s61, %5\n"
    "s_lshr_b32 s73, s61, 2\n"
    "s_add_u32 s73, s73, 128\n"
    "s_and_b32 s51, s61, 3\n"
    "s_cmp_eq_u32 s51, 0\n"
    "s_cselect_b32 s71, s73, -1\n"
    "s_lshl_b32 s73, s61, 1\n"
    "s_add_u32 s73, s73, %2\n"
    "s_cmp_lt_u32 %2, 2\n"
    "s_cselect_b32 s71, s73, s71\n"
    "s_max_i32 s61, s71, 0\n"
    "s_lshr_b32 s73, s61, 1\n"
    "s_lshl_b32 s73, s73, 14\n"
    "s_and_b32 s51, s61, 1\n"
    "s_lshl_b32 s51, s51, 12\n"
    "s_add_u32 s65, s73, s51\n"
    "s_add_u32 s65, s65, s60\n"
    "s_cmp_ge_u32 s61, 48\n"
    "s_cselect_b32 s94, 1, 0\n"
    "s_cmp_ge_u32 s61, 96\n"
    "s_cselect_b32 s94, 2, s94\n"
    "s_mul_i32 s73, s94, 48\n"
    "s_sub_u32 s73, s61, s73\n"
    "s_lshl_b32 s73, s73, 6\n"
    "v_lshlrev_b32 v9, 4, v114\n"
    "v_add_u32 v9, s73, v9\n"
    "v_bfrev_b32 v118, 1\n"
    "v_cmp_eq_u32 vcc, 0, v113\n"
    "s_nop 1\n"
    "v_cndmask_b32 v9, v118, v9, vcc\n"
    "s_mul_i32 s73, %2, 3072\n"
    "v_add_u32 v10, s73, v0\n"
    "s_cmp_eq_u32 %2, 3\n"
    "s_cbranch_scc0 .Lnw3_%=\n"
    "v_lshl_add_u32 v10, v113, 1, %5\n"
    "v_lshlrev_b32 v118, 3, v114\n"
    "v_sub_u32 v10, v10, v118\n"
    "v_cmp_le_i32 s[76:77], 0, v10\n"
    "v_cmp_le_i32 s[78:79], 2, v10\n"
    "v_cmp_le_i32 s[80:81], 4, v10\n"
    "v_cmp_le_i32 s[82:83], 6, v10\n"
    "v_cmp_le_i32 s[84:85], 1, v10\n"
    "v_cmp_le_i32 s[86:87], 3, v10\n"
    "v_cmp_le_i32 s[88:89], 5, v10\n"
    "v_cmp_le_i32 s[90:91], 7, v10\n"
    ".Lnw3_%=:\n"
    "s_lshr_b32 s73, %4, 2\n"
    "s_lshl1_add_u32 s73, s73, %7\n"
    "s_and_b32 s51, %4, 3\n"
    "s_lshl3_add_u32 s73, s73, s51\n"
    "s_lshl_b32 s73, s73, 8\n"
    "s_add_u32 s92, s73, 86016\n"
    "s_lshr_b32 s51, %4, 3\n"
    "s_lshl2_add_u32 s73, %5, s51\n"
    "s_lshl1_add_u32 s73, s73, %7\n"
    "s_and_b32 s51, %4, 7\n"
    "s_lshl4_add_u32 s73, s73, s51\n"
    "s_lshl_b32 s73, s73, 7\n"
    "s_add_u32 s93, s73, 53248\n"
    "s_lshl_b32 s73, %5, 12\n"
    "s_lshl_b32 s51, %4, 14\n"
    "s_add_u32 s73, s73, s51\n"
    "s_add_u32 s75, s73, 0x9c00000\n"
    "s_mov_b32 s48, 0\n"
    "s_mov_b32 s54, 68\n"
    "s_mov_b32 s55, 114\n"
    "s_mov_b32 s96, 1280\n"
    "s_mov_b32 s56, 0\n"
    "v_mov_b32 v134, 128\n"
    ".Lpoll2_%=:\n"
    "global_load_dword v135, v134, %1 sc1\n"
    "s_waitcnt vmcnt(0)\n"
    "v_readfirstlane_b32 s73, v135\n"
    "s_cmp_ge_u32 s73, 64\n"
    "s_cbranch_scc1 .Lpoll2_x%=\n"
    "s_add_u32 s56, s56, 1\n"
    "s_cmp_lt_u32 s56, 3\n"
    "s_cbranch_scc1 .Lpoll2_%=\n"
    "s_sleep 8\n"
    "s_cmp_lt_u32 s56, s57\n"
    "s_cbranch_scc1 .Lpoll2_%=\n"
    "s_mov_b32 s57, 0\n"
    "v_mov_b32 v135, 1\n"
    "global_store_dword v11, v135, %1 sc1\n"
    ".Lpoll2_x%=:\n"
    "ds_read_b128 v[32:35], v2\n"
    "ds_read_b128 v[36:39], v2 offset:1024\n"
    "ds_read_b128 v[40:43], v2 offset:2048\n"
    "ds_read_b128 v[44:47], v2 offset:3072\n"
    "ds_read_b128 v[48:51], v2 offset:4096\n"
    "ds_read_b128 v[52:55], v2 offset:5120\n"
    "ds_read_b128 v[56:59], v2 offset:6144\n"
    "ds_read_b128 v[60:63], v2 offset:7168\n"
    "ds_read_b128 v[64:67], v2 offset:8192\n"
    "ds_read_b128 v[68:71], v2 offset:9216\n"
    "ds_read_b128 v[72:75], v2 offset:10240\n"
    "ds_read_b128 v[76:79], v2 offset:11264\n"
    "ds_read_b128 v[80:83], v2 offset:12288\n"
    "ds_read_b128 v[84:87], v2 offset:13312\n"
    "v_mov_b64 v[124:125], 0\n"
    "v_mov_b64 v[126:127], 0\n"
    "v_mov_b64 v[128:129], 0\n"
    "v_mov_b64 v[130:131], 0\n"
    "s_add_u32 s73, s48, 0\n"
    "s_and_b32 s73, s73, 63\n"
    "s_lshr_b32 s51, s73, 2\n"
    "s_xor_b32 s51, s51, s69\n"
    "s_and_b32 s51, s51, 1\n"
    "s_lshl_b32 s51, s51, 13\n"
    "s_lshl_b32 s73, s73, 5\n"
    "s_add_u32 s73, s73, %4\n"
    "s_lshl_b32 s73, s73, 14\n"
    "s_add_u32 s73, s73, s51\n"
    "s_add_u32 s60, s73, 0x5c00000\n"
    "s_add_u32 s61, s73, 0x5c01000\n"
    "s_add_u32 s62, s73, 0x7c00000\n"
    "s_add_u32 s63, s73, 0x7c01000\n"
    "s_lshl_b32 s51, %5, 12\n"
    "s_add_u32 s73, s73, s51\n"
    "s_add_u32 s64, s73, 0x3c00000\n"
    "s_waitcnt lgkmcnt(0)\n"
    "s_mov_b32 s56, 0\n"
    "v_mov_b32 v134, s92\n"
    ".Lpoll3_%=:\n"
    "global_load_dword v132, v134, %1 sc1\n"
    "global_load_dword v133, v134, %1 offset:128 sc1\n"
    "s_waitcnt vmcnt(0)\n"
    "v_and_b32 v133, v132, v133\n"
    "s_nop 0\n"
    "v_readfirstlane_b32 s73, v133\n"
    "s_cmp_ge_u32 s73, 1\n"
    "s_cbranch_scc1 .Lpoll3_x%=\n"
    "s_add_u32 s56, s56, 1\n"
    "s_cmp_lt_u32 s56, 3\n"
    "s_cbranch_scc1 .Lpoll3_%=\n"
    "s_sleep 8\n"
    "s_cmp_lt_u32 s56, s57\n"
    "s_cbranch_scc1 .Lpoll3_%=\n"
    "s_mov_b32 s57, 0\n"
    "v_mov_b32 v135, 1\n"
    "global_store_dword v11, v135, %1 sc1\n"
    ".Lpoll3_x%=:\n"
    "buffer_load_dwordx4 v[12:15], v5, s[40:43], s60 offen sc1\n"
    "buffer_load_dwordx4 v[16:19], v5, s[40:43], s61 offen sc1\n"
    "buffer_load_dwordx4 v[20:23], v5, s[40:43], s62 offen sc1\n"
    "buffer_load_dwordx4 v[24:27], v5, s[40:43], s63 offen sc1\n"
    "buffer_load_dwordx4 v[28:31], v5, s[40:43], s64 offen sc1\n"
    "s_add_u32 s73, s48, 1\n"
    "s_and_b32 s51, s73, 32\n"
    "s_and_b32 s73, s73, 31\n"
    "s_lshl3_add_u32 s73, s51, s73\n"
    "s_lshl2_add_u32 s73, s73, s92\n"
    "v_mov_b32 v134, s73\n"
    "global_load_dword v132, v134, %1 sc1\n"
    "global_load_dword v133, v134, %1 offset:128 sc1\n"
    "s_add_u32 s73, s48, 1\n"
    "s_and_b32 s73, s73, 63\n"
    "s_lshr_b32 s51, s73, 2\n"
    "s_xor_b32 s51, s51, s69\n"
    "s_and_b32 s51, s51, 1\n"
    "s_lshl_b32 s51, s51, 13\n"
    "s_lshl_b32 s73, s73, 5\n"
    "s_add_u32 s73, s73, %4\n"
    "s_lshl_b32 s73, s73, 14\n"
    "s_add_u32 s73, s73, s51\n"
    "s_add_u32 s60, s73, 0x5c00000\n"
    "s_add_u32 s61, s73, 0x5c01000\n"
    "s_add_u32 s62, s73, 0x7c00000\n"
    "s_add_u32 s63, s73, 0x7c01000\n"
    "s_lshl_b32 s51, %5, 12\n"
    "s_add_u32 s73, s73, s51\n"
    "s_add_u32 s64, s73, 0x3c00000\n"
    "s_waitcnt vmcnt(6)\n"
    "ds_write_b128 v5, v[12:15]\n"
    "s_waitcnt vmcnt(5)\n"
    "ds_write_b128 v5, v[16:19] offset:4096\n"
    "s_waitcnt vmcnt(2)\n"
    "ds_write_b128 v5, v[28:31] offset:16384\n"
    "v_swap_b32 v22, v24\n"
    "v_swap_b32 v23, v25\n"
    "ds_write_b128 v5, v[20:23] offset:8192\n"
    "ds_write_b128 v5, v[24:27] offset:12288\n"
    "s_waitcnt vmcnt(0)\n"
    "v_and_b32 v133, v132, v133\n"
    "s_nop 0\n"
    "v_readfirstlane_b32 s59, v133\n"
    "s_memrealtime s[50:51]\n"
    "s_waitcnt lgkmcnt(0)\n"
    "v_mov_b32 v134, s50\n"
    "ds_write_b32 v11, v134 offset:31920\n"
    "s_mov_b32 s58, s50\n"
    "s_waitcnt lgkmcnt(0)\n"
    "s_barrier\n"
    ".Lstep_%=:\n"
    "ds_read_b128 v[88:91], v0 offset:20480\n"
    "ds_read_b128 v[92:95], v0 offset:21504\n"
    "ds_read_b128 v[96:99], v0 offset:22528\n"
    "ds_read_b128 v[100:103], v0 offset:23552\n"
    "ds_read_b128 v[104:107], v0 offset:24576\n"
    "ds_read_b128 v[108:111], v0 offset:25600\n"
    "ds_read_b128 v[112:115], v0 offset:26624\n"
    "ds_read_b128 v[116:119], v0 offset:27648\n"
    "ds_read_b128 v[120:123], v0 offset:28672\n"
    "buffer_load_dwordx4 v[12:15], v5, s[40:43], s60 offen sc1\n"
    "buffer_load_dwordx4 v[16:19], v5, s[40:43], s61 offen sc1\n"
    "buffer_load_dwordx4 v[20:23], v5, s[40:43], s62 offen sc1\n"
    "buffer_load_dwordx4 v[24:27], v5, s[40:43], s63 offen sc1\n"
    "buffer_load_dwordx4 v[28:31], v5, s[40:43], s64 offen sc1\n"
    "s_waitcnt lgkmcnt(8)\n"
    "v_smfmac_f32_16x16x64_bf16 v[124:127], v[88:91], v[32:39], v1\n"
    "v_smfmac_f32_16x16x64_bf16 v[128:131], v[88:91], v[40:47], v1\n"
    "s_waitcnt lgkmcnt(7)\n"
    "v_smfmac_f32_16x16x64_bf16 v[124:127], v[92:95], v[48:55], v1\n"
    "v_mov_b64 v[32:33], 0\n"
    "v_mov_b64 v[34:35], 0\n"
    "v_smfmac_f32_16x16x64_bf16 v[128:131], v[92:95], v[56:63], v1\n"
    "v_mov_b64 v[36:37], 0\n"
    "v_mov_b64 v[38:39], 0\n"
    "s_waitcnt lgkmcnt(6)\n"
    "v_smfmac_f32_16x16x64_bf16 v[124:127], v[96:99], v[64:71], v1\n"
    "v_smfmac_f32_16x16x64_bf16 v[128:131], v[96:99], v[72:79], v1\n"
    "s_waitcnt lgkmcnt(5)\n"
    "v_smfmac_f32_16x16x64_bf16 v[124:127], v[100:103], v[80:87], v1\n"
    "v_smfmac_f32_16x16x64_bf16 v[128:131], v[100:103], v[136:143], v1\n"
    "s_waitcnt lgkmcnt(4)\n"
    "v_smfmac_f32_16x16x64_bf16 v[124:127], v[104:107], v[144:151], v1\n"
    "ds_read_b128 v[40:43], v0\n"
    "v_smfmac_f32_16x16x64_bf16 v[128:131], v[104:107], v[152:159], v1\n"
    "ds_read_b128 v[44:47], v0 offset:4096\n"
    "s_waitcnt lgkmcnt(5)\n"
    "v_smfmac_f32_16x16x64_bf16 v[124:127], v[108:111], v[160:167], v1\n"
    "ds_read_b128 v[48:51], v0 offset:1024\n"
    "v_smfmac_f32_16x16x64_bf16 v[128:131], v[108:111], v[168:175], v1\n"
    "ds_read_b128 v[52:55], v0 offset:5120\n"
    "s_waitcnt lgkmcnt(6)\n"
    "v_smfmac_f32_16x16x64_bf16 v[124:127], v[112:115], v[176:183], v1\n"
    "ds_read_b128 v[56:59], v0 offset:2048\n"
    "v_smfmac_f32_16x16x64_bf16 v[128:131], v[112:115], v[184:191], v1\n"
    "ds_read_b128 v[60:63], v0 offset:6144\n"
    "s_waitcnt lgkmcnt(7)\n"
    "v_smfmac_f32_16x16x64_bf16 v[124:127], v[116:119], v[192:199], v1\n"
    "ds_read_b128 v[64:67], v0 offset:3072\n"
    "v_smfmac_f32_16x16x64_bf16 v[128:131], v[116:119], v[200:207], v1\n"
    "ds_read_b128 v[68:71], v0 offset:7168\n"
    "s_waitcnt lgkmcnt(8)\n"
    "v_smfmac_f32_16x16x64_bf16 v[124:127], v[120:123], v[208:215], v1\n"
    "v_smfmac_f32_16x16x64_bf16 v[128:131], v[120:123], v[216:223], v1\n"
    "v_smfmac_f32_16x16x64_bf16 v[32:35], v[88:91], v[224:231], v1\n"
    "v_smfmac_f32_16x16x64_bf16 v[36:39], v[88:91], v[232:239], v1\n"
    "v_smfmac_f32_16x16x64_bf16 v[32:35], v[92:95], v[240:247], v1\n"
    "v_permlane32_swap_b32 v124, v126\n"
    "v_permlane32_swap_b32 v128, v130\n"
    "v_smfmac_f32_16x16x64_bf16 v[36:39], v[92:95], v[248:255], v1\n"
    "v_permlane32_swap_b32 v125, v127\n"
    "v_permlane32_swap_b32 v129, v131\n"
    "v_smfmac_f32_16x16x64_bf16 v[32:35], v[96:99], a[0:7], v1\n"
    "v_add_f32 v124, v124, v126\n"
    "v_add_f32 v128, v128, v130\n"
    "v_smfmac_f32_16x16x64_bf16 v[36:39], v[96:99], a[8:15], v1\n"
    "v_add_f32 v125, v125, v127\n"
    "v_add_f32 v129, v129, v131\n"
    "v_smfmac_f32_16x16x64_bf16 v[32:35], v[100:103], a[16:23], v1\n"
    "v_cvt_pk_bf16_f32 v76, v124, v128\n"
    "v_cvt_pk_bf16_f32 v77, v125, v129\n"
    "ds_write_b32 v3, v76 offset:9216\n"
    "ds_write_b32 v3, v77 offset:9488\n"
    "v_smfmac_f32_16x16x64_bf16 v[36:39], v[100:103], a[24:31], v1\n"
    "v_smfmac_f32_16x16x64_bf16 v[32:35], v[104:107], a[32:39], v1\n"
    "v_smfmac_f32_16x16x64_bf16 v[36:39], v[104:107], a[40:47], v1\n"
    "v_smfmac_f32_16x16x64_bf16 v[32:35], v[108:111], a[48:55], v1\n"
    "s_waitcnt lgkmcnt(0)\n"
    "s_barrier\n"
    "ds_read_b128 v[124:127], v4 offset:29696\n"
    "v_smfmac_f32_16x16x64_bf16 v[36:39], v[108:111], a[56:63], v1\n"
    "ds_read_b128 v[128:131], v4 offset:29760\n"
    "v_smfmac_f32_16x16x64_bf16 v[32:35], v[112:115], a[64:71], v1\n"
    "ds_read_b128 v[132:135], v4 offset:29824\n"
    "v_smfmac_f32_16x16x64_bf16 v[36:39], v[112:115], a[72:79], v1\n"
    "ds_read_b128 v[72:75], v4 offset:29888\n"
    "v_smfmac_f32_16x16x64_bf16 v[32:35], v[116:119], a[80:87], v1\n"
    "v_smfmac_f32_16x16x64_bf16 v[36:39], v[116:119], a[88:95], v1\n"
    "v_smfmac_f32_16x16x64_bf16 v[32:35], v[120:123], a[96:103], v1\n"
    "v_smfmac_f32_16x16x64_bf16 v[36:39], v[120:123], a[104:111], v1\n"
    "s_waitcnt lgkmcnt(3)\n"
    "v_mfma_f32_16x16x32_bf16 v[80:83], v[40:43], v[124:127], 0\n"
    "v_mfma_f32_16x16x32_bf16 v[84:87], v[44:47], v[124:127], 0\n"
    "ds_read_b128 v[40:43], v11 offset:31872\n"
    "ds_read_b128 v[44:47], v11 offset:31872\n"
    "s_waitcnt lgkmcnt(4)\n"
    "v_mfma_f32_16x16x32_bf16 v[80:83], v[48:51], v[128:131], v[80:83]\n"
    "v_permlane32_swap_b32 v32, v34\n"
    "v_permlane32_swap_b32 v36, v38\n"
    "v_mfma_f32_16x16x32_bf16 v[84:87], v[52:55], v[128:131], v[84:87]\n"
    "v_permlane32_swap_b32 v33, v35\n"
    "v_permlane32_swap_b32 v37, v39\n"
    "s_waitcnt lgkmcnt(3)\n"
    "v_mfma_f32_16x16x32_bf16 v[80:83], v[56:59], v[132:135], v[80:83]\n"
    "v_add_f32 v32, v32, v34\n"
    "v_add_f32 v36, v36, v38\n"
    "v_mfma_f32_16x16x32_bf16 v[84:87], v[60:63], v[132:135], v[84:87]\n"
    "v_add_f32 v33, v33, v35\n"
    "v_add_f32 v37, v37, v39\n"
    "s_waitcnt lgkmcnt(2)\n"
    "v_mfma_f32_16x16x32_bf16 v[80:83], v[64:67], v[72:75], v[80:83]\n"
    "v_cvt_pk_bf16_f32 v76, v32, v36\n"
    "v_cvt_pk_bf16_f32 v77, v33, v37\n"
    "ds_write2_b32 v3, v76, v77 offset1:68\n"
    "v_mfma_f32_16x16x32_bf16 v[84:87], v[68:71], v[72:75], v[84:87]\n"
    "s_waitcnt lgkmcnt(2)\n"
    "v_smfmac_f32_16x16x64_bf16 v[40:43], v[88:91], a[112:119], v1\n"
    "s_waitcnt lgkmcnt(1)\n"
    "v_smfmac_f32_16x16x64_bf16 v[44:47], v[88:91], a[120:127], v1\n"
    "s_waitcnt lgkmcnt(0)\n"
    "s_barrier\n"
    "ds_read_b128 v[48:51], v4 offset:20480\n"
    "ds_read_b128 v[52:55], v4 offset:20544\n"
    "ds_read_b128 v[56:59], v4 offset:20608\n"
    "ds_read_b128 v[60:63], v4 offset:20672\n"
    "v_smfmac_f32_16x16x64_bf16 v[40:43], v[92:95], a[128:135], v1\n"
    "v_maximum3_f32 v78, v80, v81, v82\n"
    "v_maximum3_f32 v78, v78, v83, v84\n"
    "v_smfmac_f32_16x16x64_bf16 v[44:47], v[92:95], a[136:143], v1\n"
    "v_maximum3_f32 v78, v78, v85, v86\n"
    "v_maximum3_f32 v78, v78, v87, v87\n"
    "ds_read_b128 v[32:35], v5 offset:8192\n"
    "ds_read_b128 v[36:39], v5 offset:12288\n"
    "v_smfmac_f32_16x16x64_bf16 v[40:43], v[96:99], a[144:151], v1\n"
    "v_mov_b32 v79, v78\n"
    "ds_read_b128 v[68:71], v11 offset:31888\n"
    "ds_read_b64 v[76:77], v11 offset:31904\n"
    "v_smfmac_f32_16x16x64_bf16 v[44:47], v[96:99], a[152:159], v1\n"
    "v_permlane32_swap_b32 v79, v78\n"
    "v_maximum3_f32 v78, v78, v79, v79\n"
    "s_waitcnt lgkmcnt(7)\n"
    "v_mfma_f32_16x16x32_bf16 v[64:67], v[48:51], v[124:127], 0\n"
    "ds_read_b128 v[88:91], v2 offset:14336\n"
    "ds_read_b128 v[92:95], v2 offset:15360\n"
    "s_waitcnt lgkmcnt(8)\n"
    "v_mfma_f32_16x16x32_bf16 v[64:67], v[52:55], v[128:131], v[64:67]\n"
    "s_waitcnt lgkmcnt(7)\n"
    "v_mfma_f32_16x16x32_bf16 v[64:67], v[56:59], v[132:135], v[64:67]\n"
    "s_waitcnt lgkmcnt(6)\n"
    "v_mfma_f32_16x16x32_bf16 v[64:67], v[60:63], v[72:75], v[64:67]\n"
    "v_smfmac_f32_16x16x64_bf16 v[40:43], v[100:103], a[160:167], v1\n"
    "ds_read_b128 v[48:51], v2 offset:18432\n"
    "ds_read_b128 v[52:55], v2 offset:19456\n"
    "v_smfmac_f32_16x16x64_bf16 v[44:47], v[100:103], a[168:175], v1\n"
    "ds_read_b128 v[56:59], v2 offset:20480\n"
    "ds_read_b128 v[60:63], v2 offset:21504\n"
    "v_maximum3_f32 v79, v64, v65, v66\n"
    "v_maximum3_f32 v78, v79, v67, v78\n"
    "v_mov_b32 v79, v78\n"
    "v_smfmac_f32_16x16x64_bf16 v[40:43], v[104:107], a[176:183], v1\n"
    "ds_read_b128 v[96:99], v2 offset:16384\n"
    "ds_read_b128 v[100:103], v2 offset:17408\n"
    "v_permlane16_swap_b32 v79, v78\n"
    "v_maximum3_f32 v78, v78, v79, v79\n"
    "v_mul_f32 v78, 0xbe0293ee, v78\n"
    "v_smfmac_f32_16x16x64_bf16 v[44:47], v[104:107], a[184:191], v1\n"
    "v_fmamk_f32 v80, v80, 0x3e0293ee, v78\n"
    "v_fmamk_f32 v81, v81, 0x3e0293ee, v78\n"
    "v_exp_f32 v80, v80\n"
    "v_exp_f32 v81, v81\n"
    "ds_read_b128 v[124:127], v2 offset:22528\n"
    "ds_read_b128 v[128:131], v2 offset:23552\n"
    "v_smfmac_f32_16x16x64_bf16 v[40:43], v[108:111], a[192:199], v1\n"
    "v_fmamk_f32 v82, v82, 0x3e0293ee, v78\n"
    "v_fmamk_f32 v83, v83, 0x3e0293ee, v78\n"
    "v_exp_f32 v82, v82\n"
    "v_exp_f32 v83, v83\n"
    "v_cvt_pk_bf16_f32 v80, v80, v81\n"
    "v_smfmac_f32_16x16x64_bf16 v[44:47], v[108:111], a[200:207], v1\n"
    "v_fmamk_f32 v84, v84, 0x3e0293ee, v78\n"
    "v_fmamk_f32 v85, v85, 0x3e0293ee, v78\n"
    "v_exp_f32 v84, v84\n"
    "v_exp_f32 v85, v85\n"
    "v_cvt_pk_bf16_f32 v81, v82, v83\n"
    "v_smfmac_f32_16x16x64_bf16 v[40:43], v[112:115], a[208:215], v1\n"
    "v_fmamk_f32 v86, v86, 0x3e0293ee, v78\n"
    "v_fmamk_f32 v87, v87, 0x3e0293ee, v78\n"
    "v_exp_f32 v86, v86\n"
    "v_exp_f32 v87, v87\n"
    "v_cvt_pk_bf16_f32 v82, v84, v85\n"
    "ds_read_b128 v[104:107], v2 offset:24576\n"
    "ds_read_b128 v[108:111], v2 offset:25600\n"
    "v_smfmac_f32_16x16x64_bf16 v[44:47], v[112:115], a[216:223], v1\n"
    "v_fmamk_f32 v64, v64, 0x3e0293ee, v78\n"
    "v_fmamk_f32 v65, v65, 0x3e0293ee, v78\n"
    "v_exp_f32 v64, v64\n"
    "v_exp_f32 v65, v65\n"
    "v_cvt_pk_bf16_f32 v83, v86, v87\n"
    "v_smfmac_f32_16x16x64_bf16 v[40:43], v[116:119], a[224:231], v1\n"
    "v_fmamk_f32 v66, v66, 0x3e0293ee, v78\n"
    "v_fmamk_f32 v67, v67, 0x3e0293ee, v78\n"
    "v_exp_f32 v66, v66\n"
    "v_exp_f32 v67, v67\n"
    "v_cvt_pk_bf16_f32 v64, v64, v65\n"
    "v_smfmac_f32_16x16x64_bf16 v[44:47], v[116:119], a[232:239], v1\n"
    "v_cvt_pk_bf16_f32 v65, v66, v67\n"
    "v_smfmac_f32_16x16x64_bf16 v[40:43], v[120:123], a[240:247], v1\n"
    "ds_read_b128 v[112:115], v2 offset:26624\n"
    "ds_read_b128 v[116:119], v2 offset:27648\n"
    "v_smfmac_f32_16x16x64_bf16 v[44:47], v[120:123], a[248:255], v1\n"
    "s_waitcnt lgkmcnt(14)\n"
    "v_mfma_f32_16x16x32_bf16 v[72:75], v[68:71], v[80:83], 0\n"
    "v_mfma_f32_16x16x32_bf16 v[84:87], v[32:35], v[80:83], 0\n"
    "ds_read_b128 v[120:123], v11 offset:31872\n"
    "ds_read_b128 v[68:71], v11 offset:31872\n"
    "v_mfma_f32_16x16x32_bf16 v[132:135], v[36:39], v[80:83], 0\n"
    "ds_read_b128 v[32:35], v2 offset:28672\n"
    "ds_read_b128 v[36:39], v2 offset:29696\n"
    "v_cvt_pk_bf16_f32 v40, v40, v41\n"
    "v_cvt_pk_bf16_f32 v41, v42, v43\n"
    "s_waitcnt lgkmcnt(14)\n"
    "v_mfma_f32_16x16x16_bf16 v[72:75], v[76:77], v[64:65], v[72:75]\n"
    "v_cvt_pk_bf16_f32 v42, v44, v45\n"
    "v_cvt_pk_bf16_f32 v43, v46, v47\n"
    "v_mfma_f32_16x16x16_bf16 v[84:87], v[40:41], v[64:65], v[84:87]\n"
    "ds_read_b128 v[80:83], v11 offset:31872\n"
    "ds_read_b128 v[44:47], v11 offset:31872\n"
    "v_mfma_f32_16x16x16_bf16 v[132:135], v[42:43], v[64:65], v[132:135]\n"
    "ds_write_b128 v5, v[40:43] offset:22784\n"
    "ds_read_b128 v[64:67], v11 offset:31872\n"
    "s_and_b32 s49, s48, 1\n"
    "s_lshl_b32 s70, s49, 3\n"
    "v_rcp_f32 v78, v72\n"
    "s_nop 0\n"
    "v_pk_mul_f32 v[84:85], v[84:85], v[78:79] op_sel_hi:[1,0]\n"
    "v_pk_mul_f32 v[86:87], v[86:87], v[78:79] op_sel_hi:[1,0]\n"
    "s_nop 0\n"
    "v_pk_mul_f32 v[132:133], v[132:133], v[78:79] op_sel_hi:[1,0]\n"
    "v_pk_mul_f32 v[134:135], v[134:135], v[78:79] op_sel_hi:[1,0]\n"
    "v_cvt_pk_bf16_f32 v84, v84, v132\n"
    "v_cvt_pk_bf16_f32 v85, v85, v133\n"
    "v_cvt_pk_bf16_f32 v86, v86, v134\n"
    "v_cvt_pk_bf16_f32 v87, v87, v135\n"
    "ds_write_b128 v6, v[84:87] offset:26880\n"
    "s_cmp_eq_u32 s48, 63\n"
    "s_waitcnt lgkmcnt(0)\n"
    "s_barrier\n"
    "s_cbranch_scc1 .Lend_%=\n"
    "ds_read_b128 v[132:135], v0 offset:26880\n"
    "ds_read_b128 v[72:75], v0 offset:27904\n"
    "s_cmp_lt_u32 %2, 2\n"
    "s_waitcnt lgkmcnt(1)\n"
    "v_smfmac_f32_16x16x64_bf16 v[68:71], v[132:135], v[88:95], v1\n"
    "v_smfmac_f32_16x16x64_bf16 v[80:83], v[132:135], v[96:103], v1\n"
    "ds_read_b128 v[88:91], v2 offset:30720\n"
    "ds_read_b128 v[92:95], v2 offset:31744\n"
    "s_waitcnt lgkmcnt(2)\n"
    "v_smfmac_f32_16x16x64_bf16 v[68:71], v[72:75], v[48:55], v1\n"
    "ds_read_b128 v[96:99], v2 offset:32768\n"
    "ds_read_b128 v[100:103], v2 offset:33792\n"
    "v_smfmac_f32_16x16x64_bf16 v[80:83], v[72:75], v[56:63], v1\n"
    "v_smfmac_f32_16x16x64_bf16 v[120:123], v[132:135], v[124:131], v1\n"
    "v_smfmac_f32_16x16x64_bf16 v[44:47], v[132:135], v[104:111], v1\n"
    "v_smfmac_f32_16x16x64_bf16 v[120:123], v[72:75], v[112:119], v1\n"
    "v_permlane32_swap_b32 v68, v80\n"
    "v_permlane32_swap_b32 v69, v81\n"
    "v_smfmac_f32_16x16x64_bf16 v[44:47], v[72:75], v[32:39], v1\n"
    "s_waitcnt lgkmcnt(0)\n"
    "s_cbranch_scc1 .Lx1_%=\n"
    "v_smfmac_f32_16x16x64_bf16 v[64:67], v[132:135], v[88:95], v1\n"
    "v_smfmac_f32_16x16x64_bf16 v[64:67], v[72:75], v[96:103], v1\n"
    ".Lx1_%=:\n"
    "v_permlane32_swap_b32 v70, v82\n"
    "v_permlane32_swap_b32 v71, v83\n"
    "v_pk_add_f32 v[68:69], v[68:69], v[80:81]\n"
    "v_pk_add_f32 v[70:71], v[70:71], v[82:83]\n"
    "v_bfi_b32 v68, 15, s49, v68\n"
    "v_bfi_b32 v70, 15, s49, v70\n"
    "buffer_store_dwordx4 v[68:71], v7, s[40:43], 0 offen sc1\n"
    "v_permlane32_swap_b32 v120, v44\n"
    "v_permlane32_swap_b32 v121, v45\n"
    "v_permlane32_swap_b32 v122, v46\n"
    "v_permlane32_swap_b32 v123, v47\n"
    "v_pk_add_f32 v[120:121], v[120:121], v[44:45]\n"
    "v_pk_add_f32 v[122:123], v[122:123], v[46:47]\n"
    "v_bfi_b32 v120, 15, s49, v120\n"
    "v_bfi_b32 v122, 15, s49, v122\n"
    "buffer_store_dwordx4 v[120:123], v7, s[40:43], s74 offen sc1\n"
    "s_cbranch_scc1 .Lx2_%=\n"
    "v_permlane32_swap_b32 v64, v66\n"
    "v_permlane32_swap_b32 v65, v67\n"
    "v_pk_add_f32 v[64:65], v[64:65], v[66:67]\n"
    "v_bfi_b32 v64, 15, s49, v64\n"
    "buffer_store_dwordx2 v[64:65], v8, s[40:43], 0 offen sc1\n"
    ".Lx2_%=:\n"
    "ds_read_b32 v135, v11 offset:31920\n"
    "s_memrealtime s[38:39]\n"
    "s_waitcnt lgkmcnt(0)\n"
    "v_readfirstlane_b32 s95, v135\n"
    "s_add_u32 s53, s54, 16\n"
    "s_sub_u32 s73, s38, s58\n"
    "s_lshl_b32 s73, s73, 3\n"
    "s_sub_u32 s73, s73, s96\n"
    "s_ashr_i32 s73, s73, 2\n"
    "s_max_i32 s73, s73, -32\n"
    "s_min_i32 s73, s73, 16\n"
    "s_add_u32 s96, s96, s73\n"
    "s_add_u32 s73, s38, s53\n"
    ".Lsh4_%=:\n"
    "s_sub_u32 s51, s95, s73\n"
    "s_cmp_lt_u32 s51, 1024\n"
    "s_cbranch_scc1 .Lsh4_x%=\n"
    "s_add_u32 s95, s95, 48\n"
    "s_cmp_lt_u32 s51, -1024\n"
    "s_cselect_b32 s95, s73, s95\n"
    "s_branch .Lsh4_%=\n"
    ".Lsh4_x%=:\n"
    "s_cmp_eq_u32 %2, 3\n"
    "s_cbranch_scc1 .Lw3_%=\n"
    "ds_read_b128 v[32:35], v2\n"
    "ds_read_b128 v[36:39], v2 offset:1024\n"
    "ds_read_b128 v[40:43], v2 offset:2048\n"
    "ds_read_b128 v[44:47], v2 offset:3072\n"
    "ds_read_b128 v[48:51], v2 offset:4096\n"
    "ds_read_b128 v[52:55], v2 offset:5120\n"
    "ds_read_b128 v[56:59], v2 offset:6144\n"
    "ds_read_b128 v[60:63], v2 offset:7168\n"
    "ds_read_b128 v[64:67], v2 offset:8192\n"
    "ds_read_b128 v[68:71], v2 offset:9216\n"
    "ds_read_b128 v[72:75], v2 offset:10240\n"
    "ds_read_b128 v[76:79], v2 offset:11264\n"
    "ds_read_b128 v[80:83], v2 offset:12288\n"
    "ds_read_b128 v[84:87], v2 offset:13312\n"
    "v_mov_b64 v[124:125], 0\n"
    "v_mov_b64 v[126:127], 0\n"
    "v_mov_b64 v[128:129], 0\n"
    "v_mov_b64 v[130:131], 0\n"
    "s_mul_i32 s73, s48, 3\n"
    "s_add_u32 s73, s73, s94\n"
    "s_and_b32 s51, s73, 1\n"
    "s_lshl_b32 s51, s51, 12\n"
    "s_lshr_b32 s73, s73, 1\n"
    "s_lshl_b32 s73, s73, 14\n"
    "s_add_u32 s73, s73, s51\n"
    "s_add_u32 s66, s73, s68\n"
    "s_mul_i32 s73, s48, 3\n"
    "s_add_u32 s73, s73, %2\n"
    "s_and_b32 s51, s73, 1\n"
    "s_lshl_b32 s51, s51, 12\n"
    "s_lshr_b32 s73, s73, 1\n"
    "s_lshl_b32 s73, s73, 14\n"
    "s_add_u32 s73, s73, s51\n"
    "s_add_u32 s67, s73, s68\n"
    "s_waitcnt lgkmcnt(0)\n"
    "s_cmp_lt_i32 s71, 0\n"
    "s_cbranch_scc1 .Lnored_%=\n"
    "s_mov_b32 s56, 0\n"
    ".Lred_%=:\n"
    "s_sub_u32 s53, s95, s54\n"
    "s_waitcnt lgkmcnt(0)\n"
    ".Lgl5_%=:\n"
    "s_memrealtime s[50:51]\n"
    "s_waitcnt lgkmcnt(0)\n"
    "s_sub_u32 s50, s50, s53\n"
    "s_cmp_lt_i32 s50, 0\n"
    "s_cbranch_scc1 .Lgl5_%=\n"
    "buffer_load_dwordx4 v[88:91], v0, s[40:43], s65 offen sc1\n"
    "buffer_load_dwordx4 v[92:95], v0, s[40:43], s65 offen offset:1024 sc1\n"
    "buffer_load_dwordx4 v[96:99], v0, s[40:43], s65 offen offset:2048 sc1\n"
    "buffer_load_dwordx4 v[100:103], v0, s[40:43], s65 offen offset:3072 sc1\n"
    "s_lshr_b32 s73, s96, 3\n"
    "s_add_u32 s73, s73, s95\n"
    "s_add_u32 s73, s73, s55\n"
    "s_and_b32 s73, s73, -2\n"
    "v_mov_b32 v105, s73\n"
    "s_waitcnt vmcnt(2)\n"
    "v_add3_u32 v104, v88, v90, v92\n"
    "v_pk_add_f32 v[108:109], v[88:89], v[92:93]\n"
    "v_pk_add_f32 v[110:111], v[90:91], v[94:95]\n"
    "s_waitcnt vmcnt(1)\n"
    "v_add3_u32 v104, v104, v94, v96\n"
    "v_pk_add_f32 v[108:109], v[108:109], v[96:97]\n"
    "v_pk_add_f32 v[110:111], v[110:111], v[98:99]\n"
    "s_waitcnt vmcnt(0)\n"
    "v_add3_u32 v104, v104, v98, v100\n"
    "v_pk_add_f32 v[108:109], v[108:109], v[100:101]\n"
    "v_pk_add_f32 v[110:111], v[110:111], v[102:103]\n"
    "v_add_u32 v104, v104, v102\n"
    "v_and_b32 v104, 15, v104\n"
    "v_permlane32_swap_b32 v108, v110\n"
    "v_permlane32_swap_b32 v109, v111\n"
    "v_cmp_ne_u32 vcc, s70, v104\n"
    "v_pk_add_f32 v[108:109], v[108:109], v[110:111]\n"
    "s_cbranch_vccz .Lredok_%=\n"
    "s_memrealtime s[38:39]\n"
    "s_waitcnt lgkmcnt(0)\n"
    "s_add_u32 s73, s38, 44\n"
    ".Lsh6_%=:\n"
    "s_sub_u32 s51, s95, s73\n"
    "s_cmp_lt_u32 s51, 1024\n"
    "s_cbranch_scc1 .Lsh6_x%=\n"
    "s_add_u32 s95, s95, 48\n"
    "s_cmp_lt_u32 s51, -1024\n"
    "s_cselect_b32 s95, s73, s95\n"
    "s_branch .Lsh6_%=\n"
    ".Lsh6_x%=:\n"
    "s_add_u32 s56, s56, 1\n"
    "s_cmp_lt_u32 s56, s57\n"
    "s_cbranch_scc1 .Lred_%=\n"
    "s_mov_b32 s57, 0\n"
    "v_mov_b32 v135, 1\n"
    "global_store_dword v11, v135, %1 sc1\n"
    ".Lredok_%=:\n"
    "s_nop 0\n"
    "v_permlane16_swap_b32 v108, v109\n"
    "v_add_f32 v108, v108, v109\n"
    "s_nop 1\n"
    "v_add_f32_dpp v108, v108, v108 row_ror:8 row_mask:0xf bank_mask:0xf\n"
    "s_nop 1\n"
    "v_mov_b32_dpp v109, v108 quad_perm:[1,0,3,2] row_mask:0xf bank_mask:0xf\n"
    "v_cvt_pk_bf16_f32 v108, v108, v109\n"
    "v_min_u32 v108, -2, v108\n"
    "s_nop 1\n"
    "v_mov_b32_dpp v109, v108 row_shl:2 row_mask:0xf bank_mask:0xf\n"
    "v_mov_b32_dpp v110, v108 row_shl:4 row_mask:0xf bank_mask:0xf\n"
    "v_mov_b32_dpp v111, v108 row_shl:6 row_mask:0xf bank_mask:0xf\n"
    "buffer_store_dwordx4 v[108:111], v9, s[40:43], s66 offen sc1\n"
    "s_cmp_eq_u32 s71, 0\n"
    "s_cbranch_scc0 .Lnl_%=\n"
    "s_mov_b64 exec, 1\n"
    "buffer_store_dword v105, v11, s[40:43], s66 offen offset:3072 sc1\n"
    "s_mov_b64 exec, -1\n"
    ".Lnl_%=:\n"
    ".Lnored_%=:\n"
    "s_waitcnt vmcnt(2)\n"
    "s_cmp_eq_u32 %2, 0\n"
    "s_cselect_b32 s73, 0, 0x80000000\n"
    "v_mov_b32 v106, s73\n"
    "s_lshl_b32 s73, %2, 2\n"
    "v_mov_b32 v105, s73\n"
    "s_cmp_lg_u32 s59, 0\n"
    "s_cbranch_scc1 .Lkn7_%=\n"
    "s_waitcnt lgkmcnt(0)\n"
    "s_add_u32 s73, s48, 1\n"
    "s_and_b32 s51, s73, 32\n"
    "s_and_b32 s73, s73, 31\n"
    "s_lshl3_add_u32 s73, s51, s73\n"
    "s_lshl2_add_u32 s73, s73, s92\n"
    "s_mov_b32 s53, s73\n"
    "s_mov_b32 s56, 0\n"
    "v_mov_b32 v134, s53\n"
    ".Lpoll8_%=:\n"
    "global_load_dword v132, v134, %1 sc1\n"
    "global_load_dword v133, v134, %1 offset:128 sc1\n"
    "s_waitcnt vmcnt(0)\n"
    "v_and_b32 v133, v132, v133\n"
    "s_nop 0\n"
    "v_readfirstlane_b32 s73, v133\n"
    "s_cmp_ge_u32 s73, 1\n"
    "s_cbranch_scc1 .Lpoll8_x%=\n"
    "s_add_u32 s56, s56, 1\n"
    "s_cmp_lt_u32 s56, 3\n"
    "s_cbranch_scc1 .Lpoll8_%=\n"
    "s_sleep 8\n"
    "s_cmp_lt_u32 s56, s57\n"
    "s_cbranch_scc1 .Lpoll8_%=\n"
    "s_mov_b32 s57, 0\n"
    "v_mov_b32 v135, 1\n"
    "global_store_dword v11, v135, %1 sc1\n"
    ".Lpoll8_x%=:\n"
    "buffer_load_dwordx4 v[12:15], v5, s[40:43], s60 offen sc1\n"
    "buffer_load_dwordx4 v[16:19], v5, s[40:43], s61 offen sc1\n"
    "buffer_load_dwordx4 v[20:23], v5, s[40:43], s62 offen sc1\n"
    "buffer_load_dwordx4 v[24:27], v5, s[40:43], s63 offen sc1\n"
    "buffer_load_dwordx4 v[28:31], v5, s[40:43], s64 offen sc1\n"
    "s_waitcnt vmcnt(0)\n"
    ".Lkn7_%=:\n"
    "s_add_u32 s73, s48, 2\n"
    "s_and_b32 s51, s73, 32\n"
    "s_and_b32 s73, s73, 31\n"
    "s_lshl3_add_u32 s73, s51, s73\n"
    "s_lshl2_add_u32 s73, s73, s92\n"
    "v_mov_b32 v134, s73\n"
    "global_load_dword v132, v134, %1 sc1\n"
    "global_load_dword v133, v134, %1 offset:128 sc1\n"
    "s_add_u32 s73, s48, 2\n"
    "s_and_b32 s73, s73, 63\n"
    "s_lshr_b32 s51, s73, 2\n"
    "s_xor_b32 s51, s51, s69\n"
    "s_and_b32 s51, s51, 1\n"
    "s_lshl_b32 s51, s51, 13\n"
    "s_lshl_b32 s73, s73, 5\n"
    "s_add_u32 s73, s73, %4\n"
    "s_lshl_b32 s73, s73, 14\n"
    "s_add_u32 s73, s73, s51\n"
    "s_add_u32 s60, s73, 0x5c00000\n"
    "s_add_u32 s61, s73, 0x5c01000\n"
    "s_add_u32 s62, s73, 0x7c00000\n"
    "s_add_u32 s63, s73, 0x7c01000\n"
    "s_lshl_b32 s51, %5, 12\n"
    "s_add_u32 s73, s73, s51\n"
    "s_add_u32 s64, s73, 0x3c00000\n"
    "s_mov_b32 s56, 0\n"
    "s_sub_u32 s53, s95, 0\n"
    "s_waitcnt lgkmcnt(0)\n"
    ".Lgl9_%=:\n"
    "s_memrealtime s[50:51]\n"
    "s_waitcnt lgkmcnt(0)\n"
    "s_sub_u32 s50, s50, s53\n"
    "s_cmp_lt_i32 s50, 0\n"
    "s_cbranch_scc1 .Lgl9_%=\n"
    "buffer_load_dwordx4 v[88:91], v0, s[40:43], s67 offen sc1\n"
    "buffer_load_dwordx4 v[92:95], v0, s[40:43], s67 offen offset:1024 sc1\n"
    "buffer_load_dwordx4 v[96:99], v0, s[40:43], s67 offen offset:2048 sc1\n"
    "buffer_load_dword v101, v106, s[40:43], s67 offen offset:3072 sc1\n"
    "s_waitcnt lgkmcnt(0)\n"
    "s_barrier\n"
    "ds_write_b128 v5, v[12:15]\n"
    "ds_write_b128 v5, v[16:19] offset:4096\n"
    "ds_write_b128 v5, v[28:31] offset:16384\n"
    "v_swap_b32 v22, v24\n"
    "v_swap_b32 v23, v25\n"
    "ds_write_b128 v5, v[20:23] offset:8192\n"
    "ds_write_b128 v5, v[24:27] offset:12288\n"
    "s_waitcnt vmcnt(4)\n"
    "v_and_b32 v133, v132, v133\n"
    "s_nop 0\n"
    "v_readfirstlane_b32 s59, v133\n"
    ".Lgchk_%=:\n"
    "s_waitcnt vmcnt(2)\n"
    "v_max3_u32 v100, v88, v90, v92\n"
    "ds_write_b128 v10, v[88:91] offset:20480\n"
    "ds_write_b128 v10, v[92:95] offset:21504\n"
    "s_waitcnt vmcnt(1)\n"
    "v_max3_u32 v100, v100, v94, v96\n"
    "ds_write_b128 v10, v[96:99] offset:22528\n"
    "s_waitcnt vmcnt(0)\n"
    "v_max3_u32 v100, v100, v98, v101\n"
    "ds_write_b32 v105, v101 offset:31920\n"
    "v_cmp_eq_u32 vcc, -1, v100\n"
    "s_cbranch_vccz .Lgok_%=\n"
    "s_add_u32 s95, s95, 48\n"
    "s_memrealtime s[38:39]\n"
    "s_waitcnt lgkmcnt(0)\n"
    "s_add_u32 s73, s38, -28\n"
    ".Lsh10_%=:\n"
    "s_sub_u32 s51, s95, s73\n"
    "s_cmp_lt_u32 s51, 1024\n"
    "s_cbranch_scc1 .Lsh10_x%=\n"
    "s_add_u32 s95, s95, 48\n"
    "s_cmp_lt_u32 s51, -1024\n"
    "s_cselect_b32 s95, s73, s95\n"
    "s_branch .Lsh10_%=\n"
    ".Lsh10_x%=:\n"
    "s_sub_u32 s53, s95, 0\n"
    "s_waitcnt lgkmcnt(0)\n"
    ".Lgl11_%=:\n"
    "s_memrealtime s[50:51]\n"
    "s_waitcnt lgkmcnt(0)\n"
    "s_sub_u32 s50, s50, s53\n"
    "s_cmp_lt_i32 s50, 0\n"
    "s_cbranch_scc1 .Lgl11_%=\n"
    "buffer_load_dwordx4 v[88:91], v0, s[40:43], s67 offen sc1\n"
    "buffer_load_dwordx4 v[92:95], v0, s[40:43], s67 offen offset:1024 sc1\n"
    "buffer_load_dwordx4 v[96:99], v0, s[40:43], s67 offen offset:2048 sc1\n"
    "buffer_load_dword v101, v106, s[40:43], s67 offen offset:3072 sc1\n"
    "s_add_u32 s56, s56, 1\n"
    "s_cmp_lt_u32 s56, s57\n"
    "s_cbranch_scc1 .Lgchk_%=\n"
    "s_mov_b32 s57, 0\n"
    "v_mov_b32 v135, 1\n"
    "global_store_dword v11, v135, %1 sc1\n"
    "s_waitcnt vmcnt(0)\n"
    ".Lgok_%=:\n"
    "s_mov_b32 s58, s95\n"
    "s_add_u32 s48, s48, 1\n"
    "s_waitcnt lgkmcnt(0)\n"
    "s_barrier\n"
    "s_branch .Lstep_%=\n"
    ".Lw3_%=:\n"
    "ds_read_b128 v[32:35], v0 offset:16384\n"
    "ds_read_b128 v[48:51], v0\n"
    "ds_read_b128 v[64:67], v0 offset:4096\n"
    "ds_read_b128 v[80:83], v4 offset:20480\n"
    "ds_read_b128 v[36:39], v0 offset:17408\n"
    "ds_read_b128 v[52:55], v0 offset:1024\n"
    "ds_read_b128 v[68:71], v0 offset:5120\n"
    "ds_read_b128 v[84:87], v4 offset:20544\n"
    "ds_read_b128 v[40:43], v0 offset:18432\n"
    "ds_read_b128 v[56:59], v0 offset:2048\n"
    "ds_read_b128 v[72:75], v0 offset:6144\n"
    "ds_read_b128 v[88:91], v4 offset:20608\n"
    "ds_read_b128 v[44:47], v0 offset:19456\n"
    "ds_read_b128 v[60:63], v0 offset:3072\n"
    "ds_read_b128 v[76:79], v0 offset:7168\n"
    "ds_read_b128 v[92:95], v4 offset:20672\n"
    "s_waitcnt lgkmcnt(14)\n"
    "v_mfma_f32_16x16x32_bf16 v[96:99], v[48:51], v[32:35], 0\n"
    "s_waitcnt lgkmcnt(13)\n"
    "v_mfma_f32_16x16x32_bf16 v[100:103], v[64:67], v[32:35], 0\n"
    "s_waitcnt lgkmcnt(12)\n"
    "v_mfma_f32_16x16x32_bf16 v[104:107], v[80:83], v[32:35], 0\n"
    "s_waitcnt lgkmcnt(10)\n"
    "v_mfma_f32_16x16x32_bf16 v[96:99], v[52:55], v[36:39], v[96:99]\n"
    "s_waitcnt lgkmcnt(9)\n"
    "v_mfma_f32_16x16x32_bf16 v[100:103], v[68:71], v[36:39], v[100:103]\n"
    "s_waitcnt lgkmcnt(8)\n"
    "v_mfma_f32_16x16x32_bf16 v[104:107], v[84:87], v[36:39], v[104:107]\n"
    "s_waitcnt lgkmcnt(6)\n"
    "v_mfma_f32_16x16x32_bf16 v[96:99], v[56:59], v[40:43], v[96:99]\n"
    "s_waitcnt lgkmcnt(5)\n"
    "v_mfma_f32_16x16x32_bf16 v[100:103], v[72:75], v[40:43], v[100:103]\n"
    "s_waitcnt lgkmcnt(4)\n"
    "v_mfma_f32_16x16x32_bf16 v[104:107], v[88:91], v[40:43], v[104:107]\n"
    "s_waitcnt lgkmcnt(2)\n"
    "v_mfma_f32_16x16x32_bf16 v[96:99], v[60:63], v[44:47], v[96:99]\n"
    "s_waitcnt lgkmcnt(1)\n"
    "v_mfma_f32_16x16x32_bf16 v[100:103], v[76:79], v[44:47], v[100:103]\n"
    "s_waitcnt lgkmcnt(0)\n"
    "v_mfma_f32_16x16x32_bf16 v[104:107], v[92:95], v[44:47], v[104:107]\n"
    "ds_read_b128 v[32:35], v0 offset:8192\n"
    "ds_read_b128 v[36:39], v0 offset:12288\n"
    "ds_read_b128 v[40:43], v0 offset:9216\n"
    "ds_read_b128 v[44:47], v0 offset:13312\n"
    "ds_read_b128 v[48:51], v0 offset:10240\n"
    "ds_read_b128 v[52:55], v0 offset:14336\n"
    "ds_read_b128 v[56:59], v0 offset:11264\n"
    "ds_read_b128 v[60:63], v0 offset:15360\n"
    "ds_read_b128 v[64:67], v0 offset:22784\n"
    "ds_read_b128 v[68:71], v0 offset:23808\n"
    "ds_read_b128 v[72:75], v0 offset:24832\n"
    "ds_read_b128 v[76:79], v0 offset:25856\n"
    "ds_read_b128 v[80:83], v11 offset:31888\n"
    "ds_read_b64 v[84:85], v11 offset:31904\n"
    "v_mov_b32 v86, 0xd01502f9\n"
    "v_cndmask_b32 v96, v86, v96, s[76:77]\n"
    "v_cndmask_b32 v97, v86, v97, s[78:79]\n"
    "v_cndmask_b32 v98, v86, v98, s[80:81]\n"
    "v_cndmask_b32 v99, v86, v99, s[82:83]\n"
    "v_cndmask_b32 v100, v86, v100, s[84:85]\n"
    "v_cndmask_b32 v101, v86, v101, s[86:87]\n"
    "v_cndmask_b32 v102, v86, v102, s[88:89]\n"
    "v_cndmask_b32 v103, v86, v103, s[90:91]\n"
    "v_maximum3_f32 v87, v104, v105, v106\n"
    "v_maximum3_f32 v87, v87, v107, v96\n"
    "v_maximum3_f32 v87, v87, v97, v98\n"
    "v_maximum3_f32 v87, v87, v99, v100\n"
    "v_maximum3_f32 v87, v87, v101, v102\n"
    "v_maximum3_f32 v87, v87, v103, v103\n"
    "v_mov_b32 v86, v87\n"
    "s_nop 1\n"
    "v_permlane16_swap_b32 v86, v87\n"
    "v_maximum3_f32 v87, v87, v86, v86\n"
    "v_mov_b32 v86, v87\n"
    "s_nop 1\n"
    "v_permlane32_swap_b32 v86, v87\n"
    "v_maximum3_f32 v87, v87, v86, v86\n"
    "v_mul_f32 v87, 0xbe0293ee, v87\n"
    "v_fmamk_f32 v96, v96, 0x3e0293ee, v87\n"
    "v_fmamk_f32 v97, v97, 0x3e0293ee, v87\n"
    "v_fmamk_f32 v98, v98, 0x3e0293ee, v87\n"
    "v_fmamk_f32 v99, v99, 0x3e0293ee, v87\n"
    "v_fmamk_f32 v100, v100, 0x3e0293ee, v87\n"
    "v_fmamk_f32 v101, v101, 0x3e0293ee, v87\n"
    "v_fmamk_f32 v102, v102, 0x3e0293ee, v87\n"
    "v_fmamk_f32 v103, v103, 0x3e0293ee, v87\n"
    "v_fmamk_f32 v104, v104, 0x3e0293ee, v87\n"
    "v_fmamk_f32 v105, v105, 0x3e0293ee, v87\n"
    "v_fmamk_f32 v106, v106, 0x3e0293ee, v87\n"
    "v_fmamk_f32 v107, v107, 0x3e0293ee, v87\n"
    "v_exp_f32 v96, v96\n"
    "v_exp_f32 v97, v97\n"
    "v_exp_f32 v98, v98\n"
    "v_exp_f32 v99, v99\n"
    "v_exp_f32 v100, v100\n"
    "v_exp_f32 v101, v101\n"
    "v_exp_f32 v102, v102\n"
    "v_exp_f32 v103, v103\n"
    "v_exp_f32 v104, v104\n"
    "v_exp_f32 v105, v105\n"
    "v_exp_f32 v106, v106\n"
    "v_exp_f32 v107, v107\n"
    "v_cvt_pk_bf16_f32 v96, v96, v97\n"
    "v_cvt_pk_bf16_f32 v97, v98, v99\n"
    "v_cvt_pk_bf16_f32 v98, v100, v101\n"
    "v_cvt_pk_bf16_f32 v99, v102, v103\n"
    "v_cvt_pk_bf16_f32 v104, v104, v105\n"
    "v_cvt_pk_bf16_f32 v105, v106, v107\n"
    "s_waitcnt lgkmcnt(1)\n"
    "v_mfma_f32_16x16x32_bf16 v[108:111], v[80:83], v[96:99], 0\n"
    "v_mfma_f32_16x16x32_bf16 v[88:91], v[32:35], v[96:99], 0\n"
    "v_mfma_f32_16x16x32_bf16 v[120:123], v[36:39], v[96:99], 0\n"
    "v_mfma_f32_16x16x32_bf16 v[92:95], v[40:43], v[96:99], 0\n"
    "s_waitcnt lgkmcnt(0)\n"
    "v_mfma_f32_16x16x16_bf16 v[108:111], v[84:85], v[104:105], v[108:111]\n"
    "v_mfma_f32_16x16x32_bf16 v[124:127], v[44:47], v[96:99], 0\n"
    "v_mfma_f32_16x16x32_bf16 v[112:115], v[48:51], v[96:99], 0\n"
    "v_mfma_f32_16x16x32_bf16 v[128:131], v[52:55], v[96:99], 0\n"
    "v_mfma_f32_16x16x32_bf16 v[116:119], v[56:59], v[96:99], 0\n"
    "v_mfma_f32_16x16x32_bf16 v[132:135], v[60:63], v[96:99], 0\n"
    "v_rcp_f32 v86, v108\n"
    "v_mfma_f32_16x16x16_bf16 v[88:91], v[64:65], v[104:105], v[88:91]\n"
    "v_mfma_f32_16x16x16_bf16 v[120:123], v[66:67], v[104:105], v[120:123]\n"
    "v_mfma_f32_16x16x16_bf16 v[92:95], v[68:69], v[104:105], v[92:95]\n"
    "v_mfma_f32_16x16x16_bf16 v[124:127], v[70:71], v[104:105], v[124:127]\n"
    "v_mfma_f32_16x16x16_bf16 v[112:115], v[72:73], v[104:105], v[112:115]\n"
    "v_mfma_f32_16x16x16_bf16 v[128:131], v[74:75], v[104:105], v[128:131]\n"
    "v_mfma_f32_16x16x16_bf16 v[116:119], v[76:77], v[104:105], v[116:119]\n"
    "v_mfma_f32_16x16x16_bf16 v[132:135], v[78:79], v[104:105], v[132:135]\n"
    "v_pk_mul_f32 v[88:89], v[88:89], v[86:87] op_sel_hi:[1,0]\n"
    "v_pk_mul_f32 v[90:91], v[90:91], v[86:87] op_sel_hi:[1,0]\n"
    "v_pk_mul_f32 v[120:121], v[120:121], v[86:87] op_sel_hi:[1,0]\n"
    "v_pk_mul_f32 v[122:123], v[122:123], v[86:87] op_sel_hi:[1,0]\n"
    "v_cvt_pk_bf16_f32 v88, v88, v120\n"
    "v_cvt_pk_bf16_f32 v89, v89, v121\n"
    "v_cvt_pk_bf16_f32 v90, v90, v122\n"
    "v_cvt_pk_bf16_f32 v91, v91, v123\n"
    "v_pk_mul_f32 v[92:93], v[92:93], v[86:87] op_sel_hi:[1,0]\n"
    "v_pk_mul_f32 v[94:95], v[94:95], v[86:87] op_sel_hi:[1,0]\n"
    "v_pk_mul_f32 v[124:125], v[124:125], v[86:87] op_sel_hi:[1,0]\n"
    "v_pk_mul_f32 v[126:127], v[126:127], v[86:87] op_sel_hi:[1,0]\n"
    "v_cvt_pk_bf16_f32 v92, v92, v124\n"
    "v_cvt_pk_bf16_f32 v93, v93, v125\n"
    "v_cvt_pk_bf16_f32 v94, v94, v126\n"
    "v_cvt_pk_bf16_f32 v95, v95, v127\n"
    "v_pk_mul_f32 v[112:113], v[112:113], v[86:87] op_sel_hi:[1,0]\n"
    "v_pk_mul_f32 v[114:115], v[114:115], v[86:87] op_sel_hi:[1,0]\n"
    "v_pk_mul_f32 v[128:129], v[128:129], v[86:87] op_sel_hi:[1,0]\n"
    "v_pk_mul_f32 v[130:131], v[130:131], v[86:87] op_sel_hi:[1,0]\n"
    "v_cvt_pk_bf16_f32 v112, v112, v128\n"
    "v_cvt_pk_bf16_f32 v113, v113, v129\n"
    "v_cvt_pk_bf16_f32 v114, v114, v130\n"
    "v_cvt_pk_bf16_f32 v115, v115, v131\n"
    "v_pk_mul_f32 v[116:117], v[116:117], v[86:87] op_sel_hi:[1,0]\n"
    "v_pk_mul_f32 v[118:119], v[118:119], v[86:87] op_sel_hi:[1,0]\n"
    "v_pk_mul_f32 v[132:133], v[132:133], v[86:87] op_sel_hi:[1,0]\n"
    "v_pk_mul_f32 v[134:135], v[134:135], v[86:87] op_sel_hi:[1,0]\n"
    "v_cvt_pk_bf16_f32 v116, v116, v132\n"
    "v_cvt_pk_bf16_f32 v117, v117, v133\n"
    "v_cvt_pk_bf16_f32 v118, v118, v134\n"
    "v_cvt_pk_bf16_f32 v119, v119, v135\n"
    "ds_read_b128 v[32:35], v2\n"
    "ds_read_b128 v[36:39], v2 offset:1024\n"
    "ds_read_b128 v[40:43], v2 offset:2048\n"
    "ds_read_b128 v[44:47], v2 offset:3072\n"
    "ds_read_b128 v[48:51], v2 offset:4096\n"
    "ds_read_b128 v[52:55], v2 offset:5120\n"
    "ds_read_b128 v[56:59], v2 offset:6144\n"
    "ds_read_b128 v[60:63], v2 offset:7168\n"
    "ds_read_b128 v[64:67], v2 offset:8192\n"
    "ds_read_b128 v[68:71], v2 offset:9216\n"
    "ds_read_b128 v[72:75], v2 offset:10240\n"
    "ds_read_b128 v[76:79], v2 offset:11264\n"
    "ds_read_b128 v[80:83], v2 offset:12288\n"
    "ds_read_b128 v[84:87], v2 offset:13312\n"
    "v_mov_b64 v[124:125], 0\n"
    "v_mov_b64 v[126:127], 0\n"
    "v_mov_b64 v[128:129], 0\n"
    "v_mov_b64 v[130:131], 0\n"
    "s_waitcnt vmcnt(0)\n"
    "s_cmp_lg_u32 s59, 0\n"
    "s_cbranch_scc1 .Lkn12_%=\n"
    "s_waitcnt lgkmcnt(0)\n"
    "s_add_u32 s73, s48, 1\n"
    "s_and_b32 s51, s73, 32\n"
    "s_and_b32 s73, s73, 31\n"
    "s_lshl3_add_u32 s73, s51, s73\n"
    "s_lshl2_add_u32 s73, s73, s92\n"
    "s_mov_b32 s53, s73\n"
    "s_mov_b32 s56, 0\n"
    "v_mov_b32 v134, s53\n"
    ".Lpoll13_%=:\n"
    "global_load_dword v132, v134, %1 sc1\n"
    "global_load_dword v133, v134, %1 offset:128 sc1\n"
    "s_waitcnt vmcnt(0)\n"
    "v_and_b32 v133, v132, v133\n"
    "s_nop 0\n"
    "v_readfirstlane_b32 s73, v133\n"
    "s_cmp_ge_u32 s73, 1\n"
    "s_cbranch_scc1 .Lpoll13_x%=\n"
    "s_add_u32 s56, s56, 1\n"
    "s_cmp_lt_u32 s56, 3\n"
    "s_cbranch_scc1 .Lpoll13_%=\n"
    "s_sleep 8\n"
    "s_cmp_lt_u32 s56, s57\n"
    "s_cbranch_scc1 .Lpoll13_%=\n"
    "s_mov_b32 s57, 0\n"
    "v_mov_b32 v135, 1\n"
    "global_store_dword v11, v135, %1 sc1\n"
    ".Lpoll13_x%=:\n"
    "buffer_load_dwordx4 v[12:15], v5, s[40:43], s60 offen sc1\n"
    "buffer_load_dwordx4 v[16:19], v5, s[40:43], s61 offen sc1\n"
    "buffer_load_dwordx4 v[20:23], v5, s[40:43], s62 offen sc1\n"
    "buffer_load_dwordx4 v[24:27], v5, s[40:43], s63 offen sc1\n"
    "buffer_load_dwordx4 v[28:31], v5, s[40:43], s64 offen sc1\n"
    "s_waitcnt vmcnt(0)\n"
    ".Lkn12_%=:\n"
    "s_add_u32 s73, s48, 2\n"
    "s_and_b32 s51, s73, 32\n"
    "s_and_b32 s73, s73, 31\n"
    "s_lshl3_add_u32 s73, s51, s73\n"
    "s_lshl2_add_u32 s73, s73, s92\n"
    "v_mov_b32 v134, s73\n"
    "global_load_dword v132, v134, %1 sc1\n"
    "global_load_dword v133, v134, %1 offset:128 sc1\n"
    "s_add_u32 s73, s48, 2\n"
    "s_and_b32 s73, s73, 63\n"
    "s_lshr_b32 s51, s73, 2\n"
    "s_xor_b32 s51, s51, s69\n"
    "s_and_b32 s51, s51, 1\n"
    "s_lshl_b32 s51, s51, 13\n"
    "s_lshl_b32 s73, s73, 5\n"
    "s_add_u32 s73, s73, %4\n"
    "s_lshl_b32 s73, s73, 14\n"
    "s_add_u32 s73, s73, s51\n"
    "s_add_u32 s60, s73, 0x5c00000\n"
    "s_add_u32 s61, s73, 0x5c01000\n"
    "s_add_u32 s62, s73, 0x7c00000\n"
    "s_add_u32 s63, s73, 0x7c01000\n"
    "s_lshl_b32 s51, %5, 12\n"
    "s_add_u32 s73, s73, s51\n"
    "s_add_u32 s64, s73, 0x3c00000\n"
    "s_sub_u32 s53, s95, 16\n"
    "s_waitcnt lgkmcnt(0)\n"
    ".Lgl14_%=:\n"
    "s_memrealtime s[50:51]\n"
    "s_waitcnt lgkmcnt(0)\n"
    "s_sub_u32 s50, s50, s53\n"
    "s_cmp_lt_i32 s50, 0\n"
    "s_cbranch_scc1 .Lgl14_%=\n"
    "s_lshr_b32 s51, s48, 2\n"
    "s_xor_b32 s51, s51, s69\n"
    "s_and_b32 s51, s51, 1\n"
    "s_lshl_b32 s51, s51, 13\n"
    "s_lshl_b32 s73, s48, 19\n"
    "s_add_u32 s73, s73, s75\n"
    "s_add_u32 s73, s73, s51\n"
    "buffer_store_dwordx4 v[88:91], v0, s[40:43], s73 offen sc1\n"
    "buffer_store_dwordx4 v[92:95], v0, s[40:43], s73 offen offset:1024 sc1\n"
    "buffer_store_dwordx4 v[112:115], v0, s[40:43], s73 offen offset:2048 sc1\n"
    "buffer_store_dwordx4 v[116:119], v0, s[40:43], s73 offen offset:3072 sc1\n"
    "s_waitcnt lgkmcnt(0)\n"
    "s_barrier\n"
    "ds_write_b128 v5, v[12:15]\n"
    "ds_write_b128 v5, v[16:19] offset:4096\n"
    "ds_write_b128 v5, v[28:31] offset:16384\n"
    "v_swap_b32 v22, v24\n"
    "v_swap_b32 v23, v25\n"
    "ds_write_b128 v5, v[20:23] offset:8192\n"
    "ds_write_b128 v5, v[24:27] offset:12288\n"
    "s_waitcnt vmcnt(0)\n"
    "v_and_b32 v133, v132, v133\n"
    "s_nop 0\n"
    "v_readfirstlane_b32 s59, v133\n"
    "s_add_u32 s73, s48, 0\n"
    "s_and_b32 s51, s73, 32\n"
    "s_and_b32 s73, s73, 31\n"
    "s_lshl3_add_u32 s73, s51, s73\n"
    "s_lshl2_add_u32 s73, s73, s93\n"
    "v_mov_b32 v134, s73\n"
    "v_mov_b32 v135, 1\n"
    "s_mov_b64 exec, 1\n"
    "global_store_dword v134, v135, %1 sc1\n"
    "s_mov_b64 exec, -1\n"
    "s_add_u32 s48, s48, 1\n"
    "s_waitcnt lgkmcnt(0)\n"
    "s_barrier\n"
    "s_branch .Lstep_%=\n"
    ".Lend_%=:\n"
    "s_cmp_eq_u32 %2, 3\n"
    "s_cbranch_scc0 .Lout_%=\n"
    "ds_read_b128 v[32:35], v0 offset:16384\n"
    "ds_read_b128 v[48:51], v0\n"
    "ds_read_b128 v[64:67], v0 offset:4096\n"
    "ds_read_b128 v[80:83], v4 offset:20480\n"
    "ds_read_b128 v[36:39], v0 offset:17408\n"
    "ds_read_b128 v[52:55], v0 offset:1024\n"
    "ds_read_b128 v[68:71], v0 offset:5120\n"
    "ds_read_b128 v[84:87], v4 offset:20544\n"
    "ds_read_b128 v[40:43], v0 offset:18432\n"
    "ds_read_b128 v[56:59], v0 offset:2048\n"
    "ds_read_b128 v[72:75], v0 offset:6144\n"
    "ds_read_b128 v[88:91], v4 offset:20608\n"
    "ds_read_b128 v[44:47], v0 offset:19456\n"
    "ds_read_b128 v[60:63], v0 offset:3072\n"
    "ds_read_b128 v[76:79], v0 offset:7168\n"
    "ds_read_b128 v[92:95], v4 offset:20672\n"
    "s_waitcnt lgkmcnt(14)\n"
    "v_mfma_f32_16x16x32_bf16 v[96:99], v[48:51], v[32:35], 0\n"
    "s_waitcnt lgkmcnt(13)\n"
    "v_mfma_f32_16x16x32_bf16 v[100:103], v[64:67], v[32:35], 0\n"
    "s_waitcnt lgkmcnt(12)\n"
    "v_mfma_f32_16x16x32_bf16 v[104:107], v[80:83], v[32:35], 0\n"
    "s_waitcnt lgkmcnt(10)\n"
    "v_mfma_f32_16x16x32_bf16 v[96:99], v[52:55], v[36:39], v[96:99]\n"
    "s_waitcnt lgkmcnt(9)\n"
    "v_mfma_f32_16x16x32_bf16 v[100:103], v[68:71], v[36:39], v[100:103]\n"
    "s_waitcnt lgkmcnt(8)\n"
    "v_mfma_f32_16x16x32_bf16 v[104:107], v[84:87], v[36:39], v[104:107]\n"
    "s_waitcnt lgkmcnt(6)\n"
    "v_mfma_f32_16x16x32_bf16 v[96:99], v[56:59], v[40:43], v[96:99]\n"
    "s_waitcnt lgkmcnt(5)\n"
    "v_mfma_f32_16x16x32_bf16 v[100:103], v[72:75], v[40:43], v[100:103]\n"
    "s_waitcnt lgkmcnt(4)\n"
    "v_mfma_f32_16x16x32_bf16 v[104:107], v[88:91], v[40:43], v[104:107]\n"
    "s_waitcnt lgkmcnt(2)\n"
    "v_mfma_f32_16x16x32_bf16 v[96:99], v[60:63], v[44:47], v[96:99]\n"
    "s_waitcnt lgkmcnt(1)\n"
    "v_mfma_f32_16x16x32_bf16 v[100:103], v[76:79], v[44:47], v[100:103]\n"
    "s_waitcnt lgkmcnt(0)\n"
    "v_mfma_f32_16x16x32_bf16 v[104:107], v[92:95], v[44:47], v[104:107]\n"
    "ds_read_b128 v[32:35], v0 offset:8192\n"
    "ds_read_b128 v[36:39], v0 offset:12288\n"
    "ds_read_b128 v[40:43], v0 offset:9216\n"
    "ds_read_b128 v[44:47], v0 offset:13312\n"
    "ds_read_b128 v[48:51], v0 offset:10240\n"
    "ds_read_b128 v[52:55], v0 offset:14336\n"
    "ds_read_b128 v[56:59], v0 offset:11264\n"
    "ds_read_b128 v[60:63], v0 offset:15360\n"
    "ds_read_b128 v[64:67], v0 offset:22784\n"
    "ds_read_b128 v[68:71], v0 offset:23808\n"
    "ds_read_b128 v[72:75], v0 offset:24832\n"
    "ds_read_b128 v[76:79], v0 offset:25856\n"
    "ds_read_b128 v[80:83], v11 offset:31888\n"
    "ds_read_b64 v[84:85], v11 offset:31904\n"
    "v_mov_b32 v86, 0xd01502f9\n"
    "v_cndmask_b32 v96, v86, v96, s[76:77]\n"
    "v_cndmask_b32 v97, v86, v97, s[78:79]\n"
    "v_cndmask_b32 v98, v86, v98, s[80:81]\n"
    "v_cndmask_b32 v99, v86, v99, s[82:83]\n"
    "v_cndmask_b32 v100, v86, v100, s[84:85]\n"
    "v_cndmask_b32 v101, v86, v101, s[86:87]\n"
    "v_cndmask_b32 v102, v86, v102, s[88:89]\n"
    "v_cndmask_b32 v103, v86, v103, s[90:91]\n"
    "v_maximum3_f32 v87, v104, v105, v106\n"
    "v_maximum3_f32 v87, v87, v107, v96\n"
    "v_maximum3_f32 v87, v87, v97, v98\n"
    "v_maximum3_f32 v87, v87, v99, v100\n"
    "v_maximum3_f32 v87, v87, v101, v102\n"
    "v_maximum3_f32 v87, v87, v103, v103\n"
    "v_mov_b32 v86, v87\n"
    "s_nop 1\n"
    "v_permlane16_swap_b32 v86, v87\n"
    "v_maximum3_f32 v87, v87, v86, v86\n"
    "v_mov_b32 v86, v87\n"
    "s_nop 1\n"
    "v_permlane32_swap_b32 v86, v87\n"
    "v_maximum3_f32 v87, v87, v86, v86\n"
    "v_mul_f32 v87, 0xbe0293ee, v87\n"
    "v_fmamk_f32 v96, v96, 0x3e0293ee, v87\n"
    "v_fmamk_f32 v97, v97, 0x3e0293ee, v87\n"
    "v_fmamk_f32 v98, v98, 0x3e0293ee, v87\n"
    "v_fmamk_f32 v99, v99, 0x3e0293ee, v87\n"
    "v_fmamk_f32 v100, v100, 0x3e0293ee, v87\n"
    "v_fmamk_f32 v101, v101, 0x3e0293ee, v87\n"
    "v_fmamk_f32 v102, v102, 0x3e0293ee, v87\n"
    "v_fmamk_f32 v103, v103, 0x3e0293ee, v87\n"
    "v_fmamk_f32 v104, v104, 0x3e0293ee, v87\n"
    "v_fmamk_f32 v105, v105, 0x3e0293ee, v87\n"
    "v_fmamk_f32 v106, v106, 0x3e0293ee, v87\n"
    "v_fmamk_f32 v107, v107, 0x3e0293ee, v87\n"
    "v_exp_f32 v96, v96\n"
    "v_exp_f32 v97, v97\n"
    "v_exp_f32 v98, v98\n"
    "v_exp_f32 v99, v99\n"
    "v_exp_f32 v100, v100\n"
    "v_exp_f32 v101, v101\n"
    "v_exp_f32 v102, v102\n"
    "v_exp_f32 v103, v103\n"
    "v_exp_f32 v104, v104\n"
    "v_exp_f32 v105, v105\n"
    "v_exp_f32 v106, v106\n"
    "v_exp_f32 v107, v107\n"
    "v_cvt_pk_bf16_f32 v96, v96, v97\n"
    "v_cvt_pk_bf16_f32 v97, v98, v99\n"
    "v_cvt_pk_bf16_f32 v98, v100, v101\n"
    "v_cvt_pk_bf16_f32 v99, v102, v103\n"
    "v_cvt_pk_bf16_f32 v104, v104, v105\n"
    "v_cvt_pk_bf16_f32 v105, v106, v107\n"
    "s_waitcnt lgkmcnt(1)\n"
    "v_mfma_f32_16x16x32_bf16 v[108:111], v[80:83], v[96:99], 0\n"
    "v_mfma_f32_16x16x32_bf16 v[88:91], v[32:35], v[96:99], 0\n"
    "v_mfma_f32_16x16x32_bf16 v[120:123], v[36:39], v[96:99], 0\n"
    "v_mfma_f32_16x16x32_bf16 v[92:95], v[40:43], v[96:99], 0\n"
    "s_waitcnt lgkmcnt(0)\n"
    "v_mfma_f32_16x16x16_bf16 v[108:111], v[84:85], v[104:105], v[108:111]\n"
    "v_mfma_f32_16x16x32_bf16 v[124:127], v[44:47], v[96:99], 0\n"
    "v_mfma_f32_16x16x32_bf16 v[112:115], v[48:51], v[96:99], 0\n"
    "v_mfma_f32_16x16x32_bf16 v[128:131], v[52:55], v[96:99], 0\n"
    "v_mfma_f32_16x16x32_bf16 v[116:119], v[56:59], v[96:99], 0\n"
    "v_mfma_f32_16x16x32_bf16 v[132:135], v[60:63], v[96:99], 0\n"
    "v_rcp_f32 v86, v108\n"
    "v_mfma_f32_16x16x16_bf16 v[88:91], v[64:65], v[104:105], v[88:91]\n"
    "v_mfma_f32_16x16x16_bf16 v[120:123], v[66:67], v[104:105], v[120:123]\n"
    "v_mfma_f32_16x16x16_bf16 v[92:95], v[68:69], v[104:105], v[92:95]\n"
    "v_mfma_f32_16x16x16_bf16 v[124:127], v[70:71], v[104:105], v[124:127]\n"
    "v_mfma_f32_16x16x16_bf16 v[112:115], v[72:73], v[104:105], v[112:115]\n"
    "v_mfma_f32_16x16x16_bf16 v[128:131], v[74:75], v[104:105], v[128:131]\n"
    "v_mfma_f32_16x16x16_bf16 v[116:119], v[76:77], v[104:105], v[116:119]\n"
    "v_mfma_f32_16x16x16_bf16 v[132:135], v[78:79], v[104:105], v[132:135]\n"
    "v_pk_mul_f32 v[88:89], v[88:89], v[86:87] op_sel_hi:[1,0]\n"
    "v_pk_mul_f32 v[90:91], v[90:91], v[86:87] op_sel_hi:[1,0]\n"
    "v_pk_mul_f32 v[120:121], v[120:121], v[86:87] op_sel_hi:[1,0]\n"
    "v_pk_mul_f32 v[122:123], v[122:123], v[86:87] op_sel_hi:[1,0]\n"
    "v_cvt_pk_bf16_f32 v88, v88, v120\n"
    "v_cvt_pk_bf16_f32 v89, v89, v121\n"
    "v_cvt_pk_bf16_f32 v90, v90, v122\n"
    "v_cvt_pk_bf16_f32 v91, v91, v123\n"
    "v_pk_mul_f32 v[92:93], v[92:93], v[86:87] op_sel_hi:[1,0]\n"
    "v_pk_mul_f32 v[94:95], v[94:95], v[86:87] op_sel_hi:[1,0]\n"
    "v_pk_mul_f32 v[124:125], v[124:125], v[86:87] op_sel_hi:[1,0]\n"
    "v_pk_mul_f32 v[126:127], v[126:127], v[86:87] op_sel_hi:[1,0]\n"
    "v_cvt_pk_bf16_f32 v92, v92, v124\n"
    "v_cvt_pk_bf16_f32 v93, v93, v125\n"
    "v_cvt_pk_bf16_f32 v94, v94, v126\n"
    "v_cvt_pk_bf16_f32 v95, v95, v127\n"
    "v_pk_mul_f32 v[112:113], v[112:113], v[86:87] op_sel_hi:[1,0]\n"
    "v_pk_mul_f32 v[114:115], v[114:115], v[86:87] op_sel_hi:[1,0]\n"
    "v_pk_mul_f32 v[128:129], v[128:129], v[86:87] op_sel_hi:[1,0]\n"
    "v_pk_mul_f32 v[130:131], v[130:131], v[86:87] op_sel_hi:[1,0]\n"
    "v_cvt_pk_bf16_f32 v112, v112, v128\n"
    "v_cvt_pk_bf16_f32 v113, v113, v129\n"
    "v_cvt_pk_bf16_f32 v114, v114, v130\n"
    "v_cvt_pk_bf16_f32 v115, v115, v131\n"
    "v_pk_mul_f32 v[116:117], v[116:117], v[86:87] op_sel_hi:[1,0]\n"
    "v_pk_mul_f32 v[118:119], v[118:119], v[86:87] op_sel_hi:[1,0]\n"
    "v_pk_mul_f32 v[132:133], v[132:133], v[86:87] op_sel_hi:[1,0]\n"
    "v_pk_mul_f32 v[134:135], v[134:135], v[86:87] op_sel_hi:[1,0]\n"
    "v_cvt_pk_bf16_f32 v116, v116, v132\n"
    "v_cvt_pk_bf16_f32 v117, v117, v133\n"
    "v_cvt_pk_bf16_f32 v118, v118, v134\n"
    "v_cvt_pk_bf16_f32 v119, v119, v135\n"
    "s_lshr_b32 s51, s48, 2\n"
    "s_xor_b32 s51, s51, s69\n"
    "s_and_b32 s51, s51, 1\n"
    "s_lshl_b32 s51, s51, 13\n"
    "s_lshl_b32 s73, s48, 19\n"
    "s_add_u32 s73, s73, s75\n"
    "s_add_u32 s73, s73, s51\n"
    "buffer_store_dwordx4 v[88:91], v0, s[40:43], s73 offen sc1\n"
    "buffer_store_dwordx4 v[92:95], v0, s[40:43], s73 offen offset:1024 sc1\n"
    "buffer_store_dwordx4 v[112:115], v0, s[40:43], s73 offen offset:2048 sc1\n"
    "buffer_store_dwordx4 v[116:119], v0, s[40:43], s73 offen offset:3072 sc1\n"
    "s_waitcnt vmcnt(0)\n"
    "s_add_u32 s73, s48, 0\n"
    "s_and_b32 s51, s73, 32\n"
    "s_and_b32 s73, s73, 31\n"
    "s_lshl3_add_u32 s73, s51, s73\n"
    "s_lshl2_add_u32 s73, s73, s93\n"
    "v_mov_b32 v134, s73\n"
    "v_mov_b32 v135, 1\n"
    "s_mov_b64 exec, 1\n"
    "global_store_dword v134, v135, %1 sc1\n"
    "s_mov_b64 exec, -1\n"
    "s_cmp_eq_u32 %3, 128\n"
    "s_cbranch_scc0 .Lout_%=\n"
    "v_mov_b32 v130, 0x20400\n"
    "global_load_dwordx2 v[132:133], v130, %1 sc1\n"
    "v_mbcnt_lo_u32_b32 v131, -1, 0\n"
    "v_min_u32 v131, 7, v131\n"
    "v_lshlrev_b32 v131, 7, v131\n"
    "v_add_u32 v131, 0x20000, v131\n"
    "global_load_dwordx2 v[134:135], v131, %1 sc1\n"
    "s_waitcnt vmcnt(0)\n"
    "v_add_u32 v132, 1, v132\n"
    "v_add_u32 v133, 1, v133\n"
    "global_store_dwordx2 v130, v[132:133], %1 sc1\n"
    ".Lout_%=:\n"
    "s_waitcnt vmcnt(0)\n"
    "s_endpgm\n"
    ".p2align 6\n"
    ".Lrole1_%=:\n"
    "s_mov_b64 s[40:41], %0\n"
    "s_and_b32 s41, s41, 0xffff\n"
    "s_mov_b32 s42, 0xbc00000\n"
    "s_mov_b32 s43, 0x20000\n"
    "s_mov_b64 s[92:93], %0\n"
    "s_and_b32 s93, s93, 0xffff\n"
    "s_mov_b32 s94, 0xbc00000\n"
    "s_mov_b32 s95, 0x20000\n"
    "s_mov_b32 s53, 131072\n"
    "s_mov_b64 s[56:57], 0\n"
    "v_mbcnt_lo_u32_b32 v7, -1, 0\n"
    "v_mbcnt_hi_u32_b32 v7, -1, v7\n"
    "s_lshl_b32 s50, %2, 8\n"
    "s_add_u32 s50, s50, 147456\n"
    "v_lshl_add_u32 v7, v7, 2, s50\n"
    "ds_read_b32 v1, v7\n"
    "ds_read_b32 v2, v7 offset:1024\n"
    "ds_read_b32 v3, v7 offset:2048\n"
    "ds_read_b32 v5, v7 offset:3072\n"
    "ds_read_b32 v6, v7 offset:4096\n"
    "ds_read_b32 v48, v7 offset:5120\n"
    "ds_read_b32 v49, v7 offset:6144\n"
    "ds_read_b32 v50, v7 offset:7168\n"
    "s_waitcnt lgkmcnt(0)\n"
    "s_barrier\n"
    "s_lshl_b32 s54, %2, 10\n"
    "s_add_u32 s88, s40, 32768\n"
    "s_mov_b32 s89, s41\n"
    "s_cmp_lt_u32 %2, 2\n"
    "s_cselect_b32 s96, s42, 0\n"
    "s_mov_b32 s91, s43\n"
    "s_mov_b32 s86, 0x3f803f80\n"
    "s_mov_b32 s87, 0x3f803f80\n"
    "v_mov_b64 v[76:77], s[86:87]\n"
    "v_mov_b64 v[78:79], s[86:87]\n"
    "s_mov_b64 s[44:45], %8\n"
    "s_and_b32 s45, s45, 0xffff\n"
    "s_mov_b32 s46, 0x480000\n"
    "s_mov_b32 s47, 0x20000\n"
    "s_add_u32 m0, s54, 0\n"
    "s_add_u32 s55, %6, 0\n"
    "buffer_load_dwordx4 v48, s[44:47], s55 offen lds\n"
    "s_add_u32 m0, s54, 4096\n"
    "s_add_u32 s55, %6, 1048576\n"
    "buffer_load_dwordx4 v48, s[44:47], s55 offen lds\n"
    "s_add_u32 m0, s54, 8192\n"
    "s_add_u32 s55, %6, 2097152\n"
    "buffer_load_dwordx4 v48, s[44:47], s55 offen lds\n"
    "s_add_u32 m0, s54, 12288\n"
    "s_add_u32 s55, %6, 3145728\n"
    "buffer_load_dwordx4 v48, s[44:47], s55 offen lds\n"
    "s_add_u32 m0, s54, 16384\n"
    "s_add_u32 s55, %6, 32768\n"
    "buffer_load_dwordx4 v48, s[44:47], s55 offen lds\n"
    "s_add_u32 m0, s54, 20480\n"
    "s_add_u32 s55, %6, 1081344\n"
    "buffer_load_dwordx4 v48, s[44:47], s55 offen lds\n"
    "s_add_u32 m0, s54, 24576\n"
    "s_add_u32 s55, %6, 2129920\n"
    "buffer_load_dwordx4 v48, s[44:47], s55 offen lds\n"
    "s_add_u32 m0, s54, 28672\n"
    "s_add_u32 s55, %6, 3178496\n"
    "buffer_load_dwordx4 v48, s[44:47], s55 offen lds\n"
    "s_add_u32 m0, s54, 32768\n"
    "s_add_u32 s55, %6, 262144\n"
    "buffer_load_dwordx4 v48, s[44:47], s55 offen lds\n"
    "s_add_u32 m0, s54, 36864\n"
    "s_add_u32 s55, %6, 1310720\n"
    "buffer_load_dwordx4 v48, s[44:47], s55 offen lds\n"
    "s_add_u32 m0, s54, 40960\n"
    "s_add_u32 s55, %6, 2359296\n"
    "buffer_load_dwordx4 v48, s[44:47], s55 offen lds\n"
    "s_add_u32 m0, s54, 45056\n"
    "s_add_u32 s55, %6, 3407872\n"
    "buffer_load_dwordx4 v48, s[44:47], s55 offen lds\n"
    "s_add_u32 m0, s54, 49152\n"
    "s_add_u32 s55, %6, 294912\n"
    "buffer_load_dwordx4 v48, s[44:47], s55 offen lds\n"
    "s_add_u32 m0, s54, 53248\n"
    "s_add_u32 s55, %6, 1343488\n"
    "buffer_load_dwordx4 v48, s[44:47], s55 offen lds\n"
    "s_add_u32 m0, s54, 57344\n"
    "s_add_u32 s55, %6, 2392064\n"
    "buffer_load_dwordx4 v48, s[44:47], s55 offen lds\n"
    "s_add_u32 m0, s54, 61440\n"
    "s_add_u32 s55, %6, 3440640\n"
    "buffer_load_dwordx4 v48, s[44:47], s55 offen lds\n"
    "s_add_u32 m0, s54, 65536\n"
    "s_add_u32 s55, %6, 524288\n"
    "buffer_load_dwordx4 v48, s[44:47], s55 offen lds\n"
    "s_add_u32 m0, s54, 69632\n"
    "s_add_u32 s55, %6, 1572864\n"
    "buffer_load_dwordx4 v48, s[44:47], s55 offen lds\n"
    "s_add_u32 m0, s54, 73728\n"
    "s_add_u32 s55, %6, 2621440\n"
    "buffer_load_dwordx4 v48, s[44:47], s55 offen lds\n"
    "s_add_u32 m0, s54, 77824\n"
    "s_add_u32 s55, %6, 3670016\n"
    "buffer_load_dwordx4 v48, s[44:47], s55 offen lds\n"
    "s_add_u32 m0, s54, 81920\n"
    "s_add_u32 s55, %6, 557056\n"
    "buffer_load_dwordx4 v48, s[44:47], s55 offen lds\n"
    "s_add_u32 m0, s54, 86016\n"
    "s_add_u32 s55, %6, 1605632\n"
    "buffer_load_dwordx4 v48, s[44:47], s55 offen lds\n"
    "s_add_u32 m0, s54, 90112\n"
    "s_add_u32 s55, %6, 2654208\n"
    "buffer_load_dwordx4 v48, s[44:47], s55 offen lds\n"
    "s_add_u32 m0, s54, 94208\n"
    "s_add_u32 s55, %6, 3702784\n"
    "buffer_load_dwordx4 v48, s[44:47], s55 offen lds\n"
    "s_add_u32 m0, s54, 98304\n"
    "s_add_u32 s55, %6, 786432\n"
    "buffer_load_dwordx4 v48, s[44:47], s55 offen lds\n"
    "s_add_u32 m0, s54, 102400\n"
    "s_add_u32 s55, %6, 1835008\n"
    "buffer_load_dwordx4 v48, s[44:47], s55 offen lds\n"
    "s_add_u32 m0, s54, 106496\n"
    "s_add_u32 s55, %6, 2883584\n"
    "buffer_load_dwordx4 v48, s[44:47], s55 offen lds\n"
    "s_add_u32 m0, s54, 110592\n"
    "s_add_u32 s55, %6, 3932160\n"
    "buffer_load_dwordx4 v48, s[44:47], s55 offen lds\n"
    "s_add_u32 m0, s54, 114688\n"
    "s_add_u32 s55, %6, 819200\n"
    "buffer_load_dwordx4 v48, s[44:47], s55 offen lds\n"
    "s_add_u32 m0, s54, 118784\n"
    "s_add_u32 s55, %6, 1867776\n"
    "buffer_load_dwordx4 v48, s[44:47], s55 offen lds\n"
    "s_add_u32 m0, s54, 122880\n"
    "s_add_u32 s55, %6, 2916352\n"
    "buffer_load_dwordx4 v48, s[44:47], s55 offen lds\n"
    "s_add_u32 m0, s54, 126976\n"
    "s_add_u32 s55, %6, 3964928\n"
    "buffer_load_dwordx4 v48, s[44:47], s55 offen lds\n"
    "s_add_u32 m0, s54, 131072\n"
    "s_add_u32 s55, %6, 4194304\n"
    "buffer_load_dwordx4 v48, s[44:47], s55 offen lds\n"
    "s_add_u32 m0, s54, 135168\n"
    "s_add_u32 s55, %6, 4227072\n"
    "buffer_load_dwordx4 v48, s[44:47], s55 offen lds\n"
    "s_add_u32 m0, s54, 139264\n"
    "s_add_u32 s55, %6, 4456448\n"
    "buffer_load_dwordx4 v48, s[44:47], s55 offen lds\n"
    "s_add_u32 m0, s54, 143360\n"
    "s_add_u32 s55, %6, 4489216\n"
    "buffer_load_dwordx4 v48, s[44:47], s55 offen lds\n"
    "s_xor_b32 s50, %2, 0\n"
    "s_lshl_b32 s50, s50, 12\n"
    "v_add_u32 v32, s50, v49\n"
    "v_add_u32 v33, s50, v50\n"
    "s_xor_b32 s50, %2, 1\n"
    "s_lshl_b32 s50, s50, 12\n"
    "v_add_u32 v34, s50, v49\n"
    "v_add_u32 v35, s50, v50\n"
    "s_xor_b32 s50, %2, 2\n"
    "s_lshl_b32 s50, s50, 12\n"
    "v_add_u32 v36, s50, v49\n"
    "v_add_u32 v37, s50, v50\n"
    "s_xor_b32 s50, %2, 3\n"
    "s_lshl_b32 s50, s50, 12\n"
    "v_add_u32 v38, s50, v49\n"
    "v_add_u32 v39, s50, v50\n"
    "s_waitcnt vmcnt(32) lgkmcnt(0)\n"
    "s_barrier\n"
    "ds_read_b64_tr_b16 a[0:1], v32\n"
    "ds_read_b64_tr_b16 a[4:5], v33\n"
    "ds_read_b64_tr_b16 a[32:33], v34\n"
    "ds_read_b64_tr_b16 a[36:37], v35\n"
    "ds_read_b64_tr_b16 a[64:65], v36\n"
    "ds_read_b64_tr_b16 a[68:69], v37\n"
    "ds_read_b64_tr_b16 a[96:97], v38\n"
    "ds_read_b64_tr_b16 a[100:101], v39\n"
    "s_waitcnt vmcnt(28) lgkmcnt(0)\n"
    "s_barrier\n"
    "s_mov_b64 s[44:45], %9\n"
    "s_and_b32 s45, s45, 0xffff\n"
    "s_mov_b32 s46, 0x480000\n"
    "s_mov_b32 s47, 0x20000\n"
    "s_add_u32 m0, s54, 0\n"
    "s_add_u32 s55, %6, 0\n"
    "buffer_load_dwordx4 v48, s[44:47], s55 offen lds\n"
    "s_add_u32 m0, s54, 4096\n"
    "s_add_u32 s55, %6, 1048576\n"
    "buffer_load_dwordx4 v48, s[44:47], s55 offen lds\n"
    "s_add_u32 m0, s54, 8192\n"
    "s_add_u32 s55, %6, 2097152\n"
    "buffer_load_dwordx4 v48, s[44:47], s55 offen lds\n"
    "s_add_u32 m0, s54, 12288\n"
    "s_add_u32 s55, %6, 3145728\n"
    "buffer_load_dwordx4 v48, s[44:47], s55 offen lds\n"
    "ds_read_b64_tr_b16 a[2:3], v32 offset:16384\n"
    "ds_read_b64_tr_b16 a[6:7], v33 offset:16384\n"
    "ds_read_b64_tr_b16 a[34:35], v34 offset:16384\n"
    "ds_read_b64_tr_b16 a[38:39], v35 offset:16384\n"
    "ds_read_b64_tr_b16 a[66:67], v36 offset:16384\n"
    "ds_read_b64_tr_b16 a[70:71], v37 offset:16384\n"
    "ds_read_b64_tr_b16 a[98:99], v38 offset:16384\n"
    "ds_read_b64_tr_b16 a[102:103], v39 offset:16384\n"
    "s_waitcnt vmcnt(28) lgkmcnt(0)\n"
    "s_barrier\n"
    "s_add_u32 m0, s54, 16384\n"
    "s_add_u32 s55, %6, 32768\n"
    "buffer_load_dwordx4 v48, s[44:47], s55 offen lds\n"
    "s_add_u32 m0, s54, 20480\n"
    "s_add_u32 s55, %6, 1081344\n"
    "buffer_load_dwordx4 v48, s[44:47], s55 offen lds\n"
    "s_add_u32 m0, s54, 24576\n"
    "s_add_u32 s55, %6, 2129920\n"
    "buffer_load_dwordx4 v48, s[44:47], s55 offen lds\n"
    "s_add_u32 m0, s54, 28672\n"
    "s_add_u32 s55, %6, 3178496\n"
    "buffer_load_dwordx4 v48, s[44:47], s55 offen lds\n"
    "v_mfma_f32_16x16x32_bf16 v[8:11], v[76:79], a[0:3], 0\n"
    "v_mfma_f32_16x16x32_bf16 v[12:15], v[76:79], a[4:7], 0\n"
    "v_mfma_f32_16x16x32_bf16 v[8:11], v[76:79], a[32:35], v[8:11]\n"
    "v_mfma_f32_16x16x32_bf16 v[12:15], v[76:79], a[36:39], v[12:15]\n"
    "v_mfma_f32_16x16x32_bf16 v[8:11], v[76:79], a[64:67], v[8:11]\n"
    "v_mfma_f32_16x16x32_bf16 v[12:15], v[76:79], a[68:71], v[12:15]\n"
    "v_mfma_f32_16x16x32_bf16 v[8:11], v[76:79], a[96:99], v[8:11]\n"
    "v_mfma_f32_16x16x32_bf16 v[12:15], v[76:79], a[100:103], v[12:15]\n"
    "ds_read_b64_tr_b16 a[8:9], v32 offset:32768\n"
    "ds_read_b64_tr_b16 a[12:13], v33 offset:32768\n"
    "ds_read_b64_tr_b16 a[40:41], v34 offset:32768\n"
    "ds_read_b64_tr_b16 a[44:45], v35 offset:32768\n"
    "ds_read_b64_tr_b16 a[72:73], v36 offset:32768\n"
    "ds_read_b64_tr_b16 a[76:77], v37 offset:32768\n"
    "ds_read_b64_tr_b16 a[104:105], v38 offset:32768\n"
    "ds_read_b64_tr_b16 a[108:109], v39 offset:32768\n"
    "s_waitcnt vmcnt(28) lgkmcnt(0)\n"
    "s_barrier\n"
    "s_add_u32 m0, s54, 32768\n"
    "s_add_u32 s55, %6, 262144\n"
    "buffer_load_dwordx4 v48, s[44:47], s55 offen lds\n"
    "s_add_u32 m0, s54, 36864\n"
    "s_add_u32 s55, %6, 1310720\n"
    "buffer_load_dwordx4 v48, s[44:47], s55 offen lds\n"
    "s_add_u32 m0, s54, 40960\n"
    "s_add_u32 s55, %6, 2359296\n"
    "buffer_load_dwordx4 v48, s[44:47], s55 offen lds\n"
    "s_add_u32 m0, s54, 45056\n"
    "s_add_u32 s55, %6, 3407872\n"
    "buffer_load_dwordx4 v48, s[44:47], s55 offen lds\n"
    "ds_read_b64_tr_b16 a[10:11], v32 offset:49152\n"
    "ds_read_b64_tr_b16 a[14:15], v33 offset:49152\n"
    "ds_read_b64_tr_b16 a[42:43], v34 offset:49152\n"
    "ds_read_b64_tr_b16 a[46:47], v35 offset:49152\n"
    "ds_read_b64_tr_b16 a[74:75], v36 offset:49152\n"
    "ds_read_b64_tr_b16 a[78:79], v37 offset:49152\n"
    "ds_read_b64_tr_b16 a[106:107], v38 offset:49152\n"
    "ds_read_b64_tr_b16 a[110:111], v39 offset:49152\n"
    "s_waitcnt vmcnt(28) lgkmcnt(0)\n"
    "s_barrier\n"
    "s_add_u32 m0, s54, 49152\n"
    "s_add_u32 s55, %6, 294912\n"
    "buffer_load_dwordx4 v48, s[44:47], s55 offen lds\n"
    "s_add_u32 m0, s54, 53248\n"
    "s_add_u32 s55, %6, 1343488\n"
    "buffer_load_dwordx4 v48, s[44:47], s55 offen lds\n"
    "s_add_u32 m0, s54, 57344\n"
    "s_add_u32 s55, %6, 2392064\n"
    "buffer_load_dwordx4 v48, s[44:47], s55 offen lds\n"
    "s_add_u32 m0, s54, 61440\n"
    "s_add_u32 s55, %6, 3440640\n"
    "buffer_load_dwordx4 v48, s[44:47], s55 offen lds\n"
    "v_mfma_f32_16x16x32_bf16 v[8:11], v[76:79], a[8:11], v[8:11]\n"
    "v_mfma_f32_16x16x32_bf16 v[12:15], v[76:79], a[12:15], v[12:15]\n"
    "v_mfma_f32_16x16x32_bf16 v[8:11], v[76:79], a[40:43], v[8:11]\n"
    "v_mfma_f32_16x16x32_bf16 v[12:15], v[76:79], a[44:47], v[12:15]\n"
    "v_mfma_f32_16x16x32_bf16 v[8:11], v[76:79], a[72:75], v[8:11]\n"
    "v_mfma_f32_16x16x32_bf16 v[12:15], v[76:79], a[76:79], v[12:15]\n"
    "v_mfma_f32_16x16x32_bf16 v[8:11], v[76:79], a[104:107], v[8:11]\n"
    "v_mfma_f32_16x16x32_bf16 v[12:15], v[76:79], a[108:111], v[12:15]\n"
    "v_add_u32 v71, 0x10000, v32\n"
    "ds_read_b64_tr_b16 a[16:17], v71\n"
    "v_add_u32 v70, 0x10000, v33\n"
    "ds_read_b64_tr_b16 a[20:21], v70\n"
    "v_add_u32 v69, 0x10000, v34\n"
    "ds_read_b64_tr_b16 a[48:49], v69\n"
    "v_add_u32 v68, 0x10000, v35\n"
    "ds_read_b64_tr_b16 a[52:53], v68\n"
    "v_add_u32 v67, 0x10000, v36\n"
    "ds_read_b64_tr_b16 a[80:81], v67\n"
    "v_add_u32 v66, 0x10000, v37\n"
    "ds_read_b64_tr_b16 a[84:85], v66\n"
    "v_add_u32 v65, 0x10000, v38\n"
    "ds_read_b64_tr_b16 a[112:113], v65\n"
    "v_add_u32 v64, 0x10000, v39\n"
    "ds_read_b64_tr_b16 a[116:117], v64\n"
    "s_waitcnt vmcnt(28) lgkmcnt(0)\n"
    "s_barrier\n"
    "s_add_u32 m0, s54, 65536\n"
    "s_add_u32 s55, %6, 524288\n"
    "buffer_load_dwordx4 v48, s[44:47], s55 offen lds\n"
    "s_add_u32 m0, s54, 69632\n"
    "s_add_u32 s55, %6, 1572864\n"
    "buffer_load_dwordx4 v48, s[44:47], s55 offen lds\n"
    "s_add_u32 m0, s54, 73728\n"
    "s_add_u32 s55, %6, 2621440\n"
    "buffer_load_dwordx4 v48, s[44:47], s55 offen lds\n"
    "s_add_u32 m0, s54, 77824\n"
    "s_add_u32 s55, %6, 3670016\n"
    "buffer_load_dwordx4 v48, s[44:47], s55 offen lds\n"
    "ds_read_b64_tr_b16 a[18:19], v71 offset:16384\n"
    "ds_read_b64_tr_b16 a[22:23], v70 offset:16384\n"
    "ds_read_b64_tr_b16 a[50:51], v69 offset:16384\n"
    "ds_read_b64_tr_b16 a[54:55], v68 offset:16384\n"
    "ds_read_b64_tr_b16 a[82:83], v67 offset:16384\n"
    "ds_read_b64_tr_b16 a[86:87], v66 offset:16384\n"
    "ds_read_b64_tr_b16 a[114:115], v65 offset:16384\n"
    "ds_read_b64_tr_b16 a[118:119], v64 offset:16384\n"
    "s_waitcnt vmcnt(28) lgkmcnt(0)\n"
    "s_barrier\n"
    "s_add_u32 m0, s54, 81920\n"
    "s_add_u32 s55, %6, 557056\n"
    "buffer_load_dwordx4 v48, s[44:47], s55 offen lds\n"
    "s_add_u32 m0, s54, 86016\n"
    "s_add_u32 s55, %6, 1605632\n"
    "buffer_load_dwordx4 v48, s[44:47], s55 offen lds\n"
    "s_add_u32 m0, s54, 90112\n"
    "s_add_u32 s55, %6, 2654208\n"
    "buffer_load_dwordx4 v48, s[44:47], s55 offen lds\n"
    "s_add_u32 m0, s54, 94208\n"
    "s_add_u32 s55, %6, 3702784\n"
    "buffer_load_dwordx4 v48, s[44:47], s55 offen lds\n"
    "v_mfma_f32_16x16x32_bf16 v[8:11], v[76:79], a[16:19], v[8:11]\n"
    "v_mfma_f32_16x16x32_bf16 v[12:15], v[76:79], a[20:23], v[12:15]\n"
    "v_mfma_f32_16x16x32_bf16 v[8:11], v[76:79], a[48:51], v[8:11]\n"
    "v_mfma_f32_16x16x32_bf16 v[12:15], v[76:79], a[52:55], v[12:15]\n"
    "v_mfma_f32_16x16x32_bf16 v[8:11], v[76:79], a[80:83], v[8:11]\n"
    "v_mfma_f32_16x16x32_bf16 v[12:15], v[76:79], a[84:87], v[12:15]\n"
    "v_mfma_f32_16x16x32_bf16 v[8:11], v[76:79], a[112:115], v[8:11]\n"
    "v_mfma_f32_16x16x32_bf16 v[12:15], v[76:79], a[116:119], v[12:15]\n"
    "ds_read_b64_tr_b16 a[24:25], v71 offset:32768\n"
    "ds_read_b64_tr_b16 a[28:29], v70 offset:32768\n"
    "ds_read_b64_tr_b16 a[56:57], v69 offset:32768\n"
    "ds_read_b64_tr_b16 a[60:61], v68 offset:32768\n"
    "ds_read_b64_tr_b16 a[88:89], v67 offset:32768\n"
    "ds_read_b64_tr_b16 a[92:93], v66 offset:32768\n"
    "ds_read_b64_tr_b16 a[120:121], v65 offset:32768\n"
    "ds_read_b64_tr_b16 a[124:125], v64 offset:32768\n"
    "s_waitcnt vmcnt(28) lgkmcnt(0)\n"
    "s_barrier\n"
    "s_add_u32 m0, s54, 98304\n"
    "s_add_u32 s55, %6, 786432\n"
    "buffer_load_dwordx4 v48, s[44:47], s55 offen lds\n"
    "s_add_u32 m0, s54, 102400\n"
    "s_add_u32 s55, %6, 1835008\n"
    "buffer_load_dwordx4 v48, s[44:47], s55 offen lds\n"
    "s_add_u32 m0, s54, 106496\n"
    "s_add_u32 s55, %6, 2883584\n"
    "buffer_load_dwordx4 v48, s[44:47], s55 offen lds\n"
    "s_add_u32 m0, s54, 110592\n"
    "s_add_u32 s55, %6, 3932160\n"
    "buffer_load_dwordx4 v48, s[44:47], s55 offen lds\n"
    "ds_read_b64_tr_b16 a[26:27], v71 offset:49152\n"
    "ds_read_b64_tr_b16 a[30:31], v70 offset:49152\n"
    "ds_read_b64_tr_b16 a[58:59], v69 offset:49152\n"
    "ds_read_b64_tr_b16 a[62:63], v68 offset:49152\n"
    "ds_read_b64_tr_b16 a[90:91], v67 offset:49152\n"
    "ds_read_b64_tr_b16 a[94:95], v66 offset:49152\n"
    "ds_read_b64_tr_b16 a[122:123], v65 offset:49152\n"
    "ds_read_b64_tr_b16 a[126:127], v64 offset:49152\n"
    "s_waitcnt vmcnt(28) lgkmcnt(0)\n"
    "s_barrier\n"
    "s_add_u32 m0, s54, 114688\n"
    "s_add_u32 s55, %6, 819200\n"
    "buffer_load_dwordx4 v48, s[44:47], s55 offen lds\n"
    "s_add_u32 m0, s54, 118784\n"
    "s_add_u32 s55, %6, 1867776\n"
    "buffer_load_dwordx4 v48, s[44:47], s55 offen lds\n"
    "s_add_u32 m0, s54, 122880\n"
    "s_add_u32 s55, %6, 2916352\n"
    "buffer_load_dwordx4 v48, s[44:47], s55 offen lds\n"
    "s_add_u32 m0, s54, 126976\n"
    "s_add_u32 s55, %6, 3964928\n"
    "buffer_load_dwordx4 v48, s[44:47], s55 offen lds\n"
    "v_mfma_f32_16x16x32_bf16 v[8:11], v[76:79], a[24:27], v[8:11]\n"
    "v_mfma_f32_16x16x32_bf16 v[12:15], v[76:79], a[28:31], v[12:15]\n"
    "v_mfma_f32_16x16x32_bf16 v[8:11], v[76:79], a[56:59], v[8:11]\n"
    "v_mfma_f32_16x16x32_bf16 v[12:15], v[76:79], a[60:63], v[12:15]\n"
    "v_mfma_f32_16x16x32_bf16 v[8:11], v[76:79], a[88:91], v[8:11]\n"
    "v_mfma_f32_16x16x32_bf16 v[12:15], v[76:79], a[92:95], v[12:15]\n"
    "v_mfma_f32_16x16x32_bf16 v[8:11], v[76:79], a[120:123], v[8:11]\n"
    "v_mfma_f32_16x16x32_bf16 v[12:15], v[76:79], a[124:127], v[12:15]\n"
    "v_add_u32 v63, 0x20000, v49\n"
    "ds_read_b64_tr_b16 a[128:129], v63\n"
    "v_add_u32 v62, 0x20000, v50\n"
    "ds_read_b64_tr_b16 a[132:133], v62\n"
    "ds_read_b64_tr_b16 a[130:131], v63 offset:4096\n"
    "ds_read_b64_tr_b16 a[134:135], v62 offset:4096\n"
    "ds_read_b64_tr_b16 a[136:137], v63 offset:8192\n"
    "ds_read_b64_tr_b16 a[140:141], v62 offset:8192\n"
    "ds_read_b64_tr_b16 a[138:139], v63 offset:12288\n"
    "ds_read_b64_tr_b16 a[142:143], v62 offset:12288\n"
    "s_waitcnt vmcnt(28) lgkmcnt(0)\n"
    "s_barrier\n"
    "s_add_u32 m0, s54, 131072\n"
    "s_add_u32 s55, %6, 4194304\n"
    "buffer_load_dwordx4 v48, s[44:47], s55 offen lds\n"
    "s_add_u32 m0, s54, 135168\n"
    "s_add_u32 s55, %6, 4227072\n"
    "buffer_load_dwordx4 v48, s[44:47], s55 offen lds\n"
    "s_add_u32 m0, s54, 139264\n"
    "s_add_u32 s55, %6, 4456448\n"
    "buffer_load_dwordx4 v48, s[44:47], s55 offen lds\n"
    "s_add_u32 m0, s54, 143360\n"
    "s_add_u32 s55, %6, 4489216\n"
    "buffer_load_dwordx4 v48, s[44:47], s55 offen lds\n"
    "v_mfma_f32_16x16x32_bf16 v[8:11], v[76:79], a[128:131], v[8:11]\n"
    "v_mfma_f32_16x16x32_bf16 v[12:15], v[76:79], a[132:135], v[12:15]\n"
    "v_mfma_f32_16x16x32_bf16 v[8:11], v[76:79], a[136:139], v[8:11]\n"
    "v_mfma_f32_16x16x32_bf16 v[12:15], v[76:79], a[140:143], v[12:15]\n"
    "ds_read_b64_tr_b16 a[144:145], v32\n"
    "ds_read_b64_tr_b16 a[148:149], v33\n"
    "ds_read_b64_tr_b16 a[176:177], v34\n"
    "ds_read_b64_tr_b16 a[180:181], v35\n"
    "ds_read_b64_tr_b16 a[208:209], v36\n"
    "ds_read_b64_tr_b16 a[212:213], v37\n"
    "ds_read_b64_tr_b16 a[240:241], v38\n"
    "ds_read_b64_tr_b16 a[244:245], v39\n"
    "s_waitcnt vmcnt(28) lgkmcnt(0)\n"
    "s_barrier\n"
    "s_mov_b64 s[44:45], %10\n"
    "s_and_b32 s45, s45, 0xffff\n"
    "s_mov_b32 s46, 0x480000\n"
    "s_mov_b32 s47, 0x20000\n"
    "s_add_u32 m0, s54, 0\n"
    "s_add_u32 s55, %6, 0\n"
    "buffer_load_dwordx4 v48, s[44:47], s55 offen lds\n"
    "s_add_u32 m0, s54, 4096\n"
    "s_add_u32 s55, %6, 1048576\n"
    "buffer_load_dwordx4 v48, s[44:47], s55 offen lds\n"
    "s_add_u32 m0, s54, 8192\n"
    "s_add_u32 s55, %6, 2097152\n"
    "buffer_load_dwordx4 v48, s[44:47], s55 offen lds\n"
    "s_add_u32 m0, s54, 12288\n"
    "s_add_u32 s55, %6, 3145728\n"
    "buffer_load_dwordx4 v48, s[44:47], s55 offen lds\n"
    "ds_read_b64_tr_b16 a[146:147], v32 offset:16384\n"
    "ds_read_b64_tr_b16 a[150:151], v33 offset:16384\n"
    "ds_read_b64_tr_b16 a[178:179], v34 offset:16384\n"
    "ds_read_b64_tr_b16 a[182:183], v35 offset:16384\n"
    "ds_read_b64_tr_b16 a[210:211], v36 offset:16384\n"
    "ds_read_b64_tr_b16 a[214:215], v37 offset:16384\n"
    "ds_read_b64_tr_b16 a[242:243], v38 offset:16384\n"
    "ds_read_b64_tr_b16 a[246:247], v39 offset:16384\n"
    "s_waitcnt vmcnt(28) lgkmcnt(0)\n"
    "s_barrier\n"
    "s_add_u32 m0, s54, 16384\n"
    "s_add_u32 s55, %6, 32768\n"
    "buffer_load_dwordx4 v48, s[44:47], s55 offen lds\n"
    "s_add_u32 m0, s54, 20480\n"
    "s_add_u32 s55, %6, 1081344\n"
    "buffer_load_dwordx4 v48, s[44:47], s55 offen lds\n"
    "s_add_u32 m0, s54, 24576\n"
    "s_add_u32 s55, %6, 2129920\n"
    "buffer_load_dwordx4 v48, s[44:47], s55 offen lds\n"
    "s_add_u32 m0, s54, 28672\n"
    "s_add_u32 s55, %6, 3178496\n"
    "buffer_load_dwordx4 v48, s[44:47], s55 offen lds\n"
    "v_mfma_f32_16x16x32_bf16 v[16:19], v[76:79], a[144:147], 0\n"
    "v_mfma_f32_16x16x32_bf16 v[20:23], v[76:79], a[148:151], 0\n"
    "v_mfma_f32_16x16x32_bf16 v[16:19], v[76:79], a[176:179], v[16:19]\n"
    "v_mfma_f32_16x16x32_bf16 v[20:23], v[76:79], a[180:183], v[20:23]\n"
    "v_mfma_f32_16x16x32_bf16 v[16:19], v[76:79], a[208:211], v[16:19]\n"
    "v_mfma_f32_16x16x32_bf16 v[20:23], v[76:79], a[212:215], v[20:23]\n"
    "v_mfma_f32_16x16x32_bf16 v[16:19], v[76:79], a[240:243], v[16:19]\n"
    "v_mfma_f32_16x16x32_bf16 v[20:23], v[76:79], a[244:247], v[20:23]\n"
    "ds_read_b64_tr_b16 a[152:153], v32 offset:32768\n"
    "ds_read_b64_tr_b16 a[156:157], v33 offset:32768\n"
    "ds_read_b64_tr_b16 a[184:185], v34 offset:32768\n"
    "ds_read_b64_tr_b16 a[188:189], v35 offset:32768\n"
    "ds_read_b64_tr_b16 a[216:217], v36 offset:32768\n"
    "ds_read_b64_tr_b16 a[220:221], v37 offset:32768\n"
    "ds_read_b64_tr_b16 a[248:249], v38 offset:32768\n"
    "ds_read_b64_tr_b16 a[252:253], v39 offset:32768\n"
    "s_waitcnt vmcnt(28) lgkmcnt(0)\n"
    "s_barrier\n"
    "s_add_u32 m0, s54, 32768\n"
    "s_add_u32 s55, %6, 262144\n"
    "buffer_load_dwordx4 v48, s[44:47], s55 offen lds\n"
    "s_add_u32 m0, s54, 36864\n"
    "s_add_u32 s55, %6, 1310720\n"
    "buffer_load_dwordx4 v48, s[44:47], s55 offen lds\n"
    "s_add_u32 m0, s54, 40960\n"
    "s_add_u32 s55, %6, 2359296\n"
    "buffer_load_dwordx4 v48, s[44:47], s55 offen lds\n"
    "s_add_u32 m0, s54, 45056\n"
    "s_add_u32 s55, %6, 3407872\n"
    "buffer_load_dwordx4 v48, s[44:47], s55 offen lds\n"
    "ds_read_b64_tr_b16 a[154:155], v32 offset:49152\n"
    "ds_read_b64_tr_b16 a[158:159], v33 offset:49152\n"
    "ds_read_b64_tr_b16 a[186:187], v34 offset:49152\n"
    "ds_read_b64_tr_b16 a[190:191], v35 offset:49152\n"
    "ds_read_b64_tr_b16 a[218:219], v36 offset:49152\n"
    "ds_read_b64_tr_b16 a[222:223], v37 offset:49152\n"
    "ds_read_b64_tr_b16 a[250:251], v38 offset:49152\n"
    "ds_read_b64_tr_b16 a[254:255], v39 offset:49152\n"
    "s_waitcnt vmcnt(28) lgkmcnt(0)\n"
    "s_barrier\n"
    "s_add_u32 m0, s54, 49152\n"
    "s_add_u32 s55, %6, 294912\n"
    "buffer_load_dwordx4 v48, s[44:47], s55 offen lds\n"
    "s_add_u32 m0, s54, 53248\n"
    "s_add_u32 s55, %6, 1343488\n"
    "buffer_load_dwordx4 v48, s[44:47], s55 offen lds\n"
    "s_add_u32 m0, s54, 57344\n"
    "s_add_u32 s55, %6, 2392064\n"
    "buffer_load_dwordx4 v48, s[44:47], s55 offen lds\n"
    "s_add_u32 m0, s54, 61440\n"
    "s_add_u32 s55, %6, 3440640\n"
    "buffer_load_dwordx4 v48, s[44:47], s55 offen lds\n"
    "v_mfma_f32_16x16x32_bf16 v[16:19], v[76:79], a[152:155], v[16:19]\n"
    "v_mfma_f32_16x16x32_bf16 v[20:23], v[76:79], a[156:159], v[20:23]\n"
    "v_mfma_f32_16x16x32_bf16 v[16:19], v[76:79], a[184:187], v[16:19]\n"
    "v_mfma_f32_16x16x32_bf16 v[20:23], v[76:79], a[188:191], v[20:23]\n"
    "v_mfma_f32_16x16x32_bf16 v[16:19], v[76:79], a[216:219], v[16:19]\n"
    "v_mfma_f32_16x16x32_bf16 v[20:23], v[76:79], a[220:223], v[20:23]\n"
    "v_mfma_f32_16x16x32_bf16 v[16:19], v[76:79], a[248:251], v[16:19]\n"
    "v_mfma_f32_16x16x32_bf16 v[20:23], v[76:79], a[252:255], v[20:23]\n"
    "ds_read_b64_tr_b16 a[160:161], v71\n"
    "ds_read_b64_tr_b16 a[164:165], v70\n"
    "ds_read_b64_tr_b16 a[192:193], v69\n"
    "ds_read_b64_tr_b16 a[196:197], v68\n"
    "ds_read_b64_tr_b16 a[224:225], v67\n"
    "ds_read_b64_tr_b16 a[228:229], v66\n"
    "ds_read_b64_tr_b16 v[84:85], v65\n"
    "ds_read_b64_tr_b16 v[88:89], v64\n"
    "s_waitcnt vmcnt(28) lgkmcnt(0)\n"
    "s_barrier\n"
    "s_add_u32 m0, s54, 65536\n"
    "s_add_u32 s55, %6, 524288\n"
    "buffer_load_dwordx4 v48, s[44:47], s55 offen lds\n"
    "s_add_u32 m0, s54, 69632\n"
    "s_add_u32 s55, %6, 1572864\n"
    "buffer_load_dwordx4 v48, s[44:47], s55 offen lds\n"
    "s_add_u32 m0, s54, 73728\n"
    "s_add_u32 s55, %6, 2621440\n"
    "buffer_load_dwordx4 v48, s[44:47], s55 offen lds\n"
    "s_add_u32 m0, s54, 77824\n"
    "s_add_u32 s55, %6, 3670016\n"
    "buffer_load_dwordx4 v48, s[44:47], s55 offen lds\n"
    "ds_read_b64_tr_b16 a[162:163], v71 offset:16384\n"
    "ds_read_b64_tr_b16 a[166:167], v70 offset:16384\n"
    "ds_read_b64_tr_b16 a[194:195], v69 offset:16384\n"
    "ds_read_b64_tr_b16 a[198:199], v68 offset:16384\n"
    "ds_read_b64_tr_b16 a[226:227], v67 offset:16384\n"
    "ds_read_b64_tr_b16 a[230:231], v66 offset:16384\n"
    "ds_read_b64_tr_b16 v[86:87], v65 offset:16384\n"
    "ds_read_b64_tr_b16 v[90:91], v64 offset:16384\n"
    "s_waitcnt vmcnt(28) lgkmcnt(0)\n"
    "s_barrier\n"
    "s_add_u32 m0, s54, 81920\n"
    "s_add_u32 s55, %6, 557056\n"
    "buffer_load_dwordx4 v48, s[44:47], s55 offen lds\n"
    "s_add_u32 m0, s54, 86016\n"
    "s_add_u32 s55, %6, 1605632\n"
    "buffer_load_dwordx4 v48, s[44:47], s55 offen lds\n"
    "s_add_u32 m0, s54, 90112\n"
    "s_add_u32 s55, %6, 2654208\n"
    "buffer_load_dwordx4 v48, s[44:47], s55 offen lds\n"
    "s_add_u32 m0, s54, 94208\n"
    "s_add_u32 s55, %6, 3702784\n"
    "buffer_load_dwordx4 v48, s[44:47], s55 offen lds\n"
    "v_mfma_f32_16x16x32_bf16 v[16:19], v[76:79], a[160:163], v[16:19]\n"
    "v_mfma_f32_16x16x32_bf16 v[20:23], v[76:79], a[164:167], v[20:23]\n"
    "v_mfma_f32_16x16x32_bf16 v[16:19], v[76:79], a[192:195], v[16:19]\n"
    "v_mfma_f32_16x16x32_bf16 v[20:23], v[76:79], a[196:199], v[20:23]\n"
    "v_mfma_f32_16x16x32_bf16 v[16:19], v[76:79], a[224:227], v[16:19]\n"
    "v_mfma_f32_16x16x32_bf16 v[20:23], v[76:79], a[228:231], v[20:23]\n"
    "v_mfma_f32_16x16x32_bf16 v[16:19], v[76:79], v[84:87], v[16:19]\n"
    "v_mfma_f32_16x16x32_bf16 v[20:23], v[76:79], v[88:91], v[20:23]\n"
    "ds_read_b64_tr_b16 a[168:169], v71 offset:32768\n"
    "ds_read_b64_tr_b16 a[172:173], v70 offset:32768\n"
    "ds_read_b64_tr_b16 a[200:201], v69 offset:32768\n"
    "ds_read_b64_tr_b16 a[204:205], v68 offset:32768\n"
    "ds_read_b64_tr_b16 a[232:233], v67 offset:32768\n"
    "ds_read_b64_tr_b16 a[236:237], v66 offset:32768\n"
    "ds_read_b64_tr_b16 v[92:93], v65 offset:32768\n"
    "ds_read_b64_tr_b16 v[96:97], v64 offset:32768\n"
    "s_waitcnt vmcnt(28) lgkmcnt(0)\n"
    "s_barrier\n"
    "s_add_u32 m0, s54, 98304\n"
    "s_add_u32 s55, %6, 786432\n"
    "buffer_load_dwordx4 v48, s[44:47], s55 offen lds\n"
    "s_add_u32 m0, s54, 102400\n"
    "s_add_u32 s55, %6, 1835008\n"
    "buffer_load_dwordx4 v48, s[44:47], s55 offen lds\n"
    "s_add_u32 m0, s54, 106496\n"
    "s_add_u32 s55, %6, 2883584\n"
    "buffer_load_dwordx4 v48, s[44:47], s55 offen lds\n"
    "s_add_u32 m0, s54, 110592\n"
    "s_add_u32 s55, %6, 3932160\n"
    "buffer_load_dwordx4 v48, s[44:47], s55 offen lds\n"
    "ds_read_b64_tr_b16 a[170:171], v71 offset:49152\n"
    "ds_read_b64_tr_b16 a[174:175], v70 offset:49152\n"
    "ds_read_b64_tr_b16 a[202:203], v69 offset:49152\n"
    "ds_read_b64_tr_b16 a[206:207], v68 offset:49152\n"
    "ds_read_b64_tr_b16 a[234:235], v67 offset:49152\n"
    "ds_read_b64_tr_b16 a[238:239], v66 offset:49152\n"
    "ds_read_b64_tr_b16 v[94:95], v65 offset:49152\n"
    "ds_read_b64_tr_b16 v[98:99], v64 offset:49152\n"
    "s_waitcnt vmcnt(28) lgkmcnt(0)\n"
    "s_barrier\n"
    "s_add_u32 m0, s54, 114688\n"
    "s_add_u32 s55, %6, 819200\n"
    "buffer_load_dwordx4 v48, s[44:47], s55 offen lds\n"
    "s_add_u32 m0, s54, 118784\n"
    "s_add_u32 s55, %6, 1867776\n"
    "buffer_load_dwordx4 v48, s[44:47], s55 offen lds\n"
    "s_add_u32 m0, s54, 122880\n"
    "s_add_u32 s55, %6, 2916352\n"
    "buffer_load_dwordx4 v48, s[44:47], s55 offen lds\n"
    "s_add_u32 m0, s54, 126976\n"
    "s_add_u32 s55, %6, 3964928\n"
    "buffer_load_dwordx4 v48, s[44:47], s55 offen lds\n"
    "v_mfma_f32_16x16x32_bf16 v[16:19], v[76:79], a[168:171], v[16:19]\n"
    "v_mfma_f32_16x16x32_bf16 v[20:23], v[76:79], a[172:175], v[20:23]\n"
    "v_mfma_f32_16x16x32_bf16 v[16:19], v[76:79], a[200:203], v[16:19]\n"
    "v_mfma_f32_16x16x32_bf16 v[20:23], v[76:79], a[204:207], v[20:23]\n"
    "v_mfma_f32_16x16x32_bf16 v[16:19], v[76:79], a[232:235], v[16:19]\n"
    "v_mfma_f32_16x16x32_bf16 v[20:23], v[76:79], a[236:239], v[20:23]\n"
    "v_mfma_f32_16x16x32_bf16 v[16:19], v[76:79], v[92:95], v[16:19]\n"
    "v_mfma_f32_16x16x32_bf16 v[20:23], v[76:79], v[96:99], v[20:23]\n"
    "ds_read_b64_tr_b16 v[100:101], v63\n"
    "ds_read_b64_tr_b16 v[104:105], v62\n"
    "ds_read_b64_tr_b16 v[102:103], v63 offset:4096\n"
    "ds_read_b64_tr_b16 v[106:107], v62 offset:4096\n"
    "ds_read_b64_tr_b16 v[108:109], v63 offset:8192\n"
    "ds_read_b64_tr_b16 v[112:113], v62 offset:8192\n"
    "ds_read_b64_tr_b16 v[110:111], v63 offset:12288\n"
    "ds_read_b64_tr_b16 v[114:115], v62 offset:12288\n"
    "s_waitcnt vmcnt(28) lgkmcnt(0)\n"
    "s_barrier\n"
    "s_add_u32 m0, s54, 131072\n"
    "s_add_u32 s55, %6, 4194304\n"
    "buffer_load_dwordx4 v48, s[44:47], s55 offen lds\n"
    "s_add_u32 m0, s54, 135168\n"
    "s_add_u32 s55, %6, 4227072\n"
    "buffer_load_dwordx4 v48, s[44:47], s55 offen lds\n"
    "s_add_u32 m0, s54, 139264\n"
    "s_add_u32 s55, %6, 4456448\n"
    "buffer_load_dwordx4 v48, s[44:47], s55 offen lds\n"
    "s_add_u32 m0, s54, 143360\n"
    "s_add_u32 s55, %6, 4489216\n"
    "buffer_load_dwordx4 v48, s[44:47], s55 offen lds\n"
    "v_mov_b32 v7, 0x20400\n"
    "global_load_dwordx2 v[80:81], v7, %1 sc1\n"
    "v_mbcnt_lo_u32_b32 v4, -1, 0\n"
    "v_min_u32 v4, 7, v4\n"
    "v_lshlrev_b32 v4, 7, v4\n"
    "v_add_u32 v4, 0x20000, v4\n"
    "global_load_dwordx2 v[82:83], v4, %1 sc1\n"
    "v_mfma_f32_16x16x32_bf16 v[16:19], v[76:79], v[100:103], v[16:19]\n"
    "v_mfma_f32_16x16x32_bf16 v[20:23], v[76:79], v[104:107], v[20:23]\n"
    "v_mfma_f32_16x16x32_bf16 v[16:19], v[76:79], v[108:111], v[16:19]\n"
    "v_mfma_f32_16x16x32_bf16 v[20:23], v[76:79], v[112:115], v[20:23]\n"
    "ds_read_b64_tr_b16 v[116:117], v32\n"
    "ds_read_b64_tr_b16 v[120:121], v33\n"
    "ds_read_b64_tr_b16 v[148:149], v34\n"
    "ds_read_b64_tr_b16 v[152:153], v35\n"
    "ds_read_b64_tr_b16 v[180:181], v36\n"
    "ds_read_b64_tr_b16 v[184:185], v37\n"
    "ds_read_b64_tr_b16 v[212:213], v38\n"
    "ds_read_b64_tr_b16 v[216:217], v39\n"
    "s_waitcnt vmcnt(30) lgkmcnt(0)\n"
    "s_barrier\n"
    "ds_read_b64_tr_b16 v[118:119], v32 offset:16384\n"
    "ds_read_b64_tr_b16 v[122:123], v33 offset:16384\n"
    "ds_read_b64_tr_b16 v[150:151], v34 offset:16384\n"
    "ds_read_b64_tr_b16 v[154:155], v35 offset:16384\n"
    "ds_read_b64_tr_b16 v[182:183], v36 offset:16384\n"
    "ds_read_b64_tr_b16 v[186:187], v37 offset:16384\n"
    "ds_read_b64_tr_b16 v[214:215], v38 offset:16384\n"
    "ds_read_b64_tr_b16 v[218:219], v39 offset:16384\n"
    "s_waitcnt vmcnt(26) lgkmcnt(0)\n"
    "s_barrier\n"
    "v_mfma_f32_16x16x32_bf16 v[24:27], v[76:79], v[116:119], 0\n"
    "v_mfma_f32_16x16x32_bf16 v[28:31], v[76:79], v[120:123], 0\n"
    "v_mfma_f32_16x16x32_bf16 v[24:27], v[76:79], v[148:151], v[24:27]\n"
    "v_mfma_f32_16x16x32_bf16 v[28:31], v[76:79], v[152:155], v[28:31]\n"
    "v_mfma_f32_16x16x32_bf16 v[24:27], v[76:79], v[180:183], v[24:27]\n"
    "v_mfma_f32_16x16x32_bf16 v[28:31], v[76:79], v[184:187], v[28:31]\n"
    "v_mfma_f32_16x16x32_bf16 v[24:27], v[76:79], v[212:215], v[24:27]\n"
    "v_mfma_f32_16x16x32_bf16 v[28:31], v[76:79], v[216:219], v[28:31]\n"
    "ds_read_b64_tr_b16 v[124:125], v32 offset:32768\n"
    "ds_read_b64_tr_b16 v[128:129], v33 offset:32768\n"
    "ds_read_b64_tr_b16 v[156:157], v34 offset:32768\n"
    "ds_read_b64_tr_b16 v[160:161], v35 offset:32768\n"
    "ds_read_b64_tr_b16 v[188:189], v36 offset:32768\n"
    "ds_read_b64_tr_b16 v[192:193], v37 offset:32768\n"
    "ds_read_b64_tr_b16 v[220:221], v38 offset:32768\n"
    "ds_read_b64_tr_b16 v[224:225], v39 offset:32768\n"
    "s_waitcnt vmcnt(22) lgkmcnt(0)\n"
    "s_barrier\n"
    "ds_read_b64_tr_b16 v[126:127], v32 offset:49152\n"
    "ds_read_b64_tr_b16 v[130:131], v33 offset:49152\n"
    "ds_read_b64_tr_b16 v[158:159], v34 offset:49152\n"
    "ds_read_b64_tr_b16 v[162:163], v35 offset:49152\n"
    "ds_read_b64_tr_b16 v[190:191], v36 offset:49152\n"
    "ds_read_b64_tr_b16 v[194:195], v37 offset:49152\n"
    "ds_read_b64_tr_b16 v[222:223], v38 offset:49152\n"
    "ds_read_b64_tr_b16 v[226:227], v39 offset:49152\n"
    "s_waitcnt vmcnt(18) lgkmcnt(0)\n"
    "s_barrier\n"
    "v_mfma_f32_16x16x32_bf16 v[24:27], v[76:79], v[124:127], v[24:27]\n"
    "v_mfma_f32_16x16x32_bf16 v[28:31], v[76:79], v[128:131], v[28:31]\n"
    "v_mfma_f32_16x16x32_bf16 v[24:27], v[76:79], v[156:159], v[24:27]\n"
    "v_mfma_f32_16x16x32_bf16 v[28:31], v[76:79], v[160:163], v[28:31]\n"
    "v_mfma_f32_16x16x32_bf16 v[24:27], v[76:79], v[188:191], v[24:27]\n"
    "v_mfma_f32_16x16x32_bf16 v[28:31], v[76:79], v[192:195], v[28:31]\n"
    "v_mfma_f32_16x16x32_bf16 v[24:27], v[76:79], v[220:223], v[24:27]\n"
    "v_mfma_f32_16x16x32_bf16 v[28:31], v[76:79], v[224:227], v[28:31]\n"
    "ds_read_b64_tr_b16 v[132:133], v71\n"
    "ds_read_b64_tr_b16 v[136:137], v70\n"
    "ds_read_b64_tr_b16 v[164:165], v69\n"
    "ds_read_b64_tr_b16 v[168:169], v68\n"
    "ds_read_b64_tr_b16 v[196:197], v67\n"
    "ds_read_b64_tr_b16 v[200:201], v66\n"
    "ds_read_b64_tr_b16 v[228:229], v65\n"
    "ds_read_b64_tr_b16 v[232:233], v64\n"
    "s_waitcnt vmcnt(14) lgkmcnt(0)\n"
    "s_barrier\n"
    "ds_read_b64_tr_b16 v[134:135], v71 offset:16384\n"
    "ds_read_b64_tr_b16 v[138:139], v70 offset:16384\n"
    "ds_read_b64_tr_b16 v[166:167], v69 offset:16384\n"
    "ds_read_b64_tr_b16 v[170:171], v68 offset:16384\n"
    "ds_read_b64_tr_b16 v[198:199], v67 offset:16384\n"
    "ds_read_b64_tr_b16 v[202:203], v66 offset:16384\n"
    "ds_read_b64_tr_b16 v[230:231], v65 offset:16384\n"
    "ds_read_b64_tr_b16 v[234:235], v64 offset:16384\n"
    "s_waitcnt vmcnt(10) lgkmcnt(0)\n"
    "s_barrier\n"
    "v_mfma_f32_16x16x32_bf16 v[24:27], v[76:79], v[132:135], v[24:27]\n"
    "v_mfma_f32_16x16x32_bf16 v[28:31], v[76:79], v[136:139], v[28:31]\n"
    "v_mfma_f32_16x16x32_bf16 v[24:27], v[76:79], v[164:167], v[24:27]\n"
    "v_mfma_f32_16x16x32_bf16 v[28:31], v[76:79], v[168:171], v[28:31]\n"
    "v_mfma_f32_16x16x32_bf16 v[24:27], v[76:79], v[196:199], v[24:27]\n"
    "v_mfma_f32_16x16x32_bf16 v[28:31], v[76:79], v[200:203], v[28:31]\n"
    "v_mfma_f32_16x16x32_bf16 v[24:27], v[76:79], v[228:231], v[24:27]\n"
    "v_mfma_f32_16x16x32_bf16 v[28:31], v[76:79], v[232:235], v[28:31]\n"
    "ds_read_b64_tr_b16 v[140:141], v71 offset:32768\n"
    "ds_read_b64_tr_b16 v[144:145], v70 offset:32768\n"
    "ds_read_b64_tr_b16 v[172:173], v69 offset:32768\n"
    "ds_read_b64_tr_b16 v[176:177], v68 offset:32768\n"
    "ds_read_b64_tr_b16 v[204:205], v67 offset:32768\n"
    "ds_read_b64_tr_b16 v[208:209], v66 offset:32768\n"
    "ds_read_b64_tr_b16 v[236:237], v65 offset:32768\n"
    "ds_read_b64_tr_b16 v[240:241], v64 offset:32768\n"
    "s_waitcnt vmcnt(6) lgkmcnt(0)\n"
    "s_barrier\n"
    "ds_read_b64_tr_b16 v[142:143], v71 offset:49152\n"
    "ds_read_b64_tr_b16 v[146:147], v70 offset:49152\n"
    "ds_read_b64_tr_b16 v[174:175], v69 offset:49152\n"
    "ds_read_b64_tr_b16 v[178:179], v68 offset:49152\n"
    "ds_read_b64_tr_b16 v[206:207], v67 offset:49152\n"
    "ds_read_b64_tr_b16 v[210:211], v66 offset:49152\n"
    "ds_read_b64_tr_b16 v[238:239], v65 offset:49152\n"
    "ds_read_b64_tr_b16 v[242:243], v64 offset:49152\n"
    "s_waitcnt vmcnt(2) lgkmcnt(0)\n"
    "s_barrier\n"
    "v_mfma_f32_16x16x32_bf16 v[24:27], v[76:79], v[140:143], v[24:27]\n"
    "v_mfma_f32_16x16x32_bf16 v[28:31], v[76:79], v[144:147], v[28:31]\n"
    "v_mfma_f32_16x16x32_bf16 v[24:27], v[76:79], v[172:175], v[24:27]\n"
    "v_mfma_f32_16x16x32_bf16 v[28:31], v[76:79], v[176:179], v[28:31]\n"
    "v_mfma_f32_16x16x32_bf16 v[24:27], v[76:79], v[204:207], v[24:27]\n"
    "v_mfma_f32_16x16x32_bf16 v[28:31], v[76:79], v[208:211], v[28:31]\n"
    "v_mfma_f32_16x16x32_bf16 v[24:27], v[76:79], v[236:239], v[24:27]\n"
    "v_mfma_f32_16x16x32_bf16 v[28:31], v[76:79], v[240:243], v[28:31]\n"
    "ds_read_b64_tr_b16 v[244:245], v63\n"
    "ds_read_b64_tr_b16 v[248:249], v62\n"
    "ds_read_b64_tr_b16 v[246:247], v63 offset:4096\n"
    "ds_read_b64_tr_b16 v[250:251], v62 offset:4096\n"
    "ds_read_b64_tr_b16 v[252:253], v63 offset:8192\n"
    "ds_read_b64_tr_b16 v[72:73], v62 offset:8192\n"
    "ds_read_b64_tr_b16 v[254:255], v63 offset:12288\n"
    "ds_read_b64_tr_b16 v[74:75], v62 offset:12288\n"
    "s_waitcnt lgkmcnt(0)\n"
    "s_barrier\n"
    "v_mfma_f32_16x16x32_bf16 v[24:27], v[76:79], v[244:247], v[24:27]\n"
    "v_mfma_f32_16x16x32_bf16 v[28:31], v[76:79], v[248:251], v[28:31]\n"
    "v_mfma_f32_16x16x32_bf16 v[24:27], v[76:79], v[252:255], v[24:27]\n"
    "v_mfma_f32_16x16x32_bf16 v[28:31], v[76:79], v[72:75], v[28:31]\n"
    "ds_write_b128 v2, v[72:75] offset:20480\n"
    "v_cvt_pk_bf16_f32 v9, 0, v8\n"
    "v_sub_f32 v8, v8, v9\n"
    "v_cvt_pk_bf16_f32 v10, 0, v8\n"
    "v_sub_f32 v8, v8, v10\n"
    "v_cvt_pk_bf16_f32 v9, v9, v10\n"
    "v_cvt_pk_bf16_f32 v10, v8, 0\n"
    "s_mov_b64 vcc, 0xffff\n"
    "v_cndmask_b32 v32, 0, v9, vcc\n"
    "v_cndmask_b32 v33, 0, v10, vcc\n"
    "v_cvt_pk_bf16_f32 v13, 0, v12\n"
    "v_sub_f32 v12, v12, v13\n"
    "v_cvt_pk_bf16_f32 v14, 0, v12\n"
    "v_sub_f32 v12, v12, v14\n"
    "v_cvt_pk_bf16_f32 v13, v13, v14\n"
    "v_cvt_pk_bf16_f32 v14, v12, 0\n"
    "s_mov_b64 vcc, 0xffff\n"
    "v_cndmask_b32 v34, 0, v13, vcc\n"
    "v_cndmask_b32 v35, 0, v14, vcc\n"
    "ds_write_b128 v2, v[32:35] offset:8192\n"
    "v_cvt_pk_bf16_f32 v17, 0, v16\n"
    "v_sub_f32 v16, v16, v17\n"
    "v_cvt_pk_bf16_f32 v18, 0, v16\n"
    "v_sub_f32 v16, v16, v18\n"
    "v_cvt_pk_bf16_f32 v17, v17, v18\n"
    "v_cvt_pk_bf16_f32 v18, v16, 0\n"
    "s_mov_b64 vcc, 0xffff\n"
    "v_cndmask_b32 v36, 0, v17, vcc\n"
    "v_cndmask_b32 v37, 0, v18, vcc\n"
    "v_cvt_pk_bf16_f32 v21, 0, v20\n"
    "v_sub_f32 v20, v20, v21\n"
    "v_cvt_pk_bf16_f32 v22, 0, v20\n"
    "v_sub_f32 v20, v20, v22\n"
    "v_cvt_pk_bf16_f32 v21, v21, v22\n"
    "v_cvt_pk_bf16_f32 v22, v20, 0\n"
    "s_mov_b64 vcc, 0xffff\n"
    "v_cndmask_b32 v38, 0, v21, vcc\n"
    "v_cndmask_b32 v39, 0, v22, vcc\n"
    "ds_write_b128 v2, v[36:39] offset:12288\n"
    "v_cvt_pk_bf16_f32 v25, 0, v24\n"
    "v_sub_f32 v24, v24, v25\n"
    "v_cvt_pk_bf16_f32 v26, 0, v24\n"
    "v_sub_f32 v24, v24, v26\n"
    "v_cvt_pk_bf16_f32 v25, v25, v26\n"
    "v_cvt_pk_bf16_f32 v26, v24, 0\n"
    "s_mov_b64 vcc, 0xffff\n"
    "v_cndmask_b32 v40, 0, v25, vcc\n"
    "v_cndmask_b32 v41, 0, v26, vcc\n"
    "v_cvt_pk_bf16_f32 v29, 0, v28\n"
    "v_sub_f32 v28, v28, v29\n"
    "v_cvt_pk_bf16_f32 v30, 0, v28\n"
    "v_sub_f32 v28, v28, v30\n"
    "v_cvt_pk_bf16_f32 v29, v29, v30\n"
    "v_cvt_pk_bf16_f32 v30, v28, 0\n"
    "s_mov_b64 vcc, 0xffff\n"
    "v_cndmask_b32 v42, 0, v29, vcc\n"
    "v_cndmask_b32 v43, 0, v30, vcc\n"
    "ds_write_b128 v2, v[40:43] offset:16384\n"
    "s_waitcnt vmcnt(0)\n"
    "v_add_u32 v80, 1, v80\n"
    "v_add_u32 v81, 1, v81\n"
    "s_mov_b32 s52, 1048576\n"
    ".Lhgo_a%=:\n"
    "v_cmp_ne_u64 vcc, v[80:81], v[82:83]\n"
    "s_cbranch_vccz .Lhgo_x%=\n"
    "global_load_dwordx2 v[82:83], v4, %1 sc1\n"
    "s_sub_u32 s52, s52, 1\n"
    "s_waitcnt vmcnt(0)\n"
    "s_cbranch_scc0 .Lhgo_a%=\n"
    ".Lhgo_x%=:\n"
    "v_mbcnt_lo_u32_b32 v80, -1, 0\n"
    "v_mbcnt_hi_u32_b32 v80, -1, v80\n"
    "v_min_u32 v4, 5, v80\n"
    "v_lshlrev_b32 v4, 7, v4\n"
    "v_mov_b32 v7, 0x26c00\n"
    "ds_read_b128 v[80:83], v7\n"
    "s_waitcnt lgkmcnt(0)\n"
    "v_readfirstlane_b32 s49, v81\n"
    "v_add_u32 v80, v80, v4\n"
    "s_branch .Lhchk_%=\n"
    ".Lhgo_%=:\n"
    "s_waitcnt vmcnt(2)\n"
    "v_mfma_f32_16x16x32_bf16 v[68:71], v[48:51], v[48:51], 0\n"
    "v_add_u32 v7, 32, v7\n"
    "ds_read_b128 v[80:83], v7\n"
    "v_mfma_f32_16x16x32_bf16 v[68:71], v[52:55], v[52:55], v[68:71]\n"
    "ds_write_b128 v1, v[48:51]\n"
    "ds_write_b128 v1, v[52:55] offset:1024\n"
    "v_mfma_f32_16x16x32_bf16 v[68:71], v[56:59], v[56:59], v[68:71]\n"
    "ds_write_b128 v1, v[56:59] offset:2048\n"
    "ds_write_b128 v1, v[60:63] offset:3072\n"
    "v_mfma_f32_16x16x32_bf16 v[68:71], v[60:63], v[60:63], v[68:71]\n"
    "v_mul_f32 v24, v24, v76\n"
    "v_mul_f32 v25, v25, v77\n"
    "v_mfma_f32_16x16x32_bf16 v[68:71], v[64:67], v[64:67], v[68:71]\n"
    "v_mul_f32 v26, v26, v78\n"
    "v_mul_f32 v27, v27, v79\n"
    "v_mfma_f32_16x16x32_bf16 v[8:11], a[0:3], v[48:51], 0\n"
    "v_mul_f32 v28, v28, v76\n"
    "v_mul_f32 v29, v29, v77\n"
    "v_mfma_f32_16x16x32_bf16 v[12:15], a[4:7], v[48:51], 0\n"
    "v_mul_f32 v30, v30, v78\n"
    "v_mul_f32 v31, v31, v79\n"
    "v_mfma_f32_16x16x32_bf16 v[16:19], a[144:147], v[48:51], 0\n"
    "v_mov_b64 v[76:77], s[86:87]\n"
    "v_mov_b64 v[78:79], s[86:87]\n"
    "v_mfma_f32_16x16x32_bf16 v[20:23], a[148:151], v[48:51], 0\n"
    "ds_write_b128 v1, v[64:67] offset:4096\n"
    "v_cvt_pk_bf16_f32 v24, v24, v25\n"
    "v_mfma_f32_16x16x32_bf16 v[72:75], v[76:79], v[48:51], 0\n"
    "v_cvt_pk_bf16_f32 v25, v26, v27\n"
    "v_cvt_pk_bf16_f32 v26, v28, v29\n"
    "v_mfma_f32_16x16x32_bf16 v[72:75], v[76:79], v[52:55], v[72:75]\n"
    "v_cvt_pk_bf16_f32 v27, v30, v31\n"
    "buffer_store_dwordx4 v[24:27], v2, s[40:43], s64 offen sc1\n"
    "v_mfma_f32_16x16x32_bf16 v[72:75], v[76:79], v[56:59], v[72:75]\n"
    "ds_write_b128 v1, v[68:71] offset:5120\n"
    "s_waitcnt lgkmcnt(6)\n"
    "v_readfirstlane_b32 s49, v81\n"
    "v_mfma_f32_16x16x32_bf16 v[72:75], v[76:79], v[60:63], v[72:75]\n"
    "v_readfirstlane_b32 s62, v82\n"
    "v_add_u32 v80, v80, v4\n"
    "v_mfma_f32_16x16x32_bf16 v[72:75], v[76:79], v[64:67], v[72:75]\n"
    ".Lhtop_%=:\n"
    "v_mfma_f32_16x16x32_bf16 v[24:27], v[48:51], v[116:119], 0\n"
    "global_load_dword v81, v80, %1 sc1\n"
    "s_add_u32 s63, s62, 0x2000000\n"
    "v_mfma_f32_16x16x32_bf16 v[28:31], v[48:51], v[120:123], 0\n"
    "s_add_u32 s64, s62, 0x4000000\n"
    "v_mfma_f32_16x16x32_bf16 v[8:11], a[8:11], v[52:55], v[8:11]\n"
    "ds_write_b128 v1, v[72:75] offset:6144\n"
    "v_xor_b32 v0, 0x2000, v1\n"
    "v_mfma_f32_16x16x32_bf16 v[12:15], a[12:15], v[52:55], v[12:15]\n"
    "v_mfma_f32_16x16x32_bf16 v[16:19], a[152:155], v[52:55], v[16:19]\n"
    "v_mfma_f32_16x16x32_bf16 v[20:23], a[156:159], v[52:55], v[20:23]\n"
    "v_mfma_f32_16x16x32_bf16 v[24:27], v[52:55], v[124:127], v[24:27]\n"
    "s_waitcnt lgkmcnt(0)\n"
    "s_barrier\n"
    "v_mfma_f32_16x16x32_bf16 v[28:31], v[52:55], v[128:131], v[28:31]\n"
    "ds_read_b128 v[32:35], v0\n"
    "ds_read_b128 v[36:39], v0 offset:1024\n"
    "v_mfma_f32_16x16x32_bf16 v[8:11], a[16:19], v[56:59], v[8:11]\n"
    "ds_read2st64_b32 v[68:69], v6 offset0:20 offset1:52\n"
    "ds_read2st64_b32 v[70:71], v6 offset0:84 offset1:116\n"
    "v_mfma_f32_16x16x32_bf16 v[12:15], a[20:23], v[56:59], v[12:15]\n"
    "ds_read2st64_b32 v[74:75], v6 offset0:88 offset1:120\n"
    "ds_read2st64_b32 v[72:73], v6 offset0:24 offset1:56\n"
    "v_mfma_f32_16x16x32_bf16 v[16:19], a[160:163], v[56:59], v[16:19]\n"
    "v_mov_b64 v[64:65], 0\n"
    "v_mfma_f32_16x16x32_bf16 v[20:23], a[164:167], v[56:59], v[20:23]\n"
    "v_mfma_f32_16x16x32_bf16 v[24:27], v[56:59], v[132:135], v[24:27]\n"
    "v_mfma_f32_16x16x32_bf16 v[28:31], v[56:59], v[136:139], v[28:31]\n"
    "ds_read_b128 v[48:51], v2 offset:8192\n"
    "ds_read_b128 v[52:55], v2 offset:12288\n"
    "v_mfma_f32_16x16x32_bf16 v[8:11], a[24:27], v[60:63], v[8:11]\n"
    "ds_read_b128 v[56:59], v2 offset:16384\n"
    "ds_read_b128 v[44:47], v0 offset:3072\n"
    "v_mfma_f32_16x16x32_bf16 v[12:15], a[28:31], v[60:63], v[12:15]\n"
    "ds_read_b128 v[40:43], v0 offset:2048\n"
    "v_mfma_f32_16x16x32_bf16 v[16:19], a[168:171], v[60:63], v[16:19]\n"
    "s_waitcnt lgkmcnt(5)\n"
    "v_add_f32 v72, v72, v73\n"
    "v_add_f32 v74, v74, v75\n"
    "v_mfma_f32_16x16x32_bf16 v[20:23], a[172:175], v[60:63], v[20:23]\n"
    "v_add_f32 v68, v68, v69\n"
    "v_add_f32 v70, v70, v71\n"
    "v_mfma_f32_16x16x32_bf16 v[24:27], v[60:63], v[140:143], v[24:27]\n"
    "v_add_f32 v72, v72, v74\n"
    "v_add_f32 v70, v68, v70\n"
    "v_mfma_f32_16x16x32_bf16 v[28:31], v[60:63], v[144:147], v[28:31]\n"
    "v_mul_f32 v72, 0xbae38e39, v72\n"
    "v_mul_f32 v70, 0x3ae38e39, v70\n"
    "v_mfma_f32_16x16x32_bf16 v[8:11], a[32:35], v[32:35], v[8:11]\n"
    "v_cvt_pk_bf16_f32 v62, v72, v72\n"
    "v_fma_f32 v70, -v72, v72, v70\n"
    "v_mfma_f32_16x16x32_bf16 v[12:15], a[36:39], v[32:35], v[12:15]\n"
    "v_cvt_pk_bf16_f32 v63, v72, v72\n"
    "v_max_f32 v70, 0, v70\n"
    "v_mfma_f32_16x16x32_bf16 v[16:19], a[176:179], v[32:35], v[16:19]\n"
    "v_cvt_pk_bf16_f32 v70, 0, v70\n"
    "v_add_f32 v70, 0x3727c5ac, v70\n"
    "v_mfma_f32_16x16x32_bf16 v[20:23], a[180:183], v[32:35], v[20:23]\n"
    "v_cvt_pk_bf16_f32 v70, 0, v70\n"
    "v_sqrt_f32 v70, v70\n"
    "v_mfma_f32_16x16x32_bf16 v[24:27], v[32:35], v[148:151], v[24:27]\n"
    "v_cvt_pk_bf16_f32 v70, 0, v70\n"
    "v_mfma_f32_16x16x32_bf16 v[28:31], v[32:35], v[152:155], v[28:31]\n"
    "v_rcp_f32 v70, v70\n"
    "v_mfma_f32_16x16x32_bf16 v[8:11], a[40:43], v[36:39], v[8:11]\n"
    "v_mov_b64 v[60:61], 0\n"
    "v_mfma_f32_16x16x32_bf16 v[12:15], a[44:47], v[36:39], v[12:15]\n"
    "ds_write_b32 v5, v70\n"
    "v_lshrrev_b32 v71, 4, v2\n"
    "v_mfma_f32_16x16x32_bf16 v[16:19], a[184:187], v[36:39], v[16:19]\n"
    "v_and_b32 v71, 48, v71\n"
    "ds_read_b128 v[76:79], v71 offset:24576\n"
    "v_mfma_f32_16x16x32_bf16 v[20:23], a[188:191], v[36:39], v[20:23]\n"
    "ds_read_b128 v[72:75], v2 offset:20480\n"
    "v_mfma_f32_16x16x32_bf16 v[24:27], v[36:39], v[156:159], v[24:27]\n"
    "v_xor_b32 v0, 0x4000, v1\n"
    "v_mfma_f32_16x16x32_bf16 v[28:31], v[36:39], v[160:163], v[28:31]\n"
    "s_waitcnt lgkmcnt(3)\n"
    "v_mfma_f32_16x16x32_bf16 v[8:11], a[48:51], v[40:43], v[8:11]\n"
    "ds_read_b128 v[36:39], v0 offset:1024\n"
    "ds_read_b128 v[32:35], v0\n"
    "v_mfma_f32_16x16x32_bf16 v[12:15], a[52:55], v[40:43], v[12:15]\n"
    "v_mfma_f32_16x16x32_bf16 v[16:19], a[192:195], v[40:43], v[16:19]\n"
    "v_mfma_f32_16x16x32_bf16 v[20:23], a[196:199], v[40:43], v[20:23]\n"
    "v_mfma_f32_16x16x32_bf16 v[24:27], v[40:43], v[164:167], v[24:27]\n"
    "v_mfma_f32_16x16x32_bf16 v[28:31], v[40:43], v[168:171], v[28:31]\n"
    "v_mfma_f32_16x16x32_bf16 v[8:11], v[48:51], v[62:65], v[8:11]\n"
    "v_mfma_f32_16x16x32_bf16 v[12:15], v[48:51], v[60:63], v[12:15]\n"
    "v_mfma_f32_16x16x32_bf16 v[16:19], v[52:55], v[62:65], v[16:19]\n"
    "s_waitcnt vmcnt(0)\n"
    "v_mov_b32 v82, 1\n"
    "v_mfma_f32_16x16x32_bf16 v[20:23], v[52:55], v[60:63], v[20:23]\n"
    "s_mov_b64 exec, s[56:57]\n"
    "ds_add_rtn_u32 v82, v7, v82 offset:16\n"
    "s_mov_b64 exec, -1\n"
    "v_mfma_f32_16x16x32_bf16 v[24:27], v[62:65], v[56:59], v[24:27]\n"
    "v_cmp_eq_u32 vcc, 0, v81\n"
    "s_cmp_eq_u64 vcc, 0\n"
    "v_mfma_f32_16x16x32_bf16 v[28:31], v[60:63], v[56:59], v[28:31]\n"
    "s_cselect_b32 s94, s42, 0\n"
    "s_cselect_b32 s90, s96, 0\n"
    "v_mfma_f32_16x16x32_bf16 v[8:11], a[56:59], v[44:47], v[8:11]\n"
    "global_load_dword v81, v80, %1 sc1\n"
    "v_mfma_f32_16x16x32_bf16 v[12:15], a[60:63], v[44:47], v[12:15]\n"
    "buffer_load_dwordx4 v[48:51], v3, s[92:95], s49 offen sc1\n"
    "v_mfma_f32_16x16x32_bf16 v[16:19], a[200:203], v[44:47], v[16:19]\n"
    "buffer_load_dwordx4 v[52:55], v3, s[92:95], s49 offen offset:1024 sc1\n"
    "v_mfma_f32_16x16x32_bf16 v[20:23], a[204:207], v[44:47], v[20:23]\n"
    "s_waitcnt lgkmcnt(0)\n"
    "v_readfirstlane_b32 s50, v82\n"
    "v_mfma_f32_16x16x32_bf16 v[24:27], v[44:47], v[172:175], v[24:27]\n"
    "buffer_load_dwordx4 v[56:59], v3, s[92:95], s49 offen offset:2048 sc1\n"
    "s_cmp_eq_u32 s50, 3\n"
    "v_mfma_f32_16x16x32_bf16 v[28:31], v[44:47], v[176:179], v[28:31]\n"
    "v_mfma_f32_16x16x32_bf16 v[8:11], a[64:67], v[32:35], v[8:11]\n"
    "ds_read_b128 v[44:47], v0 offset:3072\n"
    "ds_read_b128 v[40:43], v0 offset:2048\n"
    "v_mfma_f32_16x16x32_bf16 v[12:15], a[68:71], v[32:35], v[12:15]\n"
    "s_cselect_b64 exec, 1, 0\n"
    "global_store_dword v83, v82, %1 sc1\n"
    "s_mov_b64 exec, -1\n"
    "v_mfma_f32_16x16x32_bf16 v[16:19], a[208:211], v[32:35], v[16:19]\n"
    "buffer_load_dwordx4 v[60:63], v3, s[92:95], s49 offen offset:3072 sc1\n"
    "v_mfma_f32_16x16x32_bf16 v[20:23], a[212:215], v[32:35], v[20:23]\n"
    "buffer_load_dwordx4 v[64:67], v2, s[88:91], s49 offen sc1\n"
    "v_mfma_f32_16x16x32_bf16 v[24:27], v[32:35], v[180:183], v[24:27]\n"
    "v_mfma_f32_16x16x32_bf16 v[28:31], v[32:35], v[184:187], v[28:31]\n"
    "v_mfma_f32_16x16x32_bf16 v[8:11], a[72:75], v[36:39], v[8:11]\n"
    "v_mfma_f32_16x16x32_bf16 v[12:15], a[76:79], v[36:39], v[12:15]\n"
    "v_mfma_f32_16x16x32_bf16 v[16:19], a[216:219], v[36:39], v[16:19]\n"
    "v_mfma_f32_16x16x32_bf16 v[20:23], a[220:223], v[36:39], v[20:23]\n"
    "v_mfma_f32_16x16x32_bf16 v[24:27], v[36:39], v[188:191], v[24:27]\n"
    "v_xor_b32 v0, 0x6000, v1\n"
    "v_mfma_f32_16x16x32_bf16 v[28:31], v[36:39], v[192:195], v[28:31]\n"
    "s_waitcnt lgkmcnt(0)\n"
    "v_mfma_f32_16x16x32_bf16 v[8:11], a[80:83], v[40:43], v[8:11]\n"
    "ds_read_b128 v[36:39], v0 offset:1024\n"
    "ds_read_b128 v[32:35], v0\n"
    "v_mfma_f32_16x16x32_bf16 v[12:15], a[84:87], v[40:43], v[12:15]\n"
    "v_mfma_f32_16x16x32_bf16 v[16:19], a[224:227], v[40:43], v[16:19]\n"
    "v_mfma_f32_16x16x32_bf16 v[20:23], a[228:231], v[40:43], v[20:23]\n"
    "v_mfma_f32_16x16x32_bf16 v[24:27], v[40:43], v[196:199], v[24:27]\n"
    "v_mfma_f32_16x16x32_bf16 v[28:31], v[40:43], v[200:203], v[28:31]\n"
    "v_mfma_f32_16x16x32_bf16 v[8:11], a[88:91], v[44:47], v[8:11]\n"
    "v_mfma_f32_16x16x32_bf16 v[12:15], a[92:95], v[44:47], v[12:15]\n"
    "v_mfma_f32_16x16x32_bf16 v[16:19], a[232:235], v[44:47], v[16:19]\n"
    "v_mfma_f32_16x16x32_bf16 v[20:23], a[236:239], v[44:47], v[20:23]\n"
    "v_mfma_f32_16x16x32_bf16 v[24:27], v[44:47], v[204:207], v[24:27]\n"
    "v_mfma_f32_16x16x32_bf16 v[28:31], v[44:47], v[208:211], v[28:31]\n"
    "s_waitcnt lgkmcnt(0)\n"
    "v_mfma_f32_16x16x32_bf16 v[8:11], a[96:99], v[32:35], v[8:11]\n"
    "ds_read_b128 v[44:47], v0 offset:3072\n"
    "ds_read_b128 v[40:43], v0 offset:2048\n"
    "v_mfma_f32_16x16x32_bf16 v[12:15], a[100:103], v[32:35], v[12:15]\n"
    "v_mfma_f32_16x16x32_bf16 v[16:19], a[240:243], v[32:35], v[16:19]\n"
    "s_cmp_eq_u32 s94, 0\n"
    "s_cbranch_scc1 .Lhk1_%=\n"
    ".Lhk1_x%=:\n"
    "v_mfma_f32_16x16x32_bf16 v[20:23], a[244:247], v[32:35], v[20:23]\n"
    "v_mfma_f32_16x16x32_bf16 v[24:27], v[32:35], v[212:215], v[24:27]\n"
    "v_and_b32 v0, 0xffff9fff, v1\n"
    "v_mfma_f32_16x16x32_bf16 v[28:31], v[32:35], v[216:219], v[28:31]\n"
    "ds_read_b128 v[32:35], v0 offset:4096\n"
    "v_mfma_f32_16x16x32_bf16 v[8:11], a[104:107], v[36:39], v[8:11]\n"
    "v_mfma_f32_16x16x32_bf16 v[12:15], a[108:111], v[36:39], v[12:15]\n"
    "v_mfma_f32_16x16x32_bf16 v[16:19], a[248:251], v[36:39], v[16:19]\n"
    "v_mfma_f32_16x16x32_bf16 v[20:23], a[252:255], v[36:39], v[20:23]\n"
    "v_mfma_f32_16x16x32_bf16 v[24:27], v[36:39], v[220:223], v[24:27]\n"
    "v_mfma_f32_16x16x32_bf16 v[28:31], v[36:39], v[224:227], v[28:31]\n"
    "ds_read_b128 v[36:39], v0 offset:12288\n"
    "s_waitcnt lgkmcnt(2)\n"
    "v_mfma_f32_16x16x32_bf16 v[8:11], a[112:115], v[40:43], v[8:11]\n"
    "v_mfma_f32_16x16x32_bf16 v[12:15], a[116:119], v[40:43], v[12:15]\n"
    "v_xor_b32 v1, 0x8000, v1\n"
    "v_xor_b32 v6, 0x8000, v6\n"
    "v_mfma_f32_16x16x32_bf16 v[8:11], a[120:123], v[44:47], v[8:11]\n"
    "v_mfma_f32_16x16x32_bf16 v[12:15], a[124:127], v[44:47], v[12:15]\n"
    "s_waitcnt lgkmcnt(1)\n"
    "v_mfma_f32_16x16x32_bf16 v[8:11], a[128:131], v[32:35], v[8:11]\n"
    "v_mfma_f32_16x16x32_bf16 v[12:15], a[132:135], v[32:35], v[12:15]\n"
    "s_waitcnt lgkmcnt(0)\n"
    "v_mfma_f32_16x16x32_bf16 v[8:11], a[136:139], v[36:39], v[8:11]\n"
    "v_mfma_f32_16x16x32_bf16 v[12:15], a[140:143], v[36:39], v[12:15]\n"
    "v_mfma_f32_16x16x32_bf16 v[16:19], v[84:87], v[40:43], v[16:19]\n"
    "v_mfma_f32_16x16x32_bf16 v[20:23], v[88:91], v[40:43], v[20:23]\n"
    "v_mul_f32 v8, v8, v70\n"
    "v_mul_f32 v12, v12, v70\n"
    "v_mfma_f32_16x16x32_bf16 v[16:19], v[92:95], v[44:47], v[16:19]\n"
    "v_cvt_pk_bf16_f32 v8, v8, v12\n"
    "v_mul_f32 v9, v9, v70\n"
    "v_mfma_f32_16x16x32_bf16 v[20:23], v[96:99], v[44:47], v[20:23]\n"
    "v_mul_f32 v13, v13, v70\n"
    "v_cvt_pk_bf16_f32 v9, v9, v13\n"
    "v_mfma_f32_16x16x32_bf16 v[16:19], v[100:103], v[32:35], v[16:19]\n"
    "v_mul_f32 v10, v10, v70\n"
    "v_mul_f32 v14, v14, v70\n"
    "v_mfma_f32_16x16x32_bf16 v[20:23], v[104:107], v[32:35], v[20:23]\n"
    "v_cvt_pk_bf16_f32 v10, v10, v14\n"
    "v_mul_f32 v11, v11, v70\n"
    "v_mfma_f32_16x16x32_bf16 v[16:19], v[108:111], v[36:39], v[16:19]\n"
    "v_mul_f32 v15, v15, v70\n"
    "v_cvt_pk_bf16_f32 v11, v11, v15\n"
    "v_mfma_f32_16x16x32_bf16 v[20:23], v[112:115], v[36:39], v[20:23]\n"
    "buffer_store_dwordx4 v[8:11], v2, s[40:43], s62 offen sc1\n"
    "v_mfma_f32_16x16x32_bf16 v[24:27], v[40:43], v[228:231], v[24:27]\n"
    "v_mfma_f32_16x16x32_bf16 v[28:31], v[40:43], v[232:235], v[28:31]\n"
    "v_mul_f32 v16, v16, v70\n"
    "v_mul_f32 v20, v20, v70\n"
    "v_mfma_f32_16x16x32_bf16 v[24:27], v[44:47], v[236:239], v[24:27]\n"
    "v_cvt_pk_bf16_f32 v16, v16, v20\n"
    "v_mul_f32 v17, v17, v70\n"
    "v_mfma_f32_16x16x32_bf16 v[28:31], v[44:47], v[240:243], v[28:31]\n"
    "v_mul_f32 v21, v21, v70\n"
    "v_cvt_pk_bf16_f32 v17, v17, v21\n"
    "v_mfma_f32_16x16x32_bf16 v[24:27], v[32:35], v[244:247], v[24:27]\n"
    "v_mul_f32 v18, v18, v70\n"
    "v_mul_f32 v22, v22, v70\n"
    "v_mfma_f32_16x16x32_bf16 v[28:31], v[32:35], v[248:251], v[28:31]\n"
    "v_cvt_pk_bf16_f32 v18, v18, v22\n"
    "v_mul_f32 v19, v19, v70\n"
    "v_mfma_f32_16x16x32_bf16 v[24:27], v[36:39], v[252:255], v[24:27]\n"
    "v_mul_f32 v23, v23, v70\n"
    "v_cvt_pk_bf16_f32 v19, v19, v23\n"
    "v_mfma_f32_16x16x32_bf16 v[28:31], v[36:39], v[72:75], v[28:31]\n"
    "buffer_store_dwordx4 v[16:19], v2, s[40:43], s63 offen sc1\n"
    "s_mov_b32 s56, 1\n"
    "s_cmp_lg_u32 s94, 0\n"
    "s_cbranch_scc1 .Lhgo_%=\n"
    "s_nop 0\n"
    "v_mul_f32 v24, v24, v76\n"
    "v_mul_f32 v25, v25, v77\n"
    "v_cvt_pk_bf16_f32 v24, v24, v25\n"
    "v_mul_f32 v26, v26, v78\n"
    "v_mul_f32 v27, v27, v79\n"
    "v_cvt_pk_bf16_f32 v25, v26, v27\n"
    "v_mul_f32 v28, v28, v76\n"
    "v_mul_f32 v29, v29, v77\n"
    "v_cvt_pk_bf16_f32 v26, v28, v29\n"
    "v_mul_f32 v30, v30, v78\n"
    "v_mul_f32 v31, v31, v79\n"
    "v_cvt_pk_bf16_f32 v27, v30, v31\n"
    "buffer_store_dwordx4 v[24:27], v2, s[40:43], s64 offen sc1\n"
    "s_waitcnt vmcnt(3)\n"
    "v_cmp_eq_u32 vcc, 0, v81\n"
    "s_cmp_eq_u64 vcc, 0\n"
    "s_cbranch_scc1 .Lhld_%=\n"
    "ds_read_b32 v83, v7 offset:44\n"
    "global_load_dword v81, v80, %1 sc1\n"
    "s_waitcnt vmcnt(0)\n"
    "v_mov_b32 v82, 1\n"
    "s_mov_b64 exec, 1\n"
    "ds_add_rtn_u32 v82, v7, v82 offset:48\n"
    "s_mov_b64 exec, -1\n"
    "s_waitcnt lgkmcnt(0)\n"
    "v_readfirstlane_b32 s50, v82\n"
    "s_cmp_eq_u32 s50, 3\n"
    "s_cselect_b64 exec, 1, 0\n"
    "global_store_dword v83, v82, %1 sc1\n"
    "s_mov_b64 exec, -1\n"
    "s_mov_b32 s56, 0\n"
    "v_cmp_eq_u32 vcc, 0, v81\n"
    "s_cmp_eq_u64 vcc, 0\n"
    "s_cbranch_scc1 .Lhld_%=\n"
    ".Lhchk_%=:\n"
    "v_readfirstlane_b32 s50, v80\n"
    "s_cmp_lt_u32 s50, 200\n"
    "s_cbranch_scc1 .Lhend_%=\n"
    ".Lhwait_%=:\n"
    "global_load_dword v81, v80, %1 sc1\n"
    "s_mov_b32 s52, 0\n"
    ".Lhp2_%=:\n"
    "s_waitcnt vmcnt(0)\n"
    "v_cmp_eq_u32 vcc, 0, v81\n"
    "s_cmp_eq_u64 vcc, 0\n"
    "s_cbranch_scc1 .Lhp2_x%=\n"
    "s_add_u32 s52, s52, 1\n"
    "s_sleep 2\n"
    "global_load_dword v81, v80, %1 sc1\n"
    "s_cmp_lt_u32 s52, s53\n"
    "s_cbranch_scc1 .Lhp2_%=\n"
    "s_mov_b32 s53, 0\n"
    "v_mov_b32 v82, 0\n"
    "v_mov_b32 v83, 1\n"
    "global_store_dword v82, v83, %1 sc1\n"
    "s_waitcnt vmcnt(0)\n"
    ".Lhp2_x%=:\n"
    ".Lhld_%=:\n"
    "s_mov_b32 s94, s42\n"
    "s_mov_b32 s90, s96\n"
    "buffer_load_dwordx4 v[48:51], v3, s[92:95], s49 offen sc1\n"
    "buffer_load_dwordx4 v[52:55], v3, s[92:95], s49 offen offset:1024 sc1\n"
    "buffer_load_dwordx4 v[56:59], v3, s[92:95], s49 offen offset:2048 sc1\n"
    "buffer_load_dwordx4 v[60:63], v3, s[92:95], s49 offen offset:3072 sc1\n"
    "buffer_load_dwordx4 v[64:67], v2, s[88:91], s49 offen sc1\n"
    ".Lhin_%=:\n"
    "v_add_u32 v7, 32, v7\n"
    "ds_read_b128 v[80:83], v7\n"
    "s_waitcnt vmcnt(4)\n"
    "v_mfma_f32_16x16x32_bf16 v[68:71], v[48:51], v[48:51], 0\n"
    "s_waitcnt vmcnt(3)\n"
    "v_mfma_f32_16x16x32_bf16 v[68:71], v[52:55], v[52:55], v[68:71]\n"
    "ds_write_b128 v1, v[48:51]\n"
    "ds_write_b128 v1, v[52:55] offset:1024\n"
    "s_waitcnt vmcnt(2)\n"
    "v_mfma_f32_16x16x32_bf16 v[68:71], v[56:59], v[56:59], v[68:71]\n"
    "ds_write_b128 v1, v[56:59] offset:2048\n"
    "s_waitcnt vmcnt(1)\n"
    "ds_write_b128 v1, v[60:63] offset:3072\n"
    "v_mfma_f32_16x16x32_bf16 v[68:71], v[60:63], v[60:63], v[68:71]\n"
    "s_waitcnt vmcnt(0)\n"
    "v_mfma_f32_16x16x32_bf16 v[68:71], v[64:67], v[64:67], v[68:71]\n"
    "v_mfma_f32_16x16x32_bf16 v[8:11], a[0:3], v[48:51], 0\n"
    "v_mfma_f32_16x16x32_bf16 v[12:15], a[4:7], v[48:51], 0\n"
    "v_mfma_f32_16x16x32_bf16 v[16:19], a[144:147], v[48:51], 0\n"
    "v_mov_b64 v[76:77], s[86:87]\n"
    "v_mov_b64 v[78:79], s[86:87]\n"
    "v_mfma_f32_16x16x32_bf16 v[20:23], a[148:151], v[48:51], 0\n"
    "ds_write_b128 v1, v[64:67] offset:4096\n"
    "v_mfma_f32_16x16x32_bf16 v[72:75], v[76:79], v[48:51], 0\n"
    "v_mfma_f32_16x16x32_bf16 v[72:75], v[76:79], v[52:55], v[72:75]\n"
    "v_mfma_f32_16x16x32_bf16 v[72:75], v[76:79], v[56:59], v[72:75]\n"
    "ds_write_b128 v1, v[68:71] offset:5120\n"
    "s_waitcnt lgkmcnt(6)\n"
    "v_readfirstlane_b32 s49, v81\n"
    "v_mfma_f32_16x16x32_bf16 v[72:75], v[76:79], v[60:63], v[72:75]\n"
    "v_readfirstlane_b32 s62, v82\n"
    "v_add_u32 v80, v80, v4\n"
    "v_mfma_f32_16x16x32_bf16 v[72:75], v[76:79], v[64:67], v[72:75]\n"
    "s_cmp_eq_u32 s56, 0\n"
    "s_cbranch_scc1 .Lhc3_%=\n"
    "s_waitcnt vmcnt(0)\n"
    "v_mov_b32 v82, 1\n"
    "s_mov_b64 exec, 1\n"
    "ds_add_rtn_u32 v82, v7, v82 offset:16\n"
    "s_mov_b64 exec, -1\n"
    "s_waitcnt lgkmcnt(0)\n"
    "v_readfirstlane_b32 s50, v82\n"
    "s_cmp_eq_u32 s50, 3\n"
    "s_cselect_b64 exec, 1, 0\n"
    "global_store_dword v83, v82, %1 sc1\n"
    "s_mov_b64 exec, -1\n"
    "s_mov_b32 s56, 0\n"
    ".Lhc3_%=:\n"
    "s_branch .Lhtop_%=\n"
    ".Lhk1_%=:\n"
    "s_waitcnt vmcnt(5)\n"
    "v_cmp_eq_u32 vcc, 0, v81\n"
    "s_cmp_eq_u64 vcc, 0\n"
    "s_cselect_b32 s94, s42, 0\n"
    "s_cselect_b32 s90, s96, 0\n"
    "global_load_dword v81, v80, %1 sc1\n"
    "buffer_load_dwordx4 v[48:51], v3, s[92:95], s49 offen sc1\n"
    "buffer_load_dwordx4 v[52:55], v3, s[92:95], s49 offen offset:1024 sc1\n"
    "buffer_load_dwordx4 v[56:59], v3, s[92:95], s49 offen offset:2048 sc1\n"
    "buffer_load_dwordx4 v[60:63], v3, s[92:95], s49 offen offset:3072 sc1\n"
    "buffer_load_dwordx4 v[64:67], v2, s[88:91], s49 offen sc1\n"
    "s_branch .Lhk1_x%=\n"
    ".Lhpol_%=:\n"
    "v_mov_b32 v80, 64\n"
    "global_load_dword v81, v80, %1 sc1\n"
    "s_mov_b32 s52, 0\n"
    ".Lhp4_%=:\n"
    "s_waitcnt vmcnt(0)\n"
    "v_cmp_eq_u32 vcc, 0, v81\n"
    "s_cmp_eq_u64 vcc, 0\n"
    "s_cbranch_scc1 .Lhp4_x%=\n"
    "s_add_u32 s52, s52, 1\n"
    "s_sleep 2\n"
    "global_load_dword v81, v80, %1 sc1\n"
    "s_cmp_lt_u32 s52, s53\n"
    "s_cbranch_scc1 .Lhp4_%=\n"
    "s_mov_b32 s53, 0\n"
    "v_mov_b32 v82, 0\n"
    "v_mov_b32 v83, 1\n"
    "global_store_dword v82, v83, %1 sc1\n"
    "s_waitcnt vmcnt(0)\n"
    ".Lhp4_x%=:\n"
    "v_readfirstlane_b32 s50, v81\n"
    "v_mbcnt_lo_u32_b32 v80, -1, 0\n"
    "v_mbcnt_hi_u32_b32 v80, -1, v80\n"
    "s_lshl_b32 s51, %2, 6\n"
    "v_add_u32 v80, s51, v80\n"
    "v_min_u32 v80, 129, v80\n"
    "v_lshlrev_b32 v80, 5, v80\n"
    "v_add_u32 v80, 0x26c00, v80\n"
    "ds_read2_b32 v[82:83], v80 offset0:1 offset1:2\n"
    "s_and_b32 s50, s50, 1\n"
    "s_waitcnt lgkmcnt(0)\n"
    "v_and_b32 v81, s50, v82\n"
    "v_lshlrev_b32 v81, 13, v81\n"
    "v_and_b32 v82, -2, v82\n"
    "v_xor_b32 v82, v81, v82\n"
    "v_and_b32 v81, s50, v83\n"
    "v_lshlrev_b32 v81, 13, v81\n"
    "v_and_b32 v83, -2, v83\n"
    "v_xor_b32 v83, v81, v83\n"
    "ds_write2_b32 v80, v82, v83 offset0:1 offset1:2\n"
    "s_waitcnt lgkmcnt(0)\n"
    "s_barrier\n"
    "ds_read_b32 v81, v7 offset:4\n"
    "v_add_u32 v80, %4, v4\n"
    "s_waitcnt lgkmcnt(0)\n"
    "v_readfirstlane_b32 s49, v81\n"
    "s_branch .Lhwait_%=\n"
    ".Lhend_%=:\n"
    "s_cmp_eq_u32 s50, 192\n"
    "s_cbranch_scc0 .Lhpol_%=\n"
    "s_waitcnt vmcnt(0) lgkmcnt(0)\n"
    "s_endpgm\n"
    ".p2align 6\n"
    ".Lrole2_%=:\n"
    "s_mov_b64 s[40:41], %0\n"
    "s_and_b32 s41, s41, 0xffff\n"
    "s_mov_b32 s42, 0xbc00000\n"
    "s_mov_b32 s43, 0x20000\n"
    "s_mov_b64 s[92:93], %0\n"
    "s_and_b32 s93, s93, 0xffff\n"
    "s_mov_b32 s94, 0xbc00000\n"
    "s_mov_b32 s95, 0x20000\n"
    "s_mov_b32 s53, 131072\n"
    "s_mov_b64 s[56:57], 0\n"
    "v_mbcnt_lo_u32_b32 v7, -1, 0\n"
    "v_mbcnt_hi_u32_b32 v7, -1, v7\n"
    "s_lshl_b32 s50, %2, 8\n"
    "s_add_u32 s50, s50, 147456\n"
    "v_lshl_add_u32 v7, v7, 2, s50\n"
    "ds_read_b32 v1, v7\n"
    "ds_read_b32 v4, v7 offset:1024\n"
    "ds_read_b32 v2, v7 offset:2048\n"
    "ds_read_b32 v8, v7 offset:3072\n"
    "ds_read_b32 v9, v7 offset:4096\n"
    "ds_read_b32 v10, v7 offset:5120\n"
    "ds_read_b32 v11, v7 offset:6144\n"
    "ds_read_b32 v3, v7 offset:7168\n"
    "s_waitcnt lgkmcnt(0)\n"
    "s_barrier\n"
    "s_mul_i32 s54, %2, 6144\n"
    "v_add_u32 v5, 16384, v4\n"
    "s_xor_b32 s50, %2, 0\n"
    "s_mul_i32 s50, s50, 6144\n"
    "v_add_u32 v32, s50, v9\n"
    "v_add_u32 v33, s50, v10\n"
    "v_add_u32 v34, s50, v11\n"
    "s_xor_b32 s50, %2, 1\n"
    "s_mul_i32 s50, s50, 6144\n"
    "v_add_u32 v35, s50, v9\n"
    "v_add_u32 v36, s50, v10\n"
    "v_add_u32 v37, s50, v11\n"
    "s_xor_b32 s50, %2, 2\n"
    "s_mul_i32 s50, s50, 6144\n"
    "v_add_u32 v38, s50, v9\n"
    "v_add_u32 v39, s50, v10\n"
    "v_add_u32 v40, s50, v11\n"
    "s_xor_b32 s50, %2, 3\n"
    "s_mul_i32 s50, s50, 6144\n"
    "v_add_u32 v41, s50, v9\n"
    "v_add_u32 v42, s50, v10\n"
    "v_add_u32 v43, s50, v11\n"
    "s_mov_b64 s[44:45], %8\n"
    "s_and_b32 s45, s45, 0xffff\n"
    "s_mov_b32 s46, 0x480000\n"
    "s_mov_b32 s47, 0x20000\n"
    "s_add_u32 m0, s54, 0\n"
    "s_add_u32 s55, %6, 0\n"
    "buffer_load_dwordx4 v8, s[44:47], s55 offen lds\n"
    "s_add_u32 m0, s54, 1024\n"
    "s_add_u32 s55, %6, 18432\n"
    "buffer_load_dwordx4 v8, s[44:47], s55 offen lds\n"
    "s_add_u32 m0, s54, 2048\n"
    "s_add_u32 s55, %6, 128\n"
    "buffer_load_dwordx4 v8, s[44:47], s55 offen lds\n"
    "s_add_u32 m0, s54, 3072\n"
    "s_add_u32 s55, %6, 18560\n"
    "buffer_load_dwordx4 v8, s[44:47], s55 offen lds\n"
    "s_add_u32 m0, s54, 4096\n"
    "s_add_u32 s55, %6, 256\n"
    "buffer_load_dwordx4 v8, s[44:47], s55 offen lds\n"
    "s_add_u32 m0, s54, 5120\n"
    "s_add_u32 s55, %6, 18688\n"
    "buffer_load_dwordx4 v8, s[44:47], s55 offen lds\n"
    "s_add_u32 m0, s54, 24576\n"
    "s_add_u32 s55, %6, 2304\n"
    "buffer_load_dwordx4 v8, s[44:47], s55 offen lds\n"
    "s_add_u32 m0, s54, 25600\n"
    "s_add_u32 s55, %6, 20736\n"
    "buffer_load_dwordx4 v8, s[44:47], s55 offen lds\n"
    "s_add_u32 m0, s54, 26624\n"
    "s_add_u32 s55, %6, 2432\n"
    "buffer_load_dwordx4 v8, s[44:47], s55 offen lds\n"
    "s_add_u32 m0, s54, 27648\n"
    "s_add_u32 s55, %6, 20864\n"
    "buffer_load_dwordx4 v8, s[44:47], s55 offen lds\n"
    "s_add_u32 m0, s54, 28672\n"
    "s_add_u32 s55, %6, 2560\n"
    "buffer_load_dwordx4 v8, s[44:47], s55 offen lds\n"
    "s_add_u32 m0, s54, 29696\n"
    "s_add_u32 s55, %6, 20992\n"
    "buffer_load_dwordx4 v8, s[44:47], s55 offen lds\n"
    "s_add_u32 m0, s54, 49152\n"
    "s_add_u32 s55, %6, 36864\n"
    "buffer_load_dwordx4 v8, s[44:47], s55 offen lds\n"
    "s_add_u32 m0, s54, 50176\n"
    "s_add_u32 s55, %6, 55296\n"
    "buffer_load_dwordx4 v8, s[44:47], s55 offen lds\n"
    "s_add_u32 m0, s54, 51200\n"
    "s_add_u32 s55, %6, 36992\n"
    "buffer_load_dwordx4 v8, s[44:47], s55 offen lds\n"
    "s_add_u32 m0, s54, 52224\n"
    "s_add_u32 s55, %6, 55424\n"
    "buffer_load_dwordx4 v8, s[44:47], s55 offen lds\n"
    "s_add_u32 m0, s54, 53248\n"
    "s_add_u32 s55, %6, 37120\n"
    "buffer_load_dwordx4 v8, s[44:47], s55 offen lds\n"
    "s_add_u32 m0, s54, 54272\n"
    "s_add_u32 s55, %6, 55552\n"
    "buffer_load_dwordx4 v8, s[44:47], s55 offen lds\n"
    "s_add_u32 m0, s54, 73728\n"
    "s_add_u32 s55, %6, 39168\n"
    "buffer_load_dwordx4 v8, s[44:47], s55 offen lds\n"
    "s_add_u32 m0, s54, 74752\n"
    "s_add_u32 s55, %6, 57600\n"
    "buffer_load_dwordx4 v8, s[44:47], s55 offen lds\n"
    "s_add_u32 m0, s54, 75776\n"
    "s_add_u32 s55, %6, 39296\n"
    "buffer_load_dwordx4 v8, s[44:47], s55 offen lds\n"
    "s_add_u32 m0, s54, 76800\n"
    "s_add_u32 s55, %6, 57728\n"
    "buffer_load_dwordx4 v8, s[44:47], s55 offen lds\n"
    "s_add_u32 m0, s54, 77824\n"
    "s_add_u32 s55, %6, 39424\n"
    "buffer_load_dwordx4 v8, s[44:47], s55 offen lds\n"
    "s_add_u32 m0, s54, 78848\n"
    "s_add_u32 s55, %6, 57856\n"
    "buffer_load_dwordx4 v8, s[44:47], s55 offen lds\n"
    "s_add_u32 m0, s54, 98304\n"
    "s_add_u32 s55, %6, 73728\n"
    "buffer_load_dwordx4 v8, s[44:47], s55 offen lds\n"
    "s_add_u32 m0, s54, 99328\n"
    "s_add_u32 s55, %6, 92160\n"
    "buffer_load_dwordx4 v8, s[44:47], s55 offen lds\n"
    "s_add_u32 m0, s54, 100352\n"
    "s_add_u32 s55, %6, 73856\n"
    "buffer_load_dwordx4 v8, s[44:47], s55 offen lds\n"
    "s_add_u32 m0, s54, 101376\n"
    "s_add_u32 s55, %6, 92288\n"
    "buffer_load_dwordx4 v8, s[44:47], s55 offen lds\n"
    "s_add_u32 m0, s54, 102400\n"
    "s_add_u32 s55, %6, 73984\n"
    "buffer_load_dwordx4 v8, s[44:47], s55 offen lds\n"
    "s_add_u32 m0, s54, 103424\n"
    "s_add_u32 s55, %6, 92416\n"
    "buffer_load_dwordx4 v8, s[44:47], s55 offen lds\n"
    "s_add_u32 m0, s54, 122880\n"
    "s_add_u32 s55, %6, 76032\n"
    "buffer_load_dwordx4 v8, s[44:47], s55 offen lds\n"
    "s_add_u32 m0, s54, 123904\n"
    "s_add_u32 s55, %6, 94464\n"
    "buffer_load_dwordx4 v8, s[44:47], s55 offen lds\n"
    "s_add_u32 m0, s54, 124928\n"
    "s_add_u32 s55, %6, 76160\n"
    "buffer_load_dwordx4 v8, s[44:47], s55 offen lds\n"
    "s_add_u32 m0, s54, 125952\n"
    "s_add_u32 s55, %6, 94592\n"
    "buffer_load_dwordx4 v8, s[44:47], s55 offen lds\n"
    "s_add_u32 m0, s54, 126976\n"
    "s_add_u32 s55, %6, 76288\n"
    "buffer_load_dwordx4 v8, s[44:47], s55 offen lds\n"
    "s_add_u32 m0, s54, 128000\n"
    "s_add_u32 s55, %6, 94720\n"
    "buffer_load_dwordx4 v8, s[44:47], s55 offen lds\n"
    "s_waitcnt vmcnt(30) lgkmcnt(0)\n"
    "s_barrier\n"
    "ds_read_b64_tr_b16 a[0:1], v32\n"
    "ds_read_b64_tr_b16 a[4:5], v33\n"
    "ds_read_b64_tr_b16 a[8:9], v34\n"
    "ds_read_b64_tr_b16 a[96:97], v35\n"
    "ds_read_b64_tr_b16 a[100:101], v36\n"
    "ds_read_b64_tr_b16 a[104:105], v37\n"
    "ds_read_b64_tr_b16 a[192:193], v38\n"
    "ds_read_b64_tr_b16 a[196:197], v39\n"
    "ds_read_b64_tr_b16 a[200:201], v40\n"
    "ds_read_b64_tr_b16 v[160:161], v41\n"
    "ds_read_b64_tr_b16 v[164:165], v42\n"
    "ds_read_b64_tr_b16 v[168:169], v43\n"
    "s_waitcnt vmcnt(24) lgkmcnt(0)\n"
    "s_barrier\n"
    "s_add_u32 m0, s54, 0\n"
    "s_add_u32 s55, %6, 110592\n"
    "buffer_load_dwordx4 v8, s[44:47], s55 offen lds\n"
    "s_add_u32 m0, s54, 1024\n"
    "s_add_u32 s55, %6, 129024\n"
    "buffer_load_dwordx4 v8, s[44:47], s55 offen lds\n"
    "s_add_u32 m0, s54, 2048\n"
    "s_add_u32 s55, %6, 110720\n"
    "buffer_load_dwordx4 v8, s[44:47], s55 offen lds\n"
    "s_add_u32 m0, s54, 3072\n"
    "s_add_u32 s55, %6, 129152\n"
    "buffer_load_dwordx4 v8, s[44:47], s55 offen lds\n"
    "s_add_u32 m0, s54, 4096\n"
    "s_add_u32 s55, %6, 110848\n"
    "buffer_load_dwordx4 v8, s[44:47], s55 offen lds\n"
    "s_add_u32 m0, s54, 5120\n"
    "s_add_u32 s55, %6, 129280\n"
    "buffer_load_dwordx4 v8, s[44:47], s55 offen lds\n"
    "ds_read_b64_tr_b16 a[2:3], v32 offset:24576\n"
    "ds_read_b64_tr_b16 a[6:7], v33 offset:24576\n"
    "ds_read_b64_tr_b16 a[10:11], v34 offset:24576\n"
    "ds_read_b64_tr_b16 a[98:99], v35 offset:24576\n"
    "ds_read_b64_tr_b16 a[102:103], v36 offset:24576\n"
    "ds_read_b64_tr_b16 a[106:107], v37 offset:24576\n"
    "ds_read_b64_tr_b16 a[194:195], v38 offset:24576\n"
    "ds_read_b64_tr_b16 a[198:199], v39 offset:24576\n"
    "ds_read_b64_tr_b16 a[202:203], v40 offset:24576\n"
    "ds_read_b64_tr_b16 v[162:163], v41 offset:24576\n"
    "ds_read_b64_tr_b16 v[166:167], v42 offset:24576\n"
    "ds_read_b64_tr_b16 v[170:171], v43 offset:24576\n"
    "s_waitcnt vmcnt(24) lgkmcnt(0)\n"
    "s_barrier\n"
    "s_add_u32 m0, s54, 24576\n"
    "s_add_u32 s55, %6, 112896\n"
    "buffer_load_dwordx4 v8, s[44:47], s55 offen lds\n"
    "s_add_u32 m0, s54, 25600\n"
    "s_add_u32 s55, %6, 131328\n"
    "buffer_load_dwordx4 v8, s[44:47], s55 offen lds\n"
    "s_add_u32 m0, s54, 26624\n"
    "s_add_u32 s55, %6, 113024\n"
    "buffer_load_dwordx4 v8, s[44:47], s55 offen lds\n"
    "s_add_u32 m0, s54, 27648\n"
    "s_add_u32 s55, %6, 131456\n"
    "buffer_load_dwordx4 v8, s[44:47], s55 offen lds\n"
    "s_add_u32 m0, s54, 28672\n"
    "s_add_u32 s55, %6, 113152\n"
    "buffer_load_dwordx4 v8, s[44:47], s55 offen lds\n"
    "s_add_u32 m0, s54, 29696\n"
    "s_add_u32 s55, %6, 131584\n"
    "buffer_load_dwordx4 v8, s[44:47], s55 offen lds\n"
    "ds_read_b64_tr_b16 a[12:13], v32 offset:49152\n"
    "ds_read_b64_tr_b16 a[16:17], v33 offset:49152\n"
    "ds_read_b64_tr_b16 a[20:21], v34 offset:49152\n"
    "ds_read_b64_tr_b16 a[108:109], v35 offset:49152\n"
    "ds_read_b64_tr_b16 a[112:113], v36 offset:49152\n"
    "ds_read_b64_tr_b16 a[116:117], v37 offset:49152\n"
    "ds_read_b64_tr_b16 a[204:205], v38 offset:49152\n"
    "ds_read_b64_tr_b16 a[208:209], v39 offset:49152\n"
    "ds_read_b64_tr_b16 a[212:213], v40 offset:49152\n"
    "ds_read_b64_tr_b16 v[172:173], v41 offset:49152\n"
    "ds_read_b64_tr_b16 v[176:177], v42 offset:49152\n"
    "ds_read_b64_tr_b16 v[180:181], v43 offset:49152\n"
    "s_waitcnt vmcnt(24) lgkmcnt(0)\n"
    "s_barrier\n"
    "s_add_u32 m0, s54, 49152\n"
    "s_add_u32 s55, %6, 147456\n"
    "buffer_load_dwordx4 v8, s[44:47], s55 offen lds\n"
    "s_add_u32 m0, s54, 50176\n"
    "s_add_u32 s55, %6, 165888\n"
    "buffer_load_dwordx4 v8, s[44:47], s55 offen lds\n"
    "s_add_u32 m0, s54, 51200\n"
    "s_add_u32 s55, %6, 147584\n"
    "buffer_load_dwordx4 v8, s[44:47], s55 offen lds\n"
    "s_add_u32 m0, s54, 52224\n"
    "s_add_u32 s55, %6, 166016\n"
    "buffer_load_dwordx4 v8, s[44:47], s55 offen lds\n"
    "s_add_u32 m0, s54, 53248\n"
    "s_add_u32 s55, %6, 147712\n"
    "buffer_load_dwordx4 v8, s[44:47], s55 offen lds\n"
    "s_add_u32 m0, s54, 54272\n"
    "s_add_u32 s55, %6, 166144\n"
    "buffer_load_dwordx4 v8, s[44:47], s55 offen lds\n"
    "v_add_u32 v71, 0x10000, v32\n"
    "ds_read_b64_tr_b16 a[14:15], v71 offset:8192\n"
    "v_add_u32 v70, 0x10000, v33\n"
    "ds_read_b64_tr_b16 a[18:19], v70 offset:8192\n"
    "v_add_u32 v69, 0x10000, v34\n"
    "ds_read_b64_tr_b16 a[22:23], v69 offset:8192\n"
    "v_add_u32 v68, 0x10000, v35\n"
    "ds_read_b64_tr_b16 a[110:111], v68 offset:8192\n"
    "v_add_u32 v67, 0x10000, v36\n"
    "ds_read_b64_tr_b16 a[114:115], v67 offset:8192\n"
    "v_add_u32 v66, 0x10000, v37\n"
    "ds_read_b64_tr_b16 a[118:119], v66 offset:8192\n"
    "v_add_u32 v65, 0x10000, v38\n"
    "ds_read_b64_tr_b16 a[206:207], v65 offset:8192\n"
    "v_add_u32 v64, 0x10000, v39\n"
    "ds_read_b64_tr_b16 a[210:211], v64 offset:8192\n"
    "v_add_u32 v63, 0x10000, v40\n"
    "ds_read_b64_tr_b16 a[214:215], v63 offset:8192\n"
    "v_add_u32 v62, 0x10000, v41\n"
    "ds_read_b64_tr_b16 v[174:175], v62 offset:8192\n"
    "v_add_u32 v61, 0x10000, v42\n"
    "ds_read_b64_tr_b16 v[178:179], v61 offset:8192\n"
    "v_add_u32 v60, 0x10000, v43\n"
    "ds_read_b64_tr_b16 v[182:183], v60 offset:8192\n"
    "s_waitcnt vmcnt(24) lgkmcnt(0)\n"
    "s_barrier\n"
    "s_add_u32 m0, s54, 73728\n"
    "s_add_u32 s55, %6, 149760\n"
    "buffer_load_dwordx4 v8, s[44:47], s55 offen lds\n"
    "s_add_u32 m0, s54, 74752\n"
    "s_add_u32 s55, %6, 168192\n"
    "buffer_load_dwordx4 v8, s[44:47], s55 offen lds\n"
    "s_add_u32 m0, s54, 75776\n"
    "s_add_u32 s55, %6, 149888\n"
    "buffer_load_dwordx4 v8, s[44:47], s55 offen lds\n"
    "s_add_u32 m0, s54, 76800\n"
    "s_add_u32 s55, %6, 168320\n"
    "buffer_load_dwordx4 v8, s[44:47], s55 offen lds\n"
    "s_add_u32 m0, s54, 77824\n"
    "s_add_u32 s55, %6, 150016\n"
    "buffer_load_dwordx4 v8, s[44:47], s55 offen lds\n"
    "s_add_u32 m0, s54, 78848\n"
    "s_add_u32 s55, %6, 168448\n"
    "buffer_load_dwordx4 v8, s[44:47], s55 offen lds\n"
    "ds_read_b64_tr_b16 a[24:25], v71 offset:32768\n"
    "ds_read_b64_tr_b16 a[28:29], v70 offset:32768\n"
    "ds_read_b64_tr_b16 a[32:33], v69 offset:32768\n"
    "ds_read_b64_tr_b16 a[120:121], v68 offset:32768\n"
    "ds_read_b64_tr_b16 a[124:125], v67 offset:32768\n"
    "ds_read_b64_tr_b16 a[128:129], v66 offset:32768\n"
    "ds_read_b64_tr_b16 a[216:217], v65 offset:32768\n"
    "ds_read_b64_tr_b16 a[220:221], v64 offset:32768\n"
    "ds_read_b64_tr_b16 a[224:225], v63 offset:32768\n"
    "ds_read_b64_tr_b16 v[184:185], v62 offset:32768\n"
    "ds_read_b64_tr_b16 v[188:189], v61 offset:32768\n"
    "ds_read_b64_tr_b16 v[192:193], v60 offset:32768\n"
    "s_waitcnt vmcnt(24) lgkmcnt(0)\n"
    "s_barrier\n"
    "s_add_u32 m0, s54, 98304\n"
    "s_add_u32 s55, %6, 184320\n"
    "buffer_load_dwordx4 v8, s[44:47], s55 offen lds\n"
    "s_add_u32 m0, s54, 99328\n"
    "s_add_u32 s55, %6, 202752\n"
    "buffer_load_dwordx4 v8, s[44:47], s55 offen lds\n"
    "s_add_u32 m0, s54, 100352\n"
    "s_add_u32 s55, %6, 184448\n"
    "buffer_load_dwordx4 v8, s[44:47], s55 offen lds\n"
    "s_add_u32 m0, s54, 101376\n"
    "s_add_u32 s55, %6, 202880\n"
    "buffer_load_dwordx4 v8, s[44:47], s55 offen lds\n"
    "s_add_u32 m0, s54, 102400\n"
    "s_add_u32 s55, %6, 184576\n"
    "buffer_load_dwordx4 v8, s[44:47], s55 offen lds\n"
    "s_add_u32 m0, s54, 103424\n"
    "s_add_u32 s55, %6, 203008\n"
    "buffer_load_dwordx4 v8, s[44:47], s55 offen lds\n"
    "ds_read_b64_tr_b16 a[26:27], v71 offset:57344\n"
    "ds_read_b64_tr_b16 a[30:31], v70 offset:57344\n"
    "ds_read_b64_tr_b16 a[34:35], v69 offset:57344\n"
    "ds_read_b64_tr_b16 a[122:123], v68 offset:57344\n"
    "ds_read_b64_tr_b16 a[126:127], v67 offset:57344\n"
    "ds_read_b64_tr_b16 a[130:131], v66 offset:57344\n"
    "ds_read_b64_tr_b16 a[218:219], v65 offset:57344\n"
    "ds_read_b64_tr_b16 a[222:223], v64 offset:57344\n"
    "ds_read_b64_tr_b16 a[226:227], v63 offset:57344\n"
    "ds_read_b64_tr_b16 v[186:187], v62 offset:57344\n"
    "ds_read_b64_tr_b16 v[190:191], v61 offset:57344\n"
    "ds_read_b64_tr_b16 v[194:195], v60 offset:57344\n"
    "s_waitcnt vmcnt(24) lgkmcnt(0)\n"
    "s_barrier\n"
    "s_add_u32 m0, s54, 122880\n"
    "s_add_u32 s55, %6, 186624\n"
    "buffer_load_dwordx4 v8, s[44:47], s55 offen lds\n"
    "s_add_u32 m0, s54, 123904\n"
    "s_add_u32 s55, %6, 205056\n"
    "buffer_load_dwordx4 v8, s[44:47], s55 offen lds\n"
    "s_add_u32 m0, s54, 124928\n"
    "s_add_u32 s55, %6, 186752\n"
    "buffer_load_dwordx4 v8, s[44:47], s55 offen lds\n"
    "s_add_u32 m0, s54, 125952\n"
    "s_add_u32 s55, %6, 205184\n"
    "buffer_load_dwordx4 v8, s[44:47], s55 offen lds\n"
    "s_add_u32 m0, s54, 126976\n"
    "s_add_u32 s55, %6, 186880\n"
    "buffer_load_dwordx4 v8, s[44:47], s55 offen lds\n"
    "s_add_u32 m0, s54, 128000\n"
    "s_add_u32 s55, %6, 205312\n"
    "buffer_load_dwordx4 v8, s[44:47], s55 offen lds\n"
    "ds_read_b64_tr_b16 a[36:37], v32\n"
    "ds_read_b64_tr_b16 a[40:41], v33\n"
    "ds_read_b64_tr_b16 a[44:45], v34\n"
    "ds_read_b64_tr_b16 a[132:133], v35\n"
    "ds_read_b64_tr_b16 a[136:137], v36\n"
    "ds_read_b64_tr_b16 a[140:141], v37\n"
    "ds_read_b64_tr_b16 a[228:229], v38\n"
    "ds_read_b64_tr_b16 a[232:233], v39\n"
    "ds_read_b64_tr_b16 a[236:237], v40\n"
    "ds_read_b64_tr_b16 v[196:197], v41\n"
    "ds_read_b64_tr_b16 v[200:201], v42\n"
    "ds_read_b64_tr_b16 v[204:205], v43\n"
    "s_waitcnt vmcnt(24) lgkmcnt(0)\n"
    "s_barrier\n"
    "s_add_u32 m0, s54, 0\n"
    "s_add_u32 s55, %6, 221184\n"
    "buffer_load_dwordx4 v8, s[44:47], s55 offen lds\n"
    "s_add_u32 m0, s54, 1024\n"
    "s_add_u32 s55, %6, 239616\n"
    "buffer_load_dwordx4 v8, s[44:47], s55 offen lds\n"
    "s_add_u32 m0, s54, 2048\n"
    "s_add_u32 s55, %6, 221312\n"
    "buffer_load_dwordx4 v8, s[44:47], s55 offen lds\n"
    "s_add_u32 m0, s54, 3072\n"
    "s_add_u32 s55, %6, 239744\n"
    "buffer_load_dwordx4 v8, s[44:47], s55 offen lds\n"
    "s_add_u32 m0, s54, 4096\n"
    "s_add_u32 s55, %6, 221440\n"
    "buffer_load_dwordx4 v8, s[44:47], s55 offen lds\n"
    "s_add_u32 m0, s54, 5120\n"
    "s_add_u32 s55, %6, 239872\n"
    "buffer_load_dwordx4 v8, s[44:47], s55 offen lds\n"
    "ds_read_b64_tr_b16 a[38:39], v32 offset:24576\n"
    "ds_read_b64_tr_b16 a[42:43], v33 offset:24576\n"
    "ds_read_b64_tr_b16 a[46:47], v34 offset:24576\n"
    "ds_read_b64_tr_b16 a[134:135], v35 offset:24576\n"
    "ds_read_b64_tr_b16 a[138:139], v36 offset:24576\n"
    "ds_read_b64_tr_b16 a[142:143], v37 offset:24576\n"
    "ds_read_b64_tr_b16 a[230:231], v38 offset:24576\n"
    "ds_read_b64_tr_b16 a[234:235], v39 offset:24576\n"
    "ds_read_b64_tr_b16 a[238:239], v40 offset:24576\n"
    "ds_read_b64_tr_b16 v[198:199], v41 offset:24576\n"
    "ds_read_b64_tr_b16 v[202:203], v42 offset:24576\n"
    "ds_read_b64_tr_b16 v[206:207], v43 offset:24576\n"
    "s_waitcnt vmcnt(24) lgkmcnt(0)\n"
    "s_barrier\n"
    "s_add_u32 m0, s54, 24576\n"
    "s_add_u32 s55, %6, 223488\n"
    "buffer_load_dwordx4 v8, s[44:47], s55 offen lds\n"
    "s_add_u32 m0, s54, 25600\n"
    "s_add_u32 s55, %6, 241920\n"
    "buffer_load_dwordx4 v8, s[44:47], s55 offen lds\n"
    "s_add_u32 m0, s54, 26624\n"
    "s_add_u32 s55, %6, 223616\n"
    "buffer_load_dwordx4 v8, s[44:47], s55 offen lds\n"
    "s_add_u32 m0, s54, 27648\n"
    "s_add_u32 s55, %6, 242048\n"
    "buffer_load_dwordx4 v8, s[44:47], s55 offen lds\n"
    "s_add_u32 m0, s54, 28672\n"
    "s_add_u32 s55, %6, 223744\n"
    "buffer_load_dwordx4 v8, s[44:47], s55 offen lds\n"
    "s_add_u32 m0, s54, 29696\n"
    "s_add_u32 s55, %6, 242176\n"
    "buffer_load_dwordx4 v8, s[44:47], s55 offen lds\n"
    "ds_read_b64_tr_b16 a[48:49], v32 offset:49152\n"
    "ds_read_b64_tr_b16 a[52:53], v33 offset:49152\n"
    "ds_read_b64_tr_b16 a[56:57], v34 offset:49152\n"
    "ds_read_b64_tr_b16 a[144:145], v35 offset:49152\n"
    "ds_read_b64_tr_b16 a[148:149], v36 offset:49152\n"
    "ds_read_b64_tr_b16 a[152:153], v37 offset:49152\n"
    "ds_read_b64_tr_b16 a[240:241], v38 offset:49152\n"
    "ds_read_b64_tr_b16 a[244:245], v39 offset:49152\n"
    "ds_read_b64_tr_b16 a[248:249], v40 offset:49152\n"
    "ds_read_b64_tr_b16 v[208:209], v41 offset:49152\n"
    "ds_read_b64_tr_b16 v[212:213], v42 offset:49152\n"
    "ds_read_b64_tr_b16 v[216:217], v43 offset:49152\n"
    "s_waitcnt vmcnt(24) lgkmcnt(0)\n"
    "s_barrier\n"
    "s_add_u32 m0, s54, 49152\n"
    "s_add_u32 s55, %6, 258048\n"
    "buffer_load_dwordx4 v8, s[44:47], s55 offen lds\n"
    "s_add_u32 m0, s54, 50176\n"
    "s_add_u32 s55, %6, 276480\n"
    "buffer_load_dwordx4 v8, s[44:47], s55 offen lds\n"
    "s_add_u32 m0, s54, 51200\n"
    "s_add_u32 s55, %6, 258176\n"
    "buffer_load_dwordx4 v8, s[44:47], s55 offen lds\n"
    "s_add_u32 m0, s54, 52224\n"
    "s_add_u32 s55, %6, 276608\n"
    "buffer_load_dwordx4 v8, s[44:47], s55 offen lds\n"
    "s_add_u32 m0, s54, 53248\n"
    "s_add_u32 s55, %6, 258304\n"
    "buffer_load_dwordx4 v8, s[44:47], s55 offen lds\n"
    "s_add_u32 m0, s54, 54272\n"
    "s_add_u32 s55, %6, 276736\n"
    "buffer_load_dwordx4 v8, s[44:47], s55 offen lds\n"
    "ds_read_b64_tr_b16 a[50:51], v71 offset:8192\n"
    "ds_read_b64_tr_b16 a[54:55], v70 offset:8192\n"
    "ds_read_b64_tr_b16 a[58:59], v69 offset:8192\n"
    "ds_read_b64_tr_b16 a[146:147], v68 offset:8192\n"
    "ds_read_b64_tr_b16 a[150:151], v67 offset:8192\n"
    "ds_read_b64_tr_b16 a[154:155], v66 offset:8192\n"
    "ds_read_b64_tr_b16 a[242:243], v65 offset:8192\n"
    "ds_read_b64_tr_b16 a[246:247], v64 offset:8192\n"
    "ds_read_b64_tr_b16 a[250:251], v63 offset:8192\n"
    "ds_read_b64_tr_b16 v[210:211], v62 offset:8192\n"
    "ds_read_b64_tr_b16 v[214:215], v61 offset:8192\n"
    "ds_read_b64_tr_b16 v[218:219], v60 offset:8192\n"
    "s_waitcnt vmcnt(24) lgkmcnt(0)\n"
    "s_barrier\n"
    "s_add_u32 m0, s54, 73728\n"
    "s_add_u32 s55, %6, 260352\n"
    "buffer_load_dwordx4 v8, s[44:47], s55 offen lds\n"
    "s_add_u32 m0, s54, 74752\n"
    "s_add_u32 s55, %6, 278784\n"
    "buffer_load_dwordx4 v8, s[44:47], s55 offen lds\n"
    "s_add_u32 m0, s54, 75776\n"
    "s_add_u32 s55, %6, 260480\n"
    "buffer_load_dwordx4 v8, s[44:47], s55 offen lds\n"
    "s_add_u32 m0, s54, 76800\n"
    "s_add_u32 s55, %6, 278912\n"
    "buffer_load_dwordx4 v8, s[44:47], s55 offen lds\n"
    "s_add_u32 m0, s54, 77824\n"
    "s_add_u32 s55, %6, 260608\n"
    "buffer_load_dwordx4 v8, s[44:47], s55 offen lds\n"
    "s_add_u32 m0, s54, 78848\n"
    "s_add_u32 s55, %6, 279040\n"
    "buffer_load_dwordx4 v8, s[44:47], s55 offen lds\n"
    "v_mov_b32 v7, 0x20400\n"
    "global_load_dwordx2 v[20:21], v7, %1 sc1\n"
    "v_mbcnt_lo_u32_b32 v6, -1, 0\n"
    "v_min_u32 v6, 7, v6\n"
    "v_lshlrev_b32 v6, 7, v6\n"
    "v_add_u32 v6, 0x20000, v6\n"
    "global_load_dwordx2 v[22:23], v6, %1 sc1\n"
    "ds_read_b64_tr_b16 a[60:61], v71 offset:32768\n"
    "ds_read_b64_tr_b16 a[64:65], v70 offset:32768\n"
    "ds_read_b64_tr_b16 a[68:69], v69 offset:32768\n"
    "ds_read_b64_tr_b16 a[156:157], v68 offset:32768\n"
    "ds_read_b64_tr_b16 a[160:161], v67 offset:32768\n"
    "ds_read_b64_tr_b16 a[164:165], v66 offset:32768\n"
    "ds_read_b64_tr_b16 a[252:253], v65 offset:32768\n"
    "ds_read_b64_tr_b16 v[128:129], v64 offset:32768\n"
    "ds_read_b64_tr_b16 v[132:133], v63 offset:32768\n"
    "ds_read_b64_tr_b16 v[220:221], v62 offset:32768\n"
    "ds_read_b64_tr_b16 v[224:225], v61 offset:32768\n"
    "ds_read_b64_tr_b16 v[228:229], v60 offset:32768\n"
    "s_waitcnt vmcnt(26) lgkmcnt(0)\n"
    "s_barrier\n"
    "ds_read_b64_tr_b16 a[62:63], v71 offset:57344\n"
    "ds_read_b64_tr_b16 a[66:67], v70 offset:57344\n"
    "ds_read_b64_tr_b16 a[70:71], v69 offset:57344\n"
    "ds_read_b64_tr_b16 a[158:159], v68 offset:57344\n"
    "ds_read_b64_tr_b16 a[162:163], v67 offset:57344\n"
    "ds_read_b64_tr_b16 a[166:167], v66 offset:57344\n"
    "ds_read_b64_tr_b16 a[254:255], v65 offset:57344\n"
    "ds_read_b64_tr_b16 v[130:131], v64 offset:57344\n"
    "ds_read_b64_tr_b16 v[134:135], v63 offset:57344\n"
    "ds_read_b64_tr_b16 v[222:223], v62 offset:57344\n"
    "ds_read_b64_tr_b16 v[226:227], v61 offset:57344\n"
    "ds_read_b64_tr_b16 v[230:231], v60 offset:57344\n"
    "s_waitcnt vmcnt(20) lgkmcnt(0)\n"
    "s_barrier\n"
    "ds_read_b64_tr_b16 a[72:73], v32\n"
    "ds_read_b64_tr_b16 a[76:77], v33\n"
    "ds_read_b64_tr_b16 a[80:81], v34\n"
    "ds_read_b64_tr_b16 a[168:169], v35\n"
    "ds_read_b64_tr_b16 a[172:173], v36\n"
    "ds_read_b64_tr_b16 a[176:177], v37\n"
    "ds_read_b64_tr_b16 v[136:137], v38\n"
    "ds_read_b64_tr_b16 v[140:141], v39\n"
    "ds_read_b64_tr_b16 v[144:145], v40\n"
    "ds_read_b64_tr_b16 v[232:233], v41\n"
    "ds_read_b64_tr_b16 v[236:237], v42\n"
    "ds_read_b64_tr_b16 v[240:241], v43\n"
    "s_waitcnt vmcnt(14) lgkmcnt(0)\n"
    "s_barrier\n"
    "ds_read_b64_tr_b16 a[74:75], v32 offset:24576\n"
    "ds_read_b64_tr_b16 a[78:79], v33 offset:24576\n"
    "ds_read_b64_tr_b16 a[82:83], v34 offset:24576\n"
    "ds_read_b64_tr_b16 a[170:171], v35 offset:24576\n"
    "ds_read_b64_tr_b16 a[174:175], v36 offset:24576\n"
    "ds_read_b64_tr_b16 a[178:179], v37 offset:24576\n"
    "ds_read_b64_tr_b16 v[138:139], v38 offset:24576\n"
    "ds_read_b64_tr_b16 v[142:143], v39 offset:24576\n"
    "ds_read_b64_tr_b16 v[146:147], v40 offset:24576\n"
    "ds_read_b64_tr_b16 v[234:235], v41 offset:24576\n"
    "ds_read_b64_tr_b16 v[238:239], v42 offset:24576\n"
    "ds_read_b64_tr_b16 v[242:243], v43 offset:24576\n"
    "s_waitcnt vmcnt(8) lgkmcnt(0)\n"
    "s_barrier\n"
    "ds_read_b64_tr_b16 a[84:85], v32 offset:49152\n"
    "ds_read_b64_tr_b16 a[88:89], v33 offset:49152\n"
    "ds_read_b64_tr_b16 a[92:93], v34 offset:49152\n"
    "ds_read_b64_tr_b16 a[180:181], v35 offset:49152\n"
    "ds_read_b64_tr_b16 a[184:185], v36 offset:49152\n"
    "ds_read_b64_tr_b16 a[188:189], v37 offset:49152\n"
    "ds_read_b64_tr_b16 v[148:149], v38 offset:49152\n"
    "ds_read_b64_tr_b16 v[152:153], v39 offset:49152\n"
    "ds_read_b64_tr_b16 v[156:157], v40 offset:49152\n"
    "ds_read_b64_tr_b16 v[244:245], v41 offset:49152\n"
    "ds_read_b64_tr_b16 v[248:249], v42 offset:49152\n"
    "ds_read_b64_tr_b16 v[252:253], v43 offset:49152\n"
    "s_waitcnt vmcnt(2) lgkmcnt(0)\n"
    "s_barrier\n"
    "ds_read_b64_tr_b16 a[86:87], v71 offset:8192\n"
    "ds_read_b64_tr_b16 a[90:91], v70 offset:8192\n"
    "ds_read_b64_tr_b16 a[94:95], v69 offset:8192\n"
    "ds_read_b64_tr_b16 a[182:183], v68 offset:8192\n"
    "ds_read_b64_tr_b16 a[186:187], v67 offset:8192\n"
    "ds_read_b64_tr_b16 a[190:191], v66 offset:8192\n"
    "ds_read_b64_tr_b16 v[150:151], v65 offset:8192\n"
    "ds_read_b64_tr_b16 v[154:155], v64 offset:8192\n"
    "ds_read_b64_tr_b16 v[158:159], v63 offset:8192\n"
    "ds_read_b64_tr_b16 v[246:247], v62 offset:8192\n"
    "ds_read_b64_tr_b16 v[250:251], v61 offset:8192\n"
    "ds_read_b64_tr_b16 v[254:255], v60 offset:8192\n"
    "s_waitcnt lgkmcnt(0)\n"
    "s_barrier\n"
    "s_waitcnt vmcnt(0)\n"
    "v_add_u32 v20, 1, v20\n"
    "v_add_u32 v21, 1, v21\n"
    "s_mov_b32 s52, 1048576\n"
    ".Lwgo_a%=:\n"
    "v_cmp_ne_u64 vcc, v[20:21], v[22:23]\n"
    "s_cbranch_vccz .Lwgo_x%=\n"
    "global_load_dwordx2 v[22:23], v6, %1 sc1\n"
    "s_sub_u32 s52, s52, 1\n"
    "s_waitcnt vmcnt(0)\n"
    "s_cbranch_scc0 .Lwgo_a%=\n"
    ".Lwgo_x%=:\n"
    "v_mbcnt_lo_u32_b32 v20, -1, 0\n"
    "v_mbcnt_hi_u32_b32 v20, -1, v20\n"
    "v_min_u32 v6, 7, v20\n"
    "v_lshlrev_b32 v6, 7, v6\n"
    "v_mov_b32 v7, 0x26c00\n"
    "ds_read_b128 v[20:23], v7\n"
    "s_waitcnt lgkmcnt(0)\n"
    "v_readfirstlane_b32 s49, v21\n"
    "v_add_u32 v20, v20, v6\n"
    "s_branch .Lwchk_%=\n"
    ".Lwgo_%=:\n"
    "s_waitcnt vmcnt(1)\n"
    "v_mfma_f32_16x16x32_bf16 v[8:11], a[0:3], v[64:67], 0\n"
    "v_add_u32 v7, 32, v7\n"
    "ds_read_b128 v[20:23], v7\n"
    "v_mfma_f32_16x16x32_bf16 v[12:15], a[4:7], v[64:67], 0\n"
    "ds_write_b128 v1, v[64:67]\n"
    "ds_write_b128 v1, v[68:71] offset:1024\n"
    "v_mfma_f32_16x16x32_bf16 v[8:11], a[12:15], v[68:71], v[8:11]\n"
    "ds_write_b128 v1, v[72:75] offset:2048\n"
    "ds_write_b128 v1, v[76:79] offset:3072\n"
    "v_mfma_f32_16x16x32_bf16 v[12:15], a[16:19], v[68:71], v[12:15]\n"
    "v_cvt_pk_bf16_f32 v28, v16, v17\n"
    "v_cvt_pk_bf16_f32 v30, v16, v17\n"
    "v_mfma_f32_16x16x32_bf16 v[8:11], a[24:27], v[72:75], v[8:11]\n"
    "v_cvt_pk_bf16_f32 v29, v18, v19\n"
    "v_cvt_pk_bf16_f32 v31, v18, v19\n"
    "v_mfma_f32_16x16x32_bf16 v[12:15], a[28:31], v[72:75], v[12:15]\n"
    "ds_write_b128 v1, v[80:83] offset:4096\n"
    "ds_write_b128 v1, v[84:87] offset:5120\n"
    "v_mfma_f32_16x16x32_bf16 v[8:11], a[36:39], v[76:79], v[8:11]\n"
    "v_permlane16_swap_b32 v28, v30\n"
    "v_permlane16_swap_b32 v29, v31\n"
    "v_mfma_f32_16x16x32_bf16 v[12:15], a[40:43], v[76:79], v[12:15]\n"
    "ds_write_b128 v1, v[88:91] offset:6144\n"
    "ds_write_b128 v1, v[92:95] offset:7168\n"
    "v_mfma_f32_16x16x32_bf16 v[8:11], a[48:51], v[80:83], v[8:11]\n"
    "buffer_store_dwordx4 v[28:31], v3, s[40:43], s62 offen sc1\n"
    "v_xor_b32 v0, 0x2000, v1\n"
    "v_mfma_f32_16x16x32_bf16 v[12:15], a[52:55], v[80:83], v[12:15]\n"
    "s_waitcnt lgkmcnt(8)\n"
    "v_readfirstlane_b32 s49, v21\n"
    "v_mfma_f32_16x16x32_bf16 v[8:11], a[60:63], v[84:87], v[8:11]\n"
    "v_readfirstlane_b32 s62, v22\n"
    "v_add_u32 v20, v20, v6\n"
    "v_mfma_f32_16x16x32_bf16 v[12:15], a[64:67], v[84:87], v[12:15]\n"
    ".Lwtop_%=:\n"
    "v_mfma_f32_16x16x32_bf16 v[16:19], a[8:11], v[64:67], 0\n"
    "s_waitcnt lgkmcnt(0)\n"
    "s_barrier\n"
    "v_mfma_f32_16x16x32_bf16 v[16:19], a[20:23], v[68:71], v[16:19]\n"
    "ds_read_b128 v[32:35], v0\n"
    "ds_read_b128 v[36:39], v0 offset:1024\n"
    "v_mfma_f32_16x16x32_bf16 v[16:19], a[32:35], v[72:75], v[16:19]\n"
    "ds_read_b128 v[44:47], v0 offset:3072\n"
    "ds_read_b128 v[40:43], v0 offset:2048\n"
    "v_mfma_f32_16x16x32_bf16 v[16:19], a[44:47], v[76:79], v[16:19]\n"
    "ds_read_b128 v[52:55], v0 offset:5120\n"
    "ds_read_b128 v[48:51], v0 offset:4096\n"
    "v_mfma_f32_16x16x32_bf16 v[16:19], a[56:59], v[80:83], v[16:19]\n"
    "ds_read_b128 v[60:63], v0 offset:7168\n"
    "ds_read_b128 v[56:59], v0 offset:6144\n"
    "v_mfma_f32_16x16x32_bf16 v[16:19], a[68:71], v[84:87], v[16:19]\n"
    "global_load_dword v21, v20, %1 sc1\n"
    "v_mfma_f32_16x16x32_bf16 v[8:11], a[72:75], v[88:91], v[8:11]\n"
    "v_mfma_f32_16x16x32_bf16 v[12:15], a[76:79], v[88:91], v[12:15]\n"
    "v_mfma_f32_16x16x32_bf16 v[16:19], a[80:83], v[88:91], v[16:19]\n"
    "v_mfma_f32_16x16x32_bf16 v[8:11], a[84:87], v[92:95], v[8:11]\n"
    "v_mfma_f32_16x16x32_bf16 v[12:15], a[88:91], v[92:95], v[12:15]\n"
    "v_mfma_f32_16x16x32_bf16 v[16:19], a[92:95], v[92:95], v[16:19]\n"
    "s_waitcnt lgkmcnt(7)\n"
    "v_mfma_f32_16x16x32_bf16 v[8:11], a[96:99], v[32:35], v[8:11]\n"
    "v_mfma_f32_16x16x32_bf16 v[12:15], a[100:103], v[32:35], v[12:15]\n"
    "v_mfma_f32_16x16x32_bf16 v[16:19], a[104:107], v[32:35], v[16:19]\n"
    "s_waitcnt lgkmcnt(6)\n"
    "v_mfma_f32_16x16x32_bf16 v[8:11], a[108:111], v[36:39], v[8:11]\n"
    "v_mfma_f32_16x16x32_bf16 v[12:15], a[112:115], v[36:39], v[12:15]\n"
    "v_xor_b32 v0, 0x4000, v1\n"
    "v_mfma_f32_16x16x32_bf16 v[16:19], a[116:119], v[36:39], v[16:19]\n"
    "s_waitcnt lgkmcnt(4)\n"
    "v_mfma_f32_16x16x32_bf16 v[8:11], a[120:123], v[40:43], v[8:11]\n"
    "ds_read_b128 v[36:39], v0 offset:1024\n"
    "ds_read_b128 v[32:35], v0\n"
    "v_mfma_f32_16x16x32_bf16 v[12:15], a[124:127], v[40:43], v[12:15]\n"
    "v_mfma_f32_16x16x32_bf16 v[16:19], a[128:131], v[40:43], v[16:19]\n"
    "v_mfma_f32_16x16x32_bf16 v[8:11], a[132:135], v[44:47], v[8:11]\n"
    "v_mfma_f32_16x16x32_bf16 v[12:15], a[136:139], v[44:47], v[12:15]\n"
    "v_mfma_f32_16x16x32_bf16 v[16:19], a[140:143], v[44:47], v[16:19]\n"
    "s_waitcnt lgkmcnt(4)\n"
    "v_mfma_f32_16x16x32_bf16 v[8:11], a[144:147], v[48:51], v[8:11]\n"
    "ds_read_b128 v[44:47], v0 offset:3072\n"
    "ds_read_b128 v[40:43], v0 offset:2048\n"
    "v_mfma_f32_16x16x32_bf16 v[12:15], a[148:151], v[48:51], v[12:15]\n"
    "v_mfma_f32_16x16x32_bf16 v[16:19], a[152:155], v[48:51], v[16:19]\n"
    "v_mfma_f32_16x16x32_bf16 v[8:11], a[156:159], v[52:55], v[8:11]\n"
    "v_mfma_f32_16x16x32_bf16 v[12:15], a[160:163], v[52:55], v[12:15]\n"
    "v_mfma_f32_16x16x32_bf16 v[16:19], a[164:167], v[52:55], v[16:19]\n"
    "s_waitcnt lgkmcnt(4)\n"
    "v_mfma_f32_16x16x32_bf16 v[8:11], a[168:171], v[56:59], v[8:11]\n"
    "ds_read_b128 v[52:55], v0 offset:5120\n"
    "ds_read_b128 v[48:51], v0 offset:4096\n"
    "v_mfma_f32_16x16x32_bf16 v[12:15], a[172:175], v[56:59], v[12:15]\n"
    "v_mfma_f32_16x16x32_bf16 v[16:19], a[176:179], v[56:59], v[16:19]\n"
    "s_waitcnt vmcnt(0)\n"
    "v_mov_b32 v22, 1\n"
    "v_mfma_f32_16x16x32_bf16 v[8:11], a[180:183], v[60:63], v[8:11]\n"
    "s_mov_b64 exec, s[56:57]\n"
    "ds_add_rtn_u32 v22, v7, v22 offset:16\n"
    "s_mov_b64 exec, -1\n"
    "v_mfma_f32_16x16x32_bf16 v[12:15], a[184:187], v[60:63], v[12:15]\n"
    "v_cmp_eq_u32 vcc, 0, v21\n"
    "s_cmp_eq_u64 vcc, 0\n"
    "v_mfma_f32_16x16x32_bf16 v[16:19], a[188:191], v[60:63], v[16:19]\n"
    "s_waitcnt lgkmcnt(4)\n"
    "v_mfma_f32_16x16x32_bf16 v[8:11], a[192:195], v[32:35], v[8:11]\n"
    "ds_read_b128 v[60:63], v0 offset:7168\n"
    "ds_read_b128 v[56:59], v0 offset:6144\n"
    "v_mfma_f32_16x16x32_bf16 v[12:15], a[196:199], v[32:35], v[12:15]\n"
    "s_cselect_b32 s94, s42, 0\n"
    "v_mfma_f32_16x16x32_bf16 v[16:19], a[200:203], v[32:35], v[16:19]\n"
    "global_load_dword v21, v20, %1 sc1\n"
    "v_mfma_f32_16x16x32_bf16 v[8:11], a[204:207], v[36:39], v[8:11]\n"
    "buffer_load_dwordx4 v[64:67], v4, s[92:95], s49 offen sc1\n"
    "v_mfma_f32_16x16x32_bf16 v[12:15], a[208:211], v[36:39], v[12:15]\n"
    "v_xor_b32 v0, 0x6000, v1\n"
    "buffer_load_dwordx4 v[68:71], v4, s[92:95], s49 offen offset:1024 sc1\n"
    "v_mfma_f32_16x16x32_bf16 v[16:19], a[212:215], v[36:39], v[16:19]\n"
    "s_waitcnt lgkmcnt(4)\n"
    "v_mfma_f32_16x16x32_bf16 v[8:11], a[216:219], v[40:43], v[8:11]\n"
    "ds_read_b128 v[36:39], v0 offset:1024\n"
    "ds_read_b128 v[32:35], v0\n"
    "v_mfma_f32_16x16x32_bf16 v[12:15], a[220:223], v[40:43], v[12:15]\n"
    "s_waitcnt lgkmcnt(4)\n"
    "v_readfirstlane_b32 s50, v22\n"
    "v_mfma_f32_16x16x32_bf16 v[16:19], a[224:227], v[40:43], v[16:19]\n"
    "buffer_load_dwordx4 v[72:75], v4, s[92:95], s49 offen offset:2048 sc1\n"
    "s_cmp_eq_u32 s50, 3\n"
    "v_mfma_f32_16x16x32_bf16 v[8:11], a[228:231], v[44:47], v[8:11]\n"
    "s_cselect_b64 exec, 1, 0\n"
    "global_store_dword v23, v22, %1 sc1\n"
    "s_mov_b64 exec, -1\n"
    "v_mfma_f32_16x16x32_bf16 v[12:15], a[232:235], v[44:47], v[12:15]\n"
    "buffer_load_dwordx4 v[76:79], v4, s[92:95], s49 offen offset:3072 sc1\n"
    "v_mfma_f32_16x16x32_bf16 v[16:19], a[236:239], v[44:47], v[16:19]\n"
    "buffer_load_dwordx4 v[80:83], v5, s[92:95], s49 offen sc1\n"
    "v_mfma_f32_16x16x32_bf16 v[8:11], a[240:243], v[48:51], v[8:11]\n"
    "ds_read_b128 v[44:47], v0 offset:3072\n"
    "ds_read_b128 v[40:43], v0 offset:2048\n"
    "v_mfma_f32_16x16x32_bf16 v[12:15], a[244:247], v[48:51], v[12:15]\n"
    "buffer_load_dwordx4 v[84:87], v5, s[92:95], s49 offen offset:1024 sc1\n"
    "v_mfma_f32_16x16x32_bf16 v[16:19], a[248:251], v[48:51], v[16:19]\n"
    "buffer_load_dwordx4 v[88:91], v5, s[92:95], s49 offen offset:2048 sc1\n"
    "v_mfma_f32_16x16x32_bf16 v[8:11], a[252:255], v[52:55], v[8:11]\n"
    "buffer_load_dwordx4 v[92:95], v5, s[92:95], s49 offen offset:3072 sc1\n"
    "v_mfma_f32_16x16x32_bf16 v[12:15], v[128:131], v[52:55], v[12:15]\n"
    "v_mfma_f32_16x16x32_bf16 v[16:19], v[132:135], v[52:55], v[16:19]\n"
    "s_waitcnt lgkmcnt(4)\n"
    "v_mfma_f32_16x16x32_bf16 v[8:11], v[136:139], v[56:59], v[8:11]\n"
    "ds_read_b128 v[52:55], v0 offset:5120\n"
    "ds_read_b128 v[48:51], v0 offset:4096\n"
    "v_mfma_f32_16x16x32_bf16 v[12:15], v[140:143], v[56:59], v[12:15]\n"
    "v_mfma_f32_16x16x32_bf16 v[16:19], v[144:147], v[56:59], v[16:19]\n"
    "v_mfma_f32_16x16x32_bf16 v[8:11], v[148:151], v[60:63], v[8:11]\n"
    "v_mfma_f32_16x16x32_bf16 v[12:15], v[152:155], v[60:63], v[12:15]\n"
    "v_mfma_f32_16x16x32_bf16 v[16:19], v[156:159], v[60:63], v[16:19]\n"
    "s_waitcnt lgkmcnt(4)\n"
    "v_mfma_f32_16x16x32_bf16 v[8:11], v[160:163], v[32:35], v[8:11]\n"
    "ds_read_b128 v[60:63], v0 offset:7168\n"
    "ds_read_b128 v[56:59], v0 offset:6144\n"
    "v_mfma_f32_16x16x32_bf16 v[12:15], v[164:167], v[32:35], v[12:15]\n"
    "s_cmp_eq_u32 s94, 0\n"
    "s_cbranch_scc1 .Lwk1_%=\n"
    ".Lwk1_x%=:\n"
    "v_mfma_f32_16x16x32_bf16 v[16:19], v[168:171], v[32:35], v[16:19]\n"
    "v_mfma_f32_16x16x32_bf16 v[8:11], v[172:175], v[36:39], v[8:11]\n"
    "v_mfma_f32_16x16x32_bf16 v[12:15], v[176:179], v[36:39], v[12:15]\n"
    "v_mfma_f32_16x16x32_bf16 v[16:19], v[180:183], v[36:39], v[16:19]\n"
    "s_waitcnt lgkmcnt(4)\n"
    "v_mfma_f32_16x16x32_bf16 v[8:11], v[184:187], v[40:43], v[8:11]\n"
    "v_mfma_f32_16x16x32_bf16 v[12:15], v[188:191], v[40:43], v[12:15]\n"
    "v_mfma_f32_16x16x32_bf16 v[16:19], v[192:195], v[40:43], v[16:19]\n"
    "v_mfma_f32_16x16x32_bf16 v[8:11], v[196:199], v[44:47], v[8:11]\n"
    "v_xor_b32 v1, 0x8000, v1\n"
    "v_mfma_f32_16x16x32_bf16 v[12:15], v[200:203], v[44:47], v[12:15]\n"
    "s_waitcnt lgkmcnt(2)\n"
    "v_mfma_f32_16x16x32_bf16 v[8:11], v[208:211], v[48:51], v[8:11]\n"
    "v_mfma_f32_16x16x32_bf16 v[12:15], v[212:215], v[48:51], v[12:15]\n"
    "v_mfma_f32_16x16x32_bf16 v[8:11], v[220:223], v[52:55], v[8:11]\n"
    "v_mfma_f32_16x16x32_bf16 v[12:15], v[224:227], v[52:55], v[12:15]\n"
    "s_waitcnt lgkmcnt(0)\n"
    "v_mfma_f32_16x16x32_bf16 v[8:11], v[232:235], v[56:59], v[8:11]\n"
    "v_mfma_f32_16x16x32_bf16 v[12:15], v[236:239], v[56:59], v[12:15]\n"
    "v_mfma_f32_16x16x32_bf16 v[8:11], v[244:247], v[60:63], v[8:11]\n"
    "v_mfma_f32_16x16x32_bf16 v[12:15], v[248:251], v[60:63], v[12:15]\n"
    "v_mfma_f32_16x16x32_bf16 v[16:19], v[204:207], v[44:47], v[16:19]\n"
    "v_mfma_f32_16x16x32_bf16 v[16:19], v[216:219], v[48:51], v[16:19]\n"
    "v_cvt_pk_bf16_f32 v24, v8, v9\n"
    "v_cvt_pk_bf16_f32 v25, v10, v11\n"
    "v_mfma_f32_16x16x32_bf16 v[16:19], v[228:231], v[52:55], v[16:19]\n"
    "v_cvt_pk_bf16_f32 v26, v12, v13\n"
    "v_cvt_pk_bf16_f32 v27, v14, v15\n"
    "v_mfma_f32_16x16x32_bf16 v[16:19], v[240:243], v[56:59], v[16:19]\n"
    "buffer_store_dwordx4 v[24:27], v2, s[40:43], s62 offen sc1\n"
    "v_mfma_f32_16x16x32_bf16 v[16:19], v[252:255], v[60:63], v[16:19]\n"
    "s_mov_b32 s56, 1\n"
    "s_cmp_lg_u32 s94, 0\n"
    "s_cbranch_scc1 .Lwgo_%=\n"
    "s_nop 5\n"
    "v_cvt_pk_bf16_f32 v28, v16, v17\n"
    "v_cvt_pk_bf16_f32 v30, v16, v17\n"
    "v_cvt_pk_bf16_f32 v29, v18, v19\n"
    "v_cvt_pk_bf16_f32 v31, v18, v19\n"
    "v_permlane16_swap_b32 v28, v30\n"
    "s_nop 0\n"
    "v_permlane16_swap_b32 v29, v31\n"
    "buffer_store_dwordx4 v[28:31], v3, s[40:43], s62 offen sc1\n"
    "s_waitcnt vmcnt(2)\n"
    "v_cmp_eq_u32 vcc, 0, v21\n"
    "s_cmp_eq_u64 vcc, 0\n"
    "s_cbranch_scc1 .Lwld_%=\n"
    "ds_read_b32 v23, v7 offset:44\n"
    "global_load_dword v21, v20, %1 sc1\n"
    "s_waitcnt vmcnt(0)\n"
    "v_mov_b32 v22, 1\n"
    "s_mov_b64 exec, 1\n"
    "ds_add_rtn_u32 v22, v7, v22 offset:48\n"
    "s_mov_b64 exec, -1\n"
    "s_waitcnt lgkmcnt(0)\n"
    "v_readfirstlane_b32 s50, v22\n"
    "s_cmp_eq_u32 s50, 3\n"
    "s_cselect_b64 exec, 1, 0\n"
    "global_store_dword v23, v22, %1 sc1\n"
    "s_mov_b64 exec, -1\n"
    "s_mov_b32 s56, 0\n"
    "v_cmp_eq_u32 vcc, 0, v21\n"
    "s_cmp_eq_u64 vcc, 0\n"
    "s_cbranch_scc1 .Lwld_%=\n"
    ".Lwchk_%=:\n"
    "v_readfirstlane_b32 s50, v20\n"
    "s_cmp_lt_u32 s50, 200\n"
    "s_cbranch_scc1 .Lwend_%=\n"
    ".Lwwait_%=:\n"
    "global_load_dword v21, v20, %1 sc1\n"
    "s_mov_b32 s52, 0\n"
    ".Lwp2_%=:\n"
    "s_waitcnt vmcnt(0)\n"
    "v_cmp_eq_u32 vcc, 0, v21\n"
    "s_cmp_eq_u64 vcc, 0\n"
    "s_cbranch_scc1 .Lwp2_x%=\n"
    "s_add_u32 s52, s52, 1\n"
    "s_sleep 2\n"
    "global_load_dword v21, v20, %1 sc1\n"
    "s_cmp_lt_u32 s52, s53\n"
    "s_cbranch_scc1 .Lwp2_%=\n"
    "s_mov_b32 s53, 0\n"
    "v_mov_b32 v22, 0\n"
    "v_mov_b32 v23, 1\n"
    "global_store_dword v22, v23, %1 sc1\n"
    "s_waitcnt vmcnt(0)\n"
    ".Lwp2_x%=:\n"
    ".Lwld_%=:\n"
    "s_mov_b32 s94, s42\n"
    "buffer_load_dwordx4 v[64:67], v4, s[92:95], s49 offen sc1\n"
    "buffer_load_dwordx4 v[68:71], v4, s[92:95], s49 offen offset:1024 sc1\n"
    "buffer_load_dwordx4 v[72:75], v4, s[92:95], s49 offen offset:2048 sc1\n"
    "buffer_load_dwordx4 v[76:79], v4, s[92:95], s49 offen offset:3072 sc1\n"
    "buffer_load_dwordx4 v[80:83], v5, s[92:95], s49 offen sc1\n"
    "buffer_load_dwordx4 v[84:87], v5, s[92:95], s49 offen offset:1024 sc1\n"
    "buffer_load_dwordx4 v[88:91], v5, s[92:95], s49 offen offset:2048 sc1\n"
    "buffer_load_dwordx4 v[92:95], v5, s[92:95], s49 offen offset:3072 sc1\n"
    ".Lwin_%=:\n"
    "v_add_u32 v7, 32, v7\n"
    "ds_read_b128 v[20:23], v7\n"
    "s_waitcnt vmcnt(7)\n"
    "v_mfma_f32_16x16x32_bf16 v[8:11], a[0:3], v[64:67], 0\n"
    "v_mfma_f32_16x16x32_bf16 v[12:15], a[4:7], v[64:67], 0\n"
    "ds_write_b128 v1, v[64:67]\n"
    "s_waitcnt vmcnt(6)\n"
    "ds_write_b128 v1, v[68:71] offset:1024\n"
    "v_mfma_f32_16x16x32_bf16 v[8:11], a[12:15], v[68:71], v[8:11]\n"
    "s_waitcnt vmcnt(5)\n"
    "ds_write_b128 v1, v[72:75] offset:2048\n"
    "s_waitcnt vmcnt(4)\n"
    "ds_write_b128 v1, v[76:79] offset:3072\n"
    "v_mfma_f32_16x16x32_bf16 v[12:15], a[16:19], v[68:71], v[12:15]\n"
    "v_mfma_f32_16x16x32_bf16 v[8:11], a[24:27], v[72:75], v[8:11]\n"
    "v_mfma_f32_16x16x32_bf16 v[12:15], a[28:31], v[72:75], v[12:15]\n"
    "s_waitcnt vmcnt(3)\n"
    "ds_write_b128 v1, v[80:83] offset:4096\n"
    "s_waitcnt vmcnt(2)\n"
    "ds_write_b128 v1, v[84:87] offset:5120\n"
    "v_mfma_f32_16x16x32_bf16 v[8:11], a[36:39], v[76:79], v[8:11]\n"
    "v_mfma_f32_16x16x32_bf16 v[12:15], a[40:43], v[76:79], v[12:15]\n"
    "s_waitcnt vmcnt(1)\n"
    "ds_write_b128 v1, v[88:91] offset:6144\n"
    "s_waitcnt vmcnt(0)\n"
    "ds_write_b128 v1, v[92:95] offset:7168\n"
    "v_mfma_f32_16x16x32_bf16 v[8:11], a[48:51], v[80:83], v[8:11]\n"
    "v_xor_b32 v0, 0x2000, v1\n"
    "v_mfma_f32_16x16x32_bf16 v[12:15], a[52:55], v[80:83], v[12:15]\n"
    "s_waitcnt lgkmcnt(8)\n"
    "v_readfirstlane_b32 s49, v21\n"
    "v_mfma_f32_16x16x32_bf16 v[8:11], a[60:63], v[84:87], v[8:11]\n"
    "v_readfirstlane_b32 s62, v22\n"
    "v_add_u32 v20, v20, v6\n"
    "v_mfma_f32_16x16x32_bf16 v[12:15], a[64:67], v[84:87], v[12:15]\n"
    "s_cmp_eq_u32 s56, 0\n"
    "s_cbranch_scc1 .Lwc3_%=\n"
    "s_waitcnt vmcnt(0)\n"
    "v_mov_b32 v22, 1\n"
    "s_mov_b64 exec, 1\n"
    "ds_add_rtn_u32 v22, v7, v22 offset:16\n"
    "s_mov_b64 exec, -1\n"
    "s_waitcnt lgkmcnt(0)\n"
    "v_readfirstlane_b32 s50, v22\n"
    "s_cmp_eq_u32 s50, 3\n"
    "s_cselect_b64 exec, 1, 0\n"
    "global_store_dword v23, v22, %1 sc1\n"
    "s_mov_b64 exec, -1\n"
    "s_mov_b32 s56, 0\n"
    ".Lwc3_%=:\n"
    "s_branch .Lwtop_%=\n"
    ".Lwk1_%=:\n"
    "s_waitcnt vmcnt(8)\n"
    "v_cmp_eq_u32 vcc, 0, v21\n"
    "s_cmp_eq_u64 vcc, 0\n"
    "s_cselect_b32 s94, s42, 0\n"
    "global_load_dword v21, v20, %1 sc1\n"
    "buffer_load_dwordx4 v[64:67], v4, s[92:95], s49 offen sc1\n"
    "buffer_load_dwordx4 v[68:71], v4, s[92:95], s49 offen offset:1024 sc1\n"
    "buffer_load_dwordx4 v[72:75], v4, s[92:95], s49 offen offset:2048 sc1\n"
    "buffer_load_dwordx4 v[76:79], v4, s[92:95], s49 offen offset:3072 sc1\n"
    "buffer_load_dwordx4 v[80:83], v5, s[92:95], s49 offen sc1\n"
    "buffer_load_dwordx4 v[84:87], v5, s[92:95], s49 offen offset:1024 sc1\n"
    "buffer_load_dwordx4 v[88:91], v5, s[92:95], s49 offen offset:2048 sc1\n"
    "buffer_load_dwordx4 v[92:95], v5, s[92:95], s49 offen offset:3072 sc1\n"
    "s_branch .Lwk1_x%=\n"
    ".Lwpol_%=:\n"
    "v_mov_b32 v20, 64\n"
    "global_load_dword v21, v20, %1 sc1\n"
    "s_mov_b32 s52, 0\n"
    ".Lwp4_%=:\n"
    "s_waitcnt vmcnt(0)\n"
    "v_cmp_eq_u32 vcc, 0, v21\n"
    "s_cmp_eq_u64 vcc, 0\n"
    "s_cbranch_scc1 .Lwp4_x%=\n"
    "s_add_u32 s52, s52, 1\n"
    "s_sleep 2\n"
    "global_load_dword v21, v20, %1 sc1\n"
    "s_cmp_lt_u32 s52, s53\n"
    "s_cbranch_scc1 .Lwp4_%=\n"
    "s_mov_b32 s53, 0\n"
    "v_mov_b32 v22, 0\n"
    "v_mov_b32 v23, 1\n"
    "global_store_dword v22, v23, %1 sc1\n"
    "s_waitcnt vmcnt(0)\n"
    ".Lwp4_x%=:\n"
    "v_readfirstlane_b32 s50, v21\n"
    "v_mbcnt_lo_u32_b32 v20, -1, 0\n"
    "v_mbcnt_hi_u32_b32 v20, -1, v20\n"
    "s_lshl_b32 s51, %2, 6\n"
    "v_add_u32 v20, s51, v20\n"
    "v_min_u32 v20, 129, v20\n"
    "v_lshlrev_b32 v20, 5, v20\n"
    "v_add_u32 v20, 0x26c00, v20\n"
    "ds_read2_b32 v[22:23], v20 offset0:1 offset1:2\n"
    "s_and_b32 s50, s50, 1\n"
    "s_waitcnt lgkmcnt(0)\n"
    "v_and_b32 v21, s50, v22\n"
    "v_lshlrev_b32 v21, 13, v21\n"
    "v_and_b32 v22, -2, v22\n"
    "v_xor_b32 v22, v21, v22\n"
    "v_and_b32 v21, s50, v23\n"
    "v_lshlrev_b32 v21, 13, v21\n"
    "v_and_b32 v23, -2, v23\n"
    "v_xor_b32 v23, v21, v23\n"
    "ds_write2_b32 v20, v22, v23 offset0:1 offset1:2\n"
    "s_waitcnt lgkmcnt(0)\n"
    "s_barrier\n"
    "ds_read_b32 v21, v7 offset:4\n"
    "v_add_u32 v20, %4, v6\n"
    "s_waitcnt lgkmcnt(0)\n"
    "v_readfirstlane_b32 s49, v21\n"
    "s_branch .Lwwait_%=\n"
    ".Lwend_%=:\n"
    "s_cmp_eq_u32 s50, 192\n"
    "s_cbranch_scc0 .Lwpol_%=\n"
    "s_waitcnt vmcnt(0) lgkmcnt(0)\n"
    "s_endpgm\n"
    ".p2align 6\n"
    ".Lrole3_%=:\n"
    "s_mov_b64 s[40:41], %0\n"
    "s_and_b32 s41, s41, 0xffff\n"
    "s_mov_b32 s42, 0xbc00000\n"
    "s_mov_b32 s43, 0x20000\n"
    "s_mov_b64 s[92:93], %0\n"
    "s_and_b32 s93, s93, 0xffff\n"
    "s_mov_b32 s94, 0xbc00000\n"
    "s_mov_b32 s95, 0x20000\n"
    "s_mov_b32 s53, 131072\n"
    "s_mov_b64 s[56:57], 0\n"
    "v_mbcnt_lo_u32_b32 v92, -1, 0\n"
    "v_mbcnt_hi_u32_b32 v92, -1, v92\n"
    "s_lshl_b32 s50, %2, 8\n"
    "s_add_u32 s50, s50, 144384\n"
    "v_lshl_add_u32 v92, v92, 2, s50\n"
    "ds_read_b32 v0, v92\n"
    "ds_read_b32 v1, v92 offset:1024\n"
    "ds_read_b32 v2, v92 offset:2048\n"
    "ds_read_b32 v3, v92 offset:3072\n"
    "ds_read_b32 v4, v92 offset:4096\n"
    "ds_read_b32 v73, v92 offset:5120\n"
    "ds_read_b32 v6, v92 offset:6144\n"
    "ds_read_b32 v7, v92 offset:7168\n"
    "ds_read_b32 v8, v92 offset:8192\n"
    "ds_read_b32 v9, v92 offset:9216\n"
    "ds_read_b32 v10, v92 offset:10240\n"
    "ds_read_b32 v95, v92 offset:11264\n"
    "ds_read_b32 v90, v92 offset:12288\n"
    "ds_read_b32 v91, v92 offset:13312\n"
    "s_waitcnt lgkmcnt(0)\n"
    "s_barrier\n"
    "v_mbcnt_lo_u32_b32 v92, -1, 0\n"
    "v_mbcnt_hi_u32_b32 v92, -1, v92\n"
    "v_add_u32 v93, 0, v92\n"
    "v_mul_u32_u24 v94, 7282, v93\n"
    "v_lshrrev_b32 v94, 16, v94\n"
    "v_mul_u32_u24 v94, 3312, v94\n"
    "v_lshl_add_u32 v80, v93, 4, v94\n"
    "v_add_u32 v93, 64, v92\n"
    "v_mul_u32_u24 v94, 7282, v93\n"
    "v_lshrrev_b32 v94, 16, v94\n"
    "v_mul_u32_u24 v94, 3312, v94\n"
    "v_lshl_add_u32 v81, v93, 4, v94\n"
    "v_add_u32 v93, 128, v92\n"
    "v_mul_u32_u24 v94, 7282, v93\n"
    "v_lshrrev_b32 v94, 16, v94\n"
    "v_mul_u32_u24 v94, 3312, v94\n"
    "v_lshl_add_u32 v82, v93, 4, v94\n"
    "v_add_u32 v93, 192, v92\n"
    "v_mul_u32_u24 v94, 7282, v93\n"
    "v_lshrrev_b32 v94, 16, v94\n"
    "v_mul_u32_u24 v94, 3312, v94\n"
    "v_lshl_add_u32 v83, v93, 4, v94\n"
    "v_add_u32 v93, 256, v92\n"
    "v_mul_u32_u24 v94, 7282, v93\n"
    "v_lshrrev_b32 v94, 16, v94\n"
    "v_mul_u32_u24 v94, 3312, v94\n"
    "v_lshl_add_u32 v84, v93, 4, v94\n"
    "v_add_u32 v93, 320, v92\n"
    "v_mul_u32_u24 v94, 7282, v93\n"
    "v_lshrrev_b32 v94, 16, v94\n"
    "v_mul_u32_u24 v94, 3312, v94\n"
    "v_lshl_add_u32 v85, v93, 4, v94\n"
    "v_add_u32 v93, 384, v92\n"
    "v_mul_u32_u24 v94, 7282, v93\n"
    "v_lshrrev_b32 v94, 16, v94\n"
    "v_mul_u32_u24 v94, 3312, v94\n"
    "v_lshl_add_u32 v86, v93, 4, v94\n"
    "v_add_u32 v93, 448, v92\n"
    "v_mul_u32_u24 v94, 7282, v93\n"
    "v_lshrrev_b32 v94, 16, v94\n"
    "v_mul_u32_u24 v94, 3312, v94\n"
    "v_lshl_add_u32 v87, v93, 4, v94\n"
    "v_add_u32 v93, 512, v92\n"
    "v_mul_u32_u24 v94, 7282, v93\n"
    "v_lshrrev_b32 v94, 16, v94\n"
    "v_mul_u32_u24 v94, 3312, v94\n"
    "v_lshl_add_u32 v88, v93, 4, v94\n"
    "v_and_b32 v72, 48, v92\n"
    "v_add_u32 v5, 0x21c00, v73\n"
    "s_min_u32 s50, %2, 2\n"
    "s_mul_i32 s54, s50, 9216\n"
    "s_mul_i32 s50, s50, 1152\n"
    "s_add_u32 s51, s50, %6\n"
    "s_mov_b64 s[44:45], %8\n"
    "s_and_b32 s45, s45, 0xffff\n"
    "s_mov_b32 s46, 0x1e6000\n"
    "s_mov_b32 s47, 0x20000\n"
    "v_mov_b32 v56, 0x3f803f80\n"
    "v_mov_b32 v57, 0x3f803f80\n"
    "v_mov_b32 v58, 0x3f803f80\n"
    "v_mov_b32 v59, 0x3f803f80\n"
    "s_cmp_eq_u32 %2, 3\n"
    "s_cbranch_scc1 .Lac1_%=\n"
    "s_add_u32 s55, s51, 0\n"
    "s_add_u32 m0, s54, 0\n"
    "s_nop 0\n"
    "buffer_load_dwordx4 v80, s[44:47], s55 offen lds\n"
    "s_add_u32 m0, s54, 1024\n"
    "s_nop 0\n"
    "buffer_load_dwordx4 v81, s[44:47], s55 offen lds\n"
    "s_add_u32 m0, s54, 2048\n"
    "s_nop 0\n"
    "buffer_load_dwordx4 v82, s[44:47], s55 offen lds\n"
    "s_add_u32 m0, s54, 3072\n"
    "s_nop 0\n"
    "buffer_load_dwordx4 v83, s[44:47], s55 offen lds\n"
    "s_add_u32 m0, s54, 4096\n"
    "s_nop 0\n"
    "buffer_load_dwordx4 v84, s[44:47], s55 offen lds\n"
    "s_add_u32 m0, s54, 5120\n"
    "s_nop 0\n"
    "buffer_load_dwordx4 v85, s[44:47], s55 offen lds\n"
    "s_add_u32 m0, s54, 6144\n"
    "s_nop 0\n"
    "buffer_load_dwordx4 v86, s[44:47], s55 offen lds\n"
    "s_add_u32 m0, s54, 7168\n"
    "s_nop 0\n"
    "buffer_load_dwordx4 v87, s[44:47], s55 offen lds\n"
    "s_add_u32 m0, s54, 8192\n"
    "s_nop 0\n"
    "buffer_load_dwordx4 v88, s[44:47], s55 offen lds\n"
    ".Lac1_%=:\n"
    "s_cmp_eq_u32 %2, 3\n"
    "s_cbranch_scc1 .Lac2_%=\n"
    "s_add_u32 s55, s51, 221184\n"
    "s_add_u32 m0, s54, 27648\n"
    "s_nop 0\n"
    "buffer_load_dwordx4 v80, s[44:47], s55 offen lds\n"
    "s_add_u32 m0, s54, 28672\n"
    "s_nop 0\n"
    "buffer_load_dwordx4 v81, s[44:47], s55 offen lds\n"
    "s_add_u32 m0, s54, 29696\n"
    "s_nop 0\n"
    "buffer_load_dwordx4 v82, s[44:47], s55 offen lds\n"
    "s_add_u32 m0, s54, 30720\n"
    "s_nop 0\n"
    "buffer_load_dwordx4 v83, s[44:47], s55 offen lds\n"
    "s_add_u32 m0, s54, 31744\n"
    "s_nop 0\n"
    "buffer_load_dwordx4 v84, s[44:47], s55 offen lds\n"
    "s_add_u32 m0, s54, 32768\n"
    "s_nop 0\n"
    "buffer_load_dwordx4 v85, s[44:47], s55 offen lds\n"
    "s_add_u32 m0, s54, 33792\n"
    "s_nop 0\n"
    "buffer_load_dwordx4 v86, s[44:47], s55 offen lds\n"
    "s_add_u32 m0, s54, 34816\n"
    "s_nop 0\n"
    "buffer_load_dwordx4 v87, s[44:47], s55 offen lds\n"
    "s_add_u32 m0, s54, 35840\n"
    "s_nop 0\n"
    "buffer_load_dwordx4 v88, s[44:47], s55 offen lds\n"
    ".Lac2_%=:\n"
    "s_cmp_eq_u32 %2, 3\n"
    "s_cbranch_scc1 .Lac3_%=\n"
    "s_add_u32 s55, s51, 442368\n"
    "s_add_u32 m0, s54, 55296\n"
    "s_nop 0\n"
    "buffer_load_dwordx4 v80, s[44:47], s55 offen lds\n"
    "s_add_u32 m0, s54, 56320\n"
    "s_nop 0\n"
    "buffer_load_dwordx4 v81, s[44:47], s55 offen lds\n"
    "s_add_u32 m0, s54, 57344\n"
    "s_nop 0\n"
    "buffer_load_dwordx4 v82, s[44:47], s55 offen lds\n"
    "s_add_u32 m0, s54, 58368\n"
    "s_nop 0\n"
    "buffer_load_dwordx4 v83, s[44:47], s55 offen lds\n"
    "s_add_u32 m0, s54, 59392\n"
    "s_nop 0\n"
    "buffer_load_dwordx4 v84, s[44:47], s55 offen lds\n"
    "s_add_u32 m0, s54, 60416\n"
    "s_nop 0\n"
    "buffer_load_dwordx4 v85, s[44:47], s55 offen lds\n"
    "s_add_u32 m0, s54, 61440\n"
    "s_nop 0\n"
    "buffer_load_dwordx4 v86, s[44:47], s55 offen lds\n"
    "s_add_u32 m0, s54, 62464\n"
    "s_nop 0\n"
    "buffer_load_dwordx4 v87, s[44:47], s55 offen lds\n"
    "s_add_u32 m0, s54, 63488\n"
    "s_nop 0\n"
    "buffer_load_dwordx4 v88, s[44:47], s55 offen lds\n"
    ".Lac3_%=:\n"
    "s_cmp_eq_u32 %2, 3\n"
    "s_cbranch_scc1 .Lac4_%=\n"
    "s_add_u32 s55, s51, 663552\n"
    "s_add_u32 m0, s54, 82944\n"
    "s_nop 0\n"
    "buffer_load_dwordx4 v80, s[44:47], s55 offen lds\n"
    "s_add_u32 m0, s54, 83968\n"
    "s_nop 0\n"
    "buffer_load_dwordx4 v81, s[44:47], s55 offen lds\n"
    "s_add_u32 m0, s54, 84992\n"
    "s_nop 0\n"
    "buffer_load_dwordx4 v82, s[44:47], s55 offen lds\n"
    "s_add_u32 m0, s54, 86016\n"
    "s_nop 0\n"
    "buffer_load_dwordx4 v83, s[44:47], s55 offen lds\n"
    "s_add_u32 m0, s54, 87040\n"
    "s_nop 0\n"
    "buffer_load_dwordx4 v84, s[44:47], s55 offen lds\n"
    "s_add_u32 m0, s54, 88064\n"
    "s_nop 0\n"
    "buffer_load_dwordx4 v85, s[44:47], s55 offen lds\n"
    "s_add_u32 m0, s54, 89088\n"
    "s_nop 0\n"
    "buffer_load_dwordx4 v86, s[44:47], s55 offen lds\n"
    "s_add_u32 m0, s54, 90112\n"
    "s_nop 0\n"
    "buffer_load_dwordx4 v87, s[44:47], s55 offen lds\n"
    "s_add_u32 m0, s54, 91136\n"
    "s_nop 0\n"
    "buffer_load_dwordx4 v88, s[44:47], s55 offen lds\n"
    ".Lac4_%=:\n"
    "s_cmp_eq_u32 %2, 3\n"
    "s_cbranch_scc1 .Lac5_%=\n"
    "s_add_u32 s55, s51, 884736\n"
    "s_add_u32 m0, s54, 110592\n"
    "s_nop 0\n"
    "buffer_load_dwordx4 v80, s[44:47], s55 offen lds\n"
    "s_add_u32 m0, s54, 111616\n"
    "s_nop 0\n"
    "buffer_load_dwordx4 v81, s[44:47], s55 offen lds\n"
    "s_add_u32 m0, s54, 112640\n"
    "s_nop 0\n"
    "buffer_load_dwordx4 v82, s[44:47], s55 offen lds\n"
    "s_add_u32 m0, s54, 113664\n"
    "s_nop 0\n"
    "buffer_load_dwordx4 v83, s[44:47], s55 offen lds\n"
    "s_add_u32 m0, s54, 114688\n"
    "s_nop 0\n"
    "buffer_load_dwordx4 v84, s[44:47], s55 offen lds\n"
    "s_add_u32 m0, s54, 115712\n"
    "s_nop 0\n"
    "buffer_load_dwordx4 v85, s[44:47], s55 offen lds\n"
    "s_add_u32 m0, s54, 116736\n"
    "s_nop 0\n"
    "buffer_load_dwordx4 v86, s[44:47], s55 offen lds\n"
    "s_add_u32 m0, s54, 117760\n"
    "s_nop 0\n"
    "buffer_load_dwordx4 v87, s[44:47], s55 offen lds\n"
    "s_add_u32 m0, s54, 118784\n"
    "s_nop 0\n"
    "buffer_load_dwordx4 v88, s[44:47], s55 offen lds\n"
    ".Lac5_%=:\n"
    "s_waitcnt vmcnt(36) lgkmcnt(0)\n"
    "s_barrier\n"
    "ds_read_b64_tr_b16 a[0:1], v90\n"
    "ds_read_b64_tr_b16 a[2:3], v90 offset:576\n"
    "ds_read_b64_tr_b16 a[72:73], v90 offset:9216\n"
    "ds_read_b64_tr_b16 a[74:75], v90 offset:9792\n"
    "ds_read_b64_tr_b16 a[144:145], v90 offset:18432\n"
    "ds_read_b64_tr_b16 a[146:147], v90 offset:19008\n"
    "ds_read_b64_tr_b16 a[216:217], v91\n"
    "ds_read_b64_tr_b16 a[218:219], v91 offset:576\n"
    "ds_read_b64_tr_b16 a[4:5], v90 offset:4608\n"
    "ds_read_b64_tr_b16 a[6:7], v90 offset:5184\n"
    "ds_read_b64_tr_b16 a[76:77], v90 offset:13824\n"
    "ds_read_b64_tr_b16 a[78:79], v90 offset:14400\n"
    "ds_read_b64_tr_b16 a[148:149], v90 offset:23040\n"
    "ds_read_b64_tr_b16 a[150:151], v90 offset:23616\n"
    "ds_read_b64_tr_b16 a[220:221], v91 offset:4608\n"
    "ds_read_b64_tr_b16 a[222:223], v91 offset:5184\n"
    "s_waitcnt vmcnt(27) lgkmcnt(0)\n"
    "s_barrier\n"
    "s_cmp_eq_u32 %2, 3\n"
    "s_cbranch_scc1 .Lac6_%=\n"
    "s_add_u32 s55, s51, 1105920\n"
    "s_add_u32 m0, s54, 0\n"
    "s_nop 0\n"
    "buffer_load_dwordx4 v80, s[44:47], s55 offen lds\n"
    "s_add_u32 m0, s54, 1024\n"
    "s_nop 0\n"
    "buffer_load_dwordx4 v81, s[44:47], s55 offen lds\n"
    "s_add_u32 m0, s54, 2048\n"
    "s_nop 0\n"
    "buffer_load_dwordx4 v82, s[44:47], s55 offen lds\n"
    "s_add_u32 m0, s54, 3072\n"
    "s_nop 0\n"
    "buffer_load_dwordx4 v83, s[44:47], s55 offen lds\n"
    "s_add_u32 m0, s54, 4096\n"
    "s_nop 0\n"
    "buffer_load_dwordx4 v84, s[44:47], s55 offen lds\n"
    "s_add_u32 m0, s54, 5120\n"
    "s_nop 0\n"
    "buffer_load_dwordx4 v85, s[44:47], s55 offen lds\n"
    "s_add_u32 m0, s54, 6144\n"
    "s_nop 0\n"
    "buffer_load_dwordx4 v86, s[44:47], s55 offen lds\n"
    "s_add_u32 m0, s54, 7168\n"
    "s_nop 0\n"
    "buffer_load_dwordx4 v87, s[44:47], s55 offen lds\n"
    "s_add_u32 m0, s54, 8192\n"
    "s_nop 0\n"
    "buffer_load_dwordx4 v88, s[44:47], s55 offen lds\n"
    ".Lac6_%=:\n"
    "v_mfma_f32_16x16x32_bf16 v[12:15], v[56:59], a[0:3], 0\n"
    "v_mfma_f32_16x16x32_bf16 v[16:19], v[56:59], a[72:75], 0\n"
    "v_mfma_f32_16x16x32_bf16 v[20:23], v[56:59], a[144:147], 0\n"
    "v_mfma_f32_16x16x32_bf16 v[24:27], v[56:59], a[216:219], 0\n"
    "v_mfma_f32_16x16x32_bf16 v[12:15], v[56:59], a[4:7], v[12:15]\n"
    "v_mfma_f32_16x16x32_bf16 v[16:19], v[56:59], a[76:79], v[16:19]\n"
    "v_mfma_f32_16x16x32_bf16 v[20:23], v[56:59], a[148:151], v[20:23]\n"
    "v_mfma_f32_16x16x32_bf16 v[24:27], v[56:59], a[220:223], v[24:27]\n"
    "ds_read_b64_tr_b16 a[8:9], v90 offset:27648\n"
    "ds_read_b64_tr_b16 a[10:11], v90 offset:28224\n"
    "ds_read_b64_tr_b16 a[80:81], v90 offset:36864\n"
    "ds_read_b64_tr_b16 a[82:83], v90 offset:37440\n"
    "ds_read_b64_tr_b16 a[152:153], v90 offset:46080\n"
    "ds_read_b64_tr_b16 a[154:155], v90 offset:46656\n"
    "ds_read_b64_tr_b16 a[224:225], v91 offset:27648\n"
    "ds_read_b64_tr_b16 a[226:227], v91 offset:28224\n"
    "ds_read_b64_tr_b16 a[12:13], v90 offset:32256\n"
    "ds_read_b64_tr_b16 a[14:15], v90 offset:32832\n"
    "ds_read_b64_tr_b16 a[84:85], v90 offset:41472\n"
    "ds_read_b64_tr_b16 a[86:87], v90 offset:42048\n"
    "ds_read_b64_tr_b16 a[156:157], v90 offset:50688\n"
    "ds_read_b64_tr_b16 a[158:159], v90 offset:51264\n"
    "ds_read_b64_tr_b16 a[228:229], v91 offset:32256\n"
    "ds_read_b64_tr_b16 a[230:231], v91 offset:32832\n"
    "s_waitcnt vmcnt(27) lgkmcnt(0)\n"
    "s_barrier\n"
    "s_cmp_eq_u32 %2, 3\n"
    "s_cbranch_scc1 .Lac7_%=\n"
    "s_add_u32 s55, s51, 1327104\n"
    "s_add_u32 m0, s54, 27648\n"
    "s_nop 0\n"
    "buffer_load_dwordx4 v80, s[44:47], s55 offen lds\n"
    "s_add_u32 m0, s54, 28672\n"
    "s_nop 0\n"
    "buffer_load_dwordx4 v81, s[44:47], s55 offen lds\n"
    "s_add_u32 m0, s54, 29696\n"
    "s_nop 0\n"
    "buffer_load_dwordx4 v82, s[44:47], s55 offen lds\n"
    "s_add_u32 m0, s54, 30720\n"
    "s_nop 0\n"
    "buffer_load_dwordx4 v83, s[44:47], s55 offen lds\n"
    "s_add_u32 m0, s54, 31744\n"
    "s_nop 0\n"
    "buffer_load_dwordx4 v84, s[44:47], s55 offen lds\n"
    "s_add_u32 m0, s54, 32768\n"
    "s_nop 0\n"
    "buffer_load_dwordx4 v85, s[44:47], s55 offen lds\n"
    "s_add_u32 m0, s54, 33792\n"
    "s_nop 0\n"
    "buffer_load_dwordx4 v86, s[44:47], s55 offen lds\n"
    "s_add_u32 m0, s54, 34816\n"
    "s_nop 0\n"
    "buffer_load_dwordx4 v87, s[44:47], s55 offen lds\n"
    "s_add_u32 m0, s54, 35840\n"
    "s_nop 0\n"
    "buffer_load_dwordx4 v88, s[44:47], s55 offen lds\n"
    ".Lac7_%=:\n"
    "v_mfma_f32_16x16x32_bf16 v[12:15], v[56:59], a[8:11], v[12:15]\n"
    "v_mfma_f32_16x16x32_bf16 v[16:19], v[56:59], a[80:83], v[16:19]\n"
    "v_mfma_f32_16x16x32_bf16 v[20:23], v[56:59], a[152:155], v[20:23]\n"
    "v_mfma_f32_16x16x32_bf16 v[24:27], v[56:59], a[224:227], v[24:27]\n"
    "v_mfma_f32_16x16x32_bf16 v[12:15], v[56:59], a[12:15], v[12:15]\n"
    "v_mfma_f32_16x16x32_bf16 v[16:19], v[56:59], a[84:87], v[16:19]\n"
    "v_mfma_f32_16x16x32_bf16 v[20:23], v[56:59], a[156:159], v[20:23]\n"
    "v_mfma_f32_16x16x32_bf16 v[24:27], v[56:59], a[228:231], v[24:27]\n"
    "ds_read_b64_tr_b16 a[16:17], v90 offset:55296\n"
    "ds_read_b64_tr_b16 a[18:19], v90 offset:55872\n"
    "ds_read_b64_tr_b16 a[88:89], v90 offset:64512\n"
    "ds_read_b64_tr_b16 a[90:91], v90 offset:65088\n"
    "v_add_u32 v99, 0x10000, v90\n"
    "ds_read_b64_tr_b16 a[160:161], v99 offset:8192\n"
    "ds_read_b64_tr_b16 a[162:163], v99 offset:8768\n"
    "ds_read_b64_tr_b16 a[232:233], v91 offset:55296\n"
    "ds_read_b64_tr_b16 a[234:235], v91 offset:55872\n"
    "ds_read_b64_tr_b16 a[20:21], v90 offset:59904\n"
    "ds_read_b64_tr_b16 a[22:23], v90 offset:60480\n"
    "ds_read_b64_tr_b16 a[92:93], v99 offset:3584\n"
    "ds_read_b64_tr_b16 a[94:95], v99 offset:4160\n"
    "ds_read_b64_tr_b16 a[164:165], v99 offset:12800\n"
    "ds_read_b64_tr_b16 a[166:167], v99 offset:13376\n"
    "ds_read_b64_tr_b16 a[236:237], v91 offset:59904\n"
    "ds_read_b64_tr_b16 a[238:239], v91 offset:60480\n"
    "s_waitcnt vmcnt(27) lgkmcnt(0)\n"
    "s_barrier\n"
    "s_cmp_eq_u32 %2, 3\n"
    "s_cbranch_scc1 .Lac8_%=\n"
    "s_add_u32 s55, s51, 1548288\n"
    "s_add_u32 m0, s54, 55296\n"
    "s_nop 0\n"
    "buffer_load_dwordx4 v80, s[44:47], s55 offen lds\n"
    "s_add_u32 m0, s54, 56320\n"
    "s_nop 0\n"
    "buffer_load_dwordx4 v81, s[44:47], s55 offen lds\n"
    "s_add_u32 m0, s54, 57344\n"
    "s_nop 0\n"
    "buffer_load_dwordx4 v82, s[44:47], s55 offen lds\n"
    "s_add_u32 m0, s54, 58368\n"
    "s_nop 0\n"
    "buffer_load_dwordx4 v83, s[44:47], s55 offen lds\n"
    "s_add_u32 m0, s54, 59392\n"
    "s_nop 0\n"
    "buffer_load_dwordx4 v84, s[44:47], s55 offen lds\n"
    "s_add_u32 m0, s54, 60416\n"
    "s_nop 0\n"
    "buffer_load_dwordx4 v85, s[44:47], s55 offen lds\n"
    "s_add_u32 m0, s54, 61440\n"
    "s_nop 0\n"
    "buffer_load_dwordx4 v86, s[44:47], s55 offen lds\n"
    "s_add_u32 m0, s54, 62464\n"
    "s_nop 0\n"
    "buffer_load_dwordx4 v87, s[44:47], s55 offen lds\n"
    "s_add_u32 m0, s54, 63488\n"
    "s_nop 0\n"
    "buffer_load_dwordx4 v88, s[44:47], s55 offen lds\n"
    ".Lac8_%=:\n"
    "v_mfma_f32_16x16x32_bf16 v[12:15], v[56:59], a[16:19], v[12:15]\n"
    "v_mfma_f32_16x16x32_bf16 v[16:19], v[56:59], a[88:91], v[16:19]\n"
    "v_mfma_f32_16x16x32_bf16 v[20:23], v[56:59], a[160:163], v[20:23]\n"
    "v_mfma_f32_16x16x32_bf16 v[24:27], v[56:59], a[232:235], v[24:27]\n"
    "v_mfma_f32_16x16x32_bf16 v[12:15], v[56:59], a[20:23], v[12:15]\n"
    "v_mfma_f32_16x16x32_bf16 v[16:19], v[56:59], a[92:95], v[16:19]\n"
    "v_mfma_f32_16x16x32_bf16 v[20:23], v[56:59], a[164:167], v[20:23]\n"
    "v_mfma_f32_16x16x32_bf16 v[24:27], v[56:59], a[236:239], v[24:27]\n"
    "ds_read_b64_tr_b16 a[24:25], v99 offset:17408\n"
    "ds_read_b64_tr_b16 a[26:27], v99 offset:17984\n"
    "ds_read_b64_tr_b16 a[96:97], v99 offset:26624\n"
    "ds_read_b64_tr_b16 a[98:99], v99 offset:27200\n"
    "ds_read_b64_tr_b16 a[168:169], v99 offset:35840\n"
    "ds_read_b64_tr_b16 a[170:171], v99 offset:36416\n"
    "v_add_u32 v98, 0x10000, v91\n"
    "ds_read_b64_tr_b16 a[240:241], v98 offset:17408\n"
    "ds_read_b64_tr_b16 a[242:243], v98 offset:17984\n"
    "ds_read_b64_tr_b16 a[28:29], v99 offset:22016\n"
    "ds_read_b64_tr_b16 a[30:31], v99 offset:22592\n"
    "ds_read_b64_tr_b16 a[100:101], v99 offset:31232\n"
    "ds_read_b64_tr_b16 a[102:103], v99 offset:31808\n"
    "ds_read_b64_tr_b16 a[172:173], v99 offset:40448\n"
    "ds_read_b64_tr_b16 a[174:175], v99 offset:41024\n"
    "ds_read_b64_tr_b16 a[244:245], v98 offset:22016\n"
    "ds_read_b64_tr_b16 a[246:247], v98 offset:22592\n"
    "s_waitcnt vmcnt(27) lgkmcnt(0)\n"
    "s_barrier\n"
    "s_cmp_eq_u32 %2, 3\n"
    "s_cbranch_scc1 .Lac9_%=\n"
    "s_add_u32 s55, s51, 1769472\n"
    "s_add_u32 m0, s54, 82944\n"
    "s_nop 0\n"
    "buffer_load_dwordx4 v80, s[44:47], s55 offen lds\n"
    "s_add_u32 m0, s54, 83968\n"
    "s_nop 0\n"
    "buffer_load_dwordx4 v81, s[44:47], s55 offen lds\n"
    "s_add_u32 m0, s54, 84992\n"
    "s_nop 0\n"
    "buffer_load_dwordx4 v82, s[44:47], s55 offen lds\n"
    "s_add_u32 m0, s54, 86016\n"
    "s_nop 0\n"
    "buffer_load_dwordx4 v83, s[44:47], s55 offen lds\n"
    "s_add_u32 m0, s54, 87040\n"
    "s_nop 0\n"
    "buffer_load_dwordx4 v84, s[44:47], s55 offen lds\n"
    "s_add_u32 m0, s54, 88064\n"
    "s_nop 0\n"
    "buffer_load_dwordx4 v85, s[44:47], s55 offen lds\n"
    "s_add_u32 m0, s54, 89088\n"
    "s_nop 0\n"
    "buffer_load_dwordx4 v86, s[44:47], s55 offen lds\n"
    "s_add_u32 m0, s54, 90112\n"
    "s_nop 0\n"
    "buffer_load_dwordx4 v87, s[44:47], s55 offen lds\n"
    "s_add_u32 m0, s54, 91136\n"
    "s_nop 0\n"
    "buffer_load_dwordx4 v88, s[44:47], s55 offen lds\n"
    ".Lac9_%=:\n"
    "v_mov_b32 v11, 0x20400\n"
    "global_load_dwordx2 v[76:77], v11, %1 sc1\n"
    "v_mbcnt_lo_u32_b32 v220, -1, 0\n"
    "v_min_u32 v220, 7, v220\n"
    "v_lshlrev_b32 v220, 7, v220\n"
    "v_add_u32 v220, 0x20000, v220\n"
    "global_load_dwordx2 v[78:79], v220, %1 sc1\n"
    "v_mfma_f32_16x16x32_bf16 v[12:15], v[56:59], a[24:27], v[12:15]\n"
    "v_mfma_f32_16x16x32_bf16 v[16:19], v[56:59], a[96:99], v[16:19]\n"
    "v_mfma_f32_16x16x32_bf16 v[20:23], v[56:59], a[168:171], v[20:23]\n"
    "v_mfma_f32_16x16x32_bf16 v[24:27], v[56:59], a[240:243], v[24:27]\n"
    "v_mfma_f32_16x16x32_bf16 v[12:15], v[56:59], a[28:31], v[12:15]\n"
    "v_mfma_f32_16x16x32_bf16 v[16:19], v[56:59], a[100:103], v[16:19]\n"
    "v_mfma_f32_16x16x32_bf16 v[20:23], v[56:59], a[172:175], v[20:23]\n"
    "v_mfma_f32_16x16x32_bf16 v[24:27], v[56:59], a[244:247], v[24:27]\n"
    "ds_read_b64_tr_b16 a[32:33], v99 offset:45056\n"
    "ds_read_b64_tr_b16 a[34:35], v99 offset:45632\n"
    "ds_read_b64_tr_b16 a[104:105], v99 offset:54272\n"
    "ds_read_b64_tr_b16 a[106:107], v99 offset:54848\n"
    "ds_read_b64_tr_b16 a[176:177], v99 offset:63488\n"
    "ds_read_b64_tr_b16 a[178:179], v99 offset:64064\n"
    "ds_read_b64_tr_b16 a[248:249], v98 offset:45056\n"
    "ds_read_b64_tr_b16 a[250:251], v98 offset:45632\n"
    "ds_read_b64_tr_b16 a[36:37], v99 offset:49664\n"
    "ds_read_b64_tr_b16 a[38:39], v99 offset:50240\n"
    "ds_read_b64_tr_b16 a[108:109], v99 offset:58880\n"
    "ds_read_b64_tr_b16 a[110:111], v99 offset:59456\n"
    "v_add_u32 v97, 0x20000, v90\n"
    "ds_read_b64_tr_b16 a[180:181], v97 offset:2560\n"
    "ds_read_b64_tr_b16 a[182:183], v97 offset:3136\n"
    "ds_read_b64_tr_b16 a[252:253], v98 offset:49664\n"
    "ds_read_b64_tr_b16 a[254:255], v98 offset:50240\n"
    "s_waitcnt vmcnt(29) lgkmcnt(0)\n"
    "s_barrier\n"
    "v_mfma_f32_16x16x32_bf16 v[12:15], v[56:59], a[32:35], v[12:15]\n"
    "v_mfma_f32_16x16x32_bf16 v[16:19], v[56:59], a[104:107], v[16:19]\n"
    "v_mfma_f32_16x16x32_bf16 v[20:23], v[56:59], a[176:179], v[20:23]\n"
    "v_mfma_f32_16x16x32_bf16 v[24:27], v[56:59], a[248:251], v[24:27]\n"
    "v_mfma_f32_16x16x32_bf16 v[12:15], v[56:59], a[36:39], v[12:15]\n"
    "v_mfma_f32_16x16x32_bf16 v[16:19], v[56:59], a[108:111], v[16:19]\n"
    "v_mfma_f32_16x16x32_bf16 v[20:23], v[56:59], a[180:183], v[20:23]\n"
    "v_mfma_f32_16x16x32_bf16 v[24:27], v[56:59], a[252:255], v[24:27]\n"
    "ds_read_b64_tr_b16 a[40:41], v90\n"
    "ds_read_b64_tr_b16 a[42:43], v90 offset:576\n"
    "ds_read_b64_tr_b16 a[112:113], v90 offset:9216\n"
    "ds_read_b64_tr_b16 a[114:115], v90 offset:9792\n"
    "ds_read_b64_tr_b16 a[184:185], v90 offset:18432\n"
    "ds_read_b64_tr_b16 a[186:187], v90 offset:19008\n"
    "ds_read_b64_tr_b16 v[224:225], v91\n"
    "ds_read_b64_tr_b16 v[226:227], v91 offset:576\n"
    "ds_read_b64_tr_b16 a[44:45], v90 offset:4608\n"
    "ds_read_b64_tr_b16 a[46:47], v90 offset:5184\n"
    "ds_read_b64_tr_b16 a[116:117], v90 offset:13824\n"
    "ds_read_b64_tr_b16 a[118:119], v90 offset:14400\n"
    "ds_read_b64_tr_b16 a[188:189], v90 offset:23040\n"
    "ds_read_b64_tr_b16 a[190:191], v90 offset:23616\n"
    "ds_read_b64_tr_b16 v[228:229], v91 offset:4608\n"
    "ds_read_b64_tr_b16 v[230:231], v91 offset:5184\n"
    "s_waitcnt vmcnt(20) lgkmcnt(0)\n"
    "s_barrier\n"
    "v_mfma_f32_16x16x32_bf16 v[12:15], v[56:59], a[40:43], v[12:15]\n"
    "v_mfma_f32_16x16x32_bf16 v[16:19], v[56:59], a[112:115], v[16:19]\n"
    "v_mfma_f32_16x16x32_bf16 v[20:23], v[56:59], a[184:187], v[20:23]\n"
    "v_mfma_f32_16x16x32_bf16 v[24:27], v[56:59], v[224:227], v[24:27]\n"
    "v_mfma_f32_16x16x32_bf16 v[12:15], v[56:59], a[44:47], v[12:15]\n"
    "v_mfma_f32_16x16x32_bf16 v[16:19], v[56:59], a[116:119], v[16:19]\n"
    "v_mfma_f32_16x16x32_bf16 v[20:23], v[56:59], a[188:191], v[20:23]\n"
    "v_mfma_f32_16x16x32_bf16 v[24:27], v[56:59], v[228:231], v[24:27]\n"
    "ds_read_b64_tr_b16 a[48:49], v90 offset:27648\n"
    "ds_read_b64_tr_b16 a[50:51], v90 offset:28224\n"
    "ds_read_b64_tr_b16 a[120:121], v90 offset:36864\n"
    "ds_read_b64_tr_b16 a[122:123], v90 offset:37440\n"
    "ds_read_b64_tr_b16 a[192:193], v90 offset:46080\n"
    "ds_read_b64_tr_b16 a[194:195], v90 offset:46656\n"
    "ds_read_b64_tr_b16 v[232:233], v91 offset:27648\n"
    "ds_read_b64_tr_b16 v[234:235], v91 offset:28224\n"
    "ds_read_b64_tr_b16 a[52:53], v90 offset:32256\n"
    "ds_read_b64_tr_b16 a[54:55], v90 offset:32832\n"
    "ds_read_b64_tr_b16 a[124:125], v90 offset:41472\n"
    "ds_read_b64_tr_b16 a[126:127], v90 offset:42048\n"
    "ds_read_b64_tr_b16 a[196:197], v90 offset:50688\n"
    "ds_read_b64_tr_b16 a[198:199], v90 offset:51264\n"
    "ds_read_b64_tr_b16 v[236:237], v91 offset:32256\n"
    "ds_read_b64_tr_b16 v[238:239], v91 offset:32832\n"
    "s_waitcnt vmcnt(11) lgkmcnt(0)\n"
    "s_barrier\n"
    "v_mfma_f32_16x16x32_bf16 v[12:15], v[56:59], a[48:51], v[12:15]\n"
    "v_mfma_f32_16x16x32_bf16 v[16:19], v[56:59], a[120:123], v[16:19]\n"
    "v_mfma_f32_16x16x32_bf16 v[20:23], v[56:59], a[192:195], v[20:23]\n"
    "v_mfma_f32_16x16x32_bf16 v[24:27], v[56:59], v[232:235], v[24:27]\n"
    "v_mfma_f32_16x16x32_bf16 v[12:15], v[56:59], a[52:55], v[12:15]\n"
    "v_mfma_f32_16x16x32_bf16 v[16:19], v[56:59], a[124:127], v[16:19]\n"
    "v_mfma_f32_16x16x32_bf16 v[20:23], v[56:59], a[196:199], v[20:23]\n"
    "v_mfma_f32_16x16x32_bf16 v[24:27], v[56:59], v[236:239], v[24:27]\n"
    "ds_read_b64_tr_b16 a[56:57], v90 offset:55296\n"
    "ds_read_b64_tr_b16 a[58:59], v90 offset:55872\n"
    "ds_read_b64_tr_b16 a[128:129], v90 offset:64512\n"
    "ds_read_b64_tr_b16 a[130:131], v90 offset:65088\n"
    "ds_read_b64_tr_b16 a[200:201], v99 offset:8192\n"
    "ds_read_b64_tr_b16 a[202:203], v99 offset:8768\n"
    "ds_read_b64_tr_b16 v[240:241], v91 offset:55296\n"
    "ds_read_b64_tr_b16 v[242:243], v91 offset:55872\n"
    "ds_read_b64_tr_b16 a[60:61], v90 offset:59904\n"
    "ds_read_b64_tr_b16 a[62:63], v90 offset:60480\n"
    "ds_read_b64_tr_b16 a[132:133], v99 offset:3584\n"
    "ds_read_b64_tr_b16 a[134:135], v99 offset:4160\n"
    "ds_read_b64_tr_b16 a[204:205], v99 offset:12800\n"
    "ds_read_b64_tr_b16 a[206:207], v99 offset:13376\n"
    "ds_read_b64_tr_b16 v[244:245], v91 offset:59904\n"
    "ds_read_b64_tr_b16 v[246:247], v91 offset:60480\n"
    "s_waitcnt vmcnt(2) lgkmcnt(0)\n"
    "s_barrier\n"
    "v_mfma_f32_16x16x32_bf16 v[12:15], v[56:59], a[56:59], v[12:15]\n"
    "v_mfma_f32_16x16x32_bf16 v[16:19], v[56:59], a[128:131], v[16:19]\n"
    "v_mfma_f32_16x16x32_bf16 v[20:23], v[56:59], a[200:203], v[20:23]\n"
    "v_mfma_f32_16x16x32_bf16 v[24:27], v[56:59], v[240:243], v[24:27]\n"
    "v_mfma_f32_16x16x32_bf16 v[12:15], v[56:59], a[60:63], v[12:15]\n"
    "v_mfma_f32_16x16x32_bf16 v[16:19], v[56:59], a[132:135], v[16:19]\n"
    "v_mfma_f32_16x16x32_bf16 v[20:23], v[56:59], a[204:207], v[20:23]\n"
    "v_mfma_f32_16x16x32_bf16 v[24:27], v[56:59], v[244:247], v[24:27]\n"
    "ds_read_b64_tr_b16 a[64:65], v99 offset:17408\n"
    "ds_read_b64_tr_b16 a[66:67], v99 offset:17984\n"
    "ds_read_b64_tr_b16 a[136:137], v99 offset:26624\n"
    "ds_read_b64_tr_b16 a[138:139], v99 offset:27200\n"
    "ds_read_b64_tr_b16 a[208:209], v99 offset:35840\n"
    "ds_read_b64_tr_b16 a[210:211], v99 offset:36416\n"
    "ds_read_b64_tr_b16 v[248:249], v98 offset:17408\n"
    "ds_read_b64_tr_b16 v[250:251], v98 offset:17984\n"
    "ds_read_b64_tr_b16 a[68:69], v99 offset:22016\n"
    "ds_read_b64_tr_b16 a[70:71], v99 offset:22592\n"
    "ds_read_b64_tr_b16 a[140:141], v99 offset:31232\n"
    "ds_read_b64_tr_b16 a[142:143], v99 offset:31808\n"
    "ds_read_b64_tr_b16 a[212:213], v99 offset:40448\n"
    "ds_read_b64_tr_b16 a[214:215], v99 offset:41024\n"
    "ds_read_b64_tr_b16 v[252:253], v98 offset:22016\n"
    "ds_read_b64_tr_b16 v[254:255], v98 offset:22592\n"
    "s_waitcnt lgkmcnt(0)\n"
    "s_barrier\n"
    "v_mfma_f32_16x16x32_bf16 v[12:15], v[56:59], a[64:67], v[12:15]\n"
    "v_mfma_f32_16x16x32_bf16 v[16:19], v[56:59], a[136:139], v[16:19]\n"
    "v_mfma_f32_16x16x32_bf16 v[20:23], v[56:59], a[208:211], v[20:23]\n"
    "v_mfma_f32_16x16x32_bf16 v[24:27], v[56:59], v[248:251], v[24:27]\n"
    "v_mfma_f32_16x16x32_bf16 v[12:15], v[56:59], a[68:71], v[12:15]\n"
    "v_mfma_f32_16x16x32_bf16 v[16:19], v[56:59], a[140:143], v[16:19]\n"
    "v_mfma_f32_16x16x32_bf16 v[20:23], v[56:59], a[212:215], v[20:23]\n"
    "v_mfma_f32_16x16x32_bf16 v[24:27], v[56:59], v[252:255], v[24:27]\n"
    "v_mov_b32 v28, 0\n"
    "v_mov_b32 v29, 0\n"
    "v_mov_b32 v30, 0\n"
    "v_mov_b32 v31, 0\n"
    "v_mov_b32 v32, 0\n"
    "v_mov_b32 v33, 0\n"
    "v_mov_b32 v34, 0\n"
    "v_mov_b32 v35, 0\n"
    "v_mov_b32 v36, 0\n"
    "v_mov_b32 v37, 0\n"
    "v_mov_b32 v38, 0\n"
    "v_mov_b32 v39, 0\n"
    "v_mov_b32 v172, 0\n"
    "v_mov_b32 v173, 0\n"
    "v_mov_b32 v174, 0\n"
    "v_mov_b32 v175, 0\n"
    "v_mov_b32 v176, 0\n"
    "v_mov_b32 v177, 0\n"
    "v_mov_b32 v178, 0\n"
    "v_mov_b32 v199, 0\n"
    "v_mov_b32 v215, 0\n"
    "v_mov_b32 v218, 0\n"
    "v_mov_b32 v219, 0\n"
    "v_mov_b32 v94, 0xf149f2ca\n"
    "v_add_u32 v93, 7, v95\n"
    "v_cmp_gt_u32 vcc, 8, v93\n"
    "s_nop 1\n"
    "v_cndmask_b32 v44, v94, v28, vcc\n"
    "v_cmp_le_i32 vcc, 9, v95\n"
    "s_nop 1\n"
    "v_cndmask_b32 v68, v94, v28, vcc\n"
    "v_add_u32 v93, 8, v95\n"
    "v_cmp_gt_u32 vcc, 8, v93\n"
    "s_nop 1\n"
    "v_cndmask_b32 v45, v94, v28, vcc\n"
    "v_cmp_le_i32 vcc, 8, v95\n"
    "s_nop 1\n"
    "v_cndmask_b32 v69, v94, v28, vcc\n"
    "v_add_u32 v93, 9, v95\n"
    "v_cmp_gt_u32 vcc, 8, v93\n"
    "s_nop 1\n"
    "v_cndmask_b32 v46, v94, v28, vcc\n"
    "v_cmp_le_i32 vcc, 7, v95\n"
    "s_nop 1\n"
    "v_cndmask_b32 v70, v94, v28, vcc\n"
    "v_add_u32 v93, 10, v95\n"
    "v_cmp_gt_u32 vcc, 8, v93\n"
    "s_nop 1\n"
    "v_cndmask_b32 v47, v94, v28, vcc\n"
    "v_cmp_le_i32 vcc, 6, v95\n"
    "s_nop 1\n"
    "v_cndmask_b32 v71, v94, v28, vcc\n"
    "s_mov_b32 s86, 0xcccccccc\n"
    "s_mov_b32 s87, 0xcccccccc\n"
    "v_cvt_pk_bf16_f32 v13, 0, v12\n"
    "v_sub_f32 v12, v12, v13\n"
    "v_cvt_pk_bf16_f32 v14, 0, v12\n"
    "v_sub_f32 v12, v12, v14\n"
    "v_cvt_pk_bf16_f32 v13, v13, v14\n"
    "v_cvt_pk_bf16_f32 v14, v12, 0\n"
    "s_mov_b64 vcc, 0xffff\n"
    "v_cndmask_b32 v60, 0, v13, vcc\n"
    "v_cndmask_b32 v61, 0, v14, vcc\n"
    "v_cvt_pk_bf16_f32 v17, 0, v16\n"
    "v_sub_f32 v16, v16, v17\n"
    "v_cvt_pk_bf16_f32 v18, 0, v16\n"
    "v_sub_f32 v16, v16, v18\n"
    "v_cvt_pk_bf16_f32 v17, v17, v18\n"
    "v_cvt_pk_bf16_f32 v18, v16, 0\n"
    "s_mov_b64 vcc, 0xffff\n"
    "v_cndmask_b32 v62, 0, v17, vcc\n"
    "v_cndmask_b32 v63, 0, v18, vcc\n"
    "v_cvt_pk_bf16_f32 v21, 0, v20\n"
    "v_sub_f32 v20, v20, v21\n"
    "v_cvt_pk_bf16_f32 v22, 0, v20\n"
    "v_sub_f32 v20, v20, v22\n"
    "v_cvt_pk_bf16_f32 v21, v21, v22\n"
    "v_cvt_pk_bf16_f32 v22, v20, 0\n"
    "s_mov_b64 vcc, 0xffff\n"
    "v_cndmask_b32 v64, 0, v21, vcc\n"
    "v_cndmask_b32 v65, 0, v22, vcc\n"
    "v_cvt_pk_bf16_f32 v25, 0, v24\n"
    "v_sub_f32 v24, v24, v25\n"
    "v_cvt_pk_bf16_f32 v26, 0, v24\n"
    "v_sub_f32 v24, v24, v26\n"
    "v_cvt_pk_bf16_f32 v25, v25, v26\n"
    "v_cvt_pk_bf16_f32 v26, v24, 0\n"
    "s_mov_b64 vcc, 0xffff\n"
    "v_cndmask_b32 v66, 0, v25, vcc\n"
    "v_cndmask_b32 v67, 0, v26, vcc\n"
    "s_waitcnt vmcnt(0)\n"
    "v_add_u32 v76, 1, v76\n"
    "v_add_u32 v77, 1, v77\n"
    "s_mov_b32 s52, 1048576\n"
    ".Lago_a%=:\n"
    "v_cmp_ne_u64 vcc, v[76:77], v[78:79]\n"
    "s_cbranch_vccz .Lago_x%=\n"
    "global_load_dwordx2 v[78:79], v220, %1 sc1\n"
    "s_sub_u32 s52, s52, 1\n"
    "s_waitcnt vmcnt(0)\n"
    "s_cbranch_scc0 .Lago_a%=\n"
    ".Lago_x%=:\n"
    "v_mbcnt_lo_u32_b32 v76, -1, 0\n"
    "v_mbcnt_hi_u32_b32 v76, -1, v76\n"
    "v_min_u32 v220, 5, v76\n"
    "v_lshlrev_b32 v220, 7, v220\n"
    "v_mov_b32 v11, 0x26c00\n"
    "ds_read_b128 v[76:79], v11\n"
    "s_waitcnt lgkmcnt(0)\n"
    "v_readfirstlane_b32 s49, v77\n"
    "v_add_u32 v76, v76, v220\n"
    "s_branch .Lachk_%=\n"
    ".Lago_%=:\n"
    "s_waitcnt vmcnt(1)\n"
    "v_add_u32 v11, 32, v11\n"
    "ds_read_b128 v[76:79], v11\n"
    "ds_read2_b32 v[74:75], v11 offset0:5 offset1:6\n"
    "ds_write_b128 v1, v[152:155]\n"
    "v_mfma_f32_16x16x32_bf16 v[48:51], v[152:155], v[152:155], 0\n"
    "ds_write_b128 v1, v[156:159] offset:1024\n"
    "v_mfma_f32_16x16x32_bf16 v[48:51], v[156:159], v[156:159], v[48:51]\n"
    "ds_write_b128 v1, v[160:163] offset:2048\n"
    "v_mfma_f32_16x16x32_bf16 v[48:51], v[160:163], v[160:163], v[48:51]\n"
    "ds_write_b128 v1, v[164:167] offset:3072\n"
    "v_mfma_f32_16x16x32_bf16 v[48:51], v[164:167], v[164:167], v[48:51]\n"
    "ds_write_b128 v1, v[168:171] offset:4096\n"
    "v_mfma_f32_16x16x32_bf16 v[48:51], v[168:171], v[168:171], v[48:51]\n"
    "s_waitcnt lgkmcnt(5)\n"
    "v_readfirstlane_b32 s50, v75\n"
    "s_cmp_eq_u32 s50, 0\n"
    "s_cbranch_scc1 .Lac10_%=\n"
    "v_swap_b32 v28, v172\n"
    "v_swap_b32 v29, v173\n"
    "v_swap_b32 v30, v174\n"
    "v_swap_b32 v32, v175\n"
    "v_swap_b32 v33, v176\n"
    "v_swap_b32 v36, v177\n"
    "v_swap_b32 v37, v178\n"
    ".Lac10_%=:\n"
    "v_readfirstlane_b32 s49, v77\n"
    "v_readfirstlane_b32 s62, v78\n"
    "v_add_u32 v76, v76, v220\n"
    "s_waitcnt lgkmcnt(0)\n"
    ".Latop_%=:\n"
    "s_waitcnt lgkmcnt(0)\n"
    "s_barrier\n"
    "s_cmp_eq_u32 %2, 1\n"
    "s_cbranch_scc1 .Laq1_%=\n"
    "s_cmp_eq_u32 %2, 2\n"
    "s_cbranch_scc1 .Laq2_%=\n"
    "s_cmp_eq_u32 %2, 3\n"
    "s_cbranch_scc1 .Laq3_%=\n"
    "ds_read_b128 v[80:83], v0\n"
    "ds_read_b128 v[88:91], v0 offset:2048\n"
    "ds_read_b128 v[84:87], v0 offset:1024\n"
    "ds_read_b128 v[100:103], v0 offset:6144\n"
    "ds_read_b128 v[96:99], v0 offset:5120\n"
    "ds_read_b128 v[92:95], v0 offset:3072\n"
    "ds_read_b128 v[112:115], v0 offset:10240\n"
    "ds_read_b128 v[108:111], v0 offset:8192\n"
    "ds_read_b128 v[104:107], v0 offset:7168\n"
    "global_load_dword v77, v76, %1 sc1\n"
    "ds_write_b128 v73, v[48:51] offset:20480\n"
    "v_mfma_f32_16x16x32_bf16 v[52:55], v[56:59], v[152:155], 0\n"
    "v_mfma_f32_16x16x32_bf16 v[52:55], v[56:59], v[156:159], v[52:55]\n"
    "v_mfma_f32_16x16x32_bf16 v[52:55], v[56:59], v[160:163], v[52:55]\n"
    "v_mfma_f32_16x16x32_bf16 v[52:55], v[56:59], v[164:167], v[52:55]\n"
    "v_mfma_f32_16x16x32_bf16 v[52:55], v[56:59], v[168:171], v[52:55]\n"
    "s_waitcnt lgkmcnt(9)\n"
    "v_mfma_f32_16x16x32_bf16 v[24:27], a[216:219], v[80:83], 0\n"
    "ds_read_b128 v[116:119], v0 offset:11264\n"
    "ds_read_b128 v[120:123], v0 offset:12288\n"
    "s_waitcnt lgkmcnt(9)\n"
    "v_mfma_f32_16x16x32_bf16 v[24:27], a[220:223], v[84:87], v[24:27]\n"
    "ds_read_b128 v[124:127], v0 offset:13312\n"
    "ds_read_b128 v[128:131], v0 offset:15360\n"
    "v_mfma_f32_16x16x32_bf16 v[24:27], a[224:227], v[88:91], v[24:27]\n"
    "ds_write_b128 v73, v[52:55] offset:24576\n"
    "ds_read_b128 v[132:135], v0 offset:16384\n"
    "s_waitcnt lgkmcnt(10)\n"
    "v_mfma_f32_16x16x32_bf16 v[24:27], a[228:231], v[92:95], v[24:27]\n"
    "ds_read_b128 v[136:139], v0 offset:17408\n"
    "ds_read_b128 v[140:143], v0 offset:18432\n"
    "v_mfma_f32_16x16x32_bf16 v[24:27], a[232:235], v[96:99], v[24:27]\n"
    "ds_read_b128 v[144:147], v0 offset:4096\n"
    "ds_read_b128 v[148:151], v0 offset:9216\n"
    "v_mfma_f32_16x16x32_bf16 v[24:27], a[236:239], v[100:103], v[24:27]\n"
    "s_waitcnt lgkmcnt(11)\n"
    "v_mfma_f32_16x16x32_bf16 v[24:27], a[240:243], v[104:107], v[24:27]\n"
    "v_mfma_f32_16x16x32_bf16 v[24:27], a[244:247], v[108:111], v[24:27]\n"
    "s_waitcnt lgkmcnt(0)\n"
    "s_barrier\n"
    "v_mfma_f32_16x16x32_bf16 v[24:27], a[248:251], v[112:115], v[24:27]\n"
    "ds_read2st64_b32 v[48:49], v10 offset0:80 offset1:84\n"
    "ds_read2st64_b32 v[50:51], v10 offset0:88 offset1:92\n"
    "v_mfma_f32_16x16x32_bf16 v[12:15], a[0:3], v[80:83], 0\n"
    "ds_read2st64_b32 v[54:55], v10 offset0:104 offset1:108\n"
    "ds_read2st64_b32 v[52:53], v10 offset0:96 offset1:100\n"
    "v_mfma_f32_16x16x32_bf16 v[16:19], a[72:75], v[80:83], 0\n"
    "v_min_f32 v40, v74, v68\n"
    "v_min_f32 v41, v74, v69\n"
    "v_mfma_f32_16x16x32_bf16 v[12:15], a[4:7], v[84:87], v[12:15]\n"
    "v_min_f32 v42, v74, v70\n"
    "v_min_f32 v43, v74, v71\n"
    "v_mfma_f32_16x16x32_bf16 v[16:19], a[76:79], v[84:87], v[16:19]\n"
    "v_mfma_f32_16x16x32_bf16 v[12:15], a[8:11], v[88:91], v[12:15]\n"
    "s_waitcnt lgkmcnt(0)\n"
    "v_add_f32 v52, v52, v53\n"
    "v_add_f32 v54, v54, v55\n"
    "v_mfma_f32_16x16x32_bf16 v[16:19], a[80:83], v[88:91], v[16:19]\n"
    "v_add_f32 v48, v48, v49\n"
    "v_add_f32 v50, v50, v51\n"
    "v_mfma_f32_16x16x32_bf16 v[12:15], a[12:15], v[92:95], v[12:15]\n"
    "v_add_f32 v52, v52, v54\n"
    "v_add_f32 v50, v48, v50\n"
    "v_mfma_f32_16x16x32_bf16 v[16:19], a[84:87], v[92:95], v[16:19]\n"
    "v_mul_f32 v52, 0xbae38e39, v52\n"
    "v_mul_f32 v50, 0x3ae38e39, v50\n"
    "v_mfma_f32_16x16x32_bf16 v[12:15], a[16:19], v[96:99], v[12:15]\n"
    "v_cvt_pk_bf16_f32 v216, v52, v52\n"
    "v_fma_f32 v50, -v52, v52, v50\n"
    "v_mfma_f32_16x16x32_bf16 v[16:19], a[88:91], v[96:99], v[16:19]\n"
    "v_cvt_pk_bf16_f32 v217, v52, v52\n"
    "v_max_f32 v50, 0, v50\n"
    "v_mfma_f32_16x16x32_bf16 v[12:15], a[20:23], v[100:103], v[12:15]\n"
    "v_cvt_pk_bf16_f32 v50, 0, v50\n"
    "v_add_f32 v50, 0x3727c5ac, v50\n"
    "v_mfma_f32_16x16x32_bf16 v[16:19], a[92:95], v[100:103], v[16:19]\n"
    "v_mfma_f32_16x16x32_bf16 v[24:27], v[66:69], v[216:219], v[24:27]\n"
    "v_cvt_pk_bf16_f32 v50, 0, v50\n"
    "v_mfma_f32_16x16x32_bf16 v[12:15], a[24:27], v[104:107], v[12:15]\n"
    "v_sqrt_f32 v50, v50\n"
    "v_mfma_f32_16x16x32_bf16 v[16:19], a[96:99], v[104:107], v[16:19]\n"
    "v_cvt_pk_bf16_f32 v50, 0, v50\n"
    "v_rcp_f32 v50, v50\n"
    "v_mfma_f32_16x16x32_bf16 v[12:15], a[28:31], v[108:111], v[12:15]\n"
    "ds_write_b32 v9, v50\n"
    "v_mfma_f32_16x16x32_bf16 v[16:19], a[100:103], v[108:111], v[16:19]\n"
    "ds_read_b128 v[200:203], v72 offset:28672\n"
    "v_mfma_f32_16x16x32_bf16 v[12:15], v[60:63], v[216:219], v[12:15]\n"
    "v_mfma_f32_16x16x32_bf16 v[12:15], a[32:35], v[112:115], v[12:15]\n"
    "v_mfma_f32_16x16x32_bf16 v[16:19], v[62:65], v[216:219], v[16:19]\n"
    "v_mul_f32 v24, v24, v50\n"
    "v_mfma_f32_16x16x32_bf16 v[16:19], a[104:107], v[112:115], v[16:19]\n"
    "v_mul_f32 v25, v25, v50\n"
    "v_mul_f32 v26, v26, v50\n"
    "v_mfma_f32_16x16x32_bf16 v[12:15], a[36:39], v[116:119], v[12:15]\n"
    "v_mul_f32 v27, v27, v50\n"
    "ds_write_b128 v5, v[24:27]\n"
    "v_mfma_f32_16x16x32_bf16 v[16:19], a[108:111], v[116:119], v[16:19]\n"
    "v_mfma_f32_16x16x32_bf16 v[12:15], a[40:43], v[120:123], v[12:15]\n"
    "v_mfma_f32_16x16x32_bf16 v[16:19], a[112:115], v[120:123], v[16:19]\n"
    "v_mfma_f32_16x16x32_bf16 v[12:15], a[44:47], v[124:127], v[12:15]\n"
    "s_waitcnt vmcnt(0)\n"
    "v_mov_b32 v78, 1\n"
    "v_mfma_f32_16x16x32_bf16 v[16:19], a[116:119], v[124:127], v[16:19]\n"
    "s_mov_b64 exec, s[56:57]\n"
    "ds_add_rtn_u32 v78, v11, v78 offset:16\n"
    "s_mov_b64 exec, -1\n"
    "v_mfma_f32_16x16x32_bf16 v[12:15], a[48:51], v[128:131], v[12:15]\n"
    "s_waitcnt lgkmcnt(0)\n"
    "s_barrier\n"
    "v_mfma_f32_16x16x32_bf16 v[16:19], a[120:123], v[128:131], v[16:19]\n"
    "ds_read_b128 v[188:191], v8\n"
    "ds_read_b128 v[192:195], v8 offset:1024\n"
    "v_mfma_f32_16x16x32_bf16 v[12:15], a[52:55], v[132:135], v[12:15]\n"
    "ds_read2st64_b64 v[184:187], v7 offset1:2\n"
    "ds_read2st64_b64 v[180:183], v6 offset1:2\n"
    "v_cmp_eq_u32 vcc, 0, v77\n"
    "s_cmp_eq_u64 vcc, 0\n"
    "v_mfma_f32_16x16x32_bf16 v[16:19], a[124:127], v[132:135], v[16:19]\n"
    "s_cselect_b32 s94, s42, 0\n"
    "v_mfma_f32_16x16x32_bf16 v[12:15], a[56:59], v[136:139], v[12:15]\n"
    "global_load_dword v77, v76, %1 sc1\n"
    "v_mfma_f32_16x16x32_bf16 v[16:19], a[128:131], v[136:139], v[16:19]\n"
    "buffer_load_dwordx4 v[152:155], v3, s[92:95], s49 offen sc1\n"
    "v_mfma_f32_16x16x32_bf16 v[12:15], a[60:63], v[140:143], v[12:15]\n"
    "buffer_load_dwordx4 v[156:159], v3, s[92:95], s49 offen offset:1024 sc1\n"
    "v_mfma_f32_16x16x32_bf16 v[16:19], a[132:135], v[140:143], v[16:19]\n"
    "v_readfirstlane_b32 s50, v78\n"
    "v_mfma_f32_16x16x32_bf16 v[12:15], a[64:67], v[144:147], v[12:15]\n"
    "buffer_load_dwordx4 v[160:163], v3, s[92:95], s49 offen offset:2048 sc1\n"
    "s_cmp_eq_u32 s50, 3\n"
    "v_mfma_f32_16x16x32_bf16 v[16:19], a[136:139], v[144:147], v[16:19]\n"
    "s_waitcnt lgkmcnt(0)\n"
    "v_add_f32 v180, v180, v182\n"
    "v_add_f32 v181, v181, v183\n"
    "s_cselect_b64 exec, 1, 0\n"
    "global_store_dword v79, v78, %1 sc1\n"
    "s_mov_b64 exec, -1\n"
    "v_mfma_f32_16x16x32_bf16 v[12:15], a[68:71], v[148:151], v[12:15]\n"
    "v_cvt_pk_bf16_f32 v198, v180, v181\n"
    "v_add_f32 v184, v184, v186\n"
    "buffer_load_dwordx4 v[164:167], v3, s[92:95], s49 offen offset:3072 sc1\n"
    "v_mfma_f32_16x16x32_bf16 v[16:19], a[140:143], v[148:151], v[16:19]\n"
    "v_add_f32 v185, v185, v187\n"
    "v_cvt_pk_bf16_f32 v214, v184, v185\n"
    "buffer_load_dwordx4 v[168:171], v4, s[92:95], s49 offen sc1\n"
    "s_branch .Lak_%=\n"
    ".Laq1_%=:\n"
    "ds_read_b128 v[116:119], v0 offset:11264\n"
    "ds_read_b128 v[124:127], v0 offset:13312\n"
    "ds_read_b128 v[120:123], v0 offset:12288\n"
    "ds_read_b128 v[136:139], v0 offset:17408\n"
    "ds_read_b128 v[132:135], v0 offset:16384\n"
    "ds_read_b128 v[128:131], v0 offset:15360\n"
    "ds_read_b128 v[148:151], v0 offset:9216\n"
    "ds_read_b128 v[144:147], v0 offset:4096\n"
    "ds_read_b128 v[140:143], v0 offset:18432\n"
    "global_load_dword v77, v76, %1 sc1\n"
    "ds_write_b128 v73, v[48:51] offset:20480\n"
    "v_mfma_f32_16x16x32_bf16 v[52:55], v[56:59], v[152:155], 0\n"
    "v_mfma_f32_16x16x32_bf16 v[52:55], v[56:59], v[156:159], v[52:55]\n"
    "v_mfma_f32_16x16x32_bf16 v[52:55], v[56:59], v[160:163], v[52:55]\n"
    "v_mfma_f32_16x16x32_bf16 v[52:55], v[56:59], v[164:167], v[52:55]\n"
    "v_mfma_f32_16x16x32_bf16 v[52:55], v[56:59], v[168:171], v[52:55]\n"
    "s_waitcnt lgkmcnt(9)\n"
    "v_mfma_f32_16x16x32_bf16 v[24:27], a[252:255], v[116:119], 0\n"
    "ds_read_b128 v[80:83], v0\n"
    "ds_read_b128 v[84:87], v0 offset:1024\n"
    "s_waitcnt lgkmcnt(9)\n"
    "v_mfma_f32_16x16x32_bf16 v[24:27], v[224:227], v[120:123], v[24:27]\n"
    "ds_read_b128 v[88:91], v0 offset:2048\n"
    "ds_read_b128 v[92:95], v0 offset:3072\n"
    "v_mfma_f32_16x16x32_bf16 v[24:27], v[228:231], v[124:127], v[24:27]\n"
    "ds_write_b128 v73, v[52:55] offset:24576\n"
    "ds_read_b128 v[96:99], v0 offset:5120\n"
    "s_waitcnt lgkmcnt(10)\n"
    "v_mfma_f32_16x16x32_bf16 v[24:27], v[232:235], v[128:131], v[24:27]\n"
    "ds_read_b128 v[100:103], v0 offset:6144\n"
    "ds_read_b128 v[104:107], v0 offset:7168\n"
    "v_mfma_f32_16x16x32_bf16 v[24:27], v[236:239], v[132:135], v[24:27]\n"
    "ds_read_b128 v[108:111], v0 offset:8192\n"
    "ds_read_b128 v[112:115], v0 offset:10240\n"
    "v_mfma_f32_16x16x32_bf16 v[24:27], v[240:243], v[136:139], v[24:27]\n"
    "s_waitcnt lgkmcnt(11)\n"
    "v_mfma_f32_16x16x32_bf16 v[24:27], v[244:247], v[140:143], v[24:27]\n"
    "v_mfma_f32_16x16x32_bf16 v[24:27], v[248:251], v[144:147], v[24:27]\n"
    "s_waitcnt lgkmcnt(0)\n"
    "s_barrier\n"
    "v_mfma_f32_16x16x32_bf16 v[24:27], v[252:255], v[148:151], v[24:27]\n"
    "ds_read2st64_b32 v[48:49], v10 offset0:80 offset1:84\n"
    "ds_read2st64_b32 v[50:51], v10 offset0:88 offset1:92\n"
    "v_mfma_f32_16x16x32_bf16 v[12:15], a[36:39], v[116:119], 0\n"
    "ds_read2st64_b32 v[54:55], v10 offset0:104 offset1:108\n"
    "ds_read2st64_b32 v[52:53], v10 offset0:96 offset1:100\n"
    "v_mfma_f32_16x16x32_bf16 v[16:19], a[108:111], v[116:119], 0\n"
    "v_min_f32 v40, v74, v68\n"
    "v_min_f32 v41, v74, v69\n"
    "v_mfma_f32_16x16x32_bf16 v[12:15], a[40:43], v[120:123], v[12:15]\n"
    "v_min_f32 v42, v74, v70\n"
    "v_min_f32 v43, v74, v71\n"
    "v_mfma_f32_16x16x32_bf16 v[16:19], a[112:115], v[120:123], v[16:19]\n"
    "v_mfma_f32_16x16x32_bf16 v[12:15], a[44:47], v[124:127], v[12:15]\n"
    "s_waitcnt lgkmcnt(0)\n"
    "v_add_f32 v52, v52, v53\n"
    "v_add_f32 v54, v54, v55\n"
    "v_mfma_f32_16x16x32_bf16 v[16:19], a[116:119], v[124:127], v[16:19]\n"
    "v_add_f32 v48, v48, v49\n"
    "v_add_f32 v50, v50, v51\n"
    "v_mfma_f32_16x16x32_bf16 v[12:15], a[48:51], v[128:131], v[12:15]\n"
    "v_add_f32 v52, v52, v54\n"
    "v_add_f32 v50, v48, v50\n"
    "v_mfma_f32_16x16x32_bf16 v[16:19], a[120:123], v[128:131], v[16:19]\n"
    "v_mul_f32 v52, 0xbae38e39, v52\n"
    "v_mul_f32 v50, 0x3ae38e39, v50\n"
    "v_mfma_f32_16x16x32_bf16 v[12:15], a[52:55], v[132:135], v[12:15]\n"
    "v_cvt_pk_bf16_f32 v216, v52, v52\n"
    "v_fma_f32 v50, -v52, v52, v50\n"
    "v_mfma_f32_16x16x32_bf16 v[16:19], a[124:127], v[132:135], v[16:19]\n"
    "v_cvt_pk_bf16_f32 v217, v52, v52\n"
    "v_max_f32 v50, 0, v50\n"
    "v_mfma_f32_16x16x32_bf16 v[12:15], a[56:59], v[136:139], v[12:15]\n"
    "v_cvt_pk_bf16_f32 v50, 0, v50\n"
    "v_add_f32 v50, 0x3727c5ac, v50\n"
    "v_mfma_f32_16x16x32_bf16 v[16:19], a[128:131], v[136:139], v[16:19]\n"
    "v_cvt_pk_bf16_f32 v50, 0, v50\n"
    "v_sqrt_f32 v50, v50\n"
    "v_mfma_f32_16x16x32_bf16 v[12:15], a[60:63], v[140:143], v[12:15]\n"
    "v_cvt_pk_bf16_f32 v50, 0, v50\n"
    "v_mfma_f32_16x16x32_bf16 v[16:19], a[132:135], v[140:143], v[16:19]\n"
    "v_rcp_f32 v50, v50\n"
    "v_mfma_f32_16x16x32_bf16 v[12:15], a[64:67], v[144:147], v[12:15]\n"
    "ds_write_b32 v9, v50\n"
    "ds_read_b128 v[200:203], v72 offset:28672\n"
    "v_mfma_f32_16x16x32_bf16 v[16:19], a[136:139], v[144:147], v[16:19]\n"
    "v_mfma_f32_16x16x32_bf16 v[12:15], v[60:63], v[216:219], v[12:15]\n"
    "v_mfma_f32_16x16x32_bf16 v[16:19], v[62:65], v[216:219], v[16:19]\n"
    "v_mfma_f32_16x16x32_bf16 v[12:15], a[68:71], v[148:151], v[12:15]\n"
    "v_mul_f32 v24, v24, v50\n"
    "v_mul_f32 v25, v25, v50\n"
    "v_mfma_f32_16x16x32_bf16 v[16:19], a[140:143], v[148:151], v[16:19]\n"
    "v_mul_f32 v26, v26, v50\n"
    "v_mul_f32 v27, v27, v50\n"
    "v_mfma_f32_16x16x32_bf16 v[12:15], a[0:3], v[80:83], v[12:15]\n"
    "ds_write_b128 v5, v[24:27]\n"
    "v_mfma_f32_16x16x32_bf16 v[16:19], a[72:75], v[80:83], v[16:19]\n"
    "v_mfma_f32_16x16x32_bf16 v[12:15], a[4:7], v[84:87], v[12:15]\n"
    "v_mfma_f32_16x16x32_bf16 v[16:19], a[76:79], v[84:87], v[16:19]\n"
    "v_mfma_f32_16x16x32_bf16 v[12:15], a[8:11], v[88:91], v[12:15]\n"
    "s_waitcnt vmcnt(0)\n"
    "v_mov_b32 v78, 1\n"
    "v_mfma_f32_16x16x32_bf16 v[16:19], a[80:83], v[88:91], v[16:19]\n"
    "s_mov_b64 exec, s[56:57]\n"
    "ds_add_rtn_u32 v78, v11, v78 offset:16\n"
    "s_mov_b64 exec, -1\n"
    "v_mfma_f32_16x16x32_bf16 v[12:15], a[12:15], v[92:95], v[12:15]\n"
    "s_waitcnt lgkmcnt(0)\n"
    "s_barrier\n"
    "v_mfma_f32_16x16x32_bf16 v[16:19], a[84:87], v[92:95], v[16:19]\n"
    "ds_read_b128 v[188:191], v8\n"
    "ds_read_b128 v[192:195], v8 offset:1024\n"
    "v_mfma_f32_16x16x32_bf16 v[12:15], a[16:19], v[96:99], v[12:15]\n"
    "ds_read2st64_b64 v[184:187], v7 offset1:2\n"
    "ds_read2st64_b64 v[180:183], v6 offset1:2\n"
    "v_cmp_eq_u32 vcc, 0, v77\n"
    "s_cmp_eq_u64 vcc, 0\n"
    "v_mfma_f32_16x16x32_bf16 v[16:19], a[88:91], v[96:99], v[16:19]\n"
    "s_cselect_b32 s94, s42, 0\n"
    "v_mfma_f32_16x16x32_bf16 v[12:15], a[20:23], v[100:103], v[12:15]\n"
    "global_load_dword v77, v76, %1 sc1\n"
    "v_mfma_f32_16x16x32_bf16 v[16:19], a[92:95], v[100:103], v[16:19]\n"
    "buffer_load_dwordx4 v[152:155], v3, s[92:95], s49 offen sc1\n"
    "v_mfma_f32_16x16x32_bf16 v[12:15], a[24:27], v[104:107], v[12:15]\n"
    "buffer_load_dwordx4 v[156:159], v3, s[92:95], s49 offen offset:1024 sc1\n"
    "v_mfma_f32_16x16x32_bf16 v[16:19], a[96:99], v[104:107], v[16:19]\n"
    "v_readfirstlane_b32 s50, v78\n"
    "v_mfma_f32_16x16x32_bf16 v[12:15], a[28:31], v[108:111], v[12:15]\n"
    "buffer_load_dwordx4 v[160:163], v3, s[92:95], s49 offen offset:2048 sc1\n"
    "s_cmp_eq_u32 s50, 3\n"
    "v_mfma_f32_16x16x32_bf16 v[16:19], a[100:103], v[108:111], v[16:19]\n"
    "s_waitcnt lgkmcnt(0)\n"
    "v_add_f32 v180, v180, v182\n"
    "v_add_f32 v181, v181, v183\n"
    "s_cselect_b64 exec, 1, 0\n"
    "global_store_dword v79, v78, %1 sc1\n"
    "s_mov_b64 exec, -1\n"
    "v_mfma_f32_16x16x32_bf16 v[12:15], a[32:35], v[112:115], v[12:15]\n"
    "v_cvt_pk_bf16_f32 v198, v180, v181\n"
    "v_add_f32 v184, v184, v186\n"
    "buffer_load_dwordx4 v[164:167], v3, s[92:95], s49 offen offset:3072 sc1\n"
    "v_mfma_f32_16x16x32_bf16 v[16:19], a[104:107], v[112:115], v[16:19]\n"
    "v_add_f32 v185, v185, v187\n"
    "v_cvt_pk_bf16_f32 v214, v184, v185\n"
    "buffer_load_dwordx4 v[168:171], v4, s[92:95], s49 offen sc1\n"
    "s_branch .Lak_%=\n"
    ".Laq2_%=:\n"
    "ds_read_b128 v[80:83], v0\n"
    "ds_read_b128 v[88:91], v0 offset:2048\n"
    "ds_read_b128 v[84:87], v0 offset:1024\n"
    "ds_read_b128 v[100:103], v0 offset:6144\n"
    "ds_read_b128 v[96:99], v0 offset:5120\n"
    "ds_read_b128 v[92:95], v0 offset:3072\n"
    "ds_read_b128 v[112:115], v0 offset:10240\n"
    "ds_read_b128 v[108:111], v0 offset:8192\n"
    "ds_read_b128 v[104:107], v0 offset:7168\n"
    "global_load_dword v77, v76, %1 sc1\n"
    "ds_write_b128 v73, v[48:51] offset:20480\n"
    "v_mfma_f32_16x16x32_bf16 v[52:55], v[56:59], v[152:155], 0\n"
    "v_mfma_f32_16x16x32_bf16 v[52:55], v[56:59], v[156:159], v[52:55]\n"
    "v_mfma_f32_16x16x32_bf16 v[52:55], v[56:59], v[160:163], v[52:55]\n"
    "v_mfma_f32_16x16x32_bf16 v[52:55], v[56:59], v[164:167], v[52:55]\n"
    "v_mfma_f32_16x16x32_bf16 v[52:55], v[56:59], v[168:171], v[52:55]\n"
    "s_waitcnt lgkmcnt(9)\n"
    "v_mfma_f32_16x16x32_bf16 v[24:27], v[80:83], a[216:219], 0\n"
    "ds_read_b128 v[116:119], v0 offset:11264\n"
    "ds_read_b128 v[120:123], v0 offset:12288\n"
    "s_waitcnt lgkmcnt(9)\n"
    "v_mfma_f32_16x16x32_bf16 v[24:27], v[84:87], a[220:223], v[24:27]\n"
    "ds_read_b128 v[124:127], v0 offset:13312\n"
    "ds_read_b128 v[128:131], v0 offset:15360\n"
    "v_mfma_f32_16x16x32_bf16 v[24:27], v[88:91], a[224:227], v[24:27]\n"
    "ds_write_b128 v73, v[52:55] offset:24576\n"
    "ds_read_b128 v[132:135], v0 offset:16384\n"
    "s_waitcnt lgkmcnt(10)\n"
    "v_mfma_f32_16x16x32_bf16 v[24:27], v[92:95], a[228:231], v[24:27]\n"
    "ds_read_b128 v[136:139], v0 offset:17408\n"
    "ds_read_b128 v[140:143], v0 offset:18432\n"
    "v_mfma_f32_16x16x32_bf16 v[24:27], v[96:99], a[232:235], v[24:27]\n"
    "ds_read_b128 v[144:147], v0 offset:4096\n"
    "ds_read_b128 v[148:151], v0 offset:9216\n"
    "v_mfma_f32_16x16x32_bf16 v[24:27], v[100:103], a[236:239], v[24:27]\n"
    "s_waitcnt lgkmcnt(11)\n"
    "v_mfma_f32_16x16x32_bf16 v[24:27], v[104:107], a[240:243], v[24:27]\n"
    "v_mfma_f32_16x16x32_bf16 v[24:27], v[108:111], a[244:247], v[24:27]\n"
    "s_waitcnt lgkmcnt(0)\n"
    "s_barrier\n"
    "v_mfma_f32_16x16x32_bf16 v[24:27], v[112:115], a[248:251], v[24:27]\n"
    "ds_read2st64_b32 v[48:49], v10 offset0:80 offset1:84\n"
    "ds_read2st64_b32 v[50:51], v10 offset0:88 offset1:92\n"
    "v_mfma_f32_16x16x32_bf16 v[12:15], a[0:3], v[80:83], 0\n"
    "ds_read2st64_b32 v[54:55], v10 offset0:104 offset1:108\n"
    "ds_read2st64_b32 v[52:53], v10 offset0:96 offset1:100\n"
    "v_mfma_f32_16x16x32_bf16 v[16:19], a[72:75], v[80:83], 0\n"
    "v_min_f32 v40, v74, v68\n"
    "v_min_f32 v41, v74, v69\n"
    "v_mfma_f32_16x16x32_bf16 v[12:15], a[4:7], v[84:87], v[12:15]\n"
    "v_min_f32 v42, v74, v70\n"
    "v_min_f32 v43, v74, v71\n"
    "v_mfma_f32_16x16x32_bf16 v[16:19], a[76:79], v[84:87], v[16:19]\n"
    "v_mfma_f32_16x16x32_bf16 v[12:15], a[8:11], v[88:91], v[12:15]\n"
    "s_waitcnt lgkmcnt(0)\n"
    "v_add_f32 v52, v52, v53\n"
    "v_add_f32 v54, v54, v55\n"
    "v_mfma_f32_16x16x32_bf16 v[16:19], a[80:83], v[88:91], v[16:19]\n"
    "v_add_f32 v48, v48, v49\n"
    "v_add_f32 v50, v50, v51\n"
    "v_mfma_f32_16x16x32_bf16 v[12:15], a[12:15], v[92:95], v[12:15]\n"
    "v_add_f32 v52, v52, v54\n"
    "v_add_f32 v50, v48, v50\n"
    "v_mfma_f32_16x16x32_bf16 v[16:19], a[84:87], v[92:95], v[16:19]\n"
    "v_mul_f32 v52, 0xbae38e39, v52\n"
    "v_mul_f32 v50, 0x3ae38e39, v50\n"
    "v_mfma_f32_16x16x32_bf16 v[12:15], a[16:19], v[96:99], v[12:15]\n"
    "v_cvt_pk_bf16_f32 v216, v52, v52\n"
    "v_fma_f32 v50, -v52, v52, v50\n"
    "v_mfma_f32_16x16x32_bf16 v[16:19], a[88:91], v[96:99], v[16:19]\n"
    "v_cvt_pk_bf16_f32 v217, v52, v52\n"
    "v_max_f32 v50, 0, v50\n"
    "v_mfma_f32_16x16x32_bf16 v[12:15], a[20:23], v[100:103], v[12:15]\n"
    "v_cvt_pk_bf16_f32 v50, 0, v50\n"
    "v_add_f32 v50, 0x3727c5ac, v50\n"
    "v_mfma_f32_16x16x32_bf16 v[16:19], a[92:95], v[100:103], v[16:19]\n"
    "v_mfma_f32_16x16x32_bf16 v[24:27], v[216:219], v[66:69], v[24:27]\n"
    "v_cvt_pk_bf16_f32 v50, 0, v50\n"
    "v_mfma_f32_16x16x32_bf16 v[12:15], a[24:27], v[104:107], v[12:15]\n"
    "v_sqrt_f32 v50, v50\n"
    "v_mfma_f32_16x16x32_bf16 v[16:19], a[96:99], v[104:107], v[16:19]\n"
    "v_cvt_pk_bf16_f32 v50, 0, v50\n"
    "v_rcp_f32 v50, v50\n"
    "v_mfma_f32_16x16x32_bf16 v[12:15], a[28:31], v[108:111], v[12:15]\n"
    "ds_write_b32 v9, v50\n"
    "v_mfma_f32_16x16x32_bf16 v[16:19], a[100:103], v[108:111], v[16:19]\n"
    "ds_read_b128 v[200:203], v72 offset:28672\n"
    "v_mfma_f32_16x16x32_bf16 v[12:15], v[60:63], v[216:219], v[12:15]\n"
    "v_mfma_f32_16x16x32_bf16 v[12:15], a[32:35], v[112:115], v[12:15]\n"
    "v_mfma_f32_16x16x32_bf16 v[16:19], v[62:65], v[216:219], v[16:19]\n"
    "v_mfma_f32_16x16x32_bf16 v[16:19], a[104:107], v[112:115], v[16:19]\n"
    "v_mfma_f32_16x16x32_bf16 v[12:15], a[36:39], v[116:119], v[12:15]\n"
    "v_mfma_f32_16x16x32_bf16 v[16:19], a[108:111], v[116:119], v[16:19]\n"
    "s_waitcnt lgkmcnt(0)\n"
    "v_mul_f32 v24, v24, v200\n"
    "v_mul_f32 v25, v25, v201\n"
    "v_mfma_f32_16x16x32_bf16 v[12:15], a[40:43], v[120:123], v[12:15]\n"
    "v_mul_f32 v26, v26, v202\n"
    "v_mul_f32 v27, v27, v203\n"
    "v_mfma_f32_16x16x32_bf16 v[16:19], a[112:115], v[120:123], v[16:19]\n"
    "ds_write_b128 v5, v[24:27]\n"
    "v_mfma_f32_16x16x32_bf16 v[12:15], a[44:47], v[124:127], v[12:15]\n"
    "s_waitcnt vmcnt(0)\n"
    "v_mov_b32 v78, 1\n"
    "v_mfma_f32_16x16x32_bf16 v[16:19], a[116:119], v[124:127], v[16:19]\n"
    "s_mov_b64 exec, s[56:57]\n"
    "ds_add_rtn_u32 v78, v11, v78 offset:16\n"
    "s_mov_b64 exec, -1\n"
    "v_mfma_f32_16x16x32_bf16 v[12:15], a[48:51], v[128:131], v[12:15]\n"
    "s_waitcnt lgkmcnt(0)\n"
    "s_barrier\n"
    "v_mfma_f32_16x16x32_bf16 v[16:19], a[120:123], v[128:131], v[16:19]\n"
    "ds_read_b128 v[188:191], v8\n"
    "ds_read_b128 v[192:195], v8 offset:1024\n"
    "v_mfma_f32_16x16x32_bf16 v[12:15], a[52:55], v[132:135], v[12:15]\n"
    "ds_read2st64_b64 v[184:187], v7 offset1:2\n"
    "ds_read2st64_b64 v[180:183], v6 offset1:2\n"
    "v_cmp_eq_u32 vcc, 0, v77\n"
    "s_cmp_eq_u64 vcc, 0\n"
    "v_mfma_f32_16x16x32_bf16 v[16:19], a[124:127], v[132:135], v[16:19]\n"
    "s_cselect_b32 s94, s42, 0\n"
    "v_mfma_f32_16x16x32_bf16 v[12:15], a[56:59], v[136:139], v[12:15]\n"
    "global_load_dword v77, v76, %1 sc1\n"
    "v_mfma_f32_16x16x32_bf16 v[16:19], a[128:131], v[136:139], v[16:19]\n"
    "buffer_load_dwordx4 v[152:155], v3, s[92:95], s49 offen sc1\n"
    "v_mfma_f32_16x16x32_bf16 v[12:15], a[60:63], v[140:143], v[12:15]\n"
    "buffer_load_dwordx4 v[156:159], v3, s[92:95], s49 offen offset:1024 sc1\n"
    "v_mfma_f32_16x16x32_bf16 v[16:19], a[132:135], v[140:143], v[16:19]\n"
    "v_readfirstlane_b32 s50, v78\n"
    "v_mfma_f32_16x16x32_bf16 v[12:15], a[64:67], v[144:147], v[12:15]\n"
    "buffer_load_dwordx4 v[160:163], v3, s[92:95], s49 offen offset:2048 sc1\n"
    "s_cmp_eq_u32 s50, 3\n"
    "v_mfma_f32_16x16x32_bf16 v[16:19], a[136:139], v[144:147], v[16:19]\n"
    "s_waitcnt lgkmcnt(0)\n"
    "v_add_f32 v180, v180, v182\n"
    "v_add_f32 v181, v181, v183\n"
    "s_cselect_b64 exec, 1, 0\n"
    "global_store_dword v79, v78, %1 sc1\n"
    "s_mov_b64 exec, -1\n"
    "v_mfma_f32_16x16x32_bf16 v[12:15], a[68:71], v[148:151], v[12:15]\n"
    "v_cvt_pk_bf16_f32 v198, v180, v181\n"
    "v_add_f32 v184, v184, v186\n"
    "buffer_load_dwordx4 v[164:167], v3, s[92:95], s49 offen offset:3072 sc1\n"
    "v_mfma_f32_16x16x32_bf16 v[16:19], a[140:143], v[148:151], v[16:19]\n"
    "v_add_f32 v185, v185, v187\n"
    "v_cvt_pk_bf16_f32 v214, v184, v185\n"
    "buffer_load_dwordx4 v[168:171], v4, s[92:95], s49 offen sc1\n"
    "s_branch .Lak_%=\n"
    ".Laq3_%=:\n"
    "ds_read_b128 v[116:119], v0 offset:11264\n"
    "ds_read_b128 v[124:127], v0 offset:13312\n"
    "ds_read_b128 v[120:123], v0 offset:12288\n"
    "ds_read_b128 v[136:139], v0 offset:17408\n"
    "ds_read_b128 v[132:135], v0 offset:16384\n"
    "ds_read_b128 v[128:131], v0 offset:15360\n"
    "ds_read_b128 v[148:151], v0 offset:9216\n"
    "ds_read_b128 v[144:147], v0 offset:4096\n"
    "ds_read_b128 v[140:143], v0 offset:18432\n"
    "global_load_dword v77, v76, %1 sc1\n"
    "ds_write_b128 v73, v[48:51] offset:20480\n"
    "v_mfma_f32_16x16x32_bf16 v[52:55], v[56:59], v[152:155], 0\n"
    "v_mfma_f32_16x16x32_bf16 v[52:55], v[56:59], v[156:159], v[52:55]\n"
    "v_mfma_f32_16x16x32_bf16 v[52:55], v[56:59], v[160:163], v[52:55]\n"
    "v_mfma_f32_16x16x32_bf16 v[52:55], v[56:59], v[164:167], v[52:55]\n"
    "v_mfma_f32_16x16x32_bf16 v[52:55], v[56:59], v[168:171], v[52:55]\n"
    "s_waitcnt lgkmcnt(9)\n"
    "v_mfma_f32_16x16x32_bf16 v[24:27], v[116:119], a[252:255], 0\n"
    "ds_read_b128 v[80:83], v0\n"
    "ds_read_b128 v[84:87], v0 offset:1024\n"
    "s_waitcnt lgkmcnt(9)\n"
    "v_mfma_f32_16x16x32_bf16 v[24:27], v[120:123], v[224:227], v[24:27]\n"
    "ds_read_b128 v[88:91], v0 offset:2048\n"
    "ds_read_b128 v[92:95], v0 offset:3072\n"
    "v_mfma_f32_16x16x32_bf16 v[24:27], v[124:127], v[228:231], v[24:27]\n"
    "ds_write_b128 v73, v[52:55] offset:24576\n"
    "ds_read_b128 v[96:99], v0 offset:5120\n"
    "s_waitcnt lgkmcnt(10)\n"
    "v_mfma_f32_16x16x32_bf16 v[24:27], v[128:131], v[232:235], v[24:27]\n"
    "ds_read_b128 v[100:103], v0 offset:6144\n"
    "ds_read_b128 v[104:107], v0 offset:7168\n"
    "v_mfma_f32_16x16x32_bf16 v[24:27], v[132:135], v[236:239], v[24:27]\n"
    "ds_read_b128 v[108:111], v0 offset:8192\n"
    "ds_read_b128 v[112:115], v0 offset:10240\n"
    "v_mfma_f32_16x16x32_bf16 v[24:27], v[136:139], v[240:243], v[24:27]\n"
    "s_waitcnt lgkmcnt(11)\n"
    "v_mfma_f32_16x16x32_bf16 v[24:27], v[140:143], v[244:247], v[24:27]\n"
    "v_mfma_f32_16x16x32_bf16 v[24:27], v[144:147], v[248:251], v[24:27]\n"
    "s_waitcnt lgkmcnt(0)\n"
    "s_barrier\n"
    "v_mfma_f32_16x16x32_bf16 v[24:27], v[148:151], v[252:255], v[24:27]\n"
    "ds_read2st64_b32 v[48:49], v10 offset0:80 offset1:84\n"
    "ds_read2st64_b32 v[50:51], v10 offset0:88 offset1:92\n"
    "v_mfma_f32_16x16x32_bf16 v[12:15], a[36:39], v[116:119], 0\n"
    "ds_read2st64_b32 v[54:55], v10 offset0:104 offset1:108\n"
    "ds_read2st64_b32 v[52:53], v10 offset0:96 offset1:100\n"
    "v_mfma_f32_16x16x32_bf16 v[16:19], a[108:111], v[116:119], 0\n"
    "v_min_f32 v40, v74, v68\n"
    "v_min_f32 v41, v74, v69\n"
    "v_mfma_f32_16x16x32_bf16 v[12:15], a[40:43], v[120:123], v[12:15]\n"
    "v_min_f32 v42, v74, v70\n"
    "v_min_f32 v43, v74, v71\n"
    "v_mfma_f32_16x16x32_bf16 v[16:19], a[112:115], v[120:123], v[16:19]\n"
    "v_mfma_f32_16x16x32_bf16 v[12:15], a[44:47], v[124:127], v[12:15]\n"
    "s_waitcnt lgkmcnt(0)\n"
    "v_add_f32 v52, v52, v53\n"
    "v_add_f32 v54, v54, v55\n"
    "v_mfma_f32_16x16x32_bf16 v[16:19], a[116:119], v[124:127], v[16:19]\n"
    "v_add_f32 v48, v48, v49\n"
    "v_add_f32 v50, v50, v51\n"
    "v_mfma_f32_16x16x32_bf16 v[12:15], a[48:51], v[128:131], v[12:15]\n"
    "v_add_f32 v52, v52, v54\n"
    "v_add_f32 v50, v48, v50\n"
    "v_mfma_f32_16x16x32_bf16 v[16:19], a[120:123], v[128:131], v[16:19]\n"
    "v_mul_f32 v52, 0xbae38e39, v52\n"
    "v_mul_f32 v50, 0x3ae38e39, v50\n"
    "v_mfma_f32_16x16x32_bf16 v[12:15], a[52:55], v[132:135], v[12:15]\n"
    "v_cvt_pk_bf16_f32 v216, v52, v52\n"
    "v_fma_f32 v50, -v52, v52, v50\n"
    "v_mfma_f32_16x16x32_bf16 v[16:19], a[124:127], v[132:135], v[16:19]\n"
    "v_cvt_pk_bf16_f32 v217, v52, v52\n"
    "v_max_f32 v50, 0, v50\n"
    "v_mfma_f32_16x16x32_bf16 v[12:15], a[56:59], v[136:139], v[12:15]\n"
    "v_cvt_pk_bf16_f32 v50, 0, v50\n"
    "v_add_f32 v50, 0x3727c5ac, v50\n"
    "v_mfma_f32_16x16x32_bf16 v[16:19], a[128:131], v[136:139], v[16:19]\n"
    "v_cvt_pk_bf16_f32 v50, 0, v50\n"
    "v_sqrt_f32 v50, v50\n"
    "v_mfma_f32_16x16x32_bf16 v[12:15], a[60:63], v[140:143], v[12:15]\n"
    "v_cvt_pk_bf16_f32 v50, 0, v50\n"
    "v_mfma_f32_16x16x32_bf16 v[16:19], a[132:135], v[140:143], v[16:19]\n"
    "v_rcp_f32 v50, v50\n"
    "v_mfma_f32_16x16x32_bf16 v[12:15], a[64:67], v[144:147], v[12:15]\n"
    "ds_write_b32 v9, v50\n"
    "ds_read_b128 v[200:203], v72 offset:28672\n"
    "v_mfma_f32_16x16x32_bf16 v[16:19], a[136:139], v[144:147], v[16:19]\n"
    "v_mfma_f32_16x16x32_bf16 v[12:15], v[60:63], v[216:219], v[12:15]\n"
    "v_mfma_f32_16x16x32_bf16 v[16:19], v[62:65], v[216:219], v[16:19]\n"
    "v_mfma_f32_16x16x32_bf16 v[12:15], a[68:71], v[148:151], v[12:15]\n"
    "v_mfma_f32_16x16x32_bf16 v[16:19], a[140:143], v[148:151], v[16:19]\n"
    "v_mfma_f32_16x16x32_bf16 v[12:15], a[0:3], v[80:83], v[12:15]\n"
    "s_waitcnt lgkmcnt(0)\n"
    "v_mul_f32 v24, v24, v200\n"
    "v_mul_f32 v25, v25, v201\n"
    "v_mfma_f32_16x16x32_bf16 v[16:19], a[72:75], v[80:83], v[16:19]\n"
    "v_mul_f32 v26, v26, v202\n"
    "v_mul_f32 v27, v27, v203\n"
    "v_mfma_f32_16x16x32_bf16 v[12:15], a[4:7], v[84:87], v[12:15]\n"
    "ds_write_b128 v5, v[24:27]\n"
    "v_mfma_f32_16x16x32_bf16 v[16:19], a[76:79], v[84:87], v[16:19]\n"
    "v_mfma_f32_16x16x32_bf16 v[12:15], a[8:11], v[88:91], v[12:15]\n"
    "s_waitcnt vmcnt(0)\n"
    "v_mov_b32 v78, 1\n"
    "v_mfma_f32_16x16x32_bf16 v[16:19], a[80:83], v[88:91], v[16:19]\n"
    "s_mov_b64 exec, s[56:57]\n"
    "ds_add_rtn_u32 v78, v11, v78 offset:16\n"
    "s_mov_b64 exec, -1\n"
    "v_mfma_f32_16x16x32_bf16 v[12:15], a[12:15], v[92:95], v[12:15]\n"
    "s_waitcnt lgkmcnt(0)\n"
    "s_barrier\n"
    "v_mfma_f32_16x16x32_bf16 v[16:19], a[84:87], v[92:95], v[16:19]\n"
    "ds_read_b128 v[188:191], v8\n"
    "ds_read_b128 v[192:195], v8 offset:1024\n"
    "v_mfma_f32_16x16x32_bf16 v[12:15], a[16:19], v[96:99], v[12:15]\n"
    "ds_read2st64_b64 v[184:187], v7 offset1:2\n"
    "ds_read2st64_b64 v[180:183], v6 offset1:2\n"
    "v_cmp_eq_u32 vcc, 0, v77\n"
    "s_cmp_eq_u64 vcc, 0\n"
    "v_mfma_f32_16x16x32_bf16 v[16:19], a[88:91], v[96:99], v[16:19]\n"
    "s_cselect_b32 s94, s42, 0\n"
    "v_mfma_f32_16x16x32_bf16 v[12:15], a[20:23], v[100:103], v[12:15]\n"
    "global_load_dword v77, v76, %1 sc1\n"
    "v_mfma_f32_16x16x32_bf16 v[16:19], a[92:95], v[100:103], v[16:19]\n"
    "buffer_load_dwordx4 v[152:155], v3, s[92:95], s49 offen sc1\n"
    "v_mfma_f32_16x16x32_bf16 v[12:15], a[24:27], v[104:107], v[12:15]\n"
    "buffer_load_dwordx4 v[156:159], v3, s[92:95], s49 offen offset:1024 sc1\n"
    "v_mfma_f32_16x16x32_bf16 v[16:19], a[96:99], v[104:107], v[16:19]\n"
    "v_readfirstlane_b32 s50, v78\n"
    "v_mfma_f32_16x16x32_bf16 v[12:15], a[28:31], v[108:111], v[12:15]\n"
    "buffer_load_dwordx4 v[160:163], v3, s[92:95], s49 offen offset:2048 sc1\n"
    "s_cmp_eq_u32 s50, 3\n"
    "v_mfma_f32_16x16x32_bf16 v[16:19], a[100:103], v[108:111], v[16:19]\n"
    "s_waitcnt lgkmcnt(0)\n"
    "v_add_f32 v180, v180, v182\n"
    "v_add_f32 v181, v181, v183\n"
    "s_cselect_b64 exec, 1, 0\n"
    "global_store_dword v79, v78, %1 sc1\n"
    "s_mov_b64 exec, -1\n"
    "v_mfma_f32_16x16x32_bf16 v[12:15], a[32:35], v[112:115], v[12:15]\n"
    "v_cvt_pk_bf16_f32 v198, v180, v181\n"
    "v_add_f32 v184, v184, v186\n"
    "buffer_load_dwordx4 v[164:167], v3, s[92:95], s49 offen offset:3072 sc1\n"
    "v_mfma_f32_16x16x32_bf16 v[16:19], a[104:107], v[112:115], v[16:19]\n"
    "v_add_f32 v185, v185, v187\n"
    "v_cvt_pk_bf16_f32 v214, v184, v185\n"
    "buffer_load_dwordx4 v[168:171], v4, s[92:95], s49 offen sc1\n"
    ".Lak_%=:\n"
    "v_mfma_f32_16x16x32_bf16 v[20:23], v[216:219], v[64:67], 0\n"
    "v_add_f32 v188, v188, v192\n"
    "v_add_f32 v189, v189, v193\n"
    "v_mfma_f32_16x16x32_bf16 v[20:23], v[80:83], a[144:147], v[20:23]\n"
    "v_add_f32 v190, v190, v194\n"
    "v_add_f32 v191, v191, v195\n"
    "v_mfma_f32_16x16x32_bf16 v[20:23], v[84:87], a[148:151], v[20:23]\n"
    "v_mul_f32 v12, v12, v50\n"
    "v_mul_f32 v13, v13, v50\n"
    "v_mfma_f32_16x16x32_bf16 v[20:23], v[88:91], a[152:155], v[20:23]\n"
    "v_mul_f32 v14, v14, v50\n"
    "v_mul_f32 v15, v15, v50\n"
    "v_mfma_f32_16x16x32_bf16 v[20:23], v[92:95], a[156:159], v[20:23]\n"
    "v_cvt_pk_bf16_f32 v196, v12, v13\n"
    "v_cvt_pk_bf16_f32 v197, v14, v15\n"
    "v_mfma_f32_16x16x32_bf16 v[20:23], v[96:99], a[160:163], v[20:23]\n"
    "v_mul_f32 v16, v16, v50\n"
    "v_mul_f32 v17, v17, v50\n"
    "v_mfma_f32_16x16x32_bf16 v[20:23], v[100:103], a[164:167], v[20:23]\n"
    "v_mfma_f32_16x16x32_bf16 v[204:207], v[28:31], v[196:199], v[40:43]\n"
    "v_mul_f32 v18, v18, v50\n"
    "v_mfma_f32_16x16x32_bf16 v[20:23], v[104:107], a[168:171], v[20:23]\n"
    "v_mul_f32 v19, v19, v50\n"
    "v_cvt_pk_bf16_f32 v212, v16, v17\n"
    "v_mfma_f32_16x16x32_bf16 v[20:23], v[108:111], a[172:175], v[20:23]\n"
    "v_cvt_pk_bf16_f32 v213, v18, v19\n"
    "v_cvt_pk_bf16_f32 v38, v188, v189\n"
    "v_mfma_f32_16x16x32_bf16 v[20:23], v[112:115], a[176:179], v[20:23]\n"
    "v_cvt_pk_bf16_f32 v39, v190, v191\n"
    "v_mfma_f32_16x16x32_bf16 v[208:211], v[212:215], v[196:199], v[44:47]\n"
    "v_mfma_f32_16x16x32_bf16 v[20:23], v[116:119], a[180:183], v[20:23]\n"
    "v_cndmask_b32 v38, v38, v56, s[86:87]\n"
    "v_cndmask_b32 v39, v39, v56, s[86:87]\n"
    "v_mfma_f32_16x16x32_bf16 v[20:23], v[120:123], a[184:187], v[20:23]\n"
    "v_maximum3_f32 v188, v204, v205, v206\n"
    "v_mov_b32 v28, v212\n"
    "v_mfma_f32_16x16x32_bf16 v[20:23], v[124:127], a[188:191], v[20:23]\n"
    "v_mov_b32 v29, v213\n"
    "v_mov_b32 v30, v214\n"
    "v_mfma_f32_16x16x32_bf16 v[20:23], v[128:131], a[192:195], v[20:23]\n"
    "v_maximum3_f32 v188, v188, v207, v208\n"
    "v_maximum3_f32 v188, v188, v209, v210\n"
    "v_mfma_f32_16x16x32_bf16 v[20:23], v[132:135], a[196:199], v[20:23]\n"
    "v_maximum3_f32 v188, v188, v211, v211\n"
    "v_mov_b32 v189, v188\n"
    "v_mfma_f32_16x16x32_bf16 v[20:23], v[136:139], a[200:203], v[20:23]\n"
    "s_nop 0\n"
    "v_permlane16_swap_b32 v189, v188\n"
    "v_maximum3_f32 v188, v188, v189, v189\n"
    "v_mfma_f32_16x16x32_bf16 v[20:23], v[140:143], a[204:207], v[20:23]\n"
    "v_mov_b32 v189, v188\n"
    "v_mfma_f32_16x16x32_bf16 v[20:23], v[144:147], a[208:211], v[20:23]\n"
    "s_nop 0\n"
    "v_permlane32_swap_b32 v189, v188\n"
    "v_maximum3_f32 v188, v188, v189, v189\n"
    "v_mfma_f32_16x16x32_bf16 v[20:23], v[148:151], a[212:215], v[20:23]\n"
    "v_mul_f32 v188, 0xbeae1a93, v188\n"
    "v_fmamk_f32 v204, v204, 0x3eae1a93, v188\n"
    "v_fmamk_f32 v205, v205, 0x3eae1a93, v188\n"
    "v_fmamk_f32 v206, v206, 0x3eae1a93, v188\n"
    "v_fmamk_f32 v207, v207, 0x3eae1a93, v188\n"
    "v_fmamk_f32 v208, v208, 0x3eae1a93, v188\n"
    "v_fmamk_f32 v209, v209, 0x3eae1a93, v188\n"
    "v_fmamk_f32 v210, v210, 0x3eae1a93, v188\n"
    "v_fmamk_f32 v211, v211, 0x3eae1a93, v188\n"
    "v_exp_f32 v204, v204\n"
    "v_exp_f32 v205, v205\n"
    "v_exp_f32 v206, v206\n"
    "v_exp_f32 v207, v207\n"
    "v_exp_f32 v208, v208\n"
    "v_exp_f32 v209, v209\n"
    "v_exp_f32 v210, v210\n"
    "v_exp_f32 v211, v211\n"
    "v_mul_f32 v20, v20, v200\n"
    "v_mul_f32 v21, v21, v201\n"
    "v_mul_f32 v22, v22, v202\n"
    "v_mul_f32 v23, v23, v203\n"
    "v_cvt_pk_bf16_f32 v204, v204, v205\n"
    "v_cvt_pk_bf16_f32 v205, v206, v207\n"
    "v_cvt_pk_bf16_f32 v206, v208, v209\n"
    "v_cvt_pk_bf16_f32 v207, v210, v211\n"
    "v_cvt_pk_bf16_f32 v34, v20, v21\n"
    "v_cvt_pk_bf16_f32 v35, v22, v23\n"
    "v_mfma_f32_16x16x32_bf16 v[184:187], v[36:39], v[204:207], 0\n"
    "s_nop 0\n"
    "v_mfma_f32_16x16x32_bf16 v[180:183], v[32:35], v[204:207], 0\n"
    "v_mov_b64 v[36:37], v[38:39]\n"
    "v_mov_b64 v[32:33], v[34:35]\n"
    "s_nop 2\n"
    "v_rcp_f32 v188, v186\n"
    "s_nop 0\n"
    "v_mul_f32 v184, v184, v188\n"
    "v_mul_f32 v185, v185, v188\n"
    "v_cvt_pk_bf16_f32 v184, v184, v185\n"
    "v_pk_mul_f32 v[180:181], v[180:181], v[188:189] op_sel_hi:[1,0]\n"
    "v_pk_mul_f32 v[182:183], v[182:183], v[188:189] op_sel_hi:[1,0]\n"
    "v_cvt_pk_bf16_f32 v204, v180, v181\n"
    "v_cvt_pk_bf16_f32 v205, v182, v183\n"
    "v_cvt_pk_bf16_f32 v206, v180, v181\n"
    "v_cvt_pk_bf16_f32 v207, v182, v183\n"
    "s_nop 0\n"
    "v_permlane16_swap_b32 v204, v206\n"
    "v_permlane16_swap_b32 v205, v207\n"
    "s_nop 0\n"
    "v_mov_b32_dpp v204, v184 quad_perm:[0,0,0,0] row_mask:0x2 bank_mask:0xf\n"
    "v_mov_b32_dpp v205, v184 quad_perm:[1,1,1,1] row_mask:0x2 bank_mask:0xf\n"
    "v_mov_b32_dpp v206, v184 quad_perm:[2,2,2,2] row_mask:0x2 bank_mask:0xf\n"
    "v_mov_b32_dpp v207, v184 quad_perm:[3,3,3,3] row_mask:0x2 bank_mask:0xf\n"
    "buffer_store_dwordx4 v[204:207], v2, s[40:43], s62 offen sc1\n"
    "s_mov_b32 s56, 1\n"
    "s_cmp_lg_u32 s94, 0\n"
    "s_cbranch_scc1 .Lago_%=\n"
    "s_waitcnt vmcnt(1)\n"
    "v_cmp_eq_u32 vcc, 0, v77\n"
    "s_cmp_eq_u64 vcc, 0\n"
    "s_cbranch_scc1 .Lald_%=\n"
    "ds_read_b32 v79, v11 offset:44\n"
    "global_load_dword v77, v76, %1 sc1\n"
    "s_waitcnt vmcnt(0)\n"
    "v_mov_b32 v78, 1\n"
    "s_mov_b64 exec, 1\n"
    "ds_add_rtn_u32 v78, v11, v78 offset:48\n"
    "s_mov_b64 exec, -1\n"
    "s_waitcnt lgkmcnt(0)\n"
    "v_readfirstlane_b32 s50, v78\n"
    "s_cmp_eq_u32 s50, 3\n"
    "s_cselect_b64 exec, 1, 0\n"
    "global_store_dword v79, v78, %1 sc1\n"
    "s_mov_b64 exec, -1\n"
    "s_mov_b32 s56, 0\n"
    "v_cmp_eq_u32 vcc, 0, v77\n"
    "s_cmp_eq_u64 vcc, 0\n"
    "s_cbranch_scc1 .Lald_%=\n"
    ".Lachk_%=:\n"
    "v_readfirstlane_b32 s50, v76\n"
    "s_cmp_lt_u32 s50, 200\n"
    "s_cbranch_scc1 .Laend_%=\n"
    ".Lawait_%=:\n"
    "global_load_dword v77, v76, %1 sc1\n"
    "s_mov_b32 s52, 0\n"
    ".Lap11_%=:\n"
    "s_waitcnt vmcnt(0)\n"
    "v_cmp_eq_u32 vcc, 0, v77\n"
    "s_cmp_eq_u64 vcc, 0\n"
    "s_cbranch_scc1 .Lap11_x%=\n"
    "s_add_u32 s52, s52, 1\n"
    "s_sleep 2\n"
    "global_load_dword v77, v76, %1 sc1\n"
    "s_cmp_lt_u32 s52, s53\n"
    "s_cbranch_scc1 .Lap11_%=\n"
    "s_mov_b32 s53, 0\n"
    "v_mov_b32 v78, 0\n"
    "v_mov_b32 v79, 1\n"
    "global_store_dword v78, v79, %1 sc1\n"
    "s_waitcnt vmcnt(0)\n"
    ".Lap11_x%=:\n"
    ".Lald_%=:\n"
    "s_mov_b32 s94, s42\n"
    "buffer_load_dwordx4 v[152:155], v3, s[92:95], s49 offen sc1\n"
    "buffer_load_dwordx4 v[156:159], v3, s[92:95], s49 offen offset:1024 sc1\n"
    "buffer_load_dwordx4 v[160:163], v3, s[92:95], s49 offen offset:2048 sc1\n"
    "buffer_load_dwordx4 v[164:167], v3, s[92:95], s49 offen offset:3072 sc1\n"
    "buffer_load_dwordx4 v[168:171], v4, s[92:95], s49 offen sc1\n"
    ".Lain_%=:\n"
    "v_add_u32 v11, 32, v11\n"
    "ds_read_b128 v[76:79], v11\n"
    "ds_read2_b32 v[74:75], v11 offset0:5 offset1:6\n"
    "s_waitcnt vmcnt(4)\n"
    "ds_write_b128 v1, v[152:155]\n"
    "v_mfma_f32_16x16x32_bf16 v[48:51], v[152:155], v[152:155], 0\n"
    "s_waitcnt vmcnt(3)\n"
    "ds_write_b128 v1, v[156:159] offset:1024\n"
    "v_mfma_f32_16x16x32_bf16 v[48:51], v[156:159], v[156:159], v[48:51]\n"
    "s_waitcnt vmcnt(2)\n"
    "ds_write_b128 v1, v[160:163] offset:2048\n"
    "v_mfma_f32_16x16x32_bf16 v[48:51], v[160:163], v[160:163], v[48:51]\n"
    "s_waitcnt vmcnt(1)\n"
    "ds_write_b128 v1, v[164:167] offset:3072\n"
    "v_mfma_f32_16x16x32_bf16 v[48:51], v[164:167], v[164:167], v[48:51]\n"
    "s_waitcnt vmcnt(0)\n"
    "ds_write_b128 v1, v[168:171] offset:4096\n"
    "v_mfma_f32_16x16x32_bf16 v[48:51], v[168:171], v[168:171], v[48:51]\n"
    "s_waitcnt lgkmcnt(5)\n"
    "v_readfirstlane_b32 s50, v75\n"
    "s_cmp_eq_u32 s50, 0\n"
    "s_cbranch_scc1 .Lac12_%=\n"
    "v_swap_b32 v28, v172\n"
    "v_swap_b32 v29, v173\n"
    "v_swap_b32 v30, v174\n"
    "v_swap_b32 v32, v175\n"
    "v_swap_b32 v33, v176\n"
    "v_swap_b32 v36, v177\n"
    "v_swap_b32 v37, v178\n"
    ".Lac12_%=:\n"
    "v_readfirstlane_b32 s49, v77\n"
    "v_readfirstlane_b32 s62, v78\n"
    "v_add_u32 v76, v76, v220\n"
    "s_waitcnt lgkmcnt(0)\n"
    "s_cmp_eq_u32 s56, 0\n"
    "s_cbranch_scc1 .Lac13_%=\n"
    "s_waitcnt vmcnt(0)\n"
    "v_mov_b32 v78, 1\n"
    "s_mov_b64 exec, 1\n"
    "ds_add_rtn_u32 v78, v11, v78 offset:16\n"
    "s_mov_b64 exec, -1\n"
    "s_waitcnt lgkmcnt(0)\n"
    "v_readfirstlane_b32 s50, v78\n"
    "s_cmp_eq_u32 s50, 3\n"
    "s_cselect_b64 exec, 1, 0\n"
    "global_store_dword v79, v78, %1 sc1\n"
    "s_mov_b64 exec, -1\n"
    "s_mov_b32 s56, 0\n"
    ".Lac13_%=:\n"
    "s_branch .Latop_%=\n"
    ".Lapol_%=:\n"
    "v_mov_b32 v76, 64\n"
    "global_load_dword v77, v76, %1 sc1\n"
    "s_mov_b32 s52, 0\n"
    ".Lap14_%=:\n"
    "s_waitcnt vmcnt(0)\n"
    "v_cmp_eq_u32 vcc, 0, v77\n"
    "s_cmp_eq_u64 vcc, 0\n"
    "s_cbranch_scc1 .Lap14_x%=\n"
    "s_add_u32 s52, s52, 1\n"
    "s_sleep 2\n"
    "global_load_dword v77, v76, %1 sc1\n"
    "s_cmp_lt_u32 s52, s53\n"
    "s_cbranch_scc1 .Lap14_%=\n"
    "s_mov_b32 s53, 0\n"
    "v_mov_b32 v78, 0\n"
    "v_mov_b32 v79, 1\n"
    "global_store_dword v78, v79, %1 sc1\n"
    "s_waitcnt vmcnt(0)\n"
    ".Lap14_x%=:\n"
    "v_readfirstlane_b32 s50, v77\n"
    "v_mbcnt_lo_u32_b32 v76, -1, 0\n"
    "v_mbcnt_hi_u32_b32 v76, -1, v76\n"
    "s_lshl_b32 s51, %2, 6\n"
    "v_add_u32 v76, s51, v76\n"
    "v_min_u32 v76, 129, v76\n"
    "v_lshlrev_b32 v76, 5, v76\n"
    "v_add_u32 v76, 0x26c00, v76\n"
    "ds_read2_b32 v[78:79], v76 offset0:1 offset1:2\n"
    "s_and_b32 s50, s50, 1\n"
    "s_waitcnt lgkmcnt(0)\n"
    "v_and_b32 v77, s50, v78\n"
    "v_lshlrev_b32 v77, 13, v77\n"
    "v_and_b32 v78, -2, v78\n"
    "v_xor_b32 v78, v77, v78\n"
    "v_and_b32 v77, s50, v79\n"
    "v_lshlrev_b32 v77, 13, v77\n"
    "v_and_b32 v79, -2, v79\n"
    "v_xor_b32 v79, v77, v79\n"
    "ds_write2_b32 v76, v78, v79 offset0:1 offset1:2\n"
    "s_waitcnt lgkmcnt(0)\n"
    "s_barrier\n"
    "ds_read_b32 v77, v11 offset:4\n"
    "v_add_u32 v76, %4, v220\n"
    "s_waitcnt lgkmcnt(0)\n"
    "v_readfirstlane_b32 s49, v77\n"
    "s_branch .Lawait_%=\n"
    ".Laend_%=:\n"
    "s_cmp_eq_u32 s50, 192\n"
    "s_cbranch_scc0 .Lapol_%=\n"
    "s_waitcnt vmcnt(0) lgkmcnt(0)\n"
    "s_endpgm\n"
    ".p2align 6\n"
    ".Lrole4_%=:\n"
    "s_mov_b64 s[40:41], %0\n"
    "s_and_b32 s41, s41, 0xffff\n"
    "s_mov_b32 s42, 0xbc00000\n"
    "s_mov_b32 s43, 0x20000\n"
    "s_mov_b64 s[92:93], %0\n"
    "s_and_b32 s93, s93, 0xffff\n"
    "s_mov_b32 s94, 0xbc00000\n"
    "s_mov_b32 s95, 0x20000\n"
    "s_mov_b32 s53, 131072\n"
    "s_mov_b64 s[56:57], 0\n"
    "v_mbcnt_lo_u32_b32 v20, -1, 0\n"
    "v_mbcnt_hi_u32_b32 v20, -1, v20\n"
    "s_lshl_b32 s50, %2, 8\n"
    "s_add_u32 s50, s50, 147456\n"
    "v_lshl_add_u32 v20, v20, 2, s50\n"
    "ds_read_b32 v0, v20\n"
    "ds_read_b32 v1, v20 offset:1024\n"
    "ds_read_b32 v2, v20 offset:2048\n"
    "ds_read_b32 v3, v20 offset:3072\n"
    "ds_read_b32 v4, v20 offset:4096\n"
    "ds_read_b32 v5, v20 offset:5120\n"
    "ds_read_b32 v8, v20 offset:6144\n"
    "ds_read_b32 v9, v20 offset:7168\n"
    "ds_read_b32 v10, v20 offset:8192\n"
    "s_waitcnt lgkmcnt(0)\n"
    "s_barrier\n"
    "s_lshl_b32 s54, %2, 10\n"
    "s_mov_b64 s[44:45], %8\n"
    "s_and_b32 s45, s45, 0xffff\n"
    "s_mov_b32 s46, 0xa2000\n"
    "s_mov_b32 s47, 0x20000\n"
    "s_add_u32 m0, s54, 0\n"
    "s_add_u32 s55, %6, 0\n"
    "buffer_load_dwordx4 v8, s[44:47], s55 offen lds\n"
    "s_add_u32 m0, s54, 36864\n"
    "s_add_u32 s55, %6, 64\n"
    "buffer_load_dwordx4 v8, s[44:47], s55 offen lds\n"
    "s_add_u32 m0, s54, 73728\n"
    "s_add_u32 s55, %6, 128\n"
    "buffer_load_dwordx4 v8, s[44:47], s55 offen lds\n"
    "s_add_u32 m0, s54, 4096\n"
    "s_add_u32 s55, %6, 73728\n"
    "buffer_load_dwordx4 v8, s[44:47], s55 offen lds\n"
    "s_add_u32 m0, s54, 40960\n"
    "s_add_u32 s55, %6, 73792\n"
    "buffer_load_dwordx4 v8, s[44:47], s55 offen lds\n"
    "s_add_u32 m0, s54, 77824\n"
    "s_add_u32 s55, %6, 73856\n"
    "buffer_load_dwordx4 v8, s[44:47], s55 offen lds\n"
    "s_add_u32 m0, s54, 8192\n"
    "s_add_u32 s55, %6, 147456\n"
    "buffer_load_dwordx4 v8, s[44:47], s55 offen lds\n"
    "s_add_u32 m0, s54, 45056\n"
    "s_add_u32 s55, %6, 147520\n"
    "buffer_load_dwordx4 v8, s[44:47], s55 offen lds\n"
    "s_add_u32 m0, s54, 81920\n"
    "s_add_u32 s55, %6, 147584\n"
    "buffer_load_dwordx4 v8, s[44:47], s55 offen lds\n"
    "s_add_u32 m0, s54, 12288\n"
    "s_add_u32 s55, %6, 221184\n"
    "buffer_load_dwordx4 v8, s[44:47], s55 offen lds\n"
    "s_add_u32 m0, s54, 49152\n"
    "s_add_u32 s55, %6, 221248\n"
    "buffer_load_dwordx4 v8, s[44:47], s55 offen lds\n"
    "s_add_u32 m0, s54, 86016\n"
    "s_add_u32 s55, %6, 221312\n"
    "buffer_load_dwordx4 v8, s[44:47], s55 offen lds\n"
    "s_add_u32 m0, s54, 16384\n"
    "s_add_u32 s55, %6, 294912\n"
    "buffer_load_dwordx4 v8, s[44:47], s55 offen lds\n"
    "s_add_u32 m0, s54, 53248\n"
    "s_add_u32 s55, %6, 294976\n"
    "buffer_load_dwordx4 v8, s[44:47], s55 offen lds\n"
    "s_add_u32 m0, s54, 90112\n"
    "s_add_u32 s55, %6, 295040\n"
    "buffer_load_dwordx4 v8, s[44:47], s55 offen lds\n"
    "s_add_u32 m0, s54, 20480\n"
    "s_add_u32 s55, %6, 368640\n"
    "buffer_load_dwordx4 v8, s[44:47], s55 offen lds\n"
    "s_add_u32 m0, s54, 57344\n"
    "s_add_u32 s55, %6, 368704\n"
    "buffer_load_dwordx4 v8, s[44:47], s55 offen lds\n"
    "s_add_u32 m0, s54, 94208\n"
    "s_add_u32 s55, %6, 368768\n"
    "buffer_load_dwordx4 v8, s[44:47], s55 offen lds\n"
    "s_add_u32 m0, s54, 24576\n"
    "s_add_u32 s55, %6, 442368\n"
    "buffer_load_dwordx4 v8, s[44:47], s55 offen lds\n"
    "s_add_u32 m0, s54, 61440\n"
    "s_add_u32 s55, %6, 442432\n"
    "buffer_load_dwordx4 v8, s[44:47], s55 offen lds\n"
    "s_add_u32 m0, s54, 98304\n"
    "s_add_u32 s55, %6, 442496\n"
    "buffer_load_dwordx4 v8, s[44:47], s55 offen lds\n"
    "s_add_u32 m0, s54, 28672\n"
    "s_add_u32 s55, %6, 516096\n"
    "buffer_load_dwordx4 v8, s[44:47], s55 offen lds\n"
    "s_add_u32 m0, s54, 65536\n"
    "s_add_u32 s55, %6, 516160\n"
    "buffer_load_dwordx4 v8, s[44:47], s55 offen lds\n"
    "s_add_u32 m0, s54, 102400\n"
    "s_add_u32 s55, %6, 516224\n"
    "buffer_load_dwordx4 v8, s[44:47], s55 offen lds\n"
    "s_add_u32 m0, s54, 32768\n"
    "s_add_u32 s55, %6, 589824\n"
    "buffer_load_dwordx4 v8, s[44:47], s55 offen lds\n"
    "s_add_u32 m0, s54, 69632\n"
    "s_add_u32 s55, %6, 589888\n"
    "buffer_load_dwordx4 v8, s[44:47], s55 offen lds\n"
    "s_add_u32 m0, s54, 106496\n"
    "s_add_u32 s55, %6, 589952\n"
    "buffer_load_dwordx4 v8, s[44:47], s55 offen lds\n"
    "v_mov_b32 v7, 0x20400\n"
    "global_load_dwordx2 v[20:21], v7, %1 sc1\n"
    "v_mbcnt_lo_u32_b32 v6, -1, 0\n"
    "v_min_u32 v6, 7, v6\n"
    "v_lshlrev_b32 v6, 7, v6\n"
    "v_add_u32 v6, 0x20000, v6\n"
    "global_load_dwordx2 v[22:23], v6, %1 sc1\n"
    "s_waitcnt vmcnt(2) lgkmcnt(0)\n"
    "s_barrier\n"
    "ds_read_b64_tr_b16 a[0:1], v9\n"
    "ds_read_b64_tr_b16 a[2:3], v9 offset:256\n"
    "ds_read_b64_tr_b16 a[4:5], v9 offset:8\n"
    "ds_read_b64_tr_b16 a[6:7], v9 offset:264\n"
    "ds_read_b64_tr_b16 a[8:9], v9 offset:2304\n"
    "ds_read_b64_tr_b16 a[10:11], v9 offset:2560\n"
    "ds_read_b64_tr_b16 a[12:13], v9 offset:2312\n"
    "ds_read_b64_tr_b16 a[14:15], v9 offset:2568\n"
    "ds_read_b64_tr_b16 a[16:17], v9 offset:4608\n"
    "ds_read_b64_tr_b16 a[18:19], v9 offset:4864\n"
    "ds_read_b64_tr_b16 a[20:21], v9 offset:4616\n"
    "ds_read_b64_tr_b16 a[22:23], v9 offset:4872\n"
    "ds_read_b64_tr_b16 a[24:25], v9 offset:6912\n"
    "ds_read_b64_tr_b16 a[26:27], v9 offset:7168\n"
    "ds_read_b64_tr_b16 a[28:29], v9 offset:6920\n"
    "ds_read_b64_tr_b16 a[30:31], v9 offset:7176\n"
    "ds_read_b64_tr_b16 a[32:33], v9 offset:9216\n"
    "ds_read_b64_tr_b16 a[34:35], v9 offset:9472\n"
    "ds_read_b64_tr_b16 a[36:37], v9 offset:9224\n"
    "ds_read_b64_tr_b16 a[38:39], v9 offset:9480\n"
    "ds_read_b64_tr_b16 a[40:41], v9 offset:11520\n"
    "ds_read_b64_tr_b16 a[42:43], v9 offset:11776\n"
    "ds_read_b64_tr_b16 a[44:45], v9 offset:11528\n"
    "ds_read_b64_tr_b16 a[46:47], v9 offset:11784\n"
    "ds_read_b64_tr_b16 a[48:49], v9 offset:13824\n"
    "ds_read_b64_tr_b16 a[50:51], v9 offset:14080\n"
    "ds_read_b64_tr_b16 a[52:53], v9 offset:13832\n"
    "ds_read_b64_tr_b16 a[54:55], v9 offset:14088\n"
    "ds_read_b64_tr_b16 a[56:57], v9 offset:16128\n"
    "ds_read_b64_tr_b16 a[58:59], v9 offset:16384\n"
    "ds_read_b64_tr_b16 a[60:61], v9 offset:16136\n"
    "ds_read_b64_tr_b16 a[62:63], v9 offset:16392\n"
    "ds_read_b64_tr_b16 a[64:65], v9 offset:18432\n"
    "ds_read_b64_tr_b16 a[66:67], v9 offset:18688\n"
    "ds_read_b64_tr_b16 a[68:69], v9 offset:18440\n"
    "ds_read_b64_tr_b16 a[70:71], v9 offset:18696\n"
    "ds_read_b64_tr_b16 a[72:73], v9 offset:20736\n"
    "ds_read_b64_tr_b16 a[74:75], v9 offset:20992\n"
    "ds_read_b64_tr_b16 a[76:77], v9 offset:20744\n"
    "ds_read_b64_tr_b16 a[78:79], v9 offset:21000\n"
    "ds_read_b64_tr_b16 a[80:81], v9 offset:23040\n"
    "ds_read_b64_tr_b16 a[82:83], v9 offset:23296\n"
    "ds_read_b64_tr_b16 a[84:85], v9 offset:23048\n"
    "ds_read_b64_tr_b16 a[86:87], v9 offset:23304\n"
    "ds_read_b64_tr_b16 a[88:89], v9 offset:25344\n"
    "ds_read_b64_tr_b16 a[90:91], v9 offset:25600\n"
    "ds_read_b64_tr_b16 a[92:93], v9 offset:25352\n"
    "ds_read_b64_tr_b16 a[94:95], v9 offset:25608\n"
    "ds_read_b64_tr_b16 a[96:97], v9 offset:27648\n"
    "ds_read_b64_tr_b16 a[98:99], v9 offset:27904\n"
    "ds_read_b64_tr_b16 a[100:101], v9 offset:27656\n"
    "ds_read_b64_tr_b16 a[102:103], v9 offset:27912\n"
    "ds_read_b64_tr_b16 a[104:105], v9 offset:29952\n"
    "ds_read_b64_tr_b16 a[106:107], v9 offset:30208\n"
    "ds_read_b64_tr_b16 a[108:109], v9 offset:29960\n"
    "ds_read_b64_tr_b16 a[110:111], v9 offset:30216\n"
    "ds_read_b64_tr_b16 a[112:113], v9 offset:32256\n"
    "ds_read_b64_tr_b16 a[114:115], v9 offset:32512\n"
    "ds_read_b64_tr_b16 a[116:117], v9 offset:32264\n"
    "ds_read_b64_tr_b16 a[118:119], v9 offset:32520\n"
    "ds_read_b64_tr_b16 a[120:121], v9 offset:34560\n"
    "ds_read_b64_tr_b16 a[122:123], v9 offset:34816\n"
    "ds_read_b64_tr_b16 a[124:125], v9 offset:34568\n"
    "ds_read_b64_tr_b16 a[126:127], v9 offset:34824\n"
    "ds_read_b64_tr_b16 a[128:129], v10\n"
    "ds_read_b64_tr_b16 a[130:131], v10 offset:2304\n"
    "ds_read_b64_tr_b16 a[132:133], v10 offset:8\n"
    "ds_read_b64_tr_b16 a[134:135], v10 offset:2312\n"
    "ds_read_b64_tr_b16 a[136:137], v10 offset:18432\n"
    "ds_read_b64_tr_b16 a[138:139], v10 offset:20736\n"
    "ds_read_b64_tr_b16 a[140:141], v10 offset:18440\n"
    "ds_read_b64_tr_b16 a[142:143], v10 offset:20744\n"
    "s_waitcnt lgkmcnt(0)\n"
    "s_barrier\n"
    "s_waitcnt vmcnt(0)\n"
    "v_add_u32 v20, 1, v20\n"
    "v_add_u32 v21, 1, v21\n"
    "s_mov_b32 s52, 1048576\n"
    ".Lbgo_a%=:\n"
    "v_cmp_ne_u64 vcc, v[20:21], v[22:23]\n"
    "s_cbranch_vccz .Lbgo_x%=\n"
    "global_load_dwordx2 v[22:23], v6, %1 sc1\n"
    "s_sub_u32 s52, s52, 1\n"
    "s_waitcnt vmcnt(0)\n"
    "s_cbranch_scc0 .Lbgo_a%=\n"
    ".Lbgo_x%=:\n"
    "v_mbcnt_lo_u32_b32 v20, -1, 0\n"
    "v_mbcnt_hi_u32_b32 v20, -1, v20\n"
    "v_min_u32 v6, 7, v20\n"
    "v_lshlrev_b32 v6, 7, v6\n"
    "v_mov_b32 v7, 0x26c00\n"
    "ds_read_b128 v[20:23], v7\n"
    "s_waitcnt lgkmcnt(0)\n"
    "v_readfirstlane_b32 s49, v21\n"
    "v_add_u32 v20, v20, v6\n"
    "s_branch .Lbchk_%=\n"
    ".Lbgo_%=:\n"
    "s_waitcnt vmcnt(1)\n"
    "v_add_u32 v7, 32, v7\n"
    "ds_read_b128 v[20:23], v7\n"
    "ds_write_b128 v1, v[100:103]\n"
    "ds_write_b128 v1, v[104:107] offset:1024\n"
    "ds_write_b128 v1, v[108:111] offset:2048\n"
    "ds_write_b128 v1, v[112:115] offset:3072\n"
    "ds_write_b32 v4, v116\n"
    "ds_write_b32 v4, v117 offset:16\n"
    "ds_write_b32 v4, v118 offset:32\n"
    "ds_write_b32 v4, v119 offset:48\n"
    "v_lshlrev_b32 v8, 16, v120\n"
    "v_and_b32 v9, 0xffff0000, v120\n"
    "v_lshlrev_b32 v10, 16, v121\n"
    "v_and_b32 v11, 0xffff0000, v121\n"
    "v_lshlrev_b32 v12, 16, v122\n"
    "v_and_b32 v13, 0xffff0000, v122\n"
    "v_lshlrev_b32 v14, 16, v123\n"
    "v_and_b32 v15, 0xffff0000, v123\n"
    "v_xor_b32 v0, 0x8000, v0\n"
    "v_xor_b32 v1, 0x8000, v1\n"
    "v_xor_b32 v4, 0x8000, v4\n"
    "s_waitcnt lgkmcnt(8)\n"
    "v_readfirstlane_b32 s49, v21\n"
    "v_readfirstlane_b32 s62, v22\n"
    "v_add_u32 v20, v20, v6\n"
    "s_waitcnt lgkmcnt(0)\n"
    ".Lbtop_%=:\n"
    "s_waitcnt lgkmcnt(0)\n"
    "s_barrier\n"
    "ds_read_b128 v[24:27], v0\n"
    "ds_read_b128 v[28:31], v0 offset:1024\n"
    "ds_read_b128 v[36:39], v0 offset:3072\n"
    "ds_read_b128 v[32:35], v0 offset:2048\n"
    "ds_read_b128 v[44:47], v0 offset:5120\n"
    "ds_read_b128 v[40:43], v0 offset:4096\n"
    "global_load_dword v21, v20, %1 sc1\n"
    "s_waitcnt lgkmcnt(5)\n"
    "v_mfma_f32_16x16x32_bf16 v[8:11], a[0:3], v[24:27], v[8:11]\n"
    "ds_read_b128 v[52:55], v0 offset:7168\n"
    "ds_read_b128 v[48:51], v0 offset:6144\n"
    "v_mfma_f32_16x16x32_bf16 v[12:15], a[4:7], v[24:27], v[12:15]\n"
    "ds_read_b128 v[60:63], v0 offset:9216\n"
    "ds_read_b128 v[56:59], v0 offset:8192\n"
    "s_waitcnt lgkmcnt(8)\n"
    "v_mfma_f32_16x16x32_bf16 v[8:11], a[8:11], v[28:31], v[8:11]\n"
    "ds_read_b128 v[68:71], v0 offset:11264\n"
    "ds_read_b128 v[64:67], v0 offset:10240\n"
    "v_mfma_f32_16x16x32_bf16 v[12:15], a[12:15], v[28:31], v[12:15]\n"
    "ds_read_b128 v[76:79], v0 offset:13312\n"
    "ds_read_b128 v[72:75], v0 offset:12288\n"
    "s_waitcnt lgkmcnt(10)\n"
    "v_mfma_f32_16x16x32_bf16 v[8:11], a[16:19], v[32:35], v[8:11]\n"
    "ds_read_b128 v[84:87], v0 offset:15360\n"
    "ds_read_b128 v[80:83], v0 offset:14336\n"
    "v_mfma_f32_16x16x32_bf16 v[12:15], a[20:23], v[32:35], v[12:15]\n"
    "ds_read_b128 v[92:95], v0 offset:17408\n"
    "ds_read_b128 v[88:91], v0 offset:16384\n"
    "v_mfma_f32_16x16x32_bf16 v[8:11], a[24:27], v[36:39], v[8:11]\n"
    "v_mfma_f32_16x16x32_bf16 v[12:15], a[28:31], v[36:39], v[12:15]\n"
    "s_waitcnt lgkmcnt(12)\n"
    "v_mfma_f32_16x16x32_bf16 v[8:11], a[32:35], v[40:43], v[8:11]\n"
    "v_mfma_f32_16x16x32_bf16 v[12:15], a[36:39], v[40:43], v[12:15]\n"
    "v_mfma_f32_16x16x32_bf16 v[8:11], a[40:43], v[44:47], v[8:11]\n"
    "v_mfma_f32_16x16x32_bf16 v[12:15], a[44:47], v[44:47], v[12:15]\n"
    "s_waitcnt lgkmcnt(10)\n"
    "v_mfma_f32_16x16x32_bf16 v[8:11], a[48:51], v[48:51], v[8:11]\n"
    "v_mfma_f32_16x16x32_bf16 v[12:15], a[52:55], v[48:51], v[12:15]\n"
    "v_mfma_f32_16x16x32_bf16 v[8:11], a[56:59], v[52:55], v[8:11]\n"
    "v_mfma_f32_16x16x32_bf16 v[12:15], a[60:63], v[52:55], v[12:15]\n"
    "s_waitcnt lgkmcnt(8)\n"
    "v_mfma_f32_16x16x32_bf16 v[8:11], a[64:67], v[56:59], v[8:11]\n"
    "v_mfma_f32_16x16x32_bf16 v[12:15], a[68:71], v[56:59], v[12:15]\n"
    "v_mfma_f32_16x16x32_bf16 v[8:11], a[72:75], v[60:63], v[8:11]\n"
    "v_mfma_f32_16x16x32_bf16 v[12:15], a[76:79], v[60:63], v[12:15]\n"
    "s_waitcnt lgkmcnt(6)\n"
    "v_mfma_f32_16x16x32_bf16 v[8:11], a[80:83], v[64:67], v[8:11]\n"
    "v_mfma_f32_16x16x32_bf16 v[12:15], a[84:87], v[64:67], v[12:15]\n"
    "v_mfma_f32_16x16x32_bf16 v[8:11], a[88:91], v[68:71], v[8:11]\n"
    "v_mfma_f32_16x16x32_bf16 v[12:15], a[92:95], v[68:71], v[12:15]\n"
    "s_waitcnt lgkmcnt(4)\n"
    "v_mfma_f32_16x16x32_bf16 v[8:11], a[96:99], v[72:75], v[8:11]\n"
    "v_mfma_f32_16x16x32_bf16 v[12:15], a[100:103], v[72:75], v[12:15]\n"
    "v_mfma_f32_16x16x32_bf16 v[8:11], a[104:107], v[76:79], v[8:11]\n"
    "v_mfma_f32_16x16x32_bf16 v[12:15], a[108:111], v[76:79], v[12:15]\n"
    "s_waitcnt lgkmcnt(2)\n"
    "v_mfma_f32_16x16x32_bf16 v[8:11], a[112:115], v[80:83], v[8:11]\n"
    "v_mfma_f32_16x16x32_bf16 v[12:15], a[116:119], v[80:83], v[12:15]\n"
    "v_mfma_f32_16x16x32_bf16 v[8:11], a[120:123], v[84:87], v[8:11]\n"
    "v_mfma_f32_16x16x32_bf16 v[12:15], a[124:127], v[84:87], v[12:15]\n"
    "s_waitcnt lgkmcnt(0)\n"
    "v_mfma_f32_16x16x32_bf16 v[8:11], a[128:131], v[88:91], v[8:11]\n"
    "v_mfma_f32_16x16x32_bf16 v[12:15], a[132:135], v[88:91], v[12:15]\n"
    "v_mfma_f32_16x16x32_bf16 v[8:11], a[136:139], v[92:95], v[8:11]\n"
    "v_mfma_f32_16x16x32_bf16 v[12:15], a[140:143], v[92:95], v[12:15]\n"
    "s_nop 4\n"
    "v_cvt_pk_bf16_f32 v16, v8, v9\n"
    "v_cvt_pk_bf16_f32 v17, v10, v11\n"
    "s_nop 1\n"
    "v_cvt_pk_bf16_f32 v18, v12, v13\n"
    "v_cvt_pk_bf16_f32 v19, v14, v15\n"
    "buffer_store_dwordx4 v[16:19], v3, s[40:43], s62 offen sc1\n"
    "s_mov_b32 s56, 1\n"
    "s_waitcnt vmcnt(1)\n"
    "v_cmp_eq_u32 vcc, 0, v21\n"
    "s_cmp_eq_u64 vcc, 0\n"
    "ds_read_b32 v23, v7 offset:44\n"
    "s_cbranch_scc0 .Lbno_%=\n"
    "buffer_load_dwordx4 v[100:103], v2, s[92:95], s49 offen sc1\n"
    "buffer_load_dwordx4 v[104:107], v2, s[92:95], s49 offen offset:1024 sc1\n"
    "buffer_load_dwordx4 v[108:111], v2, s[92:95], s49 offen offset:2048 sc1\n"
    "buffer_load_dwordx4 v[112:115], v2, s[92:95], s49 offen offset:3072 sc1\n"
    "buffer_load_dwordx4 v[116:119], v5, s[92:95], s49 offen sc1\n"
    "s_sub_u32 s55, s49, 0x1000000\n"
    "buffer_load_dwordx4 v[120:123], v3, s[40:43], s55 offen sc1\n"
    "s_waitcnt vmcnt(6)\n"
    "v_mov_b32 v22, 1\n"
    "s_mov_b64 exec, 1\n"
    "ds_add_rtn_u32 v22, v7, v22 offset:48\n"
    "s_mov_b64 exec, -1\n"
    "s_waitcnt lgkmcnt(0)\n"
    "v_readfirstlane_b32 s50, v22\n"
    "s_cmp_eq_u32 s50, 3\n"
    "s_cselect_b64 exec, 1, 0\n"
    "global_store_dword v23, v22, %1 sc1\n"
    "s_mov_b64 exec, -1\n"
    "s_branch .Lbin_%=\n"
    ".Lbno_%=:\n"
    "global_load_dword v21, v20, %1 sc1\n"
    "s_waitcnt vmcnt(0)\n"
    "v_mov_b32 v22, 1\n"
    "s_mov_b64 exec, 1\n"
    "ds_add_rtn_u32 v22, v7, v22 offset:48\n"
    "s_mov_b64 exec, -1\n"
    "s_waitcnt lgkmcnt(0)\n"
    "v_readfirstlane_b32 s50, v22\n"
    "s_cmp_eq_u32 s50, 3\n"
    "s_cselect_b64 exec, 1, 0\n"
    "global_store_dword v23, v22, %1 sc1\n"
    "s_mov_b64 exec, -1\n"
    "s_mov_b32 s56, 0\n"
    "v_cmp_eq_u32 vcc, 0, v21\n"
    "s_cmp_eq_u64 vcc, 0\n"
    "s_cbranch_scc1 .Lbld_%=\n"
    ".Lbchk_%=:\n"
    "v_readfirstlane_b32 s50, v20\n"
    "s_cmp_lt_u32 s50, 200\n"
    "s_cbranch_scc1 .Lbend_%=\n"
    ".Lbwait_%=:\n"
    "global_load_dword v21, v20, %1 sc1\n"
    "s_mov_b32 s52, 0\n"
    ".Lbp1_%=:\n"
    "s_waitcnt vmcnt(0)\n"
    "v_cmp_eq_u32 vcc, 0, v21\n"
    "s_cmp_eq_u64 vcc, 0\n"
    "s_cbranch_scc1 .Lbp1_x%=\n"
    "s_add_u32 s52, s52, 1\n"
    "s_sleep 2\n"
    "global_load_dword v21, v20, %1 sc1\n"
    "s_cmp_lt_u32 s52, s53\n"
    "s_cbranch_scc1 .Lbp1_%=\n"
    "s_mov_b32 s53, 0\n"
    "v_mov_b32 v22, 0\n"
    "v_mov_b32 v23, 1\n"
    "global_store_dword v22, v23, %1 sc1\n"
    "s_waitcnt vmcnt(0)\n"
    ".Lbp1_x%=:\n"
    ".Lbld_%=:\n"
    "s_mov_b32 s94, s42\n"
    "buffer_load_dwordx4 v[100:103], v2, s[92:95], s49 offen sc1\n"
    "buffer_load_dwordx4 v[104:107], v2, s[92:95], s49 offen offset:1024 sc1\n"
    "buffer_load_dwordx4 v[108:111], v2, s[92:95], s49 offen offset:2048 sc1\n"
    "buffer_load_dwordx4 v[112:115], v2, s[92:95], s49 offen offset:3072 sc1\n"
    "buffer_load_dwordx4 v[116:119], v5, s[92:95], s49 offen sc1\n"
    "s_sub_u32 s55, s49, 0x1000000\n"
    "buffer_load_dwordx4 v[120:123], v3, s[40:43], s55 offen sc1\n"
    ".Lbin_%=:\n"
    "v_add_u32 v7, 32, v7\n"
    "ds_read_b128 v[20:23], v7\n"
    "s_waitcnt vmcnt(5)\n"
    "ds_write_b128 v1, v[100:103]\n"
    "s_waitcnt vmcnt(4)\n"
    "ds_write_b128 v1, v[104:107] offset:1024\n"
    "s_waitcnt vmcnt(3)\n"
    "ds_write_b128 v1, v[108:111] offset:2048\n"
    "s_waitcnt vmcnt(2)\n"
    "ds_write_b128 v1, v[112:115] offset:3072\n"
    "s_waitcnt vmcnt(1)\n"
    "ds_write_b32 v4, v116\n"
    "ds_write_b32 v4, v117 offset:16\n"
    "ds_write_b32 v4, v118 offset:32\n"
    "ds_write_b32 v4, v119 offset:48\n"
    "s_waitcnt vmcnt(0)\n"
    "v_lshlrev_b32 v8, 16, v120\n"
    "v_and_b32 v9, 0xffff0000, v120\n"
    "v_lshlrev_b32 v10, 16, v121\n"
    "v_and_b32 v11, 0xffff0000, v121\n"
    "v_lshlrev_b32 v12, 16, v122\n"
    "v_and_b32 v13, 0xffff0000, v122\n"
    "v_lshlrev_b32 v14, 16, v123\n"
    "v_and_b32 v15, 0xffff0000, v123\n"
    "v_xor_b32 v0, 0x8000, v0\n"
    "v_xor_b32 v1, 0x8000, v1\n"
    "v_xor_b32 v4, 0x8000, v4\n"
    "s_waitcnt lgkmcnt(8)\n"
    "v_readfirstlane_b32 s49, v21\n"
    "v_readfirstlane_b32 s62, v22\n"
    "v_add_u32 v20, v20, v6\n"
    "s_waitcnt lgkmcnt(0)\n"
    "s_branch .Lbtop_%=\n"
    ".Lbpol_%=:\n"
    "v_mov_b32 v20, 64\n"
    "global_load_dword v21, v20, %1 sc1\n"
    "s_mov_b32 s52, 0\n"
    ".Lbp2_%=:\n"
    "s_waitcnt vmcnt(0)\n"
    "v_cmp_eq_u32 vcc, 0, v21\n"
    "s_cmp_eq_u64 vcc, 0\n"
    "s_cbranch_scc1 .Lbp2_x%=\n"
    "s_add_u32 s52, s52, 1\n"
    "s_sleep 2\n"
    "global_load_dword v21, v20, %1 sc1\n"
    "s_cmp_lt_u32 s52, s53\n"
    "s_cbranch_scc1 .Lbp2_%=\n"
    "s_mov_b32 s53, 0\n"
    "v_mov_b32 v22, 0\n"
    "v_mov_b32 v23, 1\n"
    "global_store_dword v22, v23, %1 sc1\n"
    "s_waitcnt vmcnt(0)\n"
    ".Lbp2_x%=:\n"
    "v_readfirstlane_b32 s50, v21\n"
    "v_mbcnt_lo_u32_b32 v20, -1, 0\n"
    "v_mbcnt_hi_u32_b32 v20, -1, v20\n"
    "s_lshl_b32 s51, %2, 6\n"
    "v_add_u32 v20, s51, v20\n"
    "v_min_u32 v20, 129, v20\n"
    "v_lshlrev_b32 v20, 5, v20\n"
    "v_add_u32 v20, 0x26c00, v20\n"
    "ds_read2_b32 v[22:23], v20 offset0:1 offset1:2\n"
    "s_and_b32 s50, s50, 1\n"
    "s_waitcnt lgkmcnt(0)\n"
    "v_and_b32 v21, s50, v22\n"
    "v_lshlrev_b32 v21, 13, v21\n"
    "v_and_b32 v22, -2, v22\n"
    "v_xor_b32 v22, v21, v22\n"
    "v_and_b32 v21, s50, v23\n"
    "v_lshlrev_b32 v21, 13, v21\n"
    "v_and_b32 v23, -2, v23\n"
    "v_xor_b32 v23, v21, v23\n"
    "ds_write2_b32 v20, v22, v23 offset0:1 offset1:2\n"
    "s_waitcnt lgkmcnt(0)\n"
    "s_barrier\n"
    "ds_read_b32 v21, v7 offset:4\n"
    "v_add_u32 v20, %4, v6\n"
    "s_waitcnt lgkmcnt(0)\n"
    "v_readfirstlane_b32 s49, v21\n"
    "s_branch .Lbwait_%=\n"
    ".Lbend_%=:\n"
    "s_cmp_eq_u32 s50, 192\n"
    "s_cbranch_scc0 .Lbpol_%=\n"
    "s_waitcnt vmcnt(0) lgkmcnt(0)\n"
    ".Lrole5_%=:\n"
    ".Lfin:\n"
        :: "s"(wd), "s"(ws), "s"(wave), "s"(rid), "s"(P.h), "s"(P.p), "s"(P.woff), "s"(layer), "s"(w0), "s"(Wk), "s"(Wv), "s"(Wo),
           "s"(mem0), "s"(P.role) : CLOBBERS);
}

extern "C" void kernel_launch(void* const* d_in, const int* in_sizes, int n_in, void* d_out, int out_size, void* d_ws,
                              size_t ws_size, hipStream_t stream) {
    mega_kernel<<<256, 256, 0, stream>>>((const u16*)d_in[0], (const u16*)d_in[1], (const u16*)d_in[2], (const u16*)d_in[4],
                                         (const u16*)d_in[5], (const u16*)d_in[6], (const u16*)d_in[7], (const u16*)d_in[8],
                                         (u16*)d_out, (char*)d_ws);
}
```

```cpp
#include <hip/hip_runtime.h>
#include <hip/hip_bf16.h>
#include <stdint.h>

typedef unsigned short u16;
typedef unsigned u32;
typedef unsigned long long u64;
using bf16x8 = __attribute__((ext_vector_type(8))) short;
using bf16x4 = __attribute__((ext_vector_type(4))) short;
using f32x4 = __attribute__((ext_vector_type(4))) float;
using f32x2 = __attribute__((ext_vector_type(2))) float;
using v4u = __attribute__((ext_vector_type(4))) unsigned;
using v2u = __attribute__((ext_vector_type(2))) unsigned;
typedef __bf16 bfv2 __attribute__((ext_vector_type(2)));
#define GAS __attribute__((address_space(1)))
typedef GAS unsigned gu32;
typedef GAS u64 gu64;
#define RLX_AGENT __ATOMIC_RELAXED, __HIP_MEMORY_SCOPE_AGENT
typedef __amdgpu_buffer_rsrc_t rsrc_t;

constexpr int S_ = 2048, D_ = 576, NSEG = 64, SEG = 32, NB = 4096, NQA = 1728;

constexpr u32 OFF_ABORT = 0;
constexpr u32 OFF_POL = 64;
constexpr u32 OFF_RDY = 128;
constexpr u32 OFF_NEVER = 192;
constexpr u32 OFF_FA = 4096;
constexpr u32 OFF_FO = OFF_FA + 8192;
constexpr u32 OFF_FB = OFF_FO + 8192;
constexpr u32 OFF_FP = OFF_FB + 8192;
constexpr u32 OFF_COB = OFF_FP + 24576;
constexpr u32 OFF_FKV = OFF_COB + 32768;
constexpr u32 OFF_GO = 131072;
constexpr u32 OFF_EP = OFF_GO + 1024;
constexpr u32 SYNC_BYTES = OFF_EP + 128;
constexpr u32 MB2 = 2u << 20;
constexpr u32 OFF_XA = 0;
constexpr u32 OFF_XB = MB2;
constexpr u32 XB = S_ * D_ * 2;
constexpr u32 OFF_TA = 4 * MB2;
constexpr u32 OFF_TB = 8 * MB2;
constexpr u32 OFF_OA = 12 * MB2;
constexpr u32 OFF_PB = 16 * MB2;
constexpr u32 HB_ = 2 * 64 * 32 * 8192;
constexpr u32 OFF_QX = 32 * MB2;
constexpr u32 OFF_KX = OFF_QX + HB_;
constexpr u32 OFF_VX = OFF_KX + HB_;
constexpr u32 OFF_OX = OFF_VX + HB_;
constexpr u32 WS_BYTES = OFF_OX + HB_;
constexpr u32 L_TAB = 147456, L_IT = L_TAB + 11264, L_EX = 138240, L_W = 31936;

#define STAMP(i, k) do {} while (0)
#define PH(k) do {} while (0)
__device__ __forceinline__ float bf2f(u16 v) { return __uint_as_float(((u32)v) << 16); }
__device__ __forceinline__ float bflo(u32 v) { return __uint_as_float(v << 16); }
__device__ __forceinline__ float bfhi(u32 v) { return __uint_as_float(v & 0xffff0000u); }
__device__ __forceinline__ u32 pack2(float a, float b) { bfv2 v = {(__bf16)a, (__bf16)b}; return __builtin_bit_cast(u32, v); }
__device__ __forceinline__ float rbf(float f) { return (float)(__bf16)f; }
__device__ __forceinline__ v4u ld16(rsrc_t r, u32 off) { return __builtin_amdgcn_raw_buffer_load_b128(r, off, 0, 16); }
__device__ __forceinline__ v2u ld8(rsrc_t r, u32 off) { return __builtin_amdgcn_raw_buffer_load_b64(r, off, 0, 16); }
__device__ __forceinline__ void st16(rsrc_t r, u32 off, v4u v) { __builtin_amdgcn_raw_buffer_store_b128(v, r, off, 0, 16); }
__device__ __forceinline__ void st8(rsrc_t r, u32 off, v2u v) { __builtin_amdgcn_raw_buffer_store_b64(v, r, off, 0, 16); }
__device__ __forceinline__ void st4(rsrc_t r, u32 off, u32 v) { __builtin_amdgcn_raw_buffer_store_b32(v, r, off, 0, 16); }
#define DRAIN() asm volatile("s_waitcnt vmcnt(0)" ::: "memory")
#define TOUCH(p) asm volatile("global_load_dword v255, %0, off" :: "v"(p) : "v255")
#define CBAR() asm volatile("" ::: "memory")
__device__ __forceinline__ u32 ldw(char* ws, u32 off) { return __hip_atomic_load((gu32*)(ws + off), RLX_AGENT); }
__device__ __forceinline__ void setw(char* ws, u32 off) { __hip_atomic_store((gu32*)(ws + off), 1u, RLX_AGENT); }
__device__ __forceinline__ u32 fl(u32 i) { return (i >> 5) * 1024 + (i & 31) * 4; }
constexpr u32 SPIN_CAP = 1u << 20;
#define SLP_W 2
#define SLP_C 8
__device__ __forceinline__ void until(u64 t) { while (__builtin_amdgcn_s_memrealtime() < t) __builtin_amdgcn_s_sleep(8); }
#define TDEL 0
__device__ __forceinline__ u64 succ(char* ws) {
    const u64 e = __hip_atomic_load((gu64*)(ws + OFF_EP), RLX_AGENT);
    return (u64)((u32)(e >> 32) + 1) << 32 | ((u32)e + 1);
}
__device__ __forceinline__ void wait_go(char* ws) {
    const u64 e = succ(ws);
    for (u32 n = 0; n < SPIN_CAP && __builtin_amdgcn_ballot_w64(__hip_atomic_load((gu64*)(ws + OFF_GO + (threadIdx.x & 7) * 128), RLX_AGENT) != e); ++n) __builtin_amdgcn_s_sleep(2);
    CBAR();
}
__device__ __forceinline__ bool spin_fail(char* ws, u32& spins) {
    ++spins;
    if ((spins & 63u) == 0u) {
        if (ldw(ws, OFF_ABORT)) return true;
        if (spins > SPIN_CAP) { __hip_atomic_store((gu32*)(ws + OFF_ABORT), 1u, RLX_AGENT); return true; }
    }
    return false;
}
__device__ __forceinline__ void wait_set(char* ws, u32 off) {
    u32 spins = 0;
    while (__builtin_amdgcn_ballot_w64(ldw(ws, off) == 0)) {
        if (spin_fail(ws, spins)) break;
        __builtin_amdgcn_s_sleep(SLP_W);
    }
    CBAR();
}
constexpr u32 OOB = 0x80000000u;
__device__ __forceinline__ u32 cgo(u32 cg) { return (cg >> 5) * 16384 + (cg & 31) * 256; }

template <int N, bool FIN>
__device__ __forceinline__ void t_item(int rid, u32 i, u32 j, u32 m, u32 pol, u32 w, rsrc_t ro, char* ws, rsrc_t rs) {
    const u32 lane = threadIdx.x & 63, s = i & 63, side = (((s >> 5) ^ pol ^ m) & 1) << 13;
    v4u x[N], p[N][4];
    u32 v[N];
    wait_set(ws, OFF_FB + m * 4096 + j * 128 + fl(i));
#pragma unroll
    for (int u = 0; u < N; ++u) { v[u] = cgo(12 * j + 4 * (w + u) + (lane >> 4)) + (lane & 15) * 16; x[u] = ld16(rs, OFF_TB + (i << 16) + (FIN ? side : m << 13) + v[u]); }
    wait_set(ws, OFF_FP + (m * 3 + (j >> 1)) * 4096 + (lane & 3) * 128 + fl(i));
    STAMP(i, 0);
#pragma unroll
    for (int u = 0; u < N; ++u)
#pragma unroll
        for (int k = 0; k < 4; ++k) p[u][k] = __builtin_amdgcn_raw_buffer_load_b128(rs, v[u], OFF_PB + (i << 18) + (k << 16) + ((((i >> 3) ^ pol ^ m) & 1) << 13), 16);
#pragma unroll
    for (int u = 0; u < N; ++u) {
#pragma unroll
        for (int e = 0; e < 4; ++e) {
            f32x2 a = {bflo(x[u][e]), bfhi(x[u][e])};
#pragma unroll
            for (int k = 0; k < 4; ++k) a += f32x2{bflo(p[u][k][e]), bfhi(p[u][k][e])};
            x[u][e] = pack2(a[0], a[1]);
        }
        if (FIN) __builtin_amdgcn_raw_buffer_store_b128(x[u], ro, (s * 32 + 2 * (lane & 15) + m) * 1152 + (12 * j + 4 * (w + u) + (lane >> 4)) * 16, 0, 0);
        else st16(rs, OFF_TA + ((64 + s) << 16) + side + v[u], x[u]);
    }
    STAMP(i, 1);
}
__device__ __forceinline__ bool t_last(u32* cnt) {
    DRAIN();
    u32 o = 0;
    if ((threadIdx.x & 63) == 0) o = __hip_atomic_fetch_add(cnt, 1u, __ATOMIC_RELAXED, __HIP_MEMORY_SCOPE_WORKGROUP);
    return __builtin_amdgcn_readfirstlane(o) == 2;
}
__device__ __forceinline__ void role_t(u64 t_in, int rid, u32 j, u32 m, const u16* x0, u16* out, char* ws, rsrc_t rs, char* smem) {
    const u32 lane = threadIdx.x & 63, w = __builtin_amdgcn_readfirstlane(threadIdx.x >> 6);
    u32* cnt = (u32*)smem + 64;
    cnt[threadIdx.x] = 0;
    __syncthreads();
    const rsrc_t ro = __builtin_amdgcn_make_buffer_rsrc(out, 0, XB, 0x00020000);
    if (w < 3) {
        const rsrc_t rx = __builtin_amdgcn_make_buffer_rsrc((void*)x0, 0, XB, 0x00020000);
        const u32 cg = 12 * j + 4 * w + (lane >> 4), vx = (2 * (lane & 15) + m) * 1152 + cg * 16, v = OFF_TA + (m << 13) + cgo(cg) + (lane & 15) * 16;
#pragma unroll 1
        for (u32 s0 = 0; s0 < 64; s0 += s0 < 4 ? 2 : 10) {
            const u32 n = s0 < 4 ? 2 : 10;
            if (s0 == 14) until(t_in + TDEL);
            v4u x[10];
#pragma unroll
            for (u32 u = 0; u < 10; ++u) x[u] = __builtin_amdgcn_raw_buffer_load_b128(rx, u < n ? vx + (s0 + u) * 36864 : OOB, 0, 0);
#pragma unroll
            for (u32 u = 0; u < 10; ++u) st16(rs, u < n ? v + ((s0 + u) << 16) : OOB, x[u]);
            if (s0 == 0) { wait_go(ws); __builtin_amdgcn_s_barrier(); }
            if (t_last(cnt + s0) && lane < n) setw(ws, OFF_FA + m * 4096 + j * 128 + fl(s0 + lane));
        }
    }
    else __builtin_amdgcn_s_barrier();
    CBAR();
    wait_set(ws, OFF_POL);
    const u32 pol = ldw(ws, OFF_POL);
    if (w == 3) {
        for (u32 i = 64; i < 127; ++i) t_item<3, true>(rid, i, j, m, pol, 0, ro, ws, rs);
        return;
    }
    for (u32 i = 0; i < 64; ++i) {
        t_item<1, false>(rid, i, j, m, pol, w, ro, ws, rs);
        if (t_last(cnt + 64 + i) && lane == 0) setw(ws, OFF_FA + m * 4096 + j * 128 + fl(64 + i));
    }
    t_item<1, true>(rid, 127, j, m, pol, w, ro, ws, rs);
}

#define CLOBBERS "memory", "vcc", "scc", \
    "v0", "v1", "v2", "v3", "v4", "v5", "v6", "v7", "v8", "v9", "v10", "v11", "v12", "v13", "v14", "v15", "v16", "v17", "v18", "v19", "v20", "v21", "v22", "v23", \
    "v24", "v25", "v26", "v27", "v28", "v29", "v30", "v31", "v32", "v33", "v34", "v35", "v36", "v37", "v38", "v39", "v40", "v41", "v42", "v43", "v44", "v45", "v46", "v47", \
    "v48", "v49", "v50", "v51", "v52", "v53", "v54", "v55", "v56", "v57", "v58", "v59", "v60", "v61", "v62", "v63", "v64", "v65", "v66", "v67", "v68", "v69", "v70", "v71", \
    "v72", "v73", "v74", "v75", "v76", "v77", "v78", "v79", "v80", "v81", "v82", "v83", "v84", "v85", "v86", "v87", "v88", "v89", "v90", "v91", "v92", "v93", "v94", "v95", \
    "v96", "v97", "v98", "v99", "v100", "v101", "v102", "v103", "v104", "v105", "v106", "v107", "v108", "v109", "v110", "v111", "v112", "v113", "v114", "v115", "v116", "v117", "v118", "v119", \
    "v120", "v121", "v122", "v123", "v124", "v125", "v126", "v127", "v128", "v129", "v130", "v131", "v132", "v133", "v134", "v135", "v136", "v137", "v138", "v139", "v140", "v141", "v142", "v143", \
    "v144", "v145", "v146", "v147", "v148", "v149", "v150", "v151", "v152", "v153", "v154", "v155", "v156", "v157", "v158", "v159", "v160", "v161", "v162", "v163", "v164", "v165", "v166", "v167", \
    "v168", "v169", "v170", "v171", "v172", "v173", "v174", "v175", "v176", "v177", "v178", "v179", "v180", "v181", "v182", "v183", "v184", "v185", "v186", "v187", "v188", "v189", "v190", "v191", \
    "v192", "v193", "v194", "v195", "v196", "v197", "v198", "v199", "v200", "v201", "v202", "v203", "v204", "v205", "v206", "v207", "v208", "v209", "v210", "v211", "v212", "v213", "v214", "v215", \
    "v216", "v217", "v218", "v219", "v220", "v221", "v222", "v223", "v224", "v225", "v226", "v227", "v228", "v229", "v230", "v231", "v232", "v233", "v234", "v235", "v236", "v237", "v238", "v239", \
    "v240", "v241", "v242", "v243", "v244", "v245", "v246", "v247", "v248", "v249", "v250", "v251", "v252", "v253", "v254", "v255", "a0", "a1", "a2", "a3", "a4", "a5", "a6", "a7", \
    "a8", "a9", "a10", "a11", "a12", "a13", "a14", "a15", "a16", "a17", "a18", "a19", "a20", "a21", "a22", "a23", "a24", "a25", "a26", "a27", "a28", "a29", "a30", "a31", \
    "a32", "a33", "a34", "a35", "a36", "a37", "a38", "a39", "a40", "a41", "a42", "a43", "a44", "a45", "a46", "a47", "a48", "a49", "a50", "a51", "a52", "a53", "a54", "a55", \
    "a56", "a57", "a58", "a59", "a60", "a61", "a62", "a63", "a64", "a65", "a66", "a67", "a68", "a69", "a70", "a71", "a72", "a73", "a74", "a75", "a76", "a77", "a78", "a79", \
    "a80", "a81", "a82", "a83", "a84", "a85", "a86", "a87", "a88", "a89", "a90", "a91", "a92", "a93", "a94", "a95", "a96", "a97", "a98", "a99", "a100", "a101", "a102", "a103", \
    "a104", "a105", "a106", "a107", "a108", "a109", "a110", "a111", "a112", "a113", "a114", "a115", "a116", "a117", "a118", "a119", "a120", "a121", "a122", "a123", "a124", "a125", "a126", "a127", \
    "a128", "a129", "a130", "a131", "a132", "a133", "a134", "a135", "a136", "a137", "a138", "a139", "a140", "a141", "a142", "a143", "a144", "a145", "a146", "a147", "a148", "a149", "a150", "a151", \
    "a152", "a153", "a154", "a155", "a156", "a157", "a158", "a159", "a160", "a161", "a162", "a163", "a164", "a165", "a166", "a167", "a168", "a169", "a170", "a171", "a172", "a173", "a174", "a175", \
    "a176", "a177", "a178", "a179", "a180", "a181", "a182", "a183", "a184", "a185", "a186", "a187", "a188", "a189", "a190", "a191", "a192", "a193", "a194", "a195", "a196", "a197", "a198", "a199", \
    "a200", "a201", "a202", "a203", "a204", "a205", "a206", "a207", "a208", "a209", "a210", "a211", "a212", "a213", "a214", "a215", "a216", "a217", "a218", "a219", "a220", "a221", "a222", "a223", \
    "a224", "a225", "a226", "a227", "a228", "a229", "a230", "a231", "a232", "a233", "a234", "a235", "a236", "a237", "a238", "a239", "a240", "a241", "a242", "a243", "a244", "a245", "a246", "a247", \
    "a248", "a249", "a250", "a251", "a252", "a253", "a254", "a255", "s38", "s39", "s40", "s41", "s42", "s43", "s44", "s45", "s46", "s47", "s48", "s49", "s50", "s51", "s52", "s53", \
    "s54", "s55", "s56", "s57", "s58", "s59", "s60", "s61", "s62", "s63", "s64", "s65", "s66", "s67", "s68", "s69", "s70", "s71", "s72", "s73", "s74", "s75", "s76", "s77", \
    "s78", "s79", "s80", "s81", "s82", "s83", "s84", "s85", "s86", "s87", "s88", "s89", "s90", "s91", "s92", "s93", "s94", "s95", "s96", "s97", "s98", "s99", "s100", "s101"
#define LANES() const int tid = threadIdx.x, lane = tid & 63, c = lane & 15, g = lane >> 4, q = c >> 2, p4 = c & 3; \
    const int wave = __builtin_amdgcn_readfirstlane(tid >> 6); u32* tab = (u32*)(smem + L_TAB) + tid; (void)q; (void)p4; (void)g
struct Par { u32 role, h, p, woff; };
__device__ __forceinline__ u32 rows(u32 i, u32 m) { return (i << 16) + (i < 64 ? m << 13 : (((i >> 5 ^ m) & 1) << 13) + 1); }
__device__ __forceinline__ u32 slot(u32 i) { return ((i & 63) << 19) + (((i >> 2 ^ i >> 6) & 1) << 13) + 1; }
__device__ __forceinline__ u32 part(u32 i, u32 m) { return (i << 18) + (((i >> 3 ^ m) & 1) << 13) + 1; }
template <class FS, class FD>
__device__ __forceinline__ u32 items(u32 K, u32 mk, u32 fin, u32 fout, FS src, FD dst, char* smem) {
    const u32 n = threadIdx.x - 1;
    auto idx = [&](u32 k) { const u32 t = k - K; return t < 2 * (64 - K) ? ((t & 1) ? K : 64) + (t >> 1) : k & 127; };
    if (threadIdx.x < 130) {
        v4u* row = (v4u*)(smem + L_IT) + 2 * threadIdx.x;
        row[0] = v4u{n + 1 == mk ? OFF_NEVER + 4 : n + 1 < 128 ? fin + fl(idx(n + 1)) : OFF_NEVER, src(idx(n + 1)), dst(idx(n)), fout + fl(idx(n - 1))};
        row[1] = v4u{0u, idx(n) & 63 ? 0u : 0xf149f2cau, (idx(n) ^ idx(n - 1)) & 64, idx(n)};
    }
    return fin + fl(mk ? 64 : 0);
}
#define KA 11
#define KB 10
#define KH 9
#define KW 9

__device__ __forceinline__ Par role_h(u32 m, u32 h, char* smem) {
    LANES();
    tab[0] = 0x10000 + wave * 8192 + lane * 16;
    tab[256] = tid * 16;
    tab[2 * 256] = cgo(16 * wave) + lane * 16;
    tab[3 * 256] = 0x6000 + c * 4;
    tab[4 * 256] = 0x10000 + (q * 16 + c) * 16 + p4 * 4;
    tab[5 * 256] = (8 * wave + g) * 8192 + (c ^ ((g | (wave & 1) << 2) << 1)) * 16;
    tab[6 * 256] = (4 * g + q) * 256 + ((2 * wave) ^ (q | (g & 1) << 2)) * 32 + p4 * 8;
    tab[7 * 256] = tab[6 * 256] ^ 32;
    const u32 f = items(KH, 0, OFF_FB + m * 4096, OFF_FKV + (h >> 2) * 4096 + (h & 3) * 256 + m * 128, [&](u32 i) { return OFF_TB + rows(i, m); }, [&](u32 i) { return OFF_QX + h * 16384 + m * 4096 + slot(i); }, smem);
    return Par{1, f, 0, h * 256};
}

__device__ __forceinline__ Par role_w(u32 m, u32 kb, u32 nb, char* smem) {
    LANES();
    tab[0] = wave * 8192 + lane * 16;
    tab[256] = wave * 32768 + lane * 16;
    tab[512] = cgo(24 * nb + 6 * wave + g) + c * 16;
    tab[768] = (256 * wave + 4 * g + (c >> 3)) * 1152 + ((lane & 7) ^ ((g & 1) << 2)) * 16;
#pragma unroll
    for (int t = 0; t < 3; ++t) {
        const int col = wave * 96 + (t < 2 ? p4 * 16 + t * 8 : 64 + p4 * 8);
        tab[(4 + t) * 256] = (col >> 7) * 2048 + (q & 1) * 1024 + (2 * g + (q >> 1)) * 128 + ((col & 127) ^ ((g & 1) << 6));
    }
    tab[7 * 256] = (g & 1) ? OOB : cgo(24 * nb + 6 * wave + 4 + (g >> 1)) + c * 16;
    const u32 f = items(KW, 0, OFF_COB + (m * 4 + kb) * 4096, OFF_FP + (m * 3 + nb) * 4096 + kb * 128, [&](u32 i) { return OFF_OX + kb * 8 * 16384 + m * 4096 + slot(i); },
          [&](u32 i) { return OFF_PB + kb * 65536 + part(i, m); }, smem);
    return Par{2, f, 0, kb * 1024 * 1152 + nb * 384};
}

__device__ __forceinline__ Par role_a2(u32 m, u32 j, char* smem) {
    LANES();
    const int hd = tid >> 2, w = wave < 3 ? wave : 2;
    tab[0] = 0x8000 + lane * 16;
    tab[256] = wave * 4096 + lane * 16;
    tab[512] = cgo(16 * wave) + lane * 16;
    tab[768] = wave < 3 ? cgo(12 * j + 4 * wave + g) + c * 16 : OOB;
    tab[4 * 256] = (16 + (hd >> 4)) * 1024 + (((hd >> 2) & 3) * 16 + 4 * (tid & 3)) * 16 + (hd & 3) * 4;
    tab[5 * 256] = wave < 2 ? 32768 + tid * 16 : OOB;
    tab[6 * 256] = (16 * wave + (lane >> 2)) * 1152 + (lane & 3) * 16;
    tab[7 * 256] = w * 36864 + p4 * 16 + (20 * (g >> 1) + 8 * (g & 1) + q) * 64;
    tab[8 * 256] = w * 36864 + p4 * 16 + (72 * g + 16 + q) * 64;
    const u32 f = items(KB, KB, OFF_FO + m * 4096, OFF_FB + m * 4096 + j * 128, [&](u32 i) { return OFF_OA + rows(i, m); }, [&](u32 i) { return OFF_TB + rows(i, m); }, smem);
    return Par{4, f, 0, j * 192};
}

__device__ __forceinline__ Par role_a1(u32 m, u32 hg, char* smem) {
    LANES();
    const int hd = 4 * hg + wave;
    tab -= 768;
    tab[0] = lane * 16;
    tab[256] = wave * 5120 + lane * 16;
    tab[2 * 256] = !(g & 1) ? cgo(2 * hd + (g >> 1)) + c * 16 : g == 1 && !p4 ? 32768 + hd * 64 + c * 4 : OOB;
    tab[3 * 256] = cgo(16 * wave) + lane * 16;
    tab[4 * 256] = wave < 2 ? 32768 + tid * 16 : OOB;
    tab[5 * 256] = tid * 16;
    tab[6 * 256] = L_EX + ((wave >> 1) * 16 + c) * 16 + (wave & 1) * 8;
    tab[7 * 256] = g ? L_EX + 4096 : tab[6 * 256] + 512;
    tab[8 * 256] = L_EX + 2048 + (g * 16 + 2 * wave + (c & 1)) * 16;
    tab[9 * 256] = 0x7000 + c * 4;
    tab[10 * 256] = (q * 16 + c) * 16 + p4 * 4;
    tab[11 * 256] = 4 * g - c;
    tab[12 * 256] = (8 * g + q) * 144 + 36 * wave + 4 * (wave & 1) + p4 * 8;
    tab[13 * 256] = (wave < 2 ? p4 >> 1 : 2) * 9216 + (8 * g + q) * 144 + 72 * (p4 & 1) + 32;
    if (tid < 2) *(v4u*)(smem + L_EX + 4096 + tid * 1024) = v4u{0u, 0u, 0u, 0u};
    const u32 f = items(KA, KA, OFF_FA + m * 4096, OFF_FO + m * 4096 + hg * 128, [&](u32 i) { return OFF_TA + rows(i, m); }, [&](u32 i) { return OFF_OA + rows(i, m); }, smem);
    return Par{3, f, 0, hg * 144};
}

#define SB() __builtin_amdgcn_sched_barrier(0)
__device__ __forceinline__ u32 now32() { return (u32)__builtin_amdgcn_s_memtime(); }
__device__ __forceinline__ Par role_chain(u32 h, u32 p, char* smem) {
    LANES();
    const int ro = 16 * (q & 1) + 4 * (q >> 1) + 8 * (g >> 1) + 2 * (g & 1);
    tab[0] = lane * 16;
    tab[256] = L_W + (wave * 15 + (wave == 3 ? 2 : 0)) * 2048 + lane * 16;
    tab[2 * 256] = (4 * wave + g) * 8192 + (c ^ (((g & 1) | wave << 1) << 1)) * 16;
    tab[3 * 256] = (16 * (g >> 1) + 4 * (g & 1) + 8 * (q >> 1) + (q & 1)) * 256 + ((2 * wave) ^ ((q & 1) | (g & 1) << 1 | (q >> 1) << 2)) * 32 + p4 * 8;
    tab[4 * 256] = tab[3 * 256] ^ 32;
    tab[5 * 256] = (2 * wave + (lane >> 5)) * 1152 + ((lane & 31) ^ (wave << 1)) * 16;
    tab[6 * 256] = ro * 512 + ((2 * wave) ^ ((g & 1) | (q >> 1) << 1)) * 32 + p4 * 8;
    tab[7 * 256] = tab[6 * 256] ^ 32;
    tab[8 * 256] = (32 * wave + (lane >> 2)) * 1152 + (lane & 3) * 16;
    tab[9 * 256] = ro * 64 + (wave & 1) * 32 + p4 * 8;
    return Par{0, h, p, 0};
}

#define MAP 3
constexpr int RW = 90, RA1 = 24, RA2 = MAP >= 2 ? 184 : 56, RTT = 58;
#define PFO 1
#define WDEL1 700
#define CDEL 1000
#define WDEL 700
#define NPOL 6
static_assert(OFF_GO == 131072u);
static_assert(OFF_EP == 132096u);
static_assert(OFF_POL == 64u);
static_assert(OFF_RDY == 128u);
static_assert(OFF_COB == 53248u);
static_assert(OFF_FKV == 86016u);
static_assert(OFF_XA == 0u);
static_assert(OFF_XB == 2097152u);
static_assert(OFF_TA == 8388608u);
static_assert(OFF_OA == 25165824u);
static_assert(OFF_NEVER == 192u);
static_assert(L_IT == 158720u);
static_assert(OFF_QX == 67108864u);
static_assert(OFF_KX == 100663296u);
static_assert(OFF_VX == 134217728u);
static_assert(OFF_OX == 167772160u);
static_assert(WS_BYTES == 201326592u);
static_assert(L_TAB == 147456u);
static_assert(L_EX == 138240u);
static_assert(L_W == 31936);
__device__ __forceinline__ void measure_pol(char* ws, rsrc_t rs, char* smem) {
    const int wave = __builtin_amdgcn_readfirstlane(threadIdx.x >> 6);
    u32 best[2] = {~0u, ~0u};
    for (int n = 0; n < NPOL; ++n)
#pragma unroll
        for (int i = 0; i < 2; ++i) {
            SB();
            const u32 t0 = now32();
            SB();
            st4(rs, (wave & 1) * MB2 + (508 + 2 * i + (wave >> 1)) * 4096 + n * 128, t0);
            DRAIN();
            const u32 t = now32() - t0;
            best[i] = t < best[i] ? t : best[i];
        }
    int* d = (int*)smem;
    if ((threadIdx.x & 63) == 0) d[wave] = (int)(best[0] - best[1]);
    wait_go(ws);
    __syncthreads();
    if (threadIdx.x == 0) __hip_atomic_store((gu32*)(ws + OFF_POL), 4u | (d[0] + d[2] >= 0) | (d[1] + d[3] >= 0) << 1, RLX_AGENT);
}

__global__ __launch_bounds__(256, 1) void mega_kernel(const u16* __restrict__ x0, const u16* __restrict__ wqkv, const u16* __restrict__ woA,
                                                     const u16* __restrict__ mem0, const u16* __restrict__ Wq, const u16* __restrict__ Wk,
                                                     const u16* __restrict__ Wv, const u16* __restrict__ Wo, u16* out, char* ws) {
    __shared__ __attribute__((aligned(16))) char smem[163840];
    char* wd = (char*)(((uintptr_t)ws + SYNC_BYTES + MB2 - 1) & ~(uintptr_t)(MB2 - 1));
    const rsrc_t rs = __builtin_amdgcn_make_buffer_rsrc(wd, 0, WS_BYTES, 0x00020000);
    const u64 t_in = __builtin_amdgcn_s_memrealtime();
    {
        const char* pc; u32 n;
        asm volatile("s_getpc_b64 %0\n.Lbeg:\ns_mov_b32 %1, .Lfin-.Lbeg" : "=s"(pc), "=s"(n));
        const u32 o = ((blockIdx.x >> 3 & 3) * 256 + threadIdx.x) * 128;
        if (o < n) TOUCH(pc + o);
    }
    u32 rid = (blockIdx.x & 7) * 32 + (blockIdx.x >> 3);
    rid = rid - 24 < 8 ? rid + 120 : rid - 144 < 8 ? rid - 120 : rid;
    asm volatile("" : "+s"(rid));
    STAMP(159, 0);
    const u32 xq = (rid >> 5) & 3, t = rid & 31, layer = rid >> 7;
    const int u = t - 24;
    const int role = t < 16 ? 0 : t < 24 ? 1 : xq == 0 ? 3 : u < 2 ? 4 : xq == 1 ? 5 : 2;
    const int ix = role == 3 ? u : role == 4 ? 2 * xq - 2 + u : role == 5 ? u - 2 : (xq * 6 + u - 5) % 12;
    if (role == 5) { role_t(t_in, rid, ix, layer, x0, out, ws, rs, smem); asm volatile("s_endpgm" ::: "memory"); }
    if (role == 0 && layer) {
        const u32 i = t * 256 + threadIdx.x;
        for (u32 k = xq; k < (xq ? xq + 1 : 4); ++k) if (i < 3888) TOUCH((const char*)wqkv + (k * 3888 + i) * 128);
        if (xq && i < 1728) TOUCH((const char*)woA + (i / 3) * 1152 + (xq - 1) * 384 + (i % 3) * 128);
        for (const u16* w : {Wk, Wv})
            for (u32 k = i; k < 9216; k += 4096) TOUCH((const char*)w + (k >> 4) * 8192 + xq * 2048 + (k & 15) * 128);
        if (PFO) for (u32 k = i; k < 9216; k += 4096) TOUCH((const char*)Wo + (xq * 9216 + k) * 128);
        for (u32 k = 0; k < 18; ++k) st16(rs, (k % 3 * 3 >> 1) * MB2 + k / 3 * 262144 + (xq * 4096 + i) * 16, v4u{~0u, ~0u, ~0u, ~0u});
        DRAIN();
        wait_go(ws);
        __syncthreads();
        if (threadIdx.x == 0) __hip_atomic_fetch_add((gu32*)(ws + OFF_RDY), 1u, RLX_AGENT);
    }
    if (role == 2 && layer && ix < 8) {
        const u64 e = succ(ws);
        const rsrc_t rc = __builtin_amdgcn_make_buffer_rsrc(ws, 0, OFF_GO, 0x00020000);
        for (u32 k = 0; k < 4; ++k) st16(rc, ix * 16384 + k * 4096 + threadIdx.x * 16, v4u{0u, 0u, 0u, 0u});
        DRAIN();
        __syncthreads();
        if (threadIdx.x == 0) __hip_atomic_store((gu64*)(ws + OFF_GO + ix * 128), e, RLX_AGENT);
    }
    if (rid == RW) measure_pol(ws, rs, smem);
    until(t_in + (role == 2 ? (layer ? WDEL1 : WDEL) : role == 0 && layer ? CDEL : 0));
    Par P{5, 0, 0, 0};
    const u16* w0 = role == 2 ? Wo : role == 3 ? wqkv : role == 4 ? woA : Wq;
    if (role == 0) P = role_chain(xq * 8 + (t >> 1), t & 1, smem);
    else if (role == 1) P = role_h(layer, xq * 8 + t - 16, smem);
    else if (role == 2) P = role_w(layer, ix / 3, ix % 3, smem);
    else if (role == 3) P = role_a1(layer, ix, smem);
    else if (role == 4) P = role_a2(layer ^ (MAP >= 2), ix, smem);
    if (role == 0) P.woff = (u32)t_in;
    const int wave = __builtin_amdgcn_readfirstlane(threadIdx.x >> 6);
    asm volatile(
    "s_cmp_eq_u32 %13, 1\n"
    "s_cbranch_scc1 .Lrole1_%=\n"
    "s_cmp_eq_u32 %13, 2\n"
    "s_cbranch_scc1 .Lrole2_%=\n"
    "s_cmp_eq_u32 %13, 3\n"
    "s_cbranch_scc1 .Lrole3_%=\n"
    "s_cmp_eq_u32 %13, 4\n"
    "s_cbranch_scc1 .Lrole4_%=\n"
    "s_cmp_eq_u32 %13, 5\n"
    "s_cbranch_scc1 .Lrole5_%=\n"
    "s_mov_b64 s[40:41], %0\n"
    "s_and_b32 s41, s41, 0xffff\n"
    "s_mov_b32 s42, 0xc000000\n"
    "s_mov_b32 s43, 0x20000\n"
    "s_mov_b32 s57, 131072\n"
    "v_mbcnt_lo_u32_b32 v10, -1, 0\n"
    "v_mbcnt_hi_u32_b32 v10, -1, v10\n"
    "s_lshl_b32 s73, %2, 8\n"
    "s_add_u32 s73, s73, 147456\n"
    "v_lshl_add_u32 v10, v10, 2, s73\n"
    "ds_read_b32 v0, v10\n"
    "ds_read_b32 v2, v10 offset:1024\n"
    "ds_read_b32 v1, v10 offset:2048\n"
    "ds_read_b32 v3, v10 offset:3072\n"
    "ds_read_b32 v4, v10 offset:4096\n"
    "ds_read_b32 v5, v10 offset:5120\n"
    "ds_read_b32 v6, v10 offset:6144\n"
    "ds_read_b32 v7, v10 offset:7168\n"
    "ds_read_b32 v8, v10 offset:8192\n"
    "ds_read_b32 v9, v10 offset:9216\n"
    "s_waitcnt lgkmcnt(0)\n"
    "s_barrier\n"
    "s_lshl_b32 s60, %4, 8\n"
    "s_lshl_b32 s61, %2, 10\n"
    "s_mul_i32 s63, %4, 147456\n"
    "s_mul_i32 s62, %5, 576\n"
    "s_add_u32 s63, s63, s62\n"
    "s_mov_b64 s[44:45], %11\n"
    "s_and_b32 s45, s45, 0xffff\n"
    "s_mov_b32 s46, 0x480000\n"
    "s_mov_b32 s47, 0x20000\n"
    "s_lshl_b32 s73, s61, 1\n"
    "s_add_u32 m0, s73, 0\n"
    "s_add_u32 s62, s63, 512\n"
    "buffer_load_dwordx4 v8, s[44:47], s62 offen lds\n"
    "s_lshl_b32 s73, s61, 1\n"
    "s_add_u32 m0, s73, 1024\n"
    "s_add_u32 s62, s63, 18944\n"
    "buffer_load_dwordx4 v8, s[44:47], s62 offen lds\n"
    "s_mov_b64 vcc, 0\n"
    "s_mov_b32 vcc_lo, %7\n"
    "s_cbranch_vccz .Lsl0_%=\n"
    "s_sleep 10\n"
    ".Lsl0_%=:\n"
    "s_mov_b64 s[44:45], %8\n"
    "s_and_b32 s45, s45, 0xffff\n"
    "s_mov_b32 s46, 0x480000\n"
    "s_mov_b32 s47, 0x20000\n"
    "s_add_u32 m0, s61, 16384\n"
    "s_add_u32 s62, s60, 2097152\n"
    "buffer_load_dwordx4 v1, s[44:47], s62 offen lds\n"
    "s_add_u32 m0, s61, 20480\n"
    "s_add_u32 s62, s60, 2228224\n"
    "buffer_load_dwordx4 v1, s[44:47], s62 offen lds\n"
    "s_add_u32 m0, s61, 24576\n"
    "s_add_u32 s62, s60, 2359296\n"
    "buffer_load_dwordx4 v1, s[44:47], s62 offen lds\n"
    "s_add_u32 m0, s61, 28672\n"
    "s_add_u32 s62, s60, 2490368\n"
    "buffer_load_dwordx4 v1, s[44:47], s62 offen lds\n"
    "s_cbranch_vccz .Lsl1_%=\n"
    "s_sleep 10\n"
    ".Lsl1_%=:\n"
    "s_add_u32 m0, s61, 32768\n"
    "s_add_u32 s62, s60, 2621440\n"
    "buffer_load_dwordx4 v1, s[44:47], s62 offen lds\n"
    "s_add_u32 m0, s61, 36864\n"
    "s_add_u32 s62, s60, 2752512\n"
    "buffer_load_dwordx4 v1, s[44:47], s62 offen lds\n"
    "s_add_u32 m0, s61, 40960\n"
    "s_add_u32 s62, s60, 2883584\n"
    "buffer_load_dwordx4 v1, s[44:47], s62 offen lds\n"
    "s_add_u32 m0, s61, 45056\n"
    "s_add_u32 s62, s60, 3014656\n"
    "buffer_load_dwordx4 v1, s[44:47], s62 offen lds\n"
    "s_cbranch_vccz .Lsl2_%=\n"
    "s_sleep 10\n"
    ".Lsl2_%=:\n"
    "s_add_u32 m0, s61, 49152\n"
    "s_add_u32 s62, s60, 3145728\n"
    "buffer_load_dwordx4 v1, s[44:47], s62 offen lds\n"
    "s_add_u32 m0, s61, 53248\n"
    "s_add_u32 s62, s60, 3276800\n"
    "buffer_load_dwordx4 v1, s[44:47], s62 offen lds\n"
    "s_add_u32 m0, s61, 57344\n"
    "s_add_u32 s62, s60, 3407872\n"
    "buffer_load_dwordx4 v1, s[44:47], s62 offen lds\n"
    "s_add_u32 m0, s61, 61440\n"
    "s_add_u32 s62, s60, 3538944\n"
    "buffer_load_dwordx4 v1, s[44:47], s62 offen lds\n"
    "s_cbranch_vccz .Lsl3_%=\n"
    "s_sleep 10\n"
    ".Lsl3_%=:\n"
    "s_add_u32 m0, s61, 65536\n"
    "s_add_u32 s62, s60, 3670016\n"
    "buffer_load_dwordx4 v1, s[44:47], s62 offen lds\n"
    "s_add_u32 m0, s61, 69632\n"
    "s_add_u32 s62, s60, 3801088\n"
    "buffer_load_dwordx4 v1, s[44:47], s62 offen lds\n"
    "s_add_u32 m0, s61, 73728\n"
    "s_add_u32 s62, s60, 3932160\n"
    "buffer_load_dwordx4 v1, s[44:47], s62 offen lds\n"
    "s_add_u32 m0, s61, 77824\n"
    "s_add_u32 s62, s60, 4063232\n"
    "buffer_load_dwordx4 v1, s[44:47], s62 offen lds\n"
    "s_cbranch_vccz .Lsl4_%=\n"
    "s_sleep 10\n"
    ".Lsl4_%=:\n"
    "s_add_u32 m0, s61, 81920\n"
    "s_add_u32 s62, s60, 4194304\n"
    "buffer_load_dwordx4 v1, s[44:47], s62 offen lds\n"
    "s_add_u32 m0, s61, 86016\n"
    "s_add_u32 s62, s60, 4325376\n"
    "buffer_load_dwordx4 v1, s[44:47], s62 offen lds\n"
    "s_add_u32 m0, s61, 90112\n"
    "s_add_u32 s62, s60, 4456448\n"
    "buffer_load_dwordx4 v1, s[44:47], s62 offen lds\n"
    "s_add_u32 m0, s61, 94208\n"
    "s_add_u32 s62, s60, 4587520\n"
    "buffer_load_dwordx4 v1, s[44:47], s62 offen lds\n"
    "s_cbranch_vccz .Lsl5_%=\n"
    "s_sleep 10\n"
    ".Lsl5_%=:\n"
    "s_mov_b64 s[44:45], %9\n"
    "s_and_b32 s45, s45, 0xffff\n"
    "s_mov_b32 s46, 0x480000\n"
    "s_mov_b32 s47, 0x20000\n"
    "s_add_u32 m0, s61, 98304\n"
    "s_add_u32 s62, s60, 0\n"
    "buffer_load_dwordx4 v1, s[44:47], s62 offen lds\n"
    "s_add_u32 m0, s61, 102400\n"
    "s_add_u32 s62, s60, 131072\n"
    "buffer_load_dwordx4 v1, s[44:47], s62 offen lds\n"
    "s_add_u32 m0, s61, 106496\n"
    "s_add_u32 s62, s60, 262144\n"
    "buffer_load_dwordx4 v1, s[44:47], s62 offen lds\n"
    "s_add_u32 m0, s61, 110592\n"
    "s_add_u32 s62, s60, 393216\n"
    "buffer_load_dwordx4 v1, s[44:47], s62 offen lds\n"
    "s_cbranch_vccz .Lsl6_%=\n"
    "s_sleep 10\n"
    ".Lsl6_%=:\n"
    "s_add_u32 m0, s61, 114688\n"
    "s_add_u32 s62, s60, 524288\n"
    "buffer_load_dwordx4 v1, s[44:47], s62 offen lds\n"
    "s_add_u32 m0, s61, 118784\n"
    "s_add_u32 s62, s60, 655360\n"
    "buffer_load_dwordx4 v1, s[44:47], s62 offen lds\n"
    "s_add_u32 m0, s61, 122880\n"
    "s_add_u32 s62, s60, 786432\n"
    "buffer_load_dwordx4 v1, s[44:47], s62 offen lds\n"
    "s_add_u32 m0, s61, 126976\n"
    "s_add_u32 s62, s60, 917504\n"
    "buffer_load_dwordx4 v1, s[44:47], s62 offen lds\n"
    "s_cbranch_vccz .Lsl7_%=\n"
    "s_sleep 10\n"
    ".Lsl7_%=:\n"
    "s_add_u32 m0, s61, 131072\n"
    "s_add_u32 s62, s60, 1048576\n"
    "buffer_load_dwordx4 v1, s[44:47], s62 offen lds\n"
    "s_add_u32 m0, s61, 135168\n"
    "s_add_u32 s62, s60, 1179648\n"
    "buffer_load_dwordx4 v1, s[44:47], s62 offen lds\n"
    "s_add_u32 m0, s61, 139264\n"
    "s_add_u32 s62, s60, 1310720\n"
    "buffer_load_dwordx4 v1, s[44:47], s62 offen lds\n"
    "s_add_u32 m0, s61, 143360\n"
    "s_add_u32 s62, s60, 1441792\n"
    "buffer_load_dwordx4 v1, s[44:47], s62 offen lds\n"
    "s_cbranch_vccz .Lsl8_%=\n"
    "s_sleep 10\n"
    ".Lsl8_%=:\n"
    "s_add_u32 m0, s61, 147456\n"
    "s_add_u32 s62, s60, 1572864\n"
    "buffer_load_dwordx4 v1, s[44:47], s62 offen lds\n"
    "s_add_u32 m0, s61, 151552\n"
    "s_add_u32 s62, s60, 1703936\n"
    "buffer_load_dwordx4 v1, s[44:47], s62 offen lds\n"
    "s_add_u32 m0, s61, 155648\n"
    "s_add_u32 s62, s60, 1835008\n"
    "buffer_load_dwordx4 v1, s[44:47], s62 offen lds\n"
    "s_add_u32 m0, s61, 159744\n"
    "s_add_u32 s62, s60, 1966080\n"
    "buffer_load_dwordx4 v1, s[44:47], s62 offen lds\n"
    "s_waitcnt vmcnt(32) lgkmcnt(0)\n"
    "s_barrier\n"
    "ds_read_b64_tr_b16 v[144:145], v3 offset:16384\n"
    "ds_read_b64_tr_b16 v[146:147], v3 offset:16896\n"
    "ds_read_b64_tr_b16 v[148:149], v3 offset:24576\n"
    "ds_read_b64_tr_b16 v[150:151], v3 offset:25088\n"
    "ds_read_b64_tr_b16 v[152:153], v4 offset:16384\n"
    "ds_read_b64_tr_b16 v[154:155], v4 offset:16896\n"
    "ds_read_b64_tr_b16 v[156:157], v4 offset:24576\n"
    "ds_read_b64_tr_b16 v[158:159], v4 offset:25088\n"
    "s_waitcnt vmcnt(28) lgkmcnt(0)\n"
    "s_barrier\n"
    "s_cbranch_vccz .Lsl9_%=\n"
    "s_sleep 10\n"
    ".Lsl9_%=:\n"
    "s_add_u32 m0, s61, 16384\n"
    "s_add_u32 s62, s60, 2097152\n"
    "buffer_load_dwordx4 v1, s[44:47], s62 offen lds\n"
    "s_add_u32 m0, s61, 20480\n"
    "s_add_u32 s62, s60, 2228224\n"
    "buffer_load_dwordx4 v1, s[44:47], s62 offen lds\n"
    "s_add_u32 m0, s61, 24576\n"
    "s_add_u32 s62, s60, 2359296\n"
    "buffer_load_dwordx4 v1, s[44:47], s62 offen lds\n"
    "s_add_u32 m0, s61, 28672\n"
    "s_add_u32 s62, s60, 2490368\n"
    "buffer_load_dwordx4 v1, s[44:47], s62 offen lds\n"
    "ds_read_b64_tr_b16 v[160:161], v3 offset:32768\n"
    "ds_read_b64_tr_b16 v[162:163], v3 offset:33280\n"
    "ds_read_b64_tr_b16 v[164:165], v3 offset:40960\n"
    "ds_read_b64_tr_b16 v[166:167], v3 offset:41472\n"
    "ds_read_b64_tr_b16 v[168:169], v4 offset:32768\n"
    "ds_read_b64_tr_b16 v[170:171], v4 offset:33280\n"
    "ds_read_b64_tr_b16 v[172:173], v4 offset:40960\n"
    "ds_read_b64_tr_b16 v[174:175], v4 offset:41472\n"
    "s_waitcnt vmcnt(28) lgkmcnt(0)\n"
    "s_barrier\n"
    "s_cbranch_vccz .Lsl10_%=\n"
    "s_sleep 10\n"
    ".Lsl10_%=:\n"
    "s_add_u32 m0, s61, 32768\n"
    "s_add_u32 s62, s60, 2621440\n"
    "buffer_load_dwordx4 v1, s[44:47], s62 offen lds\n"
    "s_add_u32 m0, s61, 36864\n"
    "s_add_u32 s62, s60, 2752512\n"
    "buffer_load_dwordx4 v1, s[44:47], s62 offen lds\n"
    "s_add_u32 m0, s61, 40960\n"
    "s_add_u32 s62, s60, 2883584\n"
    "buffer_load_dwordx4 v1, s[44:47], s62 offen lds\n"
    "s_add_u32 m0, s61, 45056\n"
    "s_add_u32 s62, s60, 3014656\n"
    "buffer_load_dwordx4 v1, s[44:47], s62 offen lds\n"
    "ds_read_b64_tr_b16 v[176:177], v3 offset:49152\n"
    "ds_read_b64_tr_b16 v[178:179], v3 offset:49664\n"
    "ds_read_b64_tr_b16 v[180:181], v3 offset:57344\n"
    "ds_read_b64_tr_b16 v[182:183], v3 offset:57856\n"
    "ds_read_b64_tr_b16 v[184:185], v4 offset:49152\n"
    "ds_read_b64_tr_b16 v[186:187], v4 offset:49664\n"
    "ds_read_b64_tr_b16 v[188:189], v4 offset:57344\n"
    "ds_read_b64_tr_b16 v[190:191], v4 offset:57856\n"
    "s_waitcnt vmcnt(28) lgkmcnt(0)\n"
    "s_barrier\n"
    "s_cbranch_vccz .Lsl11_%=\n"
    "s_sleep 10\n"
    ".Lsl11_%=:\n"
    "s_add_u32 m0, s61, 49152\n"
    "s_add_u32 s62, s60, 3145728\n"
    "buffer_load_dwordx4 v1, s[44:47], s62 offen lds\n"
    "s_add_u32 m0, s61, 53248\n"
    "s_add_u32 s62, s60, 3276800\n"
    "buffer_load_dwordx4 v1, s[44:47], s62 offen lds\n"
    "s_add_u32 m0, s61, 57344\n"
    "s_add_u32 s62, s60, 3407872\n"
    "buffer_load_dwordx4 v1, s[44:47], s62 offen lds\n"
    "s_add_u32 m0, s61, 61440\n"
    "s_add_u32 s62, s60, 3538944\n"
    "buffer_load_dwordx4 v1, s[44:47], s62 offen lds\n"
    "v_add_u32 v31, 0x10000, v3\n"
    "ds_read_b64_tr_b16 v[192:193], v31\n"
    "ds_read_b64_tr_b16 v[194:195], v31 offset:512\n"
    "ds_read_b64_tr_b16 v[196:197], v31 offset:8192\n"
    "ds_read_b64_tr_b16 v[198:199], v31 offset:8704\n"
    "v_add_u32 v30, 0x10000, v4\n"
    "ds_read_b64_tr_b16 v[200:201], v30\n"
    "ds_read_b64_tr_b16 v[202:203], v30 offset:512\n"
    "ds_read_b64_tr_b16 v[204:205], v30 offset:8192\n"
    "ds_read_b64_tr_b16 v[206:207], v30 offset:8704\n"
    "s_waitcnt vmcnt(28) lgkmcnt(0)\n"
    "s_barrier\n"
    "s_cbranch_vccz .Lsl12_%=\n"
    "s_sleep 10\n"
    ".Lsl12_%=:\n"
    "s_add_u32 m0, s61, 65536\n"
    "s_add_u32 s62, s60, 3670016\n"
    "buffer_load_dwordx4 v1, s[44:47], s62 offen lds\n"
    "s_add_u32 m0, s61, 69632\n"
    "s_add_u32 s62, s60, 3801088\n"
    "buffer_load_dwordx4 v1, s[44:47], s62 offen lds\n"
    "s_add_u32 m0, s61, 73728\n"
    "s_add_u32 s62, s60, 3932160\n"
    "buffer_load_dwordx4 v1, s[44:47], s62 offen lds\n"
    "s_add_u32 m0, s61, 77824\n"
    "s_add_u32 s62, s60, 4063232\n"
    "buffer_load_dwordx4 v1, s[44:47], s62 offen lds\n"
    "ds_read_b64_tr_b16 v[208:209], v31 offset:16384\n"
    "ds_read_b64_tr_b16 v[210:211], v31 offset:16896\n"
    "ds_read_b64_tr_b16 v[212:213], v31 offset:24576\n"
    "ds_read_b64_tr_b16 v[214:215], v31 offset:25088\n"
    "ds_read_b64_tr_b16 v[216:217], v30 offset:16384\n"
    "ds_read_b64_tr_b16 v[218:219], v30 offset:16896\n"
    "ds_read_b64_tr_b16 v[220:221], v30 offset:24576\n"
    "ds_read_b64_tr_b16 v[222:223], v30 offset:25088\n"
    "s_waitcnt vmcnt(28) lgkmcnt(0)\n"
    "s_barrier\n"
    "s_cbranch_vccz .Lsl13_%=\n"
    "s_sleep 10\n"
    ".Lsl13_%=:\n"
    "s_add_u32 m0, s61, 81920\n"
    "s_add_u32 s62, s60, 4194304\n"
    "buffer_load_dwordx4 v1, s[44:47], s62 offen lds\n"
    "s_add_u32 m0, s61, 86016\n"
    "s_add_u32 s62, s60, 4325376\n"
    "buffer_load_dwordx4 v1, s[44:47], s62 offen lds\n"
    "s_add_u32 m0, s61, 90112\n"
    "s_add_u32 s62, s60, 4456448\n"
    "buffer_load_dwordx4 v1, s[44:47], s62 offen lds\n"
    "s_add_u32 m0, s61, 94208\n"
    "s_add_u32 s62, s60, 4587520\n"
    "buffer_load_dwordx4 v1, s[44:47], s62 offen lds\n"
    "ds_read_b64_tr_b16 v[224:225], v31 offset:32768\n"
    "ds_read_b64_tr_b16 v[226:227], v31 offset:33280\n"
    "ds_read_b64_tr_b16 v[228:229], v31 offset:40960\n"
    "ds_read_b64_tr_b16 v[230:231], v31 offset:41472\n"
    "ds_read_b64_tr_b16 v[232:233], v30 offset:32768\n"
    "ds_read_b64_tr_b16 v[234:235], v30 offset:33280\n"
    "ds_read_b64_tr_b16 v[236:237], v30 offset:40960\n"
    "ds_read_b64_tr_b16 v[238:239], v30 offset:41472\n"
    "s_waitcnt vmcnt(28) lgkmcnt(0)\n"
    "s_barrier\n"
    "s_cbranch_vccz .Lsl14_%=\n"
    "s_sleep 10\n"
    ".Lsl14_%=:\n"
    "s_mov_b64 s[44:45], %10\n"
    "s_and_b32 s45, s45, 0xffff\n"
    "s_mov_b32 s46, 0x480000\n"
    "s_mov_b32 s47, 0x20000\n"
    "s_add_u32 m0, s61, 98304\n"
    "s_add_u32 s62, s60, 0\n"
    "buffer_load_dwordx4 v1, s[44:47], s62 offen lds\n"
    "s_add_u32 m0, s61, 102400\n"
    "s_add_u32 s62, s60, 131072\n"
    "buffer_load_dwordx4 v1, s[44:47], s62 offen lds\n"
    "s_add_u32 m0, s61, 106496\n"
    "s_add_u32 s62, s60, 262144\n"
    "buffer_load_dwordx4 v1, s[44:47], s62 offen lds\n"
    "s_add_u32 m0, s61, 110592\n"
    "s_add_u32 s62, s60, 393216\n"
    "buffer_load_dwordx4 v1, s[44:47], s62 offen lds\n"
    "ds_read_b64_tr_b16 v[240:241], v31 offset:49152\n"
    "ds_read_b64_tr_b16 v[242:243], v31 offset:49664\n"
    "ds_read_b64_tr_b16 v[244:245], v31 offset:57344\n"
    "ds_read_b64_tr_b16 v[246:247], v31 offset:57856\n"
    "ds_read_b64_tr_b16 v[248:249], v30 offset:49152\n"
    "ds_read_b64_tr_b16 v[250:251], v30 offset:49664\n"
    "ds_read_b64_tr_b16 v[252:253], v30 offset:57344\n"
    "ds_read_b64_tr_b16 v[254:255], v30 offset:57856\n"
    "s_waitcnt vmcnt(28) lgkmcnt(0)\n"
    "s_barrier\n"
    "s_cbranch_vccz .Lsl15_%=\n"
    "s_sleep 10\n"
    ".Lsl15_%=:\n"
    "s_add_u32 m0, s61, 114688\n"
    "s_add_u32 s62, s60, 524288\n"
    "buffer_load_dwordx4 v1, s[44:47], s62 offen lds\n"
    "s_add_u32 m0, s61, 118784\n"
    "s_add_u32 s62, s60, 655360\n"
    "buffer_load_dwordx4 v1, s[44:47], s62 offen lds\n"
    "s_add_u32 m0, s61, 122880\n"
    "s_add_u32 s62, s60, 786432\n"
    "buffer_load_dwordx4 v1, s[44:47], s62 offen lds\n"
    "s_add_u32 m0, s61, 126976\n"
    "s_add_u32 s62, s60, 917504\n"
    "buffer_load_dwordx4 v1, s[44:47], s62 offen lds\n"
    "v_add_u32 v29, 0x20000, v3\n"
    "ds_read_b64_tr_b16 a[0:1], v29\n"
    "ds_read_b64_tr_b16 a[2:3], v29 offset:512\n"
    "ds_read_b64_tr_b16 a[4:5], v29 offset:8192\n"
    "ds_read_b64_tr_b16 a[6:7], v29 offset:8704\n"
    "v_add_u32 v28, 0x20000, v4\n"
    "ds_read_b64_tr_b16 a[8:9], v28\n"
    "ds_read_b64_tr_b16 a[10:11], v28 offset:512\n"
    "ds_read_b64_tr_b16 a[12:13], v28 offset:8192\n"
    "ds_read_b64_tr_b16 a[14:15], v28 offset:8704\n"
    "s_waitcnt vmcnt(28) lgkmcnt(0)\n"
    "s_barrier\n"
    "s_cbranch_vccz .Lsl16_%=\n"
    "s_sleep 10\n"
    ".Lsl16_%=:\n"
    "s_add_u32 m0, s61, 131072\n"
    "s_add_u32 s62, s60, 1048576\n"
    "buffer_load_dwordx4 v1, s[44:47], s62 offen lds\n"
    "s_add_u32 m0, s61, 135168\n"
    "s_add_u32 s62, s60, 1179648\n"
    "buffer_load_dwordx4 v1, s[44:47], s62 offen lds\n"
    "s_add_u32 m0, s61, 139264\n"
    "s_add_u32 s62, s60, 1310720\n"
    "buffer_load_dwordx4 v1, s[44:47], s62 offen lds\n"
    "s_add_u32 m0, s61, 143360\n"
    "s_add_u32 s62, s60, 1441792\n"
    "buffer_load_dwordx4 v1, s[44:47], s62 offen lds\n"
    "ds_read_b64_tr_b16 a[16:17], v29 offset:16384\n"
    "ds_read_b64_tr_b16 a[18:19], v29 offset:16896\n"
    "ds_read_b64_tr_b16 a[20:21], v29 offset:24576\n"
    "ds_read_b64_tr_b16 a[22:23], v29 offset:25088\n"
    "ds_read_b64_tr_b16 a[24:25], v28 offset:16384\n"
    "ds_read_b64_tr_b16 a[26:27], v28 offset:16896\n"
    "ds_read_b64_tr_b16 a[28:29], v28 offset:24576\n"
    "ds_read_b64_tr_b16 a[30:31], v28 offset:25088\n"
    "s_waitcnt vmcnt(28) lgkmcnt(0)\n"
    "s_barrier\n"
    "s_cbranch_vccz .Lsl17_%=\n"
    "s_sleep 10\n"
    ".Lsl17_%=:\n"
    "s_add_u32 m0, s61, 147456\n"
    "s_add_u32 s62, s60, 1572864\n"
    "buffer_load_dwordx4 v1, s[44:47], s62 offen lds\n"
    "s_add_u32 m0, s61, 151552\n"
    "s_add_u32 s62, s60, 1703936\n"
    "buffer_load_dwordx4 v1, s[44:47], s62 offen lds\n"
    "s_add_u32 m0, s61, 155648\n"
    "s_add_u32 s62, s60, 1835008\n"
    "buffer_load_dwordx4 v1, s[44:47], s62 offen lds\n"
    "s_add_u32 m0, s61, 159744\n"
    "s_add_u32 s62, s60, 1966080\n"
    "buffer_load_dwordx4 v1, s[44:47], s62 offen lds\n"
    "ds_read_b64_tr_b16 a[32:33], v3 offset:16384\n"
    "ds_read_b64_tr_b16 a[34:35], v3 offset:16896\n"
    "ds_read_b64_tr_b16 a[36:37], v3 offset:24576\n"
    "ds_read_b64_tr_b16 a[38:39], v3 offset:25088\n"
    "ds_read_b64_tr_b16 a[40:41], v4 offset:16384\n"
    "ds_read_b64_tr_b16 a[42:43], v4 offset:16896\n"
    "ds_read_b64_tr_b16 a[44:45], v4 offset:24576\n"
    "ds_read_b64_tr_b16 a[46:47], v4 offset:25088\n"
    "s_waitcnt vmcnt(28) lgkmcnt(0)\n"
    "s_barrier\n"
    "s_cbranch_vccz .Lsl18_%=\n"
    "s_sleep 10\n"
    ".Lsl18_%=:\n"
    "s_add_u32 m0, s61, 16384\n"
    "s_add_u32 s62, s60, 2097152\n"
    "buffer_load_dwordx4 v1, s[44:47], s62 offen lds\n"
    "s_add_u32 m0, s61, 20480\n"
    "s_add_u32 s62, s60, 2228224\n"
    "buffer_load_dwordx4 v1, s[44:47], s62 offen lds\n"
    "s_add_u32 m0, s61, 24576\n"
    "s_add_u32 s62, s60, 2359296\n"
    "buffer_load_dwordx4 v1, s[44:47], s62 offen lds\n"
    "s_add_u32 m0, s61, 28672\n"
    "s_add_u32 s62, s60, 2490368\n"
    "buffer_load_dwordx4 v1, s[44:47], s62 offen lds\n"
    "ds_read_b64_tr_b16 a[48:49], v3 offset:32768\n"
    "ds_read_b64_tr_b16 a[50:51], v3 offset:33280\n"
    "ds_read_b64_tr_b16 a[52:53], v3 offset:40960\n"
    "ds_read_b64_tr_b16 a[54:55], v3 offset:41472\n"
    "ds_read_b64_tr_b16 a[56:57], v4 offset:32768\n"
    "ds_read_b64_tr_b16 a[58:59], v4 offset:33280\n"
    "ds_read_b64_tr_b16 a[60:61], v4 offset:40960\n"
    "ds_read_b64_tr_b16 a[62:63], v4 offset:41472\n"
    "s_waitcnt vmcnt(28) lgkmcnt(0)\n"
    "s_barrier\n"
    "s_cbranch_vccz .Lsl19_%=\n"
    "s_sleep 10\n"
    ".Lsl19_%=:\n"
    "s_add_u32 m0, s61, 32768\n"
    "s_add_u32 s62, s60, 2621440\n"
    "buffer_load_dwordx4 v1, s[44:47], s62 offen lds\n"
    "s_add_u32 m0, s61, 36864\n"
    "s_add_u32 s62, s60, 2752512\n"
    "buffer_load_dwordx4 v1, s[44:47], s62 offen lds\n"
    "s_add_u32 m0, s61, 40960\n"
    "s_add_u32 s62, s60, 2883584\n"
    "buffer_load_dwordx4 v1, s[44:47], s62 offen lds\n"
    "s_add_u32 m0, s61, 45056\n"
    "s_add_u32 s62, s60, 3014656\n"
    "buffer_load_dwordx4 v1, s[44:47], s62 offen lds\n"
    "ds_read_b64_tr_b16 a[64:65], v3 offset:49152\n"
    "ds_read_b64_tr_b16 a[66:67], v3 offset:49664\n"
    "ds_read_b64_tr_b16 a[68:69], v3 offset:57344\n"
    "ds_read_b64_tr_b16 a[70:71], v3 offset:57856\n"
    "ds_read_b64_tr_b16 a[72:73], v4 offset:49152\n"
    "ds_read_b64_tr_b16 a[74:75], v4 offset:49664\n"
    "ds_read_b64_tr_b16 a[76:77], v4 offset:57344\n"
    "ds_read_b64_tr_b16 a[78:79], v4 offset:57856\n"
    "s_waitcnt vmcnt(28) lgkmcnt(0)\n"
    "s_barrier\n"
    "s_cbranch_vccz .Lsl20_%=\n"
    "s_sleep 10\n"
    ".Lsl20_%=:\n"
    "s_add_u32 m0, s61, 49152\n"
    "s_add_u32 s62, s60, 3145728\n"
    "buffer_load_dwordx4 v1, s[44:47], s62 offen lds\n"
    "s_add_u32 m0, s61, 53248\n"
    "s_add_u32 s62, s60, 3276800\n"
    "buffer_load_dwordx4 v1, s[44:47], s62 offen lds\n"
    "s_add_u32 m0, s61, 57344\n"
    "s_add_u32 s62, s60, 3407872\n"
    "buffer_load_dwordx4 v1, s[44:47], s62 offen lds\n"
    "s_add_u32 m0, s61, 61440\n"
    "s_add_u32 s62, s60, 3538944\n"
    "buffer_load_dwordx4 v1, s[44:47], s62 offen lds\n"
    "ds_read_b64_tr_b16 a[80:81], v31\n"
    "ds_read_b64_tr_b16 a[82:83], v31 offset:512\n"
    "ds_read_b64_tr_b16 a[84:85], v31 offset:8192\n"
    "ds_read_b64_tr_b16 a[86:87], v31 offset:8704\n"
    "ds_read_b64_tr_b16 a[88:89], v30\n"
    "ds_read_b64_tr_b16 a[90:91], v30 offset:512\n"
    "ds_read_b64_tr_b16 a[92:93], v30 offset:8192\n"
    "ds_read_b64_tr_b16 a[94:95], v30 offset:8704\n"
    "s_waitcnt vmcnt(28) lgkmcnt(0)\n"
    "s_barrier\n"
    "s_cbranch_vccz .Lsl21_%=\n"
    "s_sleep 10\n"
    ".Lsl21_%=:\n"
    "s_add_u32 m0, s61, 65536\n"
    "s_add_u32 s62, s60, 3670016\n"
    "buffer_load_dwordx4 v1, s[44:47], s62 offen lds\n"
    "s_add_u32 m0, s61, 69632\n"
    "s_add_u32 s62, s60, 3801088\n"
    "buffer_load_dwordx4 v1, s[44:47], s62 offen lds\n"
    "s_add_u32 m0, s61, 73728\n"
    "s_add_u32 s62, s60, 3932160\n"
    "buffer_load_dwordx4 v1, s[44:47], s62 offen lds\n"
    "s_add_u32 m0, s61, 77824\n"
    "s_add_u32 s62, s60, 4063232\n"
    "buffer_load_dwordx4 v1, s[44:47], s62 offen lds\n"
    "ds_read_b64_tr_b16 a[96:97], v31 offset:16384\n"
    "ds_read_b64_tr_b16 a[98:99], v31 offset:16896\n"
    "ds_read_b64_tr_b16 a[100:101], v31 offset:24576\n"
    "ds_read_b64_tr_b16 a[102:103], v31 offset:25088\n"
    "ds_read_b64_tr_b16 a[104:105], v30 offset:16384\n"
    "ds_read_b64_tr_b16 a[106:107], v30 offset:16896\n"
    "ds_read_b64_tr_b16 a[108:109], v30 offset:24576\n"
    "ds_read_b64_tr_b16 a[110:111], v30 offset:25088\n"
    "s_waitcnt vmcnt(28) lgkmcnt(0)\n"
    "s_barrier\n"
    "s_cbranch_vccz .Lsl22_%=\n"
    "s_sleep 10\n"
    ".Lsl22_%=:\n"
    "s_add_u32 m0, s61, 81920\n"
    "s_add_u32 s62, s60, 4194304\n"
    "buffer_load_dwordx4 v1, s[44:47], s62 offen lds\n"
    "s_add_u32 m0, s61, 86016\n"
    "s_add_u32 s62, s60, 4325376\n"
    "buffer_load_dwordx4 v1, s[44:47], s62 offen lds\n"
    "s_add_u32 m0, s61, 90112\n"
    "s_add_u32 s62, s60, 4456448\n"
    "buffer_load_dwordx4 v1, s[44:47], s62 offen lds\n"
    "s_add_u32 m0, s61, 94208\n"
    "s_add_u32 s62, s60, 4587520\n"
    "buffer_load_dwordx4 v1, s[44:47], s62 offen lds\n"
    "ds_read_b64_tr_b16 a[112:113], v31 offset:32768\n"
    "ds_read_b64_tr_b16 a[114:115], v31 offset:33280\n"
    "ds_read_b64_tr_b16 a[116:117], v31 offset:40960\n"
    "ds_read_b64_tr_b16 a[118:119], v31 offset:41472\n"
    "ds_read_b64_tr_b16 a[120:121], v30 offset:32768\n"
    "ds_read_b64_tr_b16 a[122:123], v30 offset:33280\n"
    "ds_read_b64_tr_b16 a[124:125], v30 offset:40960\n"
    "ds_read_b64_tr_b16 a[126:127], v30 offset:41472\n"
    "s_waitcnt vmcnt(28) lgkmcnt(0)\n"
    "s_barrier\n"
    "s_cbranch_vccz .Lsl23_%=\n"
    "s_sleep 10\n"
    ".Lsl23_%=:\n"
    "s_mov_b64 s[44:45], %8\n"
    "s_and_b32 s45, s45, 0xffff\n"
    "s_mov_b32 s46, 0x480000\n"
    "s_mov_b32 s47, 0x20000\n"
    "s_add_u32 m0, s61, 98304\n"
    "s_add_u32 s62, s60, 0\n"
    "buffer_load_dwordx4 v1, s[44:47], s62 offen lds\n"
    "s_add_u32 m0, s61, 102400\n"
    "s_add_u32 s62, s60, 131072\n"
    "buffer_load_dwordx4 v1, s[44:47], s62 offen lds\n"
    "s_add_u32 m0, s61, 106496\n"
    "s_add_u32 s62, s60, 262144\n"
    "buffer_load_dwordx4 v1, s[44:47], s62 offen lds\n"
    "s_add_u32 m0, s61, 110592\n"
    "s_add_u32 s62, s60, 393216\n"
    "buffer_load_dwordx4 v1, s[44:47], s62 offen lds\n"
    "ds_read_b64_tr_b16 a[128:129], v31 offset:49152\n"
    "ds_read_b64_tr_b16 a[130:131], v31 offset:49664\n"
    "ds_read_b64_tr_b16 a[132:133], v31 offset:57344\n"
    "ds_read_b64_tr_b16 a[134:135], v31 offset:57856\n"
    "ds_read_b64_tr_b16 a[136:137], v30 offset:49152\n"
    "ds_read_b64_tr_b16 a[138:139], v30 offset:49664\n"
    "ds_read_b64_tr_b16 a[140:141], v30 offset:57344\n"
    "ds_read_b64_tr_b16 a[142:143], v30 offset:57856\n"
    "s_waitcnt vmcnt(28) lgkmcnt(0)\n"
    "s_barrier\n"
    "s_cbranch_vccz .Lsl24_%=\n"
    "s_sleep 10\n"
    ".Lsl24_%=:\n"
    "s_add_u32 m0, s61, 114688\n"
    "s_add_u32 s62, s60, 524288\n"
    "buffer_load_dwordx4 v1, s[44:47], s62 offen lds\n"
    "s_add_u32 m0, s61, 118784\n"
    "s_add_u32 s62, s60, 655360\n"
    "buffer_load_dwordx4 v1, s[44:47], s62 offen lds\n"
    "s_add_u32 m0, s61, 122880\n"
    "s_add_u32 s62, s60, 786432\n"
    "buffer_load_dwordx4 v1, s[44:47], s62 offen lds\n"
    "s_add_u32 m0, s61, 126976\n"
    "s_add_u32 s62, s60, 917504\n"
    "buffer_load_dwordx4 v1, s[44:47], s62 offen lds\n"
    "ds_read_b64_tr_b16 a[144:145], v29\n"
    "ds_read_b64_tr_b16 a[146:147], v29 offset:512\n"
    "ds_read_b64_tr_b16 a[148:149], v29 offset:8192\n"
    "ds_read_b64_tr_b16 a[150:151], v29 offset:8704\n"
    "ds_read_b64_tr_b16 a[152:153], v28\n"
    "ds_read_b64_tr_b16 a[154:155], v28 offset:512\n"
    "ds_read_b64_tr_b16 a[156:157], v28 offset:8192\n"
    "ds_read_b64_tr_b16 a[158:159], v28 offset:8704\n"
    "s_waitcnt vmcnt(28) lgkmcnt(0)\n"
    "s_barrier\n"
    "s_cbranch_vccz .Lsl25_%=\n"
    "s_sleep 10\n"
    ".Lsl25_%=:\n"
    "s_add_u32 m0, s61, 131072\n"
    "s_add_u32 s62, s60, 1048576\n"
    "buffer_load_dwordx4 v1, s[44:47], s62 offen lds\n"
    "s_add_u32 m0, s61, 135168\n"
    "s_add_u32 s62, s60, 1179648\n"
    "buffer_load_dwordx4 v1, s[44:47], s62 offen lds\n"
    "s_add_u32 m0, s61, 139264\n"
    "s_add_u32 s62, s60, 1310720\n"
    "buffer_load_dwordx4 v1, s[44:47], s62 offen lds\n"
    "s_add_u32 m0, s61, 143360\n"
    "s_add_u32 s62, s60, 1441792\n"
    "buffer_load_dwordx4 v1, s[44:47], s62 offen lds\n"
    "ds_read_b64_tr_b16 a[160:161], v29 offset:16384\n"
    "ds_read_b64_tr_b16 a[162:163], v29 offset:16896\n"
    "ds_read_b64_tr_b16 a[164:165], v29 offset:24576\n"
    "ds_read_b64_tr_b16 a[166:167], v29 offset:25088\n"
    "ds_read_b64_tr_b16 a[168:169], v28 offset:16384\n"
    "ds_read_b64_tr_b16 a[170:171], v28 offset:16896\n"
    "ds_read_b64_tr_b16 a[172:173], v28 offset:24576\n"
    "ds_read_b64_tr_b16 a[174:175], v28 offset:25088\n"
    "s_waitcnt vmcnt(28) lgkmcnt(0)\n"
    "s_barrier\n"
    "s_cbranch_vccz .Lsl26_%=\n"
    "s_sleep 10\n"
    ".Lsl26_%=:\n"
    "s_add_u32 m0, s61, 147456\n"
    "s_add_u32 s62, s60, 1572864\n"
    "buffer_load_dwordx4 v1, s[44:47], s62 offen lds\n"
    "s_add_u32 m0, s61, 151552\n"
    "s_add_u32 s62, s60, 1703936\n"
    "buffer_load_dwordx4 v1, s[44:47], s62 offen lds\n"
    "s_add_u32 m0, s61, 155648\n"
    "s_add_u32 s62, s60, 1835008\n"
    "buffer_load_dwordx4 v1, s[44:47], s62 offen lds\n"
    "s_add_u32 m0, s61, 159744\n"
    "s_add_u32 s62, s60, 1966080\n"
    "buffer_load_dwordx4 v1, s[44:47], s62 offen lds\n"
    "ds_read_b64_tr_b16 a[176:177], v3 offset:16384\n"
    "ds_read_b64_tr_b16 a[178:179], v3 offset:16896\n"
    "ds_read_b64_tr_b16 a[180:181], v3 offset:24576\n"
    "ds_read_b64_tr_b16 a[182:183], v3 offset:25088\n"
    "ds_read_b64_tr_b16 a[184:185], v4 offset:16384\n"
    "ds_read_b64_tr_b16 a[186:187], v4 offset:16896\n"
    "ds_read_b64_tr_b16 a[188:189], v4 offset:24576\n"
    "ds_read_b64_tr_b16 a[190:191], v4 offset:25088\n"
    "s_waitcnt vmcnt(28) lgkmcnt(0)\n"
    "s_barrier\n"
    "s_cbranch_vccz .Lsl27_%=\n"
    "s_sleep 10\n"
    ".Lsl27_%=:\n"
    "s_mov_b64 s[44:45], %11\n"
    "s_and_b32 s45, s45, 0xffff\n"
    "s_mov_b32 s46, 0x480000\n"
    "s_mov_b32 s47, 0x20000\n"
    "s_add_u32 m0, s61, 16384\n"
    "s_add_u32 s62, s63, 0\n"
    "buffer_load_dwordx4 v5, s[44:47], s62 offen lds\n"
    "s_add_u32 m0, s61, 20480\n"
    "s_add_u32 s62, s63, 9216\n"
    "buffer_load_dwordx4 v5, s[44:47], s62 offen lds\n"
    "s_add_u32 m0, s61, 24576\n"
    "s_add_u32 s62, s63, 18432\n"
    "buffer_load_dwordx4 v5, s[44:47], s62 offen lds\n"
    "s_add_u32 m0, s61, 28672\n"
    "s_add_u32 s62, s63, 27648\n"
    "buffer_load_dwordx4 v5, s[44:47], s62 offen lds\n"
    "ds_read_b64_tr_b16 a[192:193], v3 offset:32768\n"
    "ds_read_b64_tr_b16 a[194:195], v3 offset:33280\n"
    "ds_read_b64_tr_b16 a[196:197], v3 offset:40960\n"
    "ds_read_b64_tr_b16 a[198:199], v3 offset:41472\n"
    "ds_read_b64_tr_b16 a[200:201], v4 offset:32768\n"
    "ds_read_b64_tr_b16 a[202:203], v4 offset:33280\n"
    "ds_read_b64_tr_b16 a[204:205], v4 offset:40960\n"
    "ds_read_b64_tr_b16 a[206:207], v4 offset:41472\n"
    "s_waitcnt vmcnt(28) lgkmcnt(0)\n"
    "s_barrier\n"
    "s_cbranch_vccz .Lsl28_%=\n"
    "s_sleep 10\n"
    ".Lsl28_%=:\n"
    "s_add_u32 m0, s61, 32768\n"
    "s_add_u32 s62, s63, 36864\n"
    "buffer_load_dwordx4 v5, s[44:47], s62 offen lds\n"
    "s_add_u32 m0, s61, 36864\n"
    "s_add_u32 s62, s63, 46080\n"
    "buffer_load_dwordx4 v5, s[44:47], s62 offen lds\n"
    "s_add_u32 m0, s61, 40960\n"
    "s_add_u32 s62, s63, 55296\n"
    "buffer_load_dwordx4 v5, s[44:47], s62 offen lds\n"
    "s_add_u32 m0, s61, 45056\n"
    "s_add_u32 s62, s63, 64512\n"
    "buffer_load_dwordx4 v5, s[44:47], s62 offen lds\n"
    "ds_read_b64_tr_b16 a[208:209], v3 offset:49152\n"
    "ds_read_b64_tr_b16 a[210:211], v3 offset:49664\n"
    "ds_read_b64_tr_b16 a[212:213], v3 offset:57344\n"
    "ds_read_b64_tr_b16 a[214:215], v3 offset:57856\n"
    "ds_read_b64_tr_b16 a[216:217], v4 offset:49152\n"
    "ds_read_b64_tr_b16 a[218:219], v4 offset:49664\n"
    "ds_read_b64_tr_b16 a[220:221], v4 offset:57344\n"
    "ds_read_b64_tr_b16 a[222:223], v4 offset:57856\n"
    "s_waitcnt vmcnt(28) lgkmcnt(0)\n"
    "s_barrier\n"
    "s_cbranch_vccz .Lsl29_%=\n"
    "s_sleep 10\n"
    ".Lsl29_%=:\n"
    "s_add_u32 m0, s61, 49152\n"
    "s_add_u32 s62, s63, 73728\n"
    "buffer_load_dwordx4 v5, s[44:47], s62 offen lds\n"
    "s_add_u32 m0, s61, 53248\n"
    "s_add_u32 s62, s63, 82944\n"
    "buffer_load_dwordx4 v5, s[44:47], s62 offen lds\n"
    "s_add_u32 m0, s61, 57344\n"
    "s_add_u32 s62, s63, 92160\n"
    "buffer_load_dwordx4 v5, s[44:47], s62 offen lds\n"
    "s_add_u32 m0, s61, 61440\n"
    "s_add_u32 s62, s63, 101376\n"
    "buffer_load_dwordx4 v5, s[44:47], s62 offen lds\n"
    "ds_read_b64_tr_b16 a[224:225], v31\n"
    "ds_read_b64_tr_b16 a[226:227], v31 offset:512\n"
    "ds_read_b64_tr_b16 a[228:229], v31 offset:8192\n"
    "ds_read_b64_tr_b16 a[230:231], v31 offset:8704\n"
    "ds_read_b64_tr_b16 a[232:233], v30\n"
    "ds_read_b64_tr_b16 a[234:235], v30 offset:512\n"
    "ds_read_b64_tr_b16 a[236:237], v30 offset:8192\n"
    "ds_read_b64_tr_b16 a[238:239], v30 offset:8704\n"
    "s_waitcnt vmcnt(28) lgkmcnt(0)\n"
    "s_barrier\n"
    "s_cbranch_vccz .Lsl30_%=\n"
    "s_sleep 10\n"
    ".Lsl30_%=:\n"
    "s_add_u32 m0, s61, 65536\n"
    "s_add_u32 s62, s63, 110592\n"
    "buffer_load_dwordx4 v5, s[44:47], s62 offen lds\n"
    "s_add_u32 m0, s61, 69632\n"
    "s_add_u32 s62, s63, 119808\n"
    "buffer_load_dwordx4 v5, s[44:47], s62 offen lds\n"
    "s_add_u32 m0, s61, 73728\n"
    "s_add_u32 s62, s63, 129024\n"
    "buffer_load_dwordx4 v5, s[44:47], s62 offen lds\n"
    "s_add_u32 m0, s61, 77824\n"
    "s_add_u32 s62, s63, 138240\n"
    "buffer_load_dwordx4 v5, s[44:47], s62 offen lds\n"
    "ds_read_b64_tr_b16 a[240:241], v31 offset:16384\n"
    "ds_read_b64_tr_b16 a[242:243], v31 offset:16896\n"
    "ds_read_b64_tr_b16 a[244:245], v31 offset:24576\n"
    "ds_read_b64_tr_b16 a[246:247], v31 offset:25088\n"
    "ds_read_b64_tr_b16 a[248:249], v30 offset:16384\n"
    "ds_read_b64_tr_b16 a[250:251], v30 offset:16896\n"
    "ds_read_b64_tr_b16 a[252:253], v30 offset:24576\n"
    "ds_read_b64_tr_b16 a[254:255], v30 offset:25088\n"
    "s_waitcnt vmcnt(28) lgkmcnt(0)\n"
    "s_barrier\n"
    "ds_read_b64_tr_b16 v[32:33], v31 offset:32768\n"
    "ds_read_b64_tr_b16 v[34:35], v31 offset:33280\n"
    "ds_read_b64_tr_b16 v[36:37], v31 offset:40960\n"
    "ds_read_b64_tr_b16 v[38:39], v31 offset:41472\n"
    "ds_read_b64_tr_b16 v[40:41], v30 offset:32768\n"
    "ds_read_b64_tr_b16 v[42:43], v30 offset:33280\n"
    "ds_read_b64_tr_b16 v[44:45], v30 offset:40960\n"
    "ds_read_b64_tr_b16 v[46:47], v30 offset:41472\n"
    "s_waitcnt vmcnt(24) lgkmcnt(0)\n"
    "s_barrier\n"
    "ds_read_b64_tr_b16 v[48:49], v31 offset:49152\n"
    "ds_read_b64_tr_b16 v[50:51], v31 offset:49664\n"
    "ds_read_b64_tr_b16 v[52:53], v31 offset:57344\n"
    "ds_read_b64_tr_b16 v[54:55], v31 offset:57856\n"
    "ds_read_b64_tr_b16 v[56:57], v30 offset:49152\n"
    "ds_read_b64_tr_b16 v[58:59], v30 offset:49664\n"
    "ds_read_b64_tr_b16 v[60:61], v30 offset:57344\n"
    "ds_read_b64_tr_b16 v[62:63], v30 offset:57856\n"
    "s_waitcnt vmcnt(20) lgkmcnt(0)\n"
    "s_barrier\n"
    "ds_read_b64_tr_b16 v[64:65], v29\n"
    "ds_read_b64_tr_b16 v[66:67], v29 offset:512\n"
    "ds_read_b64_tr_b16 v[68:69], v29 offset:8192\n"
    "ds_read_b64_tr_b16 v[70:71], v29 offset:8704\n"
    "ds_read_b64_tr_b16 v[72:73], v28\n"
    "ds_read_b64_tr_b16 v[74:75], v28 offset:512\n"
    "ds_read_b64_tr_b16 v[76:77], v28 offset:8192\n"
    "ds_read_b64_tr_b16 v[78:79], v28 offset:8704\n"
    "s_waitcnt vmcnt(16) lgkmcnt(0)\n"
    "s_barrier\n"
    "ds_read_b64_tr_b16 v[80:81], v29 offset:16384\n"
    "ds_read_b64_tr_b16 v[82:83], v29 offset:16896\n"
    "ds_read_b64_tr_b16 v[84:85], v29 offset:24576\n"
    "ds_read_b64_tr_b16 v[86:87], v29 offset:25088\n"
    "ds_read_b64_tr_b16 v[136:137], v28 offset:16384\n"
    "ds_read_b64_tr_b16 v[138:139], v28 offset:16896\n"
    "ds_read_b64_tr_b16 v[140:141], v28 offset:24576\n"
    "ds_read_b64_tr_b16 v[142:143], v28 offset:25088\n"
    "s_waitcnt vmcnt(12) lgkmcnt(0)\n"
    "s_barrier\n"
    "ds_read_b64_tr_b16 v[88:89], v6 offset:16384\n"
    "ds_read_b64_tr_b16 v[90:91], v6 offset:16896\n"
    "ds_read_b64_tr_b16 v[120:121], v6 offset:16640\n"
    "ds_read_b64_tr_b16 v[122:123], v6 offset:17152\n"
    "ds_read_b64_tr_b16 v[96:97], v7 offset:16384\n"
    "ds_read_b64_tr_b16 v[98:99], v7 offset:16896\n"
    "ds_read_b64_tr_b16 v[128:129], v7 offset:16640\n"
    "ds_read_b64_tr_b16 v[130:131], v7 offset:17152\n"
    "s_waitcnt vmcnt(8) lgkmcnt(0)\n"
    "s_barrier\n"
    "ds_read_b64_tr_b16 v[92:93], v6 offset:32768\n"
    "ds_read_b64_tr_b16 v[94:95], v6 offset:33280\n"
    "ds_read_b64_tr_b16 v[124:125], v6 offset:33024\n"
    "ds_read_b64_tr_b16 v[126:127], v6 offset:33536\n"
    "ds_read_b64_tr_b16 v[100:101], v7 offset:32768\n"
    "ds_read_b64_tr_b16 v[102:103], v7 offset:33280\n"
    "ds_read_b64_tr_b16 v[132:133], v7 offset:33024\n"
    "ds_read_b64_tr_b16 v[134:135], v7 offset:33536\n"
    "s_waitcnt vmcnt(4) lgkmcnt(0)\n"
    "s_barrier\n"
    "ds_read_b64_tr_b16 v[104:105], v6 offset:49152\n"
    "ds_read_b64_tr_b16 v[106:107], v6 offset:49664\n"
    "ds_read_b64_tr_b16 v[12:13], v6 offset:49408\n"
    "ds_read_b64_tr_b16 v[14:15], v6 offset:49920\n"
    "ds_read_b64_tr_b16 v[112:113], v7 offset:49152\n"
    "ds_read_b64_tr_b16 v[114:115], v7 offset:49664\n"
    "ds_read_b64_tr_b16 v[20:21], v7 offset:49408\n"
    "ds_read_b64_tr_b16 v[22:23], v7 offset:49920\n"
    "s_waitcnt vmcnt(0) lgkmcnt(0)\n"
    "s_barrier\n"
    "v_add_u32 v11, 0x10000, v6\n"
    "ds_read_b64_tr_b16 v[108:109], v11\n"
    "ds_read_b64_tr_b16 v[110:111], v11 offset:512\n"
    "ds_read_b64_tr_b16 v[16:17], v11 offset:256\n"
    "ds_read_b64_tr_b16 v[18:19], v11 offset:768\n"
    "v_add_u32 v10, 0x10000, v7\n"
    "ds_read_b64_tr_b16 v[116:117], v10\n"
    "ds_read_b64_tr_b16 v[118:119], v10 offset:512\n"
    "ds_read_b64_tr_b16 v[24:25], v10 offset:256\n"
    "ds_read_b64_tr_b16 v[26:27], v10 offset:768\n"
    "s_waitcnt lgkmcnt(0)\n"
    "s_barrier\n"
    "ds_write_b128 v2, v[88:91] offset:14336\n"
    "ds_write_b128 v2, v[92:95] offset:15360\n"
    "ds_write_b128 v2, v[96:99] offset:16384\n"
    "ds_write_b128 v2, v[100:103] offset:17408\n"
    "ds_write_b128 v2, v[104:107] offset:18432\n"
    "ds_write_b128 v2, v[108:111] offset:19456\n"
    "ds_write_b128 v2, v[112:115] offset:20480\n"
    "ds_write_b128 v2, v[116:119] offset:21504\n"
    "ds_write_b128 v2, v[120:123] offset:22528\n"
    "ds_write_b128 v2, v[124:127] offset:23552\n"
    "ds_write_b128 v2, v[128:131] offset:24576\n"
    "ds_write_b128 v2, v[132:135] offset:25600\n"
    "ds_write_b128 v2, v[12:15] offset:26624\n"
    "ds_write_b128 v2, v[16:19] offset:27648\n"
    "ds_write_b128 v2, v[20:23] offset:28672\n"
    "ds_write_b128 v2, v[24:27] offset:29696\n"
    "ds_read_b64_tr_b16 v[88:89], v9\n"
    "ds_read_b64_tr_b16 v[90:91], v9 offset:64\n"
    "ds_read_b64_tr_b16 v[92:93], v9 offset:2048\n"
    "ds_read_b64_tr_b16 v[94:95], v9 offset:2112\n"
    "ds_read_b64_tr_b16 v[96:97], v9 offset:4096\n"
    "ds_read_b64_tr_b16 v[98:99], v9 offset:4160\n"
    "ds_read_b64_tr_b16 v[100:101], v9 offset:6144\n"
    "ds_read_b64_tr_b16 v[102:103], v9 offset:6208\n"
    "ds_write_b128 v2, v[32:35]\n"
    "ds_write_b128 v2, v[36:39] offset:1024\n"
    "ds_write_b128 v2, v[40:43] offset:2048\n"
    "ds_write_b128 v2, v[44:47] offset:3072\n"
    "ds_write_b128 v2, v[48:51] offset:4096\n"
    "ds_write_b128 v2, v[52:55] offset:5120\n"
    "ds_write_b128 v2, v[56:59] offset:6144\n"
    "ds_write_b128 v2, v[60:63] offset:7168\n"
    "ds_write_b128 v2, v[64:67] offset:8192\n"
    "ds_write_b128 v2, v[68:71] offset:9216\n"
    "ds_write_b128 v2, v[72:75] offset:10240\n"
    "ds_write_b128 v2, v[76:79] offset:11264\n"
    "ds_write_b128 v2, v[80:83] offset:12288\n"
    "ds_write_b128 v2, v[84:87] offset:13312\n"
    "v_lshrrev_b32 v112, 4, v0\n"
    "v_and_b32 v113, 15, v112\n"
    "v_lshrrev_b32 v114, 4, v112\n"
    "v_mov_b32 v11, 0\n"
    "v_and_b32 v115, 7, v112\n"
    "v_mul_u32_u24 v115, 1152, v115\n"
    "v_lshrrev_b32 v116, 3, v112\n"
    "v_lshl_add_u32 v117, v116, 4, v115\n"
    "v_and_b32 v115, 1, v114\n"
    "v_lshrrev_b32 v116, 1, v114\n"
    "v_mov_b32 v120, 0\n"
    "v_mov_b32 v121, 0\n"
    "v_mov_b32 v122, 0\n"
    "v_mov_b32 v123, 0\n"
    "ds_write_b128 v11, v[120:123] offset:31872\n"
    "v_mov_b32 v124, 0x3f803f80\n"
    "v_mov_b32 v125, 0x3f803f80\n"
    "v_mov_b32 v126, 0x3f803f80\n"
    "v_mov_b32 v127, 0x3f803f80\n"
    "ds_write_b128 v11, v[124:127] offset:31888\n"
    "v_mov_b32 v128, 0x3f003f00\n"
    "v_mov_b32 v129, 0x3f003f00\n"
    "v_mov_b32 v130, 0x3f003f00\n"
    "v_mov_b32 v131, 0x3f003f00\n"
    "ds_write_b128 v11, v[128:131] offset:31904\n"
    "s_waitcnt lgkmcnt(0)\n"
    "s_cmp_lt_u32 %2, 2\n"
    "s_cbranch_scc1 .Lsk_%=\n"
    "ds_write_b128 v2, v[88:91] offset:30720\n"
    "ds_write_b128 v2, v[92:95] offset:31744\n"
    "ds_write_b128 v2, v[96:99] offset:32768\n"
    "ds_write_b128 v2, v[100:103] offset:33792\n"
    ".Lsk_%=:\n"
    "s_cmp_eq_u32 %2, 3\n"
    "s_cbranch_scc1 .Lsm_%=\n"
    "s_mov_b64 s[44:45], %12\n"
    "s_and_b32 s45, s45, 0xffff\n"
    "s_mov_b32 s46, 0x2400\n"
    "s_mov_b32 s47, 0x20000\n"
    "s_mul_i32 s62, %2, 384\n"
    "s_mul_i32 s61, %2, 3072\n"
    "s_add_u32 m0, s61, 20480\n"
    "s_add_u32 s73, s62, 0\n"
    "buffer_load_dwordx4 v117, s[44:47], s73 offen lds\n"
    "s_add_u32 m0, s61, 21504\n"
    "s_add_u32 s73, s62, 128\n"
    "buffer_load_dwordx4 v117, s[44:47], s73 offen lds\n"
    "s_add_u32 m0, s61, 22528\n"
    "s_add_u32 s73, s62, 256\n"
    "buffer_load_dwordx4 v117, s[44:47], s73 offen lds\n"
    ".Lsm_%=:\n"
    "v_mov_b32 v130, 0x20400\n"
    "global_load_dwordx2 v[132:133], v130, %1 sc1\n"
    "v_mbcnt_lo_u32_b32 v131, -1, 0\n"
    "v_min_u32 v131, 7, v131\n"
    "v_lshlrev_b32 v131, 7, v131\n"
    "v_add_u32 v131, 0x20000, v131\n"
    "global_load_dwordx2 v[134:135], v131, %1 sc1\n"
    "s_waitcnt vmcnt(0) lgkmcnt(0)\n"
    "s_barrier\n"
    "v_mov_b32 v1, 0x4444\n"
    "v_mov_b32 v118, 0xeeee\n"
    "v_cmp_gt_u32 vcc, 8, v113\n"
    "s_nop 1\n"
    "v_cndmask_b32 v1, v118, v1, vcc\n"
    "s_lshl_b32 s73, %2, 10\n"
    "v_add_u32 v5, s73, v0\n"
    "v_and_b32 v119, 7, v113\n"
    "v_lshrrev_b32 v120, 3, v113\n"
    "v_lshl_add_u32 v3, v115, 1, v116\n"
    "v_mul_u32_u24 v3, 544, v3\n"
    "v_lshl_add_u32 v3, v113, 2, v3\n"
    "s_lshl_b32 s73, %2, 6\n"
    "s_add_u32 s73, s73, 20480\n"
    "v_add_u32 v3, s73, v3\n"
    "v_mul_u32_u24 v4, 272, v119\n"
    "v_lshl_add_u32 v4, v114, 4, v4\n"
    "v_lshlrev_b32 v6, 7, v114\n"
    "v_lshl_add_u32 v6, v119, 4, v6\n"
    "s_lshl_b32 s73, %2, 9\n"
    "v_add_u32 v6, s73, v6\n"
    "s_waitcnt vmcnt(0)\n"
    "v_add_u32 v132, 1, v132\n"
    "v_add_u32 v133, 1, v133\n"
    "s_mov_b32 s56, 1048576\n"
    ".Lcgo_a%=:\n"
    "v_cmp_ne_u64 vcc, v[132:133], v[134:135]\n"
    "s_cbranch_vccz .Lcgo_x%=\n"
    "global_load_dwordx2 v[134:135], v131, %1 sc1\n"
    "s_sub_u32 s56, s56, 1\n"
    "s_waitcnt vmcnt(0)\n"
    "s_cbranch_scc0 .Lcgo_a%=\n"
    ".Lcgo_x%=:\n"
    "s_mov_b32 s56, 0\n"
    "v_mov_b32 v134, 64\n"
    ".Lpoll1_%=:\n"
    "global_load_dword v135, v134, %1 sc1\n"
    "s_waitcnt vmcnt(0)\n"
    "v_readfirstlane_b32 s73, v135\n"
    "s_cmp_ge_u32 s73, 4\n"
    "s_cbranch_scc1 .Lpoll1_x%=\n"
    "s_add_u32 s56, s56, 1\n"
    "s_cmp_lt_u32 s56, 3\n"
    "s_cbranch_scc1 .Lpoll1_%=\n"
    "s_sleep 8\n"
    "s_cmp_lt_u32 s56, s57\n"
    "s_cbranch_scc1 .Lpoll1_%=\n"
    "s_mov_b32 s57, 0\n"
    "v_mov_b32 v135, 1\n"
    "global_store_dword v11, v135, %1 sc1\n"
    ".Lpoll1_x%=:\n"
    "s_xor_b32 s69, s73, %7\n"
    "s_lshr_b32 s51, s73, 1\n"
    "s_xor_b32 s51, s51, %7\n"
    "s_and_b32 s51, s51, 1\n"
    "s_lshl_b32 s51, s51, 13\n"
    "s_add_u32 s68, s51, 0x200000\n"
    "s_and_b32 s60, s69, 1\n"
    "s_lshl_b32 s60, s60, 13\n"
    "v_lshl_add_u32 v8, v120, 2, v115\n"
    "v_lshlrev_b32 v8, 12, v8\n"
    "v_lshl_add_u32 v8, v119, 4, v8\n"
    "v_lshl_add_u32 v7, v116, 15, v8\n"
    "v_lshl_add_u32 v8, v116, 3, v8\n"
    "s_mul_i32 s73, %5, 589824\n"
    "s_lshl_b32 s51, %4, 7\n"
    "s_add_u32 s73, s73, s51\n"
    "s_add_u32 s73, s73, s60\n"
    "s_lshl_b32 s51, %2, 16\n"
    "s_add_u32 s51, s51, s73\n"
    "v_add_u32 v7, s51, v7\n"
    "s_and_b32 s51, %2, 1\n"
    "s_lshl_b32 s51, s51, 15\n"
    "s_add_u32 s51, s51, 524288\n"
    "s_add_u32 s51, s51, s73\n"
    "v_add_u32 v8, s51, v8\n"
    "s_mov_b32 s74, 262144\n"
    "s_lshl_b32 s61, %4, 1\n"
    "s_add_u32 s61, s61, %5\n"
    "s_lshl4_add_u32 s44, s61, 2048\n"
    "s_lshr_b32 s73, s61, 2\n"
    "s_add_u32 s73, s73, 128\n"
    "s_and_b32 s51, s61, 3\n"
    "s_cmp_eq_u32 s51, 0\n"
    "s_cselect_b32 s71, s73, -1\n"
    "s_lshl_b32 s73, s61, 1\n"
    "s_add_u32 s73, s73, %2\n"
    "s_cmp_lt_u32 %2, 2\n"
    "s_cselect_b32 s71, s73, s71\n"
    "s_max_i32 s61, s71, 0\n"
    "s_lshr_b32 s73, s61, 1\n"
    "s_lshl_b32 s73, s73, 14\n"
    "s_and_b32 s51, s61, 1\n"
    "s_lshl_b32 s51, s51, 12\n"
    "s_add_u32 s65, s73, s51\n"
    "s_add_u32 s65, s65, s60\n"
    "s_cmp_ge_u32 s61, 48\n"
    "s_cselect_b32 s94, 1, 0\n"
    "s_cmp_ge_u32 s61, 96\n"
    "s_cselect_b32 s94, 2, s94\n"
    "s_mul_i32 s73, s94, 48\n"
    "s_sub_u32 s73, s61, s73\n"
    "s_lshl_b32 s73, s73, 6\n"
    "v_lshlrev_b32 v9, 4, v114\n"
    "v_add_u32 v9, s73, v9\n"
    "v_bfrev_b32 v118, 1\n"
    "v_cmp_eq_u32 vcc, 0, v113\n"
    "s_nop 1\n"
    "v_cndmask_b32 v9, v118, v9, vcc\n"
    "s_mul_i32 s73, %2, 3072\n"
    "v_add_u32 v10, s73, v0\n"
    "s_cmp_eq_u32 %2, 3\n"
    "s_cbranch_scc0 .Lnw3_%=\n"
    "v_lshl_add_u32 v10, v113, 1, %5\n"
    "v_lshlrev_b32 v118, 3, v114\n"
    "v_sub_u32 v10, v10, v118\n"
    "v_cmp_le_i32 s[76:77], 0, v10\n"
    "v_cmp_le_i32 s[78:79], 2, v10\n"
    "v_cmp_le_i32 s[80:81], 4, v10\n"
    "v_cmp_le_i32 s[82:83], 6, v10\n"
    "v_cmp_le_i32 s[84:85], 1, v10\n"
    "v_cmp_le_i32 s[86:87], 3, v10\n"
    "v_cmp_le_i32 s[88:89], 5, v10\n"
    "v_cmp_le_i32 s[90:91], 7, v10\n"
    ".Lnw3_%=:\n"
    "s_lshr_b32 s73, %4, 2\n"
    "s_lshl1_add_u32 s73, s73, %7\n"
    "s_and_b32 s51, %4, 3\n"
    "s_lshl3_add_u32 s73, s73, s51\n"
    "s_lshl_b32 s73, s73, 8\n"
    "s_add_u32 s92, s73, 86016\n"
    "s_lshr_b32 s51, %4, 3\n"
    "s_lshl2_add_u32 s73, %5, s51\n"
    "s_lshl1_add_u32 s73, s73, %7\n"
    "s_and_b32 s51, %4, 7\n"
    "s_lshl4_add_u32 s73, s73, s51\n"
    "s_lshl_b32 s73, s73, 7\n"
    "s_add_u32 s93, s73, 53248\n"
    "s_lshl_b32 s73, %5, 12\n"
    "s_lshl_b32 s51, %4, 14\n"
    "s_add_u32 s73, s73, s51\n"
    "s_add_u32 s75, s73, 0xa000000\n"
    "s_brev_b32 s45, 1\n"
    "s_mov_b32 s48, 0\n"
    "s_mov_b32 s54, 66\n"
    "s_mov_b32 s55, 92\n"
    "s_mov_b32 s96, 896\n"
    "s_mov_b32 s56, 0\n"
    "v_mov_b32 v134, 128\n"
    ".Lpoll2_%=:\n"
    "global_load_dword v135, v134, %1 sc1\n"
    "s_waitcnt vmcnt(0)\n"
    "v_readfirstlane_b32 s73, v135\n"
    "s_cmp_ge_u32 s73, 64\n"
    "s_cbranch_scc1 .Lpoll2_x%=\n"
    "s_add_u32 s56, s56, 1\n"
    "s_cmp_lt_u32 s56, 3\n"
    "s_cbranch_scc1 .Lpoll2_%=\n"
    "s_sleep 8\n"
    "s_cmp_lt_u32 s56, s57\n"
    "s_cbranch_scc1 .Lpoll2_%=\n"
    "s_mov_b32 s57, 0\n"
    "v_mov_b32 v135, 1\n"
    "global_store_dword v11, v135, %1 sc1\n"
    ".Lpoll2_x%=:\n"
    "ds_read_b128 v[32:35], v2\n"
    "ds_read_b128 v[36:39], v2 offset:1024\n"
    "ds_read_b128 v[40:43], v2 offset:2048\n"
    "ds_read_b128 v[44:47], v2 offset:3072\n"
    "ds_read_b128 v[48:51], v2 offset:4096\n"
    "ds_read_b128 v[52:55], v2 offset:5120\n"
    "ds_read_b128 v[56:59], v2 offset:6144\n"
    "ds_read_b128 v[60:63], v2 offset:7168\n"
    "ds_read_b128 v[64:67], v2 offset:8192\n"
    "ds_read_b128 v[68:71], v2 offset:9216\n"
    "ds_read_b128 v[72:75], v2 offset:10240\n"
    "ds_read_b128 v[76:79], v2 offset:11264\n"
    "ds_read_b128 v[80:83], v2 offset:12288\n"
    "ds_read_b128 v[84:87], v2 offset:13312\n"
    "v_mov_b64 v[124:125], 0\n"
    "v_mov_b64 v[126:127], 0\n"
    "v_mov_b64 v[128:129], 0\n"
    "v_mov_b64 v[130:131], 0\n"
    "s_add_u32 s73, s48, 0\n"
    "s_and_b32 s73, s73, 63\n"
    "s_lshr_b32 s51, s73, 2\n"
    "s_xor_b32 s51, s51, s69\n"
    "s_and_b32 s51, s51, 1\n"
    "s_lshl_b32 s51, s51, 13\n"
    "s_lshl_b32 s73, s73, 5\n"
    "s_add_u32 s73, s73, %4\n"
    "s_lshl_b32 s73, s73, 14\n"
    "s_add_u32 s73, s73, s51\n"
    "s_add_u32 s60, s73, 0x6000000\n"
    "s_add_u32 s61, s73, 0x6001000\n"
    "s_add_u32 s62, s73, 0x8000000\n"
    "s_add_u32 s63, s73, 0x8001000\n"
    "s_lshl_b32 s51, %5, 12\n"
    "s_add_u32 s73, s73, s51\n"
    "s_add_u32 s64, s73, 0x4000000\n"
    "s_mul_i32 s73, s48, 3\n"
    "s_add_u32 s73, s73, 0\n"
    "s_and_b32 s51, s73, 1\n"
    "s_lshl_b32 s51, s51, 12\n"
    "s_lshr_b32 s73, s73, 1\n"
    "s_lshl_b32 s73, s73, 14\n"
    "s_add_u32 s73, s73, s51\n"
    "s_add_u32 s58, s73, s68\n"
    "s_waitcnt lgkmcnt(0)\n"
    "s_mov_b32 s56, 0\n"
    "v_mov_b32 v134, s92\n"
    ".Lpoll3_%=:\n"
    "global_load_dword v132, v134, %1 sc1\n"
    "global_load_dword v133, v134, %1 offset:128 sc1\n"
    "s_waitcnt vmcnt(0)\n"
    "v_and_b32 v133, v132, v133\n"
    "s_nop 0\n"
    "v_readfirstlane_b32 s73, v133\n"
    "s_cmp_ge_u32 s73, 1\n"
    "s_cbranch_scc1 .Lpoll3_x%=\n"
    "s_add_u32 s56, s56, 1\n"
    "s_cmp_lt_u32 s56, 3\n"
    "s_cbranch_scc1 .Lpoll3_%=\n"
    "s_sleep 8\n"
    "s_cmp_lt_u32 s56, s57\n"
    "s_cbranch_scc1 .Lpoll3_%=\n"
    "s_mov_b32 s57, 0\n"
    "v_mov_b32 v135, 1\n"
    "global_store_dword v11, v135, %1 sc1\n"
    ".Lpoll3_x%=:\n"
    "buffer_load_dwordx4 v[12:15], v5, s[40:43], s60 offen sc1\n"
    "buffer_load_dwordx4 v[16:19], v5, s[40:43], s61 offen sc1\n"
    "buffer_load_dwordx4 v[20:23], v5, s[40:43], s62 offen sc1\n"
    "buffer_load_dwordx4 v[24:27], v5, s[40:43], s63 offen sc1\n"
    "buffer_load_dwordx4 v[28:31], v5, s[40:43], s64 offen sc1\n"
    "s_add_u32 s73, s48, 1\n"
    "s_and_b32 s73, s73, 63\n"
    "s_lshr_b32 s51, s73, 2\n"
    "s_xor_b32 s51, s51, s69\n"
    "s_and_b32 s51, s51, 1\n"
    "s_lshl_b32 s51, s51, 13\n"
    "s_lshl_b32 s73, s73, 5\n"
    "s_add_u32 s73, s73, %4\n"
    "s_lshl_b32 s73, s73, 14\n"
    "s_add_u32 s73, s73, s51\n"
    "s_add_u32 s60, s73, 0x6000000\n"
    "s_add_u32 s61, s73, 0x6001000\n"
    "s_add_u32 s62, s73, 0x8000000\n"
    "s_add_u32 s63, s73, 0x8001000\n"
    "s_lshl_b32 s51, %5, 12\n"
    "s_add_u32 s73, s73, s51\n"
    "s_add_u32 s64, s73, 0x4000000\n"
    "s_add_u32 s73, s48, 1\n"
    "s_and_b32 s51, s73, 32\n"
    "s_and_b32 s73, s73, 31\n"
    "s_lshl3_add_u32 s73, s51, s73\n"
    "s_lshl2_add_u32 s73, s73, s92\n"
    "s_waitcnt vmcnt(4)\n"
    "ds_write_b128 v5, v[12:15]\n"
    "s_waitcnt vmcnt(3)\n"
    "ds_write_b128 v5, v[16:19] offset:4096\n"
    "s_waitcnt vmcnt(0)\n"
    "ds_write_b128 v5, v[28:31] offset:16384\n"
    "v_swap_b32 v22, v24\n"
    "v_swap_b32 v23, v25\n"
    "ds_write_b128 v5, v[20:23] offset:8192\n"
    "ds_write_b128 v5, v[24:27] offset:12288\n"
    "v_mov_b32 v14, s73\n"
    "global_load_dword v12, v14, %1 sc1\n"
    "global_load_dword v13, v14, %1 offset:128 sc1\n"
    "global_load_dword v14, v14, %1 sc1\n"
    "s_waitcnt lgkmcnt(0)\n"
    "s_barrier\n"
    ".Lstep_%=:\n"
    "ds_read_b128 v[88:91], v0 offset:20480\n"
    "ds_read_b128 v[92:95], v0 offset:21504\n"
    "ds_read_b128 v[96:99], v0 offset:22528\n"
    "ds_read_b128 v[100:103], v0 offset:23552\n"
    "ds_read_b128 v[104:107], v0 offset:24576\n"
    "ds_read_b128 v[108:111], v0 offset:25600\n"
    "ds_read_b128 v[112:115], v0 offset:26624\n"
    "ds_read_b128 v[116:119], v0 offset:27648\n"
    "ds_read_b128 v[120:123], v0 offset:28672\n"
    "s_waitcnt lgkmcnt(8)\n"
    "v_smfmac_f32_16x16x64_bf16 v[124:127], v[88:91], v[32:39], v1\n"
    "v_smfmac_f32_16x16x64_bf16 v[128:131], v[88:91], v[40:47], v1\n"
    "s_waitcnt lgkmcnt(7)\n"
    "v_smfmac_f32_16x16x64_bf16 v[124:127], v[92:95], v[48:55], v1\n"
    "v_mov_b64 v[32:33], 0\n"
    "v_mov_b64 v[34:35], 0\n"
    "v_smfmac_f32_16x16x64_bf16 v[128:131], v[92:95], v[56:63], v1\n"
    "v_mov_b64 v[36:37], 0\n"
    "v_mov_b64 v[38:39], 0\n"
    "s_waitcnt lgkmcnt(6)\n"
    "v_smfmac_f32_16x16x64_bf16 v[124:127], v[96:99], v[64:71], v1\n"
    "v_smfmac_f32_16x16x64_bf16 v[128:131], v[96:99], v[72:79], v1\n"
    "s_waitcnt lgkmcnt(5)\n"
    "v_smfmac_f32_16x16x64_bf16 v[124:127], v[100:103], v[80:87], v1\n"
    "v_smfmac_f32_16x16x64_bf16 v[128:131], v[100:103], v[136:143], v1\n"
    "s_waitcnt lgkmcnt(4)\n"
    "v_smfmac_f32_16x16x64_bf16 v[124:127], v[104:107], v[144:151], v1\n"
    "ds_read_b128 v[40:43], v0\n"
    "v_smfmac_f32_16x16x64_bf16 v[128:131], v[104:107], v[152:159], v1\n"
    "ds_read_b128 v[44:47], v0 offset:4096\n"
    "s_waitcnt lgkmcnt(5)\n"
    "v_smfmac_f32_16x16x64_bf16 v[124:127], v[108:111], v[160:167], v1\n"
    "ds_read_b128 v[48:51], v0 offset:1024\n"
    "v_smfmac_f32_16x16x64_bf16 v[128:131], v[108:111], v[168:175], v1\n"
    "ds_read_b128 v[52:55], v0 offset:5120\n"
    "s_waitcnt lgkmcnt(6)\n"
    "v_smfmac_f32_16x16x64_bf16 v[124:127], v[112:115], v[176:183], v1\n"
    "ds_read_b128 v[56:59], v0 offset:2048\n"
    "v_smfmac_f32_16x16x64_bf16 v[128:131], v[112:115], v[184:191], v1\n"
    "ds_read_b128 v[60:63], v0 offset:6144\n"
    "s_waitcnt lgkmcnt(7)\n"
    "v_smfmac_f32_16x16x64_bf16 v[124:127], v[116:119], v[192:199], v1\n"
    "ds_read_b128 v[64:67], v0 offset:3072\n"
    "v_smfmac_f32_16x16x64_bf16 v[128:131], v[116:119], v[200:207], v1\n"
    "ds_read_b128 v[68:71], v0 offset:7168\n"
    "s_waitcnt lgkmcnt(8)\n"
    "v_smfmac_f32_16x16x64_bf16 v[124:127], v[120:123], v[208:215], v1\n"
    "v_smfmac_f32_16x16x64_bf16 v[128:131], v[120:123], v[216:223], v1\n"
    "v_smfmac_f32_16x16x64_bf16 v[32:35], v[88:91], v[224:231], v1\n"
    "s_memrealtime s[52:53]\n"
    "v_cmp_eq_u32 vcc, s44, v5\n"
    "v_smfmac_f32_16x16x64_bf16 v[36:39], v[88:91], v[232:239], v1\n"
    "s_lshr_b32 s73, s96, 3\n"
    "v_mov_b32 v79, s45\n"
    "v_smfmac_f32_16x16x64_bf16 v[32:35], v[92:95], v[240:247], v1\n"
    "v_permlane32_swap_b32 v124, v126\n"
    "v_permlane32_swap_b32 v128, v130\n"
    "v_smfmac_f32_16x16x64_bf16 v[36:39], v[92:95], v[248:255], v1\n"
    "v_permlane32_swap_b32 v125, v127\n"
    "v_permlane32_swap_b32 v129, v131\n"
    "v_smfmac_f32_16x16x64_bf16 v[32:35], v[96:99], a[0:7], v1\n"
    "v_add_f32 v124, v124, v126\n"
    "v_add_f32 v128, v128, v130\n"
    "v_smfmac_f32_16x16x64_bf16 v[36:39], v[96:99], a[8:15], v1\n"
    "v_add_f32 v125, v125, v127\n"
    "v_add_f32 v129, v129, v131\n"
    "v_smfmac_f32_16x16x64_bf16 v[32:35], v[100:103], a[16:23], v1\n"
    "v_cvt_pk_bf16_f32 v76, v124, v128\n"
    "v_cvt_pk_bf16_f32 v77, v125, v129\n"
    "ds_write_b32 v3, v76 offset:9216\n"
    "ds_write_b32 v3, v77 offset:9488\n"
    "v_smfmac_f32_16x16x64_bf16 v[36:39], v[100:103], a[24:31], v1\n"
    "v_cndmask_b32 v79, v79, v0, vcc\n"
    "v_smfmac_f32_16x16x64_bf16 v[32:35], v[104:107], a[32:39], v1\n"
    "v_smfmac_f32_16x16x64_bf16 v[36:39], v[104:107], a[40:47], v1\n"
    "v_smfmac_f32_16x16x64_bf16 v[32:35], v[108:111], a[48:55], v1\n"
    "s_waitcnt lgkmcnt(0)\n"
    "s_barrier\n"
    "ds_read_b128 v[124:127], v4 offset:29696\n"
    "v_smfmac_f32_16x16x64_bf16 v[36:39], v[108:111], a[56:63], v1\n"
    "ds_read_b128 v[128:131], v4 offset:29760\n"
    "s_add_u32 s73, s73, s52\n"
    "v_smfmac_f32_16x16x64_bf16 v[32:35], v[112:115], a[64:71], v1\n"
    "ds_read_b128 v[132:135], v4 offset:29824\n"
    "v_and_b32 v78, -2, s73\n"
    "v_smfmac_f32_16x16x64_bf16 v[36:39], v[112:115], a[72:79], v1\n"
    "ds_read_b128 v[72:75], v4 offset:29888\n"
    "buffer_store_dword v78, v79, s[40:43], s58 offen offset:3072 sc1\n"
    "v_smfmac_f32_16x16x64_bf16 v[32:35], v[116:119], a[80:87], v1\n"
    "s_waitcnt vmcnt(2)\n"
    "v_and_b32 v76, v12, v13\n"
    "v_smfmac_f32_16x16x64_bf16 v[36:39], v[116:119], a[88:95], v1\n"
    "v_add_u32 v77, -1, v76\n"
    "v_readfirstlane_b32 s59, v76\n"
    "v_smfmac_f32_16x16x64_bf16 v[32:35], v[120:123], a[96:103], v1\n"
    "v_and_or_b32 v15, v77, s45, v5\n"
    "buffer_load_dwordx4 v[16:19], v15, s[40:43], s61 offen sc1\n"
    "v_smfmac_f32_16x16x64_bf16 v[36:39], v[120:123], a[104:111], v1\n"
    "buffer_load_dwordx4 v[20:23], v15, s[40:43], s62 offen sc1\n"
    "buffer_load_dwordx4 v[24:27], v15, s[40:43], s63 offen sc1\n"
    "s_waitcnt lgkmcnt(3)\n"
    "v_mfma_f32_16x16x32_bf16 v[80:83], v[40:43], v[124:127], 0\n"
    "buffer_load_dwordx4 v[28:31], v15, s[40:43], s64 offen sc1\n"
    "buffer_load_dwordx4 v[12:15], v15, s[40:43], s60 offen sc1\n"
    "v_mfma_f32_16x16x32_bf16 v[84:87], v[44:47], v[124:127], 0\n"
    "ds_read_b128 v[40:43], v11 offset:31872\n"
    "ds_read_b128 v[44:47], v11 offset:31872\n"
    "s_waitcnt lgkmcnt(4)\n"
    "v_mfma_f32_16x16x32_bf16 v[80:83], v[48:51], v[128:131], v[80:83]\n"
    "v_permlane32_swap_b32 v32, v34\n"
    "v_permlane32_swap_b32 v36, v38\n"
    "v_mfma_f32_16x16x32_bf16 v[84:87], v[52:55], v[128:131], v[84:87]\n"
    "v_permlane32_swap_b32 v33, v35\n"
    "v_permlane32_swap_b32 v37, v39\n"
    "s_waitcnt lgkmcnt(3)\n"
    "v_mfma_f32_16x16x32_bf16 v[80:83], v[56:59], v[132:135], v[80:83]\n"
    "v_add_f32 v32, v32, v34\n"
    "v_add_f32 v36, v36, v38\n"
    "v_mfma_f32_16x16x32_bf16 v[84:87], v[60:63], v[132:135], v[84:87]\n"
    "v_add_f32 v33, v33, v35\n"
    "v_add_f32 v37, v37, v39\n"
    "s_waitcnt lgkmcnt(2)\n"
    "v_mfma_f32_16x16x32_bf16 v[80:83], v[64:67], v[72:75], v[80:83]\n"
    "v_cvt_pk_bf16_f32 v76, v32, v36\n"
    "v_cvt_pk_bf16_f32 v77, v33, v37\n"
    "ds_write2_b32 v3, v76, v77 offset1:68\n"
    "v_mfma_f32_16x16x32_bf16 v[84:87], v[68:71], v[72:75], v[84:87]\n"
    "s_waitcnt lgkmcnt(2)\n"
    "v_smfmac_f32_16x16x64_bf16 v[40:43], v[88:91], a[112:119], v1\n"
    "s_waitcnt lgkmcnt(1)\n"
    "v_smfmac_f32_16x16x64_bf16 v[44:47], v[88:91], a[120:127], v1\n"
    "s_waitcnt lgkmcnt(0)\n"
    "s_barrier\n"
    "ds_read_b128 v[48:51], v4 offset:20480\n"
    "ds_read_b128 v[52:55], v4 offset:20544\n"
    "ds_read_b128 v[56:59], v4 offset:20608\n"
    "ds_read_b128 v[60:63], v4 offset:20672\n"
    "v_smfmac_f32_16x16x64_bf16 v[40:43], v[92:95], a[128:135], v1\n"
    "v_maximum3_f32 v78, v80, v81, v82\n"
    "v_maximum3_f32 v78, v78, v83, v84\n"
    "v_smfmac_f32_16x16x64_bf16 v[44:47], v[92:95], a[136:143], v1\n"
    "v_maximum3_f32 v78, v78, v85, v86\n"
    "v_maximum3_f32 v78, v78, v87, v87\n"
    "ds_read_b128 v[32:35], v5 offset:8192\n"
    "ds_read_b128 v[36:39], v5 offset:12288\n"
    "v_smfmac_f32_16x16x64_bf16 v[40:43], v[96:99], a[144:151], v1\n"
    "v_mov_b32 v79, v78\n"
    "ds_read_b128 v[68:71], v11 offset:31888\n"
    "ds_read_b64 v[76:77], v11 offset:31904\n"
    "v_smfmac_f32_16x16x64_bf16 v[44:47], v[96:99], a[152:159], v1\n"
    "v_permlane32_swap_b32 v79, v78\n"
    "v_maximum3_f32 v78, v78, v79, v79\n"
    "s_waitcnt lgkmcnt(7)\n"
    "v_mfma_f32_16x16x32_bf16 v[64:67], v[48:51], v[124:127], 0\n"
    "ds_read_b128 v[88:91], v2 offset:14336\n"
    "ds_read_b128 v[92:95], v2 offset:15360\n"
    "s_waitcnt lgkmcnt(8)\n"
    "v_mfma_f32_16x16x32_bf16 v[64:67], v[52:55], v[128:131], v[64:67]\n"
    "s_waitcnt lgkmcnt(7)\n"
    "v_mfma_f32_16x16x32_bf16 v[64:67], v[56:59], v[132:135], v[64:67]\n"
    "s_waitcnt lgkmcnt(6)\n"
    "v_mfma_f32_16x16x32_bf16 v[64:67], v[60:63], v[72:75], v[64:67]\n"
    "v_smfmac_f32_16x16x64_bf16 v[40:43], v[100:103], a[160:167], v1\n"
    "ds_read_b128 v[48:51], v2 offset:18432\n"
    "ds_read_b128 v[52:55], v2 offset:19456\n"
    "v_smfmac_f32_16x16x64_bf16 v[44:47], v[100:103], a[168:175], v1\n"
    "ds_read_b128 v[56:59], v2 offset:20480\n"
    "ds_read_b128 v[60:63], v2 offset:21504\n"
    "v_maximum3_f32 v79, v64, v65, v66\n"
    "v_maximum3_f32 v78, v79, v67, v78\n"
    "v_mov_b32 v79, v78\n"
    "v_smfmac_f32_16x16x64_bf16 v[40:43], v[104:107], a[176:183], v1\n"
    "ds_read_b128 v[96:99], v2 offset:16384\n"
    "ds_read_b128 v[100:103], v2 offset:17408\n"
    "v_permlane16_swap_b32 v79, v78\n"
    "v_maximum3_f32 v78, v78, v79, v79\n"
    "v_mul_f32 v78, 0xbe0293ee, v78\n"
    "v_smfmac_f32_16x16x64_bf16 v[44:47], v[104:107], a[184:191], v1\n"
    "v_fmamk_f32 v80, v80, 0x3e0293ee, v78\n"
    "v_fmamk_f32 v81, v81, 0x3e0293ee, v78\n"
    "v_exp_f32 v80, v80\n"
    "v_exp_f32 v81, v81\n"
    "ds_read_b128 v[124:127], v2 offset:22528\n"
    "ds_read_b128 v[128:131], v2 offset:23552\n"
    "v_smfmac_f32_16x16x64_bf16 v[40:43], v[108:111], a[192:199], v1\n"
    "v_fmamk_f32 v82, v82, 0x3e0293ee, v78\n"
    "v_fmamk_f32 v83, v83, 0x3e0293ee, v78\n"
    "v_exp_f32 v82, v82\n"
    "v_exp_f32 v83, v83\n"
    "v_cvt_pk_bf16_f32 v80, v80, v81\n"
    "v_smfmac_f32_16x16x64_bf16 v[44:47], v[108:111], a[200:207], v1\n"
    "v_fmamk_f32 v84, v84, 0x3e0293ee, v78\n"
    "v_fmamk_f32 v85, v85, 0x3e0293ee, v78\n"
    "v_exp_f32 v84, v84\n"
    "v_exp_f32 v85, v85\n"
    "v_cvt_pk_bf16_f32 v81, v82, v83\n"
    "v_smfmac_f32_16x16x64_bf16 v[40:43], v[112:115], a[208:215], v1\n"
    "v_fmamk_f32 v86, v86, 0x3e0293ee, v78\n"
    "v_fmamk_f32 v87, v87, 0x3e0293ee, v78\n"
    "v_exp_f32 v86, v86\n"
    "v_exp_f32 v87, v87\n"
    "v_cvt_pk_bf16_f32 v82, v84, v85\n"
    "ds_read_b128 v[104:107], v2 offset:24576\n"
    "ds_read_b128 v[108:111], v2 offset:25600\n"
    "v_smfmac_f32_16x16x64_bf16 v[44:47], v[112:115], a[216:223], v1\n"
    "v_fmamk_f32 v64, v64, 0x3e0293ee, v78\n"
    "v_fmamk_f32 v65, v65, 0x3e0293ee, v78\n"
    "v_exp_f32 v64, v64\n"
    "v_exp_f32 v65, v65\n"
    "v_cvt_pk_bf16_f32 v83, v86, v87\n"
    "v_smfmac_f32_16x16x64_bf16 v[40:43], v[116:119], a[224:231], v1\n"
    "v_fmamk_f32 v66, v66, 0x3e0293ee, v78\n"
    "v_fmamk_f32 v67, v67, 0x3e0293ee, v78\n"
    "v_exp_f32 v66, v66\n"
    "v_exp_f32 v67, v67\n"
    "v_cvt_pk_bf16_f32 v64, v64, v65\n"
    "v_smfmac_f32_16x16x64_bf16 v[44:47], v[116:119], a[232:239], v1\n"
    "v_cvt_pk_bf16_f32 v65, v66, v67\n"
    "v_smfmac_f32_16x16x64_bf16 v[40:43], v[120:123], a[240:247], v1\n"
    "ds_read_b128 v[112:115], v2 offset:26624\n"
    "ds_read_b128 v[116:119], v2 offset:27648\n"
    "v_smfmac_f32_16x16x64_bf16 v[44:47], v[120:123], a[248:255], v1\n"
    "buffer_load_dword v79, v0, s[40:43], s58 offen offset:3072 sc1\n"
    "s_waitcnt lgkmcnt(14)\n"
    "v_mfma_f32_16x16x32_bf16 v[72:75], v[68:71], v[80:83], 0\n"
    "v_mfma_f32_16x16x32_bf16 v[84:87], v[32:35], v[80:83], 0\n"
    "ds_read_b128 v[120:123], v11 offset:31872\n"
    "ds_read_b128 v[68:71], v11 offset:31872\n"
    "v_mfma_f32_16x16x32_bf16 v[132:135], v[36:39], v[80:83], 0\n"
    "ds_read_b128 v[32:35], v2 offset:28672\n"
    "ds_read_b128 v[36:39], v2 offset:29696\n"
    "v_cvt_pk_bf16_f32 v40, v40, v41\n"
    "v_cvt_pk_bf16_f32 v41, v42, v43\n"
    "s_waitcnt lgkmcnt(14)\n"
    "v_mfma_f32_16x16x16_bf16 v[72:75], v[76:77], v[64:65], v[72:75]\n"
    "v_cvt_pk_bf16_f32 v42, v44, v45\n"
    "v_cvt_pk_bf16_f32 v43, v46, v47\n"
    "v_mfma_f32_16x16x16_bf16 v[84:87], v[40:41], v[64:65], v[84:87]\n"
    "ds_read_b128 v[80:83], v11 offset:31872\n"
    "ds_read_b128 v[44:47], v11 offset:31872\n"
    "v_mfma_f32_16x16x16_bf16 v[132:135], v[42:43], v[64:65], v[132:135]\n"
    "ds_write_b128 v5, v[40:43] offset:22784\n"
    "ds_read_b128 v[64:67], v11 offset:31872\n"
    "s_and_b32 s49, s48, 1\n"
    "s_lshl_b32 s70, s49, 3\n"
    "s_add_u32 s73, s48, 1\n"
    "s_and_b32 s51, s73, 32\n"
    "s_and_b32 s73, s73, 31\n"
    "s_lshl3_add_u32 s73, s51, s73\n"
    "s_lshl2_add_u32 s73, s73, s92\n"
    "v_rcp_f32 v78, v72\n"
    "s_nop 0\n"
    "v_pk_mul_f32 v[84:85], v[84:85], v[78:79] op_sel_hi:[1,0]\n"
    "v_pk_mul_f32 v[86:87], v[86:87], v[78:79] op_sel_hi:[1,0]\n"
    "v_pk_mul_f32 v[132:133], v[132:133], v[78:79] op_sel_hi:[1,0]\n"
    "v_pk_mul_f32 v[134:135], v[134:135], v[78:79] op_sel_hi:[1,0]\n"
    "v_cvt_pk_bf16_f32 v84, v84, v132\n"
    "v_cvt_pk_bf16_f32 v85, v85, v133\n"
    "v_cvt_pk_bf16_f32 v86, v86, v134\n"
    "v_cvt_pk_bf16_f32 v87, v87, v135\n"
    "ds_write_b128 v6, v[84:87] offset:26880\n"
    "v_mov_b32 v78, s73\n"
    "s_cmp_eq_u32 s48, 63\n"
    "s_waitcnt lgkmcnt(0)\n"
    "s_barrier\n"
    "s_cbranch_scc1 .Lend_%=\n"
    "ds_read_b128 v[132:135], v0 offset:26880\n"
    "ds_read_b128 v[72:75], v0 offset:27904\n"
    "global_load_dword v76, v78, %1 sc1\n"
    "global_load_dword v77, v78, %1 offset:128 sc1\n"
    "s_cmp_lt_u32 %2, 2\n"
    "s_waitcnt lgkmcnt(1)\n"
    "v_smfmac_f32_16x16x64_bf16 v[68:71], v[132:135], v[88:95], v1\n"
    "v_smfmac_f32_16x16x64_bf16 v[80:83], v[132:135], v[96:103], v1\n"
    "ds_read_b128 v[88:91], v2 offset:30720\n"
    "ds_read_b128 v[92:95], v2 offset:31744\n"
    "s_waitcnt lgkmcnt(2)\n"
    "v_smfmac_f32_16x16x64_bf16 v[68:71], v[72:75], v[48:55], v1\n"
    "ds_read_b128 v[96:99], v2 offset:32768\n"
    "ds_read_b128 v[100:103], v2 offset:33792\n"
    "v_smfmac_f32_16x16x64_bf16 v[80:83], v[72:75], v[56:63], v1\n"
    "v_smfmac_f32_16x16x64_bf16 v[120:123], v[132:135], v[124:131], v1\n"
    "v_smfmac_f32_16x16x64_bf16 v[44:47], v[132:135], v[104:111], v1\n"
    "v_smfmac_f32_16x16x64_bf16 v[120:123], v[72:75], v[112:119], v1\n"
    "v_permlane32_swap_b32 v68, v80\n"
    "v_permlane32_swap_b32 v69, v81\n"
    "v_smfmac_f32_16x16x64_bf16 v[44:47], v[72:75], v[32:39], v1\n"
    "s_waitcnt lgkmcnt(0)\n"
    "s_cbranch_scc1 .Lx1_%=\n"
    "v_smfmac_f32_16x16x64_bf16 v[64:67], v[132:135], v[88:95], v1\n"
    "v_smfmac_f32_16x16x64_bf16 v[64:67], v[72:75], v[96:103], v1\n"
    ".Lx1_%=:\n"
    "v_permlane32_swap_b32 v70, v82\n"
    "v_permlane32_swap_b32 v71, v83\n"
    "v_pk_add_f32 v[68:69], v[68:69], v[80:81]\n"
    "v_pk_add_f32 v[70:71], v[70:71], v[82:83]\n"
    "v_bfi_b32 v68, 15, s49, v68\n"
    "v_bfi_b32 v70, 15, s49, v70\n"
    "buffer_store_dwordx4 v[68:71], v7, s[40:43], 0 offen sc1\n"
    "v_permlane32_swap_b32 v120, v44\n"
    "v_permlane32_swap_b32 v121, v45\n"
    "v_permlane32_swap_b32 v122, v46\n"
    "v_permlane32_swap_b32 v123, v47\n"
    "v_pk_add_f32 v[120:121], v[120:121], v[44:45]\n"
    "v_pk_add_f32 v[122:123], v[122:123], v[46:47]\n"
    "v_bfi_b32 v120, 15, s49, v120\n"
    "v_bfi_b32 v122, 15, s49, v122\n"
    "buffer_store_dwordx4 v[120:123], v7, s[40:43], s74 offen sc1\n"
    "s_cbranch_scc1 .Lx2_%=\n"
    "v_permlane32_swap_b32 v64, v66\n"
    "v_permlane32_swap_b32 v65, v67\n"
    "v_pk_add_f32 v[64:65], v[64:65], v[66:67]\n"
    "v_bfi_b32 v64, 15, s49, v64\n"
    "buffer_store_dwordx2 v[64:65], v8, s[40:43], 0 offen sc1\n"
    "s_memrealtime s[38:39]\n"
    "s_waitcnt vmcnt(3)\n"
    "s_branch .Lx3_%=\n"
    ".Lx2_%=:\n"
    "s_memrealtime s[38:39]\n"
    "s_waitcnt vmcnt(2)\n"
    ".Lx3_%=:\n"
    "s_cmp_lg_u32 s59, 0\n"
    "s_cbranch_scc1 .Lk2_%=\n"
    "v_and_b32 v77, v76, v77\n"
    "s_nop 0\n"
    "v_readfirstlane_b32 s59, v77\n"
    "s_cmp_eq_u32 s59, 0\n"
    "s_cbranch_scc1 .Lk2_%=\n"
    "buffer_load_dwordx4 v[12:15], v5, s[40:43], s60 offen sc1\n"
    "buffer_load_dwordx4 v[16:19], v5, s[40:43], s61 offen sc1\n"
    "buffer_load_dwordx4 v[20:23], v5, s[40:43], s62 offen sc1\n"
    "buffer_load_dwordx4 v[24:27], v5, s[40:43], s63 offen sc1\n"
    "buffer_load_dwordx4 v[28:31], v5, s[40:43], s64 offen sc1\n"
    ".Lk2_%=:\n"
    "s_mov_b32 s56, 0\n"
    ".Lan_%=:\n"
    "v_subrev_u32 v78, s52, v79\n"
    "v_cmp_eq_u32 vcc, -1, v79\n"
    "s_cbranch_vccz .Lanok_%=\n"
    "s_sleep 2\n"
    "buffer_load_dword v79, v0, s[40:43], s58 offen offset:3072 sc1\n"
    "s_waitcnt vmcnt(0)\n"
    "s_add_u32 s56, s56, 1\n"
    "s_cmp_lt_u32 s56, s57\n"
    "s_cbranch_scc1 .Lan_%=\n"
    "s_mov_b32 s57, 0\n"
    "v_mov_b32 v135, 1\n"
    "global_store_dword v11, v135, %1 sc1\n"
    ".Lanok_%=:\n"
    "v_max_i32_dpp v78, v78, v78 row_ror:8 row_mask:0xf bank_mask:0xf\n"
    "s_nop 1\n"
    "v_max_i32_dpp v78, v78, v78 row_ror:4 row_mask:0xf bank_mask:0xf\n"
    "s_nop 1\n"
    "v_max_i32_dpp v78, v78, v78 row_ror:2 row_mask:0xf bank_mask:0xf\n"
    "s_nop 1\n"
    "v_max_i32_dpp v78, v78, v78 row_ror:1 row_mask:0xf bank_mask:0xf\n"
    "s_waitcnt lgkmcnt(0)\n"
    "s_sub_u32 s73, s38, s52\n"
    "s_lshl_b32 s73, s73, 3\n"
    "s_sub_u32 s73, s73, s96\n"
    "s_ashr_i32 s73, s73, 2\n"
    "s_max_i32 s73, s73, -16\n"
    "s_min_i32 s73, s73, 16\n"
    "s_add_u32 s96, s96, s73\n"
    "v_readlane_b32 s95, v78, 0\n"
    "v_readlane_b32 s73, v78, 16\n"
    "v_readlane_b32 s51, v78, 32\n"
    "v_readlane_b32 s53, v78, 48\n"
    "s_max_i32 s95, s95, s73\n"
    "s_max_i32 s51, s51, s53\n"
    "s_max_i32 s95, s95, s51\n"
    "s_min_i32 s95, s95, 1024\n"
    "s_add_u32 s95, s95, s52\n"
    "s_add_u32 s95, s95, s55\n"
    "s_cmp_eq_u32 %2, 3\n"
    "s_cbranch_scc1 .Lw3_%=\n"
    "ds_read_b128 v[32:35], v2\n"
    "ds_read_b128 v[36:39], v2 offset:1024\n"
    "ds_read_b128 v[40:43], v2 offset:2048\n"
    "ds_read_b128 v[44:47], v2 offset:3072\n"
    "ds_read_b128 v[48:51], v2 offset:4096\n"
    "ds_read_b128 v[52:55], v2 offset:5120\n"
    "ds_read_b128 v[56:59], v2 offset:6144\n"
    "ds_read_b128 v[60:63], v2 offset:7168\n"
    "ds_read_b128 v[64:67], v2 offset:8192\n"
    "ds_read_b128 v[68:71], v2 offset:9216\n"
    "ds_read_b128 v[72:75], v2 offset:10240\n"
    "ds_read_b128 v[76:79], v2 offset:11264\n"
    "ds_read_b128 v[80:83], v2 offset:12288\n"
    "ds_read_b128 v[84:87], v2 offset:13312\n"
    "v_mov_b64 v[124:125], 0\n"
    "v_mov_b64 v[126:127], 0\n"
    "v_mov_b64 v[128:129], 0\n"
    "v_mov_b64 v[130:131], 0\n"
    "s_mul_i32 s73, s48, 3\n"
    "s_add_u32 s73, s73, s94\n"
    "s_and_b32 s51, s73, 1\n"
    "s_lshl_b32 s51, s51, 12\n"
    "s_lshr_b32 s73, s73, 1\n"
    "s_lshl_b32 s73, s73, 14\n"
    "s_add_u32 s73, s73, s51\n"
    "s_add_u32 s66, s73, s68\n"
    "s_mul_i32 s73, s48, 3\n"
    "s_add_u32 s73, s73, %2\n"
    "s_and_b32 s51, s73, 1\n"
    "s_lshl_b32 s51, s51, 12\n"
    "s_lshr_b32 s73, s73, 1\n"
    "s_lshl_b32 s73, s73, 14\n"
    "s_add_u32 s73, s73, s51\n"
    "s_add_u32 s67, s73, s68\n"
    "s_waitcnt lgkmcnt(0)\n"
    "s_cmp_lt_i32 s71, 0\n"
    "s_cbranch_scc1 .Lnored_%=\n"
    "s_mov_b32 s56, 0\n"
    "s_sub_u32 s53, s95, s54\n"
    "s_waitcnt lgkmcnt(0)\n"
    ".Lgl4_%=:\n"
    "s_memrealtime s[50:51]\n"
    "s_waitcnt lgkmcnt(0)\n"
    "s_sub_u32 s50, s50, s53\n"
    "s_cmp_lt_i32 s50, 0\n"
    "s_cbranch_scc1 .Lgl4_%=\n"
    ".Lred_%=:\n"
    "buffer_load_dwordx4 v[88:91], v0, s[40:43], s65 offen sc1\n"
    "buffer_load_dwordx4 v[92:95], v0, s[40:43], s65 offen offset:1024 sc1\n"
    "buffer_load_dwordx4 v[96:99], v0, s[40:43], s65 offen offset:2048 sc1\n"
    "buffer_load_dwordx4 v[100:103], v0, s[40:43], s65 offen offset:3072 sc1\n"
    "s_add_u32 s73, s66, 0x400000\n"
    "s_waitcnt vmcnt(2)\n"
    "v_add3_u32 v104, v88, v90, v92\n"
    "v_pk_add_f32 v[108:109], v[88:89], v[92:93]\n"
    "v_pk_add_f32 v[110:111], v[90:91], v[94:95]\n"
    "s_waitcnt vmcnt(1)\n"
    "v_add3_u32 v104, v104, v94, v96\n"
    "v_pk_add_f32 v[108:109], v[108:109], v[96:97]\n"
    "v_pk_add_f32 v[110:111], v[110:111], v[98:99]\n"
    "s_waitcnt vmcnt(0)\n"
    "v_add3_u32 v104, v104, v98, v100\n"
    "v_pk_add_f32 v[108:109], v[108:109], v[100:101]\n"
    "v_pk_add_f32 v[110:111], v[110:111], v[102:103]\n"
    "v_add_u32 v104, v104, v102\n"
    "v_and_b32 v104, 15, v104\n"
    "v_permlane32_swap_b32 v108, v110\n"
    "v_permlane32_swap_b32 v109, v111\n"
    "v_cmp_ne_u32 vcc, s70, v104\n"
    "v_pk_add_f32 v[108:109], v[108:109], v[110:111]\n"
    "s_cbranch_vccz .Lredok_%=\n"
    "s_memrealtime s[50:51]\n"
    "s_waitcnt lgkmcnt(0)\n"
    "s_add_u32 s95, s50, 62\n"
    "s_bitset1_b32 s67, 22\n"
    "s_add_u32 s56, s56, 1\n"
    "s_cmp_lt_u32 s56, s57\n"
    "s_cbranch_scc1 .Lred_%=\n"
    "s_mov_b32 s57, 0\n"
    "v_mov_b32 v135, 1\n"
    "global_store_dword v11, v135, %1 sc1\n"
    ".Lredok_%=:\n"
    "s_nop 0\n"
    "v_permlane16_swap_b32 v108, v109\n"
    "v_add_f32 v108, v108, v109\n"
    "s_nop 1\n"
    "v_add_f32_dpp v108, v108, v108 row_ror:8 row_mask:0xf bank_mask:0xf\n"
    "s_nop 1\n"
    "v_mov_b32_dpp v109, v108 quad_perm:[1,0,3,2] row_mask:0xf bank_mask:0xf\n"
    "v_cvt_pk_bf16_f32 v108, v108, v109\n"
    "v_min_u32 v108, -2, v108\n"
    "s_nop 1\n"
    "v_mov_b32_dpp v109, v108 row_shl:2 row_mask:0xf bank_mask:0xf\n"
    "v_mov_b32_dpp v110, v108 row_shl:4 row_mask:0xf bank_mask:0xf\n"
    "v_mov_b32_dpp v111, v108 row_shl:6 row_mask:0xf bank_mask:0xf\n"
    "buffer_store_dwordx4 v[108:111], v9, s[40:43], s66 offen sc1\n"
    "buffer_store_dwordx4 v[108:111], v9, s[40:43], s73 offen sc1\n"
    ".Lpub_%=:\n"
    "s_cmp_lg_u32 s59, 0\n"
    "s_cbranch_scc1 .Lkn5_%=\n"
    "s_waitcnt lgkmcnt(0)\n"
    "s_add_u32 s73, s48, 1\n"
    "s_and_b32 s51, s73, 32\n"
    "s_and_b32 s73, s73, 31\n"
    "s_lshl3_add_u32 s73, s51, s73\n"
    "s_lshl2_add_u32 s73, s73, s92\n"
    "s_mov_b32 s53, s73\n"
    "s_mov_b32 s56, 0\n"
    "v_mov_b32 v134, s53\n"
    ".Lpoll6_%=:\n"
    "global_load_dword v132, v134, %1 sc1\n"
    "global_load_dword v133, v134, %1 offset:128 sc1\n"
    "s_waitcnt vmcnt(0)\n"
    "v_and_b32 v133, v132, v133\n"
    "s_nop 0\n"
    "v_readfirstlane_b32 s73, v133\n"
    "s_cmp_ge_u32 s73, 1\n"
    "s_cbranch_scc1 .Lpoll6_x%=\n"
    "s_add_u32 s56, s56, 1\n"
    "s_cmp_lt_u32 s56, 3\n"
    "s_cbranch_scc1 .Lpoll6_%=\n"
    "s_sleep 8\n"
    "s_cmp_lt_u32 s56, s57\n"
    "s_cbranch_scc1 .Lpoll6_%=\n"
    "s_mov_b32 s57, 0\n"
    "v_mov_b32 v135, 1\n"
    "global_store_dword v11, v135, %1 sc1\n"
    ".Lpoll6_x%=:\n"
    "buffer_load_dwordx4 v[12:15], v5, s[40:43], s60 offen sc1\n"
    "buffer_load_dwordx4 v[16:19], v5, s[40:43], s61 offen sc1\n"
    "buffer_load_dwordx4 v[20:23], v5, s[40:43], s62 offen sc1\n"
    "buffer_load_dwordx4 v[24:27], v5, s[40:43], s63 offen sc1\n"
    "buffer_load_dwordx4 v[28:31], v5, s[40:43], s64 offen sc1\n"
    "s_waitcnt vmcnt(0)\n"
    ".Lkn5_%=:\n"
    "s_add_u32 s73, s48, 2\n"
    "s_and_b32 s73, s73, 63\n"
    "s_lshr_b32 s51, s73, 2\n"
    "s_xor_b32 s51, s51, s69\n"
    "s_and_b32 s51, s51, 1\n"
    "s_lshl_b32 s51, s51, 13\n"
    "s_lshl_b32 s73, s73, 5\n"
    "s_add_u32 s73, s73, %4\n"
    "s_lshl_b32 s73, s73, 14\n"
    "s_add_u32 s73, s73, s51\n"
    "s_add_u32 s60, s73, 0x6000000\n"
    "s_add_u32 s61, s73, 0x6001000\n"
    "s_add_u32 s62, s73, 0x8000000\n"
    "s_add_u32 s63, s73, 0x8001000\n"
    "s_lshl_b32 s51, %5, 12\n"
    "s_add_u32 s73, s73, s51\n"
    "s_add_u32 s64, s73, 0x4000000\n"
    "s_mul_i32 s73, s48, 3\n"
    "s_add_u32 s73, s73, 3\n"
    "s_and_b32 s51, s73, 1\n"
    "s_lshl_b32 s51, s51, 12\n"
    "s_lshr_b32 s73, s73, 1\n"
    "s_lshl_b32 s73, s73, 14\n"
    "s_add_u32 s73, s73, s51\n"
    "s_add_u32 s58, s73, s68\n"
    "s_mov_b32 s56, 0\n"
    "s_sub_u32 s53, s95, 0\n"
    "s_waitcnt lgkmcnt(0)\n"
    ".Lgl7_%=:\n"
    "s_memrealtime s[50:51]\n"
    "s_waitcnt lgkmcnt(0)\n"
    "s_sub_u32 s50, s50, s53\n"
    "s_cmp_lt_i32 s50, 0\n"
    "s_cbranch_scc1 .Lgl7_%=\n"
    "buffer_load_dwordx4 v[88:91], v0, s[40:43], s67 offen sc1\n"
    "buffer_load_dwordx4 v[92:95], v0, s[40:43], s67 offen offset:1024 sc1\n"
    "buffer_load_dwordx4 v[96:99], v0, s[40:43], s67 offen offset:2048 sc1\n"
    "s_add_u32 s73, s48, 2\n"
    "s_and_b32 s51, s73, 32\n"
    "s_and_b32 s73, s73, 31\n"
    "s_lshl3_add_u32 s73, s51, s73\n"
    "s_lshl2_add_u32 s73, s73, s92\n"
    "s_waitcnt lgkmcnt(0)\n"
    "s_barrier\n"
    "ds_write_b128 v5, v[12:15]\n"
    "ds_write_b128 v5, v[16:19] offset:4096\n"
    "ds_write_b128 v5, v[28:31] offset:16384\n"
    "v_swap_b32 v22, v24\n"
    "v_swap_b32 v23, v25\n"
    "ds_write_b128 v5, v[20:23] offset:8192\n"
    "ds_write_b128 v5, v[24:27] offset:12288\n"
    "v_mov_b32 v14, s73\n"
    ".Lgchk_%=:\n"
    "s_waitcnt vmcnt(1)\n"
    "v_max3_u32 v100, v88, v90, v92\n"
    "ds_write_b128 v10, v[88:91] offset:20480\n"
    "ds_write_b128 v10, v[92:95] offset:21504\n"
    "s_waitcnt vmcnt(0)\n"
    "v_max3_u32 v100, v100, v94, v96\n"
    "ds_write_b128 v10, v[96:99] offset:22528\n"
    "v_max_u32 v100, v100, v98\n"
    "v_cmp_eq_u32 vcc, -1, v100\n"
    "s_cbranch_vccz .Lgok_%=\n"
    "s_bitset1_b32 s67, 22\n"
    "s_waitcnt lgkmcnt(0)\n"
    "buffer_load_dwordx4 v[88:91], v0, s[40:43], s67 offen sc1\n"
    "buffer_load_dwordx4 v[92:95], v0, s[40:43], s67 offen offset:1024 sc1\n"
    "buffer_load_dwordx4 v[96:99], v0, s[40:43], s67 offen offset:2048 sc1\n"
    "s_add_u32 s56, s56, 1\n"
    "s_cmp_lt_u32 s56, s57\n"
    "s_cbranch_scc1 .Lgchk_%=\n"
    "s_mov_b32 s57, 0\n"
    "v_mov_b32 v135, 1\n"
    "global_store_dword v11, v135, %1 sc1\n"
    "s_waitcnt vmcnt(0)\n"
    ".Lgok_%=:\n"
    "global_load_dword v12, v14, %1 sc1\n"
    "global_load_dword v13, v14, %1 offset:128 sc1\n"
    "global_load_dword v14, v14, %1 sc1\n"
    "s_add_u32 s48, s48, 1\n"
    "s_waitcnt lgkmcnt(0)\n"
    "s_barrier\n"
    "s_branch .Lstep_%=\n"
    ".Lnored_%=:\n"
    "s_waitcnt vmcnt(0)\n"
    "s_branch .Lpub_%=\n"
    ".Lw3_%=:\n"
    "ds_read_b128 v[32:35], v0 offset:16384\n"
    "ds_read_b128 v[48:51], v0\n"
    "ds_read_b128 v[64:67], v0 offset:4096\n"
    "ds_read_b128 v[80:83], v4 offset:20480\n"
    "ds_read_b128 v[36:39], v0 offset:17408\n"
    "ds_read_b128 v[52:55], v0 offset:1024\n"
    "ds_read_b128 v[68:71], v0 offset:5120\n"
    "ds_read_b128 v[84:87], v4 offset:20544\n"
    "ds_read_b128 v[40:43], v0 offset:18432\n"
    "ds_read_b128 v[56:59], v0 offset:2048\n"
    "ds_read_b128 v[72:75], v0 offset:6144\n"
    "ds_read_b128 v[88:91], v4 offset:20608\n"
    "ds_read_b128 v[44:47], v0 offset:19456\n"
    "ds_read_b128 v[60:63], v0 offset:3072\n"
    "ds_read_b128 v[76:79], v0 offset:7168\n"
    "ds_read_b128 v[92:95], v4 offset:20672\n"
    "s_waitcnt lgkmcnt(14)\n"
    "v_mfma_f32_16x16x32_bf16 v[96:99], v[48:51], v[32:35], 0\n"
    "s_waitcnt lgkmcnt(13)\n"
    "v_mfma_f32_16x16x32_bf16 v[100:103], v[64:67], v[32:35], 0\n"
    "s_waitcnt lgkmcnt(12)\n"
    "v_mfma_f32_16x16x32_bf16 v[104:107], v[80:83], v[32:35], 0\n"
    "s_waitcnt lgkmcnt(10)\n"
    "v_mfma_f32_16x16x32_bf16 v[96:99], v[52:55], v[36:39], v[96:99]\n"
    "s_waitcnt lgkmcnt(9)\n"
    "v_mfma_f32_16x16x32_bf16 v[100:103], v[68:71], v[36:39], v[100:103]\n"
    "s_waitcnt lgkmcnt(8)\n"
    "v_mfma_f32_16x16x32_bf16 v[104:107], v[84:87], v[36:39], v[104:107]\n"
    "s_waitcnt lgkmcnt(6)\n"
    "v_mfma_f32_16x16x32_bf16 v[96:99], v[56:59], v[40:43], v[96:99]\n"
    "s_waitcnt lgkmcnt(5)\n"
    "v_mfma_f32_16x16x32_bf16 v[100:103], v[72:75], v[40:43], v[100:103]\n"
    "s_waitcnt lgkmcnt(4)\n"
    "v_mfma_f32_16x16x32_bf16 v[104:107], v[88:91], v[40:43], v[104:107]\n"
    "s_waitcnt lgkmcnt(2)\n"
    "v_mfma_f32_16x16x32_bf16 v[96:99], v[60:63], v[44:47], v[96:99]\n"
    "s_waitcnt lgkmcnt(1)\n"
    "v_mfma_f32_16x16x32_bf16 v[100:103], v[76:79], v[44:47], v[100:103]\n"
    "s_waitcnt lgkmcnt(0)\n"
    "v_mfma_f32_16x16x32_bf16 v[104:107], v[92:95], v[44:47], v[104:107]\n"
    "ds_read_b128 v[32:35], v0 offset:8192\n"
    "ds_read_b128 v[36:39], v0 offset:12288\n"
    "ds_read_b128 v[40:43], v0 offset:9216\n"
    "ds_read_b128 v[44:47], v0 offset:13312\n"
    "ds_read_b128 v[48:51], v0 offset:10240\n"
    "ds_read_b128 v[52:55], v0 offset:14336\n"
    "ds_read_b128 v[56:59], v0 offset:11264\n"
    "ds_read_b128 v[60:63], v0 offset:15360\n"
    "ds_read_b128 v[64:67], v0 offset:22784\n"
    "ds_read_b128 v[68:71], v0 offset:23808\n"
    "ds_read_b128 v[72:75], v0 offset:24832\n"
    "ds_read_b128 v[76:79], v0 offset:25856\n"
    "ds_read_b128 v[80:83], v11 offset:31888\n"
    "ds_read_b64 v[84:85], v11 offset:31904\n"
    "v_mov_b32 v86, 0xd01502f9\n"
    "v_cndmask_b32 v96, v86, v96, s[76:77]\n"
    "v_cndmask_b32 v97, v86, v97, s[78:79]\n"
    "v_cndmask_b32 v98, v86, v98, s[80:81]\n"
    "v_cndmask_b32 v99, v86, v99, s[82:83]\n"
    "v_cndmask_b32 v100, v86, v100, s[84:85]\n"
    "v_cndmask_b32 v101, v86, v101, s[86:87]\n"
    "v_cndmask_b32 v102, v86, v102, s[88:89]\n"
    "v_cndmask_b32 v103, v86, v103, s[90:91]\n"
    "v_maximum3_f32 v87, v104, v105, v106\n"
    "v_maximum3_f32 v87, v87, v107, v96\n"
    "v_maximum3_f32 v87, v87, v97, v98\n"
    "v_maximum3_f32 v87, v87, v99, v100\n"
    "v_maximum3_f32 v87, v87, v101, v102\n"
    "v_maximum3_f32 v87, v87, v103, v103\n"
    "v_mov_b32 v86, v87\n"
    "s_nop 1\n"
    "v_permlane16_swap_b32 v86, v87\n"
    "v_maximum3_f32 v87, v87, v86, v86\n"
    "v_mov_b32 v86, v87\n"
    "s_nop 1\n"
    "v_permlane32_swap_b32 v86, v87\n"
    "v_maximum3_f32 v87, v87, v86, v86\n"
    "v_mul_f32 v87, 0xbe0293ee, v87\n"
    "v_fmamk_f32 v96, v96, 0x3e0293ee, v87\n"
    "v_fmamk_f32 v97, v97, 0x3e0293ee, v87\n"
    "v_fmamk_f32 v98, v98, 0x3e0293ee, v87\n"
    "v_fmamk_f32 v99, v99, 0x3e0293ee, v87\n"
    "v_fmamk_f32 v100, v100, 0x3e0293ee, v87\n"
    "v_fmamk_f32 v101, v101, 0x3e0293ee, v87\n"
    "v_fmamk_f32 v102, v102, 0x3e0293ee, v87\n"
    "v_fmamk_f32 v103, v103, 0x3e0293ee, v87\n"
    "v_fmamk_f32 v104, v104, 0x3e0293ee, v87\n"
    "v_fmamk_f32 v105, v105, 0x3e0293ee, v87\n"
    "v_fmamk_f32 v106, v106, 0x3e0293ee, v87\n"
    "v_fmamk_f32 v107, v107, 0x3e0293ee, v87\n"
    "v_exp_f32 v96, v96\n"
    "v_exp_f32 v97, v97\n"
    "v_exp_f32 v98, v98\n"
    "v_exp_f32 v99, v99\n"
    "v_exp_f32 v100, v100\n"
    "v_exp_f32 v101, v101\n"
    "v_exp_f32 v102, v102\n"
    "v_exp_f32 v103, v103\n"
    "v_exp_f32 v104, v104\n"
    "v_exp_f32 v105, v105\n"
    "v_exp_f32 v106, v106\n"
    "v_exp_f32 v107, v107\n"
    "v_cvt_pk_bf16_f32 v96, v96, v97\n"
    "v_cvt_pk_bf16_f32 v97, v98, v99\n"
    "v_cvt_pk_bf16_f32 v98, v100, v101\n"
    "v_cvt_pk_bf16_f32 v99, v102, v103\n"
    "v_cvt_pk_bf16_f32 v104, v104, v105\n"
    "v_cvt_pk_bf16_f32 v105, v106, v107\n"
    "s_waitcnt lgkmcnt(1)\n"
    "v_mfma_f32_16x16x32_bf16 v[108:111], v[80:83], v[96:99], 0\n"
    "v_mfma_f32_16x16x32_bf16 v[88:91], v[32:35], v[96:99], 0\n"
    "v_mfma_f32_16x16x32_bf16 v[120:123], v[36:39], v[96:99], 0\n"
    "v_mfma_f32_16x16x32_bf16 v[92:95], v[40:43], v[96:99], 0\n"
    "s_waitcnt lgkmcnt(0)\n"
    "v_mfma_f32_16x16x16_bf16 v[108:111], v[84:85], v[104:105], v[108:111]\n"
    "v_mfma_f32_16x16x32_bf16 v[124:127], v[44:47], v[96:99], 0\n"
    "v_mfma_f32_16x16x32_bf16 v[112:115], v[48:51], v[96:99], 0\n"
    "v_mfma_f32_16x16x32_bf16 v[128:131], v[52:55], v[96:99], 0\n"
    "v_mfma_f32_16x16x32_bf16 v[116:119], v[56:59], v[96:99], 0\n"
    "v_mfma_f32_16x16x32_bf16 v[132:135], v[60:63], v[96:99], 0\n"
    "v_rcp_f32 v86, v108\n"
    "v_mfma_f32_16x16x16_bf16 v[88:91], v[64:65], v[104:105], v[88:91]\n"
    "v_mfma_f32_16x16x16_bf16 v[120:123], v[66:67], v[104:105], v[120:123]\n"
    "v_mfma_f32_16x16x16_bf16 v[92:95], v[68:69], v[104:105], v[92:95]\n"
    "v_mfma_f32_16x16x16_bf16 v[124:127], v[70:71], v[104:105], v[124:127]\n"
    "v_mfma_f32_16x16x16_bf16 v[112:115], v[72:73], v[104:105], v[112:115]\n"
    "v_mfma_f32_16x16x16_bf16 v[128:131], v[74:75], v[104:105], v[128:131]\n"
    "v_mfma_f32_16x16x16_bf16 v[116:119], v[76:77], v[104:105], v[116:119]\n"
    "v_mfma_f32_16x16x16_bf16 v[132:135], v[78:79], v[104:105], v[132:135]\n"
    "v_pk_mul_f32 v[88:89], v[88:89], v[86:87] op_sel_hi:[1,0]\n"
    "v_pk_mul_f32 v[90:91], v[90:91], v[86:87] op_sel_hi:[1,0]\n"
    "v_pk_mul_f32 v[120:121], v[120:121], v[86:87] op_sel_hi:[1,0]\n"
    "v_pk_mul_f32 v[122:123], v[122:123], v[86:87] op_sel_hi:[1,0]\n"
    "v_cvt_pk_bf16_f32 v88, v88, v120\n"
    "v_cvt_pk_bf16_f32 v89, v89, v121\n"
    "v_cvt_pk_bf16_f32 v90, v90, v122\n"
    "v_cvt_pk_bf16_f32 v91, v91, v123\n"
    "v_pk_mul_f32 v[92:93], v[92:93], v[86:87] op_sel_hi:[1,0]\n"
    "v_pk_mul_f32 v[94:95], v[94:95], v[86:87] op_sel_hi:[1,0]\n"
    "v_pk_mul_f32 v[124:125], v[124:125], v[86:87] op_sel_hi:[1,0]\n"
    "v_pk_mul_f32 v[126:127], v[126:127], v[86:87] op_sel_hi:[1,0]\n"
    "v_cvt_pk_bf16_f32 v92, v92, v124\n"
    "v_cvt_pk_bf16_f32 v93, v93, v125\n"
    "v_cvt_pk_bf16_f32 v94, v94, v126\n"
    "v_cvt_pk_bf16_f32 v95, v95, v127\n"
    "v_pk_mul_f32 v[112:113], v[112:113], v[86:87] op_sel_hi:[1,0]\n"
    "v_pk_mul_f32 v[114:115], v[114:115], v[86:87] op_sel_hi:[1,0]\n"
    "v_pk_mul_f32 v[128:129], v[128:129], v[86:87] op_sel_hi:[1,0]\n"
    "v_pk_mul_f32 v[130:131], v[130:131], v[86:87] op_sel_hi:[1,0]\n"
    "v_cvt_pk_bf16_f32 v112, v112, v128\n"
    "v_cvt_pk_bf16_f32 v113, v113, v129\n"
    "v_cvt_pk_bf16_f32 v114, v114, v130\n"
    "v_cvt_pk_bf16_f32 v115, v115, v131\n"
    "v_pk_mul_f32 v[116:117], v[116:117], v[86:87] op_sel_hi:[1,0]\n"
    "v_pk_mul_f32 v[118:119], v[118:119], v[86:87] op_sel_hi:[1,0]\n"
    "v_pk_mul_f32 v[132:133], v[132:133], v[86:87] op_sel_hi:[1,0]\n"
    "v_pk_mul_f32 v[134:135], v[134:135], v[86:87] op_sel_hi:[1,0]\n"
    "v_cvt_pk_bf16_f32 v116, v116, v132\n"
    "v_cvt_pk_bf16_f32 v117, v117, v133\n"
    "v_cvt_pk_bf16_f32 v118, v118, v134\n"
    "v_cvt_pk_bf16_f32 v119, v119, v135\n"
    "ds_read_b128 v[32:35], v2\n"
    "ds_read_b128 v[36:39], v2 offset:1024\n"
    "ds_read_b128 v[40:43], v2 offset:2048\n"
    "ds_read_b128 v[44:47], v2 offset:3072\n"
    "ds_read_b128 v[48:51], v2 offset:4096\n"
    "ds_read_b128 v[52:55], v2 offset:5120\n"
    "ds_read_b128 v[56:59], v2 offset:6144\n"
    "ds_read_b128 v[60:63], v2 offset:7168\n"
    "ds_read_b128 v[64:67], v2 offset:8192\n"
    "ds_read_b128 v[68:71], v2 offset:9216\n"
    "ds_read_b128 v[72:75], v2 offset:10240\n"
    "ds_read_b128 v[76:79], v2 offset:11264\n"
    "ds_read_b128 v[80:83], v2 offset:12288\n"
    "ds_read_b128 v[84:87], v2 offset:13312\n"
    "v_mov_b64 v[124:125], 0\n"
    "v_mov_b64 v[126:127], 0\n"
    "v_mov_b64 v[128:129], 0\n"
    "v_mov_b64 v[130:131], 0\n"
    "s_waitcnt vmcnt(0)\n"
    "s_cmp_lg_u32 s59, 0\n"
    "s_cbranch_scc1 .Lkn8_%=\n"
    "s_waitcnt lgkmcnt(0)\n"
    "s_add_u32 s73, s48, 1\n"
    "s_and_b32 s51, s73, 32\n"
    "s_and_b32 s73, s73, 31\n"
    "s_lshl3_add_u32 s73, s51, s73\n"
    "s_lshl2_add_u32 s73, s73, s92\n"
    "s_mov_b32 s53, s73\n"
    "s_mov_b32 s56, 0\n"
    "v_mov_b32 v134, s53\n"
    ".Lpoll9_%=:\n"
    "global_load_dword v132, v134, %1 sc1\n"
    "global_load_dword v133, v134, %1 offset:128 sc1\n"
    "s_waitcnt vmcnt(0)\n"
    "v_and_b32 v133, v132, v133\n"
    "s_nop 0\n"
    "v_readfirstlane_b32 s73, v133\n"
    "s_cmp_ge_u32 s73, 1\n"
    "s_cbranch_scc1 .Lpoll9_x%=\n"
    "s_add_u32 s56, s56, 1\n"
    "s_cmp_lt_u32 s56, 3\n"
    "s_cbranch_scc1 .Lpoll9_%=\n"
    "s_sleep 8\n"
    "s_cmp_lt_u32 s56, s57\n"
    "s_cbranch_scc1 .Lpoll9_%=\n"
    "s_mov_b32 s57, 0\n"
    "v_mov_b32 v135, 1\n"
    "global_store_dword v11, v135, %1 sc1\n"
    ".Lpoll9_x%=:\n"
    "buffer_load_dwordx4 v[12:15], v5, s[40:43], s60 offen sc1\n"
    "buffer_load_dwordx4 v[16:19], v5, s[40:43], s61 offen sc1\n"
    "buffer_load_dwordx4 v[20:23], v5, s[40:43], s62 offen sc1\n"
    "buffer_load_dwordx4 v[24:27], v5, s[40:43], s63 offen sc1\n"
    "buffer_load_dwordx4 v[28:31], v5, s[40:43], s64 offen sc1\n"
    "s_waitcnt vmcnt(0)\n"
    ".Lkn8_%=:\n"
    "s_add_u32 s73, s48, 2\n"
    "s_and_b32 s73, s73, 63\n"
    "s_lshr_b32 s51, s73, 2\n"
    "s_xor_b32 s51, s51, s69\n"
    "s_and_b32 s51, s51, 1\n"
    "s_lshl_b32 s51, s51, 13\n"
    "s_lshl_b32 s73, s73, 5\n"
    "s_add_u32 s73, s73, %4\n"
    "s_lshl_b32 s73, s73, 14\n"
    "s_add_u32 s73, s73, s51\n"
    "s_add_u32 s60, s73, 0x6000000\n"
    "s_add_u32 s61, s73, 0x6001000\n"
    "s_add_u32 s62, s73, 0x8000000\n"
    "s_add_u32 s63, s73, 0x8001000\n"
    "s_lshl_b32 s51, %5, 12\n"
    "s_add_u32 s73, s73, s51\n"
    "s_add_u32 s64, s73, 0x4000000\n"
    "s_mul_i32 s73, s48, 3\n"
    "s_add_u32 s73, s73, 3\n"
    "s_and_b32 s51, s73, 1\n"
    "s_lshl_b32 s51, s51, 12\n"
    "s_lshr_b32 s73, s73, 1\n"
    "s_lshl_b32 s73, s73, 14\n"
    "s_add_u32 s73, s73, s51\n"
    "s_add_u32 s58, s73, s68\n"
    "s_sub_u32 s53, s95, 16\n"
    "s_waitcnt lgkmcnt(0)\n"
    ".Lgl10_%=:\n"
    "s_memrealtime s[50:51]\n"
    "s_waitcnt lgkmcnt(0)\n"
    "s_sub_u32 s50, s50, s53\n"
    "s_cmp_lt_i32 s50, 0\n"
    "s_cbranch_scc1 .Lgl10_%=\n"
    "s_lshr_b32 s51, s48, 2\n"
    "s_xor_b32 s51, s51, s69\n"
    "s_and_b32 s51, s51, 1\n"
    "s_lshl_b32 s51, s51, 13\n"
    "s_lshl_b32 s73, s48, 19\n"
    "s_add_u32 s73, s73, s75\n"
    "s_add_u32 s73, s73, s51\n"
    "buffer_store_dwordx4 v[88:91], v0, s[40:43], s73 offen sc1\n"
    "buffer_store_dwordx4 v[92:95], v0, s[40:43], s73 offen offset:1024 sc1\n"
    "buffer_store_dwordx4 v[112:115], v0, s[40:43], s73 offen offset:2048 sc1\n"
    "buffer_store_dwordx4 v[116:119], v0, s[40:43], s73 offen offset:3072 sc1\n"
    "s_add_u32 s73, s48, 2\n"
    "s_and_b32 s51, s73, 32\n"
    "s_and_b32 s73, s73, 31\n"
    "s_lshl3_add_u32 s73, s51, s73\n"
    "s_lshl2_add_u32 s73, s73, s92\n"
    "s_waitcnt lgkmcnt(0)\n"
    "s_barrier\n"
    "ds_write_b128 v5, v[12:15]\n"
    "ds_write_b128 v5, v[16:19] offset:4096\n"
    "ds_write_b128 v5, v[28:31] offset:16384\n"
    "v_swap_b32 v22, v24\n"
    "v_swap_b32 v23, v25\n"
    "ds_write_b128 v5, v[20:23] offset:8192\n"
    "ds_write_b128 v5, v[24:27] offset:12288\n"
    "v_mov_b32 v14, s73\n"
    "s_waitcnt vmcnt(0)\n"
    "global_load_dword v12, v14, %1 sc1\n"
    "global_load_dword v13, v14, %1 offset:128 sc1\n"
    "s_add_u32 s73, s48, 0\n"
    "s_and_b32 s51, s73, 32\n"
    "s_and_b32 s73, s73, 31\n"
    "s_lshl3_add_u32 s73, s51, s73\n"
    "s_lshl2_add_u32 s73, s73, s93\n"
    "v_mov_b32 v134, s73\n"
    "v_mov_b32 v135, 1\n"
    "s_mov_b64 exec, 1\n"
    "global_store_dword v134, v135, %1 sc1\n"
    "s_mov_b64 exec, -1\n"
    "s_add_u32 s48, s48, 1\n"
    "s_waitcnt lgkmcnt(0)\n"
    "s_barrier\n"
    "s_branch .Lstep_%=\n"
    ".Lend_%=:\n"
    "s_waitcnt vmcnt(0)\n"
    "s_cmp_eq_u32 %2, 3\n"
    "s_cbranch_scc0 .Lout_%=\n"
    "ds_read_b128 v[32:35], v0 offset:16384\n"
    "ds_read_b128 v[48:51], v0\n"
    "ds_read_b128 v[64:67], v0 offset:4096\n"
    "ds_read_b128 v[80:83], v4 offset:20480\n"
    "ds_read_b128 v[36:39], v0 offset:17408\n"
    "ds_read_b128 v[52:55], v0 offset:1024\n"
    "ds_read_b128 v[68:71], v0 offset:5120\n"
    "ds_read_b128 v[84:87], v4 offset:20544\n"
    "ds_read_b128 v[40:43], v0 offset:18432\n"
    "ds_read_b128 v[56:59], v0 offset:2048\n"
    "ds_read_b128 v[72:75], v0 offset:6144\n"
    "ds_read_b128 v[88:91], v4 offset:20608\n"
    "ds_read_b128 v[44:47], v0 offset:19456\n"
    "ds_read_b128 v[60:63], v0 offset:3072\n"
    "ds_read_b128 v[76:79], v0 offset:7168\n"
    "ds_read_b128 v[92:95], v4 offset:20672\n"
    "s_waitcnt lgkmcnt(14)\n"
    "v_mfma_f32_16x16x32_bf16 v[96:99], v[48:51], v[32:35], 0\n"
    "s_waitcnt lgkmcnt(13)\n"
    "v_mfma_f32_16x16x32_bf16 v[100:103], v[64:67], v[32:35], 0\n"
    "s_waitcnt lgkmcnt(12)\n"
    "v_mfma_f32_16x16x32_bf16 v[104:107], v[80:83], v[32:35], 0\n"
    "s_waitcnt lgkmcnt(10)\n"
    "v_mfma_f32_16x16x32_bf16 v[96:99], v[52:55], v[36:39], v[96:99]\n"
    "s_waitcnt lgkmcnt(9)\n"
    "v_mfma_f32_16x16x32_bf16 v[100:103], v[68:71], v[36:39], v[100:103]\n"
    "s_waitcnt lgkmcnt(8)\n"
    "v_mfma_f32_16x16x32_bf16 v[104:107], v[84:87], v[36:39], v[104:107]\n"
    "s_waitcnt lgkmcnt(6)\n"
    "v_mfma_f32_16x16x32_bf16 v[96:99], v[56:59], v[40:43], v[96:99]\n"
    "s_waitcnt lgkmcnt(5)\n"
    "v_mfma_f32_16x16x32_bf16 v[100:103], v[72:75], v[40:43], v[100:103]\n"
    "s_waitcnt lgkmcnt(4)\n"
    "v_mfma_f32_16x16x32_bf16 v[104:107], v[88:91], v[40:43], v[104:107]\n"
    "s_waitcnt lgkmcnt(2)\n"
    "v_mfma_f32_16x16x32_bf16 v[96:99], v[60:63], v[44:47], v[96:99]\n"
    "s_waitcnt lgkmcnt(1)\n"
    "v_mfma_f32_16x16x32_bf16 v[100:103], v[76:79], v[44:47], v[100:103]\n"
    "s_waitcnt lgkmcnt(0)\n"
    "v_mfma_f32_16x16x32_bf16 v[104:107], v[92:95], v[44:47], v[104:107]\n"
    "ds_read_b128 v[32:35], v0 offset:8192\n"
    "ds_read_b128 v[36:39], v0 offset:12288\n"
    "ds_read_b128 v[40:43], v0 offset:9216\n"
    "ds_read_b128 v[44:47], v0 offset:13312\n"
    "ds_read_b128 v[48:51], v0 offset:10240\n"
    "ds_read_b128 v[52:55], v0 offset:14336\n"
    "ds_read_b128 v[56:59], v0 offset:11264\n"
    "ds_read_b128 v[60:63], v0 offset:15360\n"
    "ds_read_b128 v[64:67], v0 offset:22784\n"
    "ds_read_b128 v[68:71], v0 offset:23808\n"
    "ds_read_b128 v[72:75], v0 offset:24832\n"
    "ds_read_b128 v[76:79], v0 offset:25856\n"
    "ds_read_b128 v[80:83], v11 offset:31888\n"
    "ds_read_b64 v[84:85], v11 offset:31904\n"
    "v_mov_b32 v86, 0xd01502f9\n"
    "v_cndmask_b32 v96, v86, v96, s[76:77]\n"
    "v_cndmask_b32 v97, v86, v97, s[78:79]\n"
    "v_cndmask_b32 v98, v86, v98, s[80:81]\n"
    "v_cndmask_b32 v99, v86, v99, s[82:83]\n"
    "v_cndmask_b32 v100, v86, v100, s[84:85]\n"
    "v_cndmask_b32 v101, v86, v101, s[86:87]\n"
    "v_cndmask_b32 v102, v86, v102, s[88:89]\n"
    "v_cndmask_b32 v103, v86, v103, s[90:91]\n"
    "v_maximum3_f32 v87, v104, v105, v106\n"
    "v_maximum3_f32 v87, v87, v107, v96\n"
    "v_maximum3_f32 v87, v87, v97, v98\n"
    "v_maximum3_f32 v87, v87, v99, v100\n"
    "v_maximum3_f32 v87, v87, v101, v102\n"
    "v_maximum3_f32 v87, v87, v103, v103\n"
    "v_mov_b32 v86, v87\n"
    "s_nop 1\n"
    "v_permlane16_swap_b32 v86, v87\n"
    "v_maximum3_f32 v87, v87, v86, v86\n"
    "v_mov_b32 v86, v87\n"
    "s_nop 1\n"
    "v_permlane32_swap_b32 v86, v87\n"
    "v_maximum3_f32 v87, v87, v86, v86\n"
    "v_mul_f32 v87, 0xbe0293ee, v87\n"
    "v_fmamk_f32 v96, v96, 0x3e0293ee, v87\n"
    "v_fmamk_f32 v97, v97, 0x3e0293ee, v87\n"
    "v_fmamk_f32 v98, v98, 0x3e0293ee, v87\n"
    "v_fmamk_f32 v99, v99, 0x3e0293ee, v87\n"
    "v_fmamk_f32 v100, v100, 0x3e0293ee, v87\n"
    "v_fmamk_f32 v101, v101, 0x3e0293ee, v87\n"
    "v_fmamk_f32 v102, v102, 0x3e0293ee, v87\n"
    "v_fmamk_f32 v103, v103, 0x3e0293ee, v87\n"
    "v_fmamk_f32 v104, v104, 0x3e0293ee, v87\n"
    "v_fmamk_f32 v105, v105, 0x3e0293ee, v87\n"
    "v_fmamk_f32 v106, v106, 0x3e0293ee, v87\n"
    "v_fmamk_f32 v107, v107, 0x3e0293ee, v87\n"
    "v_exp_f32 v96, v96\n"
    "v_exp_f32 v97, v97\n"
    "v_exp_f32 v98, v98\n"
    "v_exp_f32 v99, v99\n"
    "v_exp_f32 v100, v100\n"
    "v_exp_f32 v101, v101\n"
    "v_exp_f32 v102, v102\n"
    "v_exp_f32 v103, v103\n"
    "v_exp_f32 v104, v104\n"
    "v_exp_f32 v105, v105\n"
    "v_exp_f32 v106, v106\n"
    "v_exp_f32 v107, v107\n"
    "v_cvt_pk_bf16_f32 v96, v96, v97\n"
    "v_cvt_pk_bf16_f32 v97, v98, v99\n"
    "v_cvt_pk_bf16_f32 v98, v100, v101\n"
    "v_cvt_pk_bf16_f32 v99, v102, v103\n"
    "v_cvt_pk_bf16_f32 v104, v104, v105\n"
    "v_cvt_pk_bf16_f32 v105, v106, v107\n"
    "s_waitcnt lgkmcnt(1)\n"
    "v_mfma_f32_16x16x32_bf16 v[108:111], v[80:83], v[96:99], 0\n"
    "v_mfma_f32_16x16x32_bf16 v[88:91], v[32:35], v[96:99], 0\n"
    "v_mfma_f32_16x16x32_bf16 v[120:123], v[36:39], v[96:99], 0\n"
    "v_mfma_f32_16x16x32_bf16 v[92:95], v[40:43], v[96:99], 0\n"
    "s_waitcnt lgkmcnt(0)\n"
    "v_mfma_f32_16x16x16_bf16 v[108:111], v[84:85], v[104:105], v[108:111]\n"
    "v_mfma_f32_16x16x32_bf16 v[124:127], v[44:47], v[96:99], 0\n"
    "v_mfma_f32_16x16x32_bf16 v[112:115], v[48:51], v[96:99], 0\n"
    "v_mfma_f32_16x16x32_bf16 v[128:131], v[52:55], v[96:99], 0\n"
    "v_mfma_f32_16x16x32_bf16 v[116:119], v[56:59], v[96:99], 0\n"
    "v_mfma_f32_16x16x32_bf16 v[132:135], v[60:63], v[96:99], 0\n"
    "v_rcp_f32 v86, v108\n"
    "v_mfma_f32_16x16x16_bf16 v[88:91], v[64:65], v[104:105], v[88:91]\n"
    "v_mfma_f32_16x16x16_bf16 v[120:123], v[66:67], v[104:105], v[120:123]\n"
    "v_mfma_f32_16x16x16_bf16 v[92:95], v[68:69], v[104:105], v[92:95]\n"
    "v_mfma_f32_16x16x16_bf16 v[124:127], v[70:71], v[104:105], v[124:127]\n"
    "v_mfma_f32_16x16x16_bf16 v[112:115], v[72:73], v[104:105], v[112:115]\n"
    "v_mfma_f32_16x16x16_bf16 v[128:131], v[74:75], v[104:105], v[128:131]\n"
    "v_mfma_f32_16x16x16_bf16 v[116:119], v[76:77], v[104:105], v[116:119]\n"
    "v_mfma_f32_16x16x16_bf16 v[132:135], v[78:79], v[104:105], v[132:135]\n"
    "v_pk_mul_f32 v[88:89], v[88:89], v[86:87] op_sel_hi:[1,0]\n"
    "v_pk_mul_f32 v[90:91], v[90:91], v[86:87] op_sel_hi:[1,0]\n"
    "v_pk_mul_f32 v[120:121], v[120:121], v[86:87] op_sel_hi:[1,0]\n"
    "v_pk_mul_f32 v[122:123], v[122:123], v[86:87] op_sel_hi:[1,0]\n"
    "v_cvt_pk_bf16_f32 v88, v88, v120\n"
    "v_cvt_pk_bf16_f32 v89, v89, v121\n"
    "v_cvt_pk_bf16_f32 v90, v90, v122\n"
    "v_cvt_pk_bf16_f32 v91, v91, v123\n"
    "v_pk_mul_f32 v[92:93], v[92:93], v[86:87] op_sel_hi:[1,0]\n"
    "v_pk_mul_f32 v[94:95], v[94:95], v[86:87] op_sel_hi:[1,0]\n"
    "v_pk_mul_f32 v[124:125], v[124:125], v[86:87] op_sel_hi:[1,0]\n"
    "v_pk_mul_f32 v[126:127], v[126:127], v[86:87] op_sel_hi:[1,0]\n"
    "v_cvt_pk_bf16_f32 v92, v92, v124\n"
    "v_cvt_pk_bf16_f32 v93, v93, v125\n"
    "v_cvt_pk_bf16_f32 v94, v94, v126\n"
    "v_cvt_pk_bf16_f32 v95, v95, v127\n"
    "v_pk_mul_f32 v[112:113], v[112:113], v[86:87] op_sel_hi:[1,0]\n"
    "v_pk_mul_f32 v[114:115], v[114:115], v[86:87] op_sel_hi:[1,0]\n"
    "v_pk_mul_f32 v[128:129], v[128:129], v[86:87] op_sel_hi:[1,0]\n"
    "v_pk_mul_f32 v[130:131], v[130:131], v[86:87] op_sel_hi:[1,0]\n"
    "v_cvt_pk_bf16_f32 v112, v112, v128\n"
    "v_cvt_pk_bf16_f32 v113, v113, v129\n"
    "v_cvt_pk_bf16_f32 v114, v114, v130\n"
    "v_cvt_pk_bf16_f32 v115, v115, v131\n"
    "v_pk_mul_f32 v[116:117], v[116:117], v[86:87] op_sel_hi:[1,0]\n"
    "v_pk_mul_f32 v[118:119], v[118:119], v[86:87] op_sel_hi:[1,0]\n"
    "v_pk_mul_f32 v[132:133], v[132:133], v[86:87] op_sel_hi:[1,0]\n"
    "v_pk_mul_f32 v[134:135], v[134:135], v[86:87] op_sel_hi:[1,0]\n"
    "v_cvt_pk_bf16_f32 v116, v116, v132\n"
    "v_cvt_pk_bf16_f32 v117, v117, v133\n"
    "v_cvt_pk_bf16_f32 v118, v118, v134\n"
    "v_cvt_pk_bf16_f32 v119, v119, v135\n"
    "s_lshr_b32 s51, s48, 2\n"
    "s_xor_b32 s51, s51, s69\n"
    "s_and_b32 s51, s51, 1\n"
    "s_lshl_b32 s51, s51, 13\n"
    "s_lshl_b32 s73, s48, 19\n"
    "s_add_u32 s73, s73, s75\n"
    "s_add_u32 s73, s73, s51\n"
    "buffer_store_dwordx4 v[88:91], v0, s[40:43], s73 offen sc1\n"
    "buffer_store_dwordx4 v[92:95], v0, s[40:43], s73 offen offset:1024 sc1\n"
    "buffer_store_dwordx4 v[112:115], v0, s[40:43], s73 offen offset:2048 sc1\n"
    "buffer_store_dwordx4 v[116:119], v0, s[40:43], s73 offen offset:3072 sc1\n"
    "s_waitcnt vmcnt(0)\n"
    "s_add_u32 s73, s48, 0\n"
    "s_and_b32 s51, s73, 32\n"
    "s_and_b32 s73, s73, 31\n"
    "s_lshl3_add_u32 s73, s51, s73\n"
    "s_lshl2_add_u32 s73, s73, s93\n"
    "v_mov_b32 v134, s73\n"
    "v_mov_b32 v135, 1\n"
    "s_mov_b64 exec, 1\n"
    "global_store_dword v134, v135, %1 sc1\n"
    "s_mov_b64 exec, -1\n"
    "s_cmp_eq_u32 %3, 128\n"
    "s_cbranch_scc0 .Lout_%=\n"
    "v_mov_b32 v130, 0x20400\n"
    "global_load_dwordx2 v[132:133], v130, %1 sc1\n"
    "v_mbcnt_lo_u32_b32 v131, -1, 0\n"
    "v_min_u32 v131, 7, v131\n"
    "v_lshlrev_b32 v131, 7, v131\n"
    "v_add_u32 v131, 0x20000, v131\n"
    "global_load_dwordx2 v[134:135], v131, %1 sc1\n"
    "s_waitcnt vmcnt(0)\n"
    "v_add_u32 v132, 1, v132\n"
    "v_add_u32 v133, 1, v133\n"
    "global_store_dwordx2 v130, v[132:133], %1 sc1\n"
    ".Lout_%=:\n"
    "s_waitcnt vmcnt(0)\n"
    "s_endpgm\n"
    ".p2align 6\n"
    ".Lrole1_%=:\n"
    "s_mov_b64 s[40:41], %0\n"
    "s_and_b32 s41, s41, 0xffff\n"
    "s_mov_b32 s42, 0xc000000\n"
    "s_mov_b32 s43, 0x20000\n"
    "s_mov_b64 s[92:93], %0\n"
    "s_and_b32 s93, s93, 0xffff\n"
    "s_mov_b32 s94, 0xc000000\n"
    "s_mov_b32 s95, 0x20000\n"
    "s_mov_b32 s53, 131072\n"
    "s_mov_b64 s[56:57], 0\n"
    "v_mbcnt_lo_u32_b32 v7, -1, 0\n"
    "v_mbcnt_hi_u32_b32 v7, -1, v7\n"
    "s_lshl_b32 s50, %2, 8\n"
    "s_add_u32 s50, s50, 147456\n"
    "v_lshl_add_u32 v7, v7, 2, s50\n"
    "ds_read_b32 v1, v7\n"
    "ds_read_b32 v2, v7 offset:1024\n"
    "ds_read_b32 v3, v7 offset:2048\n"
    "ds_read_b32 v5, v7 offset:3072\n"
    "ds_read_b32 v6, v7 offset:4096\n"
    "ds_read_b32 v48, v7 offset:5120\n"
    "ds_read_b32 v49, v7 offset:6144\n"
    "ds_read_b32 v50, v7 offset:7168\n"
    "s_waitcnt lgkmcnt(0)\n"
    "s_barrier\n"
    "s_lshl_b32 s54, %2, 10\n"
    "s_add_u32 s88, s40, 32768\n"
    "s_mov_b32 s89, s41\n"
    "s_cmp_lt_u32 %2, 2\n"
    "s_cselect_b32 s96, s42, 0\n"
    "s_mov_b32 s91, s43\n"
    "s_mov_b32 s86, 0x3f803f80\n"
    "s_mov_b32 s87, 0x3f803f80\n"
    "v_mov_b64 v[76:77], s[86:87]\n"
    "v_mov_b64 v[78:79], s[86:87]\n"
    "s_mov_b64 s[44:45], %8\n"
    "s_and_b32 s45, s45, 0xffff\n"
    "s_mov_b32 s46, 0x480000\n"
    "s_mov_b32 s47, 0x20000\n"
    "s_add_u32 m0, s54, 0\n"
    "s_add_u32 s55, %6, 0\n"
    "buffer_load_dwordx4 v48, s[44:47], s55 offen lds\n"
    "s_add_u32 m0, s54, 4096\n"
    "s_add_u32 s55, %6, 1048576\n"
    "buffer_load_dwordx4 v48, s[44:47], s55 offen lds\n"
    "s_add_u32 m0, s54, 8192\n"
    "s_add_u32 s55, %6, 2097152\n"
    "buffer_load_dwordx4 v48, s[44:47], s55 offen lds\n"
    "s_add_u32 m0, s54, 12288\n"
    "s_add_u32 s55, %6, 3145728\n"
    "buffer_load_dwordx4 v48, s[44:47], s55 offen lds\n"
    "s_add_u32 m0, s54, 16384\n"
    "s_add_u32 s55, %6, 32768\n"
    "buffer_load_dwordx4 v48, s[44:47], s55 offen lds\n"
    "s_add_u32 m0, s54, 20480\n"
    "s_add_u32 s55, %6, 1081344\n"
    "buffer_load_dwordx4 v48, s[44:47], s55 offen lds\n"
    "s_add_u32 m0, s54, 24576\n"
    "s_add_u32 s55, %6, 2129920\n"
    "buffer_load_dwordx4 v48, s[44:47], s55 offen lds\n"
    "s_add_u32 m0, s54, 28672\n"
    "s_add_u32 s55, %6, 3178496\n"
    "buffer_load_dwordx4 v48, s[44:47], s55 offen lds\n"
    "s_add_u32 m0, s54, 32768\n"
    "s_add_u32 s55, %6, 262144\n"
    "buffer_load_dwordx4 v48, s[44:47], s55 offen lds\n"
    "s_add_u32 m0, s54, 36864\n"
    "s_add_u32 s55, %6, 1310720\n"
    "buffer_load_dwordx4 v48, s[44:47], s55 offen lds\n"
    "s_add_u32 m0, s54, 40960\n"
    "s_add_u32 s55, %6, 2359296\n"
    "buffer_load_dwordx4 v48, s[44:47], s55 offen lds\n"
    "s_add_u32 m0, s54, 45056\n"
    "s_add_u32 s55, %6, 3407872\n"
    "buffer_load_dwordx4 v48, s[44:47], s55 offen lds\n"
    "s_add_u32 m0, s54, 49152\n"
    "s_add_u32 s55, %6, 294912\n"
    "buffer_load_dwordx4 v48, s[44:47], s55 offen lds\n"
    "s_add_u32 m0, s54, 53248\n"
    "s_add_u32 s55, %6, 1343488\n"
    "buffer_load_dwordx4 v48, s[44:47], s55 offen lds\n"
    "s_add_u32 m0, s54, 57344\n"
    "s_add_u32 s55, %6, 2392064\n"
    "buffer_load_dwordx4 v48, s[44:47], s55 offen lds\n"
    "s_add_u32 m0, s54, 61440\n"
    "s_add_u32 s55, %6, 3440640\n"
    "buffer_load_dwordx4 v48, s[44:47], s55 offen lds\n"
    "s_add_u32 m0, s54, 65536\n"
    "s_add_u32 s55, %6, 524288\n"
    "buffer_load_dwordx4 v48, s[44:47], s55 offen lds\n"
    "s_add_u32 m0, s54, 69632\n"
    "s_add_u32 s55, %6, 1572864\n"
    "buffer_load_dwordx4 v48, s[44:47], s55 offen lds\n"
    "s_add_u32 m0, s54, 73728\n"
    "s_add_u32 s55, %6, 2621440\n"
    "buffer_load_dwordx4 v48, s[44:47], s55 offen lds\n"
    "s_add_u32 m0, s54, 77824\n"
    "s_add_u32 s55, %6, 3670016\n"
    "buffer_load_dwordx4 v48, s[44:47], s55 offen lds\n"
    "s_add_u32 m0, s54, 81920\n"
    "s_add_u32 s55, %6, 557056\n"
    "buffer_load_dwordx4 v48, s[44:47], s55 offen lds\n"
    "s_add_u32 m0, s54, 86016\n"
    "s_add_u32 s55, %6, 1605632\n"
    "buffer_load_dwordx4 v48, s[44:47], s55 offen lds\n"
    "s_add_u32 m0, s54, 90112\n"
    "s_add_u32 s55, %6, 2654208\n"
    "buffer_load_dwordx4 v48, s[44:47], s55 offen lds\n"
    "s_add_u32 m0, s54, 94208\n"
    "s_add_u32 s55, %6, 3702784\n"
    "buffer_load_dwordx4 v48, s[44:47], s55 offen lds\n"
    "s_add_u32 m0, s54, 98304\n"
    "s_add_u32 s55, %6, 786432\n"
    "buffer_load_dwordx4 v48, s[44:47], s55 offen lds\n"
    "s_add_u32 m0, s54, 102400\n"
    "s_add_u32 s55, %6, 1835008\n"
    "buffer_load_dwordx4 v48, s[44:47], s55 offen lds\n"
    "s_add_u32 m0, s54, 106496\n"
    "s_add_u32 s55, %6, 2883584\n"
    "buffer_load_dwordx4 v48, s[44:47], s55 offen lds\n"
    "s_add_u32 m0, s54, 110592\n"
    "s_add_u32 s55, %6, 3932160\n"
    "buffer_load_dwordx4 v48, s[44:47], s55 offen lds\n"
    "s_add_u32 m0, s54, 114688\n"
    "s_add_u32 s55, %6, 819200\n"
    "buffer_load_dwordx4 v48, s[44:47], s55 offen lds\n"
    "s_add_u32 m0, s54, 118784\n"
    "s_add_u32 s55, %6, 1867776\n"
    "buffer_load_dwordx4 v48, s[44:47], s55 offen lds\n"
    "s_add_u32 m0, s54, 122880\n"
    "s_add_u32 s55, %6, 2916352\n"
    "buffer_load_dwordx4 v48, s[44:47], s55 offen lds\n"
    "s_add_u32 m0, s54, 126976\n"
    "s_add_u32 s55, %6, 3964928\n"
    "buffer_load_dwordx4 v48, s[44:47], s55 offen lds\n"
    "s_add_u32 m0, s54, 131072\n"
    "s_add_u32 s55, %6, 4194304\n"
    "buffer_load_dwordx4 v48, s[44:47], s55 offen lds\n"
    "s_add_u32 m0, s54, 135168\n"
    "s_add_u32 s55, %6, 4227072\n"
    "buffer_load_dwordx4 v48, s[44:47], s55 offen lds\n"
    "s_add_u32 m0, s54, 139264\n"
    "s_add_u32 s55, %6, 4456448\n"
    "buffer_load_dwordx4 v48, s[44:47], s55 offen lds\n"
    "s_add_u32 m0, s54, 143360\n"
    "s_add_u32 s55, %6, 4489216\n"
    "buffer_load_dwordx4 v48, s[44:47], s55 offen lds\n"
    "s_xor_b32 s50, %2, 0\n"
    "s_lshl_b32 s50, s50, 12\n"
    "v_add_u32 v32, s50, v49\n"
    "v_add_u32 v33, s50, v50\n"
    "s_xor_b32 s50, %2, 1\n"
    "s_lshl_b32 s50, s50, 12\n"
    "v_add_u32 v34, s50, v49\n"
    "v_add_u32 v35, s50, v50\n"
    "s_xor_b32 s50, %2, 2\n"
    "s_lshl_b32 s50, s50, 12\n"
    "v_add_u32 v36, s50, v49\n"
    "v_add_u32 v37, s50, v50\n"
    "s_xor_b32 s50, %2, 3\n"
    "s_lshl_b32 s50, s50, 12\n"
    "v_add_u32 v38, s50, v49\n"
    "v_add_u32 v39, s50, v50\n"
    "s_waitcnt vmcnt(32) lgkmcnt(0)\n"
    "s_barrier\n"
    "ds_read_b64_tr_b16 a[0:1], v32\n"
    "ds_read_b64_tr_b16 a[4:5], v33\n"
    "ds_read_b64_tr_b16 a[32:33], v34\n"
    "ds_read_b64_tr_b16 a[36:37], v35\n"
    "ds_read_b64_tr_b16 a[64:65], v36\n"
    "ds_read_b64_tr_b16 a[68:69], v37\n"
    "ds_read_b64_tr_b16 a[96:97], v38\n"
    "ds_read_b64_tr_b16 a[100:101], v39\n"
    "s_waitcnt vmcnt(28) lgkmcnt(0)\n"
    "s_barrier\n"
    "s_mov_b64 s[44:45], %9\n"
    "s_and_b32 s45, s45, 0xffff\n"
    "s_mov_b32 s46, 0x480000\n"
    "s_mov_b32 s47, 0x20000\n"
    "s_add_u32 m0, s54, 0\n"
    "s_add_u32 s55, %6, 0\n"
    "buffer_load_dwordx4 v48, s[44:47], s55 offen lds\n"
    "s_add_u32 m0, s54, 4096\n"
    "s_add_u32 s55, %6, 1048576\n"
    "buffer_load_dwordx4 v48, s[44:47], s55 offen lds\n"
    "s_add_u32 m0, s54, 8192\n"
    "s_add_u32 s55, %6, 2097152\n"
    "buffer_load_dwordx4 v48, s[44:47], s55 offen lds\n"
    "s_add_u32 m0, s54, 12288\n"
    "s_add_u32 s55, %6, 3145728\n"
    "buffer_load_dwordx4 v48, s[44:47], s55 offen lds\n"
    "ds_read_b64_tr_b16 a[2:3], v32 offset:16384\n"
    "ds_read_b64_tr_b16 a[6:7], v33 offset:16384\n"
    "ds_read_b64_tr_b16 a[34:35], v34 offset:16384\n"
    "ds_read_b64_tr_b16 a[38:39], v35 offset:16384\n"
    "ds_read_b64_tr_b16 a[66:67], v36 offset:16384\n"
    "ds_read_b64_tr_b16 a[70:71], v37 offset:16384\n"
    "ds_read_b64_tr_b16 a[98:99], v38 offset:16384\n"
    "ds_read_b64_tr_b16 a[102:103], v39 offset:16384\n"
    "s_waitcnt vmcnt(28) lgkmcnt(0)\n"
    "s_barrier\n"
    "s_add_u32 m0, s54, 16384\n"
    "s_add_u32 s55, %6, 32768\n"
    "buffer_load_dwordx4 v48, s[44:47], s55 offen lds\n"
    "s_add_u32 m0, s54, 20480\n"
    "s_add_u32 s55, %6, 1081344\n"
    "buffer_load_dwordx4 v48, s[44:47], s55 offen lds\n"
    "s_add_u32 m0, s54, 24576\n"
    "s_add_u32 s55, %6, 2129920\n"
    "buffer_load_dwordx4 v48, s[44:47], s55 offen lds\n"
    "s_add_u32 m0, s54, 28672\n"
    "s_add_u32 s55, %6, 3178496\n"
    "buffer_load_dwordx4 v48, s[44:47], s55 offen lds\n"
    "v_mfma_f32_16x16x32_bf16 v[8:11], v[76:79], a[0:3], 0\n"
    "v_mfma_f32_16x16x32_bf16 v[12:15], v[76:79], a[4:7], 0\n"
    "v_mfma_f32_16x16x32_bf16 v[8:11], v[76:79], a[32:35], v[8:11]\n"
    "v_mfma_f32_16x16x32_bf16 v[12:15], v[76:79], a[36:39], v[12:15]\n"
    "v_mfma_f32_16x16x32_bf16 v[8:11], v[76:79], a[64:67], v[8:11]\n"
    "v_mfma_f32_16x16x32_bf16 v[12:15], v[76:79], a[68:71], v[12:15]\n"
    "v_mfma_f32_16x16x32_bf16 v[8:11], v[76:79], a[96:99], v[8:11]\n"
    "v_mfma_f32_16x16x32_bf16 v[12:15], v[76:79], a[100:103], v[12:15]\n"
    "ds_read_b64_tr_b16 a[8:9], v32 offset:32768\n"
    "ds_read_b64_tr_b16 a[12:13], v33 offset:32768\n"
    "ds_read_b64_tr_b16 a[40:41], v34 offset:32768\n"
    "ds_read_b64_tr_b16 a[44:45], v35 offset:32768\n"
    "ds_read_b64_tr_b16 a[72:73], v36 offset:32768\n"
    "ds_read_b64_tr_b16 a[76:77], v37 offset:32768\n"
    "ds_read_b64_tr_b16 a[104:105], v38 offset:32768\n"
    "ds_read_b64_tr_b16 a[108:109], v39 offset:32768\n"
    "s_waitcnt vmcnt(28) lgkmcnt(0)\n"
    "s_barrier\n"
    "s_add_u32 m0, s54, 32768\n"
    "s_add_u32 s55, %6, 262144\n"
    "buffer_load_dwordx4 v48, s[44:47], s55 offen lds\n"
    "s_add_u32 m0, s54, 36864\n"
    "s_add_u32 s55, %6, 1310720\n"
    "buffer_load_dwordx4 v48, s[44:47], s55 offen lds\n"
    "s_add_u32 m0, s54, 40960\n"
    "s_add_u32 s55, %6, 2359296\n"
    "buffer_load_dwordx4 v48, s[44:47], s55 offen lds\n"
    "s_add_u32 m0, s54, 45056\n"
    "s_add_u32 s55, %6, 3407872\n"
    "buffer_load_dwordx4 v48, s[44:47], s55 offen lds\n"
    "ds_read_b64_tr_b16 a[10:11], v32 offset:49152\n"
    "ds_read_b64_tr_b16 a[14:15], v33 offset:49152\n"
    "ds_read_b64_tr_b16 a[42:43], v34 offset:49152\n"
    "ds_read_b64_tr_b16 a[46:47], v35 offset:49152\n"
    "ds_read_b64_tr_b16 a[74:75], v36 offset:49152\n"
    "ds_read_b64_tr_b16 a[78:79], v37 offset:49152\n"
    "ds_read_b64_tr_b16 a[106:107], v38 offset:49152\n"
    "ds_read_b64_tr_b16 a[110:111], v39 offset:49152\n"
    "s_waitcnt vmcnt(28) lgkmcnt(0)\n"
    "s_barrier\n"
    "s_add_u32 m0, s54, 49152\n"
    "s_add_u32 s55, %6, 294912\n"
    "buffer_load_dwordx4 v48, s[44:47], s55 offen lds\n"
    "s_add_u32 m0, s54, 53248\n"
    "s_add_u32 s55, %6, 1343488\n"
    "buffer_load_dwordx4 v48, s[44:47], s55 offen lds\n"
    "s_add_u32 m0, s54, 57344\n"
    "s_add_u32 s55, %6, 2392064\n"
    "buffer_load_dwordx4 v48, s[44:47], s55 offen lds\n"
    "s_add_u32 m0, s54, 61440\n"
    "s_add_u32 s55, %6, 3440640\n"
    "buffer_load_dwordx4 v48, s[44:47], s55 offen lds\n"
    "v_mfma_f32_16x16x32_bf16 v[8:11], v[76:79], a[8:11], v[8:11]\n"
    "v_mfma_f32_16x16x32_bf16 v[12:15], v[76:79], a[12:15], v[12:15]\n"
    "v_mfma_f32_16x16x32_bf16 v[8:11], v[76:79], a[40:43], v[8:11]\n"
    "v_mfma_f32_16x16x32_bf16 v[12:15], v[76:79], a[44:47], v[12:15]\n"
    "v_mfma_f32_16x16x32_bf16 v[8:11], v[76:79], a[72:75], v[8:11]\n"
    "v_mfma_f32_16x16x32_bf16 v[12:15], v[76:79], a[76:79], v[12:15]\n"
    "v_mfma_f32_16x16x32_bf16 v[8:11], v[76:79], a[104:107], v[8:11]\n"
    "v_mfma_f32_16x16x32_bf16 v[12:15], v[76:79], a[108:111], v[12:15]\n"
    "v_add_u32 v71, 0x10000, v32\n"
    "ds_read_b64_tr_b16 a[16:17], v71\n"
    "v_add_u32 v70, 0x10000, v33\n"
    "ds_read_b64_tr_b16 a[20:21], v70\n"
    "v_add_u32 v69, 0x10000, v34\n"
    "ds_read_b64_tr_b16 a[48:49], v69\n"
    "v_add_u32 v68, 0x10000, v35\n"
    "ds_read_b64_tr_b16 a[52:53], v68\n"
    "v_add_u32 v67, 0x10000, v36\n"
    "ds_read_b64_tr_b16 a[80:81], v67\n"
    "v_add_u32 v66, 0x10000, v37\n"
    "ds_read_b64_tr_b16 a[84:85], v66\n"
    "v_add_u32 v65, 0x10000, v38\n"
    "ds_read_b64_tr_b16 a[112:113], v65\n"
    "v_add_u32 v64, 0x10000, v39\n"
    "ds_read_b64_tr_b16 a[116:117], v64\n"
    "s_waitcnt vmcnt(28) lgkmcnt(0)\n"
    "s_barrier\n"
    "s_add_u32 m0, s54, 65536\n"
    "s_add_u32 s55, %6, 524288\n"
    "buffer_load_dwordx4 v48, s[44:47], s55 offen lds\n"
    "s_add_u32 m0, s54, 69632\n"
    "s_add_u32 s55, %6, 1572864\n"
    "buffer_load_dwordx4 v48, s[44:47], s55 offen lds\n"
    "s_add_u32 m0, s54, 73728\n"
    "s_add_u32 s55, %6, 2621440\n"
    "buffer_load_dwordx4 v48, s[44:47], s55 offen lds\n"
    "s_add_u32 m0, s54, 77824\n"
    "s_add_u32 s55, %6, 3670016\n"
    "buffer_load_dwordx4 v48, s[44:47], s55 offen lds\n"
    "ds_read_b64_tr_b16 a[18:19], v71 offset:16384\n"
    "ds_read_b64_tr_b16 a[22:23], v70 offset:16384\n"
    "ds_read_b64_tr_b16 a[50:51], v69 offset:16384\n"
    "ds_read_b64_tr_b16 a[54:55], v68 offset:16384\n"
    "ds_read_b64_tr_b16 a[82:83], v67 offset:16384\n"
    "ds_read_b64_tr_b16 a[86:87], v66 offset:16384\n"
    "ds_read_b64_tr_b16 a[114:115], v65 offset:16384\n"
    "ds_read_b64_tr_b16 a[118:119], v64 offset:16384\n"
    "s_waitcnt vmcnt(28) lgkmcnt(0)\n"
    "s_barrier\n"
    "s_add_u32 m0, s54, 81920\n"
    "s_add_u32 s55, %6, 557056\n"
    "buffer_load_dwordx4 v48, s[44:47], s55 offen lds\n"
    "s_add_u32 m0, s54, 86016\n"
    "s_add_u32 s55, %6, 1605632\n"
    "buffer_load_dwordx4 v48, s[44:47], s55 offen lds\n"
    "s_add_u32 m0, s54, 90112\n"
    "s_add_u32 s55, %6, 2654208\n"
    "buffer_load_dwordx4 v48, s[44:47], s55 offen lds\n"
    "s_add_u32 m0, s54, 94208\n"
    "s_add_u32 s55, %6, 3702784\n"
    "buffer_load_dwordx4 v48, s[44:47], s55 offen lds\n"
    "v_mfma_f32_16x16x32_bf16 v[8:11], v[76:79], a[16:19], v[8:11]\n"
    "v_mfma_f32_16x16x32_bf16 v[12:15], v[76:79], a[20:23], v[12:15]\n"
    "v_mfma_f32_16x16x32_bf16 v[8:11], v[76:79], a[48:51], v[8:11]\n"
    "v_mfma_f32_16x16x32_bf16 v[12:15], v[76:79], a[52:55], v[12:15]\n"
    "v_mfma_f32_16x16x32_bf16 v[8:11], v[76:79], a[80:83], v[8:11]\n"
    "v_mfma_f32_16x16x32_bf16 v[12:15], v[76:79], a[84:87], v[12:15]\n"
    "v_mfma_f32_16x16x32_bf16 v[8:11], v[76:79], a[112:115], v[8:11]\n"
    "v_mfma_f32_16x16x32_bf16 v[12:15], v[76:79], a[116:119], v[12:15]\n"
    "ds_read_b64_tr_b16 a[24:25], v71 offset:32768\n"
    "ds_read_b64_tr_b16 a[28:29], v70 offset:32768\n"
    "ds_read_b64_tr_b16 a[56:57], v69 offset:32768\n"
    "ds_read_b64_tr_b16 a[60:61], v68 offset:32768\n"
    "ds_read_b64_tr_b16 a[88:89], v67 offset:32768\n"
    "ds_read_b64_tr_b16 a[92:93], v66 offset:32768\n"
    "ds_read_b64_tr_b16 a[120:121], v65 offset:32768\n"
    "ds_read_b64_tr_b16 a[124:125], v64 offset:32768\n"
    "s_waitcnt vmcnt(28) lgkmcnt(0)\n"
    "s_barrier\n"
    "s_add_u32 m0, s54, 98304\n"
    "s_add_u32 s55, %6, 786432\n"
    "buffer_load_dwordx4 v48, s[44:47], s55 offen lds\n"
    "s_add_u32 m0, s54, 102400\n"
    "s_add_u32 s55, %6, 1835008\n"
    "buffer_load_dwordx4 v48, s[44:47], s55 offen lds\n"
    "s_add_u32 m0, s54, 106496\n"
    "s_add_u32 s55, %6, 2883584\n"
    "buffer_load_dwordx4 v48, s[44:47], s55 offen lds\n"
    "s_add_u32 m0, s54, 110592\n"
    "s_add_u32 s55, %6, 3932160\n"
    "buffer_load_dwordx4 v48, s[44:47], s55 offen lds\n"
    "ds_read_b64_tr_b16 a[26:27], v71 offset:49152\n"
    "ds_read_b64_tr_b16 a[30:31], v70 offset:49152\n"
    "ds_read_b64_tr_b16 a[58:59], v69 offset:49152\n"
    "ds_read_b64_tr_b16 a[62:63], v68 offset:49152\n"
    "ds_read_b64_tr_b16 a[90:91], v67 offset:49152\n"
    "ds_read_b64_tr_b16 a[94:95], v66 offset:49152\n"
    "ds_read_b64_tr_b16 a[122:123], v65 offset:49152\n"
    "ds_read_b64_tr_b16 a[126:127], v64 offset:49152\n"
    "s_waitcnt vmcnt(28) lgkmcnt(0)\n"
    "s_barrier\n"
    "s_add_u32 m0, s54, 114688\n"
    "s_add_u32 s55, %6, 819200\n"
    "buffer_load_dwordx4 v48, s[44:47], s55 offen lds\n"
    "s_add_u32 m0, s54, 118784\n"
    "s_add_u32 s55, %6, 1867776\n"
    "buffer_load_dwordx4 v48, s[44:47], s55 offen lds\n"
    "s_add_u32 m0, s54, 122880\n"
    "s_add_u32 s55, %6, 2916352\n"
    "buffer_load_dwordx4 v48, s[44:47], s55 offen lds\n"
    "s_add_u32 m0, s54, 126976\n"
    "s_add_u32 s55, %6, 3964928\n"
    "buffer_load_dwordx4 v48, s[44:47], s55 offen lds\n"
    "v_mfma_f32_16x16x32_bf16 v[8:11], v[76:79], a[24:27], v[8:11]\n"
    "v_mfma_f32_16x16x32_bf16 v[12:15], v[76:79], a[28:31], v[12:15]\n"
    "v_mfma_f32_16x16x32_bf16 v[8:11], v[76:79], a[56:59], v[8:11]\n"
    "v_mfma_f32_16x16x32_bf16 v[12:15], v[76:79], a[60:63], v[12:15]\n"
    "v_mfma_f32_16x16x32_bf16 v[8:11], v[76:79], a[88:91], v[8:11]\n"
    "v_mfma_f32_16x16x32_bf16 v[12:15], v[76:79], a[92:95], v[12:15]\n"
    "v_mfma_f32_16x16x32_bf16 v[8:11], v[76:79], a[120:123], v[8:11]\n"
    "v_mfma_f32_16x16x32_bf16 v[12:15], v[76:79], a[124:127], v[12:15]\n"
    "v_add_u32 v63, 0x20000, v49\n"
    "ds_read_b64_tr_b16 a[128:129], v63\n"
    "v_add_u32 v62, 0x20000, v50\n"
    "ds_read_b64_tr_b16 a[132:133], v62\n"
    "ds_read_b64_tr_b16 a[130:131], v63 offset:4096\n"
    "ds_read_b64_tr_b16 a[134:135], v62 offset:4096\n"
    "ds_read_b64_tr_b16 a[136:137], v63 offset:8192\n"
    "ds_read_b64_tr_b16 a[140:141], v62 offset:8192\n"
    "ds_read_b64_tr_b16 a[138:139], v63 offset:12288\n"
    "ds_read_b64_tr_b16 a[142:143], v62 offset:12288\n"
    "s_waitcnt vmcnt(28) lgkmcnt(0)\n"
    "s_barrier\n"
    "s_add_u32 m0, s54, 131072\n"
    "s_add_u32 s55, %6, 4194304\n"
    "buffer_load_dwordx4 v48, s[44:47], s55 offen lds\n"
    "s_add_u32 m0, s54, 135168\n"
    "s_add_u32 s55, %6, 4227072\n"
    "buffer_load_dwordx4 v48, s[44:47], s55 offen lds\n"
    "s_add_u32 m0, s54, 139264\n"
    "s_add_u32 s55, %6, 4456448\n"
    "buffer_load_dwordx4 v48, s[44:47], s55 offen lds\n"
    "s_add_u32 m0, s54, 143360\n"
    "s_add_u32 s55, %6, 4489216\n"
    "buffer_load_dwordx4 v48, s[44:47], s55 offen lds\n"
    "v_mfma_f32_16x16x32_bf16 v[8:11], v[76:79], a[128:131], v[8:11]\n"
    "v_mfma_f32_16x16x32_bf16 v[12:15], v[76:79], a[132:135], v[12:15]\n"
    "v_mfma_f32_16x16x32_bf16 v[8:11], v[76:79], a[136:139], v[8:11]\n"
    "v_mfma_f32_16x16x32_bf16 v[12:15], v[76:79], a[140:143], v[12:15]\n"
    "ds_read_b64_tr_b16 a[144:145], v32\n"
    "ds_read_b64_tr_b16 a[148:149], v33\n"
    "ds_read_b64_tr_b16 a[176:177], v34\n"
    "ds_read_b64_tr_b16 a[180:181], v35\n"
    "ds_read_b64_tr_b16 a[208:209], v36\n"
    "ds_read_b64_tr_b16 a[212:213], v37\n"
    "ds_read_b64_tr_b16 a[240:241], v38\n"
    "ds_read_b64_tr_b16 a[244:245], v39\n"
    "s_waitcnt vmcnt(28) lgkmcnt(0)\n"
    "s_barrier\n"
    "s_mov_b64 s[44:45], %10\n"
    "s_and_b32 s45, s45, 0xffff\n"
    "s_mov_b32 s46, 0x480000\n"
    "s_mov_b32 s47, 0x20000\n"
    "s_add_u32 m0, s54, 0\n"
    "s_add_u32 s55, %6, 0\n"
    "buffer_load_dwordx4 v48, s[44:47], s55 offen lds\n"
    "s_add_u32 m0, s54, 4096\n"
    "s_add_u32 s55, %6, 1048576\n"
    "buffer_load_dwordx4 v48, s[44:47], s55 offen lds\n"
    "s_add_u32 m0, s54, 8192\n"
    "s_add_u32 s55, %6, 2097152\n"
    "buffer_load_dwordx4 v48, s[44:47], s55 offen lds\n"
    "s_add_u32 m0, s54, 12288\n"
    "s_add_u32 s55, %6, 3145728\n"
    "buffer_load_dwordx4 v48, s[44:47], s55 offen lds\n"
    "ds_read_b64_tr_b16 a[146:147], v32 offset:16384\n"
    "ds_read_b64_tr_b16 a[150:151], v33 offset:16384\n"
    "ds_read_b64_tr_b16 a[178:179], v34 offset:16384\n"
    "ds_read_b64_tr_b16 a[182:183], v35 offset:16384\n"
    "ds_read_b64_tr_b16 a[210:211], v36 offset:16384\n"
    "ds_read_b64_tr_b16 a[214:215], v37 offset:16384\n"
    "ds_read_b64_tr_b16 a[242:243], v38 offset:16384\n"
    "ds_read_b64_tr_b16 a[246:247], v39 offset:16384\n"
    "s_waitcnt vmcnt(28) lgkmcnt(0)\n"
    "s_barrier\n"
    "s_add_u32 m0, s54, 16384\n"
    "s_add_u32 s55, %6, 32768\n"
    "buffer_load_dwordx4 v48, s[44:47], s55 offen lds\n"
    "s_add_u32 m0, s54, 20480\n"
    "s_add_u32 s55, %6, 1081344\n"
    "buffer_load_dwordx4 v48, s[44:47], s55 offen lds\n"
    "s_add_u32 m0, s54, 24576\n"
    "s_add_u32 s55, %6, 2129920\n"
    "buffer_load_dwordx4 v48, s[44:47], s55 offen lds\n"
    "s_add_u32 m0, s54, 28672\n"
    "s_add_u32 s55, %6, 3178496\n"
    "buffer_load_dwordx4 v48, s[44:47], s55 offen lds\n"
    "v_mfma_f32_16x16x32_bf16 v[16:19], v[76:79], a[144:147], 0\n"
    "v_mfma_f32_16x16x32_bf16 v[20:23], v[76:79], a[148:151], 0\n"
    "v_mfma_f32_16x16x32_bf16 v[16:19], v[76:79], a[176:179], v[16:19]\n"
    "v_mfma_f32_16x16x32_bf16 v[20:23], v[76:79], a[180:183], v[20:23]\n"
    "v_mfma_f32_16x16x32_bf16 v[16:19], v[76:79], a[208:211], v[16:19]\n"
    "v_mfma_f32_16x16x32_bf16 v[20:23], v[76:79], a[212:215], v[20:23]\n"
    "v_mfma_f32_16x16x32_bf16 v[16:19], v[76:79], a[240:243], v[16:19]\n"
    "v_mfma_f32_16x16x32_bf16 v[20:23], v[76:79], a[244:247], v[20:23]\n"
    "ds_read_b64_tr_b16 a[152:153], v32 offset:32768\n"
    "ds_read_b64_tr_b16 a[156:157], v33 offset:32768\n"
    "ds_read_b64_tr_b16 a[184:185], v34 offset:32768\n"
    "ds_read_b64_tr_b16 a[188:189], v35 offset:32768\n"
    "ds_read_b64_tr_b16 a[216:217], v36 offset:32768\n"
    "ds_read_b64_tr_b16 a[220:221], v37 offset:32768\n"
    "ds_read_b64_tr_b16 a[248:249], v38 offset:32768\n"
    "ds_read_b64_tr_b16 a[252:253], v39 offset:32768\n"
    "s_waitcnt vmcnt(28) lgkmcnt(0)\n"
    "s_barrier\n"
    "s_add_u32 m0, s54, 32768\n"
    "s_add_u32 s55, %6, 262144\n"
    "buffer_load_dwordx4 v48, s[44:47], s55 offen lds\n"
    "s_add_u32 m0, s54, 36864\n"
    "s_add_u32 s55, %6, 1310720\n"
    "buffer_load_dwordx4 v48, s[44:47], s55 offen lds\n"
    "s_add_u32 m0, s54, 40960\n"
    "s_add_u32 s55, %6, 2359296\n"
    "buffer_load_dwordx4 v48, s[44:47], s55 offen lds\n"
    "s_add_u32 m0, s54, 45056\n"
    "s_add_u32 s55, %6, 3407872\n"
    "buffer_load_dwordx4 v48, s[44:47], s55 offen lds\n"
    "ds_read_b64_tr_b16 a[154:155], v32 offset:49152\n"
    "ds_read_b64_tr_b16 a[158:159], v33 offset:49152\n"
    "ds_read_b64_tr_b16 a[186:187], v34 offset:49152\n"
    "ds_read_b64_tr_b16 a[190:191], v35 offset:49152\n"
    "ds_read_b64_tr_b16 a[218:219], v36 offset:49152\n"
    "ds_read_b64_tr_b16 a[222:223], v37 offset:49152\n"
    "ds_read_b64_tr_b16 a[250:251], v38 offset:49152\n"
    "ds_read_b64_tr_b16 a[254:255], v39 offset:49152\n"
    "s_waitcnt vmcnt(28) lgkmcnt(0)\n"
    "s_barrier\n"
    "s_add_u32 m0, s54, 49152\n"
    "s_add_u32 s55, %6, 294912\n"
    "buffer_load_dwordx4 v48, s[44:47], s55 offen lds\n"
    "s_add_u32 m0, s54, 53248\n"
    "s_add_u32 s55, %6, 1343488\n"
    "buffer_load_dwordx4 v48, s[44:47], s55 offen lds\n"
    "s_add_u32 m0, s54, 57344\n"
    "s_add_u32 s55, %6, 2392064\n"
    "buffer_load_dwordx4 v48, s[44:47], s55 offen lds\n"
    "s_add_u32 m0, s54, 61440\n"
    "s_add_u32 s55, %6, 3440640\n"
    "buffer_load_dwordx4 v48, s[44:47], s55 offen lds\n"
    "v_mfma_f32_16x16x32_bf16 v[16:19], v[76:79], a[152:155], v[16:19]\n"
    "v_mfma_f32_16x16x32_bf16 v[20:23], v[76:79], a[156:159], v[20:23]\n"
    "v_mfma_f32_16x16x32_bf16 v[16:19], v[76:79], a[184:187], v[16:19]\n"
    "v_mfma_f32_16x16x32_bf16 v[20:23], v[76:79], a[188:191], v[20:23]\n"
    "v_mfma_f32_16x16x32_bf16 v[16:19], v[76:79], a[216:219], v[16:19]\n"
    "v_mfma_f32_16x16x32_bf16 v[20:23], v[76:79], a[220:223], v[20:23]\n"
    "v_mfma_f32_16x16x32_bf16 v[16:19], v[76:79], a[248:251], v[16:19]\n"
    "v_mfma_f32_16x16x32_bf16 v[20:23], v[76:79], a[252:255], v[20:23]\n"
    "ds_read_b64_tr_b16 a[160:161], v71\n"
    "ds_read_b64_tr_b16 a[164:165], v70\n"
    "ds_read_b64_tr_b16 a[192:193], v69\n"
    "ds_read_b64_tr_b16 a[196:197], v68\n"
    "ds_read_b64_tr_b16 a[224:225], v67\n"
    "ds_read_b64_tr_b16 a[228:229], v66\n"
    "ds_read_b64_tr_b16 v[84:85], v65\n"
    "ds_read_b64_tr_b16 v[88:89], v64\n"
    "s_waitcnt vmcnt(28) lgkmcnt(0)\n"
    "s_barrier\n"
    "s_add_u32 m0, s54, 65536\n"
    "s_add_u32 s55, %6, 524288\n"
    "buffer_load_dwordx4 v48, s[44:47], s55 offen lds\n"
    "s_add_u32 m0, s54, 69632\n"
    "s_add_u32 s55, %6, 1572864\n"
    "buffer_load_dwordx4 v48, s[44:47], s55 offen lds\n"
    "s_add_u32 m0, s54, 73728\n"
    "s_add_u32 s55, %6, 2621440\n"
    "buffer_load_dwordx4 v48, s[44:47], s55 offen lds\n"
    "s_add_u32 m0, s54, 77824\n"
    "s_add_u32 s55, %6, 3670016\n"
    "buffer_load_dwordx4 v48, s[44:47], s55 offen lds\n"
    "ds_read_b64_tr_b16 a[162:163], v71 offset:16384\n"
    "ds_read_b64_tr_b16 a[166:167], v70 offset:16384\n"
    "ds_read_b64_tr_b16 a[194:195], v69 offset:16384\n"
    "ds_read_b64_tr_b16 a[198:199], v68 offset:16384\n"
    "ds_read_b64_tr_b16 a[226:227], v67 offset:16384\n"
    "ds_read_b64_tr_b16 a[230:231], v66 offset:16384\n"
    "ds_read_b64_tr_b16 v[86:87], v65 offset:16384\n"
    "ds_read_b64_tr_b16 v[90:91], v64 offset:16384\n"
    "s_waitcnt vmcnt(28) lgkmcnt(0)\n"
    "s_barrier\n"
    "s_add_u32 m0, s54, 81920\n"
    "s_add_u32 s55, %6, 557056\n"
    "buffer_load_dwordx4 v48, s[44:47], s55 offen lds\n"
    "s_add_u32 m0, s54, 86016\n"
    "s_add_u32 s55, %6, 1605632\n"
    "buffer_load_dwordx4 v48, s[44:47], s55 offen lds\n"
    "s_add_u32 m0, s54, 90112\n"
    "s_add_u32 s55, %6, 2654208\n"
    "buffer_load_dwordx4 v48, s[44:47], s55 offen lds\n"
    "s_add_u32 m0, s54, 94208\n"
    "s_add_u32 s55, %6, 3702784\n"
    "buffer_load_dwordx4 v48, s[44:47], s55 offen lds\n"
    "v_mfma_f32_16x16x32_bf16 v[16:19], v[76:79], a[160:163], v[16:19]\n"
    "v_mfma_f32_16x16x32_bf16 v[20:23], v[76:79], a[164:167], v[20:23]\n"
    "v_mfma_f32_16x16x32_bf16 v[16:19], v[76:79], a[192:195], v[16:19]\n"
    "v_mfma_f32_16x16x32_bf16 v[20:23], v[76:79], a[196:199], v[20:23]\n"
    "v_mfma_f32_16x16x32_bf16 v[16:19], v[76:79], a[224:227], v[16:19]\n"
    "v_mfma_f32_16x16x32_bf16 v[20:23], v[76:79], a[228:231], v[20:23]\n"
    "v_mfma_f32_16x16x32_bf16 v[16:19], v[76:79], v[84:87], v[16:19]\n"
    "v_mfma_f32_16x16x32_bf16 v[20:23], v[76:79], v[88:91], v[20:23]\n"
    "ds_read_b64_tr_b16 a[168:169], v71 offset:32768\n"
    "ds_read_b64_tr_b16 a[172:173], v70 offset:32768\n"
    "ds_read_b64_tr_b16 a[200:201], v69 offset:32768\n"
    "ds_read_b64_tr_b16 a[204:205], v68 offset:32768\n"
    "ds_read_b64_tr_b16 a[232:233], v67 offset:32768\n"
    "ds_read_b64_tr_b16 a[236:237], v66 offset:32768\n"
    "ds_read_b64_tr_b16 v[92:93], v65 offset:32768\n"
    "ds_read_b64_tr_b16 v[96:97], v64 offset:32768\n"
    "s_waitcnt vmcnt(28) lgkmcnt(0)\n"
    "s_barrier\n"
    "s_add_u32 m0, s54, 98304\n"
    "s_add_u32 s55, %6, 786432\n"
    "buffer_load_dwordx4 v48, s[44:47], s55 offen lds\n"
    "s_add_u32 m0, s54, 102400\n"
    "s_add_u32 s55, %6, 1835008\n"
    "buffer_load_dwordx4 v48, s[44:47], s55 offen lds\n"
    "s_add_u32 m0, s54, 106496\n"
    "s_add_u32 s55, %6, 2883584\n"
    "buffer_load_dwordx4 v48, s[44:47], s55 offen lds\n"
    "s_add_u32 m0, s54, 110592\n"
    "s_add_u32 s55, %6, 3932160\n"
    "buffer_load_dwordx4 v48, s[44:47], s55 offen lds\n"
    "ds_read_b64_tr_b16 a[170:171], v71 offset:49152\n"
    "ds_read_b64_tr_b16 a[174:175], v70 offset:49152\n"
    "ds_read_b64_tr_b16 a[202:203], v69 offset:49152\n"
    "ds_read_b64_tr_b16 a[206:207], v68 offset:49152\n"
    "ds_read_b64_tr_b16 a[234:235], v67 offset:49152\n"
    "ds_read_b64_tr_b16 a[238:239], v66 offset:49152\n"
    "ds_read_b64_tr_b16 v[94:95], v65 offset:49152\n"
    "ds_read_b64_tr_b16 v[98:99], v64 offset:49152\n"
    "s_waitcnt vmcnt(28) lgkmcnt(0)\n"
    "s_barrier\n"
    "s_add_u32 m0, s54, 114688\n"
    "s_add_u32 s55, %6, 819200\n"
    "buffer_load_dwordx4 v48, s[44:47], s55 offen lds\n"
    "s_add_u32 m0, s54, 118784\n"
    "s_add_u32 s55, %6, 1867776\n"
    "buffer_load_dwordx4 v48, s[44:47], s55 offen lds\n"
    "s_add_u32 m0, s54, 122880\n"
    "s_add_u32 s55, %6, 2916352\n"
    "buffer_load_dwordx4 v48, s[44:47], s55 offen lds\n"
    "s_add_u32 m0, s54, 126976\n"
    "s_add_u32 s55, %6, 3964928\n"
    "buffer_load_dwordx4 v48, s[44:47], s55 offen lds\n"
    "v_mfma_f32_16x16x32_bf16 v[16:19], v[76:79], a[168:171], v[16:19]\n"
    "v_mfma_f32_16x16x32_bf16 v[20:23], v[76:79], a[172:175], v[20:23]\n"
    "v_mfma_f32_16x16x32_bf16 v[16:19], v[76:79], a[200:203], v[16:19]\n"
    "v_mfma_f32_16x16x32_bf16 v[20:23], v[76:79], a[204:207], v[20:23]\n"
    "v_mfma_f32_16x16x32_bf16 v[16:19], v[76:79], a[232:235], v[16:19]\n"
    "v_mfma_f32_16x16x32_bf16 v[20:23], v[76:79], a[236:239], v[20:23]\n"
    "v_mfma_f32_16x16x32_bf16 v[16:19], v[76:79], v[92:95], v[16:19]\n"
    "v_mfma_f32_16x16x32_bf16 v[20:23], v[76:79], v[96:99], v[20:23]\n"
    "ds_read_b64_tr_b16 v[100:101], v63\n"
    "ds_read_b64_tr_b16 v[104:105], v62\n"
    "ds_read_b64_tr_b16 v[102:103], v63 offset:4096\n"
    "ds_read_b64_tr_b16 v[106:107], v62 offset:4096\n"
    "ds_read_b64_tr_b16 v[108:109], v63 offset:8192\n"
    "ds_read_b64_tr_b16 v[112:113], v62 offset:8192\n"
    "ds_read_b64_tr_b16 v[110:111], v63 offset:12288\n"
    "ds_read_b64_tr_b16 v[114:115], v62 offset:12288\n"
    "s_waitcnt vmcnt(28) lgkmcnt(0)\n"
    "s_barrier\n"
    "s_add_u32 m0, s54, 131072\n"
    "s_add_u32 s55, %6, 4194304\n"
    "buffer_load_dwordx4 v48, s[44:47], s55 offen lds\n"
    "s_add_u32 m0, s54, 135168\n"
    "s_add_u32 s55, %6, 4227072\n"
    "buffer_load_dwordx4 v48, s[44:47], s55 offen lds\n"
    "s_add_u32 m0, s54, 139264\n"
    "s_add_u32 s55, %6, 4456448\n"
    "buffer_load_dwordx4 v48, s[44:47], s55 offen lds\n"
    "s_add_u32 m0, s54, 143360\n"
    "s_add_u32 s55, %6, 4489216\n"
    "buffer_load_dwordx4 v48, s[44:47], s55 offen lds\n"
    "v_mov_b32 v7, 0x20400\n"
    "global_load_dwordx2 v[80:81], v7, %1 sc1\n"
    "v_mbcnt_lo_u32_b32 v4, -1, 0\n"
    "v_min_u32 v4, 7, v4\n"
    "v_lshlrev_b32 v4, 7, v4\n"
    "v_add_u32 v4, 0x20000, v4\n"
    "global_load_dwordx2 v[82:83], v4, %1 sc1\n"
    "v_mfma_f32_16x16x32_bf16 v[16:19], v[76:79], v[100:103], v[16:19]\n"
    "v_mfma_f32_16x16x32_bf16 v[20:23], v[76:79], v[104:107], v[20:23]\n"
    "v_mfma_f32_16x16x32_bf16 v[16:19], v[76:79], v[108:111], v[16:19]\n"
    "v_mfma_f32_16x16x32_bf16 v[20:23], v[76:79], v[112:115], v[20:23]\n"
    "ds_read_b64_tr_b16 v[116:117], v32\n"
    "ds_read_b64_tr_b16 v[120:121], v33\n"
    "ds_read_b64_tr_b16 v[148:149], v34\n"
    "ds_read_b64_tr_b16 v[152:153], v35\n"
    "ds_read_b64_tr_b16 v[180:181], v36\n"
    "ds_read_b64_tr_b16 v[184:185], v37\n"
    "ds_read_b64_tr_b16 v[212:213], v38\n"
    "ds_read_b64_tr_b16 v[216:217], v39\n"
    "s_waitcnt vmcnt(30) lgkmcnt(0)\n"
    "s_barrier\n"
    "ds_read_b64_tr_b16 v[118:119], v32 offset:16384\n"
    "ds_read_b64_tr_b16 v[122:123], v33 offset:16384\n"
    "ds_read_b64_tr_b16 v[150:151], v34 offset:16384\n"
    "ds_read_b64_tr_b16 v[154:155], v35 offset:16384\n"
    "ds_read_b64_tr_b16 v[182:183], v36 offset:16384\n"
    "ds_read_b64_tr_b16 v[186:187], v37 offset:16384\n"
    "ds_read_b64_tr_b16 v[214:215], v38 offset:16384\n"
    "ds_read_b64_tr_b16 v[218:219], v39 offset:16384\n"
    "s_waitcnt vmcnt(26) lgkmcnt(0)\n"
    "s_barrier\n"
    "v_mfma_f32_16x16x32_bf16 v[24:27], v[76:79], v[116:119], 0\n"
    "v_mfma_f32_16x16x32_bf16 v[28:31], v[76:79], v[120:123], 0\n"
    "v_mfma_f32_16x16x32_bf16 v[24:27], v[76:79], v[148:151], v[24:27]\n"
    "v_mfma_f32_16x16x32_bf16 v[28:31], v[76:79], v[152:155], v[28:31]\n"
    "v_mfma_f32_16x16x32_bf16 v[24:27], v[76:79], v[180:183], v[24:27]\n"
    "v_mfma_f32_16x16x32_bf16 v[28:31], v[76:79], v[184:187], v[28:31]\n"
    "v_mfma_f32_16x16x32_bf16 v[24:27], v[76:79], v[212:215], v[24:27]\n"
    "v_mfma_f32_16x16x32_bf16 v[28:31], v[76:79], v[216:219], v[28:31]\n"
    "ds_read_b64_tr_b16 v[124:125], v32 offset:32768\n"
    "ds_read_b64_tr_b16 v[128:129], v33 offset:32768\n"
    "ds_read_b64_tr_b16 v[156:157], v34 offset:32768\n"
    "ds_read_b64_tr_b16 v[160:161], v35 offset:32768\n"
    "ds_read_b64_tr_b16 v[188:189], v36 offset:32768\n"
    "ds_read_b64_tr_b16 v[192:193], v37 offset:32768\n"
    "ds_read_b64_tr_b16 v[220:221], v38 offset:32768\n"
    "ds_read_b64_tr_b16 v[224:225], v39 offset:32768\n"
    "s_waitcnt vmcnt(22) lgkmcnt(0)\n"
    "s_barrier\n"
    "ds_read_b64_tr_b16 v[126:127], v32 offset:49152\n"
    "ds_read_b64_tr_b16 v[130:131], v33 offset:49152\n"
    "ds_read_b64_tr_b16 v[158:159], v34 offset:49152\n"
    "ds_read_b64_tr_b16 v[162:163], v35 offset:49152\n"
    "ds_read_b64_tr_b16 v[190:191], v36 offset:49152\n"
    "ds_read_b64_tr_b16 v[194:195], v37 offset:49152\n"
    "ds_read_b64_tr_b16 v[222:223], v38 offset:49152\n"
    "ds_read_b64_tr_b16 v[226:227], v39 offset:49152\n"
    "s_waitcnt vmcnt(18) lgkmcnt(0)\n"
    "s_barrier\n"
    "v_mfma_f32_16x16x32_bf16 v[24:27], v[76:79], v[124:127], v[24:27]\n"
    "v_mfma_f32_16x16x32_bf16 v[28:31], v[76:79], v[128:131], v[28:31]\n"
    "v_mfma_f32_16x16x32_bf16 v[24:27], v[76:79], v[156:159], v[24:27]\n"
    "v_mfma_f32_16x16x32_bf16 v[28:31], v[76:79], v[160:163], v[28:31]\n"
    "v_mfma_f32_16x16x32_bf16 v[24:27], v[76:79], v[188:191], v[24:27]\n"
    "v_mfma_f32_16x16x32_bf16 v[28:31], v[76:79], v[192:195], v[28:31]\n"
    "v_mfma_f32_16x16x32_bf16 v[24:27], v[76:79], v[220:223], v[24:27]\n"
    "v_mfma_f32_16x16x32_bf16 v[28:31], v[76:79], v[224:227], v[28:31]\n"
    "ds_read_b64_tr_b16 v[132:133], v71\n"
    "ds_read_b64_tr_b16 v[136:137], v70\n"
    "ds_read_b64_tr_b16 v[164:165], v69\n"
    "ds_read_b64_tr_b16 v[168:169], v68\n"
    "ds_read_b64_tr_b16 v[196:197], v67\n"
    "ds_read_b64_tr_b16 v[200:201], v66\n"
    "ds_read_b64_tr_b16 v[228:229], v65\n"
    "ds_read_b64_tr_b16 v[232:233], v64\n"
    "s_waitcnt vmcnt(14) lgkmcnt(0)\n"
    "s_barrier\n"
    "ds_read_b64_tr_b16 v[134:135], v71 offset:16384\n"
    "ds_read_b64_tr_b16 v[138:139], v70 offset:16384\n"
    "ds_read_b64_tr_b16 v[166:167], v69 offset:16384\n"
    "ds_read_b64_tr_b16 v[170:171], v68 offset:16384\n"
    "ds_read_b64_tr_b16 v[198:199], v67 offset:16384\n"
    "ds_read_b64_tr_b16 v[202:203], v66 offset:16384\n"
    "ds_read_b64_tr_b16 v[230:231], v65 offset:16384\n"
    "ds_read_b64_tr_b16 v[234:235], v64 offset:16384\n"
    "s_waitcnt vmcnt(10) lgkmcnt(0)\n"
    "s_barrier\n"
    "v_mfma_f32_16x16x32_bf16 v[24:27], v[76:79], v[132:135], v[24:27]\n"
    "v_mfma_f32_16x16x32_bf16 v[28:31], v[76:79], v[136:139], v[28:31]\n"
    "v_mfma_f32_16x16x32_bf16 v[24:27], v[76:79], v[164:167], v[24:27]\n"
    "v_mfma_f32_16x16x32_bf16 v[28:31], v[76:79], v[168:171], v[28:31]\n"
    "v_mfma_f32_16x16x32_bf16 v[24:27], v[76:79], v[196:199], v[24:27]\n"
    "v_mfma_f32_16x16x32_bf16 v[28:31], v[76:79], v[200:203], v[28:31]\n"
    "v_mfma_f32_16x16x32_bf16 v[24:27], v[76:79], v[228:231], v[24:27]\n"
    "v_mfma_f32_16x16x32_bf16 v[28:31], v[76:79], v[232:235], v[28:31]\n"
    "ds_read_b64_tr_b16 v[140:141], v71 offset:32768\n"
    "ds_read_b64_tr_b16 v[144:145], v70 offset:32768\n"
    "ds_read_b64_tr_b16 v[172:173], v69 offset:32768\n"
    "ds_read_b64_tr_b16 v[176:177], v68 offset:32768\n"
    "ds_read_b64_tr_b16 v[204:205], v67 offset:32768\n"
    "ds_read_b64_tr_b16 v[208:209], v66 offset:32768\n"
    "ds_read_b64_tr_b16 v[236:237], v65 offset:32768\n"
    "ds_read_b64_tr_b16 v[240:241], v64 offset:32768\n"
    "s_waitcnt vmcnt(6) lgkmcnt(0)\n"
    "s_barrier\n"
    "ds_read_b64_tr_b16 v[142:143], v71 offset:49152\n"
    "ds_read_b64_tr_b16 v[146:147], v70 offset:49152\n"
    "ds_read_b64_tr_b16 v[174:175], v69 offset:49152\n"
    "ds_read_b64_tr_b16 v[178:179], v68 offset:49152\n"
    "ds_read_b64_tr_b16 v[206:207], v67 offset:49152\n"
    "ds_read_b64_tr_b16 v[210:211], v66 offset:49152\n"
    "ds_read_b64_tr_b16 v[238:239], v65 offset:49152\n"
    "ds_read_b64_tr_b16 v[242:243], v64 offset:49152\n"
    "s_waitcnt vmcnt(2) lgkmcnt(0)\n"
    "s_barrier\n"
    "v_mfma_f32_16x16x32_bf16 v[24:27], v[76:79], v[140:143], v[24:27]\n"
    "v_mfma_f32_16x16x32_bf16 v[28:31], v[76:79], v[144:147], v[28:31]\n"
    "v_mfma_f32_16x16x32_bf16 v[24:27], v[76:79], v[172:175], v[24:27]\n"
    "v_mfma_f32_16x16x32_bf16 v[28:31], v[76:79], v[176:179], v[28:31]\n"
    "v_mfma_f32_16x16x32_bf16 v[24:27], v[76:79], v[204:207], v[24:27]\n"
    "v_mfma_f32_16x16x32_bf16 v[28:31], v[76:79], v[208:211], v[28:31]\n"
    "v_mfma_f32_16x16x32_bf16 v[24:27], v[76:79], v[236:239], v[24:27]\n"
    "v_mfma_f32_16x16x32_bf16 v[28:31], v[76:79], v[240:243], v[28:31]\n"
    "ds_read_b64_tr_b16 v[244:245], v63\n"
    "ds_read_b64_tr_b16 v[248:249], v62\n"
    "ds_read_b64_tr_b16 v[246:247], v63 offset:4096\n"
    "ds_read_b64_tr_b16 v[250:251], v62 offset:4096\n"
    "ds_read_b64_tr_b16 v[252:253], v63 offset:8192\n"
    "ds_read_b64_tr_b16 v[72:73], v62 offset:8192\n"
    "ds_read_b64_tr_b16 v[254:255], v63 offset:12288\n"
    "ds_read_b64_tr_b16 v[74:75], v62 offset:12288\n"
    "s_waitcnt lgkmcnt(0)\n"
    "s_barrier\n"
    "v_mfma_f32_16x16x32_bf16 v[24:27], v[76:79], v[244:247], v[24:27]\n"
    "v_mfma_f32_16x16x32_bf16 v[28:31], v[76:79], v[248:251], v[28:31]\n"
    "v_mfma_f32_16x16x32_bf16 v[24:27], v[76:79], v[252:255], v[24:27]\n"
    "v_mfma_f32_16x16x32_bf16 v[28:31], v[76:79], v[72:75], v[28:31]\n"
    "ds_write_b128 v2, v[72:75] offset:20480\n"
    "v_cvt_pk_bf16_f32 v9, 0, v8\n"
    "v_sub_f32 v8, v8, v9\n"
    "v_cvt_pk_bf16_f32 v10, 0, v8\n"
    "v_sub_f32 v8, v8, v10\n"
    "v_cvt_pk_bf16_f32 v9, v9, v10\n"
    "v_cvt_pk_bf16_f32 v10, v8, 0\n"
    "s_mov_b64 vcc, 0xffff\n"
    "v_cndmask_b32 v32, 0, v9, vcc\n"
    "v_cndmask_b32 v33, 0, v10, vcc\n"
    "v_cvt_pk_bf16_f32 v13, 0, v12\n"
    "v_sub_f32 v12, v12, v13\n"
    "v_cvt_pk_bf16_f32 v14, 0, v12\n"
    "v_sub_f32 v12, v12, v14\n"
    "v_cvt_pk_bf16_f32 v13, v13, v14\n"
    "v_cvt_pk_bf16_f32 v14, v12, 0\n"
    "s_mov_b64 vcc, 0xffff\n"
    "v_cndmask_b32 v34, 0, v13, vcc\n"
    "v_cndmask_b32 v35, 0, v14, vcc\n"
    "ds_write_b128 v2, v[32:35] offset:8192\n"
    "v_cvt_pk_bf16_f32 v17, 0, v16\n"
    "v_sub_f32 v16, v16, v17\n"
    "v_cvt_pk_bf16_f32 v18, 0, v16\n"
    "v_sub_f32 v16, v16, v18\n"
    "v_cvt_pk_bf16_f32 v17, v17, v18\n"
    "v_cvt_pk_bf16_f32 v18, v16, 0\n"
    "s_mov_b64 vcc, 0xffff\n"
    "v_cndmask_b32 v36, 0, v17, vcc\n"
    "v_cndmask_b32 v37, 0, v18, vcc\n"
    "v_cvt_pk_bf16_f32 v21, 0, v20\n"
    "v_sub_f32 v20, v20, v21\n"
    "v_cvt_pk_bf16_f32 v22, 0, v20\n"
    "v_sub_f32 v20, v20, v22\n"
    "v_cvt_pk_bf16_f32 v21, v21, v22\n"
    "v_cvt_pk_bf16_f32 v22, v20, 0\n"
    "s_mov_b64 vcc, 0xffff\n"
    "v_cndmask_b32 v38, 0, v21, vcc\n"
    "v_cndmask_b32 v39, 0, v22, vcc\n"
    "ds_write_b128 v2, v[36:39] offset:12288\n"
    "v_cvt_pk_bf16_f32 v25, 0, v24\n"
    "v_sub_f32 v24, v24, v25\n"
    "v_cvt_pk_bf16_f32 v26, 0, v24\n"
    "v_sub_f32 v24, v24, v26\n"
    "v_cvt_pk_bf16_f32 v25, v25, v26\n"
    "v_cvt_pk_bf16_f32 v26, v24, 0\n"
    "s_mov_b64 vcc, 0xffff\n"
    "v_cndmask_b32 v40, 0, v25, vcc\n"
    "v_cndmask_b32 v41, 0, v26, vcc\n"
    "v_cvt_pk_bf16_f32 v29, 0, v28\n"
    "v_sub_f32 v28, v28, v29\n"
    "v_cvt_pk_bf16_f32 v30, 0, v28\n"
    "v_sub_f32 v28, v28, v30\n"
    "v_cvt_pk_bf16_f32 v29, v29, v30\n"
    "v_cvt_pk_bf16_f32 v30, v28, 0\n"
    "s_mov_b64 vcc, 0xffff\n"
    "v_cndmask_b32 v42, 0, v29, vcc\n"
    "v_cndmask_b32 v43, 0, v30, vcc\n"
    "ds_write_b128 v2, v[40:43] offset:16384\n"
    "s_waitcnt vmcnt(0)\n"
    "v_add_u32 v80, 1, v80\n"
    "v_add_u32 v81, 1, v81\n"
    "s_mov_b32 s52, 1048576\n"
    ".Lhgo_a%=:\n"
    "v_cmp_ne_u64 vcc, v[80:81], v[82:83]\n"
    "s_cbranch_vccz .Lhgo_x%=\n"
    "global_load_dwordx2 v[82:83], v4, %1 sc1\n"
    "s_sub_u32 s52, s52, 1\n"
    "s_waitcnt vmcnt(0)\n"
    "s_cbranch_scc0 .Lhgo_a%=\n"
    ".Lhgo_x%=:\n"
    "v_mbcnt_lo_u32_b32 v80, -1, 0\n"
    "v_mbcnt_hi_u32_b32 v80, -1, v80\n"
    "v_min_u32 v4, 5, v80\n"
    "v_lshlrev_b32 v4, 7, v4\n"
    "v_mov_b32 v7, 0x26c00\n"
    "ds_read_b128 v[80:83], v7\n"
    "s_waitcnt lgkmcnt(0)\n"
    "v_readfirstlane_b32 s49, v81\n"
    "v_add_u32 v80, v80, v4\n"
    "s_branch .Lhchk_%=\n"
    ".Lhgo_%=:\n"
    "s_waitcnt vmcnt(2)\n"
    "v_mfma_f32_16x16x32_bf16 v[68:71], v[48:51], v[48:51], 0\n"
    "v_add_u32 v7, 32, v7\n"
    "ds_read_b128 v[80:83], v7\n"
    "v_mfma_f32_16x16x32_bf16 v[68:71], v[52:55], v[52:55], v[68:71]\n"
    "ds_write_b128 v1, v[48:51]\n"
    "ds_write_b128 v1, v[52:55] offset:1024\n"
    "v_mfma_f32_16x16x32_bf16 v[68:71], v[56:59], v[56:59], v[68:71]\n"
    "ds_write_b128 v1, v[56:59] offset:2048\n"
    "ds_write_b128 v1, v[60:63] offset:3072\n"
    "v_mfma_f32_16x16x32_bf16 v[68:71], v[60:63], v[60:63], v[68:71]\n"
    "v_mul_f32 v24, v24, v76\n"
    "v_mul_f32 v25, v25, v77\n"
    "v_mfma_f32_16x16x32_bf16 v[68:71], v[64:67], v[64:67], v[68:71]\n"
    "v_mul_f32 v26, v26, v78\n"
    "v_mul_f32 v27, v27, v79\n"
    "v_mfma_f32_16x16x32_bf16 v[8:11], a[0:3], v[48:51], 0\n"
    "v_mul_f32 v28, v28, v76\n"
    "v_mul_f32 v29, v29, v77\n"
    "v_mfma_f32_16x16x32_bf16 v[12:15], a[4:7], v[48:51], 0\n"
    "v_mul_f32 v30, v30, v78\n"
    "v_mul_f32 v31, v31, v79\n"
    "v_mfma_f32_16x16x32_bf16 v[16:19], a[144:147], v[48:51], 0\n"
    "v_mov_b64 v[76:77], s[86:87]\n"
    "v_mov_b64 v[78:79], s[86:87]\n"
    "v_mfma_f32_16x16x32_bf16 v[20:23], a[148:151], v[48:51], 0\n"
    "ds_write_b128 v1, v[64:67] offset:4096\n"
    "v_cvt_pk_bf16_f32 v24, v24, v25\n"
    "v_mfma_f32_16x16x32_bf16 v[72:75], v[76:79], v[48:51], 0\n"
    "v_cvt_pk_bf16_f32 v25, v26, v27\n"
    "v_cvt_pk_bf16_f32 v26, v28, v29\n"
    "v_mfma_f32_16x16x32_bf16 v[72:75], v[76:79], v[52:55], v[72:75]\n"
    "v_cvt_pk_bf16_f32 v27, v30, v31\n"
    "buffer_store_dwordx4 v[24:27], v2, s[40:43], s64 offen sc1\n"
    "v_mfma_f32_16x16x32_bf16 v[72:75], v[76:79], v[56:59], v[72:75]\n"
    "ds_write_b128 v1, v[68:71] offset:5120\n"
    "s_waitcnt lgkmcnt(6)\n"
    "v_readfirstlane_b32 s49, v81\n"
    "v_mfma_f32_16x16x32_bf16 v[72:75], v[76:79], v[60:63], v[72:75]\n"
    "v_readfirstlane_b32 s62, v82\n"
    "v_add_u32 v80, v80, v4\n"
    "v_mfma_f32_16x16x32_bf16 v[72:75], v[76:79], v[64:67], v[72:75]\n"
    ".Lhtop_%=:\n"
    "v_mfma_f32_16x16x32_bf16 v[24:27], v[48:51], v[116:119], 0\n"
    "global_load_dword v81, v80, %1 sc1\n"
    "s_add_u32 s63, s62, 0x2000000\n"
    "v_mfma_f32_16x16x32_bf16 v[28:31], v[48:51], v[120:123], 0\n"
    "s_add_u32 s64, s62, 0x4000000\n"
    "v_mfma_f32_16x16x32_bf16 v[8:11], a[8:11], v[52:55], v[8:11]\n"
    "ds_write_b128 v1, v[72:75] offset:6144\n"
    "v_xor_b32 v0, 0x2000, v1\n"
    "v_mfma_f32_16x16x32_bf16 v[12:15], a[12:15], v[52:55], v[12:15]\n"
    "v_mfma_f32_16x16x32_bf16 v[16:19], a[152:155], v[52:55], v[16:19]\n"
    "v_mfma_f32_16x16x32_bf16 v[20:23], a[156:159], v[52:55], v[20:23]\n"
    "v_mfma_f32_16x16x32_bf16 v[24:27], v[52:55], v[124:127], v[24:27]\n"
    "s_waitcnt lgkmcnt(0)\n"
    "s_barrier\n"
    "v_mfma_f32_16x16x32_bf16 v[28:31], v[52:55], v[128:131], v[28:31]\n"
    "ds_read_b128 v[32:35], v0\n"
    "ds_read_b128 v[36:39], v0 offset:1024\n"
    "v_mfma_f32_16x16x32_bf16 v[8:11], a[16:19], v[56:59], v[8:11]\n"
    "ds_read2st64_b32 v[68:69], v6 offset0:20 offset1:52\n"
    "ds_read2st64_b32 v[70:71], v6 offset0:84 offset1:116\n"
    "v_mfma_f32_16x16x32_bf16 v[12:15], a[20:23], v[56:59], v[12:15]\n"
    "ds_read2st64_b32 v[74:75], v6 offset0:88 offset1:120\n"
    "ds_read2st64_b32 v[72:73], v6 offset0:24 offset1:56\n"
    "v_mfma_f32_16x16x32_bf16 v[16:19], a[160:163], v[56:59], v[16:19]\n"
    "v_mov_b64 v[64:65], 0\n"
    "v_mfma_f32_16x16x32_bf16 v[20:23], a[164:167], v[56:59], v[20:23]\n"
    "v_mfma_f32_16x16x32_bf16 v[24:27], v[56:59], v[132:135], v[24:27]\n"
    "v_mfma_f32_16x16x32_bf16 v[28:31], v[56:59], v[136:139], v[28:31]\n"
    "ds_read_b128 v[48:51], v2 offset:8192\n"
    "ds_read_b128 v[52:55], v2 offset:12288\n"
    "v_mfma_f32_16x16x32_bf16 v[8:11], a[24:27], v[60:63], v[8:11]\n"
    "ds_read_b128 v[56:59], v2 offset:16384\n"
    "ds_read_b128 v[44:47], v0 offset:3072\n"
    "v_mfma_f32_16x16x32_bf16 v[12:15], a[28:31], v[60:63], v[12:15]\n"
    "ds_read_b128 v[40:43], v0 offset:2048\n"
    "v_mfma_f32_16x16x32_bf16 v[16:19], a[168:171], v[60:63], v[16:19]\n"
    "s_waitcnt lgkmcnt(5)\n"
    "v_add_f32 v72, v72, v73\n"
    "v_add_f32 v74, v74, v75\n"
    "v_mfma_f32_16x16x32_bf16 v[20:23], a[172:175], v[60:63], v[20:23]\n"
    "v_add_f32 v68, v68, v69\n"
    "v_add_f32 v70, v70, v71\n"
    "v_mfma_f32_16x16x32_bf16 v[24:27], v[60:63], v[140:143], v[24:27]\n"
    "v_add_f32 v72, v72, v74\n"
    "v_add_f32 v70, v68, v70\n"
    "v_mfma_f32_16x16x32_bf16 v[28:31], v[60:63], v[144:147], v[28:31]\n"
    "v_mul_f32 v72, 0xbae38e39, v72\n"
    "v_mul_f32 v70, 0x3ae38e39, v70\n"
    "v_mfma_f32_16x16x32_bf16 v[8:11], a[32:35], v[32:35], v[8:11]\n"
    "v_cvt_pk_bf16_f32 v62, v72, v72\n"
    "v_fma_f32 v70, -v72, v72, v70\n"
    "v_mfma_f32_16x16x32_bf16 v[12:15], a[36:39], v[32:35], v[12:15]\n"
    "v_cvt_pk_bf16_f32 v63, v72, v72\n"
    "v_max_f32 v70, 0, v70\n"
    "v_mfma_f32_16x16x32_bf16 v[16:19], a[176:179], v[32:35], v[16:19]\n"
    "v_cvt_pk_bf16_f32 v70, 0, v70\n"
    "v_add_f32 v70, 0x3727c5ac, v70\n"
    "v_mfma_f32_16x16x32_bf16 v[20:23], a[180:183], v[32:35], v[20:23]\n"
    "v_cvt_pk_bf16_f32 v70, 0, v70\n"
    "v_sqrt_f32 v70, v70\n"
    "v_mfma_f32_16x16x32_bf16 v[24:27], v[32:35], v[148:151], v[24:27]\n"
    "v_cvt_pk_bf16_f32 v70, 0, v70\n"
    "v_mfma_f32_16x16x32_bf16 v[28:31], v[32:35], v[152:155], v[28:31]\n"
    "v_rcp_f32 v70, v70\n"
    "v_mfma_f32_16x16x32_bf16 v[8:11], a[40:43], v[36:39], v[8:11]\n"
    "v_mov_b64 v[60:61], 0\n"
    "v_mfma_f32_16x16x32_bf16 v[12:15], a[44:47], v[36:39], v[12:15]\n"
    "ds_write_b32 v5, v70\n"
    "v_lshrrev_b32 v71, 4, v2\n"
    "v_mfma_f32_16x16x32_bf16 v[16:19], a[184:187], v[36:39], v[16:19]\n"
    "v_and_b32 v71, 48, v71\n"
    "ds_read_b128 v[76:79], v71 offset:24576\n"
    "v_mfma_f32_16x16x32_bf16 v[20:23], a[188:191], v[36:39], v[20:23]\n"
    "ds_read_b128 v[72:75], v2 offset:20480\n"
    "v_mfma_f32_16x16x32_bf16 v[24:27], v[36:39], v[156:159], v[24:27]\n"
    "v_xor_b32 v0, 0x4000, v1\n"
    "v_mfma_f32_16x16x32_bf16 v[28:31], v[36:39], v[160:163], v[28:31]\n"
    "s_waitcnt lgkmcnt(3)\n"
    "v_mfma_f32_16x16x32_bf16 v[8:11], a[48:51], v[40:43], v[8:11]\n"
    "ds_read_b128 v[36:39], v0 offset:1024\n"
    "ds_read_b128 v[32:35], v0\n"
    "v_mfma_f32_16x16x32_bf16 v[12:15], a[52:55], v[40:43], v[12:15]\n"
    "v_mfma_f32_16x16x32_bf16 v[16:19], a[192:195], v[40:43], v[16:19]\n"
    "v_mfma_f32_16x16x32_bf16 v[20:23], a[196:199], v[40:43], v[20:23]\n"
    "v_mfma_f32_16x16x32_bf16 v[24:27], v[40:43], v[164:167], v[24:27]\n"
    "v_mfma_f32_16x16x32_bf16 v[28:31], v[40:43], v[168:171], v[28:31]\n"
    "v_mfma_f32_16x16x32_bf16 v[8:11], v[48:51], v[62:65], v[8:11]\n"
    "v_mfma_f32_16x16x32_bf16 v[12:15], v[48:51], v[60:63], v[12:15]\n"
    "v_mfma_f32_16x16x32_bf16 v[16:19], v[52:55], v[62:65], v[16:19]\n"
    "s_waitcnt vmcnt(0)\n"
    "v_mov_b32 v82, 1\n"
    "v_mfma_f32_16x16x32_bf16 v[20:23], v[52:55], v[60:63], v[20:23]\n"
    "s_mov_b64 exec, s[56:57]\n"
    "ds_add_rtn_u32 v82, v7, v82 offset:16\n"
    "s_mov_b64 exec, -1\n"
    "v_mfma_f32_16x16x32_bf16 v[24:27], v[62:65], v[56:59], v[24:27]\n"
    "v_cmp_eq_u32 vcc, 0, v81\n"
    "s_cmp_eq_u64 vcc, 0\n"
    "v_mfma_f32_16x16x32_bf16 v[28:31], v[60:63], v[56:59], v[28:31]\n"
    "s_cselect_b32 s94, s42, 0\n"
    "s_cselect_b32 s90, s96, 0\n"
    "v_mfma_f32_16x16x32_bf16 v[8:11], a[56:59], v[44:47], v[8:11]\n"
    "global_load_dword v81, v80, %1 sc1\n"
    "v_mfma_f32_16x16x32_bf16 v[12:15], a[60:63], v[44:47], v[12:15]\n"
    "buffer_load_dwordx4 v[48:51], v3, s[92:95], s49 offen sc1\n"
    "v_mfma_f32_16x16x32_bf16 v[16:19], a[200:203], v[44:47], v[16:19]\n"
    "buffer_load_dwordx4 v[52:55], v3, s[92:95], s49 offen offset:1024 sc1\n"
    "v_mfma_f32_16x16x32_bf16 v[20:23], a[204:207], v[44:47], v[20:23]\n"
    "s_waitcnt lgkmcnt(0)\n"
    "v_readfirstlane_b32 s50, v82\n"
    "v_mfma_f32_16x16x32_bf16 v[24:27], v[44:47], v[172:175], v[24:27]\n"
    "buffer_load_dwordx4 v[56:59], v3, s[92:95], s49 offen offset:2048 sc1\n"
    "s_cmp_eq_u32 s50, 3\n"
    "v_mfma_f32_16x16x32_bf16 v[28:31], v[44:47], v[176:179], v[28:31]\n"
    "v_mfma_f32_16x16x32_bf16 v[8:11], a[64:67], v[32:35], v[8:11]\n"
    "ds_read_b128 v[44:47], v0 offset:3072\n"
    "ds_read_b128 v[40:43], v0 offset:2048\n"
    "v_mfma_f32_16x16x32_bf16 v[12:15], a[68:71], v[32:35], v[12:15]\n"
    "s_cselect_b64 exec, 1, 0\n"
    "global_store_dword v83, v82, %1 sc1\n"
    "s_mov_b64 exec, -1\n"
    "v_mfma_f32_16x16x32_bf16 v[16:19], a[208:211], v[32:35], v[16:19]\n"
    "buffer_load_dwordx4 v[60:63], v3, s[92:95], s49 offen offset:3072 sc1\n"
    "v_mfma_f32_16x16x32_bf16 v[20:23], a[212:215], v[32:35], v[20:23]\n"
    "buffer_load_dwordx4 v[64:67], v2, s[88:91], s49 offen sc1\n"
    "v_mfma_f32_16x16x32_bf16 v[24:27], v[32:35], v[180:183], v[24:27]\n"
    "v_mfma_f32_16x16x32_bf16 v[28:31], v[32:35], v[184:187], v[28:31]\n"
    "v_mfma_f32_16x16x32_bf16 v[8:11], a[72:75], v[36:39], v[8:11]\n"
    "v_mfma_f32_16x16x32_bf16 v[12:15], a[76:79], v[36:39], v[12:15]\n"
    "v_mfma_f32_16x16x32_bf16 v[16:19], a[216:219], v[36:39], v[16:19]\n"
    "v_mfma_f32_16x16x32_bf16 v[20:23], a[220:223], v[36:39], v[20:23]\n"
    "v_mfma_f32_16x16x32_bf16 v[24:27], v[36:39], v[188:191], v[24:27]\n"
    "v_xor_b32 v0, 0x6000, v1\n"
    "v_mfma_f32_16x16x32_bf16 v[28:31], v[36:39], v[192:195], v[28:31]\n"
    "s_waitcnt lgkmcnt(0)\n"
    "v_mfma_f32_16x16x32_bf16 v[8:11], a[80:83], v[40:43], v[8:11]\n"
    "ds_read_b128 v[36:39], v0 offset:1024\n"
    "ds_read_b128 v[32:35], v0\n"
    "v_mfma_f32_16x16x32_bf16 v[12:15], a[84:87], v[40:43], v[12:15]\n"
    "v_mfma_f32_16x16x32_bf16 v[16:19], a[224:227], v[40:43], v[16:19]\n"
    "v_mfma_f32_16x16x32_bf16 v[20:23], a[228:231], v[40:43], v[20:23]\n"
    "v_mfma_f32_16x16x32_bf16 v[24:27], v[40:43], v[196:199], v[24:27]\n"
    "v_mfma_f32_16x16x32_bf16 v[28:31], v[40:43], v[200:203], v[28:31]\n"
    "v_mfma_f32_16x16x32_bf16 v[8:11], a[88:91], v[44:47], v[8:11]\n"
    "v_mfma_f32_16x16x32_bf16 v[12:15], a[92:95], v[44:47], v[12:15]\n"
    "v_mfma_f32_16x16x32_bf16 v[16:19], a[232:235], v[44:47], v[16:19]\n"
    "v_mfma_f32_16x16x32_bf16 v[20:23], a[236:239], v[44:47], v[20:23]\n"
    "v_mfma_f32_16x16x32_bf16 v[24:27], v[44:47], v[204:207], v[24:27]\n"
    "v_mfma_f32_16x16x32_bf16 v[28:31], v[44:47], v[208:211], v[28:31]\n"
    "s_waitcnt lgkmcnt(0)\n"
    "v_mfma_f32_16x16x32_bf16 v[8:11], a[96:99], v[32:35], v[8:11]\n"
    "ds_read_b128 v[44:47], v0 offset:3072\n"
    "ds_read_b128 v[40:43], v0 offset:2048\n"
    "v_mfma_f32_16x16x32_bf16 v[12:15], a[100:103], v[32:35], v[12:15]\n"
    "v_mfma_f32_16x16x32_bf16 v[16:19], a[240:243], v[32:35], v[16:19]\n"
    "s_cmp_eq_u32 s94, 0\n"
    "s_cbranch_scc1 .Lhk1_%=\n"
    ".Lhk1_x%=:\n"
    "v_mfma_f32_16x16x32_bf16 v[20:23], a[244:247], v[32:35], v[20:23]\n"
    "v_mfma_f32_16x16x32_bf16 v[24:27], v[32:35], v[212:215], v[24:27]\n"
    "v_and_b32 v0, 0xffff9fff, v1\n"
    "v_mfma_f32_16x16x32_bf16 v[28:31], v[32:35], v[216:219], v[28:31]\n"
    "ds_read_b128 v[32:35], v0 offset:4096\n"
    "v_mfma_f32_16x16x32_bf16 v[8:11], a[104:107], v[36:39], v[8:11]\n"
    "v_mfma_f32_16x16x32_bf16 v[12:15], a[108:111], v[36:39], v[12:15]\n"
    "v_mfma_f32_16x16x32_bf16 v[16:19], a[248:251], v[36:39], v[16:19]\n"
    "v_mfma_f32_16x16x32_bf16 v[20:23], a[252:255], v[36:39], v[20:23]\n"
    "v_mfma_f32_16x16x32_bf16 v[24:27], v[36:39], v[220:223], v[24:27]\n"
    "v_mfma_f32_16x16x32_bf16 v[28:31], v[36:39], v[224:227], v[28:31]\n"
    "ds_read_b128 v[36:39], v0 offset:12288\n"
    "s_waitcnt lgkmcnt(2)\n"
    "v_mfma_f32_16x16x32_bf16 v[8:11], a[112:115], v[40:43], v[8:11]\n"
    "v_mfma_f32_16x16x32_bf16 v[12:15], a[116:119], v[40:43], v[12:15]\n"
    "v_xor_b32 v1, 0x8000, v1\n"
    "v_xor_b32 v6, 0x8000, v6\n"
    "v_mfma_f32_16x16x32_bf16 v[8:11], a[120:123], v[44:47], v[8:11]\n"
    "v_mfma_f32_16x16x32_bf16 v[12:15], a[124:127], v[44:47], v[12:15]\n"
    "s_waitcnt lgkmcnt(1)\n"
    "v_mfma_f32_16x16x32_bf16 v[8:11], a[128:131], v[32:35], v[8:11]\n"
    "v_mfma_f32_16x16x32_bf16 v[12:15], a[132:135], v[32:35], v[12:15]\n"
    "s_waitcnt lgkmcnt(0)\n"
    "v_mfma_f32_16x16x32_bf16 v[8:11], a[136:139], v[36:39], v[8:11]\n"
    "v_mfma_f32_16x16x32_bf16 v[12:15], a[140:143], v[36:39], v[12:15]\n"
    "v_mfma_f32_16x16x32_bf16 v[16:19], v[84:87], v[40:43], v[16:19]\n"
    "v_mfma_f32_16x16x32_bf16 v[20:23], v[88:91], v[40:43], v[20:23]\n"
    "v_mul_f32 v8, v8, v70\n"
    "v_mul_f32 v12, v12, v70\n"
    "v_mfma_f32_16x16x32_bf16 v[16:19], v[92:95], v[44:47], v[16:19]\n"
    "v_cvt_pk_bf16_f32 v8, v8, v12\n"
    "v_mul_f32 v9, v9, v70\n"
    "v_mfma_f32_16x16x32_bf16 v[20:23], v[96:99], v[44:47], v[20:23]\n"
    "v_mul_f32 v13, v13, v70\n"
    "v_cvt_pk_bf16_f32 v9, v9, v13\n"
    "v_mfma_f32_16x16x32_bf16 v[16:19], v[100:103], v[32:35], v[16:19]\n"
    "v_mul_f32 v10, v10, v70\n"
    "v_mul_f32 v14, v14, v70\n"
    "v_mfma_f32_16x16x32_bf16 v[20:23], v[104:107], v[32:35], v[20:23]\n"
    "v_cvt_pk_bf16_f32 v10, v10, v14\n"
    "v_mul_f32 v11, v11, v70\n"
    "v_mfma_f32_16x16x32_bf16 v[16:19], v[108:111], v[36:39], v[16:19]\n"
    "v_mul_f32 v15, v15, v70\n"
    "v_cvt_pk_bf16_f32 v11, v11, v15\n"
    "v_mfma_f32_16x16x32_bf16 v[20:23], v[112:115], v[36:39], v[20:23]\n"
    "buffer_store_dwordx4 v[8:11], v2, s[40:43], s62 offen sc1\n"
    "v_mfma_f32_16x16x32_bf16 v[24:27], v[40:43], v[228:231], v[24:27]\n"
    "v_mfma_f32_16x16x32_bf16 v[28:31], v[40:43], v[232:235], v[28:31]\n"
    "v_mul_f32 v16, v16, v70\n"
    "v_mul_f32 v20, v20, v70\n"
    "v_mfma_f32_16x16x32_bf16 v[24:27], v[44:47], v[236:239], v[24:27]\n"
    "v_cvt_pk_bf16_f32 v16, v16, v20\n"
    "v_mul_f32 v17, v17, v70\n"
    "v_mfma_f32_16x16x32_bf16 v[28:31], v[44:47], v[240:243], v[28:31]\n"
    "v_mul_f32 v21, v21, v70\n"
    "v_cvt_pk_bf16_f32 v17, v17, v21\n"
    "v_mfma_f32_16x16x32_bf16 v[24:27], v[32:35], v[244:247], v[24:27]\n"
    "v_mul_f32 v18, v18, v70\n"
    "v_mul_f32 v22, v22, v70\n"
    "v_mfma_f32_16x16x32_bf16 v[28:31], v[32:35], v[248:251], v[28:31]\n"
    "v_cvt_pk_bf16_f32 v18, v18, v22\n"
    "v_mul_f32 v19, v19, v70\n"
    "v_mfma_f32_16x16x32_bf16 v[24:27], v[36:39], v[252:255], v[24:27]\n"
    "v_mul_f32 v23, v23, v70\n"
    "v_cvt_pk_bf16_f32 v19, v19, v23\n"
    "v_mfma_f32_16x16x32_bf16 v[28:31], v[36:39], v[72:75], v[28:31]\n"
    "buffer_store_dwordx4 v[16:19], v2, s[40:43], s63 offen sc1\n"
    "s_mov_b32 s56, 1\n"
    "s_cmp_lg_u32 s94, 0\n"
    "s_cbranch_scc1 .Lhgo_%=\n"
    "s_nop 0\n"
    "v_mul_f32 v24, v24, v76\n"
    "v_mul_f32 v25, v25, v77\n"
    "v_cvt_pk_bf16_f32 v24, v24, v25\n"
    "v_mul_f32 v26, v26, v78\n"
    "v_mul_f32 v27, v27, v79\n"
    "v_cvt_pk_bf16_f32 v25, v26, v27\n"
    "v_mul_f32 v28, v28, v76\n"
    "v_mul_f32 v29, v29, v77\n"
    "v_cvt_pk_bf16_f32 v26, v28, v29\n"
    "v_mul_f32 v30, v30, v78\n"
    "v_mul_f32 v31, v31, v79\n"
    "v_cvt_pk_bf16_f32 v27, v30, v31\n"
    "buffer_store_dwordx4 v[24:27], v2, s[40:43], s64 offen sc1\n"
    "s_waitcnt vmcnt(3)\n"
    "v_cmp_eq_u32 vcc, 0, v81\n"
    "s_cmp_eq_u64 vcc, 0\n"
    "s_cbranch_scc1 .Lhld_%=\n"
    "ds_read_b32 v83, v7 offset:44\n"
    "global_load_dword v81, v80, %1 sc1\n"
    "s_waitcnt vmcnt(0)\n"
    "v_mov_b32 v82, 1\n"
    "s_mov_b64 exec, 1\n"
    "ds_add_rtn_u32 v82, v7, v82 offset:48\n"
    "s_mov_b64 exec, -1\n"
    "s_waitcnt lgkmcnt(0)\n"
    "v_readfirstlane_b32 s50, v82\n"
    "s_cmp_eq_u32 s50, 3\n"
    "s_cselect_b64 exec, 1, 0\n"
    "global_store_dword v83, v82, %1 sc1\n"
    "s_mov_b64 exec, -1\n"
    "s_mov_b32 s56, 0\n"
    "v_cmp_eq_u32 vcc, 0, v81\n"
    "s_cmp_eq_u64 vcc, 0\n"
    "s_cbranch_scc1 .Lhld_%=\n"
    ".Lhchk_%=:\n"
    "v_readfirstlane_b32 s50, v80\n"
    "s_cmp_lt_u32 s50, 200\n"
    "s_cbranch_scc1 .Lhend_%=\n"
    ".Lhwait_%=:\n"
    "global_load_dword v81, v80, %1 sc1\n"
    "s_mov_b32 s52, 0\n"
    ".Lhp2_%=:\n"
    "s_waitcnt vmcnt(0)\n"
    "v_cmp_eq_u32 vcc, 0, v81\n"
    "s_cmp_eq_u64 vcc, 0\n"
    "s_cbranch_scc1 .Lhp2_x%=\n"
    "s_add_u32 s52, s52, 1\n"
    "s_sleep 2\n"
    "global_load_dword v81, v80, %1 sc1\n"
    "s_cmp_lt_u32 s52, s53\n"
    "s_cbranch_scc1 .Lhp2_%=\n"
    "s_mov_b32 s53, 0\n"
    "v_mov_b32 v82, 0\n"
    "v_mov_b32 v83, 1\n"
    "global_store_dword v82, v83, %1 sc1\n"
    "s_waitcnt vmcnt(0)\n"
    ".Lhp2_x%=:\n"
    ".Lhld_%=:\n"
    "s_mov_b32 s94, s42\n"
    "s_mov_b32 s90, s96\n"
    "buffer_load_dwordx4 v[48:51], v3, s[92:95], s49 offen sc1\n"
    "buffer_load_dwordx4 v[52:55], v3, s[92:95], s49 offen offset:1024 sc1\n"
    "buffer_load_dwordx4 v[56:59], v3, s[92:95], s49 offen offset:2048 sc1\n"
    "buffer_load_dwordx4 v[60:63], v3, s[92:95], s49 offen offset:3072 sc1\n"
    "buffer_load_dwordx4 v[64:67], v2, s[88:91], s49 offen sc1\n"
    ".Lhin_%=:\n"
    "v_add_u32 v7, 32, v7\n"
    "ds_read_b128 v[80:83], v7\n"
    "s_waitcnt vmcnt(4)\n"
    "v_mfma_f32_16x16x32_bf16 v[68:71], v[48:51], v[48:51], 0\n"
    "s_waitcnt vmcnt(3)\n"
    "v_mfma_f32_16x16x32_bf16 v[68:71], v[52:55], v[52:55], v[68:71]\n"
    "ds_write_b128 v1, v[48:51]\n"
    "ds_write_b128 v1, v[52:55] offset:1024\n"
    "s_waitcnt vmcnt(2)\n"
    "v_mfma_f32_16x16x32_bf16 v[68:71], v[56:59], v[56:59], v[68:71]\n"
    "ds_write_b128 v1, v[56:59] offset:2048\n"
    "s_waitcnt vmcnt(1)\n"
    "ds_write_b128 v1, v[60:63] offset:3072\n"
    "v_mfma_f32_16x16x32_bf16 v[68:71], v[60:63], v[60:63], v[68:71]\n"
    "s_waitcnt vmcnt(0)\n"
    "v_mfma_f32_16x16x32_bf16 v[68:71], v[64:67], v[64:67], v[68:71]\n"
    "v_mfma_f32_16x16x32_bf16 v[8:11], a[0:3], v[48:51], 0\n"
    "v_mfma_f32_16x16x32_bf16 v[12:15], a[4:7], v[48:51], 0\n"
    "v_mfma_f32_16x16x32_bf16 v[16:19], a[144:147], v[48:51], 0\n"
    "v_mov_b64 v[76:77], s[86:87]\n"
    "v_mov_b64 v[78:79], s[86:87]\n"
    "v_mfma_f32_16x16x32_bf16 v[20:23], a[148:151], v[48:51], 0\n"
    "ds_write_b128 v1, v[64:67] offset:4096\n"
    "v_mfma_f32_16x16x32_bf16 v[72:75], v[76:79], v[48:51], 0\n"
    "v_mfma_f32_16x16x32_bf16 v[72:75], v[76:79], v[52:55], v[72:75]\n"
    "v_mfma_f32_16x16x32_bf16 v[72:75], v[76:79], v[56:59], v[72:75]\n"
    "ds_write_b128 v1, v[68:71] offset:5120\n"
    "s_waitcnt lgkmcnt(6)\n"
    "v_readfirstlane_b32 s49, v81\n"
    "v_mfma_f32_16x16x32_bf16 v[72:75], v[76:79], v[60:63], v[72:75]\n"
    "v_readfirstlane_b32 s62, v82\n"
    "v_add_u32 v80, v80, v4\n"
    "v_mfma_f32_16x16x32_bf16 v[72:75], v[76:79], v[64:67], v[72:75]\n"
    "s_cmp_eq_u32 s56, 0\n"
    "s_cbranch_scc1 .Lhc3_%=\n"
    "s_waitcnt vmcnt(0)\n"
    "v_mov_b32 v82, 1\n"
    "s_mov_b64 exec, 1\n"
    "ds_add_rtn_u32 v82, v7, v82 offset:16\n"
    "s_mov_b64 exec, -1\n"
    "s_waitcnt lgkmcnt(0)\n"
    "v_readfirstlane_b32 s50, v82\n"
    "s_cmp_eq_u32 s50, 3\n"
    "s_cselect_b64 exec, 1, 0\n"
    "global_store_dword v83, v82, %1 sc1\n"
    "s_mov_b64 exec, -1\n"
    "s_mov_b32 s56, 0\n"
    ".Lhc3_%=:\n"
    "s_branch .Lhtop_%=\n"
    ".Lhk1_%=:\n"
    "s_waitcnt vmcnt(5)\n"
    "v_cmp_eq_u32 vcc, 0, v81\n"
    "s_cmp_eq_u64 vcc, 0\n"
    "s_cselect_b32 s94, s42, 0\n"
    "s_cselect_b32 s90, s96, 0\n"
    "global_load_dword v81, v80, %1 sc1\n"
    "buffer_load_dwordx4 v[48:51], v3, s[92:95], s49 offen sc1\n"
    "buffer_load_dwordx4 v[52:55], v3, s[92:95], s49 offen offset:1024 sc1\n"
    "buffer_load_dwordx4 v[56:59], v3, s[92:95], s49 offen offset:2048 sc1\n"
    "buffer_load_dwordx4 v[60:63], v3, s[92:95], s49 offen offset:3072 sc1\n"
    "buffer_load_dwordx4 v[64:67], v2, s[88:91], s49 offen sc1\n"
    "s_branch .Lhk1_x%=\n"
    ".Lhpol_%=:\n"
    "v_mov_b32 v80, 64\n"
    "global_load_dword v81, v80, %1 sc1\n"
    "s_mov_b32 s52, 0\n"
    ".Lhp4_%=:\n"
    "s_waitcnt vmcnt(0)\n"
    "v_cmp_eq_u32 vcc, 0, v81\n"
    "s_cmp_eq_u64 vcc, 0\n"
    "s_cbranch_scc1 .Lhp4_x%=\n"
    "s_add_u32 s52, s52, 1\n"
    "s_sleep 2\n"
    "global_load_dword v81, v80, %1 sc1\n"
    "s_cmp_lt_u32 s52, s53\n"
    "s_cbranch_scc1 .Lhp4_%=\n"
    "s_mov_b32 s53, 0\n"
    "v_mov_b32 v82, 0\n"
    "v_mov_b32 v83, 1\n"
    "global_store_dword v82, v83, %1 sc1\n"
    "s_waitcnt vmcnt(0)\n"
    ".Lhp4_x%=:\n"
    "v_readfirstlane_b32 s50, v81\n"
    "v_mbcnt_lo_u32_b32 v80, -1, 0\n"
    "v_mbcnt_hi_u32_b32 v80, -1, v80\n"
    "s_lshl_b32 s51, %2, 6\n"
    "v_add_u32 v80, s51, v80\n"
    "v_min_u32 v80, 129, v80\n"
    "v_lshlrev_b32 v80, 5, v80\n"
    "v_add_u32 v80, 0x26c00, v80\n"
    "ds_read2_b32 v[82:83], v80 offset0:1 offset1:2\n"
    "s_and_b32 s50, s50, 1\n"
    "s_waitcnt lgkmcnt(0)\n"
    "v_and_b32 v81, s50, v82\n"
    "v_lshlrev_b32 v81, 13, v81\n"
    "v_and_b32 v82, -2, v82\n"
    "v_xor_b32 v82, v81, v82\n"
    "v_and_b32 v81, s50, v83\n"
    "v_lshlrev_b32 v81, 13, v81\n"
    "v_and_b32 v83, -2, v83\n"
    "v_xor_b32 v83, v81, v83\n"
    "ds_write2_b32 v80, v82, v83 offset0:1 offset1:2\n"
    "s_waitcnt lgkmcnt(0)\n"
    "s_barrier\n"
    "ds_read_b32 v81, v7 offset:4\n"
    "v_add_u32 v80, %4, v4\n"
    "s_waitcnt lgkmcnt(0)\n"
    "v_readfirstlane_b32 s49, v81\n"
    "s_branch .Lhwait_%=\n"
    ".Lhend_%=:\n"
    "s_cmp_eq_u32 s50, 192\n"
    "s_cbranch_scc0 .Lhpol_%=\n"
    "s_waitcnt vmcnt(0) lgkmcnt(0)\n"
    "s_endpgm\n"
    ".p2align 6\n"
    ".Lrole2_%=:\n"
    "s_mov_b64 s[40:41], %0\n"
    "s_and_b32 s41, s41, 0xffff\n"
    "s_mov_b32 s42, 0xc000000\n"
    "s_mov_b32 s43, 0x20000\n"
    "s_mov_b64 s[92:93], %0\n"
    "s_and_b32 s93, s93, 0xffff\n"
    "s_mov_b32 s94, 0xc000000\n"
    "s_mov_b32 s95, 0x20000\n"
    "s_mov_b32 s53, 131072\n"
    "s_mov_b64 s[56:57], 0\n"
    "v_mbcnt_lo_u32_b32 v7, -1, 0\n"
    "v_mbcnt_hi_u32_b32 v7, -1, v7\n"
    "s_lshl_b32 s50, %2, 8\n"
    "s_add_u32 s50, s50, 147456\n"
    "v_lshl_add_u32 v7, v7, 2, s50\n"
    "ds_read_b32 v1, v7\n"
    "ds_read_b32 v4, v7 offset:1024\n"
    "ds_read_b32 v2, v7 offset:2048\n"
    "ds_read_b32 v8, v7 offset:3072\n"
    "ds_read_b32 v9, v7 offset:4096\n"
    "ds_read_b32 v10, v7 offset:5120\n"
    "ds_read_b32 v11, v7 offset:6144\n"
    "ds_read_b32 v3, v7 offset:7168\n"
    "s_waitcnt lgkmcnt(0)\n"
    "s_barrier\n"
    "s_mul_i32 s54, %2, 6144\n"
    "v_add_u32 v5, 16384, v4\n"
    "s_xor_b32 s50, %2, 0\n"
    "s_mul_i32 s50, s50, 6144\n"
    "v_add_u32 v32, s50, v9\n"
    "v_add_u32 v33, s50, v10\n"
    "v_add_u32 v34, s50, v11\n"
    "s_xor_b32 s50, %2, 1\n"
    "s_mul_i32 s50, s50, 6144\n"
    "v_add_u32 v35, s50, v9\n"
    "v_add_u32 v36, s50, v10\n"
    "v_add_u32 v37, s50, v11\n"
    "s_xor_b32 s50, %2, 2\n"
    "s_mul_i32 s50, s50, 6144\n"
    "v_add_u32 v38, s50, v9\n"
    "v_add_u32 v39, s50, v10\n"
    "v_add_u32 v40, s50, v11\n"
    "s_xor_b32 s50, %2, 3\n"
    "s_mul_i32 s50, s50, 6144\n"
    "v_add_u32 v41, s50, v9\n"
    "v_add_u32 v42, s50, v10\n"
    "v_add_u32 v43, s50, v11\n"
    "s_mov_b64 s[44:45], %8\n"
    "s_and_b32 s45, s45, 0xffff\n"
    "s_mov_b32 s46, 0x480000\n"
    "s_mov_b32 s47, 0x20000\n"
    "s_add_u32 m0, s54, 0\n"
    "s_add_u32 s55, %6, 0\n"
    "buffer_load_dwordx4 v8, s[44:47], s55 offen lds\n"
    "s_add_u32 m0, s54, 1024\n"
    "s_add_u32 s55, %6, 18432\n"
    "buffer_load_dwordx4 v8, s[44:47], s55 offen lds\n"
    "s_add_u32 m0, s54, 2048\n"
    "s_add_u32 s55, %6, 128\n"
    "buffer_load_dwordx4 v8, s[44:47], s55 offen lds\n"
    "s_add_u32 m0, s54, 3072\n"
    "s_add_u32 s55, %6, 18560\n"
    "buffer_load_dwordx4 v8, s[44:47], s55 offen lds\n"
    "s_add_u32 m0, s54, 4096\n"
    "s_add_u32 s55, %6, 256\n"
    "buffer_load_dwordx4 v8, s[44:47], s55 offen lds\n"
    "s_add_u32 m0, s54, 5120\n"
    "s_add_u32 s55, %6, 18688\n"
    "buffer_load_dwordx4 v8, s[44:47], s55 offen lds\n"
    "s_add_u32 m0, s54, 24576\n"
    "s_add_u32 s55, %6, 2304\n"
    "buffer_load_dwordx4 v8, s[44:47], s55 offen lds\n"
    "s_add_u32 m0, s54, 25600\n"
    "s_add_u32 s55, %6, 20736\n"
    "buffer_load_dwordx4 v8, s[44:47], s55 offen lds\n"
    "s_add_u32 m0, s54, 26624\n"
    "s_add_u32 s55, %6, 2432\n"
    "buffer_load_dwordx4 v8, s[44:47], s55 offen lds\n"
    "s_add_u32 m0, s54, 27648\n"
    "s_add_u32 s55, %6, 20864\n"
    "buffer_load_dwordx4 v8, s[44:47], s55 offen lds\n"
    "s_add_u32 m0, s54, 28672\n"
    "s_add_u32 s55, %6, 2560\n"
    "buffer_load_dwordx4 v8, s[44:47], s55 offen lds\n"
    "s_add_u32 m0, s54, 29696\n"
    "s_add_u32 s55, %6, 20992\n"
    "buffer_load_dwordx4 v8, s[44:47], s55 offen lds\n"
    "s_add_u32 m0, s54, 49152\n"
    "s_add_u32 s55, %6, 36864\n"
    "buffer_load_dwordx4 v8, s[44:47], s55 offen lds\n"
    "s_add_u32 m0, s54, 50176\n"
    "s_add_u32 s55, %6, 55296\n"
    "buffer_load_dwordx4 v8, s[44:47], s55 offen lds\n"
    "s_add_u32 m0, s54, 51200\n"
    "s_add_u32 s55, %6, 36992\n"
    "buffer_load_dwordx4 v8, s[44:47], s55 offen lds\n"
    "s_add_u32 m0, s54, 52224\n"
    "s_add_u32 s55, %6, 55424\n"
    "buffer_load_dwordx4 v8, s[44:47], s55 offen lds\n"
    "s_add_u32 m0, s54, 53248\n"
    "s_add_u32 s55, %6, 37120\n"
    "buffer_load_dwordx4 v8, s[44:47], s55 offen lds\n"
    "s_add_u32 m0, s54, 54272\n"
    "s_add_u32 s55, %6, 55552\n"
    "buffer_load_dwordx4 v8, s[44:47], s55 offen lds\n"
    "s_add_u32 m0, s54, 73728\n"
    "s_add_u32 s55, %6, 39168\n"
    "buffer_load_dwordx4 v8, s[44:47], s55 offen lds\n"
    "s_add_u32 m0, s54, 74752\n"
    "s_add_u32 s55, %6, 57600\n"
    "buffer_load_dwordx4 v8, s[44:47], s55 offen lds\n"
    "s_add_u32 m0, s54, 75776\n"
    "s_add_u32 s55, %6, 39296\n"
    "buffer_load_dwordx4 v8, s[44:47], s55 offen lds\n"
    "s_add_u32 m0, s54, 76800\n"
    "s_add_u32 s55, %6, 57728\n"
    "buffer_load_dwordx4 v8, s[44:47], s55 offen lds\n"
    "s_add_u32 m0, s54, 77824\n"
    "s_add_u32 s55, %6, 39424\n"
    "buffer_load_dwordx4 v8, s[44:47], s55 offen lds\n"
    "s_add_u32 m0, s54, 78848\n"
    "s_add_u32 s55, %6, 57856\n"
    "buffer_load_dwordx4 v8, s[44:47], s55 offen lds\n"
    "s_add_u32 m0, s54, 98304\n"
    "s_add_u32 s55, %6, 73728\n"
    "buffer_load_dwordx4 v8, s[44:47], s55 offen lds\n"
    "s_add_u32 m0, s54, 99328\n"
    "s_add_u32 s55, %6, 92160\n"
    "buffer_load_dwordx4 v8, s[44:47], s55 offen lds\n"
    "s_add_u32 m0, s54, 100352\n"
    "s_add_u32 s55, %6, 73856\n"
    "buffer_load_dwordx4 v8, s[44:47], s55 offen lds\n"
    "s_add_u32 m0, s54, 101376\n"
    "s_add_u32 s55, %6, 92288\n"
    "buffer_load_dwordx4 v8, s[44:47], s55 offen lds\n"
    "s_add_u32 m0, s54, 102400\n"
    "s_add_u32 s55, %6, 73984\n"
    "buffer_load_dwordx4 v8, s[44:47], s55 offen lds\n"
    "s_add_u32 m0, s54, 103424\n"
    "s_add_u32 s55, %6, 92416\n"
    "buffer_load_dwordx4 v8, s[44:47], s55 offen lds\n"
    "s_add_u32 m0, s54, 122880\n"
    "s_add_u32 s55, %6, 76032\n"
    "buffer_load_dwordx4 v8, s[44:47], s55 offen lds\n"
    "s_add_u32 m0, s54, 123904\n"
    "s_add_u32 s55, %6, 94464\n"
    "buffer_load_dwordx4 v8, s[44:47], s55 offen lds\n"
    "s_add_u32 m0, s54, 124928\n"
    "s_add_u32 s55, %6, 76160\n"
    "buffer_load_dwordx4 v8, s[44:47], s55 offen lds\n"
    "s_add_u32 m0, s54, 125952\n"
    "s_add_u32 s55, %6, 94592\n"
    "buffer_load_dwordx4 v8, s[44:47], s55 offen lds\n"
    "s_add_u32 m0, s54, 126976\n"
    "s_add_u32 s55, %6, 76288\n"
    "buffer_load_dwordx4 v8, s[44:47], s55 offen lds\n"
    "s_add_u32 m0, s54, 128000\n"
    "s_add_u32 s55, %6, 94720\n"
    "buffer_load_dwordx4 v8, s[44:47], s55 offen lds\n"
    "s_waitcnt vmcnt(30) lgkmcnt(0)\n"
    "s_barrier\n"
    "ds_read_b64_tr_b16 a[0:1], v32\n"
    "ds_read_b64_tr_b16 a[4:5], v33\n"
    "ds_read_b64_tr_b16 a[8:9], v34\n"
    "ds_read_b64_tr_b16 a[96:97], v35\n"
    "ds_read_b64_tr_b16 a[100:101], v36\n"
    "ds_read_b64_tr_b16 a[104:105], v37\n"
    "ds_read_b64_tr_b16 a[192:193], v38\n"
    "ds_read_b64_tr_b16 a[196:197], v39\n"
    "ds_read_b64_tr_b16 a[200:201], v40\n"
    "ds_read_b64_tr_b16 v[160:161], v41\n"
    "ds_read_b64_tr_b16 v[164:165], v42\n"
    "ds_read_b64_tr_b16 v[168:169], v43\n"
    "s_waitcnt vmcnt(24) lgkmcnt(0)\n"
    "s_barrier\n"
    "s_add_u32 m0, s54, 0\n"
    "s_add_u32 s55, %6, 110592\n"
    "buffer_load_dwordx4 v8, s[44:47], s55 offen lds\n"
    "s_add_u32 m0, s54, 1024\n"
    "s_add_u32 s55, %6, 129024\n"
    "buffer_load_dwordx4 v8, s[44:47], s55 offen lds\n"
    "s_add_u32 m0, s54, 2048\n"
    "s_add_u32 s55, %6, 110720\n"
    "buffer_load_dwordx4 v8, s[44:47], s55 offen lds\n"
    "s_add_u32 m0, s54, 3072\n"
    "s_add_u32 s55, %6, 129152\n"
    "buffer_load_dwordx4 v8, s[44:47], s55 offen lds\n"
    "s_add_u32 m0, s54, 4096\n"
    "s_add_u32 s55, %6, 110848\n"
    "buffer_load_dwordx4 v8, s[44:47], s55 offen lds\n"
    "s_add_u32 m0, s54, 5120\n"
    "s_add_u32 s55, %6, 129280\n"
    "buffer_load_dwordx4 v8, s[44:47], s55 offen lds\n"
    "ds_read_b64_tr_b16 a[2:3], v32 offset:24576\n"
    "ds_read_b64_tr_b16 a[6:7], v33 offset:24576\n"
    "ds_read_b64_tr_b16 a[10:11], v34 offset:24576\n"
    "ds_read_b64_tr_b16 a[98:99], v35 offset:24576\n"
    "ds_read_b64_tr_b16 a[102:103], v36 offset:24576\n"
    "ds_read_b64_tr_b16 a[106:107], v37 offset:24576\n"
    "ds_read_b64_tr_b16 a[194:195], v38 offset:24576\n"
    "ds_read_b64_tr_b16 a[198:199], v39 offset:24576\n"
    "ds_read_b64_tr_b16 a[202:203], v40 offset:24576\n"
    "ds_read_b64_tr_b16 v[162:163], v41 offset:24576\n"
    "ds_read_b64_tr_b16 v[166:167], v42 offset:24576\n"
    "ds_read_b64_tr_b16 v[170:171], v43 offset:24576\n"
    "s_waitcnt vmcnt(24) lgkmcnt(0)\n"
    "s_barrier\n"
    "s_add_u32 m0, s54, 24576\n"
    "s_add_u32 s55, %6, 112896\n"
    "buffer_load_dwordx4 v8, s[44:47], s55 offen lds\n"
    "s_add_u32 m0, s54, 25600\n"
    "s_add_u32 s55, %6, 131328\n"
    "buffer_load_dwordx4 v8, s[44:47], s55 offen lds\n"
    "s_add_u32 m0, s54, 26624\n"
    "s_add_u32 s55, %6, 113024\n"
    "buffer_load_dwordx4 v8, s[44:47], s55 offen lds\n"
    "s_add_u32 m0, s54, 27648\n"
    "s_add_u32 s55, %6, 131456\n"
    "buffer_load_dwordx4 v8, s[44:47], s55 offen lds\n"
    "s_add_u32 m0, s54, 28672\n"
    "s_add_u32 s55, %6, 113152\n"
    "buffer_load_dwordx4 v8, s[44:47], s55 offen lds\n"
    "s_add_u32 m0, s54, 29696\n"
    "s_add_u32 s55, %6, 131584\n"
    "buffer_load_dwordx4 v8, s[44:47], s55 offen lds\n"
    "ds_read_b64_tr_b16 a[12:13], v32 offset:49152\n"
    "ds_read_b64_tr_b16 a[16:17], v33 offset:49152\n"
    "ds_read_b64_tr_b16 a[20:21], v34 offset:49152\n"
    "ds_read_b64_tr_b16 a[108:109], v35 offset:49152\n"
    "ds_read_b64_tr_b16 a[112:113], v36 offset:49152\n"
    "ds_read_b64_tr_b16 a[116:117], v37 offset:49152\n"
    "ds_read_b64_tr_b16 a[204:205], v38 offset:49152\n"
    "ds_read_b64_tr_b16 a[208:209], v39 offset:49152\n"
    "ds_read_b64_tr_b16 a[212:213], v40 offset:49152\n"
    "ds_read_b64_tr_b16 v[172:173], v41 offset:49152\n"
    "ds_read_b64_tr_b16 v[176:177], v42 offset:49152\n"
    "ds_read_b64_tr_b16 v[180:181], v43 offset:49152\n"
    "s_waitcnt vmcnt(24) lgkmcnt(0)\n"
    "s_barrier\n"
    "s_add_u32 m0, s54, 49152\n"
    "s_add_u32 s55, %6, 147456\n"
    "buffer_load_dwordx4 v8, s[44:47], s55 offen lds\n"
    "s_add_u32 m0, s54, 50176\n"
    "s_add_u32 s55, %6, 165888\n"
    "buffer_load_dwordx4 v8, s[44:47], s55 offen lds\n"
    "s_add_u32 m0, s54, 51200\n"
    "s_add_u32 s55, %6, 147584\n"
    "buffer_load_dwordx4 v8, s[44:47], s55 offen lds\n"
    "s_add_u32 m0, s54, 52224\n"
    "s_add_u32 s55, %6, 166016\n"
    "buffer_load_dwordx4 v8, s[44:47], s55 offen lds\n"
    "s_add_u32 m0, s54, 53248\n"
    "s_add_u32 s55, %6, 147712\n"
    "buffer_load_dwordx4 v8, s[44:47], s55 offen lds\n"
    "s_add_u32 m0, s54, 54272\n"
    "s_add_u32 s55, %6, 166144\n"
    "buffer_load_dwordx4 v8, s[44:47], s55 offen lds\n"
    "v_add_u32 v71, 0x10000, v32\n"
    "ds_read_b64_tr_b16 a[14:15], v71 offset:8192\n"
    "v_add_u32 v70, 0x10000, v33\n"
    "ds_read_b64_tr_b16 a[18:19], v70 offset:8192\n"
    "v_add_u32 v69, 0x10000, v34\n"
    "ds_read_b64_tr_b16 a[22:23], v69 offset:8192\n"
    "v_add_u32 v68, 0x10000, v35\n"
    "ds_read_b64_tr_b16 a[110:111], v68 offset:8192\n"
    "v_add_u32 v67, 0x10000, v36\n"
    "ds_read_b64_tr_b16 a[114:115], v67 offset:8192\n"
    "v_add_u32 v66, 0x10000, v37\n"
    "ds_read_b64_tr_b16 a[118:119], v66 offset:8192\n"
    "v_add_u32 v65, 0x10000, v38\n"
    "ds_read_b64_tr_b16 a[206:207], v65 offset:8192\n"
    "v_add_u32 v64, 0x10000, v39\n"
    "ds_read_b64_tr_b16 a[210:211], v64 offset:8192\n"
    "v_add_u32 v63, 0x10000, v40\n"
    "ds_read_b64_tr_b16 a[214:215], v63 offset:8192\n"
    "v_add_u32 v62, 0x10000, v41\n"
    "ds_read_b64_tr_b16 v[174:175], v62 offset:8192\n"
    "v_add_u32 v61, 0x10000, v42\n"
    "ds_read_b64_tr_b16 v[178:179], v61 offset:8192\n"
    "v_add_u32 v60, 0x10000, v43\n"
    "ds_read_b64_tr_b16 v[182:183], v60 offset:8192\n"
    "s_waitcnt vmcnt(24) lgkmcnt(0)\n"
    "s_barrier\n"
    "s_add_u32 m0, s54, 73728\n"
    "s_add_u32 s55, %6, 149760\n"
    "buffer_load_dwordx4 v8, s[44:47], s55 offen lds\n"
    "s_add_u32 m0, s54, 74752\n"
    "s_add_u32 s55, %6, 168192\n"
    "buffer_load_dwordx4 v8, s[44:47], s55 offen lds\n"
    "s_add_u32 m0, s54, 75776\n"
    "s_add_u32 s55, %6, 149888\n"
    "buffer_load_dwordx4 v8, s[44:47], s55 offen lds\n"
    "s_add_u32 m0, s54, 76800\n"
    "s_add_u32 s55, %6, 168320\n"
    "buffer_load_dwordx4 v8, s[44:47], s55 offen lds\n"
    "s_add_u32 m0, s54, 77824\n"
    "s_add_u32 s55, %6, 150016\n"
    "buffer_load_dwordx4 v8, s[44:47], s55 offen lds\n"
    "s_add_u32 m0, s54, 78848\n"
    "s_add_u32 s55, %6, 168448\n"
    "buffer_load_dwordx4 v8, s[44:47], s55 offen lds\n"
    "ds_read_b64_tr_b16 a[24:25], v71 offset:32768\n"
    "ds_read_b64_tr_b16 a[28:29], v70 offset:32768\n"
    "ds_read_b64_tr_b16 a[32:33], v69 offset:32768\n"
    "ds_read_b64_tr_b16 a[120:121], v68 offset:32768\n"
    "ds_read_b64_tr_b16 a[124:125], v67 offset:32768\n"
    "ds_read_b64_tr_b16 a[128:129], v66 offset:32768\n"
    "ds_read_b64_tr_b16 a[216:217], v65 offset:32768\n"
    "ds_read_b64_tr_b16 a[220:221], v64 offset:32768\n"
    "ds_read_b64_tr_b16 a[224:225], v63 offset:32768\n"
    "ds_read_b64_tr_b16 v[184:185], v62 offset:32768\n"
    "ds_read_b64_tr_b16 v[188:189], v61 offset:32768\n"
    "ds_read_b64_tr_b16 v[192:193], v60 offset:32768\n"
    "s_waitcnt vmcnt(24) lgkmcnt(0)\n"
    "s_barrier\n"
    "s_add_u32 m0, s54, 98304\n"
    "s_add_u32 s55, %6, 184320\n"
    "buffer_load_dwordx4 v8, s[44:47], s55 offen lds\n"
    "s_add_u32 m0, s54, 99328\n"
    "s_add_u32 s55, %6, 202752\n"
    "buffer_load_dwordx4 v8, s[44:47], s55 offen lds\n"
    "s_add_u32 m0, s54, 100352\n"
    "s_add_u32 s55, %6, 184448\n"
    "buffer_load_dwordx4 v8, s[44:47], s55 offen lds\n"
    "s_add_u32 m0, s54, 101376\n"
    "s_add_u32 s55, %6, 202880\n"
    "buffer_load_dwordx4 v8, s[44:47], s55 offen lds\n"
    "s_add_u32 m0, s54, 102400\n"
    "s_add_u32 s55, %6, 184576\n"
    "buffer_load_dwordx4 v8, s[44:47], s55 offen lds\n"
    "s_add_u32 m0, s54, 103424\n"
    "s_add_u32 s55, %6, 203008\n"
    "buffer_load_dwordx4 v8, s[44:47], s55 offen lds\n"
    "ds_read_b64_tr_b16 a[26:27], v71 offset:57344\n"
    "ds_read_b64_tr_b16 a[30:31], v70 offset:57344\n"
    "ds_read_b64_tr_b16 a[34:35], v69 offset:57344\n"
    "ds_read_b64_tr_b16 a[122:123], v68 offset:57344\n"
    "ds_read_b64_tr_b16 a[126:127], v67 offset:57344\n"
    "ds_read_b64_tr_b16 a[130:131], v66 offset:57344\n"
    "ds_read_b64_tr_b16 a[218:219], v65 offset:57344\n"
    "ds_read_b64_tr_b16 a[222:223], v64 offset:57344\n"
    "ds_read_b64_tr_b16 a[226:227], v63 offset:57344\n"
    "ds_read_b64_tr_b16 v[186:187], v62 offset:57344\n"
    "ds_read_b64_tr_b16 v[190:191], v61 offset:57344\n"
    "ds_read_b64_tr_b16 v[194:195], v60 offset:57344\n"
    "s_waitcnt vmcnt(24) lgkmcnt(0)\n"
    "s_barrier\n"
    "s_add_u32 m0, s54, 122880\n"
    "s_add_u32 s55, %6, 186624\n"
    "buffer_load_dwordx4 v8, s[44:47], s55 offen lds\n"
    "s_add_u32 m0, s54, 123904\n"
    "s_add_u32 s55, %6, 205056\n"
    "buffer_load_dwordx4 v8, s[44:47], s55 offen lds\n"
    "s_add_u32 m0, s54, 124928\n"
    "s_add_u32 s55, %6, 186752\n"
    "buffer_load_dwordx4 v8, s[44:47], s55 offen lds\n"
    "s_add_u32 m0, s54, 125952\n"
    "s_add_u32 s55, %6, 205184\n"
    "buffer_load_dwordx4 v8, s[44:47], s55 offen lds\n"
    "s_add_u32 m0, s54, 126976\n"
    "s_add_u32 s55, %6, 186880\n"
    "buffer_load_dwordx4 v8, s[44:47], s55 offen lds\n"
    "s_add_u32 m0, s54, 128000\n"
    "s_add_u32 s55, %6, 205312\n"
    "buffer_load_dwordx4 v8, s[44:47], s55 offen lds\n"
    "ds_read_b64_tr_b16 a[36:37], v32\n"
    "ds_read_b64_tr_b16 a[40:41], v33\n"
    "ds_read_b64_tr_b16 a[44:45], v34\n"
    "ds_read_b64_tr_b16 a[132:133], v35\n"
    "ds_read_b64_tr_b16 a[136:137], v36\n"
    "ds_read_b64_tr_b16 a[140:141], v37\n"
    "ds_read_b64_tr_b16 a[228:229], v38\n"
    "ds_read_b64_tr_b16 a[232:233], v39\n"
    "ds_read_b64_tr_b16 a[236:237], v40\n"
    "ds_read_b64_tr_b16 v[196:197], v41\n"
    "ds_read_b64_tr_b16 v[200:201], v42\n"
    "ds_read_b64_tr_b16 v[204:205], v43\n"
    "s_waitcnt vmcnt(24) lgkmcnt(0)\n"
    "s_barrier\n"
    "s_add_u32 m0, s54, 0\n"
    "s_add_u32 s55, %6, 221184\n"
    "buffer_load_dwordx4 v8, s[44:47], s55 offen lds\n"
    "s_add_u32 m0, s54, 1024\n"
    "s_add_u32 s55, %6, 239616\n"
    "buffer_load_dwordx4 v8, s[44:47], s55 offen lds\n"
    "s_add_u32 m0, s54, 2048\n"
    "s_add_u32 s55, %6, 221312\n"
    "buffer_load_dwordx4 v8, s[44:47], s55 offen lds\n"
    "s_add_u32 m0, s54, 3072\n"
    "s_add_u32 s55, %6, 239744\n"
    "buffer_load_dwordx4 v8, s[44:47], s55 offen lds\n"
    "s_add_u32 m0, s54, 4096\n"
    "s_add_u32 s55, %6, 221440\n"
    "buffer_load_dwordx4 v8, s[44:47], s55 offen lds\n"
    "s_add_u32 m0, s54, 5120\n"
    "s_add_u32 s55, %6, 239872\n"
    "buffer_load_dwordx4 v8, s[44:47], s55 offen lds\n"
    "ds_read_b64_tr_b16 a[38:39], v32 offset:24576\n"
    "ds_read_b64_tr_b16 a[42:43], v33 offset:24576\n"
    "ds_read_b64_tr_b16 a[46:47], v34 offset:24576\n"
    "ds_read_b64_tr_b16 a[134:135], v35 offset:24576\n"
    "ds_read_b64_tr_b16 a[138:139], v36 offset:24576\n"
    "ds_read_b64_tr_b16 a[142:143], v37 offset:24576\n"
    "ds_read_b64_tr_b16 a[230:231], v38 offset:24576\n"
    "ds_read_b64_tr_b16 a[234:235], v39 offset:24576\n"
    "ds_read_b64_tr_b16 a[238:239], v40 offset:24576\n"
    "ds_read_b64_tr_b16 v[198:199], v41 offset:24576\n"
    "ds_read_b64_tr_b16 v[202:203], v42 offset:24576\n"
    "ds_read_b64_tr_b16 v[206:207], v43 offset:24576\n"
    "s_waitcnt vmcnt(24) lgkmcnt(0)\n"
    "s_barrier\n"
    "s_add_u32 m0, s54, 24576\n"
    "s_add_u32 s55, %6, 223488\n"
    "buffer_load_dwordx4 v8, s[44:47], s55 offen lds\n"
    "s_add_u32 m0, s54, 25600\n"
    "s_add_u32 s55, %6, 241920\n"
    "buffer_load_dwordx4 v8, s[44:47], s55 offen lds\n"
    "s_add_u32 m0, s54, 26624\n"
    "s_add_u32 s55, %6, 223616\n"
    "buffer_load_dwordx4 v8, s[44:47], s55 offen lds\n"
    "s_add_u32 m0, s54, 27648\n"
    "s_add_u32 s55, %6, 242048\n"
    "buffer_load_dwordx4 v8, s[44:47], s55 offen lds\n"
    "s_add_u32 m0, s54, 28672\n"
    "s_add_u32 s55, %6, 223744\n"
    "buffer_load_dwordx4 v8, s[44:47], s55 offen lds\n"
    "s_add_u32 m0, s54, 29696\n"
    "s_add_u32 s55, %6, 242176\n"
    "buffer_load_dwordx4 v8, s[44:47], s55 offen lds\n"
    "ds_read_b64_tr_b16 a[48:49], v32 offset:49152\n"
    "ds_read_b64_tr_b16 a[52:53], v33 offset:49152\n"
    "ds_read_b64_tr_b16 a[56:57], v34 offset:49152\n"
    "ds_read_b64_tr_b16 a[144:145], v35 offset:49152\n"
    "ds_read_b64_tr_b16 a[148:149], v36 offset:49152\n"
    "ds_read_b64_tr_b16 a[152:153], v37 offset:49152\n"
    "ds_read_b64_tr_b16 a[240:241], v38 offset:49152\n"
    "ds_read_b64_tr_b16 a[244:245], v39 offset:49152\n"
    "ds_read_b64_tr_b16 a[248:249], v40 offset:49152\n"
    "ds_read_b64_tr_b16 v[208:209], v41 offset:49152\n"
    "ds_read_b64_tr_b16 v[212:213], v42 offset:49152\n"
    "ds_read_b64_tr_b16 v[216:217], v43 offset:49152\n"
    "s_waitcnt vmcnt(24) lgkmcnt(0)\n"
    "s_barrier\n"
    "s_add_u32 m0, s54, 49152\n"
    "s_add_u32 s55, %6, 258048\n"
    "buffer_load_dwordx4 v8, s[44:47], s55 offen lds\n"
    "s_add_u32 m0, s54, 50176\n"
    "s_add_u32 s55, %6, 276480\n"
    "buffer_load_dwordx4 v8, s[44:47], s55 offen lds\n"
    "s_add_u32 m0, s54, 51200\n"
    "s_add_u32 s55, %6, 258176\n"
    "buffer_load_dwordx4 v8, s[44:47], s55 offen lds\n"
    "s_add_u32 m0, s54, 52224\n"
    "s_add_u32 s55, %6, 276608\n"
    "buffer_load_dwordx4 v8, s[44:47], s55 offen lds\n"
    "s_add_u32 m0, s54, 53248\n"
    "s_add_u32 s55, %6, 258304\n"
    "buffer_load_dwordx4 v8, s[44:47], s55 offen lds\n"
    "s_add_u32 m0, s54, 54272\n"
    "s_add_u32 s55, %6, 276736\n"
    "buffer_load_dwordx4 v8, s[44:47], s55 offen lds\n"
    "ds_read_b64_tr_b16 a[50:51], v71 offset:8192\n"
    "ds_read_b64_tr_b16 a[54:55], v70 offset:8192\n"
    "ds_read_b64_tr_b16 a[58:59], v69 offset:8192\n"
    "ds_read_b64_tr_b16 a[146:147], v68 offset:8192\n"
    "ds_read_b64_tr_b16 a[150:151], v67 offset:8192\n"
    "ds_read_b64_tr_b16 a[154:155], v66 offset:8192\n"
    "ds_read_b64_tr_b16 a[242:243], v65 offset:8192\n"
    "ds_read_b64_tr_b16 a[246:247], v64 offset:8192\n"
    "ds_read_b64_tr_b16 a[250:251], v63 offset:8192\n"
    "ds_read_b64_tr_b16 v[210:211], v62 offset:8192\n"
    "ds_read_b64_tr_b16 v[214:215], v61 offset:8192\n"
    "ds_read_b64_tr_b16 v[218:219], v60 offset:8192\n"
    "s_waitcnt vmcnt(24) lgkmcnt(0)\n"
    "s_barrier\n"
    "s_add_u32 m0, s54, 73728\n"
    "s_add_u32 s55, %6, 260352\n"
    "buffer_load_dwordx4 v8, s[44:47], s55 offen lds\n"
    "s_add_u32 m0, s54, 74752\n"
    "s_add_u32 s55, %6, 278784\n"
    "buffer_load_dwordx4 v8, s[44:47], s55 offen lds\n"
    "s_add_u32 m0, s54, 75776\n"
    "s_add_u32 s55, %6, 260480\n"
    "buffer_load_dwordx4 v8, s[44:47], s55 offen lds\n"
    "s_add_u32 m0, s54, 76800\n"
    "s_add_u32 s55, %6, 278912\n"
    "buffer_load_dwordx4 v8, s[44:47], s55 offen lds\n"
    "s_add_u32 m0, s54, 77824\n"
    "s_add_u32 s55, %6, 260608\n"
    "buffer_load_dwordx4 v8, s[44:47], s55 offen lds\n"
    "s_add_u32 m0, s54, 78848\n"
    "s_add_u32 s55, %6, 279040\n"
    "buffer_load_dwordx4 v8, s[44:47], s55 offen lds\n"
    "v_mov_b32 v7, 0x20400\n"
    "global_load_dwordx2 v[20:21], v7, %1 sc1\n"
    "v_mbcnt_lo_u32_b32 v6, -1, 0\n"
    "v_min_u32 v6, 7, v6\n"
    "v_lshlrev_b32 v6, 7, v6\n"
    "v_add_u32 v6, 0x20000, v6\n"
    "global_load_dwordx2 v[22:23], v6, %1 sc1\n"
    "ds_read_b64_tr_b16 a[60:61], v71 offset:32768\n"
    "ds_read_b64_tr_b16 a[64:65], v70 offset:32768\n"
    "ds_read_b64_tr_b16 a[68:69], v69 offset:32768\n"
    "ds_read_b64_tr_b16 a[156:157], v68 offset:32768\n"
    "ds_read_b64_tr_b16 a[160:161], v67 offset:32768\n"
    "ds_read_b64_tr_b16 a[164:165], v66 offset:32768\n"
    "ds_read_b64_tr_b16 a[252:253], v65 offset:32768\n"
    "ds_read_b64_tr_b16 v[128:129], v64 offset:32768\n"
    "ds_read_b64_tr_b16 v[132:133], v63 offset:32768\n"
    "ds_read_b64_tr_b16 v[220:221], v62 offset:32768\n"
    "ds_read_b64_tr_b16 v[224:225], v61 offset:32768\n"
    "ds_read_b64_tr_b16 v[228:229], v60 offset:32768\n"
    "s_waitcnt vmcnt(26) lgkmcnt(0)\n"
    "s_barrier\n"
    "ds_read_b64_tr_b16 a[62:63], v71 offset:57344\n"
    "ds_read_b64_tr_b16 a[66:67], v70 offset:57344\n"
    "ds_read_b64_tr_b16 a[70:71], v69 offset:57344\n"
    "ds_read_b64_tr_b16 a[158:159], v68 offset:57344\n"
    "ds_read_b64_tr_b16 a[162:163], v67 offset:57344\n"
    "ds_read_b64_tr_b16 a[166:167], v66 offset:57344\n"
    "ds_read_b64_tr_b16 a[254:255], v65 offset:57344\n"
    "ds_read_b64_tr_b16 v[130:131], v64 offset:57344\n"
    "ds_read_b64_tr_b16 v[134:135], v63 offset:57344\n"
    "ds_read_b64_tr_b16 v[222:223], v62 offset:57344\n"
    "ds_read_b64_tr_b16 v[226:227], v61 offset:57344\n"
    "ds_read_b64_tr_b16 v[230:231], v60 offset:57344\n"
    "s_waitcnt vmcnt(20) lgkmcnt(0)\n"
    "s_barrier\n"
    "ds_read_b64_tr_b16 a[72:73], v32\n"
    "ds_read_b64_tr_b16 a[76:77], v33\n"
    "ds_read_b64_tr_b16 a[80:81], v34\n"
    "ds_read_b64_tr_b16 a[168:169], v35\n"
    "ds_read_b64_tr_b16 a[172:173], v36\n"
    "ds_read_b64_tr_b16 a[176:177], v37\n"
    "ds_read_b64_tr_b16 v[136:137], v38\n"
    "ds_read_b64_tr_b16 v[140:141], v39\n"
    "ds_read_b64_tr_b16 v[144:145], v40\n"
    "ds_read_b64_tr_b16 v[232:233], v41\n"
    "ds_read_b64_tr_b16 v[236:237], v42\n"
    "ds_read_b64_tr_b16 v[240:241], v43\n"
    "s_waitcnt vmcnt(14) lgkmcnt(0)\n"
    "s_barrier\n"
    "ds_read_b64_tr_b16 a[74:75], v32 offset:24576\n"
    "ds_read_b64_tr_b16 a[78:79], v33 offset:24576\n"
    "ds_read_b64_tr_b16 a[82:83], v34 offset:24576\n"
    "ds_read_b64_tr_b16 a[170:171], v35 offset:24576\n"
    "ds_read_b64_tr_b16 a[174:175], v36 offset:24576\n"
    "ds_read_b64_tr_b16 a[178:179], v37 offset:24576\n"
    "ds_read_b64_tr_b16 v[138:139], v38 offset:24576\n"
    "ds_read_b64_tr_b16 v[142:143], v39 offset:24576\n"
    "ds_read_b64_tr_b16 v[146:147], v40 offset:24576\n"
    "ds_read_b64_tr_b16 v[234:235], v41 offset:24576\n"
    "ds_read_b64_tr_b16 v[238:239], v42 offset:24576\n"
    "ds_read_b64_tr_b16 v[242:243], v43 offset:24576\n"
    "s_waitcnt vmcnt(8) lgkmcnt(0)\n"
    "s_barrier\n"
    "ds_read_b64_tr_b16 a[84:85], v32 offset:49152\n"
    "ds_read_b64_tr_b16 a[88:89], v33 offset:49152\n"
    "ds_read_b64_tr_b16 a[92:93], v34 offset:49152\n"
    "ds_read_b64_tr_b16 a[180:181], v35 offset:49152\n"
    "ds_read_b64_tr_b16 a[184:185], v36 offset:49152\n"
    "ds_read_b64_tr_b16 a[188:189], v37 offset:49152\n"
    "ds_read_b64_tr_b16 v[148:149], v38 offset:49152\n"
    "ds_read_b64_tr_b16 v[152:153], v39 offset:49152\n"
    "ds_read_b64_tr_b16 v[156:157], v40 offset:49152\n"
    "ds_read_b64_tr_b16 v[244:245], v41 offset:49152\n"
    "ds_read_b64_tr_b16 v[248:249], v42 offset:49152\n"
    "ds_read_b64_tr_b16 v[252:253], v43 offset:49152\n"
    "s_waitcnt vmcnt(2) lgkmcnt(0)\n"
    "s_barrier\n"
    "ds_read_b64_tr_b16 a[86:87], v71 offset:8192\n"
    "ds_read_b64_tr_b16 a[90:91], v70 offset:8192\n"
    "ds_read_b64_tr_b16 a[94:95], v69 offset:8192\n"
    "ds_read_b64_tr_b16 a[182:183], v68 offset:8192\n"
    "ds_read_b64_tr_b16 a[186:187], v67 offset:8192\n"
    "ds_read_b64_tr_b16 a[190:191], v66 offset:8192\n"
    "ds_read_b64_tr_b16 v[150:151], v65 offset:8192\n"
    "ds_read_b64_tr_b16 v[154:155], v64 offset:8192\n"
    "ds_read_b64_tr_b16 v[158:159], v63 offset:8192\n"
    "ds_read_b64_tr_b16 v[246:247], v62 offset:8192\n"
    "ds_read_b64_tr_b16 v[250:251], v61 offset:8192\n"
    "ds_read_b64_tr_b16 v[254:255], v60 offset:8192\n"
    "s_waitcnt lgkmcnt(0)\n"
    "s_barrier\n"
    "s_waitcnt vmcnt(0)\n"
    "v_add_u32 v20, 1, v20\n"
    "v_add_u32 v21, 1, v21\n"
    "s_mov_b32 s52, 1048576\n"
    ".Lwgo_a%=:\n"
    "v_cmp_ne_u64 vcc, v[20:21], v[22:23]\n"
    "s_cbranch_vccz .Lwgo_x%=\n"
    "global_load_dwordx2 v[22:23], v6, %1 sc1\n"
    "s_sub_u32 s52, s52, 1\n"
    "s_waitcnt vmcnt(0)\n"
    "s_cbranch_scc0 .Lwgo_a%=\n"
    ".Lwgo_x%=:\n"
    "v_mbcnt_lo_u32_b32 v20, -1, 0\n"
    "v_mbcnt_hi_u32_b32 v20, -1, v20\n"
    "v_min_u32 v6, 7, v20\n"
    "v_lshlrev_b32 v6, 7, v6\n"
    "v_mov_b32 v7, 0x26c00\n"
    "ds_read_b128 v[20:23], v7\n"
    "s_waitcnt lgkmcnt(0)\n"
    "v_readfirstlane_b32 s49, v21\n"
    "v_add_u32 v20, v20, v6\n"
    "s_branch .Lwchk_%=\n"
    ".Lwgo_%=:\n"
    "s_waitcnt vmcnt(1)\n"
    "v_mfma_f32_16x16x32_bf16 v[8:11], a[0:3], v[64:67], 0\n"
    "v_add_u32 v7, 32, v7\n"
    "ds_read_b128 v[20:23], v7\n"
    "v_mfma_f32_16x16x32_bf16 v[12:15], a[4:7], v[64:67], 0\n"
    "ds_write_b128 v1, v[64:67]\n"
    "ds_write_b128 v1, v[68:71] offset:1024\n"
    "v_mfma_f32_16x16x32_bf16 v[8:11], a[12:15], v[68:71], v[8:11]\n"
    "ds_write_b128 v1, v[72:75] offset:2048\n"
    "ds_write_b128 v1, v[76:79] offset:3072\n"
    "v_mfma_f32_16x16x32_bf16 v[12:15], a[16:19], v[68:71], v[12:15]\n"
    "v_cvt_pk_bf16_f32 v28, v16, v17\n"
    "v_cvt_pk_bf16_f32 v30, v16, v17\n"
    "v_mfma_f32_16x16x32_bf16 v[8:11], a[24:27], v[72:75], v[8:11]\n"
    "v_cvt_pk_bf16_f32 v29, v18, v19\n"
    "v_cvt_pk_bf16_f32 v31, v18, v19\n"
    "v_mfma_f32_16x16x32_bf16 v[12:15], a[28:31], v[72:75], v[12:15]\n"
    "ds_write_b128 v1, v[80:83] offset:4096\n"
    "ds_write_b128 v1, v[84:87] offset:5120\n"
    "v_mfma_f32_16x16x32_bf16 v[8:11], a[36:39], v[76:79], v[8:11]\n"
    "v_permlane16_swap_b32 v28, v30\n"
    "v_permlane16_swap_b32 v29, v31\n"
    "v_mfma_f32_16x16x32_bf16 v[12:15], a[40:43], v[76:79], v[12:15]\n"
    "ds_write_b128 v1, v[88:91] offset:6144\n"
    "ds_write_b128 v1, v[92:95] offset:7168\n"
    "v_mfma_f32_16x16x32_bf16 v[8:11], a[48:51], v[80:83], v[8:11]\n"
    "buffer_store_dwordx4 v[28:31], v3, s[40:43], s62 offen sc1\n"
    "v_xor_b32 v0, 0x2000, v1\n"
    "v_mfma_f32_16x16x32_bf16 v[12:15], a[52:55], v[80:83], v[12:15]\n"
    "s_waitcnt lgkmcnt(8)\n"
    "v_readfirstlane_b32 s49, v21\n"
    "v_mfma_f32_16x16x32_bf16 v[8:11], a[60:63], v[84:87], v[8:11]\n"
    "v_readfirstlane_b32 s62, v22\n"
    "v_add_u32 v20, v20, v6\n"
    "v_mfma_f32_16x16x32_bf16 v[12:15], a[64:67], v[84:87], v[12:15]\n"
    ".Lwtop_%=:\n"
    "v_mfma_f32_16x16x32_bf16 v[16:19], a[8:11], v[64:67], 0\n"
    "s_waitcnt lgkmcnt(0)\n"
    "s_barrier\n"
    "v_mfma_f32_16x16x32_bf16 v[16:19], a[20:23], v[68:71], v[16:19]\n"
    "ds_read_b128 v[32:35], v0\n"
    "ds_read_b128 v[36:39], v0 offset:1024\n"
    "v_mfma_f32_16x16x32_bf16 v[16:19], a[32:35], v[72:75], v[16:19]\n"
    "ds_read_b128 v[44:47], v0 offset:3072\n"
    "ds_read_b128 v[40:43], v0 offset:2048\n"
    "v_mfma_f32_16x16x32_bf16 v[16:19], a[44:47], v[76:79], v[16:19]\n"
    "ds_read_b128 v[52:55], v0 offset:5120\n"
    "ds_read_b128 v[48:51], v0 offset:4096\n"
    "v_mfma_f32_16x16x32_bf16 v[16:19], a[56:59], v[80:83], v[16:19]\n"
    "ds_read_b128 v[60:63], v0 offset:7168\n"
    "ds_read_b128 v[56:59], v0 offset:6144\n"
    "v_mfma_f32_16x16x32_bf16 v[16:19], a[68:71], v[84:87], v[16:19]\n"
    "global_load_dword v21, v20, %1 sc1\n"
    "v_mfma_f32_16x16x32_bf16 v[8:11], a[72:75], v[88:91], v[8:11]\n"
    "v_mfma_f32_16x16x32_bf16 v[12:15], a[76:79], v[88:91], v[12:15]\n"
    "v_mfma_f32_16x16x32_bf16 v[16:19], a[80:83], v[88:91], v[16:19]\n"
    "v_mfma_f32_16x16x32_bf16 v[8:11], a[84:87], v[92:95], v[8:11]\n"
    "v_mfma_f32_16x16x32_bf16 v[12:15], a[88:91], v[92:95], v[12:15]\n"
    "v_mfma_f32_16x16x32_bf16 v[16:19], a[92:95], v[92:95], v[16:19]\n"
    "s_waitcnt lgkmcnt(7)\n"
    "v_mfma_f32_16x16x32_bf16 v[8:11], a[96:99], v[32:35], v[8:11]\n"
    "v_mfma_f32_16x16x32_bf16 v[12:15], a[100:103], v[32:35], v[12:15]\n"
    "v_mfma_f32_16x16x32_bf16 v[16:19], a[104:107], v[32:35], v[16:19]\n"
    "s_waitcnt lgkmcnt(6)\n"
    "v_mfma_f32_16x16x32_bf16 v[8:11], a[108:111], v[36:39], v[8:11]\n"
    "v_mfma_f32_16x16x32_bf16 v[12:15], a[112:115], v[36:39], v[12:15]\n"
    "v_xor_b32 v0, 0x4000, v1\n"
    "v_mfma_f32_16x16x32_bf16 v[16:19], a[116:119], v[36:39], v[16:19]\n"
    "s_waitcnt lgkmcnt(4)\n"
    "v_mfma_f32_16x16x32_bf16 v[8:11], a[120:123], v[40:43], v[8:11]\n"
    "ds_read_b128 v[36:39], v0 offset:1024\n"
    "ds_read_b128 v[32:35], v0\n"
    "v_mfma_f32_16x16x32_bf16 v[12:15], a[124:127], v[40:43], v[12:15]\n"
    "v_mfma_f32_16x16x32_bf16 v[16:19], a[128:131], v[40:43], v[16:19]\n"
    "v_mfma_f32_16x16x32_bf16 v[8:11], a[132:135], v[44:47], v[8:11]\n"
    "v_mfma_f32_16x16x32_bf16 v[12:15], a[136:139], v[44:47], v[12:15]\n"
    "v_mfma_f32_16x16x32_bf16 v[16:19], a[140:143], v[44:47], v[16:19]\n"
    "s_waitcnt lgkmcnt(4)\n"
    "v_mfma_f32_16x16x32_bf16 v[8:11], a[144:147], v[48:51], v[8:11]\n"
    "ds_read_b128 v[44:47], v0 offset:3072\n"
    "ds_read_b128 v[40:43], v0 offset:2048\n"
    "v_mfma_f32_16x16x32_bf16 v[12:15], a[148:151], v[48:51], v[12:15]\n"
    "v_mfma_f32_16x16x32_bf16 v[16:19], a[152:155], v[48:51], v[16:19]\n"
    "v_mfma_f32_16x16x32_bf16 v[8:11], a[156:159], v[52:55], v[8:11]\n"
    "v_mfma_f32_16x16x32_bf16 v[12:15], a[160:163], v[52:55], v[12:15]\n"
    "v_mfma_f32_16x16x32_bf16 v[16:19], a[164:167], v[52:55], v[16:19]\n"
    "s_waitcnt lgkmcnt(4)\n"
    "v_mfma_f32_16x16x32_bf16 v[8:11], a[168:171], v[56:59], v[8:11]\n"
    "ds_read_b128 v[52:55], v0 offset:5120\n"
    "ds_read_b128 v[48:51], v0 offset:4096\n"
    "v_mfma_f32_16x16x32_bf16 v[12:15], a[172:175], v[56:59], v[12:15]\n"
    "v_mfma_f32_16x16x32_bf16 v[16:19], a[176:179], v[56:59], v[16:19]\n"
    "s_waitcnt vmcnt(0)\n"
    "v_mov_b32 v22, 1\n"
    "v_mfma_f32_16x16x32_bf16 v[8:11], a[180:183], v[60:63], v[8:11]\n"
    "s_mov_b64 exec, s[56:57]\n"
    "ds_add_rtn_u32 v22, v7, v22 offset:16\n"
    "s_mov_b64 exec, -1\n"
    "v_mfma_f32_16x16x32_bf16 v[12:15], a[184:187], v[60:63], v[12:15]\n"
    "v_cmp_eq_u32 vcc, 0, v21\n"
    "s_cmp_eq_u64 vcc, 0\n"
    "v_mfma_f32_16x16x32_bf16 v[16:19], a[188:191], v[60:63], v[16:19]\n"
    "s_waitcnt lgkmcnt(4)\n"
    "v_mfma_f32_16x16x32_bf16 v[8:11], a[192:195], v[32:35], v[8:11]\n"
    "ds_read_b128 v[60:63], v0 offset:7168\n"
    "ds_read_b128 v[56:59], v0 offset:6144\n"
    "v_mfma_f32_16x16x32_bf16 v[12:15], a[196:199], v[32:35], v[12:15]\n"
    "s_cselect_b32 s94, s42, 0\n"
    "v_mfma_f32_16x16x32_bf16 v[16:19], a[200:203], v[32:35], v[16:19]\n"
    "global_load_dword v21, v20, %1 sc1\n"
    "v_mfma_f32_16x16x32_bf16 v[8:11], a[204:207], v[36:39], v[8:11]\n"
    "buffer_load_dwordx4 v[64:67], v4, s[92:95], s49 offen sc1\n"
    "v_mfma_f32_16x16x32_bf16 v[12:15], a[208:211], v[36:39], v[12:15]\n"
    "v_xor_b32 v0, 0x6000, v1\n"
    "buffer_load_dwordx4 v[68:71], v4, s[92:95], s49 offen offset:1024 sc1\n"
    "v_mfma_f32_16x16x32_bf16 v[16:19], a[212:215], v[36:39], v[16:19]\n"
    "s_waitcnt lgkmcnt(4)\n"
    "v_mfma_f32_16x16x32_bf16 v[8:11], a[216:219], v[40:43], v[8:11]\n"
    "ds_read_b128 v[36:39], v0 offset:1024\n"
    "ds_read_b128 v[32:35], v0\n"
    "v_mfma_f32_16x16x32_bf16 v[12:15], a[220:223], v[40:43], v[12:15]\n"
    "s_waitcnt lgkmcnt(4)\n"
    "v_readfirstlane_b32 s50, v22\n"
    "v_mfma_f32_16x16x32_bf16 v[16:19], a[224:227], v[40:43], v[16:19]\n"
    "buffer_load_dwordx4 v[72:75], v4, s[92:95], s49 offen offset:2048 sc1\n"
    "s_cmp_eq_u32 s50, 3\n"
    "v_mfma_f32_16x16x32_bf16 v[8:11], a[228:231], v[44:47], v[8:11]\n"
    "s_cselect_b64 exec, 1, 0\n"
    "global_store_dword v23, v22, %1 sc1\n"
    "s_mov_b64 exec, -1\n"
    "v_mfma_f32_16x16x32_bf16 v[12:15], a[232:235], v[44:47], v[12:15]\n"
    "buffer_load_dwordx4 v[76:79], v4, s[92:95], s49 offen offset:3072 sc1\n"
    "v_mfma_f32_16x16x32_bf16 v[16:19], a[236:239], v[44:47], v[16:19]\n"
    "buffer_load_dwordx4 v[80:83], v5, s[92:95], s49 offen sc1\n"
    "v_mfma_f32_16x16x32_bf16 v[8:11], a[240:243], v[48:51], v[8:11]\n"
    "ds_read_b128 v[44:47], v0 offset:3072\n"
    "ds_read_b128 v[40:43], v0 offset:2048\n"
    "v_mfma_f32_16x16x32_bf16 v[12:15], a[244:247], v[48:51], v[12:15]\n"
    "buffer_load_dwordx4 v[84:87], v5, s[92:95], s49 offen offset:1024 sc1\n"
    "v_mfma_f32_16x16x32_bf16 v[16:19], a[248:251], v[48:51], v[16:19]\n"
    "buffer_load_dwordx4 v[88:91], v5, s[92:95], s49 offen offset:2048 sc1\n"
    "v_mfma_f32_16x16x32_bf16 v[8:11], a[252:255], v[52:55], v[8:11]\n"
    "buffer_load_dwordx4 v[92:95], v5, s[92:95], s49 offen offset:3072 sc1\n"
    "v_mfma_f32_16x16x32_bf16 v[12:15], v[128:131], v[52:55], v[12:15]\n"
    "v_mfma_f32_16x16x32_bf16 v[16:19], v[132:135], v[52:55], v[16:19]\n"
    "s_waitcnt lgkmcnt(4)\n"
    "v_mfma_f32_16x16x32_bf16 v[8:11], v[136:139], v[56:59], v[8:11]\n"
    "ds_read_b128 v[52:55], v0 offset:5120\n"
    "ds_read_b128 v[48:51], v0 offset:4096\n"
    "v_mfma_f32_16x16x32_bf16 v[12:15], v[140:143], v[56:59], v[12:15]\n"
    "v_mfma_f32_16x16x32_bf16 v[16:19], v[144:147], v[56:59], v[16:19]\n"
    "v_mfma_f32_16x16x32_bf16 v[8:11], v[148:151], v[60:63], v[8:11]\n"
    "v_mfma_f32_16x16x32_bf16 v[12:15], v[152:155], v[60:63], v[12:15]\n"
    "v_mfma_f32_16x16x32_bf16 v[16:19], v[156:159], v[60:63], v[16:19]\n"
    "s_waitcnt lgkmcnt(4)\n"
    "v_mfma_f32_16x16x32_bf16 v[8:11], v[160:163], v[32:35], v[8:11]\n"
    "ds_read_b128 v[60:63], v0 offset:7168\n"
    "ds_read_b128 v[56:59], v0 offset:6144\n"
    "v_mfma_f32_16x16x32_bf16 v[12:15], v[164:167], v[32:35], v[12:15]\n"
    "s_cmp_eq_u32 s94, 0\n"
    "s_cbranch_scc1 .Lwk1_%=\n"
    ".Lwk1_x%=:\n"
    "v_mfma_f32_16x16x32_bf16 v[16:19], v[168:171], v[32:35], v[16:19]\n"
    "v_mfma_f32_16x16x32_bf16 v[8:11], v[172:175], v[36:39], v[8:11]\n"
    "v_mfma_f32_16x16x32_bf16 v[12:15], v[176:179], v[36:39], v[12:15]\n"
    "v_mfma_f32_16x16x32_bf16 v[16:19], v[180:183], v[36:39], v[16:19]\n"
    "s_waitcnt lgkmcnt(4)\n"
    "v_mfma_f32_16x16x32_bf16 v[8:11], v[184:187], v[40:43], v[8:11]\n"
    "v_mfma_f32_16x16x32_bf16 v[12:15], v[188:191], v[40:43], v[12:15]\n"
    "v_mfma_f32_16x16x32_bf16 v[16:19], v[192:195], v[40:43], v[16:19]\n"
    "v_mfma_f32_16x16x32_bf16 v[8:11], v[196:199], v[44:47], v[8:11]\n"
    "v_xor_b32 v1, 0x8000, v1\n"
    "v_mfma_f32_16x16x32_bf16 v[12:15], v[200:203], v[44:47], v[12:15]\n"
    "s_waitcnt lgkmcnt(2)\n"
    "v_mfma_f32_16x16x32_bf16 v[8:11], v[208:211], v[48:51], v[8:11]\n"
    "v_mfma_f32_16x16x32_bf16 v[12:15], v[212:215], v[48:51], v[12:15]\n"
    "v_mfma_f32_16x16x32_bf16 v[8:11], v[220:223], v[52:55], v[8:11]\n"
    "v_mfma_f32_16x16x32_bf16 v[12:15], v[224:227], v[52:55], v[12:15]\n"
    "s_waitcnt lgkmcnt(0)\n"
    "v_mfma_f32_16x16x32_bf16 v[8:11], v[232:235], v[56:59], v[8:11]\n"
    "v_mfma_f32_16x16x32_bf16 v[12:15], v[236:239], v[56:59], v[12:15]\n"
    "v_mfma_f32_16x16x32_bf16 v[8:11], v[244:247], v[60:63], v[8:11]\n"
    "v_mfma_f32_16x16x32_bf16 v[12:15], v[248:251], v[60:63], v[12:15]\n"
    "v_mfma_f32_16x16x32_bf16 v[16:19], v[204:207], v[44:47], v[16:19]\n"
    "v_mfma_f32_16x16x32_bf16 v[16:19], v[216:219], v[48:51], v[16:19]\n"
    "v_cvt_pk_bf16_f32 v24, v8, v9\n"
    "v_cvt_pk_bf16_f32 v25, v10, v11\n"
    "v_mfma_f32_16x16x32_bf16 v[16:19], v[228:231], v[52:55], v[16:19]\n"
    "v_cvt_pk_bf16_f32 v26, v12, v13\n"
    "v_cvt_pk_bf16_f32 v27, v14, v15\n"
    "v_mfma_f32_16x16x32_bf16 v[16:19], v[240:243], v[56:59], v[16:19]\n"
    "buffer_store_dwordx4 v[24:27], v2, s[40:43], s62 offen sc1\n"
    "v_mfma_f32_16x16x32_bf16 v[16:19], v[252:255], v[60:63], v[16:19]\n"
    "s_mov_b32 s56, 1\n"
    "s_cmp_lg_u32 s94, 0\n"
    "s_cbranch_scc1 .Lwgo_%=\n"
    "s_nop 5\n"
    "v_cvt_pk_bf16_f32 v28, v16, v17\n"
    "v_cvt_pk_bf16_f32 v30, v16, v17\n"
    "v_cvt_pk_bf16_f32 v29, v18, v19\n"
    "v_cvt_pk_bf16_f32 v31, v18, v19\n"
    "v_permlane16_swap_b32 v28, v30\n"
    "s_nop 0\n"
    "v_permlane16_swap_b32 v29, v31\n"
    "buffer_store_dwordx4 v[28:31], v3, s[40:43], s62 offen sc1\n"
    "s_waitcnt vmcnt(2)\n"
    "v_cmp_eq_u32 vcc, 0, v21\n"
    "s_cmp_eq_u64 vcc, 0\n"
    "s_cbranch_scc1 .Lwld_%=\n"
    "ds_read_b32 v23, v7 offset:44\n"
    "global_load_dword v21, v20, %1 sc1\n"
    "s_waitcnt vmcnt(0)\n"
    "v_mov_b32 v22, 1\n"
    "s_mov_b64 exec, 1\n"
    "ds_add_rtn_u32 v22, v7, v22 offset:48\n"
    "s_mov_b64 exec, -1\n"
    "s_waitcnt lgkmcnt(0)\n"
    "v_readfirstlane_b32 s50, v22\n"
    "s_cmp_eq_u32 s50, 3\n"
    "s_cselect_b64 exec, 1, 0\n"
    "global_store_dword v23, v22, %1 sc1\n"
    "s_mov_b64 exec, -1\n"
    "s_mov_b32 s56, 0\n"
    "v_cmp_eq_u32 vcc, 0, v21\n"
    "s_cmp_eq_u64 vcc, 0\n"
    "s_cbranch_scc1 .Lwld_%=\n"
    ".Lwchk_%=:\n"
    "v_readfirstlane_b32 s50, v20\n"
    "s_cmp_lt_u32 s50, 200\n"
    "s_cbranch_scc1 .Lwend_%=\n"
    ".Lwwait_%=:\n"
    "global_load_dword v21, v20, %1 sc1\n"
    "s_mov_b32 s52, 0\n"
    ".Lwp2_%=:\n"
    "s_waitcnt vmcnt(0)\n"
    "v_cmp_eq_u32 vcc, 0, v21\n"
    "s_cmp_eq_u64 vcc, 0\n"
    "s_cbranch_scc1 .Lwp2_x%=\n"
    "s_add_u32 s52, s52, 1\n"
    "s_sleep 2\n"
    "global_load_dword v21, v20, %1 sc1\n"
    "s_cmp_lt_u32 s52, s53\n"
    "s_cbranch_scc1 .Lwp2_%=\n"
    "s_mov_b32 s53, 0\n"
    "v_mov_b32 v22, 0\n"
    "v_mov_b32 v23, 1\n"
    "global_store_dword v22, v23, %1 sc1\n"
    "s_waitcnt vmcnt(0)\n"
    ".Lwp2_x%=:\n"
    ".Lwld_%=:\n"
    "s_mov_b32 s94, s42\n"
    "buffer_load_dwordx4 v[64:67], v4, s[92:95], s49 offen sc1\n"
    "buffer_load_dwordx4 v[68:71], v4, s[92:95], s49 offen offset:1024 sc1\n"
    "buffer_load_dwordx4 v[72:75], v4, s[92:95], s49 offen offset:2048 sc1\n"
    "buffer_load_dwordx4 v[76:79], v4, s[92:95], s49 offen offset:3072 sc1\n"
    "buffer_load_dwordx4 v[80:83], v5, s[92:95], s49 offen sc1\n"
    "buffer_load_dwordx4 v[84:87], v5, s[92:95], s49 offen offset:1024 sc1\n"
    "buffer_load_dwordx4 v[88:91], v5, s[92:95], s49 offen offset:2048 sc1\n"
    "buffer_load_dwordx4 v[92:95], v5, s[92:95], s49 offen offset:3072 sc1\n"
    ".Lwin_%=:\n"
    "v_add_u32 v7, 32, v7\n"
    "ds_read_b128 v[20:23], v7\n"
    "s_waitcnt vmcnt(7)\n"
    "v_mfma_f32_16x16x32_bf16 v[8:11], a[0:3], v[64:67], 0\n"
    "v_mfma_f32_16x16x32_bf16 v[12:15], a[4:7], v[64:67], 0\n"
    "ds_write_b128 v1, v[64:67]\n"
    "s_waitcnt vmcnt(6)\n"
    "ds_write_b128 v1, v[68:71] offset:1024\n"
    "v_mfma_f32_16x16x32_bf16 v[8:11], a[12:15], v[68:71], v[8:11]\n"
    "s_waitcnt vmcnt(5)\n"
    "ds_write_b128 v1, v[72:75] offset:2048\n"
    "s_waitcnt vmcnt(4)\n"
    "ds_write_b128 v1, v[76:79] offset:3072\n"
    "v_mfma_f32_16x16x32_bf16 v[12:15], a[16:19], v[68:71], v[12:15]\n"
    "v_mfma_f32_16x16x32_bf16 v[8:11], a[24:27], v[72:75], v[8:11]\n"
    "v_mfma_f32_16x16x32_bf16 v[12:15], a[28:31], v[72:75], v[12:15]\n"
    "s_waitcnt vmcnt(3)\n"
    "ds_write_b128 v1, v[80:83] offset:4096\n"
    "s_waitcnt vmcnt(2)\n"
    "ds_write_b128 v1, v[84:87] offset:5120\n"
    "v_mfma_f32_16x16x32_bf16 v[8:11], a[36:39], v[76:79], v[8:11]\n"
    "v_mfma_f32_16x16x32_bf16 v[12:15], a[40:43], v[76:79], v[12:15]\n"
    "s_waitcnt vmcnt(1)\n"
    "ds_write_b128 v1, v[88:91] offset:6144\n"
    "s_waitcnt vmcnt(0)\n"
    "ds_write_b128 v1, v[92:95] offset:7168\n"
    "v_mfma_f32_16x16x32_bf16 v[8:11], a[48:51], v[80:83], v[8:11]\n"
    "v_xor_b32 v0, 0x2000, v1\n"
    "v_mfma_f32_16x16x32_bf16 v[12:15], a[52:55], v[80:83], v[12:15]\n"
    "s_waitcnt lgkmcnt(8)\n"
    "v_readfirstlane_b32 s49, v21\n"
    "v_mfma_f32_16x16x32_bf16 v[8:11], a[60:63], v[84:87], v[8:11]\n"
    "v_readfirstlane_b32 s62, v22\n"
    "v_add_u32 v20, v20, v6\n"
    "v_mfma_f32_16x16x32_bf16 v[12:15], a[64:67], v[84:87], v[12:15]\n"
    "s_cmp_eq_u32 s56, 0\n"
    "s_cbranch_scc1 .Lwc3_%=\n"
    "s_waitcnt vmcnt(0)\n"
    "v_mov_b32 v22, 1\n"
    "s_mov_b64 exec, 1\n"
    "ds_add_rtn_u32 v22, v7, v22 offset:16\n"
    "s_mov_b64 exec, -1\n"
    "s_waitcnt lgkmcnt(0)\n"
    "v_readfirstlane_b32 s50, v22\n"
    "s_cmp_eq_u32 s50, 3\n"
    "s_cselect_b64 exec, 1, 0\n"
    "global_store_dword v23, v22, %1 sc1\n"
    "s_mov_b64 exec, -1\n"
    "s_mov_b32 s56, 0\n"
    ".Lwc3_%=:\n"
    "s_branch .Lwtop_%=\n"
    ".Lwk1_%=:\n"
    "s_waitcnt vmcnt(8)\n"
    "v_cmp_eq_u32 vcc, 0, v21\n"
    "s_cmp_eq_u64 vcc, 0\n"
    "s_cselect_b32 s94, s42, 0\n"
    "global_load_dword v21, v20, %1 sc1\n"
    "buffer_load_dwordx4 v[64:67], v4, s[92:95], s49 offen sc1\n"
    "buffer_load_dwordx4 v[68:71], v4, s[92:95], s49 offen offset:1024 sc1\n"
    "buffer_load_dwordx4 v[72:75], v4, s[92:95], s49 offen offset:2048 sc1\n"
    "buffer_load_dwordx4 v[76:79], v4, s[92:95], s49 offen offset:3072 sc1\n"
    "buffer_load_dwordx4 v[80:83], v5, s[92:95], s49 offen sc1\n"
    "buffer_load_dwordx4 v[84:87], v5, s[92:95], s49 offen offset:1024 sc1\n"
    "buffer_load_dwordx4 v[88:91], v5, s[92:95], s49 offen offset:2048 sc1\n"
    "buffer_load_dwordx4 v[92:95], v5, s[92:95], s49 offen offset:3072 sc1\n"
    "s_branch .Lwk1_x%=\n"
    ".Lwpol_%=:\n"
    "v_mov_b32 v20, 64\n"
    "global_load_dword v21, v20, %1 sc1\n"
    "s_mov_b32 s52, 0\n"
    ".Lwp4_%=:\n"
    "s_waitcnt vmcnt(0)\n"
    "v_cmp_eq_u32 vcc, 0, v21\n"
    "s_cmp_eq_u64 vcc, 0\n"
    "s_cbranch_scc1 .Lwp4_x%=\n"
    "s_add_u32 s52, s52, 1\n"
    "s_sleep 2\n"
    "global_load_dword v21, v20, %1 sc1\n"
    "s_cmp_lt_u32 s52, s53\n"
    "s_cbranch_scc1 .Lwp4_%=\n"
    "s_mov_b32 s53, 0\n"
    "v_mov_b32 v22, 0\n"
    "v_mov_b32 v23, 1\n"
    "global_store_dword v22, v23, %1 sc1\n"
    "s_waitcnt vmcnt(0)\n"
    ".Lwp4_x%=:\n"
    "v_readfirstlane_b32 s50, v21\n"
    "v_mbcnt_lo_u32_b32 v20, -1, 0\n"
    "v_mbcnt_hi_u32_b32 v20, -1, v20\n"
    "s_lshl_b32 s51, %2, 6\n"
    "v_add_u32 v20, s51, v20\n"
    "v_min_u32 v20, 129, v20\n"
    "v_lshlrev_b32 v20, 5, v20\n"
    "v_add_u32 v20, 0x26c00, v20\n"
    "ds_read2_b32 v[22:23], v20 offset0:1 offset1:2\n"
    "s_and_b32 s50, s50, 1\n"
    "s_waitcnt lgkmcnt(0)\n"
    "v_and_b32 v21, s50, v22\n"
    "v_lshlrev_b32 v21, 13, v21\n"
    "v_and_b32 v22, -2, v22\n"
    "v_xor_b32 v22, v21, v22\n"
    "v_and_b32 v21, s50, v23\n"
    "v_lshlrev_b32 v21, 13, v21\n"
    "v_and_b32 v23, -2, v23\n"
    "v_xor_b32 v23, v21, v23\n"
    "ds_write2_b32 v20, v22, v23 offset0:1 offset1:2\n"
    "s_waitcnt lgkmcnt(0)\n"
    "s_barrier\n"
    "ds_read_b32 v21, v7 offset:4\n"
    "v_add_u32 v20, %4, v6\n"
    "s_waitcnt lgkmcnt(0)\n"
    "v_readfirstlane_b32 s49, v21\n"
    "s_branch .Lwwait_%=\n"
    ".Lwend_%=:\n"
    "s_cmp_eq_u32 s50, 192\n"
    "s_cbranch_scc0 .Lwpol_%=\n"
    "s_waitcnt vmcnt(0) lgkmcnt(0)\n"
    "s_endpgm\n"
    ".p2align 6\n"
    ".Lrole3_%=:\n"
    "s_mov_b64 s[40:41], %0\n"
    "s_and_b32 s41, s41, 0xffff\n"
    "s_mov_b32 s42, 0xc000000\n"
    "s_mov_b32 s43, 0x20000\n"
    "s_mov_b64 s[92:93], %0\n"
    "s_and_b32 s93, s93, 0xffff\n"
    "s_mov_b32 s94, 0xc000000\n"
    "s_mov_b32 s95, 0x20000\n"
    "s_mov_b32 s53, 131072\n"
    "s_mov_b64 s[56:57], 0\n"
    "v_mbcnt_lo_u32_b32 v92, -1, 0\n"
    "v_mbcnt_hi_u32_b32 v92, -1, v92\n"
    "s_lshl_b32 s50, %2, 8\n"
    "s_add_u32 s50, s50, 144384\n"
    "v_lshl_add_u32 v92, v92, 2, s50\n"
    "ds_read_b32 v0, v92\n"
    "ds_read_b32 v1, v92 offset:1024\n"
    "ds_read_b32 v2, v92 offset:2048\n"
    "ds_read_b32 v3, v92 offset:3072\n"
    "ds_read_b32 v4, v92 offset:4096\n"
    "ds_read_b32 v73, v92 offset:5120\n"
    "ds_read_b32 v6, v92 offset:6144\n"
    "ds_read_b32 v7, v92 offset:7168\n"
    "ds_read_b32 v8, v92 offset:8192\n"
    "ds_read_b32 v9, v92 offset:9216\n"
    "ds_read_b32 v10, v92 offset:10240\n"
    "ds_read_b32 v95, v92 offset:11264\n"
    "ds_read_b32 v90, v92 offset:12288\n"
    "ds_read_b32 v91, v92 offset:13312\n"
    "s_waitcnt lgkmcnt(0)\n"
    "s_barrier\n"
    "v_mbcnt_lo_u32_b32 v92, -1, 0\n"
    "v_mbcnt_hi_u32_b32 v92, -1, v92\n"
    "v_add_u32 v93, 0, v92\n"
    "v_mul_u32_u24 v94, 7282, v93\n"
    "v_lshrrev_b32 v94, 16, v94\n"
    "v_mul_u32_u24 v94, 3312, v94\n"
    "v_lshl_add_u32 v80, v93, 4, v94\n"
    "v_add_u32 v93, 64, v92\n"
    "v_mul_u32_u24 v94, 7282, v93\n"
    "v_lshrrev_b32 v94, 16, v94\n"
    "v_mul_u32_u24 v94, 3312, v94\n"
    "v_lshl_add_u32 v81, v93, 4, v94\n"
    "v_add_u32 v93, 128, v92\n"
    "v_mul_u32_u24 v94, 7282, v93\n"
    "v_lshrrev_b32 v94, 16, v94\n"
    "v_mul_u32_u24 v94, 3312, v94\n"
    "v_lshl_add_u32 v82, v93, 4, v94\n"
    "v_add_u32 v93, 192, v92\n"
    "v_mul_u32_u24 v94, 7282, v93\n"
    "v_lshrrev_b32 v94, 16, v94\n"
    "v_mul_u32_u24 v94, 3312, v94\n"
    "v_lshl_add_u32 v83, v93, 4, v94\n"
    "v_add_u32 v93, 256, v92\n"
    "v_mul_u32_u24 v94, 7282, v93\n"
    "v_lshrrev_b32 v94, 16, v94\n"
    "v_mul_u32_u24 v94, 3312, v94\n"
    "v_lshl_add_u32 v84, v93, 4, v94\n"
    "v_add_u32 v93, 320, v92\n"
    "v_mul_u32_u24 v94, 7282, v93\n"
    "v_lshrrev_b32 v94, 16, v94\n"
    "v_mul_u32_u24 v94, 3312, v94\n"
    "v_lshl_add_u32 v85, v93, 4, v94\n"
    "v_add_u32 v93, 384, v92\n"
    "v_mul_u32_u24 v94, 7282, v93\n"
    "v_lshrrev_b32 v94, 16, v94\n"
    "v_mul_u32_u24 v94, 3312, v94\n"
    "v_lshl_add_u32 v86, v93, 4, v94\n"
    "v_add_u32 v93, 448, v92\n"
    "v_mul_u32_u24 v94, 7282, v93\n"
    "v_lshrrev_b32 v94, 16, v94\n"
    "v_mul_u32_u24 v94, 3312, v94\n"
    "v_lshl_add_u32 v87, v93, 4, v94\n"
    "v_add_u32 v93, 512, v92\n"
    "v_mul_u32_u24 v94, 7282, v93\n"
    "v_lshrrev_b32 v94, 16, v94\n"
    "v_mul_u32_u24 v94, 3312, v94\n"
    "v_lshl_add_u32 v88, v93, 4, v94\n"
    "v_and_b32 v72, 48, v92\n"
    "v_add_u32 v5, 0x21c00, v73\n"
    "s_min_u32 s50, %2, 2\n"
    "s_mul_i32 s54, s50, 9216\n"
    "s_mul_i32 s50, s50, 1152\n"
    "s_add_u32 s51, s50, %6\n"
    "s_mov_b64 s[44:45], %8\n"
    "s_and_b32 s45, s45, 0xffff\n"
    "s_mov_b32 s46, 0x1e6000\n"
    "s_mov_b32 s47, 0x20000\n"
    "v_mov_b32 v56, 0x3f803f80\n"
    "v_mov_b32 v57, 0x3f803f80\n"
    "v_mov_b32 v58, 0x3f803f80\n"
    "v_mov_b32 v59, 0x3f803f80\n"
    "s_cmp_eq_u32 %2, 3\n"
    "s_cbranch_scc1 .Lac1_%=\n"
    "s_add_u32 s55, s51, 0\n"
    "s_add_u32 m0, s54, 0\n"
    "s_nop 0\n"
    "buffer_load_dwordx4 v80, s[44:47], s55 offen lds\n"
    "s_add_u32 m0, s54, 1024\n"
    "s_nop 0\n"
    "buffer_load_dwordx4 v81, s[44:47], s55 offen lds\n"
    "s_add_u32 m0, s54, 2048\n"
    "s_nop 0\n"
    "buffer_load_dwordx4 v82, s[44:47], s55 offen lds\n"
    "s_add_u32 m0, s54, 3072\n"
    "s_nop 0\n"
    "buffer_load_dwordx4 v83, s[44:47], s55 offen lds\n"
    "s_add_u32 m0, s54, 4096\n"
    "s_nop 0\n"
    "buffer_load_dwordx4 v84, s[44:47], s55 offen lds\n"
    "s_add_u32 m0, s54, 5120\n"
    "s_nop 0\n"
    "buffer_load_dwordx4 v85, s[44:47], s55 offen lds\n"
    "s_add_u32 m0, s54, 6144\n"
    "s_nop 0\n"
    "buffer_load_dwordx4 v86, s[44:47], s55 offen lds\n"
    "s_add_u32 m0, s54, 7168\n"
    "s_nop 0\n"
    "buffer_load_dwordx4 v87, s[44:47], s55 offen lds\n"
    "s_add_u32 m0, s54, 8192\n"
    "s_nop 0\n"
    "buffer_load_dwordx4 v88, s[44:47], s55 offen lds\n"
    ".Lac1_%=:\n"
    "s_cmp_eq_u32 %2, 3\n"
    "s_cbranch_scc1 .Lac2_%=\n"
    "s_add_u32 s55, s51, 221184\n"
    "s_add_u32 m0, s54, 27648\n"
    "s_nop 0\n"
    "buffer_load_dwordx4 v80, s[44:47], s55 offen lds\n"
    "s_add_u32 m0, s54, 28672\n"
    "s_nop 0\n"
    "buffer_load_dwordx4 v81, s[44:47], s55 offen lds\n"
    "s_add_u32 m0, s54, 29696\n"
    "s_nop 0\n"
    "buffer_load_dwordx4 v82, s[44:47], s55 offen lds\n"
    "s_add_u32 m0, s54, 30720\n"
    "s_nop 0\n"
    "buffer_load_dwordx4 v83, s[44:47], s55 offen lds\n"
    "s_add_u32 m0, s54, 31744\n"
    "s_nop 0\n"
    "buffer_load_dwordx4 v84, s[44:47], s55 offen lds\n"
    "s_add_u32 m0, s54, 32768\n"
    "s_nop 0\n"
    "buffer_load_dwordx4 v85, s[44:47], s55 offen lds\n"
    "s_add_u32 m0, s54, 33792\n"
    "s_nop 0\n"
    "buffer_load_dwordx4 v86, s[44:47], s55 offen lds\n"
    "s_add_u32 m0, s54, 34816\n"
    "s_nop 0\n"
    "buffer_load_dwordx4 v87, s[44:47], s55 offen lds\n"
    "s_add_u32 m0, s54, 35840\n"
    "s_nop 0\n"
    "buffer_load_dwordx4 v88, s[44:47], s55 offen lds\n"
    ".Lac2_%=:\n"
    "s_cmp_eq_u32 %2, 3\n"
    "s_cbranch_scc1 .Lac3_%=\n"
    "s_add_u32 s55, s51, 442368\n"
    "s_add_u32 m0, s54, 55296\n"
    "s_nop 0\n"
    "buffer_load_dwordx4 v80, s[44:47], s55 offen lds\n"
    "s_add_u32 m0, s54, 56320\n"
    "s_nop 0\n"
    "buffer_load_dwordx4 v81, s[44:47], s55 offen lds\n"
    "s_add_u32 m0, s54, 57344\n"
    "s_nop 0\n"
    "buffer_load_dwordx4 v82, s[44:47], s55 offen lds\n"
    "s_add_u32 m0, s54, 58368\n"
    "s_nop 0\n"
    "buffer_load_dwordx4 v83, s[44:47], s55 offen lds\n"
    "s_add_u32 m0, s54, 59392\n"
    "s_nop 0\n"
    "buffer_load_dwordx4 v84, s[44:47], s55 offen lds\n"
    "s_add_u32 m0, s54, 60416\n"
    "s_nop 0\n"
    "buffer_load_dwordx4 v85, s[44:47], s55 offen lds\n"
    "s_add_u32 m0, s54, 61440\n"
    "s_nop 0\n"
    "buffer_load_dwordx4 v86, s[44:47], s55 offen lds\n"
    "s_add_u32 m0, s54, 62464\n"
    "s_nop 0\n"
    "buffer_load_dwordx4 v87, s[44:47], s55 offen lds\n"
    "s_add_u32 m0, s54, 63488\n"
    "s_nop 0\n"
    "buffer_load_dwordx4 v88, s[44:47], s55 offen lds\n"
    ".Lac3_%=:\n"
    "s_cmp_eq_u32 %2, 3\n"
    "s_cbranch_scc1 .Lac4_%=\n"
    "s_add_u32 s55, s51, 663552\n"
    "s_add_u32 m0, s54, 82944\n"
    "s_nop 0\n"
    "buffer_load_dwordx4 v80, s[44:47], s55 offen lds\n"
    "s_add_u32 m0, s54, 83968\n"
    "s_nop 0\n"
    "buffer_load_dwordx4 v81, s[44:47], s55 offen lds\n"
    "s_add_u32 m0, s54, 84992\n"
    "s_nop 0\n"
    "buffer_load_dwordx4 v82, s[44:47], s55 offen lds\n"
    "s_add_u32 m0, s54, 86016\n"
    "s_nop 0\n"
    "buffer_load_dwordx4 v83, s[44:47], s55 offen lds\n"
    "s_add_u32 m0, s54, 87040\n"
    "s_nop 0\n"
    "buffer_load_dwordx4 v84, s[44:47], s55 offen lds\n"
    "s_add_u32 m0, s54, 88064\n"
    "s_nop 0\n"
    "buffer_load_dwordx4 v85, s[44:47], s55 offen lds\n"
    "s_add_u32 m0, s54, 89088\n"
    "s_nop 0\n"
    "buffer_load_dwordx4 v86, s[44:47], s55 offen lds\n"
    "s_add_u32 m0, s54, 90112\n"
    "s_nop 0\n"
    "buffer_load_dwordx4 v87, s[44:47], s55 offen lds\n"
    "s_add_u32 m0, s54, 91136\n"
    "s_nop 0\n"
    "buffer_load_dwordx4 v88, s[44:47], s55 offen lds\n"
    ".Lac4_%=:\n"
    "s_cmp_eq_u32 %2, 3\n"
    "s_cbranch_scc1 .Lac5_%=\n"
    "s_add_u32 s55, s51, 884736\n"
    "s_add_u32 m0, s54, 110592\n"
    "s_nop 0\n"
    "buffer_load_dwordx4 v80, s[44:47], s55 offen lds\n"
    "s_add_u32 m0, s54, 111616\n"
    "s_nop 0\n"
    "buffer_load_dwordx4 v81, s[44:47], s55 offen lds\n"
    "s_add_u32 m0, s54, 112640\n"
    "s_nop 0\n"
    "buffer_load_dwordx4 v82, s[44:47], s55 offen lds\n"
    "s_add_u32 m0, s54, 113664\n"
    "s_nop 0\n"
    "buffer_load_dwordx4 v83, s[44:47], s55 offen lds\n"
    "s_add_u32 m0, s54, 114688\n"
    "s_nop 0\n"
    "buffer_load_dwordx4 v84, s[44:47], s55 offen lds\n"
    "s_add_u32 m0, s54, 115712\n"
    "s_nop 0\n"
    "buffer_load_dwordx4 v85, s[44:47], s55 offen lds\n"
    "s_add_u32 m0, s54, 116736\n"
    "s_nop 0\n"
    "buffer_load_dwordx4 v86, s[44:47], s55 offen lds\n"
    "s_add_u32 m0, s54, 117760\n"
    "s_nop 0\n"
    "buffer_load_dwordx4 v87, s[44:47], s55 offen lds\n"
    "s_add_u32 m0, s54, 118784\n"
    "s_nop 0\n"
    "buffer_load_dwordx4 v88, s[44:47], s55 offen lds\n"
    ".Lac5_%=:\n"
    "s_waitcnt vmcnt(36) lgkmcnt(0)\n"
    "s_barrier\n"
    "ds_read_b64_tr_b16 a[0:1], v90\n"
    "ds_read_b64_tr_b16 a[2:3], v90 offset:576\n"
    "ds_read_b64_tr_b16 a[72:73], v90 offset:9216\n"
    "ds_read_b64_tr_b16 a[74:75], v90 offset:9792\n"
    "ds_read_b64_tr_b16 a[144:145], v90 offset:18432\n"
    "ds_read_b64_tr_b16 a[146:147], v90 offset:19008\n"
    "ds_read_b64_tr_b16 a[216:217], v91\n"
    "ds_read_b64_tr_b16 a[218:219], v91 offset:576\n"
    "ds_read_b64_tr_b16 a[4:5], v90 offset:4608\n"
    "ds_read_b64_tr_b16 a[6:7], v90 offset:5184\n"
    "ds_read_b64_tr_b16 a[76:77], v90 offset:13824\n"
    "ds_read_b64_tr_b16 a[78:79], v90 offset:14400\n"
    "ds_read_b64_tr_b16 a[148:149], v90 offset:23040\n"
    "ds_read_b64_tr_b16 a[150:151], v90 offset:23616\n"
    "ds_read_b64_tr_b16 a[220:221], v91 offset:4608\n"
    "ds_read_b64_tr_b16 a[222:223], v91 offset:5184\n"
    "s_waitcnt vmcnt(27) lgkmcnt(0)\n"
    "s_barrier\n"
    "s_cmp_eq_u32 %2, 3\n"
    "s_cbranch_scc1 .Lac6_%=\n"
    "s_add_u32 s55, s51, 1105920\n"
    "s_add_u32 m0, s54, 0\n"
    "s_nop 0\n"
    "buffer_load_dwordx4 v80, s[44:47], s55 offen lds\n"
    "s_add_u32 m0, s54, 1024\n"
    "s_nop 0\n"
    "buffer_load_dwordx4 v81, s[44:47], s55 offen lds\n"
    "s_add_u32 m0, s54, 2048\n"
    "s_nop 0\n"
    "buffer_load_dwordx4 v82, s[44:47], s55 offen lds\n"
    "s_add_u32 m0, s54, 3072\n"
    "s_nop 0\n"
    "buffer_load_dwordx4 v83, s[44:47], s55 offen lds\n"
    "s_add_u32 m0, s54, 4096\n"
    "s_nop 0\n"
    "buffer_load_dwordx4 v84, s[44:47], s55 offen lds\n"
    "s_add_u32 m0, s54, 5120\n"
    "s_nop 0\n"
    "buffer_load_dwordx4 v85, s[44:47], s55 offen lds\n"
    "s_add_u32 m0, s54, 6144\n"
    "s_nop 0\n"
    "buffer_load_dwordx4 v86, s[44:47], s55 offen lds\n"
    "s_add_u32 m0, s54, 7168\n"
    "s_nop 0\n"
    "buffer_load_dwordx4 v87, s[44:47], s55 offen lds\n"
    "s_add_u32 m0, s54, 8192\n"
    "s_nop 0\n"
    "buffer_load_dwordx4 v88, s[44:47], s55 offen lds\n"
    ".Lac6_%=:\n"
    "v_mfma_f32_16x16x32_bf16 v[12:15], v[56:59], a[0:3], 0\n"
    "v_mfma_f32_16x16x32_bf16 v[16:19], v[56:59], a[72:75], 0\n"
    "v_mfma_f32_16x16x32_bf16 v[20:23], v[56:59], a[144:147], 0\n"
    "v_mfma_f32_16x16x32_bf16 v[24:27], v[56:59], a[216:219], 0\n"
    "v_mfma_f32_16x16x32_bf16 v[12:15], v[56:59], a[4:7], v[12:15]\n"
    "v_mfma_f32_16x16x32_bf16 v[16:19], v[56:59], a[76:79], v[16:19]\n"
    "v_mfma_f32_16x16x32_bf16 v[20:23], v[56:59], a[148:151], v[20:23]\n"
    "v_mfma_f32_16x16x32_bf16 v[24:27], v[56:59], a[220:223], v[24:27]\n"
    "ds_read_b64_tr_b16 a[8:9], v90 offset:27648\n"
    "ds_read_b64_tr_b16 a[10:11], v90 offset:28224\n"
    "ds_read_b64_tr_b16 a[80:81], v90 offset:36864\n"
    "ds_read_b64_tr_b16 a[82:83], v90 offset:37440\n"
    "ds_read_b64_tr_b16 a[152:153], v90 offset:46080\n"
    "ds_read_b64_tr_b16 a[154:155], v90 offset:46656\n"
    "ds_read_b64_tr_b16 a[224:225], v91 offset:27648\n"
    "ds_read_b64_tr_b16 a[226:227], v91 offset:28224\n"
    "ds_read_b64_tr_b16 a[12:13], v90 offset:32256\n"
    "ds_read_b64_tr_b16 a[14:15], v90 offset:32832\n"
    "ds_read_b64_tr_b16 a[84:85], v90 offset:41472\n"
    "ds_read_b64_tr_b16 a[86:87], v90 offset:42048\n"
    "ds_read_b64_tr_b16 a[156:157], v90 offset:50688\n"
    "ds_read_b64_tr_b16 a[158:159], v90 offset:51264\n"
    "ds_read_b64_tr_b16 a[228:229], v91 offset:32256\n"
    "ds_read_b64_tr_b16 a[230:231], v91 offset:32832\n"
    "s_waitcnt vmcnt(27) lgkmcnt(0)\n"
    "s_barrier\n"
    "s_cmp_eq_u32 %2, 3\n"
    "s_cbranch_scc1 .Lac7_%=\n"
    "s_add_u32 s55, s51, 1327104\n"
    "s_add_u32 m0, s54, 27648\n"
    "s_nop 0\n"
    "buffer_load_dwordx4 v80, s[44:47], s55 offen lds\n"
    "s_add_u32 m0, s54, 28672\n"
    "s_nop 0\n"
    "buffer_load_dwordx4 v81, s[44:47], s55 offen lds\n"
    "s_add_u32 m0, s54, 29696\n"
    "s_nop 0\n"
    "buffer_load_dwordx4 v82, s[44:47], s55 offen lds\n"
    "s_add_u32 m0, s54, 30720\n"
    "s_nop 0\n"
    "buffer_load_dwordx4 v83, s[44:47], s55 offen lds\n"
    "s_add_u32 m0, s54, 31744\n"
    "s_nop 0\n"
    "buffer_load_dwordx4 v84, s[44:47], s55 offen lds\n"
    "s_add_u32 m0, s54, 32768\n"
    "s_nop 0\n"
    "buffer_load_dwordx4 v85, s[44:47], s55 offen lds\n"
    "s_add_u32 m0, s54, 33792\n"
    "s_nop 0\n"
    "buffer_load_dwordx4 v86, s[44:47], s55 offen lds\n"
    "s_add_u32 m0, s54, 34816\n"
    "s_nop 0\n"
    "buffer_load_dwordx4 v87, s[44:47], s55 offen lds\n"
    "s_add_u32 m0, s54, 35840\n"
    "s_nop 0\n"
    "buffer_load_dwordx4 v88, s[44:47], s55 offen lds\n"
    ".Lac7_%=:\n"
    "v_mfma_f32_16x16x32_bf16 v[12:15], v[56:59], a[8:11], v[12:15]\n"
    "v_mfma_f32_16x16x32_bf16 v[16:19], v[56:59], a[80:83], v[16:19]\n"
    "v_mfma_f32_16x16x32_bf16 v[20:23], v[56:59], a[152:155], v[20:23]\n"
    "v_mfma_f32_16x16x32_bf16 v[24:27], v[56:59], a[224:227], v[24:27]\n"
    "v_mfma_f32_16x16x32_bf16 v[12:15], v[56:59], a[12:15], v[12:15]\n"
    "v_mfma_f32_16x16x32_bf16 v[16:19], v[56:59], a[84:87], v[16:19]\n"
    "v_mfma_f32_16x16x32_bf16 v[20:23], v[56:59], a[156:159], v[20:23]\n"
    "v_mfma_f32_16x16x32_bf16 v[24:27], v[56:59], a[228:231], v[24:27]\n"
    "ds_read_b64_tr_b16 a[16:17], v90 offset:55296\n"
    "ds_read_b64_tr_b16 a[18:19], v90 offset:55872\n"
    "ds_read_b64_tr_b16 a[88:89], v90 offset:64512\n"
    "ds_read_b64_tr_b16 a[90:91], v90 offset:65088\n"
    "v_add_u32 v99, 0x10000, v90\n"
    "ds_read_b64_tr_b16 a[160:161], v99 offset:8192\n"
    "ds_read_b64_tr_b16 a[162:163], v99 offset:8768\n"
    "ds_read_b64_tr_b16 a[232:233], v91 offset:55296\n"
    "ds_read_b64_tr_b16 a[234:235], v91 offset:55872\n"
    "ds_read_b64_tr_b16 a[20:21], v90 offset:59904\n"
    "ds_read_b64_tr_b16 a[22:23], v90 offset:60480\n"
    "ds_read_b64_tr_b16 a[92:93], v99 offset:3584\n"
    "ds_read_b64_tr_b16 a[94:95], v99 offset:4160\n"
    "ds_read_b64_tr_b16 a[164:165], v99 offset:12800\n"
    "ds_read_b64_tr_b16 a[166:167], v99 offset:13376\n"
    "ds_read_b64_tr_b16 a[236:237], v91 offset:59904\n"
    "ds_read_b64_tr_b16 a[238:239], v91 offset:60480\n"
    "s_waitcnt vmcnt(27) lgkmcnt(0)\n"
    "s_barrier\n"
    "s_cmp_eq_u32 %2, 3\n"
    "s_cbranch_scc1 .Lac8_%=\n"
    "s_add_u32 s55, s51, 1548288\n"
    "s_add_u32 m0, s54, 55296\n"
    "s_nop 0\n"
    "buffer_load_dwordx4 v80, s[44:47], s55 offen lds\n"
    "s_add_u32 m0, s54, 56320\n"
    "s_nop 0\n"
    "buffer_load_dwordx4 v81, s[44:47], s55 offen lds\n"
    "s_add_u32 m0, s54, 57344\n"
    "s_nop 0\n"
    "buffer_load_dwordx4 v82, s[44:47], s55 offen lds\n"
    "s_add_u32 m0, s54, 58368\n"
    "s_nop 0\n"
    "buffer_load_dwordx4 v83, s[44:47], s55 offen lds\n"
    "s_add_u32 m0, s54, 59392\n"
    "s_nop 0\n"
    "buffer_load_dwordx4 v84, s[44:47], s55 offen lds\n"
    "s_add_u32 m0, s54, 60416\n"
    "s_nop 0\n"
    "buffer_load_dwordx4 v85, s[44:47], s55 offen lds\n"
    "s_add_u32 m0, s54, 61440\n"
    "s_nop 0\n"
    "buffer_load_dwordx4 v86, s[44:47], s55 offen lds\n"
    "s_add_u32 m0, s54, 62464\n"
    "s_nop 0\n"
    "buffer_load_dwordx4 v87, s[44:47], s55 offen lds\n"
    "s_add_u32 m0, s54, 63488\n"
    "s_nop 0\n"
    "buffer_load_dwordx4 v88, s[44:47], s55 offen lds\n"
    ".Lac8_%=:\n"
    "v_mfma_f32_16x16x32_bf16 v[12:15], v[56:59], a[16:19], v[12:15]\n"
    "v_mfma_f32_16x16x32_bf16 v[16:19], v[56:59], a[88:91], v[16:19]\n"
    "v_mfma_f32_16x16x32_bf16 v[20:23], v[56:59], a[160:163], v[20:23]\n"
    "v_mfma_f32_16x16x32_bf16 v[24:27], v[56:59], a[232:235], v[24:27]\n"
    "v_mfma_f32_16x16x32_bf16 v[12:15], v[56:59], a[20:23], v[12:15]\n"
    "v_mfma_f32_16x16x32_bf16 v[16:19], v[56:59], a[92:95], v[16:19]\n"
    "v_mfma_f32_16x16x32_bf16 v[20:23], v[56:59], a[164:167], v[20:23]\n"
    "v_mfma_f32_16x16x32_bf16 v[24:27], v[56:59], a[236:239], v[24:27]\n"
    "ds_read_b64_tr_b16 a[24:25], v99 offset:17408\n"
    "ds_read_b64_tr_b16 a[26:27], v99 offset:17984\n"
    "ds_read_b64_tr_b16 a[96:97], v99 offset:26624\n"
    "ds_read_b64_tr_b16 a[98:99], v99 offset:27200\n"
    "ds_read_b64_tr_b16 a[168:169], v99 offset:35840\n"
    "ds_read_b64_tr_b16 a[170:171], v99 offset:36416\n"
    "v_add_u32 v98, 0x10000, v91\n"
    "ds_read_b64_tr_b16 a[240:241], v98 offset:17408\n"
    "ds_read_b64_tr_b16 a[242:243], v98 offset:17984\n"
    "ds_read_b64_tr_b16 a[28:29], v99 offset:22016\n"
    "ds_read_b64_tr_b16 a[30:31], v99 offset:22592\n"
    "ds_read_b64_tr_b16 a[100:101], v99 offset:31232\n"
    "ds_read_b64_tr_b16 a[102:103], v99 offset:31808\n"
    "ds_read_b64_tr_b16 a[172:173], v99 offset:40448\n"
    "ds_read_b64_tr_b16 a[174:175], v99 offset:41024\n"
    "ds_read_b64_tr_b16 a[244:245], v98 offset:22016\n"
    "ds_read_b64_tr_b16 a[246:247], v98 offset:22592\n"
    "s_waitcnt vmcnt(27) lgkmcnt(0)\n"
    "s_barrier\n"
    "s_cmp_eq_u32 %2, 3\n"
    "s_cbranch_scc1 .Lac9_%=\n"
    "s_add_u32 s55, s51, 1769472\n"
    "s_add_u32 m0, s54, 82944\n"
    "s_nop 0\n"
    "buffer_load_dwordx4 v80, s[44:47], s55 offen lds\n"
    "s_add_u32 m0, s54, 83968\n"
    "s_nop 0\n"
    "buffer_load_dwordx4 v81, s[44:47], s55 offen lds\n"
    "s_add_u32 m0, s54, 84992\n"
    "s_nop 0\n"
    "buffer_load_dwordx4 v82, s[44:47], s55 offen lds\n"
    "s_add_u32 m0, s54, 86016\n"
    "s_nop 0\n"
    "buffer_load_dwordx4 v83, s[44:47], s55 offen lds\n"
    "s_add_u32 m0, s54, 87040\n"
    "s_nop 0\n"
    "buffer_load_dwordx4 v84, s[44:47], s55 offen lds\n"
    "s_add_u32 m0, s54, 88064\n"
    "s_nop 0\n"
    "buffer_load_dwordx4 v85, s[44:47], s55 offen lds\n"
    "s_add_u32 m0, s54, 89088\n"
    "s_nop 0\n"
    "buffer_load_dwordx4 v86, s[44:47], s55 offen lds\n"
    "s_add_u32 m0, s54, 90112\n"
    "s_nop 0\n"
    "buffer_load_dwordx4 v87, s[44:47], s55 offen lds\n"
    "s_add_u32 m0, s54, 91136\n"
    "s_nop 0\n"
    "buffer_load_dwordx4 v88, s[44:47], s55 offen lds\n"
    ".Lac9_%=:\n"
    "v_mov_b32 v11, 0x20400\n"
    "global_load_dwordx2 v[76:77], v11, %1 sc1\n"
    "v_mbcnt_lo_u32_b32 v220, -1, 0\n"
    "v_min_u32 v220, 7, v220\n"
    "v_lshlrev_b32 v220, 7, v220\n"
    "v_add_u32 v220, 0x20000, v220\n"
    "global_load_dwordx2 v[78:79], v220, %1 sc1\n"
    "v_mfma_f32_16x16x32_bf16 v[12:15], v[56:59], a[24:27], v[12:15]\n"
    "v_mfma_f32_16x16x32_bf16 v[16:19], v[56:59], a[96:99], v[16:19]\n"
    "v_mfma_f32_16x16x32_bf16 v[20:23], v[56:59], a[168:171], v[20:23]\n"
    "v_mfma_f32_16x16x32_bf16 v[24:27], v[56:59], a[240:243], v[24:27]\n"
    "v_mfma_f32_16x16x32_bf16 v[12:15], v[56:59], a[28:31], v[12:15]\n"
    "v_mfma_f32_16x16x32_bf16 v[16:19], v[56:59], a[100:103], v[16:19]\n"
    "v_mfma_f32_16x16x32_bf16 v[20:23], v[56:59], a[172:175], v[20:23]\n"
    "v_mfma_f32_16x16x32_bf16 v[24:27], v[56:59], a[244:247], v[24:27]\n"
    "ds_read_b64_tr_b16 a[32:33], v99 offset:45056\n"
    "ds_read_b64_tr_b16 a[34:35], v99 offset:45632\n"
    "ds_read_b64_tr_b16 a[104:105], v99 offset:54272\n"
    "ds_read_b64_tr_b16 a[106:107], v99 offset:54848\n"
    "ds_read_b64_tr_b16 a[176:177], v99 offset:63488\n"
    "ds_read_b64_tr_b16 a[178:179], v99 offset:64064\n"
    "ds_read_b64_tr_b16 a[248:249], v98 offset:45056\n"
    "ds_read_b64_tr_b16 a[250:251], v98 offset:45632\n"
    "ds_read_b64_tr_b16 a[36:37], v99 offset:49664\n"
    "ds_read_b64_tr_b16 a[38:39], v99 offset:50240\n"
    "ds_read_b64_tr_b16 a[108:109], v99 offset:58880\n"
    "ds_read_b64_tr_b16 a[110:111], v99 offset:59456\n"
    "v_add_u32 v97, 0x20000, v90\n"
    "ds_read_b64_tr_b16 a[180:181], v97 offset:2560\n"
    "ds_read_b64_tr_b16 a[182:183], v97 offset:3136\n"
    "ds_read_b64_tr_b16 a[252:253], v98 offset:49664\n"
    "ds_read_b64_tr_b16 a[254:255], v98 offset:50240\n"
    "s_waitcnt vmcnt(29) lgkmcnt(0)\n"
    "s_barrier\n"
    "v_mfma_f32_16x16x32_bf16 v[12:15], v[56:59], a[32:35], v[12:15]\n"
    "v_mfma_f32_16x16x32_bf16 v[16:19], v[56:59], a[104:107], v[16:19]\n"
    "v_mfma_f32_16x16x32_bf16 v[20:23], v[56:59], a[176:179], v[20:23]\n"
    "v_mfma_f32_16x16x32_bf16 v[24:27], v[56:59], a[248:251], v[24:27]\n"
    "v_mfma_f32_16x16x32_bf16 v[12:15], v[56:59], a[36:39], v[12:15]\n"
    "v_mfma_f32_16x16x32_bf16 v[16:19], v[56:59], a[108:111], v[16:19]\n"
    "v_mfma_f32_16x16x32_bf16 v[20:23], v[56:59], a[180:183], v[20:23]\n"
    "v_mfma_f32_16x16x32_bf16 v[24:27], v[56:59], a[252:255], v[24:27]\n"
    "ds_read_b64_tr_b16 a[40:41], v90\n"
    "ds_read_b64_tr_b16 a[42:43], v90 offset:576\n"
    "ds_read_b64_tr_b16 a[112:113], v90 offset:9216\n"
    "ds_read_b64_tr_b16 a[114:115], v90 offset:9792\n"
    "ds_read_b64_tr_b16 a[184:185], v90 offset:18432\n"
    "ds_read_b64_tr_b16 a[186:187], v90 offset:19008\n"
    "ds_read_b64_tr_b16 v[224:225], v91\n"
    "ds_read_b64_tr_b16 v[226:227], v91 offset:576\n"
    "ds_read_b64_tr_b16 a[44:45], v90 offset:4608\n"
    "ds_read_b64_tr_b16 a[46:47], v90 offset:5184\n"
    "ds_read_b64_tr_b16 a[116:117], v90 offset:13824\n"
    "ds_read_b64_tr_b16 a[118:119], v90 offset:14400\n"
    "ds_read_b64_tr_b16 a[188:189], v90 offset:23040\n"
    "ds_read_b64_tr_b16 a[190:191], v90 offset:23616\n"
    "ds_read_b64_tr_b16 v[228:229], v91 offset:4608\n"
    "ds_read_b64_tr_b16 v[230:231], v91 offset:5184\n"
    "s_waitcnt vmcnt(20) lgkmcnt(0)\n"
    "s_barrier\n"
    "v_mfma_f32_16x16x32_bf16 v[12:15], v[56:59], a[40:43], v[12:15]\n"
    "v_mfma_f32_16x16x32_bf16 v[16:19], v[56:59], a[112:115], v[16:19]\n"
    "v_mfma_f32_16x16x32_bf16 v[20:23], v[56:59], a[184:187], v[20:23]\n"
    "v_mfma_f32_16x16x32_bf16 v[24:27], v[56:59], v[224:227], v[24:27]\n"
    "v_mfma_f32_16x16x32_bf16 v[12:15], v[56:59], a[44:47], v[12:15]\n"
    "v_mfma_f32_16x16x32_bf16 v[16:19], v[56:59], a[116:119], v[16:19]\n"
    "v_mfma_f32_16x16x32_bf16 v[20:23], v[56:59], a[188:191], v[20:23]\n"
    "v_mfma_f32_16x16x32_bf16 v[24:27], v[56:59], v[228:231], v[24:27]\n"
    "ds_read_b64_tr_b16 a[48:49], v90 offset:27648\n"
    "ds_read_b64_tr_b16 a[50:51], v90 offset:28224\n"
    "ds_read_b64_tr_b16 a[120:121], v90 offset:36864\n"
    "ds_read_b64_tr_b16 a[122:123], v90 offset:37440\n"
    "ds_read_b64_tr_b16 a[192:193], v90 offset:46080\n"
    "ds_read_b64_tr_b16 a[194:195], v90 offset:46656\n"
    "ds_read_b64_tr_b16 v[232:233], v91 offset:27648\n"
    "ds_read_b64_tr_b16 v[234:235], v91 offset:28224\n"
    "ds_read_b64_tr_b16 a[52:53], v90 offset:32256\n"
    "ds_read_b64_tr_b16 a[54:55], v90 offset:32832\n"
    "ds_read_b64_tr_b16 a[124:125], v90 offset:41472\n"
    "ds_read_b64_tr_b16 a[126:127], v90 offset:42048\n"
    "ds_read_b64_tr_b16 a[196:197], v90 offset:50688\n"
    "ds_read_b64_tr_b16 a[198:199], v90 offset:51264\n"
    "ds_read_b64_tr_b16 v[236:237], v91 offset:32256\n"
    "ds_read_b64_tr_b16 v[238:239], v91 offset:32832\n"
    "s_waitcnt vmcnt(11) lgkmcnt(0)\n"
    "s_barrier\n"
    "v_mfma_f32_16x16x32_bf16 v[12:15], v[56:59], a[48:51], v[12:15]\n"
    "v_mfma_f32_16x16x32_bf16 v[16:19], v[56:59], a[120:123], v[16:19]\n"
    "v_mfma_f32_16x16x32_bf16 v[20:23], v[56:59], a[192:195], v[20:23]\n"
    "v_mfma_f32_16x16x32_bf16 v[24:27], v[56:59], v[232:235], v[24:27]\n"
    "v_mfma_f32_16x16x32_bf16 v[12:15], v[56:59], a[52:55], v[12:15]\n"
    "v_mfma_f32_16x16x32_bf16 v[16:19], v[56:59], a[124:127], v[16:19]\n"
    "v_mfma_f32_16x16x32_bf16 v[20:23], v[56:59], a[196:199], v[20:23]\n"
    "v_mfma_f32_16x16x32_bf16 v[24:27], v[56:59], v[236:239], v[24:27]\n"
    "ds_read_b64_tr_b16 a[56:57], v90 offset:55296\n"
    "ds_read_b64_tr_b16 a[58:59], v90 offset:55872\n"
    "ds_read_b64_tr_b16 a[128:129], v90 offset:64512\n"
    "ds_read_b64_tr_b16 a[130:131], v90 offset:65088\n"
    "ds_read_b64_tr_b16 a[200:201], v99 offset:8192\n"
    "ds_read_b64_tr_b16 a[202:203], v99 offset:8768\n"
    "ds_read_b64_tr_b16 v[240:241], v91 offset:55296\n"
    "ds_read_b64_tr_b16 v[242:243], v91 offset:55872\n"
    "ds_read_b64_tr_b16 a[60:61], v90 offset:59904\n"
    "ds_read_b64_tr_b16 a[62:63], v90 offset:60480\n"
    "ds_read_b64_tr_b16 a[132:133], v99 offset:3584\n"
    "ds_read_b64_tr_b16 a[134:135], v99 offset:4160\n"
    "ds_read_b64_tr_b16 a[204:205], v99 offset:12800\n"
    "ds_read_b64_tr_b16 a[206:207], v99 offset:13376\n"
    "ds_read_b64_tr_b16 v[244:245], v91 offset:59904\n"
    "ds_read_b64_tr_b16 v[246:247], v91 offset:60480\n"
    "s_waitcnt vmcnt(2) lgkmcnt(0)\n"
    "s_barrier\n"
    "v_mfma_f32_16x16x32_bf16 v[12:15], v[56:59], a[56:59], v[12:15]\n"
    "v_mfma_f32_16x16x32_bf16 v[16:19], v[56:59], a[128:131], v[16:19]\n"
    "v_mfma_f32_16x16x32_bf16 v[20:23], v[56:59], a[200:203], v[20:23]\n"
    "v_mfma_f32_16x16x32_bf16 v[24:27], v[56:59], v[240:243], v[24:27]\n"
    "v_mfma_f32_16x16x32_bf16 v[12:15], v[56:59], a[60:63], v[12:15]\n"
    "v_mfma_f32_16x16x32_bf16 v[16:19], v[56:59], a[132:135], v[16:19]\n"
    "v_mfma_f32_16x16x32_bf16 v[20:23], v[56:59], a[204:207], v[20:23]\n"
    "v_mfma_f32_16x16x32_bf16 v[24:27], v[56:59], v[244:247], v[24:27]\n"
    "ds_read_b64_tr_b16 a[64:65], v99 offset:17408\n"
    "ds_read_b64_tr_b16 a[66:67], v99 offset:17984\n"
    "ds_read_b64_tr_b16 a[136:137], v99 offset:26624\n"
    "ds_read_b64_tr_b16 a[138:139], v99 offset:27200\n"
    "ds_read_b64_tr_b16 a[208:209], v99 offset:35840\n"
    "ds_read_b64_tr_b16 a[210:211], v99 offset:36416\n"
    "ds_read_b64_tr_b16 v[248:249], v98 offset:17408\n"
    "ds_read_b64_tr_b16 v[250:251], v98 offset:17984\n"
    "ds_read_b64_tr_b16 a[68:69], v99 offset:22016\n"
    "ds_read_b64_tr_b16 a[70:71], v99 offset:22592\n"
    "ds_read_b64_tr_b16 a[140:141], v99 offset:31232\n"
    "ds_read_b64_tr_b16 a[142:143], v99 offset:31808\n"
    "ds_read_b64_tr_b16 a[212:213], v99 offset:40448\n"
    "ds_read_b64_tr_b16 a[214:215], v99 offset:41024\n"
    "ds_read_b64_tr_b16 v[252:253], v98 offset:22016\n"
    "ds_read_b64_tr_b16 v[254:255], v98 offset:22592\n"
    "s_waitcnt lgkmcnt(0)\n"
    "s_barrier\n"
    "v_mfma_f32_16x16x32_bf16 v[12:15], v[56:59], a[64:67], v[12:15]\n"
    "v_mfma_f32_16x16x32_bf16 v[16:19], v[56:59], a[136:139], v[16:19]\n"
    "v_mfma_f32_16x16x32_bf16 v[20:23], v[56:59], a[208:211], v[20:23]\n"
    "v_mfma_f32_16x16x32_bf16 v[24:27], v[56:59], v[248:251], v[24:27]\n"
    "v_mfma_f32_16x16x32_bf16 v[12:15], v[56:59], a[68:71], v[12:15]\n"
    "v_mfma_f32_16x16x32_bf16 v[16:19], v[56:59], a[140:143], v[16:19]\n"
    "v_mfma_f32_16x16x32_bf16 v[20:23], v[56:59], a[212:215], v[20:23]\n"
    "v_mfma_f32_16x16x32_bf16 v[24:27], v[56:59], v[252:255], v[24:27]\n"
    "v_mov_b32 v28, 0\n"
    "v_mov_b32 v29, 0\n"
    "v_mov_b32 v30, 0\n"
    "v_mov_b32 v31, 0\n"
    "v_mov_b32 v32, 0\n"
    "v_mov_b32 v33, 0\n"
    "v_mov_b32 v34, 0\n"
    "v_mov_b32 v35, 0\n"
    "v_mov_b32 v36, 0\n"
    "v_mov_b32 v37, 0\n"
    "v_mov_b32 v38, 0\n"
    "v_mov_b32 v39, 0\n"
    "v_mov_b32 v172, 0\n"
    "v_mov_b32 v173, 0\n"
    "v_mov_b32 v174, 0\n"
    "v_mov_b32 v175, 0\n"
    "v_mov_b32 v176, 0\n"
    "v_mov_b32 v177, 0\n"
    "v_mov_b32 v178, 0\n"
    "v_mov_b32 v199, 0\n"
    "v_mov_b32 v215, 0\n"
    "v_mov_b32 v218, 0\n"
    "v_mov_b32 v219, 0\n"
    "v_mov_b32 v94, 0xf149f2ca\n"
    "v_add_u32 v93, 7, v95\n"
    "v_cmp_gt_u32 vcc, 8, v93\n"
    "s_nop 1\n"
    "v_cndmask_b32 v44, v94, v28, vcc\n"
    "v_cmp_le_i32 vcc, 9, v95\n"
    "s_nop 1\n"
    "v_cndmask_b32 v68, v94, v28, vcc\n"
    "v_add_u32 v93, 8, v95\n"
    "v_cmp_gt_u32 vcc, 8, v93\n"
    "s_nop 1\n"
    "v_cndmask_b32 v45, v94, v28, vcc\n"
    "v_cmp_le_i32 vcc, 8, v95\n"
    "s_nop 1\n"
    "v_cndmask_b32 v69, v94, v28, vcc\n"
    "v_add_u32 v93, 9, v95\n"
    "v_cmp_gt_u32 vcc, 8, v93\n"
    "s_nop 1\n"
    "v_cndmask_b32 v46, v94, v28, vcc\n"
    "v_cmp_le_i32 vcc, 7, v95\n"
    "s_nop 1\n"
    "v_cndmask_b32 v70, v94, v28, vcc\n"
    "v_add_u32 v93, 10, v95\n"
    "v_cmp_gt_u32 vcc, 8, v93\n"
    "s_nop 1\n"
    "v_cndmask_b32 v47, v94, v28, vcc\n"
    "v_cmp_le_i32 vcc, 6, v95\n"
    "s_nop 1\n"
    "v_cndmask_b32 v71, v94, v28, vcc\n"
    "s_mov_b32 s86, 0xcccccccc\n"
    "s_mov_b32 s87, 0xcccccccc\n"
    "v_cvt_pk_bf16_f32 v13, 0, v12\n"
    "v_sub_f32 v12, v12, v13\n"
    "v_cvt_pk_bf16_f32 v14, 0, v12\n"
    "v_sub_f32 v12, v12, v14\n"
    "v_cvt_pk_bf16_f32 v13, v13, v14\n"
    "v_cvt_pk_bf16_f32 v14, v12, 0\n"
    "s_mov_b64 vcc, 0xffff\n"
    "v_cndmask_b32 v60, 0, v13, vcc\n"
    "v_cndmask_b32 v61, 0, v14, vcc\n"
    "v_cvt_pk_bf16_f32 v17, 0, v16\n"
    "v_sub_f32 v16, v16, v17\n"
    "v_cvt_pk_bf16_f32 v18, 0, v16\n"
    "v_sub_f32 v16, v16, v18\n"
    "v_cvt_pk_bf16_f32 v17, v17, v18\n"
    "v_cvt_pk_bf16_f32 v18, v16, 0\n"
    "s_mov_b64 vcc, 0xffff\n"
    "v_cndmask_b32 v62, 0, v17, vcc\n"
    "v_cndmask_b32 v63, 0, v18, vcc\n"
    "v_cvt_pk_bf16_f32 v21, 0, v20\n"
    "v_sub_f32 v20, v20, v21\n"
    "v_cvt_pk_bf16_f32 v22, 0, v20\n"
    "v_sub_f32 v20, v20, v22\n"
    "v_cvt_pk_bf16_f32 v21, v21, v22\n"
    "v_cvt_pk_bf16_f32 v22, v20, 0\n"
    "s_mov_b64 vcc, 0xffff\n"
    "v_cndmask_b32 v64, 0, v21, vcc\n"
    "v_cndmask_b32 v65, 0, v22, vcc\n"
    "v_cvt_pk_bf16_f32 v25, 0, v24\n"
    "v_sub_f32 v24, v24, v25\n"
    "v_cvt_pk_bf16_f32 v26, 0, v24\n"
    "v_sub_f32 v24, v24, v26\n"
    "v_cvt_pk_bf16_f32 v25, v25, v26\n"
    "v_cvt_pk_bf16_f32 v26, v24, 0\n"
    "s_mov_b64 vcc, 0xffff\n"
    "v_cndmask_b32 v66, 0, v25, vcc\n"
    "v_cndmask_b32 v67, 0, v26, vcc\n"
    "s_waitcnt vmcnt(0)\n"
    "v_add_u32 v76, 1, v76\n"
    "v_add_u32 v77, 1, v77\n"
    "s_mov_b32 s52, 1048576\n"
    ".Lago_a%=:\n"
    "v_cmp_ne_u64 vcc, v[76:77], v[78:79]\n"
    "s_cbranch_vccz .Lago_x%=\n"
    "global_load_dwordx2 v[78:79], v220, %1 sc1\n"
    "s_sub_u32 s52, s52, 1\n"
    "s_waitcnt vmcnt(0)\n"
    "s_cbranch_scc0 .Lago_a%=\n"
    ".Lago_x%=:\n"
    "v_mbcnt_lo_u32_b32 v76, -1, 0\n"
    "v_mbcnt_hi_u32_b32 v76, -1, v76\n"
    "v_min_u32 v220, 5, v76\n"
    "v_lshlrev_b32 v220, 7, v220\n"
    "v_mov_b32 v11, 0x26c00\n"
    "ds_read_b128 v[76:79], v11\n"
    "s_waitcnt lgkmcnt(0)\n"
    "v_readfirstlane_b32 s49, v77\n"
    "v_add_u32 v76, v76, v220\n"
    "s_branch .Lachk_%=\n"
    ".Lago_%=:\n"
    "s_waitcnt vmcnt(1)\n"
    "v_add_u32 v11, 32, v11\n"
    "ds_read_b128 v[76:79], v11\n"
    "ds_read2_b32 v[74:75], v11 offset0:5 offset1:6\n"
    "ds_write_b128 v1, v[152:155]\n"
    "v_mfma_f32_16x16x32_bf16 v[48:51], v[152:155], v[152:155], 0\n"
    "ds_write_b128 v1, v[156:159] offset:1024\n"
    "v_mfma_f32_16x16x32_bf16 v[48:51], v[156:159], v[156:159], v[48:51]\n"
    "ds_write_b128 v1, v[160:163] offset:2048\n"
    "v_mfma_f32_16x16x32_bf16 v[48:51], v[160:163], v[160:163], v[48:51]\n"
    "ds_write_b128 v1, v[164:167] offset:3072\n"
    "v_mfma_f32_16x16x32_bf16 v[48:51], v[164:167], v[164:167], v[48:51]\n"
    "ds_write_b128 v1, v[168:171] offset:4096\n"
    "v_mfma_f32_16x16x32_bf16 v[48:51], v[168:171], v[168:171], v[48:51]\n"
    "s_waitcnt lgkmcnt(5)\n"
    "v_readfirstlane_b32 s50, v75\n"
    "s_cmp_eq_u32 s50, 0\n"
    "s_cbranch_scc1 .Lac10_%=\n"
    "v_swap_b32 v28, v172\n"
    "v_swap_b32 v29, v173\n"
    "v_swap_b32 v30, v174\n"
    "v_swap_b32 v32, v175\n"
    "v_swap_b32 v33, v176\n"
    "v_swap_b32 v36, v177\n"
    "v_swap_b32 v37, v178\n"
    ".Lac10_%=:\n"
    "v_readfirstlane_b32 s49, v77\n"
    "v_readfirstlane_b32 s62, v78\n"
    "v_add_u32 v76, v76, v220\n"
    "s_waitcnt lgkmcnt(0)\n"
    ".Latop_%=:\n"
    "s_waitcnt lgkmcnt(0)\n"
    "s_barrier\n"
    "s_cmp_eq_u32 %2, 1\n"
    "s_cbranch_scc1 .Laq1_%=\n"
    "s_cmp_eq_u32 %2, 2\n"
    "s_cbranch_scc1 .Laq2_%=\n"
    "s_cmp_eq_u32 %2, 3\n"
    "s_cbranch_scc1 .Laq3_%=\n"
    "ds_read_b128 v[80:83], v0\n"
    "ds_read_b128 v[88:91], v0 offset:2048\n"
    "ds_read_b128 v[84:87], v0 offset:1024\n"
    "ds_read_b128 v[100:103], v0 offset:6144\n"
    "ds_read_b128 v[96:99], v0 offset:5120\n"
    "ds_read_b128 v[92:95], v0 offset:3072\n"
    "ds_read_b128 v[112:115], v0 offset:10240\n"
    "ds_read_b128 v[108:111], v0 offset:8192\n"
    "ds_read_b128 v[104:107], v0 offset:7168\n"
    "global_load_dword v77, v76, %1 sc1\n"
    "ds_write_b128 v73, v[48:51] offset:20480\n"
    "v_mfma_f32_16x16x32_bf16 v[52:55], v[56:59], v[152:155], 0\n"
    "v_mfma_f32_16x16x32_bf16 v[52:55], v[56:59], v[156:159], v[52:55]\n"
    "v_mfma_f32_16x16x32_bf16 v[52:55], v[56:59], v[160:163], v[52:55]\n"
    "v_mfma_f32_16x16x32_bf16 v[52:55], v[56:59], v[164:167], v[52:55]\n"
    "v_mfma_f32_16x16x32_bf16 v[52:55], v[56:59], v[168:171], v[52:55]\n"
    "s_waitcnt lgkmcnt(9)\n"
    "v_mfma_f32_16x16x32_bf16 v[24:27], a[216:219], v[80:83], 0\n"
    "ds_read_b128 v[116:119], v0 offset:11264\n"
    "ds_read_b128 v[120:123], v0 offset:12288\n"
    "s_waitcnt lgkmcnt(9)\n"
    "v_mfma_f32_16x16x32_bf16 v[24:27], a[220:223], v[84:87], v[24:27]\n"
    "ds_read_b128 v[124:127], v0 offset:13312\n"
    "ds_read_b128 v[128:131], v0 offset:15360\n"
    "v_mfma_f32_16x16x32_bf16 v[24:27], a[224:227], v[88:91], v[24:27]\n"
    "ds_write_b128 v73, v[52:55] offset:24576\n"
    "ds_read_b128 v[132:135], v0 offset:16384\n"
    "s_waitcnt lgkmcnt(10)\n"
    "v_mfma_f32_16x16x32_bf16 v[24:27], a[228:231], v[92:95], v[24:27]\n"
    "ds_read_b128 v[136:139], v0 offset:17408\n"
    "ds_read_b128 v[140:143], v0 offset:18432\n"
    "v_mfma_f32_16x16x32_bf16 v[24:27], a[232:235], v[96:99], v[24:27]\n"
    "ds_read_b128 v[144:147], v0 offset:4096\n"
    "ds_read_b128 v[148:151], v0 offset:9216\n"
    "v_mfma_f32_16x16x32_bf16 v[24:27], a[236:239], v[100:103], v[24:27]\n"
    "s_waitcnt lgkmcnt(11)\n"
    "v_mfma_f32_16x16x32_bf16 v[24:27], a[240:243], v[104:107], v[24:27]\n"
    "v_mfma_f32_16x16x32_bf16 v[24:27], a[244:247], v[108:111], v[24:27]\n"
    "s_waitcnt lgkmcnt(0)\n"
    "s_barrier\n"
    "v_mfma_f32_16x16x32_bf16 v[24:27], a[248:251], v[112:115], v[24:27]\n"
    "ds_read2st64_b32 v[48:49], v10 offset0:80 offset1:84\n"
    "ds_read2st64_b32 v[50:51], v10 offset0:88 offset1:92\n"
    "v_mfma_f32_16x16x32_bf16 v[12:15], a[0:3], v[80:83], 0\n"
    "ds_read2st64_b32 v[54:55], v10 offset0:104 offset1:108\n"
    "ds_read2st64_b32 v[52:53], v10 offset0:96 offset1:100\n"
    "v_mfma_f32_16x16x32_bf16 v[16:19], a[72:75], v[80:83], 0\n"
    "v_min_f32 v40, v74, v68\n"
    "v_min_f32 v41, v74, v69\n"
    "v_mfma_f32_16x16x32_bf16 v[12:15], a[4:7], v[84:87], v[12:15]\n"
    "v_min_f32 v42, v74, v70\n"
    "v_min_f32 v43, v74, v71\n"
    "v_mfma_f32_16x16x32_bf16 v[16:19], a[76:79], v[84:87], v[16:19]\n"
    "v_mfma_f32_16x16x32_bf16 v[12:15], a[8:11], v[88:91], v[12:15]\n"
    "s_waitcnt lgkmcnt(0)\n"
    "v_add_f32 v52, v52, v53\n"
    "v_add_f32 v54, v54, v55\n"
    "v_mfma_f32_16x16x32_bf16 v[16:19], a[80:83], v[88:91], v[16:19]\n"
    "v_add_f32 v48, v48, v49\n"
    "v_add_f32 v50, v50, v51\n"
    "v_mfma_f32_16x16x32_bf16 v[12:15], a[12:15], v[92:95], v[12:15]\n"
    "v_add_f32 v52, v52, v54\n"
    "v_add_f32 v50, v48, v50\n"
    "v_mfma_f32_16x16x32_bf16 v[16:19], a[84:87], v[92:95], v[16:19]\n"
    "v_mul_f32 v52, 0xbae38e39, v52\n"
    "v_mul_f32 v50, 0x3ae38e39, v50\n"
    "v_mfma_f32_16x16x32_bf16 v[12:15], a[16:19], v[96:99], v[12:15]\n"
    "v_cvt_pk_bf16_f32 v216, v52, v52\n"
    "v_fma_f32 v50, -v52, v52, v50\n"
    "v_mfma_f32_16x16x32_bf16 v[16:19], a[88:91], v[96:99], v[16:19]\n"
    "v_cvt_pk_bf16_f32 v217, v52, v52\n"
    "v_max_f32 v50, 0, v50\n"
    "v_mfma_f32_16x16x32_bf16 v[12:15], a[20:23], v[100:103], v[12:15]\n"
    "v_cvt_pk_bf16_f32 v50, 0, v50\n"
    "v_add_f32 v50, 0x3727c5ac, v50\n"
    "v_mfma_f32_16x16x32_bf16 v[16:19], a[92:95], v[100:103], v[16:19]\n"
    "v_mfma_f32_16x16x32_bf16 v[24:27], v[66:69], v[216:219], v[24:27]\n"
    "v_cvt_pk_bf16_f32 v50, 0, v50\n"
    "v_mfma_f32_16x16x32_bf16 v[12:15], a[24:27], v[104:107], v[12:15]\n"
    "v_sqrt_f32 v50, v50\n"
    "v_mfma_f32_16x16x32_bf16 v[16:19], a[96:99], v[104:107], v[16:19]\n"
    "v_cvt_pk_bf16_f32 v50, 0, v50\n"
    "v_rcp_f32 v50, v50\n"
    "v_mfma_f32_16x16x32_bf16 v[12:15], a[28:31], v[108:111], v[12:15]\n"
    "ds_write_b32 v9, v50\n"
    "v_mfma_f32_16x16x32_bf16 v[16:19], a[100:103], v[108:111], v[16:19]\n"
    "ds_read_b128 v[200:203], v72 offset:28672\n"
    "v_mfma_f32_16x16x32_bf16 v[12:15], v[60:63], v[216:219], v[12:15]\n"
    "v_mfma_f32_16x16x32_bf16 v[12:15], a[32:35], v[112:115], v[12:15]\n"
    "v_mfma_f32_16x16x32_bf16 v[16:19], v[62:65], v[216:219], v[16:19]\n"
    "v_mul_f32 v24, v24, v50\n"
    "v_mfma_f32_16x16x32_bf16 v[16:19], a[104:107], v[112:115], v[16:19]\n"
    "v_mul_f32 v25, v25, v50\n"
    "v_mul_f32 v26, v26, v50\n"
    "v_mfma_f32_16x16x32_bf16 v[12:15], a[36:39], v[116:119], v[12:15]\n"
    "v_mul_f32 v27, v27, v50\n"
    "ds_write_b128 v5, v[24:27]\n"
    "v_mfma_f32_16x16x32_bf16 v[16:19], a[108:111], v[116:119], v[16:19]\n"
    "v_mfma_f32_16x16x32_bf16 v[12:15], a[40:43], v[120:123], v[12:15]\n"
    "v_mfma_f32_16x16x32_bf16 v[16:19], a[112:115], v[120:123], v[16:19]\n"
    "v_mfma_f32_16x16x32_bf16 v[12:15], a[44:47], v[124:127], v[12:15]\n"
    "s_waitcnt vmcnt(0)\n"
    "v_mov_b32 v78, 1\n"
    "v_mfma_f32_16x16x32_bf16 v[16:19], a[116:119], v[124:127], v[16:19]\n"
    "s_mov_b64 exec, s[56:57]\n"
    "ds_add_rtn_u32 v78, v11, v78 offset:16\n"
    "s_mov_b64 exec, -1\n"
    "v_mfma_f32_16x16x32_bf16 v[12:15], a[48:51], v[128:131], v[12:15]\n"
    "s_waitcnt lgkmcnt(0)\n"
    "s_barrier\n"
    "v_mfma_f32_16x16x32_bf16 v[16:19], a[120:123], v[128:131], v[16:19]\n"
    "ds_read_b128 v[188:191], v8\n"
    "ds_read_b128 v[192:195], v8 offset:1024\n"
    "v_mfma_f32_16x16x32_bf16 v[12:15], a[52:55], v[132:135], v[12:15]\n"
    "ds_read2st64_b64 v[184:187], v7 offset1:2\n"
    "ds_read2st64_b64 v[180:183], v6 offset1:2\n"
    "v_cmp_eq_u32 vcc, 0, v77\n"
    "s_cmp_eq_u64 vcc, 0\n"
    "v_mfma_f32_16x16x32_bf16 v[16:19], a[124:127], v[132:135], v[16:19]\n"
    "s_cselect_b32 s94, s42, 0\n"
    "v_mfma_f32_16x16x32_bf16 v[12:15], a[56:59], v[136:139], v[12:15]\n"
    "global_load_dword v77, v76, %1 sc1\n"
    "v_mfma_f32_16x16x32_bf16 v[16:19], a[128:131], v[136:139], v[16:19]\n"
    "buffer_load_dwordx4 v[152:155], v3, s[92:95], s49 offen sc1\n"
    "v_mfma_f32_16x16x32_bf16 v[12:15], a[60:63], v[140:143], v[12:15]\n"
    "buffer_load_dwordx4 v[156:159], v3, s[92:95], s49 offen offset:1024 sc1\n"
    "v_mfma_f32_16x16x32_bf16 v[16:19], a[132:135], v[140:143], v[16:19]\n"
    "v_readfirstlane_b32 s50, v78\n"
    "v_mfma_f32_16x16x32_bf16 v[12:15], a[64:67], v[144:147], v[12:15]\n"
    "buffer_load_dwordx4 v[160:163], v3, s[92:95], s49 offen offset:2048 sc1\n"
    "s_cmp_eq_u32 s50, 3\n"
    "v_mfma_f32_16x16x32_bf16 v[16:19], a[136:139], v[144:147], v[16:19]\n"
    "s_waitcnt lgkmcnt(0)\n"
    "v_add_f32 v180, v180, v182\n"
    "v_add_f32 v181, v181, v183\n"
    "s_cselect_b64 exec, 1, 0\n"
    "global_store_dword v79, v78, %1 sc1\n"
    "s_mov_b64 exec, -1\n"
    "v_mfma_f32_16x16x32_bf16 v[12:15], a[68:71], v[148:151], v[12:15]\n"
    "v_cvt_pk_bf16_f32 v198, v180, v181\n"
    "v_add_f32 v184, v184, v186\n"
    "buffer_load_dwordx4 v[164:167], v3, s[92:95], s49 offen offset:3072 sc1\n"
    "v_mfma_f32_16x16x32_bf16 v[16:19], a[140:143], v[148:151], v[16:19]\n"
    "v_add_f32 v185, v185, v187\n"
    "v_cvt_pk_bf16_f32 v214, v184, v185\n"
    "buffer_load_dwordx4 v[168:171], v4, s[92:95], s49 offen sc1\n"
    "s_branch .Lak_%=\n"
    ".Laq1_%=:\n"
    "ds_read_b128 v[116:119], v0 offset:11264\n"
    "ds_read_b128 v[124:127], v0 offset:13312\n"
    "ds_read_b128 v[120:123], v0 offset:12288\n"
    "ds_read_b128 v[136:139], v0 offset:17408\n"
    "ds_read_b128 v[132:135], v0 offset:16384\n"
    "ds_read_b128 v[128:131], v0 offset:15360\n"
    "ds_read_b128 v[148:151], v0 offset:9216\n"
    "ds_read_b128 v[144:147], v0 offset:4096\n"
    "ds_read_b128 v[140:143], v0 offset:18432\n"
    "global_load_dword v77, v76, %1 sc1\n"
    "ds_write_b128 v73, v[48:51] offset:20480\n"
    "v_mfma_f32_16x16x32_bf16 v[52:55], v[56:59], v[152:155], 0\n"
    "v_mfma_f32_16x16x32_bf16 v[52:55], v[56:59], v[156:159], v[52:55]\n"
    "v_mfma_f32_16x16x32_bf16 v[52:55], v[56:59], v[160:163], v[52:55]\n"
    "v_mfma_f32_16x16x32_bf16 v[52:55], v[56:59], v[164:167], v[52:55]\n"
    "v_mfma_f32_16x16x32_bf16 v[52:55], v[56:59], v[168:171], v[52:55]\n"
    "s_waitcnt lgkmcnt(9)\n"
    "v_mfma_f32_16x16x32_bf16 v[24:27], a[252:255], v[116:119], 0\n"
    "ds_read_b128 v[80:83], v0\n"
    "ds_read_b128 v[84:87], v0 offset:1024\n"
    "s_waitcnt lgkmcnt(9)\n"
    "v_mfma_f32_16x16x32_bf16 v[24:27], v[224:227], v[120:123], v[24:27]\n"
    "ds_read_b128 v[88:91], v0 offset:2048\n"
    "ds_read_b128 v[92:95], v0 offset:3072\n"
    "v_mfma_f32_16x16x32_bf16 v[24:27], v[228:231], v[124:127], v[24:27]\n"
    "ds_write_b128 v73, v[52:55] offset:24576\n"
    "ds_read_b128 v[96:99], v0 offset:5120\n"
    "s_waitcnt lgkmcnt(10)\n"
    "v_mfma_f32_16x16x32_bf16 v[24:27], v[232:235], v[128:131], v[24:27]\n"
    "ds_read_b128 v[100:103], v0 offset:6144\n"
    "ds_read_b128 v[104:107], v0 offset:7168\n"
    "v_mfma_f32_16x16x32_bf16 v[24:27], v[236:239], v[132:135], v[24:27]\n"
    "ds_read_b128 v[108:111], v0 offset:8192\n"
    "ds_read_b128 v[112:115], v0 offset:10240\n"
    "v_mfma_f32_16x16x32_bf16 v[24:27], v[240:243], v[136:139], v[24:27]\n"
    "s_waitcnt lgkmcnt(11)\n"
    "v_mfma_f32_16x16x32_bf16 v[24:27], v[244:247], v[140:143], v[24:27]\n"
    "v_mfma_f32_16x16x32_bf16 v[24:27], v[248:251], v[144:147], v[24:27]\n"
    "s_waitcnt lgkmcnt(0)\n"
    "s_barrier\n"
    "v_mfma_f32_16x16x32_bf16 v[24:27], v[252:255], v[148:151], v[24:27]\n"
    "ds_read2st64_b32 v[48:49], v10 offset0:80 offset1:84\n"
    "ds_read2st64_b32 v[50:51], v10 offset0:88 offset1:92\n"
    "v_mfma_f32_16x16x32_bf16 v[12:15], a[36:39], v[116:119], 0\n"
    "ds_read2st64_b32 v[54:55], v10 offset0:104 offset1:108\n"
    "ds_read2st64_b32 v[52:53], v10 offset0:96 offset1:100\n"
    "v_mfma_f32_16x16x32_bf16 v[16:19], a[108:111], v[116:119], 0\n"
    "v_min_f32 v40, v74, v68\n"
    "v_min_f32 v41, v74, v69\n"
    "v_mfma_f32_16x16x32_bf16 v[12:15], a[40:43], v[120:123], v[12:15]\n"
    "v_min_f32 v42, v74, v70\n"
    "v_min_f32 v43, v74, v71\n"
    "v_mfma_f32_16x16x32_bf16 v[16:19], a[112:115], v[120:123], v[16:19]\n"
    "v_mfma_f32_16x16x32_bf16 v[12:15], a[44:47], v[124:127], v[12:15]\n"
    "s_waitcnt lgkmcnt(0)\n"
    "v_add_f32 v52, v52, v53\n"
    "v_add_f32 v54, v54, v55\n"
    "v_mfma_f32_16x16x32_bf16 v[16:19], a[116:119], v[124:127], v[16:19]\n"
    "v_add_f32 v48, v48, v49\n"
    "v_add_f32 v50, v50, v51\n"
    "v_mfma_f32_16x16x32_bf16 v[12:15], a[48:51], v[128:131], v[12:15]\n"
    "v_add_f32 v52, v52, v54\n"
    "v_add_f32 v50, v48, v50\n"
    "v_mfma_f32_16x16x32_bf16 v[16:19], a[120:123], v[128:131], v[16:19]\n"
    "v_mul_f32 v52, 0xbae38e39, v52\n"
    "v_mul_f32 v50, 0x3ae38e39, v50\n"
    "v_mfma_f32_16x16x32_bf16 v[12:15], a[52:55], v[132:135], v[12:15]\n"
    "v_cvt_pk_bf16_f32 v216, v52, v52\n"
    "v_fma_f32 v50, -v52, v52, v50\n"
    "v_mfma_f32_16x16x32_bf16 v[16:19], a[124:127], v[132:135], v[16:19]\n"
    "v_cvt_pk_bf16_f32 v217, v52, v52\n"
    "v_max_f32 v50, 0, v50\n"
    "v_mfma_f32_16x16x32_bf16 v[12:15], a[56:59], v[136:139], v[12:15]\n"
    "v_cvt_pk_bf16_f32 v50, 0, v50\n"
    "v_add_f32 v50, 0x3727c5ac, v50\n"
    "v_mfma_f32_16x16x32_bf16 v[16:19], a[128:131], v[136:139], v[16:19]\n"
    "v_cvt_pk_bf16_f32 v50, 0, v50\n"
    "v_sqrt_f32 v50, v50\n"
    "v_mfma_f32_16x16x32_bf16 v[12:15], a[60:63], v[140:143], v[12:15]\n"
    "v_cvt_pk_bf16_f32 v50, 0, v50\n"
    "v_mfma_f32_16x16x32_bf16 v[16:19], a[132:135], v[140:143], v[16:19]\n"
    "v_rcp_f32 v50, v50\n"
    "v_mfma_f32_16x16x32_bf16 v[12:15], a[64:67], v[144:147], v[12:15]\n"
    "ds_write_b32 v9, v50\n"
    "ds_read_b128 v[200:203], v72 offset:28672\n"
    "v_mfma_f32_16x16x32_bf16 v[16:19], a[136:139], v[144:147], v[16:19]\n"
    "v_mfma_f32_16x16x32_bf16 v[12:15], v[60:63], v[216:219], v[12:15]\n"
    "v_mfma_f32_16x16x32_bf16 v[16:19], v[62:65], v[216:219], v[16:19]\n"
    "v_mfma_f32_16x16x32_bf16 v[12:15], a[68:71], v[148:151], v[12:15]\n"
    "v_mul_f32 v24, v24, v50\n"
    "v_mul_f32 v25, v25, v50\n"
    "v_mfma_f32_16x16x32_bf16 v[16:19], a[140:143], v[148:151], v[16:19]\n"
    "v_mul_f32 v26, v26, v50\n"
    "v_mul_f32 v27, v27, v50\n"
    "v_mfma_f32_16x16x32_bf16 v[12:15], a[0:3], v[80:83], v[12:15]\n"
    "ds_write_b128 v5, v[24:27]\n"
    "v_mfma_f32_16x16x32_bf16 v[16:19], a[72:75], v[80:83], v[16:19]\n"
    "v_mfma_f32_16x16x32_bf16 v[12:15], a[4:7], v[84:87], v[12:15]\n"
    "v_mfma_f32_16x16x32_bf16 v[16:19], a[76:79], v[84:87], v[16:19]\n"
    "v_mfma_f32_16x16x32_bf16 v[12:15], a[8:11], v[88:91], v[12:15]\n"
    "s_waitcnt vmcnt(0)\n"
    "v_mov_b32 v78, 1\n"
    "v_mfma_f32_16x16x32_bf16 v[16:19], a[80:83], v[88:91], v[16:19]\n"
    "s_mov_b64 exec, s[56:57]\n"
    "ds_add_rtn_u32 v78, v11, v78 offset:16\n"
    "s_mov_b64 exec, -1\n"
    "v_mfma_f32_16x16x32_bf16 v[12:15], a[12:15], v[92:95], v[12:15]\n"
    "s_waitcnt lgkmcnt(0)\n"
    "s_barrier\n"
    "v_mfma_f32_16x16x32_bf16 v[16:19], a[84:87], v[92:95], v[16:19]\n"
    "ds_read_b128 v[188:191], v8\n"
    "ds_read_b128 v[192:195], v8 offset:1024\n"
    "v_mfma_f32_16x16x32_bf16 v[12:15], a[16:19], v[96:99], v[12:15]\n"
    "ds_read2st64_b64 v[184:187], v7 offset1:2\n"
    "ds_read2st64_b64 v[180:183], v6 offset1:2\n"
    "v_cmp_eq_u32 vcc, 0, v77\n"
    "s_cmp_eq_u64 vcc, 0\n"
    "v_mfma_f32_16x16x32_bf16 v[16:19], a[88:91], v[96:99], v[16:19]\n"
    "s_cselect_b32 s94, s42, 0\n"
    "v_mfma_f32_16x16x32_bf16 v[12:15], a[20:23], v[100:103], v[12:15]\n"
    "global_load_dword v77, v76, %1 sc1\n"
    "v_mfma_f32_16x16x32_bf16 v[16:19], a[92:95], v[100:103], v[16:19]\n"
    "buffer_load_dwordx4 v[152:155], v3, s[92:95], s49 offen sc1\n"
    "v_mfma_f32_16x16x32_bf16 v[12:15], a[24:27], v[104:107], v[12:15]\n"
    "buffer_load_dwordx4 v[156:159], v3, s[92:95], s49 offen offset:1024 sc1\n"
    "v_mfma_f32_16x16x32_bf16 v[16:19], a[96:99], v[104:107], v[16:19]\n"
    "v_readfirstlane_b32 s50, v78\n"
    "v_mfma_f32_16x16x32_bf16 v[12:15], a[28:31], v[108:111], v[12:15]\n"
    "buffer_load_dwordx4 v[160:163], v3, s[92:95], s49 offen offset:2048 sc1\n"
    "s_cmp_eq_u32 s50, 3\n"
    "v_mfma_f32_16x16x32_bf16 v[16:19], a[100:103], v[108:111], v[16:19]\n"
    "s_waitcnt lgkmcnt(0)\n"
    "v_add_f32 v180, v180, v182\n"
    "v_add_f32 v181, v181, v183\n"
    "s_cselect_b64 exec, 1, 0\n"
    "global_store_dword v79, v78, %1 sc1\n"
    "s_mov_b64 exec, -1\n"
    "v_mfma_f32_16x16x32_bf16 v[12:15], a[32:35], v[112:115], v[12:15]\n"
    "v_cvt_pk_bf16_f32 v198, v180, v181\n"
    "v_add_f32 v184, v184, v186\n"
    "buffer_load_dwordx4 v[164:167], v3, s[92:95], s49 offen offset:3072 sc1\n"
    "v_mfma_f32_16x16x32_bf16 v[16:19], a[104:107], v[112:115], v[16:19]\n"
    "v_add_f32 v185, v185, v187\n"
    "v_cvt_pk_bf16_f32 v214, v184, v185\n"
    "buffer_load_dwordx4 v[168:171], v4, s[92:95], s49 offen sc1\n"
    "s_branch .Lak_%=\n"
    ".Laq2_%=:\n"
    "ds_read_b128 v[80:83], v0\n"
    "ds_read_b128 v[88:91], v0 offset:2048\n"
    "ds_read_b128 v[84:87], v0 offset:1024\n"
    "ds_read_b128 v[100:103], v0 offset:6144\n"
    "ds_read_b128 v[96:99], v0 offset:5120\n"
    "ds_read_b128 v[92:95], v0 offset:3072\n"
    "ds_read_b128 v[112:115], v0 offset:10240\n"
    "ds_read_b128 v[108:111], v0 offset:8192\n"
    "ds_read_b128 v[104:107], v0 offset:7168\n"
    "global_load_dword v77, v76, %1 sc1\n"
    "ds_write_b128 v73, v[48:51] offset:20480\n"
    "v_mfma_f32_16x16x32_bf16 v[52:55], v[56:59], v[152:155], 0\n"
    "v_mfma_f32_16x16x32_bf16 v[52:55], v[56:59], v[156:159], v[52:55]\n"
    "v_mfma_f32_16x16x32_bf16 v[52:55], v[56:59], v[160:163], v[52:55]\n"
    "v_mfma_f32_16x16x32_bf16 v[52:55], v[56:59], v[164:167], v[52:55]\n"
    "v_mfma_f32_16x16x32_bf16 v[52:55], v[56:59], v[168:171], v[52:55]\n"
    "s_waitcnt lgkmcnt(9)\n"
    "v_mfma_f32_16x16x32_bf16 v[24:27], v[80:83], a[216:219], 0\n"
    "ds_read_b128 v[116:119], v0 offset:11264\n"
    "ds_read_b128 v[120:123], v0 offset:12288\n"
    "s_waitcnt lgkmcnt(9)\n"
    "v_mfma_f32_16x16x32_bf16 v[24:27], v[84:87], a[220:223], v[24:27]\n"
    "ds_read_b128 v[124:127], v0 offset:13312\n"
    "ds_read_b128 v[128:131], v0 offset:15360\n"
    "v_mfma_f32_16x16x32_bf16 v[24:27], v[88:91], a[224:227], v[24:27]\n"
    "ds_write_b128 v73, v[52:55] offset:24576\n"
    "ds_read_b128 v[132:135], v0 offset:16384\n"
    "s_waitcnt lgkmcnt(10)\n"
    "v_mfma_f32_16x16x32_bf16 v[24:27], v[92:95], a[228:231], v[24:27]\n"
    "ds_read_b128 v[136:139], v0 offset:17408\n"
    "ds_read_b128 v[140:143], v0 offset:18432\n"
    "v_mfma_f32_16x16x32_bf16 v[24:27], v[96:99], a[232:235], v[24:27]\n"
    "ds_read_b128 v[144:147], v0 offset:4096\n"
    "ds_read_b128 v[148:151], v0 offset:9216\n"
    "v_mfma_f32_16x16x32_bf16 v[24:27], v[100:103], a[236:239], v[24:27]\n"
    "s_waitcnt lgkmcnt(11)\n"
    "v_mfma_f32_16x16x32_bf16 v[24:27], v[104:107], a[240:243], v[24:27]\n"
    "v_mfma_f32_16x16x32_bf16 v[24:27], v[108:111], a[244:247], v[24:27]\n"
    "s_waitcnt lgkmcnt(0)\n"
    "s_barrier\n"
    "v_mfma_f32_16x16x32_bf16 v[24:27], v[112:115], a[248:251], v[24:27]\n"
    "ds_read2st64_b32 v[48:49], v10 offset0:80 offset1:84\n"
    "ds_read2st64_b32 v[50:51], v10 offset0:88 offset1:92\n"
    "v_mfma_f32_16x16x32_bf16 v[12:15], a[0:3], v[80:83], 0\n"
    "ds_read2st64_b32 v[54:55], v10 offset0:104 offset1:108\n"
    "ds_read2st64_b32 v[52:53], v10 offset0:96 offset1:100\n"
    "v_mfma_f32_16x16x32_bf16 v[16:19], a[72:75], v[80:83], 0\n"
    "v_min_f32 v40, v74, v68\n"
    "v_min_f32 v41, v74, v69\n"
    "v_mfma_f32_16x16x32_bf16 v[12:15], a[4:7], v[84:87], v[12:15]\n"
    "v_min_f32 v42, v74, v70\n"
    "v_min_f32 v43, v74, v71\n"
    "v_mfma_f32_16x16x32_bf16 v[16:19], a[76:79], v[84:87], v[16:19]\n"
    "v_mfma_f32_16x16x32_bf16 v[12:15], a[8:11], v[88:91], v[12:15]\n"
    "s_waitcnt lgkmcnt(0)\n"
    "v_add_f32 v52, v52, v53\n"
    "v_add_f32 v54, v54, v55\n"
    "v_mfma_f32_16x16x32_bf16 v[16:19], a[80:83], v[88:91], v[16:19]\n"
    "v_add_f32 v48, v48, v49\n"
    "v_add_f32 v50, v50, v51\n"
    "v_mfma_f32_16x16x32_bf16 v[12:15], a[12:15], v[92:95], v[12:15]\n"
    "v_add_f32 v52, v52, v54\n"
    "v_add_f32 v50, v48, v50\n"
    "v_mfma_f32_16x16x32_bf16 v[16:19], a[84:87], v[92:95], v[16:19]\n"
    "v_mul_f32 v52, 0xbae38e39, v52\n"
    "v_mul_f32 v50, 0x3ae38e39, v50\n"
    "v_mfma_f32_16x16x32_bf16 v[12:15], a[16:19], v[96:99], v[12:15]\n"
    "v_cvt_pk_bf16_f32 v216, v52, v52\n"
    "v_fma_f32 v50, -v52, v52, v50\n"
    "v_mfma_f32_16x16x32_bf16 v[16:19], a[88:91], v[96:99], v[16:19]\n"
    "v_cvt_pk_bf16_f32 v217, v52, v52\n"
    "v_max_f32 v50, 0, v50\n"
    "v_mfma_f32_16x16x32_bf16 v[12:15], a[20:23], v[100:103], v[12:15]\n"
    "v_cvt_pk_bf16_f32 v50, 0, v50\n"
    "v_add_f32 v50, 0x3727c5ac, v50\n"
    "v_mfma_f32_16x16x32_bf16 v[16:19], a[92:95], v[100:103], v[16:19]\n"
    "v_mfma_f32_16x16x32_bf16 v[24:27], v[216:219], v[66:69], v[24:27]\n"
    "v_cvt_pk_bf16_f32 v50, 0, v50\n"
    "v_mfma_f32_16x16x32_bf16 v[12:15], a[24:27], v[104:107], v[12:15]\n"
    "v_sqrt_f32 v50, v50\n"
    "v_mfma_f32_16x16x32_bf16 v[16:19], a[96:99], v[104:107], v[16:19]\n"
    "v_cvt_pk_bf16_f32 v50, 0, v50\n"
    "v_rcp_f32 v50, v50\n"
    "v_mfma_f32_16x16x32_bf16 v[12:15], a[28:31], v[108:111], v[12:15]\n"
    "ds_write_b32 v9, v50\n"
    "v_mfma_f32_16x16x32_bf16 v[16:19], a[100:103], v[108:111], v[16:19]\n"
    "ds_read_b128 v[200:203], v72 offset:28672\n"
    "v_mfma_f32_16x16x32_bf16 v[12:15], v[60:63], v[216:219], v[12:15]\n"
    "v_mfma_f32_16x16x32_bf16 v[12:15], a[32:35], v[112:115], v[12:15]\n"
    "v_mfma_f32_16x16x32_bf16 v[16:19], v[62:65], v[216:219], v[16:19]\n"
    "v_mfma_f32_16x16x32_bf16 v[16:19], a[104:107], v[112:115], v[16:19]\n"
    "v_mfma_f32_16x16x32_bf16 v[12:15], a[36:39], v[116:119], v[12:15]\n"
    "v_mfma_f32_16x16x32_bf16 v[16:19], a[108:111], v[116:119], v[16:19]\n"
    "s_waitcnt lgkmcnt(0)\n"
    "v_mul_f32 v24, v24, v200\n"
    "v_mul_f32 v25, v25, v201\n"
    "v_mfma_f32_16x16x32_bf16 v[12:15], a[40:43], v[120:123], v[12:15]\n"
    "v_mul_f32 v26, v26, v202\n"
    "v_mul_f32 v27, v27, v203\n"
    "v_mfma_f32_16x16x32_bf16 v[16:19], a[112:115], v[120:123], v[16:19]\n"
    "ds_write_b128 v5, v[24:27]\n"
    "v_mfma_f32_16x16x32_bf16 v[12:15], a[44:47], v[124:127], v[12:15]\n"
    "s_waitcnt vmcnt(0)\n"
    "v_mov_b32 v78, 1\n"
    "v_mfma_f32_16x16x32_bf16 v[16:19], a[116:119], v[124:127], v[16:19]\n"
    "s_mov_b64 exec, s[56:57]\n"
    "ds_add_rtn_u32 v78, v11, v78 offset:16\n"
    "s_mov_b64 exec, -1\n"
    "v_mfma_f32_16x16x32_bf16 v[12:15], a[48:51], v[128:131], v[12:15]\n"
    "s_waitcnt lgkmcnt(0)\n"
    "s_barrier\n"
    "v_mfma_f32_16x16x32_bf16 v[16:19], a[120:123], v[128:131], v[16:19]\n"
    "ds_read_b128 v[188:191], v8\n"
    "ds_read_b128 v[192:195], v8 offset:1024\n"
    "v_mfma_f32_16x16x32_bf16 v[12:15], a[52:55], v[132:135], v[12:15]\n"
    "ds_read2st64_b64 v[184:187], v7 offset1:2\n"
    "ds_read2st64_b64 v[180:183], v6 offset1:2\n"
    "v_cmp_eq_u32 vcc, 0, v77\n"
    "s_cmp_eq_u64 vcc, 0\n"
    "v_mfma_f32_16x16x32_bf16 v[16:19], a[124:127], v[132:135], v[16:19]\n"
    "s_cselect_b32 s94, s42, 0\n"
    "v_mfma_f32_16x16x32_bf16 v[12:15], a[56:59], v[136:139], v[12:15]\n"
    "global_load_dword v77, v76, %1 sc1\n"
    "v_mfma_f32_16x16x32_bf16 v[16:19], a[128:131], v[136:139], v[16:19]\n"
    "buffer_load_dwordx4 v[152:155], v3, s[92:95], s49 offen sc1\n"
    "v_mfma_f32_16x16x32_bf16 v[12:15], a[60:63], v[140:143], v[12:15]\n"
    "buffer_load_dwordx4 v[156:159], v3, s[92:95], s49 offen offset:1024 sc1\n"
    "v_mfma_f32_16x16x32_bf16 v[16:19], a[132:135], v[140:143], v[16:19]\n"
    "v_readfirstlane_b32 s50, v78\n"
    "v_mfma_f32_16x16x32_bf16 v[12:15], a[64:67], v[144:147], v[12:15]\n"
    "buffer_load_dwordx4 v[160:163], v3, s[92:95], s49 offen offset:2048 sc1\n"
    "s_cmp_eq_u32 s50, 3\n"
    "v_mfma_f32_16x16x32_bf16 v[16:19], a[136:139], v[144:147], v[16:19]\n"
    "s_waitcnt lgkmcnt(0)\n"
    "v_add_f32 v180, v180, v182\n"
    "v_add_f32 v181, v181, v183\n"
    "s_cselect_b64 exec, 1, 0\n"
    "global_store_dword v79, v78, %1 sc1\n"
    "s_mov_b64 exec, -1\n"
    "v_mfma_f32_16x16x32_bf16 v[12:15], a[68:71], v[148:151], v[12:15]\n"
    "v_cvt_pk_bf16_f32 v198, v180, v181\n"
    "v_add_f32 v184, v184, v186\n"
    "buffer_load_dwordx4 v[164:167], v3, s[92:95], s49 offen offset:3072 sc1\n"
    "v_mfma_f32_16x16x32_bf16 v[16:19], a[140:143], v[148:151], v[16:19]\n"
    "v_add_f32 v185, v185, v187\n"
    "v_cvt_pk_bf16_f32 v214, v184, v185\n"
    "buffer_load_dwordx4 v[168:171], v4, s[92:95], s49 offen sc1\n"
    "s_branch .Lak_%=\n"
    ".Laq3_%=:\n"
    "ds_read_b128 v[116:119], v0 offset:11264\n"
    "ds_read_b128 v[124:127], v0 offset:13312\n"
    "ds_read_b128 v[120:123], v0 offset:12288\n"
    "ds_read_b128 v[136:139], v0 offset:17408\n"
    "ds_read_b128 v[132:135], v0 offset:16384\n"
    "ds_read_b128 v[128:131], v0 offset:15360\n"
    "ds_read_b128 v[148:151], v0 offset:9216\n"
    "ds_read_b128 v[144:147], v0 offset:4096\n"
    "ds_read_b128 v[140:143], v0 offset:18432\n"
    "global_load_dword v77, v76, %1 sc1\n"
    "ds_write_b128 v73, v[48:51] offset:20480\n"
    "v_mfma_f32_16x16x32_bf16 v[52:55], v[56:59], v[152:155], 0\n"
    "v_mfma_f32_16x16x32_bf16 v[52:55], v[56:59], v[156:159], v[52:55]\n"
    "v_mfma_f32_16x16x32_bf16 v[52:55], v[56:59], v[160:163], v[52:55]\n"
    "v_mfma_f32_16x16x32_bf16 v[52:55], v[56:59], v[164:167], v[52:55]\n"
    "v_mfma_f32_16x16x32_bf16 v[52:55], v[56:59], v[168:171], v[52:55]\n"
    "s_waitcnt lgkmcnt(9)\n"
    "v_mfma_f32_16x16x32_bf16 v[24:27], v[116:119], a[252:255], 0\n"
    "ds_read_b128 v[80:83], v0\n"
    "ds_read_b128 v[84:87], v0 offset:1024\n"
    "s_waitcnt lgkmcnt(9)\n"
    "v_mfma_f32_16x16x32_bf16 v[24:27], v[120:123], v[224:227], v[24:27]\n"
    "ds_read_b128 v[88:91], v0 offset:2048\n"
    "ds_read_b128 v[92:95], v0 offset:3072\n"
    "v_mfma_f32_16x16x32_bf16 v[24:27], v[124:127], v[228:231], v[24:27]\n"
    "ds_write_b128 v73, v[52:55] offset:24576\n"
    "ds_read_b128 v[96:99], v0 offset:5120\n"
    "s_waitcnt lgkmcnt(10)\n"
    "v_mfma_f32_16x16x32_bf16 v[24:27], v[128:131], v[232:235], v[24:27]\n"
    "ds_read_b128 v[100:103], v0 offset:6144\n"
    "ds_read_b128 v[104:107], v0 offset:7168\n"
    "v_mfma_f32_16x16x32_bf16 v[24:27], v[132:135], v[236:239], v[24:27]\n"
    "ds_read_b128 v[108:111], v0 offset:8192\n"
    "ds_read_b128 v[112:115], v0 offset:10240\n"
    "v_mfma_f32_16x16x32_bf16 v[24:27], v[136:139], v[240:243], v[24:27]\n"
    "s_waitcnt lgkmcnt(11)\n"
    "v_mfma_f32_16x16x32_bf16 v[24:27], v[140:143], v[244:247], v[24:27]\n"
    "v_mfma_f32_16x16x32_bf16 v[24:27], v[144:147], v[248:251], v[24:27]\n"
    "s_waitcnt lgkmcnt(0)\n"
    "s_barrier\n"
    "v_mfma_f32_16x16x32_bf16 v[24:27], v[148:151], v[252:255], v[24:27]\n"
    "ds_read2st64_b32 v[48:49], v10 offset0:80 offset1:84\n"
    "ds_read2st64_b32 v[50:51], v10 offset0:88 offset1:92\n"
    "v_mfma_f32_16x16x32_bf16 v[12:15], a[36:39], v[116:119], 0\n"
    "ds_read2st64_b32 v[54:55], v10 offset0:104 offset1:108\n"
    "ds_read2st64_b32 v[52:53], v10 offset0:96 offset1:100\n"
    "v_mfma_f32_16x16x32_bf16 v[16:19], a[108:111], v[116:119], 0\n"
    "v_min_f32 v40, v74, v68\n"
    "v_min_f32 v41, v74, v69\n"
    "v_mfma_f32_16x16x32_bf16 v[12:15], a[40:43], v[120:123], v[12:15]\n"
    "v_min_f32 v42, v74, v70\n"
    "v_min_f32 v43, v74, v71\n"
    "v_mfma_f32_16x16x32_bf16 v[16:19], a[112:115], v[120:123], v[16:19]\n"
    "v_mfma_f32_16x16x32_bf16 v[12:15], a[44:47], v[124:127], v[12:15]\n"
    "s_waitcnt lgkmcnt(0)\n"
    "v_add_f32 v52, v52, v53\n"
    "v_add_f32 v54, v54, v55\n"
    "v_mfma_f32_16x16x32_bf16 v[16:19], a[116:119], v[124:127], v[16:19]\n"
    "v_add_f32 v48, v48, v49\n"
    "v_add_f32 v50, v50, v51\n"
    "v_mfma_f32_16x16x32_bf16 v[12:15], a[48:51], v[128:131], v[12:15]\n"
    "v_add_f32 v52, v52, v54\n"
    "v_add_f32 v50, v48, v50\n"
    "v_mfma_f32_16x16x32_bf16 v[16:19], a[120:123], v[128:131], v[16:19]\n"
    "v_mul_f32 v52, 0xbae38e39, v52\n"
    "v_mul_f32 v50, 0x3ae38e39, v50\n"
    "v_mfma_f32_16x16x32_bf16 v[12:15], a[52:55], v[132:135], v[12:15]\n"
    "v_cvt_pk_bf16_f32 v216, v52, v52\n"
    "v_fma_f32 v50, -v52, v52, v50\n"
    "v_mfma_f32_16x16x32_bf16 v[16:19], a[124:127], v[132:135], v[16:19]\n"
    "v_cvt_pk_bf16_f32 v217, v52, v52\n"
    "v_max_f32 v50, 0, v50\n"
    "v_mfma_f32_16x16x32_bf16 v[12:15], a[56:59], v[136:139], v[12:15]\n"
    "v_cvt_pk_bf16_f32 v50, 0, v50\n"
    "v_add_f32 v50, 0x3727c5ac, v50\n"
    "v_mfma_f32_16x16x32_bf16 v[16:19], a[128:131], v[136:139], v[16:19]\n"
    "v_cvt_pk_bf16_f32 v50, 0, v50\n"
    "v_sqrt_f32 v50, v50\n"
    "v_mfma_f32_16x16x32_bf16 v[12:15], a[60:63], v[140:143], v[12:15]\n"
    "v_cvt_pk_bf16_f32 v50, 0, v50\n"
    "v_mfma_f32_16x16x32_bf16 v[16:19], a[132:135], v[140:143], v[16:19]\n"
    "v_rcp_f32 v50, v50\n"
    "v_mfma_f32_16x16x32_bf16 v[12:15], a[64:67], v[144:147], v[12:15]\n"
    "ds_write_b32 v9, v50\n"
    "ds_read_b128 v[200:203], v72 offset:28672\n"
    "v_mfma_f32_16x16x32_bf16 v[16:19], a[136:139], v[144:147], v[16:19]\n"
    "v_mfma_f32_16x16x32_bf16 v[12:15], v[60:63], v[216:219], v[12:15]\n"
    "v_mfma_f32_16x16x32_bf16 v[16:19], v[62:65], v[216:219], v[16:19]\n"
    "v_mfma_f32_16x16x32_bf16 v[12:15], a[68:71], v[148:151], v[12:15]\n"
    "v_mfma_f32_16x16x32_bf16 v[16:19], a[140:143], v[148:151], v[16:19]\n"
    "v_mfma_f32_16x16x32_bf16 v[12:15], a[0:3], v[80:83], v[12:15]\n"
    "s_waitcnt lgkmcnt(0)\n"
    "v_mul_f32 v24, v24, v200\n"
    "v_mul_f32 v25, v25, v201\n"
    "v_mfma_f32_16x16x32_bf16 v[16:19], a[72:75], v[80:83], v[16:19]\n"
    "v_mul_f32 v26, v26, v202\n"
    "v_mul_f32 v27, v27, v203\n"
    "v_mfma_f32_16x16x32_bf16 v[12:15], a[4:7], v[84:87], v[12:15]\n"
    "ds_write_b128 v5, v[24:27]\n"
    "v_mfma_f32_16x16x32_bf16 v[16:19], a[76:79], v[84:87], v[16:19]\n"
    "v_mfma_f32_16x16x32_bf16 v[12:15], a[8:11], v[88:91], v[12:15]\n"
    "s_waitcnt vmcnt(0)\n"
    "v_mov_b32 v78, 1\n"
    "v_mfma_f32_16x16x32_bf16 v[16:19], a[80:83], v[88:91], v[16:19]\n"
    "s_mov_b64 exec, s[56:57]\n"
    "ds_add_rtn_u32 v78, v11, v78 offset:16\n"
    "s_mov_b64 exec, -1\n"
    "v_mfma_f32_16x16x32_bf16 v[12:15], a[12:15], v[92:95], v[12:15]\n"
    "s_waitcnt lgkmcnt(0)\n"
    "s_barrier\n"
    "v_mfma_f32_16x16x32_bf16 v[16:19], a[84:87], v[92:95], v[16:19]\n"
    "ds_read_b128 v[188:191], v8\n"
    "ds_read_b128 v[192:195], v8 offset:1024\n"
    "v_mfma_f32_16x16x32_bf16 v[12:15], a[16:19], v[96:99], v[12:15]\n"
    "ds_read2st64_b64 v[184:187], v7 offset1:2\n"
    "ds_read2st64_b64 v[180:183], v6 offset1:2\n"
    "v_cmp_eq_u32 vcc, 0, v77\n"
    "s_cmp_eq_u64 vcc, 0\n"
    "v_mfma_f32_16x16x32_bf16 v[16:19], a[88:91], v[96:99], v[16:19]\n"
    "s_cselect_b32 s94, s42, 0\n"
    "v_mfma_f32_16x16x32_bf16 v[12:15], a[20:23], v[100:103], v[12:15]\n"
    "global_load_dword v77, v76, %1 sc1\n"
    "v_mfma_f32_16x16x32_bf16 v[16:19], a[92:95], v[100:103], v[16:19]\n"
    "buffer_load_dwordx4 v[152:155], v3, s[92:95], s49 offen sc1\n"
    "v_mfma_f32_16x16x32_bf16 v[12:15], a[24:27], v[104:107], v[12:15]\n"
    "buffer_load_dwordx4 v[156:159], v3, s[92:95], s49 offen offset:1024 sc1\n"
    "v_mfma_f32_16x16x32_bf16 v[16:19], a[96:99], v[104:107], v[16:19]\n"
    "v_readfirstlane_b32 s50, v78\n"
    "v_mfma_f32_16x16x32_bf16 v[12:15], a[28:31], v[108:111], v[12:15]\n"
    "buffer_load_dwordx4 v[160:163], v3, s[92:95], s49 offen offset:2048 sc1\n"
    "s_cmp_eq_u32 s50, 3\n"
    "v_mfma_f32_16x16x32_bf16 v[16:19], a[100:103], v[108:111], v[16:19]\n"
    "s_waitcnt lgkmcnt(0)\n"
    "v_add_f32 v180, v180, v182\n"
    "v_add_f32 v181, v181, v183\n"
    "s_cselect_b64 exec, 1, 0\n"
    "global_store_dword v79, v78, %1 sc1\n"
    "s_mov_b64 exec, -1\n"
    "v_mfma_f32_16x16x32_bf16 v[12:15], a[32:35], v[112:115], v[12:15]\n"
    "v_cvt_pk_bf16_f32 v198, v180, v181\n"
    "v_add_f32 v184, v184, v186\n"
    "buffer_load_dwordx4 v[164:167], v3, s[92:95], s49 offen offset:3072 sc1\n"
    "v_mfma_f32_16x16x32_bf16 v[16:19], a[104:107], v[112:115], v[16:19]\n"
    "v_add_f32 v185, v185, v187\n"
    "v_cvt_pk_bf16_f32 v214, v184, v185\n"
    "buffer_load_dwordx4 v[168:171], v4, s[92:95], s49 offen sc1\n"
    ".Lak_%=:\n"
    "v_mfma_f32_16x16x32_bf16 v[20:23], v[216:219], v[64:67], 0\n"
    "v_add_f32 v188, v188, v192\n"
    "v_add_f32 v189, v189, v193\n"
    "v_mfma_f32_16x16x32_bf16 v[20:23], v[80:83], a[144:147], v[20:23]\n"
    "v_add_f32 v190, v190, v194\n"
    "v_add_f32 v191, v191, v195\n"
    "v_mfma_f32_16x16x32_bf16 v[20:23], v[84:87], a[148:151], v[20:23]\n"
    "v_mul_f32 v12, v12, v50\n"
    "v_mul_f32 v13, v13, v50\n"
    "v_mfma_f32_16x16x32_bf16 v[20:23], v[88:91], a[152:155], v[20:23]\n"
    "v_mul_f32 v14, v14, v50\n"
    "v_mul_f32 v15, v15, v50\n"
    "v_mfma_f32_16x16x32_bf16 v[20:23], v[92:95], a[156:159], v[20:23]\n"
    "v_cvt_pk_bf16_f32 v196, v12, v13\n"
    "v_cvt_pk_bf16_f32 v197, v14, v15\n"
    "v_mfma_f32_16x16x32_bf16 v[20:23], v[96:99], a[160:163], v[20:23]\n"
    "v_mul_f32 v16, v16, v50\n"
    "v_mul_f32 v17, v17, v50\n"
    "v_mfma_f32_16x16x32_bf16 v[20:23], v[100:103], a[164:167], v[20:23]\n"
    "v_mfma_f32_16x16x32_bf16 v[204:207], v[28:31], v[196:199], v[40:43]\n"
    "v_mul_f32 v18, v18, v50\n"
    "v_mfma_f32_16x16x32_bf16 v[20:23], v[104:107], a[168:171], v[20:23]\n"
    "v_mul_f32 v19, v19, v50\n"
    "v_cvt_pk_bf16_f32 v212, v16, v17\n"
    "v_mfma_f32_16x16x32_bf16 v[20:23], v[108:111], a[172:175], v[20:23]\n"
    "v_cvt_pk_bf16_f32 v213, v18, v19\n"
    "v_cvt_pk_bf16_f32 v38, v188, v189\n"
    "v_mfma_f32_16x16x32_bf16 v[20:23], v[112:115], a[176:179], v[20:23]\n"
    "v_cvt_pk_bf16_f32 v39, v190, v191\n"
    "v_mfma_f32_16x16x32_bf16 v[208:211], v[212:215], v[196:199], v[44:47]\n"
    "v_mfma_f32_16x16x32_bf16 v[20:23], v[116:119], a[180:183], v[20:23]\n"
    "v_cndmask_b32 v38, v38, v56, s[86:87]\n"
    "v_cndmask_b32 v39, v39, v56, s[86:87]\n"
    "v_mfma_f32_16x16x32_bf16 v[20:23], v[120:123], a[184:187], v[20:23]\n"
    "v_maximum3_f32 v188, v204, v205, v206\n"
    "v_mov_b32 v28, v212\n"
    "v_mfma_f32_16x16x32_bf16 v[20:23], v[124:127], a[188:191], v[20:23]\n"
    "v_mov_b32 v29, v213\n"
    "v_mov_b32 v30, v214\n"
    "v_mfma_f32_16x16x32_bf16 v[20:23], v[128:131], a[192:195], v[20:23]\n"
    "v_maximum3_f32 v188, v188, v207, v208\n"
    "v_maximum3_f32 v188, v188, v209, v210\n"
    "v_mfma_f32_16x16x32_bf16 v[20:23], v[132:135], a[196:199], v[20:23]\n"
    "v_maximum3_f32 v188, v188, v211, v211\n"
    "v_mov_b32 v189, v188\n"
    "v_mfma_f32_16x16x32_bf16 v[20:23], v[136:139], a[200:203], v[20:23]\n"
    "s_nop 0\n"
    "v_permlane16_swap_b32 v189, v188\n"
    "v_maximum3_f32 v188, v188, v189, v189\n"
    "v_mfma_f32_16x16x32_bf16 v[20:23], v[140:143], a[204:207], v[20:23]\n"
    "v_mov_b32 v189, v188\n"
    "v_mfma_f32_16x16x32_bf16 v[20:23], v[144:147], a[208:211], v[20:23]\n"
    "s_nop 0\n"
    "v_permlane32_swap_b32 v189, v188\n"
    "v_maximum3_f32 v188, v188, v189, v189\n"
    "v_mfma_f32_16x16x32_bf16 v[20:23], v[148:151], a[212:215], v[20:23]\n"
    "v_mul_f32 v188, 0xbeae1a93, v188\n"
    "v_fmamk_f32 v204, v204, 0x3eae1a93, v188\n"
    "v_fmamk_f32 v205, v205, 0x3eae1a93, v188\n"
    "v_fmamk_f32 v206, v206, 0x3eae1a93, v188\n"
    "v_fmamk_f32 v207, v207, 0x3eae1a93, v188\n"
    "v_fmamk_f32 v208, v208, 0x3eae1a93, v188\n"
    "v_fmamk_f32 v209, v209, 0x3eae1a93, v188\n"
    "v_fmamk_f32 v210, v210, 0x3eae1a93, v188\n"
    "v_fmamk_f32 v211, v211, 0x3eae1a93, v188\n"
    "v_exp_f32 v204, v204\n"
    "v_exp_f32 v205, v205\n"
    "v_exp_f32 v206, v206\n"
    "v_exp_f32 v207, v207\n"
    "v_exp_f32 v208, v208\n"
    "v_exp_f32 v209, v209\n"
    "v_exp_f32 v210, v210\n"
    "v_exp_f32 v211, v211\n"
    "v_mul_f32 v20, v20, v200\n"
    "v_mul_f32 v21, v21, v201\n"
    "v_mul_f32 v22, v22, v202\n"
    "v_mul_f32 v23, v23, v203\n"
    "v_cvt_pk_bf16_f32 v204, v204, v205\n"
    "v_cvt_pk_bf16_f32 v205, v206, v207\n"
    "v_cvt_pk_bf16_f32 v206, v208, v209\n"
    "v_cvt_pk_bf16_f32 v207, v210, v211\n"
    "v_cvt_pk_bf16_f32 v34, v20, v21\n"
    "v_cvt_pk_bf16_f32 v35, v22, v23\n"
    "v_mfma_f32_16x16x32_bf16 v[184:187], v[36:39], v[204:207], 0\n"
    "s_nop 0\n"
    "v_mfma_f32_16x16x32_bf16 v[180:183], v[32:35], v[204:207], 0\n"
    "v_mov_b64 v[36:37], v[38:39]\n"
    "v_mov_b64 v[32:33], v[34:35]\n"
    "s_nop 2\n"
    "v_rcp_f32 v188, v186\n"
    "s_nop 0\n"
    "v_mul_f32 v184, v184, v188\n"
    "v_mul_f32 v185, v185, v188\n"
    "v_cvt_pk_bf16_f32 v184, v184, v185\n"
    "v_pk_mul_f32 v[180:181], v[180:181], v[188:189] op_sel_hi:[1,0]\n"
    "v_pk_mul_f32 v[182:183], v[182:183], v[188:189] op_sel_hi:[1,0]\n"
    "v_cvt_pk_bf16_f32 v204, v180, v181\n"
    "v_cvt_pk_bf16_f32 v205, v182, v183\n"
    "v_cvt_pk_bf16_f32 v206, v180, v181\n"
    "v_cvt_pk_bf16_f32 v207, v182, v183\n"
    "s_nop 0\n"
    "v_permlane16_swap_b32 v204, v206\n"
    "v_permlane16_swap_b32 v205, v207\n"
    "s_nop 0\n"
    "v_mov_b32_dpp v204, v184 quad_perm:[0,0,0,0] row_mask:0x2 bank_mask:0xf\n"
    "v_mov_b32_dpp v205, v184 quad_perm:[1,1,1,1] row_mask:0x2 bank_mask:0xf\n"
    "v_mov_b32_dpp v206, v184 quad_perm:[2,2,2,2] row_mask:0x2 bank_mask:0xf\n"
    "v_mov_b32_dpp v207, v184 quad_perm:[3,3,3,3] row_mask:0x2 bank_mask:0xf\n"
    "buffer_store_dwordx4 v[204:207], v2, s[40:43], s62 offen sc1\n"
    "s_mov_b32 s56, 1\n"
    "s_cmp_lg_u32 s94, 0\n"
    "s_cbranch_scc1 .Lago_%=\n"
    "s_waitcnt vmcnt(1)\n"
    "v_cmp_eq_u32 vcc, 0, v77\n"
    "s_cmp_eq_u64 vcc, 0\n"
    "s_cbranch_scc1 .Lald_%=\n"
    "ds_read_b32 v79, v11 offset:44\n"
    "global_load_dword v77, v76, %1 sc1\n"
    "s_waitcnt vmcnt(0)\n"
    "v_mov_b32 v78, 1\n"
    "s_mov_b64 exec, 1\n"
    "ds_add_rtn_u32 v78, v11, v78 offset:48\n"
    "s_mov_b64 exec, -1\n"
    "s_waitcnt lgkmcnt(0)\n"
    "v_readfirstlane_b32 s50, v78\n"
    "s_cmp_eq_u32 s50, 3\n"
    "s_cselect_b64 exec, 1, 0\n"
    "global_store_dword v79, v78, %1 sc1\n"
    "s_mov_b64 exec, -1\n"
    "s_mov_b32 s56, 0\n"
    "v_cmp_eq_u32 vcc, 0, v77\n"
    "s_cmp_eq_u64 vcc, 0\n"
    "s_cbranch_scc1 .Lald_%=\n"
    ".Lachk_%=:\n"
    "v_readfirstlane_b32 s50, v76\n"
    "s_cmp_lt_u32 s50, 200\n"
    "s_cbranch_scc1 .Laend_%=\n"
    ".Lawait_%=:\n"
    "global_load_dword v77, v76, %1 sc1\n"
    "s_mov_b32 s52, 0\n"
    ".Lap11_%=:\n"
    "s_waitcnt vmcnt(0)\n"
    "v_cmp_eq_u32 vcc, 0, v77\n"
    "s_cmp_eq_u64 vcc, 0\n"
    "s_cbranch_scc1 .Lap11_x%=\n"
    "s_add_u32 s52, s52, 1\n"
    "s_sleep 2\n"
    "global_load_dword v77, v76, %1 sc1\n"
    "s_cmp_lt_u32 s52, s53\n"
    "s_cbranch_scc1 .Lap11_%=\n"
    "s_mov_b32 s53, 0\n"
    "v_mov_b32 v78, 0\n"
    "v_mov_b32 v79, 1\n"
    "global_store_dword v78, v79, %1 sc1\n"
    "s_waitcnt vmcnt(0)\n"
    ".Lap11_x%=:\n"
    ".Lald_%=:\n"
    "s_mov_b32 s94, s42\n"
    "buffer_load_dwordx4 v[152:155], v3, s[92:95], s49 offen sc1\n"
    "buffer_load_dwordx4 v[156:159], v3, s[92:95], s49 offen offset:1024 sc1\n"
    "buffer_load_dwordx4 v[160:163], v3, s[92:95], s49 offen offset:2048 sc1\n"
    "buffer_load_dwordx4 v[164:167], v3, s[92:95], s49 offen offset:3072 sc1\n"
    "buffer_load_dwordx4 v[168:171], v4, s[92:95], s49 offen sc1\n"
    ".Lain_%=:\n"
    "v_add_u32 v11, 32, v11\n"
    "ds_read_b128 v[76:79], v11\n"
    "ds_read2_b32 v[74:75], v11 offset0:5 offset1:6\n"
    "s_waitcnt vmcnt(4)\n"
    "ds_write_b128 v1, v[152:155]\n"
    "v_mfma_f32_16x16x32_bf16 v[48:51], v[152:155], v[152:155], 0\n"
    "s_waitcnt vmcnt(3)\n"
    "ds_write_b128 v1, v[156:159] offset:1024\n"
    "v_mfma_f32_16x16x32_bf16 v[48:51], v[156:159], v[156:159], v[48:51]\n"
    "s_waitcnt vmcnt(2)\n"
    "ds_write_b128 v1, v[160:163] offset:2048\n"
    "v_mfma_f32_16x16x32_bf16 v[48:51], v[160:163], v[160:163], v[48:51]\n"
    "s_waitcnt vmcnt(1)\n"
    "ds_write_b128 v1, v[164:167] offset:3072\n"
    "v_mfma_f32_16x16x32_bf16 v[48:51], v[164:167], v[164:167], v[48:51]\n"
    "s_waitcnt vmcnt(0)\n"
    "ds_write_b128 v1, v[168:171] offset:4096\n"
    "v_mfma_f32_16x16x32_bf16 v[48:51], v[168:171], v[168:171], v[48:51]\n"
    "s_waitcnt lgkmcnt(5)\n"
    "v_readfirstlane_b32 s50, v75\n"
    "s_cmp_eq_u32 s50, 0\n"
    "s_cbranch_scc1 .Lac12_%=\n"
    "v_swap_b32 v28, v172\n"
    "v_swap_b32 v29, v173\n"
    "v_swap_b32 v30, v174\n"
    "v_swap_b32 v32, v175\n"
    "v_swap_b32 v33, v176\n"
    "v_swap_b32 v36, v177\n"
    "v_swap_b32 v37, v178\n"
    ".Lac12_%=:\n"
    "v_readfirstlane_b32 s49, v77\n"
    "v_readfirstlane_b32 s62, v78\n"
    "v_add_u32 v76, v76, v220\n"
    "s_waitcnt lgkmcnt(0)\n"
    "s_cmp_eq_u32 s56, 0\n"
    "s_cbranch_scc1 .Lac13_%=\n"
    "s_waitcnt vmcnt(0)\n"
    "v_mov_b32 v78, 1\n"
    "s_mov_b64 exec, 1\n"
    "ds_add_rtn_u32 v78, v11, v78 offset:16\n"
    "s_mov_b64 exec, -1\n"
    "s_waitcnt lgkmcnt(0)\n"
    "v_readfirstlane_b32 s50, v78\n"
    "s_cmp_eq_u32 s50, 3\n"
    "s_cselect_b64 exec, 1, 0\n"
    "global_store_dword v79, v78, %1 sc1\n"
    "s_mov_b64 exec, -1\n"
    "s_mov_b32 s56, 0\n"
    ".Lac13_%=:\n"
    "s_branch .Latop_%=\n"
    ".Lapol_%=:\n"
    "v_mov_b32 v76, 64\n"
    "global_load_dword v77, v76, %1 sc1\n"
    "s_mov_b32 s52, 0\n"
    ".Lap14_%=:\n"
    "s_waitcnt vmcnt(0)\n"
    "v_cmp_eq_u32 vcc, 0, v77\n"
    "s_cmp_eq_u64 vcc, 0\n"
    "s_cbranch_scc1 .Lap14_x%=\n"
    "s_add_u32 s52, s52, 1\n"
    "s_sleep 2\n"
    "global_load_dword v77, v76, %1 sc1\n"
    "s_cmp_lt_u32 s52, s53\n"
    "s_cbranch_scc1 .Lap14_%=\n"
    "s_mov_b32 s53, 0\n"
    "v_mov_b32 v78, 0\n"
    "v_mov_b32 v79, 1\n"
    "global_store_dword v78, v79, %1 sc1\n"
    "s_waitcnt vmcnt(0)\n"
    ".Lap14_x%=:\n"
    "v_readfirstlane_b32 s50, v77\n"
    "v_mbcnt_lo_u32_b32 v76, -1, 0\n"
    "v_mbcnt_hi_u32_b32 v76, -1, v76\n"
    "s_lshl_b32 s51, %2, 6\n"
    "v_add_u32 v76, s51, v76\n"
    "v_min_u32 v76, 129, v76\n"
    "v_lshlrev_b32 v76, 5, v76\n"
    "v_add_u32 v76, 0x26c00, v76\n"
    "ds_read2_b32 v[78:79], v76 offset0:1 offset1:2\n"
    "s_and_b32 s50, s50, 1\n"
    "s_waitcnt lgkmcnt(0)\n"
    "v_and_b32 v77, s50, v78\n"
    "v_lshlrev_b32 v77, 13, v77\n"
    "v_and_b32 v78, -2, v78\n"
    "v_xor_b32 v78, v77, v78\n"
    "v_and_b32 v77, s50, v79\n"
    "v_lshlrev_b32 v77, 13, v77\n"
    "v_and_b32 v79, -2, v79\n"
    "v_xor_b32 v79, v77, v79\n"
    "ds_write2_b32 v76, v78, v79 offset0:1 offset1:2\n"
    "s_waitcnt lgkmcnt(0)\n"
    "s_barrier\n"
    "ds_read_b32 v77, v11 offset:4\n"
    "v_add_u32 v76, %4, v220\n"
    "s_waitcnt lgkmcnt(0)\n"
    "v_readfirstlane_b32 s49, v77\n"
    "s_branch .Lawait_%=\n"
    ".Laend_%=:\n"
    "s_cmp_eq_u32 s50, 192\n"
    "s_cbranch_scc0 .Lapol_%=\n"
    "s_waitcnt vmcnt(0) lgkmcnt(0)\n"
    "s_endpgm\n"
    ".p2align 6\n"
    ".Lrole4_%=:\n"
    "s_mov_b64 s[40:41], %0\n"
    "s_and_b32 s41, s41, 0xffff\n"
    "s_mov_b32 s42, 0xc000000\n"
    "s_mov_b32 s43, 0x20000\n"
    "s_mov_b64 s[92:93], %0\n"
    "s_and_b32 s93, s93, 0xffff\n"
    "s_mov_b32 s94, 0xc000000\n"
    "s_mov_b32 s95, 0x20000\n"
    "s_mov_b32 s53, 131072\n"
    "s_mov_b64 s[56:57], 0\n"
    "v_mbcnt_lo_u32_b32 v20, -1, 0\n"
    "v_mbcnt_hi_u32_b32 v20, -1, v20\n"
    "s_lshl_b32 s50, %2, 8\n"
    "s_add_u32 s50, s50, 147456\n"
    "v_lshl_add_u32 v20, v20, 2, s50\n"
    "ds_read_b32 v0, v20\n"
    "ds_read_b32 v1, v20 offset:1024\n"
    "ds_read_b32 v2, v20 offset:2048\n"
    "ds_read_b32 v3, v20 offset:3072\n"
    "ds_read_b32 v4, v20 offset:4096\n"
    "ds_read_b32 v5, v20 offset:5120\n"
    "ds_read_b32 v8, v20 offset:6144\n"
    "ds_read_b32 v9, v20 offset:7168\n"
    "ds_read_b32 v10, v20 offset:8192\n"
    "s_waitcnt lgkmcnt(0)\n"
    "s_barrier\n"
    "s_lshl_b32 s54, %2, 10\n"
    "s_mov_b64 s[44:45], %8\n"
    "s_and_b32 s45, s45, 0xffff\n"
    "s_mov_b32 s46, 0xa2000\n"
    "s_mov_b32 s47, 0x20000\n"
    "s_add_u32 m0, s54, 0\n"
    "s_add_u32 s55, %6, 0\n"
    "buffer_load_dwordx4 v8, s[44:47], s55 offen lds\n"
    "s_add_u32 m0, s54, 36864\n"
    "s_add_u32 s55, %6, 64\n"
    "buffer_load_dwordx4 v8, s[44:47], s55 offen lds\n"
    "s_add_u32 m0, s54, 73728\n"
    "s_add_u32 s55, %6, 128\n"
    "buffer_load_dwordx4 v8, s[44:47], s55 offen lds\n"
    "s_add_u32 m0, s54, 4096\n"
    "s_add_u32 s55, %6, 73728\n"
    "buffer_load_dwordx4 v8, s[44:47], s55 offen lds\n"
    "s_add_u32 m0, s54, 40960\n"
    "s_add_u32 s55, %6, 73792\n"
    "buffer_load_dwordx4 v8, s[44:47], s55 offen lds\n"
    "s_add_u32 m0, s54, 77824\n"
    "s_add_u32 s55, %6, 73856\n"
    "buffer_load_dwordx4 v8, s[44:47], s55 offen lds\n"
    "s_add_u32 m0, s54, 8192\n"
    "s_add_u32 s55, %6, 147456\n"
    "buffer_load_dwordx4 v8, s[44:47], s55 offen lds\n"
    "s_add_u32 m0, s54, 45056\n"
    "s_add_u32 s55, %6, 147520\n"
    "buffer_load_dwordx4 v8, s[44:47], s55 offen lds\n"
    "s_add_u32 m0, s54, 81920\n"
    "s_add_u32 s55, %6, 147584\n"
    "buffer_load_dwordx4 v8, s[44:47], s55 offen lds\n"
    "s_add_u32 m0, s54, 12288\n"
    "s_add_u32 s55, %6, 221184\n"
    "buffer_load_dwordx4 v8, s[44:47], s55 offen lds\n"
    "s_add_u32 m0, s54, 49152\n"
    "s_add_u32 s55, %6, 221248\n"
    "buffer_load_dwordx4 v8, s[44:47], s55 offen lds\n"
    "s_add_u32 m0, s54, 86016\n"
    "s_add_u32 s55, %6, 221312\n"
    "buffer_load_dwordx4 v8, s[44:47], s55 offen lds\n"
    "s_add_u32 m0, s54, 16384\n"
    "s_add_u32 s55, %6, 294912\n"
    "buffer_load_dwordx4 v8, s[44:47], s55 offen lds\n"
    "s_add_u32 m0, s54, 53248\n"
    "s_add_u32 s55, %6, 294976\n"
    "buffer_load_dwordx4 v8, s[44:47], s55 offen lds\n"
    "s_add_u32 m0, s54, 90112\n"
    "s_add_u32 s55, %6, 295040\n"
    "buffer_load_dwordx4 v8, s[44:47], s55 offen lds\n"
    "s_add_u32 m0, s54, 20480\n"
    "s_add_u32 s55, %6, 368640\n"
    "buffer_load_dwordx4 v8, s[44:47], s55 offen lds\n"
    "s_add_u32 m0, s54, 57344\n"
    "s_add_u32 s55, %6, 368704\n"
    "buffer_load_dwordx4 v8, s[44:47], s55 offen lds\n"
    "s_add_u32 m0, s54, 94208\n"
    "s_add_u32 s55, %6, 368768\n"
    "buffer_load_dwordx4 v8, s[44:47], s55 offen lds\n"
    "s_add_u32 m0, s54, 24576\n"
    "s_add_u32 s55, %6, 442368\n"
    "buffer_load_dwordx4 v8, s[44:47], s55 offen lds\n"
    "s_add_u32 m0, s54, 61440\n"
    "s_add_u32 s55, %6, 442432\n"
    "buffer_load_dwordx4 v8, s[44:47], s55 offen lds\n"
    "s_add_u32 m0, s54, 98304\n"
    "s_add_u32 s55, %6, 442496\n"
    "buffer_load_dwordx4 v8, s[44:47], s55 offen lds\n"
    "s_add_u32 m0, s54, 28672\n"
    "s_add_u32 s55, %6, 516096\n"
    "buffer_load_dwordx4 v8, s[44:47], s55 offen lds\n"
    "s_add_u32 m0, s54, 65536\n"
    "s_add_u32 s55, %6, 516160\n"
    "buffer_load_dwordx4 v8, s[44:47], s55 offen lds\n"
    "s_add_u32 m0, s54, 102400\n"
    "s_add_u32 s55, %6, 516224\n"
    "buffer_load_dwordx4 v8, s[44:47], s55 offen lds\n"
    "s_add_u32 m0, s54, 32768\n"
    "s_add_u32 s55, %6, 589824\n"
    "buffer_load_dwordx4 v8, s[44:47], s55 offen lds\n"
    "s_add_u32 m0, s54, 69632\n"
    "s_add_u32 s55, %6, 589888\n"
    "buffer_load_dwordx4 v8, s[44:47], s55 offen lds\n"
    "s_add_u32 m0, s54, 106496\n"
    "s_add_u32 s55, %6, 589952\n"
    "buffer_load_dwordx4 v8, s[44:47], s55 offen lds\n"
    "v_mov_b32 v7, 0x20400\n"
    "global_load_dwordx2 v[20:21], v7, %1 sc1\n"
    "v_mbcnt_lo_u32_b32 v6, -1, 0\n"
    "v_min_u32 v6, 7, v6\n"
    "v_lshlrev_b32 v6, 7, v6\n"
    "v_add_u32 v6, 0x20000, v6\n"
    "global_load_dwordx2 v[22:23], v6, %1 sc1\n"
    "s_waitcnt vmcnt(2) lgkmcnt(0)\n"
    "s_barrier\n"
    "ds_read_b64_tr_b16 a[0:1], v9\n"
    "ds_read_b64_tr_b16 a[2:3], v9 offset:256\n"
    "ds_read_b64_tr_b16 a[4:5], v9 offset:8\n"
    "ds_read_b64_tr_b16 a[6:7], v9 offset:264\n"
    "ds_read_b64_tr_b16 a[8:9], v9 offset:2304\n"
    "ds_read_b64_tr_b16 a[10:11], v9 offset:2560\n"
    "ds_read_b64_tr_b16 a[12:13], v9 offset:2312\n"
    "ds_read_b64_tr_b16 a[14:15], v9 offset:2568\n"
    "ds_read_b64_tr_b16 a[16:17], v9 offset:4608\n"
    "ds_read_b64_tr_b16 a[18:19], v9 offset:4864\n"
    "ds_read_b64_tr_b16 a[20:21], v9 offset:4616\n"
    "ds_read_b64_tr_b16 a[22:23], v9 offset:4872\n"
    "ds_read_b64_tr_b16 a[24:25], v9 offset:6912\n"
    "ds_read_b64_tr_b16 a[26:27], v9 offset:7168\n"
    "ds_read_b64_tr_b16 a[28:29], v9 offset:6920\n"
    "ds_read_b64_tr_b16 a[30:31], v9 offset:7176\n"
    "ds_read_b64_tr_b16 a[32:33], v9 offset:9216\n"
    "ds_read_b64_tr_b16 a[34:35], v9 offset:9472\n"
    "ds_read_b64_tr_b16 a[36:37], v9 offset:9224\n"
    "ds_read_b64_tr_b16 a[38:39], v9 offset:9480\n"
    "ds_read_b64_tr_b16 a[40:41], v9 offset:11520\n"
    "ds_read_b64_tr_b16 a[42:43], v9 offset:11776\n"
    "ds_read_b64_tr_b16 a[44:45], v9 offset:11528\n"
    "ds_read_b64_tr_b16 a[46:47], v9 offset:11784\n"
    "ds_read_b64_tr_b16 a[48:49], v9 offset:13824\n"
    "ds_read_b64_tr_b16 a[50:51], v9 offset:14080\n"
    "ds_read_b64_tr_b16 a[52:53], v9 offset:13832\n"
    "ds_read_b64_tr_b16 a[54:55], v9 offset:14088\n"
    "ds_read_b64_tr_b16 a[56:57], v9 offset:16128\n"
    "ds_read_b64_tr_b16 a[58:59], v9 offset:16384\n"
    "ds_read_b64_tr_b16 a[60:61], v9 offset:16136\n"
    "ds_read_b64_tr_b16 a[62:63], v9 offset:16392\n"
    "ds_read_b64_tr_b16 a[64:65], v9 offset:18432\n"
    "ds_read_b64_tr_b16 a[66:67], v9 offset:18688\n"
    "ds_read_b64_tr_b16 a[68:69], v9 offset:18440\n"
    "ds_read_b64_tr_b16 a[70:71], v9 offset:18696\n"
    "ds_read_b64_tr_b16 a[72:73], v9 offset:20736\n"
    "ds_read_b64_tr_b16 a[74:75], v9 offset:20992\n"
    "ds_read_b64_tr_b16 a[76:77], v9 offset:20744\n"
    "ds_read_b64_tr_b16 a[78:79], v9 offset:21000\n"
    "ds_read_b64_tr_b16 a[80:81], v9 offset:23040\n"
    "ds_read_b64_tr_b16 a[82:83], v9 offset:23296\n"
    "ds_read_b64_tr_b16 a[84:85], v9 offset:23048\n"
    "ds_read_b64_tr_b16 a[86:87], v9 offset:23304\n"
    "ds_read_b64_tr_b16 a[88:89], v9 offset:25344\n"
    "ds_read_b64_tr_b16 a[90:91], v9 offset:25600\n"
    "ds_read_b64_tr_b16 a[92:93], v9 offset:25352\n"
    "ds_read_b64_tr_b16 a[94:95], v9 offset:25608\n"
    "ds_read_b64_tr_b16 a[96:97], v9 offset:27648\n"
    "ds_read_b64_tr_b16 a[98:99], v9 offset:27904\n"
    "ds_read_b64_tr_b16 a[100:101], v9 offset:27656\n"
    "ds_read_b64_tr_b16 a[102:103], v9 offset:27912\n"
    "ds_read_b64_tr_b16 a[104:105], v9 offset:29952\n"
    "ds_read_b64_tr_b16 a[106:107], v9 offset:30208\n"
    "ds_read_b64_tr_b16 a[108:109], v9 offset:29960\n"
    "ds_read_b64_tr_b16 a[110:111], v9 offset:30216\n"
    "ds_read_b64_tr_b16 a[112:113], v9 offset:32256\n"
    "ds_read_b64_tr_b16 a[114:115], v9 offset:32512\n"
    "ds_read_b64_tr_b16 a[116:117], v9 offset:32264\n"
    "ds_read_b64_tr_b16 a[118:119], v9 offset:32520\n"
    "ds_read_b64_tr_b16 a[120:121], v9 offset:34560\n"
    "ds_read_b64_tr_b16 a[122:123], v9 offset:34816\n"
    "ds_read_b64_tr_b16 a[124:125], v9 offset:34568\n"
    "ds_read_b64_tr_b16 a[126:127], v9 offset:34824\n"
    "ds_read_b64_tr_b16 a[128:129], v10\n"
    "ds_read_b64_tr_b16 a[130:131], v10 offset:2304\n"
    "ds_read_b64_tr_b16 a[132:133], v10 offset:8\n"
    "ds_read_b64_tr_b16 a[134:135], v10 offset:2312\n"
    "ds_read_b64_tr_b16 a[136:137], v10 offset:18432\n"
    "ds_read_b64_tr_b16 a[138:139], v10 offset:20736\n"
    "ds_read_b64_tr_b16 a[140:141], v10 offset:18440\n"
    "ds_read_b64_tr_b16 a[142:143], v10 offset:20744\n"
    "s_waitcnt lgkmcnt(0)\n"
    "s_barrier\n"
    "s_waitcnt vmcnt(0)\n"
    "v_add_u32 v20, 1, v20\n"
    "v_add_u32 v21, 1, v21\n"
    "s_mov_b32 s52, 1048576\n"
    ".Lbgo_a%=:\n"
    "v_cmp_ne_u64 vcc, v[20:21], v[22:23]\n"
    "s_cbranch_vccz .Lbgo_x%=\n"
    "global_load_dwordx2 v[22:23], v6, %1 sc1\n"
    "s_sub_u32 s52, s52, 1\n"
    "s_waitcnt vmcnt(0)\n"
    "s_cbranch_scc0 .Lbgo_a%=\n"
    ".Lbgo_x%=:\n"
    "v_mbcnt_lo_u32_b32 v20, -1, 0\n"
    "v_mbcnt_hi_u32_b32 v20, -1, v20\n"
    "v_min_u32 v6, 7, v20\n"
    "v_lshlrev_b32 v6, 7, v6\n"
    "v_mov_b32 v7, 0x26c00\n"
    "ds_read_b128 v[20:23], v7\n"
    "s_waitcnt lgkmcnt(0)\n"
    "v_readfirstlane_b32 s49, v21\n"
    "v_add_u32 v20, v20, v6\n"
    "s_branch .Lbchk_%=\n"
    ".Lbgo_%=:\n"
    "s_waitcnt vmcnt(1)\n"
    "v_add_u32 v7, 32, v7\n"
    "ds_read_b128 v[20:23], v7\n"
    "ds_write_b128 v1, v[100:103]\n"
    "ds_write_b128 v1, v[104:107] offset:1024\n"
    "ds_write_b128 v1, v[108:111] offset:2048\n"
    "ds_write_b128 v1, v[112:115] offset:3072\n"
    "ds_write_b32 v4, v116\n"
    "ds_write_b32 v4, v117 offset:16\n"
    "ds_write_b32 v4, v118 offset:32\n"
    "ds_write_b32 v4, v119 offset:48\n"
    "v_lshlrev_b32 v8, 16, v120\n"
    "v_and_b32 v9, 0xffff0000, v120\n"
    "v_lshlrev_b32 v10, 16, v121\n"
    "v_and_b32 v11, 0xffff0000, v121\n"
    "v_lshlrev_b32 v12, 16, v122\n"
    "v_and_b32 v13, 0xffff0000, v122\n"
    "v_lshlrev_b32 v14, 16, v123\n"
    "v_and_b32 v15, 0xffff0000, v123\n"
    "v_xor_b32 v0, 0x8000, v0\n"
    "v_xor_b32 v1, 0x8000, v1\n"
    "v_xor_b32 v4, 0x8000, v4\n"
    "s_waitcnt lgkmcnt(8)\n"
    "v_readfirstlane_b32 s49, v21\n"
    "v_readfirstlane_b32 s62, v22\n"
    "v_add_u32 v20, v20, v6\n"
    "s_waitcnt lgkmcnt(0)\n"
    ".Lbtop_%=:\n"
    "s_waitcnt lgkmcnt(0)\n"
    "s_barrier\n"
    "ds_read_b128 v[24:27], v0\n"
    "ds_read_b128 v[28:31], v0 offset:1024\n"
    "ds_read_b128 v[36:39], v0 offset:3072\n"
    "ds_read_b128 v[32:35], v0 offset:2048\n"
    "ds_read_b128 v[44:47], v0 offset:5120\n"
    "ds_read_b128 v[40:43], v0 offset:4096\n"
    "global_load_dword v21, v20, %1 sc1\n"
    "s_waitcnt lgkmcnt(5)\n"
    "v_mfma_f32_16x16x32_bf16 v[8:11], a[0:3], v[24:27], v[8:11]\n"
    "ds_read_b128 v[52:55], v0 offset:7168\n"
    "ds_read_b128 v[48:51], v0 offset:6144\n"
    "v_mfma_f32_16x16x32_bf16 v[12:15], a[4:7], v[24:27], v[12:15]\n"
    "ds_read_b128 v[60:63], v0 offset:9216\n"
    "ds_read_b128 v[56:59], v0 offset:8192\n"
    "s_waitcnt lgkmcnt(8)\n"
    "v_mfma_f32_16x16x32_bf16 v[8:11], a[8:11], v[28:31], v[8:11]\n"
    "ds_read_b128 v[68:71], v0 offset:11264\n"
    "ds_read_b128 v[64:67], v0 offset:10240\n"
    "v_mfma_f32_16x16x32_bf16 v[12:15], a[12:15], v[28:31], v[12:15]\n"
    "ds_read_b128 v[76:79], v0 offset:13312\n"
    "ds_read_b128 v[72:75], v0 offset:12288\n"
    "s_waitcnt lgkmcnt(10)\n"
    "v_mfma_f32_16x16x32_bf16 v[8:11], a[16:19], v[32:35], v[8:11]\n"
    "ds_read_b128 v[84:87], v0 offset:15360\n"
    "ds_read_b128 v[80:83], v0 offset:14336\n"
    "v_mfma_f32_16x16x32_bf16 v[12:15], a[20:23], v[32:35], v[12:15]\n"
    "ds_read_b128 v[92:95], v0 offset:17408\n"
    "ds_read_b128 v[88:91], v0 offset:16384\n"
    "v_mfma_f32_16x16x32_bf16 v[8:11], a[24:27], v[36:39], v[8:11]\n"
    "v_mfma_f32_16x16x32_bf16 v[12:15], a[28:31], v[36:39], v[12:15]\n"
    "s_waitcnt lgkmcnt(12)\n"
    "v_mfma_f32_16x16x32_bf16 v[8:11], a[32:35], v[40:43], v[8:11]\n"
    "v_mfma_f32_16x16x32_bf16 v[12:15], a[36:39], v[40:43], v[12:15]\n"
    "v_mfma_f32_16x16x32_bf16 v[8:11], a[40:43], v[44:47], v[8:11]\n"
    "v_mfma_f32_16x16x32_bf16 v[12:15], a[44:47], v[44:47], v[12:15]\n"
    "s_waitcnt lgkmcnt(10)\n"
    "v_mfma_f32_16x16x32_bf16 v[8:11], a[48:51], v[48:51], v[8:11]\n"
    "v_mfma_f32_16x16x32_bf16 v[12:15], a[52:55], v[48:51], v[12:15]\n"
    "v_mfma_f32_16x16x32_bf16 v[8:11], a[56:59], v[52:55], v[8:11]\n"
    "v_mfma_f32_16x16x32_bf16 v[12:15], a[60:63], v[52:55], v[12:15]\n"
    "s_waitcnt lgkmcnt(8)\n"
    "v_mfma_f32_16x16x32_bf16 v[8:11], a[64:67], v[56:59], v[8:11]\n"
    "v_mfma_f32_16x16x32_bf16 v[12:15], a[68:71], v[56:59], v[12:15]\n"
    "v_mfma_f32_16x16x32_bf16 v[8:11], a[72:75], v[60:63], v[8:11]\n"
    "v_mfma_f32_16x16x32_bf16 v[12:15], a[76:79], v[60:63], v[12:15]\n"
    "s_waitcnt lgkmcnt(6)\n"
    "v_mfma_f32_16x16x32_bf16 v[8:11], a[80:83], v[64:67], v[8:11]\n"
    "v_mfma_f32_16x16x32_bf16 v[12:15], a[84:87], v[64:67], v[12:15]\n"
    "v_mfma_f32_16x16x32_bf16 v[8:11], a[88:91], v[68:71], v[8:11]\n"
    "v_mfma_f32_16x16x32_bf16 v[12:15], a[92:95], v[68:71], v[12:15]\n"
    "s_waitcnt lgkmcnt(4)\n"
    "v_mfma_f32_16x16x32_bf16 v[8:11], a[96:99], v[72:75], v[8:11]\n"
    "v_mfma_f32_16x16x32_bf16 v[12:15], a[100:103], v[72:75], v[12:15]\n"
    "v_mfma_f32_16x16x32_bf16 v[8:11], a[104:107], v[76:79], v[8:11]\n"
    "v_mfma_f32_16x16x32_bf16 v[12:15], a[108:111], v[76:79], v[12:15]\n"
    "s_waitcnt lgkmcnt(2)\n"
    "v_mfma_f32_16x16x32_bf16 v[8:11], a[112:115], v[80:83], v[8:11]\n"
    "v_mfma_f32_16x16x32_bf16 v[12:15], a[116:119], v[80:83], v[12:15]\n"
    "v_mfma_f32_16x16x32_bf16 v[8:11], a[120:123], v[84:87], v[8:11]\n"
    "v_mfma_f32_16x16x32_bf16 v[12:15], a[124:127], v[84:87], v[12:15]\n"
    "s_waitcnt lgkmcnt(0)\n"
    "v_mfma_f32_16x16x32_bf16 v[8:11], a[128:131], v[88:91], v[8:11]\n"
    "v_mfma_f32_16x16x32_bf16 v[12:15], a[132:135], v[88:91], v[12:15]\n"
    "v_mfma_f32_16x16x32_bf16 v[8:11], a[136:139], v[92:95], v[8:11]\n"
    "v_mfma_f32_16x16x32_bf16 v[12:15], a[140:143], v[92:95], v[12:15]\n"
    "s_nop 4\n"
    "v_cvt_pk_bf16_f32 v16, v8, v9\n"
    "v_cvt_pk_bf16_f32 v17, v10, v11\n"
    "s_nop 1\n"
    "v_cvt_pk_bf16_f32 v18, v12, v13\n"
    "v_cvt_pk_bf16_f32 v19, v14, v15\n"
    "buffer_store_dwordx4 v[16:19], v3, s[40:43], s62 offen sc1\n"
    "s_mov_b32 s56, 1\n"
    "s_waitcnt vmcnt(1)\n"
    "v_cmp_eq_u32 vcc, 0, v21\n"
    "s_cmp_eq_u64 vcc, 0\n"
    "ds_read_b32 v23, v7 offset:44\n"
    "s_cbranch_scc0 .Lbno_%=\n"
    "buffer_load_dwordx4 v[100:103], v2, s[92:95], s49 offen sc1\n"
    "buffer_load_dwordx4 v[104:107], v2, s[92:95], s49 offen offset:1024 sc1\n"
    "buffer_load_dwordx4 v[108:111], v2, s[92:95], s49 offen offset:2048 sc1\n"
    "buffer_load_dwordx4 v[112:115], v2, s[92:95], s49 offen offset:3072 sc1\n"
    "buffer_load_dwordx4 v[116:119], v5, s[92:95], s49 offen sc1\n"
    "s_sub_u32 s55, s49, 0x1000000\n"
    "buffer_load_dwordx4 v[120:123], v3, s[40:43], s55 offen sc1\n"
    "s_waitcnt vmcnt(6)\n"
    "v_mov_b32 v22, 1\n"
    "s_mov_b64 exec, 1\n"
    "ds_add_rtn_u32 v22, v7, v22 offset:48\n"
    "s_mov_b64 exec, -1\n"
    "s_waitcnt lgkmcnt(0)\n"
    "v_readfirstlane_b32 s50, v22\n"
    "s_cmp_eq_u32 s50, 3\n"
    "s_cselect_b64 exec, 1, 0\n"
    "global_store_dword v23, v22, %1 sc1\n"
    "s_mov_b64 exec, -1\n"
    "s_branch .Lbin_%=\n"
    ".Lbno_%=:\n"
    "global_load_dword v21, v20, %1 sc1\n"
    "s_waitcnt vmcnt(0)\n"
    "v_mov_b32 v22, 1\n"
    "s_mov_b64 exec, 1\n"
    "ds_add_rtn_u32 v22, v7, v22 offset:48\n"
    "s_mov_b64 exec, -1\n"
    "s_waitcnt lgkmcnt(0)\n"
    "v_readfirstlane_b32 s50, v22\n"
    "s_cmp_eq_u32 s50, 3\n"
    "s_cselect_b64 exec, 1, 0\n"
    "global_store_dword v23, v22, %1 sc1\n"
    "s_mov_b64 exec, -1\n"
    "s_mov_b32 s56, 0\n"
    "v_cmp_eq_u32 vcc, 0, v21\n"
    "s_cmp_eq_u64 vcc, 0\n"
    "s_cbranch_scc1 .Lbld_%=\n"
    ".Lbchk_%=:\n"
    "v_readfirstlane_b32 s50, v20\n"
    "s_cmp_lt_u32 s50, 200\n"
    "s_cbranch_scc1 .Lbend_%=\n"
    ".Lbwait_%=:\n"
    "global_load_dword v21, v20, %1 sc1\n"
    "s_mov_b32 s52, 0\n"
    ".Lbp1_%=:\n"
    "s_waitcnt vmcnt(0)\n"
    "v_cmp_eq_u32 vcc, 0, v21\n"
    "s_cmp_eq_u64 vcc, 0\n"
    "s_cbranch_scc1 .Lbp1_x%=\n"
    "s_add_u32 s52, s52, 1\n"
    "s_sleep 2\n"
    "global_load_dword v21, v20, %1 sc1\n"
    "s_cmp_lt_u32 s52, s53\n"
    "s_cbranch_scc1 .Lbp1_%=\n"
    "s_mov_b32 s53, 0\n"
    "v_mov_b32 v22, 0\n"
    "v_mov_b32 v23, 1\n"
    "global_store_dword v22, v23, %1 sc1\n"
    "s_waitcnt vmcnt(0)\n"
    ".Lbp1_x%=:\n"
    ".Lbld_%=:\n"
    "s_mov_b32 s94, s42\n"
    "buffer_load_dwordx4 v[100:103], v2, s[92:95], s49 offen sc1\n"
    "buffer_load_dwordx4 v[104:107], v2, s[92:95], s49 offen offset:1024 sc1\n"
    "buffer_load_dwordx4 v[108:111], v2, s[92:95], s49 offen offset:2048 sc1\n"
    "buffer_load_dwordx4 v[112:115], v2, s[92:95], s49 offen offset:3072 sc1\n"
    "buffer_load_dwordx4 v[116:119], v5, s[92:95], s49 offen sc1\n"
    "s_sub_u32 s55, s49, 0x1000000\n"
    "buffer_load_dwordx4 v[120:123], v3, s[40:43], s55 offen sc1\n"
    ".Lbin_%=:\n"
    "v_add_u32 v7, 32, v7\n"
    "ds_read_b128 v[20:23], v7\n"
    "s_waitcnt vmcnt(5)\n"
    "ds_write_b128 v1, v[100:103]\n"
    "s_waitcnt vmcnt(4)\n"
    "ds_write_b128 v1, v[104:107] offset:1024\n"
    "s_waitcnt vmcnt(3)\n"
    "ds_write_b128 v1, v[108:111] offset:2048\n"
    "s_waitcnt vmcnt(2)\n"
    "ds_write_b128 v1, v[112:115] offset:3072\n"
    "s_waitcnt vmcnt(1)\n"
    "ds_write_b32 v4, v116\n"
    "ds_write_b32 v4, v117 offset:16\n"
    "ds_write_b32 v4, v118 offset:32\n"
    "ds_write_b32 v4, v119 offset:48\n"
    "s_waitcnt vmcnt(0)\n"
    "v_lshlrev_b32 v8, 16, v120\n"
    "v_and_b32 v9, 0xffff0000, v120\n"
    "v_lshlrev_b32 v10, 16, v121\n"
    "v_and_b32 v11, 0xffff0000, v121\n"
    "v_lshlrev_b32 v12, 16, v122\n"
    "v_and_b32 v13, 0xffff0000, v122\n"
    "v_lshlrev_b32 v14, 16, v123\n"
    "v_and_b32 v15, 0xffff0000, v123\n"
    "v_xor_b32 v0, 0x8000, v0\n"
    "v_xor_b32 v1, 0x8000, v1\n"
    "v_xor_b32 v4, 0x8000, v4\n"
    "s_waitcnt lgkmcnt(8)\n"
    "v_readfirstlane_b32 s49, v21\n"
    "v_readfirstlane_b32 s62, v22\n"
    "v_add_u32 v20, v20, v6\n"
    "s_waitcnt lgkmcnt(0)\n"
    "s_branch .Lbtop_%=\n"
    ".Lbpol_%=:\n"
    "v_mov_b32 v20, 64\n"
    "global_load_dword v21, v20, %1 sc1\n"
    "s_mov_b32 s52, 0\n"
    ".Lbp2_%=:\n"
    "s_waitcnt vmcnt(0)\n"
    "v_cmp_eq_u32 vcc, 0, v21\n"
    "s_cmp_eq_u64 vcc, 0\n"
    "s_cbranch_scc1 .Lbp2_x%=\n"
    "s_add_u32 s52, s52, 1\n"
    "s_sleep 2\n"
    "global_load_dword v21, v20, %1 sc1\n"
    "s_cmp_lt_u32 s52, s53\n"
    "s_cbranch_scc1 .Lbp2_%=\n"
    "s_mov_b32 s53, 0\n"
    "v_mov_b32 v22, 0\n"
    "v_mov_b32 v23, 1\n"
    "global_store_dword v22, v23, %1 sc1\n"
    "s_waitcnt vmcnt(0)\n"
    ".Lbp2_x%=:\n"
    "v_readfirstlane_b32 s50, v21\n"
    "v_mbcnt_lo_u32_b32 v20, -1, 0\n"
    "v_mbcnt_hi_u32_b32 v20, -1, v20\n"
    "s_lshl_b32 s51, %2, 6\n"
    "v_add_u32 v20, s51, v20\n"
    "v_min_u32 v20, 129, v20\n"
    "v_lshlrev_b32 v20, 5, v20\n"
    "v_add_u32 v20, 0x26c00, v20\n"
    "ds_read2_b32 v[22:23], v20 offset0:1 offset1:2\n"
    "s_and_b32 s50, s50, 1\n"
    "s_waitcnt lgkmcnt(0)\n"
    "v_and_b32 v21, s50, v22\n"
    "v_lshlrev_b32 v21, 13, v21\n"
    "v_and_b32 v22, -2, v22\n"
    "v_xor_b32 v22, v21, v22\n"
    "v_and_b32 v21, s50, v23\n"
    "v_lshlrev_b32 v21, 13, v21\n"
    "v_and_b32 v23, -2, v23\n"
    "v_xor_b32 v23, v21, v23\n"
    "ds_write2_b32 v20, v22, v23 offset0:1 offset1:2\n"
    "s_waitcnt lgkmcnt(0)\n"
    "s_barrier\n"
    "ds_read_b32 v21, v7 offset:4\n"
    "v_add_u32 v20, %4, v6\n"
    "s_waitcnt lgkmcnt(0)\n"
    "v_readfirstlane_b32 s49, v21\n"
    "s_branch .Lbwait_%=\n"
    ".Lbend_%=:\n"
    "s_cmp_eq_u32 s50, 192\n"
    "s_cbranch_scc0 .Lbpol_%=\n"
    "s_waitcnt vmcnt(0) lgkmcnt(0)\n"
    ".Lrole5_%=:\n"
    ".Lfin:\n"
        :: "s"(wd), "s"(ws), "s"(wave), "s"(rid), "s"(P.h), "s"(P.p), "s"(P.woff), "s"(layer), "s"(w0), "s"(Wk), "s"(Wv), "s"(Wo),
           "s"(mem0), "s"(P.role) : CLOBBERS);
}

extern "C" void kernel_launch(void* const* d_in, const int* in_sizes, int n_in, void* d_out, int out_size, void* d_ws,
                              size_t ws_size, hipStream_t stream) {
    mega_kernel<<<256, 256, 0, stream>>>((const u16*)d_in[0], (const u16*)d_in[1], (const u16*)d_in[2], (const u16*)d_in[4],
                                         (const u16*)d_in[5], (const u16*)d_in[6], (const u16*)d_in[7], (const u16*)d_in[8],
                                         (u16*)d_out, (char*)d_ws);
}
```

```cpp
#include <hip/hip_runtime.h>
#include <hip/hip_bf16.h>
#include <stdint.h>

typedef unsigned short u16;
typedef unsigned u32;
typedef unsigned long long u64;
using bf16x8 = __attribute__((ext_vector_type(8))) short;
using bf16x4 = __attribute__((ext_vector_type(4))) short;
using f32x4 = __attribute__((ext_vector_type(4))) float;
using f32x2 = __attribute__((ext_vector_type(2))) float;
using v4u = __attribute__((ext_vector_type(4))) unsigned;
using v2u = __attribute__((ext_vector_type(2))) unsigned;
typedef __bf16 bfv2 __attribute__((ext_vector_type(2)));
#define GAS __attribute__((address_space(1)))
typedef GAS unsigned gu32;
typedef GAS u64 gu64;
#define RLX_AGENT __ATOMIC_RELAXED, __HIP_MEMORY_SCOPE_AGENT
typedef __amdgpu_buffer_rsrc_t rsrc_t;

constexpr int S_ = 2048, D_ = 576, NSEG = 64, SEG = 32, NB = 4096, NQA = 1728;

constexpr u32 OFF_ABORT = 0;
constexpr u32 OFF_POL = 64;
constexpr u32 OFF_RDY = 128;
constexpr u32 OFF_NEVER = 192;
constexpr u32 OFF_FA = 4096;
constexpr u32 OFF_FO = OFF_FA + 8192;
constexpr u32 OFF_FB = OFF_FO + 8192;
constexpr u32 OFF_FP = OFF_FB + 8192;
constexpr u32 OFF_COB = OFF_FP + 24576;
constexpr u32 OFF_FKV = OFF_COB + 32768;
constexpr u32 OFF_GO = 131072;
constexpr u32 OFF_EP = OFF_GO + 1024;
constexpr u32 SYNC_BYTES = OFF_EP + 128;
constexpr u32 MB2 = 2u << 20;
constexpr u32 OFF_XA = 0;
constexpr u32 OFF_XB = MB2;
constexpr u32 XB = S_ * D_ * 2;
constexpr u32 OFF_TA = 4 * MB2;
constexpr u32 OFF_TB = 8 * MB2;
constexpr u32 OFF_OA = 12 * MB2;
constexpr u32 OFF_PB = 16 * MB2;
constexpr u32 HB_ = 2 * 64 * 32 * 8192;
constexpr u32 OFF_QX = 32 * MB2;
constexpr u32 OFF_KX = OFF_QX + HB_;
constexpr u32 OFF_VX = OFF_KX + HB_;
constexpr u32 OFF_OX = OFF_VX + HB_;
constexpr u32 WS_BYTES = OFF_OX + HB_;
constexpr u32 L_TAB = 147456, L_IT = L_TAB + 11264, L_EX = 138240, L_W = 32064;

#define STAMP(i, k) do {} while (0)
#define PH(k) do {} while (0)
__device__ __forceinline__ float bf2f(u16 v) { return __uint_as_float(((u32)v) << 16); }
__device__ __forceinline__ float bflo(u32 v) { return __uint_as_float(v << 16); }
__device__ __forceinline__ float bfhi(u32 v) { return __uint_as_float(v & 0xffff0000u); }
__device__ __forceinline__ u32 pack2(float a, float b) { bfv2 v = {(__bf16)a, (__bf16)b}; return __builtin_bit_cast(u32, v); }
__device__ __forceinline__ float rbf(float f) { return (float)(__bf16)f; }
__device__ __forceinline__ v4u ld16(rsrc_t r, u32 off) { return __builtin_amdgcn_raw_buffer_load_b128(r, off, 0, 16); }
__device__ __forceinline__ v2u ld8(rsrc_t r, u32 off) { return __builtin_amdgcn_raw_buffer_load_b64(r, off, 0, 16); }
__device__ __forceinline__ void st16(rsrc_t r, u32 off, v4u v) { __builtin_amdgcn_raw_buffer_store_b128(v, r, off, 0, 16); }
__device__ __forceinline__ void st8(rsrc_t r, u32 off, v2u v) { __builtin_amdgcn_raw_buffer_store_b64(v, r, off, 0, 16); }
__device__ __forceinline__ void st4(rsrc_t r, u32 off, u32 v) { __builtin_amdgcn_raw_buffer_store_b32(v, r, off, 0, 16); }
#define DRAIN() asm volatile("s_waitcnt vmcnt(0)" ::: "memory")
#define TOUCH(p) asm volatile("global_load_dword v255, %0, off" :: "v"(p) : "v255")
#define CBAR() asm volatile("" ::: "memory")
__device__ __forceinline__ u32 ldw(char* ws, u32 off) { return __hip_atomic_load((gu32*)(ws + off), RLX_AGENT); }
__device__ __forceinline__ void setw(char* ws, u32 off) { __hip_atomic_store((gu32*)(ws + off), 1u, RLX_AGENT); }
__device__ __forceinline__ u32 fl(u32 i) { return (i >> 5) * 1024 + (i & 31) * 4; }
constexpr u32 SPIN_CAP = 1u << 20;
#define SLP_W 2
#define SLP_C 8
__device__ __forceinline__ void until(u64 t) { while (__builtin_amdgcn_s_memrealtime() < t) __builtin_amdgcn_s_sleep(8); }
#define TDEL 0
__device__ __forceinline__ u64 succ(char* ws) {
    const u64 e = __hip_atomic_load((gu64*)(ws + OFF_EP), RLX_AGENT);
    return (u64)((u32)(e >> 32) + 1) << 32 | ((u32)e + 1);
}
__device__ __forceinline__ void wait_go(char* ws) {
    const u64 e = succ(ws);
    for (u32 n = 0; n < SPIN_CAP && __builtin_amdgcn_ballot_w64(__hip_atomic_load((gu64*)(ws + OFF_GO + (threadIdx.x & 7) * 128), RLX_AGENT) != e); ++n) __builtin_amdgcn_s_sleep(2);
    CBAR();
}
__device__ __forceinline__ bool spin_fail(char* ws, u32& spins) {
    ++spins;
    if ((spins & 63u) == 0u) {
        if (ldw(ws, OFF_ABORT)) return true;
        if (spins > SPIN_CAP) { __hip_atomic_store((gu32*)(ws + OFF_ABORT), 1u, RLX_AGENT); return true; }
    }
    return false;
}
__device__ __forceinline__ void wait_set(char* ws, u32 off) {
    u32 spins = 0;
    while (__builtin_amdgcn_ballot_w64(ldw(ws, off) == 0)) {
        if (spin_fail(ws, spins)) break;
        __builtin_amdgcn_s_sleep(SLP_W);
    }
    CBAR();
}
constexpr u32 OOB = 0x80000000u;
__device__ __forceinline__ u32 cgo(u32 cg) { return (cg >> 5) * 16384 + (cg & 31) * 256; }

template <int N, bool FIN>
__device__ __forceinline__ void t_item(int rid, u32 i, u32 j, u32 m, u32 pol, u32 w, rsrc_t ro, char* ws, rsrc_t rs) {
    const u32 lane = threadIdx.x & 63, s = i & 63, side = (((s >> 5) ^ pol ^ m) & 1) << 13;
    v4u x[N], p[N][4];
    u32 v[N];
    wait_set(ws, OFF_FB + m * 4096 + j * 128 + fl(i));
#pragma unroll
    for (int u = 0; u < N; ++u) { v[u] = cgo(12 * j + 4 * (w + u) + (lane >> 4)) + (lane & 15) * 16; x[u] = ld16(rs, OFF_TB + (i << 16) + (FIN ? side : m << 13) + v[u]); }
    wait_set(ws, OFF_FP + (m * 3 + (j >> 1)) * 4096 + (lane & 3) * 128 + fl(i));
    STAMP(i, 0);
#pragma unroll
    for (int u = 0; u < N; ++u)
#pragma unroll
        for (int k = 0; k < 4; ++k) p[u][k] = __builtin_amdgcn_raw_buffer_load_b128(rs, v[u], OFF_PB + (i << 18) + (k << 16) + ((((i >> 3) ^ pol ^ m) & 1) << 13), 16);
#pragma unroll
    for (int u = 0; u < N; ++u) {
#pragma unroll
        for (int e = 0; e < 4; ++e) {
            f32x2 a = {bflo(x[u][e]), bfhi(x[u][e])};
#pragma unroll
            for (int k = 0; k < 4; ++k) a += f32x2{bflo(p[u][k][e]), bfhi(p[u][k][e])};
            x[u][e] = pack2(a[0], a[1]);
        }
        if (FIN) __builtin_amdgcn_raw_buffer_store_b128(x[u], ro, (s * 32 + 2 * (lane & 15) + m) * 1152 + (12 * j + 4 * (w + u) + (lane >> 4)) * 16, 0, 0);
        else st16(rs, OFF_TA + ((64 + s) << 16) + side + v[u], x[u]);
    }
    STAMP(i, 1);
}
__device__ __forceinline__ bool t_last(u32* cnt) {
    DRAIN();
    u32 o = 0;
    if ((threadIdx.x & 63) == 0) o = __hip_atomic_fetch_add(cnt, 1u, __ATOMIC_RELAXED, __HIP_MEMORY_SCOPE_WORKGROUP);
    return __builtin_amdgcn_readfirstlane(o) == 2;
}
__device__ __forceinline__ void role_t(u64 t_in, int rid, u32 j, u32 m, const u16* x0, u16* out, char* ws, rsrc_t rs, char* smem) {
    const u32 lane = threadIdx.x & 63, w = __builtin_amdgcn_readfirstlane(threadIdx.x >> 6);
    u32* cnt = (u32*)smem + 64;
    cnt[threadIdx.x] = 0;
    __syncthreads();
    const rsrc_t ro = __builtin_amdgcn_make_buffer_rsrc(out, 0, XB, 0x00020000);
    if (w < 3) {
        const rsrc_t rx = __builtin_amdgcn_make_buffer_rsrc((void*)x0, 0, XB, 0x00020000);
        const u32 cg = 12 * j + 4 * w + (lane >> 4), vx = (2 * (lane & 15) + m) * 1152 + cg * 16, v = OFF_TA + (m << 13) + cgo(cg) + (lane & 15) * 16;
#pragma unroll 1
        for (u32 s0 = 0; s0 < 64; s0 += s0 < 4 ? 2 : 10) {
            const u32 n = s0 < 4 ? 2 : 10;
            if (s0 == 14) until(t_in + TDEL);
            v4u x[10];
#pragma unroll
            for (u32 u = 0; u < 10; ++u) x[u] = __builtin_amdgcn_raw_buffer_load_b128(rx, u < n ? vx + (s0 + u) * 36864 : OOB, 0, 0);
#pragma unroll
            for (u32 u = 0; u < 10; ++u) st16(rs, u < n ? v + ((s0 + u) << 16) : OOB, x[u]);
            if (s0 == 0) { wait_go(ws); __builtin_amdgcn_s_barrier(); }
            if (t_last(cnt + s0) && lane < n) setw(ws, OFF_FA + m * 4096 + j * 128 + fl(s0 + lane));
        }
    }
    else __builtin_amdgcn_s_barrier();
    CBAR();
    wait_set(ws, OFF_POL);
    const u32 pol = ldw(ws, OFF_POL);
    if (w == 3) {
        for (u32 i = 64; i < 127; ++i) t_item<3, true>(rid, i, j, m, pol, 0, ro, ws, rs);
        return;
    }
    for (u32 i = 0; i < 64; ++i) {
        t_item<1, false>(rid, i, j, m, pol, w, ro, ws, rs);
        if (t_last(cnt + 64 + i) && lane == 0) setw(ws, OFF_FA + m * 4096 + j * 128 + fl(64 + i));
    }
    t_item<1, true>(rid, 127, j, m, pol, w, ro, ws, rs);
}

#define CLOBBERS "memory", "vcc", "scc", \
    "v0", "v1", "v2", "v3", "v4", "v5", "v6", "v7", "v8", "v9", "v10", "v11", "v12", "v13", "v14", "v15", "v16", "v17", "v18", "v19", "v20", "v21", "v22", "v23", \
    "v24", "v25", "v26", "v27", "v28", "v29", "v30", "v31", "v32", "v33", "v34", "v35", "v36", "v37", "v38", "v39", "v40", "v41", "v42", "v43", "v44", "v45", "v46", "v47", \
    "v48", "v49", "v50", "v51", "v52", "v53", "v54", "v55", "v56", "v57", "v58", "v59", "v60", "v61", "v62", "v63", "v64", "v65", "v66", "v67", "v68", "v69", "v70", "v71", \
    "v72", "v73", "v74", "v75", "v76", "v77", "v78", "v79", "v80", "v81", "v82", "v83", "v84", "v85", "v86", "v87", "v88", "v89", "v90", "v91", "v92", "v93", "v94", "v95", \
    "v96", "v97", "v98", "v99", "v100", "v101", "v102", "v103", "v104", "v105", "v106", "v107", "v108", "v109", "v110", "v111", "v112", "v113", "v114", "v115", "v116", "v117", "v118", "v119", \
    "v120", "v121", "v122", "v123", "v124", "v125", "v126", "v127", "v128", "v129", "v130", "v131", "v132", "v133", "v134", "v135", "v136", "v137", "v138", "v139", "v140", "v141", "v142", "v143", \
    "v144", "v145", "v146", "v147", "v148", "v149", "v150", "v151", "v152", "v153", "v154", "v155", "v156", "v157", "v158", "v159", "v160", "v161", "v162", "v163", "v164", "v165", "v166", "v167", \
    "v168", "v169", "v170", "v171", "v172", "v173", "v174", "v175", "v176", "v177", "v178", "v179", "v180", "v181", "v182", "v183", "v184", "v185", "v186", "v187", "v188", "v189", "v190", "v191", \
    "v192", "v193", "v194", "v195", "v196", "v197", "v198", "v199", "v200", "v201", "v202", "v203", "v204", "v205", "v206", "v207", "v208", "v209", "v210", "v211", "v212", "v213", "v214", "v215", \
    "v216", "v217", "v218", "v219", "v220", "v221", "v222", "v223", "v224", "v225", "v226", "v227", "v228", "v229", "v230", "v231", "v232", "v233", "v234", "v235", "v236", "v237", "v238", "v239", \
    "v240", "v241", "v242", "v243", "v244", "v245", "v246", "v247", "v248", "v249", "v250", "v251", "v252", "v253", "v254", "v255", "a0", "a1", "a2", "a3", "a4", "a5", "a6", "a7", \
    "a8", "a9", "a10", "a11", "a12", "a13", "a14", "a15", "a16", "a17", "a18", "a19", "a20", "a21", "a22", "a23", "a24", "a25", "a26", "a27", "a28", "a29", "a30", "a31", \
    "a32", "a33", "a34", "a35", "a36", "a37", "a38", "a39", "a40", "a41", "a42", "a43", "a44", "a45", "a46", "a47", "a48", "a49", "a50", "a51", "a52", "a53", "a54", "a55", \
    "a56", "a57", "a58", "a59", "a60", "a61", "a62", "a63", "a64", "a65", "a66", "a67", "a68", "a69", "a70", "a71", "a72", "a73", "a74", "a75", "a76", "a77", "a78", "a79", \
    "a80", "a81", "a82", "a83", "a84", "a85", "a86", "a87", "a88", "a89", "a90", "a91", "a92", "a93", "a94", "a95", "a96", "a97", "a98", "a99", "a100", "a101", "a102", "a103", \
    "a104", "a105", "a106", "a107", "a108", "a109", "a110", "a111", "a112", "a113", "a114", "a115", "a116", "a117", "a118", "a119", "a120", "a121", "a122", "a123", "a124", "a125", "a126", "a127", \
    "a128", "a129", "a130", "a131", "a132", "a133", "a134", "a135", "a136", "a137", "a138", "a139", "a140", "a141", "a142", "a143", "a144", "a145", "a146", "a147", "a148", "a149", "a150", "a151", \
    "a152", "a153", "a154", "a155", "a156", "a157", "a158", "a159", "a160", "a161", "a162", "a163", "a164", "a165", "a166", "a167", "a168", "a169", "a170", "a171", "a172", "a173", "a174", "a175", \
    "a176", "a177", "a178", "a179", "a180", "a181", "a182", "a183", "a184", "a185", "a186", "a187", "a188", "a189", "a190", "a191", "a192", "a193", "a194", "a195", "a196", "a197", "a198", "a199", \
    "a200", "a201", "a202", "a203", "a204", "a205", "a206", "a207", "a208", "a209", "a210", "a211", "a212", "a213", "a214", "a215", "a216", "a217", "a218", "a219", "a220", "a221", "a222", "a223", \
    "a224", "a225", "a226", "a227", "a228", "a229", "a230", "a231", "a232", "a233", "a234", "a235", "a236", "a237", "a238", "a239", "a240", "a241", "a242", "a243", "a244", "a245", "a246", "a247", \
    "a248", "a249", "a250", "a251", "a252", "a253", "a254", "a255", "s38", "s39", "s40", "s41", "s42", "s43", "s44", "s45", "s46", "s47", "s48", "s49", "s50", "s51", "s52", "s53", \
    "s54", "s55", "s56", "s57", "s58", "s59", "s60", "s61", "s62", "s63", "s64", "s65", "s66", "s67", "s68", "s69", "s70", "s71", "s72", "s73", "s74", "s75", "s76", "s77", \
    "s78", "s79", "s80", "s81", "s82", "s83", "s84", "s85", "s86", "s87", "s88", "s89", "s90", "s91", "s92", "s93", "s94", "s95", "s96", "s97", "s98", "s99", "s100", "s101"
#define LANES() const int tid = threadIdx.x, lane = tid & 63, c = lane & 15, g = lane >> 4, q = c >> 2, p4 = c & 3; \
    const int wave = __builtin_amdgcn_readfirstlane(tid >> 6); u32* tab = (u32*)(smem + L_TAB) + tid; (void)q; (void)p4; (void)g
struct Par { u32 role, h, p, woff; };
__device__ __forceinline__ u32 rows(u32 i, u32 m) { return (i << 16) + (i < 64 ? m << 13 : (((i >> 5 ^ m) & 1) << 13) + 1); }
__device__ __forceinline__ u32 slot(u32 i) { return ((i & 63) << 19) + (((i >> 2 ^ i >> 6) & 1) << 13) + 1; }
__device__ __forceinline__ u32 part(u32 i, u32 m) { return (i << 18) + (((i >> 3 ^ m) & 1) << 13) + 1; }
template <class FS, class FD>
__device__ __forceinline__ u32 items(u32 K, u32 mk, u32 fin, u32 fout, FS src, FD dst, char* smem) {
    const u32 n = threadIdx.x - 1;
    auto idx = [&](u32 k) { const u32 t = k - K; return t < 2 * (64 - K) ? ((t & 1) ? K : 64) + (t >> 1) : k & 127; };
    if (threadIdx.x < 130) {
        v4u* row = (v4u*)(smem + L_IT) + 2 * threadIdx.x;
        row[0] = v4u{n + 1 == mk ? OFF_NEVER + 4 : n + 1 < 128 ? fin + fl(idx(n + 1)) : OFF_NEVER, src(idx(n + 1)), dst(idx(n)), fout + fl(idx(n - 1))};
        row[1] = v4u{0u, idx(n) & 63 ? 0u : 0xf149f2cau, (idx(n) ^ idx(n - 1)) & 64, idx(n)};
    }
    return fin + fl(mk ? 64 : 0);
}
#define KA 13
#define KB 12
#define KH 11
#define KW 10

__device__ __forceinline__ Par role_h(u32 m, u32 h, char* smem) {
    LANES();
    tab[0] = 0x10000 + wave * 8192 + lane * 16;
    tab[256] = tid * 16;
    tab[2 * 256] = cgo(16 * wave) + lane * 16;
    tab[3 * 256] = 0x6000 + c * 4;
    tab[4 * 256] = 0x10000 + (q * 16 + c) * 16 + p4 * 4;
    tab[5 * 256] = (8 * wave + g) * 8192 + (c ^ ((g | (wave & 1) << 2) << 1)) * 16;
    tab[6 * 256] = (4 * g + q) * 256 + ((2 * wave) ^ (q | (g & 1) << 2)) * 32 + p4 * 8;
    tab[7 * 256] = tab[6 * 256] ^ 32;
    const u32 f = items(KH, 0, OFF_FB + m * 4096, OFF_FKV + (h >> 2) * 4096 + (h & 3) * 256 + m * 128, [&](u32 i) { return OFF_TB + rows(i, m); }, [&](u32 i) { return OFF_QX + h * 16384 + m * 4096 + slot(i); }, smem);
    return Par{1, f, 0, h * 256};
}

__device__ __forceinline__ Par role_w(u32 m, u32 kb, u32 nb, char* smem) {
    LANES();
    tab[0] = wave * 8192 + lane * 16;
    tab[256] = wave * 32768 + lane * 16;
    tab[512] = cgo(24 * nb + 6 * wave + g) + c * 16;
    tab[768] = (256 * wave + 4 * g + (c >> 3)) * 1152 + ((lane & 7) ^ ((g & 1) << 2)) * 16;
#pragma unroll
    for (int t = 0; t < 3; ++t) {
        const int col = wave * 96 + (t < 2 ? p4 * 16 + t * 8 : 64 + p4 * 8);
        tab[(4 + t) * 256] = (col >> 7) * 2048 + (q & 1) * 1024 + (2 * g + (q >> 1)) * 128 + ((col & 127) ^ ((g & 1) << 6));
    }
    tab[7 * 256] = (g & 1) ? OOB : cgo(24 * nb + 6 * wave + 4 + (g >> 1)) + c * 16;
    const u32 f = items(KW, 0, OFF_COB + (m * 4 + kb) * 4096, OFF_FP + (m * 3 + nb) * 4096 + kb * 128, [&](u32 i) { return OFF_OX + kb * 8 * 16384 + m * 4096 + slot(i); },
          [&](u32 i) { return OFF_PB + kb * 65536 + part(i, m); }, smem);
    return Par{2, f, 0, kb * 1024 * 1152 + nb * 384};
}

__device__ __forceinline__ Par role_a2(u32 m, u32 j, char* smem) {
    LANES();
    const int hd = tid >> 2, w = wave < 3 ? wave : 2;
    tab[0] = 0x8000 + lane * 16;
    tab[256] = wave * 4096 + lane * 16;
    tab[512] = cgo(16 * wave) + lane * 16;
    tab[768] = wave < 3 ? cgo(12 * j + 4 * wave + g) + c * 16 : OOB;
    tab[4 * 256] = (16 + (hd >> 4)) * 1024 + (((hd >> 2) & 3) * 16 + 4 * (tid & 3)) * 16 + (hd & 3) * 4;
    tab[5 * 256] = wave < 2 ? 32768 + tid * 16 : OOB;
    tab[6 * 256] = (16 * wave + (lane >> 2)) * 1152 + (lane & 3) * 16;
    tab[7 * 256] = w * 36864 + p4 * 16 + (20 * (g >> 1) + 8 * (g & 1) + q) * 64;
    tab[8 * 256] = w * 36864 + p4 * 16 + (72 * g + 16 + q) * 64;
    const u32 f = items(KB, KB, OFF_FO + m * 4096, OFF_FB + m * 4096 + j * 128, [&](u32 i) { return OFF_OA + rows(i, m); }, [&](u32 i) { return OFF_TB + rows(i, m); }, smem);
    return Par{4, f, 0, j * 192};
}

__device__ __forceinline__ Par role_a1(u32 m, u32 hg, char* smem) {
    LANES();
    const int hd = 4 * hg + wave;
    tab -= 768;
    tab[0] = lane * 16;
    tab[256] = wave * 5120 + lane * 16;
    tab[2 * 256] = !(g & 1) ? cgo(2 * hd + (g >> 1)) + c * 16 : g == 1 && !p4 ? 32768 + hd * 64 + c * 4 : OOB;
    tab[3 * 256] = cgo(16 * wave) + lane * 16;
    tab[4 * 256] = wave < 2 ? 32768 + tid * 16 : OOB;
    tab[5 * 256] = tid * 16;
    tab[6 * 256] = L_EX + ((wave >> 1) * 16 + c) * 16 + (wave & 1) * 8;
    tab[7 * 256] = g ? L_EX + 4096 : tab[6 * 256] + 512;
    tab[8 * 256] = L_EX + 2048 + (g * 16 + 2 * wave + (c & 1)) * 16;
    tab[9 * 256] = 0x7000 + c * 4;
    tab[10 * 256] = (q * 16 + c) * 16 + p4 * 4;
    tab[11 * 256] = 4 * g - c;
    tab[12 * 256] = (8 * g + q) * 144 + 36 * wave + 4 * (wave & 1) + p4 * 8;
    tab[13 * 256] = (wave < 2 ? p4 >> 1 : 2) * 9216 + (8 * g + q) * 144 + 72 * (p4 & 1) + 32;
    if (tid < 2) *(v4u*)(smem + L_EX + 4096 + tid * 1024) = v4u{0u, 0u, 0u, 0u};
    const u32 f = items(KA, KA, OFF_FA + m * 4096, OFF_FO + m * 4096 + hg * 128, [&](u32 i) { return OFF_TA + rows(i, m); }, [&](u32 i) { return OFF_OA + rows(i, m); }, smem);
    return Par{3, f, 0, hg * 144};
}

#define SB() __builtin_amdgcn_sched_barrier(0)
__device__ __forceinline__ u32 now32() { return (u32)__builtin_amdgcn_s_memtime(); }
__device__ __forceinline__ Par role_chain(u32 h, u32 p, char* smem) {
    LANES();
    const int ro = (q & 1) + 4 * (q >> 1) + 8 * (g >> 1) + 16 * (g & 1), rr = (q & 1) | (g & 1) << 1 | (q >> 1) << 2;
    tab[0] = lane * 16;
    tab[256] = L_W + (wave * 15 + (wave == 3 ? 2 : 0)) * 2048 + lane * 16;
    tab[2 * 256] = ((g & 1) + 4 * (g >> 1) + 8 * wave) * 8192 + (c ^ ((g | (wave & 1) << 2) << 1)) * 16;
    tab[3 * 256] = (rr + 8 * (g >> 1)) * 256 + ((2 * wave) ^ rr) * 32 + p4 * 8;
    tab[4 * 256] = tab[3 * 256] ^ 32;
    tab[5 * 256] = (2 * wave + (lane >> 5)) * 1152 + ((lane & 31) ^ ((lane >> 5 | (wave & 2)) << 2)) * 16;
    tab[6 * 256] = ((q & 1) + 4 * (g >> 1) + 16 * (g & 1)) * 512 + ((2 * wave + (p4 >> 1)) ^ ((q & 1) | (g & 2))) * 64 + (q >> 1) * 16 + (p4 & 1) * 8;
    tab[7 * 256] = (32 * wave + (lane >> 2)) * 1152 + (lane & 3) * 16;
    tab[8 * 256] = ro * 64 + (wave & 1) * 32 + p4 * 8;
    return Par{0, h, p, 0};
}

#define MAP 3
constexpr int RW = 90, RA1 = 24, RA2 = MAP >= 2 ? 184 : 56, RTT = 58;
#define PFO 1
#define WDEL1 700
#define CDEL 1000
#define WDEL 700
#define NPOL 6
static_assert(OFF_GO == 131072u);
static_assert(OFF_EP == 132096u);
static_assert(OFF_POL == 64u);
static_assert(OFF_RDY == 128u);
static_assert(OFF_COB == 53248u);
static_assert(OFF_FKV == 86016u);
static_assert(OFF_XA == 0u);
static_assert(OFF_XB == 2097152u);
static_assert(OFF_TA == 8388608u);
static_assert(OFF_OA == 25165824u);
static_assert(OFF_NEVER == 192u);
static_assert(L_IT == 158720u);
static_assert(OFF_QX == 67108864u);
static_assert(OFF_KX == 100663296u);
static_assert(OFF_VX == 134217728u);
static_assert(OFF_OX == 167772160u);
static_assert(WS_BYTES == 201326592u);
static_assert(L_TAB == 147456u);
static_assert(L_EX == 138240u);
static_assert(L_W == 32064);
__device__ __forceinline__ void measure_pol(char* ws, rsrc_t rs, char* smem) {
    const int wave = __builtin_amdgcn_readfirstlane(threadIdx.x >> 6);
    u32 best[2] = {~0u, ~0u};
    for (int n = 0; n < NPOL; ++n)
#pragma unroll
        for (int i = 0; i < 2; ++i) {
            SB();
            const u32 t0 = now32();
            SB();
            st4(rs, (wave & 1) * MB2 + (508 + 2 * i + (wave >> 1)) * 4096 + n * 128, t0);
            DRAIN();
            const u32 t = now32() - t0;
            best[i] = t < best[i] ? t : best[i];
        }
    int* d = (int*)smem;
    if ((threadIdx.x & 63) == 0) d[wave] = (int)(best[0] - best[1]);
    wait_go(ws);
    __syncthreads();
    if (threadIdx.x == 0) __hip_atomic_store((gu32*)(ws + OFF_POL), 4u | (d[0] + d[2] >= 0) | (d[1] + d[3] >= 0) << 1, RLX_AGENT);
}

__global__ __launch_bounds__(256, 1) void mega_kernel(const u16* __restrict__ x0, const u16* __restrict__ wqkv, const u16* __restrict__ woA,
                                                     const u16* __restrict__ mem0, const u16* __restrict__ Wq, const u16* __restrict__ Wk,
                                                     const u16* __restrict__ Wv, const u16* __restrict__ Wo, u16* out, char* ws) {
    __shared__ __attribute__((aligned(16))) char smem[163840];
    char* wd = (char*)(((uintptr_t)ws + SYNC_BYTES + MB2 - 1) & ~(uintptr_t)(MB2 - 1));
    const rsrc_t rs = __builtin_amdgcn_make_buffer_rsrc(wd, 0, WS_BYTES, 0x00020000);
    const u64 t_in = __builtin_amdgcn_s_memrealtime();
    {
        const char* pc; u32 n;
        asm volatile("s_getpc_b64 %0\n.Lbeg:\ns_mov_b32 %1, .Lfin-.Lbeg" : "=s"(pc), "=s"(n));
        const u32 o = ((blockIdx.x >> 3 & 3) * 256 + threadIdx.x) * 128;
        if (o < n) TOUCH(pc + o);
    }
    u32 rid = (blockIdx.x & 7) * 32 + (blockIdx.x >> 3);
    rid = rid - 24 < 8 ? rid + 120 : rid - 144 < 8 ? rid - 120 : rid;
    asm volatile("" : "+s"(rid));
    STAMP(159, 0);
    const u32 xq = (rid >> 5) & 3, t = rid & 31, layer = rid >> 7;
    const int u = t - 24;
    const int role = t < 16 ? 0 : t < 24 ? 1 : xq == 0 ? 3 : u < 2 ? 4 : xq == 1 ? 5 : 2;
    const int ix = role == 3 ? u : role == 4 ? 2 * xq - 2 + u : role == 5 ? u - 2 : (xq * 6 + u - 5) % 12;
    if (role == 5) { role_t(t_in, rid, ix, layer, x0, out, ws, rs, smem); asm volatile("s_endpgm" ::: "memory"); }
    if (role == 0 && layer) {
        const u32 i = t * 256 + threadIdx.x;
        for (u32 k = xq; k < (xq ? xq + 1 : 4); ++k) if (i < 3888) TOUCH((const char*)wqkv + (k * 3888 + i) * 128);
        if (xq && i < 1728) TOUCH((const char*)woA + (i / 3) * 1152 + (xq - 1) * 384 + (i % 3) * 128);
        for (const u16* w : {Wk, Wv})
            for (u32 k = i; k < 9216; k += 4096) TOUCH((const char*)w + (k >> 4) * 8192 + xq * 2048 + (k & 15) * 128);
        if (PFO) for (u32 k = i; k < 9216; k += 4096) TOUCH((const char*)Wo + (xq * 9216 + k) * 128);
        for (u32 k = 0; k < 18; ++k) st16(rs, (k % 3 * 3 >> 1) * MB2 + k / 3 * 262144 + (xq * 4096 + i) * 16, v4u{~0u, ~0u, ~0u, ~0u});
        DRAIN();
        wait_go(ws);
        __syncthreads();
        if (threadIdx.x == 0) __hip_atomic_fetch_add((gu32*)(ws + OFF_RDY), 1u, RLX_AGENT);
    }
    if (role == 2 && layer && ix < 8) {
        const u64 e = succ(ws);
        const rsrc_t rc = __builtin_amdgcn_make_buffer_rsrc(ws, 0, OFF_GO, 0x00020000);
        for (u32 k = 0; k < 4; ++k) st16(rc, ix * 16384 + k * 4096 + threadIdx.x * 16, v4u{0u, 0u, 0u, 0u});
        DRAIN();
        __syncthreads();
        if (threadIdx.x == 0) __hip_atomic_store((gu64*)(ws + OFF_GO + ix * 128), e, RLX_AGENT);
    }
    if (rid == RW) measure_pol(ws, rs, smem);
    until(t_in + (role == 2 ? (layer ? WDEL1 : WDEL) : role == 0 && layer ? CDEL : 0));
    Par P{5, 0, 0, 0};
    const u16* w0 = role == 2 ? Wo : role == 3 ? wqkv : role == 4 ? woA : Wq;
    if (role == 0) P = role_chain(xq * 8 + (t >> 1), t & 1, smem);
    else if (role == 1) P = role_h(layer, xq * 8 + t - 16, smem);
    else if (role == 2) P = role_w(layer, ix / 3, ix % 3, smem);
    else if (role == 3) P = role_a1(layer, ix, smem);
    else if (role == 4) P = role_a2(layer ^ (MAP >= 2), ix, smem);
    if (role == 0) P.woff = (u32)t_in;
    const int wave = __builtin_amdgcn_readfirstlane(threadIdx.x >> 6);
    asm volatile(
    "s_cmp_eq_u32 %13, 1\n"
    "s_cbranch_scc1 .Lrole1_%=\n"
    "s_cmp_eq_u32 %13, 2\n"
    "s_cbranch_scc1 .Lrole2_%=\n"
    "s_cmp_eq_u32 %13, 3\n"
    "s_cbranch_scc1 .Lrole3_%=\n"
    "s_cmp_eq_u32 %13, 4\n"
    "s_cbranch_scc1 .Lrole4_%=\n"
    "s_cmp_eq_u32 %13, 5\n"
    "s_cbranch_scc1 .Lrole5_%=\n"
    "s_mov_b64 s[40:41], %0\n"
    "s_and_b32 s41, s41, 0xffff\n"
    "s_mov_b32 s42, 0xc000000\n"
    "s_mov_b32 s43, 0x20000\n"
    "s_mov_b32 s57, 131072\n"
    "v_mbcnt_lo_u32_b32 v10, -1, 0\n"
    "v_mbcnt_hi_u32_b32 v10, -1, v10\n"
    "s_lshl_b32 s73, %2, 8\n"
    "s_add_u32 s73, s73, 147456\n"
    "v_lshl_add_u32 v10, v10, 2, s73\n"
    "ds_read_b32 v0, v10\n"
    "ds_read_b32 v2, v10 offset:1024\n"
    "ds_read_b32 v1, v10 offset:2048\n"
    "ds_read_b32 v3, v10 offset:3072\n"
    "ds_read_b32 v4, v10 offset:4096\n"
    "ds_read_b32 v5, v10 offset:5120\n"
    "ds_read_b32 v6, v10 offset:6144\n"
    "ds_read_b32 v8, v10 offset:7168\n"
    "ds_read_b32 v9, v10 offset:8192\n"
    "s_waitcnt lgkmcnt(0)\n"
    "s_barrier\n"
    "s_xor_b32 s73, %2, 0\n"
    "s_lshl_b32 s73, s73, 12\n"
    "v_add_u32 v96, s73, v3\n"
    "v_add_u32 v97, s73, v4\n"
    "s_xor_b32 s73, %2, 1\n"
    "s_lshl_b32 s73, s73, 12\n"
    "v_add_u32 v98, s73, v3\n"
    "v_add_u32 v99, s73, v4\n"
    "s_xor_b32 s73, %2, 2\n"
    "s_lshl_b32 s73, s73, 12\n"
    "v_add_u32 v100, s73, v3\n"
    "v_add_u32 v101, s73, v4\n"
    "s_xor_b32 s73, %2, 3\n"
    "s_lshl_b32 s73, s73, 12\n"
    "v_add_u32 v102, s73, v3\n"
    "v_add_u32 v103, s73, v4\n"
    "s_lshl_b32 s60, %4, 8\n"
    "s_lshl_b32 s61, %2, 10\n"
    "s_mul_i32 s63, %4, 147456\n"
    "s_mul_i32 s62, %5, 576\n"
    "s_add_u32 s63, s63, s62\n"
    "s_mov_b64 s[44:45], %11\n"
    "s_and_b32 s45, s45, 0xffff\n"
    "s_mov_b32 s46, 0x480000\n"
    "s_mov_b32 s47, 0x20000\n"
    "s_lshl_b32 s73, s61, 1\n"
    "s_add_u32 m0, s73, 0\n"
    "s_add_u32 s62, s63, 512\n"
    "buffer_load_dwordx4 v8, s[44:47], s62 offen lds\n"
    "s_lshl_b32 s73, s61, 1\n"
    "s_add_u32 m0, s73, 1024\n"
    "s_add_u32 s62, s63, 18944\n"
    "buffer_load_dwordx4 v8, s[44:47], s62 offen lds\n"
    "s_mov_b64 vcc, 0\n"
    "s_mov_b32 vcc_lo, %7\n"
    "s_cbranch_vccz .Lsl0_%=\n"
    "s_sleep 10\n"
    ".Lsl0_%=:\n"
    "s_mov_b64 s[44:45], %8\n"
    "s_and_b32 s45, s45, 0xffff\n"
    "s_mov_b32 s46, 0x480000\n"
    "s_mov_b32 s47, 0x20000\n"
    "s_add_u32 m0, s61, 16384\n"
    "s_add_u32 s62, s60, 262144\n"
    "buffer_load_dwordx4 v1, s[44:47], s62 offen lds\n"
    "s_add_u32 m0, s61, 20480\n"
    "s_add_u32 s62, s60, 1310720\n"
    "buffer_load_dwordx4 v1, s[44:47], s62 offen lds\n"
    "s_add_u32 m0, s61, 24576\n"
    "s_add_u32 s62, s60, 2359296\n"
    "buffer_load_dwordx4 v1, s[44:47], s62 offen lds\n"
    "s_add_u32 m0, s61, 28672\n"
    "s_add_u32 s62, s60, 3407872\n"
    "buffer_load_dwordx4 v1, s[44:47], s62 offen lds\n"
    "s_cbranch_vccz .Lsl1_%=\n"
    "s_sleep 10\n"
    ".Lsl1_%=:\n"
    "s_add_u32 m0, s61, 32768\n"
    "s_add_u32 s62, s60, 786432\n"
    "buffer_load_dwordx4 v1, s[44:47], s62 offen lds\n"
    "s_add_u32 m0, s61, 36864\n"
    "s_add_u32 s62, s60, 1835008\n"
    "buffer_load_dwordx4 v1, s[44:47], s62 offen lds\n"
    "s_add_u32 m0, s61, 40960\n"
    "s_add_u32 s62, s60, 2883584\n"
    "buffer_load_dwordx4 v1, s[44:47], s62 offen lds\n"
    "s_add_u32 m0, s61, 45056\n"
    "s_add_u32 s62, s60, 3932160\n"
    "buffer_load_dwordx4 v1, s[44:47], s62 offen lds\n"
    "s_cbranch_vccz .Lsl2_%=\n"
    "s_sleep 10\n"
    ".Lsl2_%=:\n"
    "s_add_u32 m0, s61, 49152\n"
    "s_add_u32 s62, s60, 278528\n"
    "buffer_load_dwordx4 v1, s[44:47], s62 offen lds\n"
    "s_add_u32 m0, s61, 53248\n"
    "s_add_u32 s62, s60, 1327104\n"
    "buffer_load_dwordx4 v1, s[44:47], s62 offen lds\n"
    "s_add_u32 m0, s61, 57344\n"
    "s_add_u32 s62, s60, 2375680\n"
    "buffer_load_dwordx4 v1, s[44:47], s62 offen lds\n"
    "s_add_u32 m0, s61, 61440\n"
    "s_add_u32 s62, s60, 3424256\n"
    "buffer_load_dwordx4 v1, s[44:47], s62 offen lds\n"
    "s_cbranch_vccz .Lsl3_%=\n"
    "s_sleep 10\n"
    ".Lsl3_%=:\n"
    "s_add_u32 m0, s61, 65536\n"
    "s_add_u32 s62, s60, 802816\n"
    "buffer_load_dwordx4 v1, s[44:47], s62 offen lds\n"
    "s_add_u32 m0, s61, 69632\n"
    "s_add_u32 s62, s60, 1851392\n"
    "buffer_load_dwordx4 v1, s[44:47], s62 offen lds\n"
    "s_add_u32 m0, s61, 73728\n"
    "s_add_u32 s62, s60, 2899968\n"
    "buffer_load_dwordx4 v1, s[44:47], s62 offen lds\n"
    "s_add_u32 m0, s61, 77824\n"
    "s_add_u32 s62, s60, 3948544\n"
    "buffer_load_dwordx4 v1, s[44:47], s62 offen lds\n"
    "s_cbranch_vccz .Lsl4_%=\n"
    "s_sleep 10\n"
    ".Lsl4_%=:\n"
    "s_add_u32 m0, s61, 81920\n"
    "s_add_u32 s62, s60, 4194304\n"
    "buffer_load_dwordx4 v1, s[44:47], s62 offen lds\n"
    "s_add_u32 m0, s61, 86016\n"
    "s_add_u32 s62, s60, 4210688\n"
    "buffer_load_dwordx4 v1, s[44:47], s62 offen lds\n"
    "s_add_u32 m0, s61, 90112\n"
    "s_add_u32 s62, s60, 4456448\n"
    "buffer_load_dwordx4 v1, s[44:47], s62 offen lds\n"
    "s_add_u32 m0, s61, 94208\n"
    "s_add_u32 s62, s60, 4472832\n"
    "buffer_load_dwordx4 v1, s[44:47], s62 offen lds\n"
    "s_cbranch_vccz .Lsl5_%=\n"
    "s_sleep 10\n"
    ".Lsl5_%=:\n"
    "s_mov_b64 s[44:45], %9\n"
    "s_and_b32 s45, s45, 0xffff\n"
    "s_mov_b32 s46, 0x480000\n"
    "s_mov_b32 s47, 0x20000\n"
    "s_add_u32 m0, s61, 98304\n"
    "s_add_u32 s62, s60, 0\n"
    "buffer_load_dwordx4 v1, s[44:47], s62 offen lds\n"
    "s_add_u32 m0, s61, 102400\n"
    "s_add_u32 s62, s60, 1048576\n"
    "buffer_load_dwordx4 v1, s[44:47], s62 offen lds\n"
    "s_add_u32 m0, s61, 106496\n"
    "s_add_u32 s62, s60, 2097152\n"
    "buffer_load_dwordx4 v1, s[44:47], s62 offen lds\n"
    "s_add_u32 m0, s61, 110592\n"
    "s_add_u32 s62, s60, 3145728\n"
    "buffer_load_dwordx4 v1, s[44:47], s62 offen lds\n"
    "s_cbranch_vccz .Lsl6_%=\n"
    "s_sleep 10\n"
    ".Lsl6_%=:\n"
    "s_add_u32 m0, s61, 114688\n"
    "s_add_u32 s62, s60, 524288\n"
    "buffer_load_dwordx4 v1, s[44:47], s62 offen lds\n"
    "s_add_u32 m0, s61, 118784\n"
    "s_add_u32 s62, s60, 1572864\n"
    "buffer_load_dwordx4 v1, s[44:47], s62 offen lds\n"
    "s_add_u32 m0, s61, 122880\n"
    "s_add_u32 s62, s60, 2621440\n"
    "buffer_load_dwordx4 v1, s[44:47], s62 offen lds\n"
    "s_add_u32 m0, s61, 126976\n"
    "s_add_u32 s62, s60, 3670016\n"
    "buffer_load_dwordx4 v1, s[44:47], s62 offen lds\n"
    "s_cbranch_vccz .Lsl7_%=\n"
    "s_sleep 10\n"
    ".Lsl7_%=:\n"
    "s_add_u32 m0, s61, 131072\n"
    "s_add_u32 s62, s60, 16384\n"
    "buffer_load_dwordx4 v1, s[44:47], s62 offen lds\n"
    "s_add_u32 m0, s61, 135168\n"
    "s_add_u32 s62, s60, 1064960\n"
    "buffer_load_dwordx4 v1, s[44:47], s62 offen lds\n"
    "s_add_u32 m0, s61, 139264\n"
    "s_add_u32 s62, s60, 2113536\n"
    "buffer_load_dwordx4 v1, s[44:47], s62 offen lds\n"
    "s_add_u32 m0, s61, 143360\n"
    "s_add_u32 s62, s60, 3162112\n"
    "buffer_load_dwordx4 v1, s[44:47], s62 offen lds\n"
    "s_cbranch_vccz .Lsl8_%=\n"
    "s_sleep 10\n"
    ".Lsl8_%=:\n"
    "s_add_u32 m0, s61, 147456\n"
    "s_add_u32 s62, s60, 540672\n"
    "buffer_load_dwordx4 v1, s[44:47], s62 offen lds\n"
    "s_add_u32 m0, s61, 151552\n"
    "s_add_u32 s62, s60, 1589248\n"
    "buffer_load_dwordx4 v1, s[44:47], s62 offen lds\n"
    "s_add_u32 m0, s61, 155648\n"
    "s_add_u32 s62, s60, 2637824\n"
    "buffer_load_dwordx4 v1, s[44:47], s62 offen lds\n"
    "s_add_u32 m0, s61, 159744\n"
    "s_add_u32 s62, s60, 3686400\n"
    "buffer_load_dwordx4 v1, s[44:47], s62 offen lds\n"
    "s_waitcnt vmcnt(32) lgkmcnt(0)\n"
    "s_barrier\n"
    "ds_read_b64_tr_b16 v[36:37], v96 offset:16384\n"
    "ds_read_b64_tr_b16 v[68:69], v98 offset:16384\n"
    "ds_read_b64_tr_b16 v[148:149], v100 offset:16384\n"
    "ds_read_b64_tr_b16 v[180:181], v102 offset:16384\n"
    "ds_read_b64_tr_b16 v[44:45], v97 offset:16384\n"
    "ds_read_b64_tr_b16 v[76:77], v99 offset:16384\n"
    "ds_read_b64_tr_b16 v[156:157], v101 offset:16384\n"
    "ds_read_b64_tr_b16 v[188:189], v103 offset:16384\n"
    "s_waitcnt vmcnt(28) lgkmcnt(0)\n"
    "s_barrier\n"
    "s_cbranch_vccz .Lsl9_%=\n"
    "s_sleep 10\n"
    ".Lsl9_%=:\n"
    "s_add_u32 m0, s61, 16384\n"
    "s_add_u32 s62, s60, 262144\n"
    "buffer_load_dwordx4 v1, s[44:47], s62 offen lds\n"
    "s_add_u32 m0, s61, 20480\n"
    "s_add_u32 s62, s60, 1310720\n"
    "buffer_load_dwordx4 v1, s[44:47], s62 offen lds\n"
    "s_add_u32 m0, s61, 24576\n"
    "s_add_u32 s62, s60, 2359296\n"
    "buffer_load_dwordx4 v1, s[44:47], s62 offen lds\n"
    "s_add_u32 m0, s61, 28672\n"
    "s_add_u32 s62, s60, 3407872\n"
    "buffer_load_dwordx4 v1, s[44:47], s62 offen lds\n"
    "ds_read_b64_tr_b16 v[52:53], v96 offset:32768\n"
    "ds_read_b64_tr_b16 v[84:85], v98 offset:32768\n"
    "ds_read_b64_tr_b16 v[164:165], v100 offset:32768\n"
    "ds_read_b64_tr_b16 v[196:197], v102 offset:32768\n"
    "ds_read_b64_tr_b16 v[60:61], v97 offset:32768\n"
    "ds_read_b64_tr_b16 v[140:141], v99 offset:32768\n"
    "ds_read_b64_tr_b16 v[172:173], v101 offset:32768\n"
    "ds_read_b64_tr_b16 v[204:205], v103 offset:32768\n"
    "s_waitcnt vmcnt(28) lgkmcnt(0)\n"
    "s_barrier\n"
    "s_cbranch_vccz .Lsl10_%=\n"
    "s_sleep 10\n"
    ".Lsl10_%=:\n"
    "s_add_u32 m0, s61, 32768\n"
    "s_add_u32 s62, s60, 786432\n"
    "buffer_load_dwordx4 v1, s[44:47], s62 offen lds\n"
    "s_add_u32 m0, s61, 36864\n"
    "s_add_u32 s62, s60, 1835008\n"
    "buffer_load_dwordx4 v1, s[44:47], s62 offen lds\n"
    "s_add_u32 m0, s61, 40960\n"
    "s_add_u32 s62, s60, 2883584\n"
    "buffer_load_dwordx4 v1, s[44:47], s62 offen lds\n"
    "s_add_u32 m0, s61, 45056\n"
    "s_add_u32 s62, s60, 3932160\n"
    "buffer_load_dwordx4 v1, s[44:47], s62 offen lds\n"
    "ds_read_b64_tr_b16 v[38:39], v96 offset:49152\n"
    "ds_read_b64_tr_b16 v[70:71], v98 offset:49152\n"
    "ds_read_b64_tr_b16 v[150:151], v100 offset:49152\n"
    "ds_read_b64_tr_b16 v[182:183], v102 offset:49152\n"
    "ds_read_b64_tr_b16 v[46:47], v97 offset:49152\n"
    "ds_read_b64_tr_b16 v[78:79], v99 offset:49152\n"
    "ds_read_b64_tr_b16 v[158:159], v101 offset:49152\n"
    "ds_read_b64_tr_b16 v[190:191], v103 offset:49152\n"
    "s_waitcnt vmcnt(28) lgkmcnt(0)\n"
    "s_barrier\n"
    "s_cbranch_vccz .Lsl11_%=\n"
    "s_sleep 10\n"
    ".Lsl11_%=:\n"
    "s_add_u32 m0, s61, 49152\n"
    "s_add_u32 s62, s60, 278528\n"
    "buffer_load_dwordx4 v1, s[44:47], s62 offen lds\n"
    "s_add_u32 m0, s61, 53248\n"
    "s_add_u32 s62, s60, 1327104\n"
    "buffer_load_dwordx4 v1, s[44:47], s62 offen lds\n"
    "s_add_u32 m0, s61, 57344\n"
    "s_add_u32 s62, s60, 2375680\n"
    "buffer_load_dwordx4 v1, s[44:47], s62 offen lds\n"
    "s_add_u32 m0, s61, 61440\n"
    "s_add_u32 s62, s60, 3424256\n"
    "buffer_load_dwordx4 v1, s[44:47], s62 offen lds\n"
    "v_add_u32 v127, 0x10000, v96\n"
    "ds_read_b64_tr_b16 v[54:55], v127\n"
    "v_add_u32 v126, 0x10000, v98\n"
    "ds_read_b64_tr_b16 v[86:87], v126\n"
    "v_add_u32 v125, 0x10000, v100\n"
    "ds_read_b64_tr_b16 v[166:167], v125\n"
    "v_add_u32 v124, 0x10000, v102\n"
    "ds_read_b64_tr_b16 v[198:199], v124\n"
    "v_add_u32 v123, 0x10000, v97\n"
    "ds_read_b64_tr_b16 v[62:63], v123\n"
    "v_add_u32 v122, 0x10000, v99\n"
    "ds_read_b64_tr_b16 v[142:143], v122\n"
    "v_add_u32 v121, 0x10000, v101\n"
    "ds_read_b64_tr_b16 v[174:175], v121\n"
    "v_add_u32 v120, 0x10000, v103\n"
    "ds_read_b64_tr_b16 v[206:207], v120\n"
    "s_waitcnt vmcnt(28) lgkmcnt(0)\n"
    "s_barrier\n"
    "s_cbranch_vccz .Lsl12_%=\n"
    "s_sleep 10\n"
    ".Lsl12_%=:\n"
    "s_add_u32 m0, s61, 65536\n"
    "s_add_u32 s62, s60, 802816\n"
    "buffer_load_dwordx4 v1, s[44:47], s62 offen lds\n"
    "s_add_u32 m0, s61, 69632\n"
    "s_add_u32 s62, s60, 1851392\n"
    "buffer_load_dwordx4 v1, s[44:47], s62 offen lds\n"
    "s_add_u32 m0, s61, 73728\n"
    "s_add_u32 s62, s60, 2899968\n"
    "buffer_load_dwordx4 v1, s[44:47], s62 offen lds\n"
    "s_add_u32 m0, s61, 77824\n"
    "s_add_u32 s62, s60, 3948544\n"
    "buffer_load_dwordx4 v1, s[44:47], s62 offen lds\n"
    "v_add_u32 v119, 0x10000, v3\n"
    "ds_read_b64_tr_b16 v[208:209], v119 offset:16384\n"
    "ds_read_b64_tr_b16 v[210:211], v119 offset:20480\n"
    "ds_read_b64_tr_b16 v[212:213], v119 offset:24576\n"
    "ds_read_b64_tr_b16 v[214:215], v119 offset:28672\n"
    "v_add_u32 v118, 0x10000, v4\n"
    "ds_read_b64_tr_b16 v[216:217], v118 offset:16384\n"
    "ds_read_b64_tr_b16 v[218:219], v118 offset:20480\n"
    "ds_read_b64_tr_b16 v[220:221], v118 offset:24576\n"
    "ds_read_b64_tr_b16 v[222:223], v118 offset:28672\n"
    "s_waitcnt vmcnt(28) lgkmcnt(0)\n"
    "s_barrier\n"
    "s_cbranch_vccz .Lsl13_%=\n"
    "s_sleep 10\n"
    ".Lsl13_%=:\n"
    "s_add_u32 m0, s61, 81920\n"
    "s_add_u32 s62, s60, 4194304\n"
    "buffer_load_dwordx4 v1, s[44:47], s62 offen lds\n"
    "s_add_u32 m0, s61, 86016\n"
    "s_add_u32 s62, s60, 4210688\n"
    "buffer_load_dwordx4 v1, s[44:47], s62 offen lds\n"
    "s_add_u32 m0, s61, 90112\n"
    "s_add_u32 s62, s60, 4456448\n"
    "buffer_load_dwordx4 v1, s[44:47], s62 offen lds\n"
    "s_add_u32 m0, s61, 94208\n"
    "s_add_u32 s62, s60, 4472832\n"
    "buffer_load_dwordx4 v1, s[44:47], s62 offen lds\n"
    "ds_read_b64_tr_b16 v[224:225], v127 offset:32768\n"
    "ds_read_b64_tr_b16 a[0:1], v126 offset:32768\n"
    "ds_read_b64_tr_b16 a[32:33], v125 offset:32768\n"
    "ds_read_b64_tr_b16 a[64:65], v124 offset:32768\n"
    "ds_read_b64_tr_b16 v[232:233], v123 offset:32768\n"
    "ds_read_b64_tr_b16 a[8:9], v122 offset:32768\n"
    "ds_read_b64_tr_b16 a[40:41], v121 offset:32768\n"
    "ds_read_b64_tr_b16 a[72:73], v120 offset:32768\n"
    "s_waitcnt vmcnt(28) lgkmcnt(0)\n"
    "s_barrier\n"
    "s_cbranch_vccz .Lsl14_%=\n"
    "s_sleep 10\n"
    ".Lsl14_%=:\n"
    "s_mov_b64 s[44:45], %10\n"
    "s_and_b32 s45, s45, 0xffff\n"
    "s_mov_b32 s46, 0x480000\n"
    "s_mov_b32 s47, 0x20000\n"
    "s_add_u32 m0, s61, 98304\n"
    "s_add_u32 s62, s60, 0\n"
    "buffer_load_dwordx4 v1, s[44:47], s62 offen lds\n"
    "s_add_u32 m0, s61, 102400\n"
    "s_add_u32 s62, s60, 1048576\n"
    "buffer_load_dwordx4 v1, s[44:47], s62 offen lds\n"
    "s_add_u32 m0, s61, 106496\n"
    "s_add_u32 s62, s60, 2097152\n"
    "buffer_load_dwordx4 v1, s[44:47], s62 offen lds\n"
    "s_add_u32 m0, s61, 110592\n"
    "s_add_u32 s62, s60, 3145728\n"
    "buffer_load_dwordx4 v1, s[44:47], s62 offen lds\n"
    "ds_read_b64_tr_b16 v[240:241], v127 offset:49152\n"
    "ds_read_b64_tr_b16 a[16:17], v126 offset:49152\n"
    "ds_read_b64_tr_b16 a[48:49], v125 offset:49152\n"
    "ds_read_b64_tr_b16 a[80:81], v124 offset:49152\n"
    "ds_read_b64_tr_b16 v[248:249], v123 offset:49152\n"
    "ds_read_b64_tr_b16 a[24:25], v122 offset:49152\n"
    "ds_read_b64_tr_b16 a[56:57], v121 offset:49152\n"
    "ds_read_b64_tr_b16 a[88:89], v120 offset:49152\n"
    "s_waitcnt vmcnt(28) lgkmcnt(0)\n"
    "s_barrier\n"
    "s_cbranch_vccz .Lsl15_%=\n"
    "s_sleep 10\n"
    ".Lsl15_%=:\n"
    "s_add_u32 m0, s61, 114688\n"
    "s_add_u32 s62, s60, 524288\n"
    "buffer_load_dwordx4 v1, s[44:47], s62 offen lds\n"
    "s_add_u32 m0, s61, 118784\n"
    "s_add_u32 s62, s60, 1572864\n"
    "buffer_load_dwordx4 v1, s[44:47], s62 offen lds\n"
    "s_add_u32 m0, s61, 122880\n"
    "s_add_u32 s62, s60, 2621440\n"
    "buffer_load_dwordx4 v1, s[44:47], s62 offen lds\n"
    "s_add_u32 m0, s61, 126976\n"
    "s_add_u32 s62, s60, 3670016\n"
    "buffer_load_dwordx4 v1, s[44:47], s62 offen lds\n"
    "v_add_u32 v117, 0x20000, v96\n"
    "ds_read_b64_tr_b16 v[226:227], v117\n"
    "v_add_u32 v116, 0x20000, v98\n"
    "ds_read_b64_tr_b16 a[2:3], v116\n"
    "v_add_u32 v115, 0x20000, v100\n"
    "ds_read_b64_tr_b16 a[34:35], v115\n"
    "v_add_u32 v114, 0x20000, v102\n"
    "ds_read_b64_tr_b16 a[66:67], v114\n"
    "v_add_u32 v113, 0x20000, v97\n"
    "ds_read_b64_tr_b16 v[234:235], v113\n"
    "v_add_u32 v112, 0x20000, v99\n"
    "ds_read_b64_tr_b16 a[10:11], v112\n"
    "v_add_u32 v111, 0x20000, v101\n"
    "ds_read_b64_tr_b16 a[42:43], v111\n"
    "v_add_u32 v110, 0x20000, v103\n"
    "ds_read_b64_tr_b16 a[74:75], v110\n"
    "s_waitcnt vmcnt(28) lgkmcnt(0)\n"
    "s_barrier\n"
    "s_cbranch_vccz .Lsl16_%=\n"
    "s_sleep 10\n"
    ".Lsl16_%=:\n"
    "s_add_u32 m0, s61, 131072\n"
    "s_add_u32 s62, s60, 16384\n"
    "buffer_load_dwordx4 v1, s[44:47], s62 offen lds\n"
    "s_add_u32 m0, s61, 135168\n"
    "s_add_u32 s62, s60, 1064960\n"
    "buffer_load_dwordx4 v1, s[44:47], s62 offen lds\n"
    "s_add_u32 m0, s61, 139264\n"
    "s_add_u32 s62, s60, 2113536\n"
    "buffer_load_dwordx4 v1, s[44:47], s62 offen lds\n"
    "s_add_u32 m0, s61, 143360\n"
    "s_add_u32 s62, s60, 3162112\n"
    "buffer_load_dwordx4 v1, s[44:47], s62 offen lds\n"
    "ds_read_b64_tr_b16 v[242:243], v117 offset:16384\n"
    "ds_read_b64_tr_b16 a[18:19], v116 offset:16384\n"
    "ds_read_b64_tr_b16 a[50:51], v115 offset:16384\n"
    "ds_read_b64_tr_b16 a[82:83], v114 offset:16384\n"
    "ds_read_b64_tr_b16 v[250:251], v113 offset:16384\n"
    "ds_read_b64_tr_b16 a[26:27], v112 offset:16384\n"
    "ds_read_b64_tr_b16 a[58:59], v111 offset:16384\n"
    "ds_read_b64_tr_b16 a[90:91], v110 offset:16384\n"
    "s_waitcnt vmcnt(28) lgkmcnt(0)\n"
    "s_barrier\n"
    "s_cbranch_vccz .Lsl17_%=\n"
    "s_sleep 10\n"
    ".Lsl17_%=:\n"
    "s_add_u32 m0, s61, 147456\n"
    "s_add_u32 s62, s60, 540672\n"
    "buffer_load_dwordx4 v1, s[44:47], s62 offen lds\n"
    "s_add_u32 m0, s61, 151552\n"
    "s_add_u32 s62, s60, 1589248\n"
    "buffer_load_dwordx4 v1, s[44:47], s62 offen lds\n"
    "s_add_u32 m0, s61, 155648\n"
    "s_add_u32 s62, s60, 2637824\n"
    "buffer_load_dwordx4 v1, s[44:47], s62 offen lds\n"
    "s_add_u32 m0, s61, 159744\n"
    "s_add_u32 s62, s60, 3686400\n"
    "buffer_load_dwordx4 v1, s[44:47], s62 offen lds\n"
    "ds_read_b64_tr_b16 v[228:229], v96 offset:16384\n"
    "ds_read_b64_tr_b16 a[4:5], v98 offset:16384\n"
    "ds_read_b64_tr_b16 a[36:37], v100 offset:16384\n"
    "ds_read_b64_tr_b16 a[68:69], v102 offset:16384\n"
    "ds_read_b64_tr_b16 v[236:237], v97 offset:16384\n"
    "ds_read_b64_tr_b16 a[12:13], v99 offset:16384\n"
    "ds_read_b64_tr_b16 a[44:45], v101 offset:16384\n"
    "ds_read_b64_tr_b16 a[76:77], v103 offset:16384\n"
    "s_waitcnt vmcnt(28) lgkmcnt(0)\n"
    "s_barrier\n"
    "s_cbranch_vccz .Lsl18_%=\n"
    "s_sleep 10\n"
    ".Lsl18_%=:\n"
    "s_add_u32 m0, s61, 16384\n"
    "s_add_u32 s62, s60, 262144\n"
    "buffer_load_dwordx4 v1, s[44:47], s62 offen lds\n"
    "s_add_u32 m0, s61, 20480\n"
    "s_add_u32 s62, s60, 1310720\n"
    "buffer_load_dwordx4 v1, s[44:47], s62 offen lds\n"
    "s_add_u32 m0, s61, 24576\n"
    "s_add_u32 s62, s60, 2359296\n"
    "buffer_load_dwordx4 v1, s[44:47], s62 offen lds\n"
    "s_add_u32 m0, s61, 28672\n"
    "s_add_u32 s62, s60, 3407872\n"
    "buffer_load_dwordx4 v1, s[44:47], s62 offen lds\n"
    "ds_read_b64_tr_b16 v[244:245], v96 offset:32768\n"
    "ds_read_b64_tr_b16 a[20:21], v98 offset:32768\n"
    "ds_read_b64_tr_b16 a[52:53], v100 offset:32768\n"
    "ds_read_b64_tr_b16 a[84:85], v102 offset:32768\n"
    "ds_read_b64_tr_b16 v[252:253], v97 offset:32768\n"
    "ds_read_b64_tr_b16 a[28:29], v99 offset:32768\n"
    "ds_read_b64_tr_b16 a[60:61], v101 offset:32768\n"
    "ds_read_b64_tr_b16 a[92:93], v103 offset:32768\n"
    "s_waitcnt vmcnt(28) lgkmcnt(0)\n"
    "s_barrier\n"
    "s_cbranch_vccz .Lsl19_%=\n"
    "s_sleep 10\n"
    ".Lsl19_%=:\n"
    "s_add_u32 m0, s61, 32768\n"
    "s_add_u32 s62, s60, 786432\n"
    "buffer_load_dwordx4 v1, s[44:47], s62 offen lds\n"
    "s_add_u32 m0, s61, 36864\n"
    "s_add_u32 s62, s60, 1835008\n"
    "buffer_load_dwordx4 v1, s[44:47], s62 offen lds\n"
    "s_add_u32 m0, s61, 40960\n"
    "s_add_u32 s62, s60, 2883584\n"
    "buffer_load_dwordx4 v1, s[44:47], s62 offen lds\n"
    "s_add_u32 m0, s61, 45056\n"
    "s_add_u32 s62, s60, 3932160\n"
    "buffer_load_dwordx4 v1, s[44:47], s62 offen lds\n"
    "ds_read_b64_tr_b16 v[230:231], v96 offset:49152\n"
    "ds_read_b64_tr_b16 a[6:7], v98 offset:49152\n"
    "ds_read_b64_tr_b16 a[38:39], v100 offset:49152\n"
    "ds_read_b64_tr_b16 a[70:71], v102 offset:49152\n"
    "ds_read_b64_tr_b16 v[238:239], v97 offset:49152\n"
    "ds_read_b64_tr_b16 a[14:15], v99 offset:49152\n"
    "ds_read_b64_tr_b16 a[46:47], v101 offset:49152\n"
    "ds_read_b64_tr_b16 a[78:79], v103 offset:49152\n"
    "s_waitcnt vmcnt(28) lgkmcnt(0)\n"
    "s_barrier\n"
    "s_cbranch_vccz .Lsl20_%=\n"
    "s_sleep 10\n"
    ".Lsl20_%=:\n"
    "s_add_u32 m0, s61, 49152\n"
    "s_add_u32 s62, s60, 278528\n"
    "buffer_load_dwordx4 v1, s[44:47], s62 offen lds\n"
    "s_add_u32 m0, s61, 53248\n"
    "s_add_u32 s62, s60, 1327104\n"
    "buffer_load_dwordx4 v1, s[44:47], s62 offen lds\n"
    "s_add_u32 m0, s61, 57344\n"
    "s_add_u32 s62, s60, 2375680\n"
    "buffer_load_dwordx4 v1, s[44:47], s62 offen lds\n"
    "s_add_u32 m0, s61, 61440\n"
    "s_add_u32 s62, s60, 3424256\n"
    "buffer_load_dwordx4 v1, s[44:47], s62 offen lds\n"
    "ds_read_b64_tr_b16 v[246:247], v127\n"
    "ds_read_b64_tr_b16 a[22:23], v126\n"
    "ds_read_b64_tr_b16 a[54:55], v125\n"
    "ds_read_b64_tr_b16 a[86:87], v124\n"
    "ds_read_b64_tr_b16 v[254:255], v123\n"
    "ds_read_b64_tr_b16 a[30:31], v122\n"
    "ds_read_b64_tr_b16 a[62:63], v121\n"
    "ds_read_b64_tr_b16 a[94:95], v120\n"
    "s_waitcnt vmcnt(28) lgkmcnt(0)\n"
    "s_barrier\n"
    "s_cbranch_vccz .Lsl21_%=\n"
    "s_sleep 10\n"
    ".Lsl21_%=:\n"
    "s_add_u32 m0, s61, 65536\n"
    "s_add_u32 s62, s60, 802816\n"
    "buffer_load_dwordx4 v1, s[44:47], s62 offen lds\n"
    "s_add_u32 m0, s61, 69632\n"
    "s_add_u32 s62, s60, 1851392\n"
    "buffer_load_dwordx4 v1, s[44:47], s62 offen lds\n"
    "s_add_u32 m0, s61, 73728\n"
    "s_add_u32 s62, s60, 2899968\n"
    "buffer_load_dwordx4 v1, s[44:47], s62 offen lds\n"
    "s_add_u32 m0, s61, 77824\n"
    "s_add_u32 s62, s60, 3948544\n"
    "buffer_load_dwordx4 v1, s[44:47], s62 offen lds\n"
    "ds_read_b64_tr_b16 a[96:97], v119 offset:16384\n"
    "ds_read_b64_tr_b16 a[98:99], v119 offset:20480\n"
    "ds_read_b64_tr_b16 a[100:101], v119 offset:24576\n"
    "ds_read_b64_tr_b16 a[102:103], v119 offset:28672\n"
    "ds_read_b64_tr_b16 a[104:105], v118 offset:16384\n"
    "ds_read_b64_tr_b16 a[106:107], v118 offset:20480\n"
    "ds_read_b64_tr_b16 a[108:109], v118 offset:24576\n"
    "ds_read_b64_tr_b16 a[110:111], v118 offset:28672\n"
    "s_waitcnt vmcnt(28) lgkmcnt(0)\n"
    "s_barrier\n"
    "s_cbranch_vccz .Lsl22_%=\n"
    "s_sleep 10\n"
    ".Lsl22_%=:\n"
    "s_add_u32 m0, s61, 81920\n"
    "s_add_u32 s62, s60, 4194304\n"
    "buffer_load_dwordx4 v1, s[44:47], s62 offen lds\n"
    "s_add_u32 m0, s61, 86016\n"
    "s_add_u32 s62, s60, 4210688\n"
    "buffer_load_dwordx4 v1, s[44:47], s62 offen lds\n"
    "s_add_u32 m0, s61, 90112\n"
    "s_add_u32 s62, s60, 4456448\n"
    "buffer_load_dwordx4 v1, s[44:47], s62 offen lds\n"
    "s_add_u32 m0, s61, 94208\n"
    "s_add_u32 s62, s60, 4472832\n"
    "buffer_load_dwordx4 v1, s[44:47], s62 offen lds\n"
    "ds_read_b64_tr_b16 a[112:113], v127 offset:32768\n"
    "ds_read_b64_tr_b16 a[144:145], v126 offset:32768\n"
    "ds_read_b64_tr_b16 a[176:177], v125 offset:32768\n"
    "ds_read_b64_tr_b16 a[208:209], v124 offset:32768\n"
    "ds_read_b64_tr_b16 a[120:121], v123 offset:32768\n"
    "ds_read_b64_tr_b16 a[152:153], v122 offset:32768\n"
    "ds_read_b64_tr_b16 a[184:185], v121 offset:32768\n"
    "ds_read_b64_tr_b16 a[216:217], v120 offset:32768\n"
    "s_waitcnt vmcnt(28) lgkmcnt(0)\n"
    "s_barrier\n"
    "s_cbranch_vccz .Lsl23_%=\n"
    "s_sleep 10\n"
    ".Lsl23_%=:\n"
    "s_mov_b64 s[44:45], %8\n"
    "s_and_b32 s45, s45, 0xffff\n"
    "s_mov_b32 s46, 0x480000\n"
    "s_mov_b32 s47, 0x20000\n"
    "s_add_u32 m0, s61, 98304\n"
    "s_add_u32 s62, s60, 0\n"
    "buffer_load_dwordx4 v1, s[44:47], s62 offen lds\n"
    "s_add_u32 m0, s61, 102400\n"
    "s_add_u32 s62, s60, 1048576\n"
    "buffer_load_dwordx4 v1, s[44:47], s62 offen lds\n"
    "s_add_u32 m0, s61, 106496\n"
    "s_add_u32 s62, s60, 2097152\n"
    "buffer_load_dwordx4 v1, s[44:47], s62 offen lds\n"
    "s_add_u32 m0, s61, 110592\n"
    "s_add_u32 s62, s60, 3145728\n"
    "buffer_load_dwordx4 v1, s[44:47], s62 offen lds\n"
    "ds_read_b64_tr_b16 a[128:129], v127 offset:49152\n"
    "ds_read_b64_tr_b16 a[160:161], v126 offset:49152\n"
    "ds_read_b64_tr_b16 a[192:193], v125 offset:49152\n"
    "ds_read_b64_tr_b16 a[224:225], v124 offset:49152\n"
    "ds_read_b64_tr_b16 a[136:137], v123 offset:49152\n"
    "ds_read_b64_tr_b16 a[168:169], v122 offset:49152\n"
    "ds_read_b64_tr_b16 a[200:201], v121 offset:49152\n"
    "ds_read_b64_tr_b16 a[232:233], v120 offset:49152\n"
    "s_waitcnt vmcnt(28) lgkmcnt(0)\n"
    "s_barrier\n"
    "s_cbranch_vccz .Lsl24_%=\n"
    "s_sleep 10\n"
    ".Lsl24_%=:\n"
    "s_add_u32 m0, s61, 114688\n"
    "s_add_u32 s62, s60, 524288\n"
    "buffer_load_dwordx4 v1, s[44:47], s62 offen lds\n"
    "s_add_u32 m0, s61, 118784\n"
    "s_add_u32 s62, s60, 1572864\n"
    "buffer_load_dwordx4 v1, s[44:47], s62 offen lds\n"
    "s_add_u32 m0, s61, 122880\n"
    "s_add_u32 s62, s60, 2621440\n"
    "buffer_load_dwordx4 v1, s[44:47], s62 offen lds\n"
    "s_add_u32 m0, s61, 126976\n"
    "s_add_u32 s62, s60, 3670016\n"
    "buffer_load_dwordx4 v1, s[44:47], s62 offen lds\n"
    "ds_read_b64_tr_b16 a[114:115], v117\n"
    "ds_read_b64_tr_b16 a[146:147], v116\n"
    "ds_read_b64_tr_b16 a[178:179], v115\n"
    "ds_read_b64_tr_b16 a[210:211], v114\n"
    "ds_read_b64_tr_b16 a[122:123], v113\n"
    "ds_read_b64_tr_b16 a[154:155], v112\n"
    "ds_read_b64_tr_b16 a[186:187], v111\n"
    "ds_read_b64_tr_b16 a[218:219], v110\n"
    "s_waitcnt vmcnt(28) lgkmcnt(0)\n"
    "s_barrier\n"
    "s_cbranch_vccz .Lsl25_%=\n"
    "s_sleep 10\n"
    ".Lsl25_%=:\n"
    "s_add_u32 m0, s61, 131072\n"
    "s_add_u32 s62, s60, 16384\n"
    "buffer_load_dwordx4 v1, s[44:47], s62 offen lds\n"
    "s_add_u32 m0, s61, 135168\n"
    "s_add_u32 s62, s60, 1064960\n"
    "buffer_load_dwordx4 v1, s[44:47], s62 offen lds\n"
    "s_add_u32 m0, s61, 139264\n"
    "s_add_u32 s62, s60, 2113536\n"
    "buffer_load_dwordx4 v1, s[44:47], s62 offen lds\n"
    "s_add_u32 m0, s61, 143360\n"
    "s_add_u32 s62, s60, 3162112\n"
    "buffer_load_dwordx4 v1, s[44:47], s62 offen lds\n"
    "ds_read_b64_tr_b16 a[130:131], v117 offset:16384\n"
    "ds_read_b64_tr_b16 a[162:163], v116 offset:16384\n"
    "ds_read_b64_tr_b16 a[194:195], v115 offset:16384\n"
    "ds_read_b64_tr_b16 a[226:227], v114 offset:16384\n"
    "ds_read_b64_tr_b16 a[138:139], v113 offset:16384\n"
    "ds_read_b64_tr_b16 a[170:171], v112 offset:16384\n"
    "ds_read_b64_tr_b16 a[202:203], v111 offset:16384\n"
    "ds_read_b64_tr_b16 a[234:235], v110 offset:16384\n"
    "s_waitcnt vmcnt(28) lgkmcnt(0)\n"
    "s_barrier\n"
    "s_cbranch_vccz .Lsl26_%=\n"
    "s_sleep 10\n"
    ".Lsl26_%=:\n"
    "s_add_u32 m0, s61, 147456\n"
    "s_add_u32 s62, s60, 540672\n"
    "buffer_load_dwordx4 v1, s[44:47], s62 offen lds\n"
    "s_add_u32 m0, s61, 151552\n"
    "s_add_u32 s62, s60, 1589248\n"
    "buffer_load_dwordx4 v1, s[44:47], s62 offen lds\n"
    "s_add_u32 m0, s61, 155648\n"
    "s_add_u32 s62, s60, 2637824\n"
    "buffer_load_dwordx4 v1, s[44:47], s62 offen lds\n"
    "s_add_u32 m0, s61, 159744\n"
    "s_add_u32 s62, s60, 3686400\n"
    "buffer_load_dwordx4 v1, s[44:47], s62 offen lds\n"
    "ds_read_b64_tr_b16 a[116:117], v96 offset:16384\n"
    "ds_read_b64_tr_b16 a[148:149], v98 offset:16384\n"
    "ds_read_b64_tr_b16 a[180:181], v100 offset:16384\n"
    "ds_read_b64_tr_b16 a[212:213], v102 offset:16384\n"
    "ds_read_b64_tr_b16 a[124:125], v97 offset:16384\n"
    "ds_read_b64_tr_b16 a[156:157], v99 offset:16384\n"
    "ds_read_b64_tr_b16 a[188:189], v101 offset:16384\n"
    "ds_read_b64_tr_b16 a[220:221], v103 offset:16384\n"
    "s_waitcnt vmcnt(28) lgkmcnt(0)\n"
    "s_barrier\n"
    "s_cbranch_vccz .Lsl27_%=\n"
    "s_sleep 10\n"
    ".Lsl27_%=:\n"
    "s_mov_b64 s[44:45], %11\n"
    "s_and_b32 s45, s45, 0xffff\n"
    "s_mov_b32 s46, 0x480000\n"
    "s_mov_b32 s47, 0x20000\n"
    "s_add_u32 m0, s61, 16384\n"
    "s_add_u32 s62, s63, 0\n"
    "buffer_load_dwordx4 v5, s[44:47], s62 offen lds\n"
    "s_add_u32 m0, s61, 20480\n"
    "s_add_u32 s62, s63, 9216\n"
    "buffer_load_dwordx4 v5, s[44:47], s62 offen lds\n"
    "s_add_u32 m0, s61, 24576\n"
    "s_add_u32 s62, s63, 18432\n"
    "buffer_load_dwordx4 v5, s[44:47], s62 offen lds\n"
    "s_add_u32 m0, s61, 28672\n"
    "s_add_u32 s62, s63, 27648\n"
    "buffer_load_dwordx4 v5, s[44:47], s62 offen lds\n"
    "ds_read_b64_tr_b16 a[132:133], v96 offset:32768\n"
    "ds_read_b64_tr_b16 a[164:165], v98 offset:32768\n"
    "ds_read_b64_tr_b16 a[196:197], v100 offset:32768\n"
    "ds_read_b64_tr_b16 a[228:229], v102 offset:32768\n"
    "ds_read_b64_tr_b16 a[140:141], v97 offset:32768\n"
    "ds_read_b64_tr_b16 a[172:173], v99 offset:32768\n"
    "ds_read_b64_tr_b16 a[204:205], v101 offset:32768\n"
    "ds_read_b64_tr_b16 a[236:237], v103 offset:32768\n"
    "s_waitcnt vmcnt(28) lgkmcnt(0)\n"
    "s_barrier\n"
    "s_cbranch_vccz .Lsl28_%=\n"
    "s_sleep 10\n"
    ".Lsl28_%=:\n"
    "s_add_u32 m0, s61, 32768\n"
    "s_add_u32 s62, s63, 36864\n"
    "buffer_load_dwordx4 v5, s[44:47], s62 offen lds\n"
    "s_add_u32 m0, s61, 36864\n"
    "s_add_u32 s62, s63, 46080\n"
    "buffer_load_dwordx4 v5, s[44:47], s62 offen lds\n"
    "s_add_u32 m0, s61, 40960\n"
    "s_add_u32 s62, s63, 55296\n"
    "buffer_load_dwordx4 v5, s[44:47], s62 offen lds\n"
    "s_add_u32 m0, s61, 45056\n"
    "s_add_u32 s62, s63, 64512\n"
    "buffer_load_dwordx4 v5, s[44:47], s62 offen lds\n"
    "ds_read_b64_tr_b16 a[118:119], v96 offset:49152\n"
    "ds_read_b64_tr_b16 a[150:151], v98 offset:49152\n"
    "ds_read_b64_tr_b16 a[182:183], v100 offset:49152\n"
    "ds_read_b64_tr_b16 a[214:215], v102 offset:49152\n"
    "ds_read_b64_tr_b16 a[126:127], v97 offset:49152\n"
    "ds_read_b64_tr_b16 a[158:159], v99 offset:49152\n"
    "ds_read_b64_tr_b16 a[190:191], v101 offset:49152\n"
    "ds_read_b64_tr_b16 a[222:223], v103 offset:49152\n"
    "s_waitcnt vmcnt(28) lgkmcnt(0)\n"
    "s_barrier\n"
    "s_cbranch_vccz .Lsl29_%=\n"
    "s_sleep 10\n"
    ".Lsl29_%=:\n"
    "s_add_u32 m0, s61, 49152\n"
    "s_add_u32 s62, s63, 73728\n"
    "buffer_load_dwordx4 v5, s[44:47], s62 offen lds\n"
    "s_add_u32 m0, s61, 53248\n"
    "s_add_u32 s62, s63, 82944\n"
    "buffer_load_dwordx4 v5, s[44:47], s62 offen lds\n"
    "s_add_u32 m0, s61, 57344\n"
    "s_add_u32 s62, s63, 92160\n"
    "buffer_load_dwordx4 v5, s[44:47], s62 offen lds\n"
    "s_add_u32 m0, s61, 61440\n"
    "s_add_u32 s62, s63, 101376\n"
    "buffer_load_dwordx4 v5, s[44:47], s62 offen lds\n"
    "ds_read_b64_tr_b16 a[134:135], v127\n"
    "ds_read_b64_tr_b16 a[166:167], v126\n"
    "ds_read_b64_tr_b16 a[198:199], v125\n"
    "ds_read_b64_tr_b16 a[230:231], v124\n"
    "ds_read_b64_tr_b16 a[142:143], v123\n"
    "ds_read_b64_tr_b16 a[174:175], v122\n"
    "ds_read_b64_tr_b16 a[206:207], v121\n"
    "ds_read_b64_tr_b16 a[238:239], v120\n"
    "s_waitcnt vmcnt(28) lgkmcnt(0)\n"
    "s_barrier\n"
    "s_cbranch_vccz .Lsl30_%=\n"
    "s_sleep 10\n"
    ".Lsl30_%=:\n"
    "s_add_u32 m0, s61, 65536\n"
    "s_add_u32 s62, s63, 110592\n"
    "buffer_load_dwordx4 v5, s[44:47], s62 offen lds\n"
    "s_add_u32 m0, s61, 69632\n"
    "s_add_u32 s62, s63, 119808\n"
    "buffer_load_dwordx4 v5, s[44:47], s62 offen lds\n"
    "s_add_u32 m0, s61, 73728\n"
    "s_add_u32 s62, s63, 129024\n"
    "buffer_load_dwordx4 v5, s[44:47], s62 offen lds\n"
    "s_add_u32 m0, s61, 77824\n"
    "s_add_u32 s62, s63, 138240\n"
    "buffer_load_dwordx4 v5, s[44:47], s62 offen lds\n"
    "ds_read_b64_tr_b16 a[240:241], v119 offset:16384\n"
    "ds_read_b64_tr_b16 a[242:243], v119 offset:20480\n"
    "ds_read_b64_tr_b16 a[244:245], v119 offset:24576\n"
    "ds_read_b64_tr_b16 a[246:247], v119 offset:28672\n"
    "ds_read_b64_tr_b16 a[248:249], v118 offset:16384\n"
    "ds_read_b64_tr_b16 a[250:251], v118 offset:20480\n"
    "ds_read_b64_tr_b16 a[252:253], v118 offset:24576\n"
    "ds_read_b64_tr_b16 a[254:255], v118 offset:28672\n"
    "s_waitcnt vmcnt(28) lgkmcnt(0)\n"
    "s_barrier\n"
    "ds_read_b64_tr_b16 v[32:33], v127 offset:32768\n"
    "ds_read_b64_tr_b16 v[64:65], v126 offset:32768\n"
    "ds_read_b64_tr_b16 v[144:145], v125 offset:32768\n"
    "ds_read_b64_tr_b16 v[176:177], v124 offset:32768\n"
    "ds_read_b64_tr_b16 v[40:41], v123 offset:32768\n"
    "ds_read_b64_tr_b16 v[72:73], v122 offset:32768\n"
    "ds_read_b64_tr_b16 v[152:153], v121 offset:32768\n"
    "ds_read_b64_tr_b16 v[184:185], v120 offset:32768\n"
    "s_waitcnt vmcnt(24) lgkmcnt(0)\n"
    "s_barrier\n"
    "ds_read_b64_tr_b16 v[48:49], v127 offset:49152\n"
    "ds_read_b64_tr_b16 v[80:81], v126 offset:49152\n"
    "ds_read_b64_tr_b16 v[160:161], v125 offset:49152\n"
    "ds_read_b64_tr_b16 v[192:193], v124 offset:49152\n"
    "ds_read_b64_tr_b16 v[56:57], v123 offset:49152\n"
    "ds_read_b64_tr_b16 v[136:137], v122 offset:49152\n"
    "ds_read_b64_tr_b16 v[168:169], v121 offset:49152\n"
    "ds_read_b64_tr_b16 v[200:201], v120 offset:49152\n"
    "s_waitcnt vmcnt(20) lgkmcnt(0)\n"
    "s_barrier\n"
    "ds_read_b64_tr_b16 v[34:35], v117\n"
    "ds_read_b64_tr_b16 v[66:67], v116\n"
    "ds_read_b64_tr_b16 v[146:147], v115\n"
    "ds_read_b64_tr_b16 v[178:179], v114\n"
    "ds_read_b64_tr_b16 v[42:43], v113\n"
    "ds_read_b64_tr_b16 v[74:75], v112\n"
    "ds_read_b64_tr_b16 v[154:155], v111\n"
    "ds_read_b64_tr_b16 v[186:187], v110\n"
    "s_waitcnt vmcnt(16) lgkmcnt(0)\n"
    "s_barrier\n"
    "ds_read_b64_tr_b16 v[50:51], v117 offset:16384\n"
    "ds_read_b64_tr_b16 v[82:83], v116 offset:16384\n"
    "ds_read_b64_tr_b16 v[162:163], v115 offset:16384\n"
    "ds_read_b64_tr_b16 v[194:195], v114 offset:16384\n"
    "ds_read_b64_tr_b16 v[58:59], v113 offset:16384\n"
    "ds_read_b64_tr_b16 v[138:139], v112 offset:16384\n"
    "ds_read_b64_tr_b16 v[170:171], v111 offset:16384\n"
    "ds_read_b64_tr_b16 v[202:203], v110 offset:16384\n"
    "s_waitcnt vmcnt(12) lgkmcnt(0)\n"
    "s_barrier\n"
    "ds_read_b64_tr_b16 v[88:89], v6 offset:16384\n"
    "ds_read_b64_tr_b16 v[90:91], v6 offset:17408\n"
    "ds_read_b64_tr_b16 v[92:93], v6 offset:20480\n"
    "ds_read_b64_tr_b16 v[94:95], v6 offset:21504\n"
    "ds_read_b64_tr_b16 v[96:97], v6 offset:16416\n"
    "ds_read_b64_tr_b16 v[98:99], v6 offset:17440\n"
    "ds_read_b64_tr_b16 v[100:101], v6 offset:20512\n"
    "ds_read_b64_tr_b16 v[102:103], v6 offset:21536\n"
    "s_waitcnt vmcnt(8) lgkmcnt(0)\n"
    "s_barrier\n"
    "ds_read_b64_tr_b16 v[104:105], v6 offset:32768\n"
    "ds_read_b64_tr_b16 v[106:107], v6 offset:33792\n"
    "ds_read_b64_tr_b16 v[108:109], v6 offset:36864\n"
    "ds_read_b64_tr_b16 v[110:111], v6 offset:37888\n"
    "ds_read_b64_tr_b16 v[112:113], v6 offset:32800\n"
    "ds_read_b64_tr_b16 v[114:115], v6 offset:33824\n"
    "ds_read_b64_tr_b16 v[116:117], v6 offset:36896\n"
    "ds_read_b64_tr_b16 v[118:119], v6 offset:37920\n"
    "s_waitcnt vmcnt(4) lgkmcnt(0)\n"
    "s_barrier\n"
    "ds_read_b64_tr_b16 v[120:121], v6 offset:49152\n"
    "ds_read_b64_tr_b16 v[122:123], v6 offset:50176\n"
    "ds_read_b64_tr_b16 v[124:125], v6 offset:53248\n"
    "ds_read_b64_tr_b16 v[126:127], v6 offset:54272\n"
    "ds_read_b64_tr_b16 v[128:129], v6 offset:49184\n"
    "ds_read_b64_tr_b16 v[130:131], v6 offset:50208\n"
    "ds_read_b64_tr_b16 v[132:133], v6 offset:53280\n"
    "ds_read_b64_tr_b16 v[134:135], v6 offset:54304\n"
    "s_waitcnt vmcnt(0) lgkmcnt(0)\n"
    "s_barrier\n"
    "v_add_u32 v31, 0x10000, v6\n"
    "ds_read_b64_tr_b16 v[12:13], v31\n"
    "ds_read_b64_tr_b16 v[14:15], v31 offset:1024\n"
    "ds_read_b64_tr_b16 v[16:17], v31 offset:4096\n"
    "ds_read_b64_tr_b16 v[18:19], v31 offset:5120\n"
    "ds_read_b64_tr_b16 v[20:21], v31 offset:32\n"
    "ds_read_b64_tr_b16 v[22:23], v31 offset:1056\n"
    "ds_read_b64_tr_b16 v[24:25], v31 offset:4128\n"
    "ds_read_b64_tr_b16 v[26:27], v31 offset:5152\n"
    "s_waitcnt lgkmcnt(0)\n"
    "s_barrier\n"
    "s_xor_b32 s73, %2, 0\n"
    "s_lshl_b32 s73, s73, 12\n"
    "v_add_u32 v6, s73, v2\n"
    "ds_write_b128 v6, v[88:91] offset:14336\n"
    "ds_write_b128 v6, v[92:95] offset:15360\n"
    "ds_write_b128 v6, v[96:99] offset:16384\n"
    "ds_write_b128 v6, v[100:103] offset:17408\n"
    "s_xor_b32 s73, %2, 1\n"
    "s_lshl_b32 s73, s73, 12\n"
    "v_add_u32 v6, s73, v2\n"
    "ds_write_b128 v6, v[104:107] offset:14336\n"
    "ds_write_b128 v6, v[108:111] offset:15360\n"
    "ds_write_b128 v6, v[112:115] offset:16384\n"
    "ds_write_b128 v6, v[116:119] offset:17408\n"
    "s_xor_b32 s73, %2, 2\n"
    "s_lshl_b32 s73, s73, 12\n"
    "v_add_u32 v6, s73, v2\n"
    "ds_write_b128 v6, v[120:123] offset:14336\n"
    "ds_write_b128 v6, v[124:127] offset:15360\n"
    "ds_write_b128 v6, v[128:131] offset:16384\n"
    "ds_write_b128 v6, v[132:135] offset:17408\n"
    "s_xor_b32 s73, %2, 3\n"
    "s_lshl_b32 s73, s73, 12\n"
    "v_add_u32 v6, s73, v2\n"
    "ds_write_b128 v6, v[12:15] offset:14336\n"
    "ds_write_b128 v6, v[16:19] offset:15360\n"
    "ds_write_b128 v6, v[20:23] offset:16384\n"
    "ds_write_b128 v6, v[24:27] offset:17408\n"
    "ds_read_b64_tr_b16 v[88:89], v9\n"
    "ds_read_b64_tr_b16 v[90:91], v9 offset:128\n"
    "ds_read_b64_tr_b16 v[92:93], v9 offset:2048\n"
    "ds_read_b64_tr_b16 v[94:95], v9 offset:2176\n"
    "ds_read_b64_tr_b16 v[96:97], v9 offset:4096\n"
    "ds_read_b64_tr_b16 v[98:99], v9 offset:4224\n"
    "ds_read_b64_tr_b16 v[100:101], v9 offset:6144\n"
    "ds_read_b64_tr_b16 v[102:103], v9 offset:6272\n"
    "ds_write_b128 v2, v[32:35]\n"
    "ds_write_b128 v2, v[36:39] offset:1024\n"
    "ds_write_b128 v2, v[40:43] offset:2048\n"
    "ds_write_b128 v2, v[44:47] offset:3072\n"
    "ds_write_b128 v2, v[48:51] offset:4096\n"
    "ds_write_b128 v2, v[52:55] offset:5120\n"
    "ds_write_b128 v2, v[56:59] offset:6144\n"
    "ds_write_b128 v2, v[60:63] offset:7168\n"
    "ds_write_b128 v2, v[64:67] offset:8192\n"
    "ds_write_b128 v2, v[68:71] offset:9216\n"
    "ds_write_b128 v2, v[72:75] offset:10240\n"
    "ds_write_b128 v2, v[76:79] offset:11264\n"
    "ds_write_b128 v2, v[80:83] offset:12288\n"
    "ds_write_b128 v2, v[84:87] offset:13312\n"
    "v_lshrrev_b32 v112, 4, v0\n"
    "v_and_b32 v113, 15, v112\n"
    "v_lshrrev_b32 v114, 4, v112\n"
    "v_mov_b32 v11, 0\n"
    "v_lshrrev_b32 v115, 1, v113\n"
    "v_bfi_b32 v111, 1, v113, v115\n"
    "v_and_b32 v115, 1, v115\n"
    "v_lshl_add_u32 v115, v114, 1, v115\n"
    "v_mul_u32_u24 v117, 1152, v111\n"
    "v_lshl_add_u32 v117, v115, 4, v117\n"
    "v_and_b32 v115, 1, v114\n"
    "v_lshrrev_b32 v116, 1, v114\n"
    "v_mov_b32 v120, 0\n"
    "v_mov_b32 v121, 0\n"
    "v_mov_b32 v122, 0\n"
    "v_mov_b32 v123, 0\n"
    "ds_write_b128 v11, v[120:123] offset:31872\n"
    "v_mov_b32 v124, 0x3f803f80\n"
    "v_mov_b32 v125, 0x3f803f80\n"
    "v_mov_b32 v126, 0x3f803f80\n"
    "v_mov_b32 v127, 0x3f803f80\n"
    "ds_write_b128 v11, v[124:127] offset:31888\n"
    "v_mov_b32 v128, 0x3f003f00\n"
    "v_mov_b32 v129, 0x3f003f00\n"
    "v_mov_b32 v130, 0x3f003f00\n"
    "v_mov_b32 v131, 0x3f003f00\n"
    "ds_write_b128 v11, v[128:131] offset:31904\n"
    "v_mul_u32_u24 v118, 57, v112\n"
    "v_lshrrev_b32 v118, 9, v118\n"
    "v_mul_u32_u24 v118, 9, v118\n"
    "v_cmp_eq_u32 vcc, v118, v112\n"
    "v_lshlrev_b32 v118, 1, v112\n"
    "s_nop 0\n"
    "v_cndmask_b32 v120, v11, v124, vcc\n"
    "ds_write_b16 v118, v120 offset:31936\n"
    "s_waitcnt lgkmcnt(0)\n"
    "s_cmp_lt_u32 %2, 2\n"
    "s_cbranch_scc1 .Lsk_%=\n"
    "ds_write_b128 v2, v[88:91] offset:30720\n"
    "ds_write_b128 v2, v[92:95] offset:31744\n"
    "ds_write_b128 v2, v[96:99] offset:32768\n"
    "ds_write_b128 v2, v[100:103] offset:33792\n"
    ".Lsk_%=:\n"
    "s_cmp_eq_u32 %2, 3\n"
    "s_cbranch_scc1 .Lsm_%=\n"
    "s_mov_b64 s[44:45], %12\n"
    "s_and_b32 s45, s45, 0xffff\n"
    "s_mov_b32 s46, 0x2400\n"
    "s_mov_b32 s47, 0x20000\n"
    "s_mul_i32 s62, %2, 384\n"
    "s_mul_i32 s61, %2, 3072\n"
    "s_add_u32 m0, s61, 20480\n"
    "s_add_u32 s73, s62, 0\n"
    "buffer_load_dwordx4 v117, s[44:47], s73 offen lds\n"
    "s_add_u32 m0, s61, 21504\n"
    "s_add_u32 s73, s62, 128\n"
    "buffer_load_dwordx4 v117, s[44:47], s73 offen lds\n"
    "s_add_u32 m0, s61, 22528\n"
    "s_add_u32 s73, s62, 256\n"
    "buffer_load_dwordx4 v117, s[44:47], s73 offen lds\n"
    ".Lsm_%=:\n"
    "v_mov_b32 v130, 0x20400\n"
    "global_load_dwordx2 v[132:133], v130, %1 sc1\n"
    "v_mbcnt_lo_u32_b32 v131, -1, 0\n"
    "v_min_u32 v131, 7, v131\n"
    "v_lshlrev_b32 v131, 7, v131\n"
    "v_add_u32 v131, 0x20000, v131\n"
    "global_load_dwordx2 v[134:135], v131, %1 sc1\n"
    "s_waitcnt vmcnt(0) lgkmcnt(0)\n"
    "s_barrier\n"
    "v_mov_b32 v1, 0x4444\n"
    "v_mov_b32 v118, 0xeeee\n"
    "v_and_b32 v4, 2, v113\n"
    "v_cmp_eq_u32 vcc, 0, v4\n"
    "s_nop 1\n"
    "v_cndmask_b32 v1, v118, v1, vcc\n"
    "v_xor_b32 v118, 7, v111\n"
    "v_cndmask_b32 v4, v118, v111, vcc\n"
    "s_lshl_b32 s73, %2, 10\n"
    "v_add_u32 v5, s73, v0\n"
    "v_and_b32 v119, 7, v113\n"
    "v_lshrrev_b32 v120, 3, v113\n"
    "v_mul_u32_u24 v3, 544, v114\n"
    "v_lshl_add_u32 v3, v113, 2, v3\n"
    "s_lshl_b32 s73, %2, 6\n"
    "s_add_u32 s73, s73, 20480\n"
    "v_add_u32 v3, s73, v3\n"
    "v_mul_u32_u24 v4, 272, v4\n"
    "v_lshl_add_u32 v4, v114, 4, v4\n"
    "v_and_b32 v6, 13, v113\n"
    "v_lshl_add_u32 v6, v116, 4, v6\n"
    "v_lshl_add_u32 v6, v115, 1, v6\n"
    "s_lshl_b32 s73, %2, 9\n"
    "v_lshl_add_u32 v6, v6, 4, s73\n"
    "s_waitcnt vmcnt(0)\n"
    "v_add_u32 v132, 1, v132\n"
    "v_add_u32 v133, 1, v133\n"
    "s_mov_b32 s56, 1048576\n"
    ".Lcgo_a%=:\n"
    "v_cmp_ne_u64 vcc, v[132:133], v[134:135]\n"
    "s_cbranch_vccz .Lcgo_x%=\n"
    "global_load_dwordx2 v[134:135], v131, %1 sc1\n"
    "s_sub_u32 s56, s56, 1\n"
    "s_waitcnt vmcnt(0)\n"
    "s_cbranch_scc0 .Lcgo_a%=\n"
    ".Lcgo_x%=:\n"
    "s_mov_b32 s56, 0\n"
    "v_mov_b32 v134, 64\n"
    ".Lpoll1_%=:\n"
    "global_load_dword v135, v134, %1 sc1\n"
    "s_waitcnt vmcnt(0)\n"
    "v_readfirstlane_b32 s73, v135\n"
    "s_cmp_ge_u32 s73, 4\n"
    "s_cbranch_scc1 .Lpoll1_x%=\n"
    "s_add_u32 s56, s56, 1\n"
    "s_cmp_lt_u32 s56, 3\n"
    "s_cbranch_scc1 .Lpoll1_%=\n"
    "s_sleep 8\n"
    "s_cmp_lt_u32 s56, s57\n"
    "s_cbranch_scc1 .Lpoll1_%=\n"
    "s_mov_b32 s57, 0\n"
    "v_mov_b32 v135, 1\n"
    "global_store_dword v11, v135, %1 sc1\n"
    ".Lpoll1_x%=:\n"
    "s_xor_b32 s69, s73, %7\n"
    "s_lshr_b32 s51, s73, 1\n"
    "s_xor_b32 s51, s51, %7\n"
    "s_and_b32 s51, s51, 1\n"
    "s_lshl_b32 s51, s51, 13\n"
    "s_add_u32 s68, s51, 0x200000\n"
    "s_and_b32 s60, s69, 1\n"
    "s_lshl_b32 s60, s60, 13\n"
    "v_lshl_add_u32 v8, v116, 2, v115\n"
    "v_lshlrev_b32 v8, 12, v8\n"
    "v_lshl_add_u32 v8, v119, 4, v8\n"
    "v_lshl_add_u32 v7, v120, 15, v8\n"
    "v_lshl_add_u32 v8, v120, 30, v8\n"
    "s_mul_i32 s73, %5, 294912\n"
    "s_lshl_b32 s51, %4, 7\n"
    "s_add_u32 s73, s73, s51\n"
    "s_add_u32 s73, s73, s60\n"
    "s_lshl_b32 s51, %2, 16\n"
    "s_add_u32 s51, s51, s73\n"
    "v_add_u32 v7, s51, v7\n"
    "s_lshl3_add_u32 s51, %2, s73\n"
    "s_add_u32 s51, s51, 262128\n"
    "s_cmp_lt_u32 %2, 2\n"
    "s_cselect_b32 s51, 0x80000000, s51\n"
    "v_add_u32 v8, s51, v8\n"
    "s_lshl_b32 s61, %4, 1\n"
    "s_add_u32 s61, s61, %5\n"
    "s_lshl4_add_u32 s44, s61, 1024\n"
    "s_lshr_b32 s73, s61, 3\n"
    "s_add_u32 s73, s73, 64\n"
    "s_and_b32 s51, s61, 7\n"
    "s_add_u32 s51, s51, %2\n"
    "s_cmp_eq_u32 s51, 1\n"
    "s_cselect_b32 s71, s73, -1\n"
    "s_cmp_eq_u32 %2, 0\n"
    "s_cselect_b32 s71, s61, s71\n"
    "s_max_i32 s61, s71, 0\n"
    "s_lshr_b32 s73, s61, 1\n"
    "s_lshl_b32 s73, s73, 14\n"
    "s_and_b32 s51, s61, 1\n"
    "s_lshl_b32 s51, s51, 12\n"
    "s_add_u32 s65, s73, s51\n"
    "s_add_u32 s65, s65, s60\n"
    "s_lshr_b32 s94, s61, 5\n"
    "s_and_b32 s73, s61, 31\n"
    "s_lshl_b32 s73, s73, 7\n"
    "v_lshl_add_u32 v9, v114, 2, s73\n"
    "v_lshl_add_u32 v9, v119, 4, v9\n"
    "v_lshl_add_u32 v9, v120, 22, v9\n"
    "s_lshl_b32 s73, %2, 11\n"
    "v_add_u32 v10, s73, v0\n"
    "s_cmp_ge_u32 %2, 2\n"
    "s_cbranch_scc0 .Lnw3_%=\n"
    "v_lshl_add_u32 v9, v113, 1, %5\n"
    "v_lshlrev_b32 v118, 3, v114\n"
    "v_sub_u32 v9, v9, v118\n"
    "v_cmp_le_i32 s[76:77], 0, v9\n"
    "v_cmp_le_i32 s[78:79], 2, v9\n"
    "v_cmp_le_i32 s[80:81], 4, v9\n"
    "v_cmp_le_i32 s[82:83], 6, v9\n"
    "v_cmp_le_i32 s[84:85], 1, v9\n"
    "v_cmp_le_i32 s[86:87], 3, v9\n"
    "v_cmp_le_i32 s[88:89], 5, v9\n"
    "v_cmp_le_i32 s[90:91], 7, v9\n"
    ".Lnw3_%=:\n"
    "s_lshr_b32 s73, %4, 2\n"
    "s_lshl1_add_u32 s73, s73, %7\n"
    "s_and_b32 s51, %4, 3\n"
    "s_lshl3_add_u32 s73, s73, s51\n"
    "s_lshl_b32 s73, s73, 8\n"
    "s_add_u32 s92, s73, 86016\n"
    "s_lshr_b32 s51, %4, 3\n"
    "s_lshl2_add_u32 s73, %5, s51\n"
    "s_lshl1_add_u32 s73, s73, %7\n"
    "s_and_b32 s51, %4, 7\n"
    "s_lshl4_add_u32 s73, s73, s51\n"
    "s_lshl_b32 s73, s73, 7\n"
    "s_add_u32 s93, s73, 53248\n"
    "s_lshl_b32 s73, %5, 12\n"
    "s_lshl_b32 s51, %4, 14\n"
    "s_add_u32 s73, s73, s51\n"
    "s_add_u32 s75, s73, 0x9fff000\n"
    "s_brev_b32 s45, 1\n"
    "s_mov_b32 s46, 0x3f803f80\n"
    "s_mov_b32 s47, s46\n"
    "s_mov_b32 s48, 0\n"
    "s_mov_b32 s54, 60\n"
    "s_mov_b32 s55, 84\n"
    "s_mov_b32 s96, 672\n"
    "s_mov_b32 s56, 0\n"
    "v_mov_b32 v134, 128\n"
    ".Lpoll2_%=:\n"
    "global_load_dword v135, v134, %1 sc1\n"
    "s_waitcnt vmcnt(0)\n"
    "v_readfirstlane_b32 s73, v135\n"
    "s_cmp_ge_u32 s73, 64\n"
    "s_cbranch_scc1 .Lpoll2_x%=\n"
    "s_add_u32 s56, s56, 1\n"
    "s_cmp_lt_u32 s56, 3\n"
    "s_cbranch_scc1 .Lpoll2_%=\n"
    "s_sleep 8\n"
    "s_cmp_lt_u32 s56, s57\n"
    "s_cbranch_scc1 .Lpoll2_%=\n"
    "s_mov_b32 s57, 0\n"
    "v_mov_b32 v135, 1\n"
    "global_store_dword v11, v135, %1 sc1\n"
    ".Lpoll2_x%=:\n"
    "ds_read_b128 v[32:35], v2\n"
    "ds_read_b128 v[36:39], v2 offset:1024\n"
    "ds_read_b128 v[40:43], v2 offset:2048\n"
    "ds_read_b128 v[44:47], v2 offset:3072\n"
    "ds_read_b128 v[48:51], v2 offset:4096\n"
    "ds_read_b128 v[52:55], v2 offset:5120\n"
    "ds_read_b128 v[56:59], v2 offset:6144\n"
    "ds_read_b128 v[60:63], v2 offset:7168\n"
    "ds_read_b128 v[64:67], v2 offset:8192\n"
    "ds_read_b128 v[68:71], v2 offset:9216\n"
    "ds_read_b128 v[72:75], v2 offset:10240\n"
    "ds_read_b128 v[76:79], v2 offset:11264\n"
    "ds_read_b128 v[80:83], v2 offset:12288\n"
    "ds_read_b128 v[84:87], v2 offset:13312\n"
    "v_mov_b64 v[124:125], 0\n"
    "v_mov_b64 v[126:127], 0\n"
    "v_mov_b64 v[128:129], 0\n"
    "v_mov_b64 v[130:131], 0\n"
    "s_add_u32 s73, s48, 0\n"
    "s_and_b32 s73, s73, 63\n"
    "s_lshr_b32 s51, s73, 2\n"
    "s_xor_b32 s51, s51, s69\n"
    "s_and_b32 s51, s51, 1\n"
    "s_lshl_b32 s51, s51, 13\n"
    "s_lshl_b32 s73, s73, 5\n"
    "s_add_u32 s73, s73, %4\n"
    "s_lshl_b32 s73, s73, 14\n"
    "s_add_u32 s73, s73, s51\n"
    "s_add_u32 s60, s73, 0x6000000\n"
    "s_add_u32 s61, s73, 0x6001000\n"
    "s_add_u32 s62, s73, 0x8000000\n"
    "s_add_u32 s63, s73, 0x8001000\n"
    "s_lshl_b32 s51, %5, 12\n"
    "s_add_u32 s73, s73, s51\n"
    "s_add_u32 s64, s73, 0x4000000\n"
    "s_mul_i32 s73, s48, 3\n"
    "s_add_u32 s73, s73, 2\n"
    "s_and_b32 s51, s73, 1\n"
    "s_lshl_b32 s51, s51, 12\n"
    "s_lshr_b32 s73, s73, 1\n"
    "s_lshl_b32 s73, s73, 14\n"
    "s_add_u32 s73, s73, s51\n"
    "s_add_u32 s58, s73, s68\n"
    "s_waitcnt lgkmcnt(0)\n"
    "s_mov_b32 s56, 0\n"
    "v_mov_b32 v134, s92\n"
    ".Lpoll3_%=:\n"
    "global_load_dword v132, v134, %1 sc1\n"
    "global_load_dword v133, v134, %1 offset:128 sc1\n"
    "s_waitcnt vmcnt(0)\n"
    "v_and_b32 v133, v132, v133\n"
    "s_nop 0\n"
    "v_readfirstlane_b32 s73, v133\n"
    "s_cmp_ge_u32 s73, 1\n"
    "s_cbranch_scc1 .Lpoll3_x%=\n"
    "s_add_u32 s56, s56, 1\n"
    "s_cmp_lt_u32 s56, 3\n"
    "s_cbranch_scc1 .Lpoll3_%=\n"
    "s_sleep 8\n"
    "s_cmp_lt_u32 s56, s57\n"
    "s_cbranch_scc1 .Lpoll3_%=\n"
    "s_mov_b32 s57, 0\n"
    "v_mov_b32 v135, 1\n"
    "global_store_dword v11, v135, %1 sc1\n"
    ".Lpoll3_x%=:\n"
    "buffer_load_dwordx4 v[12:15], v5, s[40:43], s60 offen sc1\n"
    "buffer_load_dwordx4 v[16:19], v5, s[40:43], s61 offen sc1\n"
    "buffer_load_dwordx4 v[20:23], v5, s[40:43], s62 offen sc1\n"
    "buffer_load_dwordx4 v[24:27], v5, s[40:43], s63 offen sc1\n"
    "buffer_load_dwordx4 v[28:31], v5, s[40:43], s64 offen sc1\n"
    "v_xor_b32 v132, 0x800, v10\n"
    "v_xor_b32 v133, 0x1000, v10\n"
    "v_xor_b32 v134, 0x1800, v10\n"
    "v_lshrrev_b32 v135, 2, v5\n"
    "s_movk_i32 s73, 0x180\n"
    "v_lshlrev_b32 v102, 1, v0\n"
    "v_bfi_b32 v102, s73, v102, v0\n"
    "v_lshrrev_b32 v101, 2, v0\n"
    "v_bfi_b32 v102, 64, v101, v102\n"
    "v_lshlrev_b32 v103, 1, v135\n"
    "v_bfi_b32 v103, s73, v103, v135\n"
    "v_lshrrev_b32 v101, 2, v135\n"
    "v_bfi_b32 v103, 64, v101, v103\n"
    "ds_read_b128 v[88:91], v10 offset:20480\n"
    "ds_read_b128 v[92:95], v10 offset:21504\n"
    "ds_read_b32 v96, v135 offset:28672\n"
    "s_add_u32 s73, s48, 1\n"
    "s_and_b32 s73, s73, 63\n"
    "s_lshr_b32 s51, s73, 2\n"
    "s_xor_b32 s51, s51, s69\n"
    "s_and_b32 s51, s51, 1\n"
    "s_lshl_b32 s51, s51, 13\n"
    "s_lshl_b32 s73, s73, 5\n"
    "s_add_u32 s73, s73, %4\n"
    "s_lshl_b32 s73, s73, 14\n"
    "s_add_u32 s73, s73, s51\n"
    "s_add_u32 s60, s73, 0x6000000\n"
    "s_add_u32 s61, s73, 0x6001000\n"
    "s_add_u32 s62, s73, 0x8000000\n"
    "s_add_u32 s63, s73, 0x8001000\n"
    "s_lshl_b32 s51, %5, 12\n"
    "s_add_u32 s73, s73, s51\n"
    "s_add_u32 s64, s73, 0x4000000\n"
    "s_add_u32 s73, s48, 1\n"
    "s_and_b32 s51, s73, 32\n"
    "s_and_b32 s73, s73, 31\n"
    "s_lshl3_add_u32 s73, s51, s73\n"
    "s_lshl2_add_u32 s73, s73, s92\n"
    "s_waitcnt lgkmcnt(2)\n"
    "v_min_u32 v88, -2, v88\n"
    "v_min_u32 v89, -2, v89\n"
    "v_min_u32 v90, -2, v90\n"
    "v_min_u32 v91, -2, v91\n"
    "s_waitcnt lgkmcnt(1)\n"
    "v_min_u32 v92, -2, v92\n"
    "v_min_u32 v93, -2, v93\n"
    "v_min_u32 v94, -2, v94\n"
    "v_min_u32 v95, -2, v95\n"
    "s_waitcnt lgkmcnt(0)\n"
    "v_min_u32 v96, -2, v96\n"
    "s_waitcnt vmcnt(4)\n"
    "ds_write_b128 v5, v[12:15]\n"
    "s_waitcnt vmcnt(3)\n"
    "ds_write_b128 v5, v[16:19] offset:4096\n"
    "s_waitcnt vmcnt(0)\n"
    "ds_write_b128 v5, v[28:31] offset:16384\n"
    "v_swap_b32 v22, v24\n"
    "v_swap_b32 v23, v25\n"
    "ds_write_b128 v5, v[20:23] offset:8192\n"
    "ds_write_b128 v5, v[24:27] offset:12288\n"
    "v_mov_b32 v14, s73\n"
    "global_load_dword v12, v14, %1 sc1\n"
    "global_load_dword v13, v14, %1 offset:128 sc1\n"
    "global_load_dword v14, v14, %1 sc1\n"
    "v_mov_b64 v[16:17], 0\n"
    "v_mov_b64 v[18:19], 0\n"
    "v_mov_b64 v[20:21], 0\n"
    "v_mov_b64 v[22:23], 0\n"
    "v_mov_b64 v[24:25], 0\n"
    "v_mov_b64 v[26:27], 0\n"
    "v_mov_b64 v[28:29], 0\n"
    "v_mov_b64 v[30:31], 0\n"
    ".Lstep_%=:\n"
    "s_waitcnt vmcnt(5)\n"
    "v_smfmac_f32_16x16x64_bf16 v[16:19], v[88:91], v[32:39], v1\n"
    "ds_write_b128 v10, v[88:91] offset:20480\n"
    "v_smfmac_f32_16x16x64_bf16 v[20:23], v[88:91], v[40:47], v1\n"
    "s_waitcnt vmcnt(4)\n"
    "ds_write_b128 v10, v[92:95] offset:21504\n"
    "v_max3_u32 v100, v88, v89, v90\n"
    "v_smfmac_f32_16x16x64_bf16 v[16:19], v[92:95], v[48:55], v1\n"
    "s_waitcnt vmcnt(3)\n"
    "ds_write_b32 v135, v96 offset:28672\n"
    "v_max3_u32 v100, v100, v91, v92\n"
    "v_smfmac_f32_16x16x64_bf16 v[20:23], v[92:95], v[56:63], v1\n"
    "v_max3_u32 v100, v100, v93, v94\n"
    "v_smfmac_f32_16x16x64_bf16 v[24:27], v[88:91], v[224:231], v1\n"
    "v_max3_u32 v100, v100, v95, v96\n"
    "v_smfmac_f32_16x16x64_bf16 v[28:31], v[88:91], v[232:239], v1\n"
    "v_cmp_eq_u32 vcc, -1, v100\n"
    "v_smfmac_f32_16x16x64_bf16 v[24:27], v[92:95], v[240:247], v1\n"
    "s_cbranch_vccnz .Lgf_%=\n"
    ".Lgx_%=:\n"
    "s_waitcnt lgkmcnt(0)\n"
    "s_barrier\n"
    "s_memrealtime s[52:53]\n"
    "ds_read_b128 v[96:99], v132 offset:20480\n"
    "v_smfmac_f32_16x16x64_bf16 v[28:31], v[92:95], v[248:255], v1\n"
    "ds_read_b128 v[100:103], v132 offset:21504\n"
    "v_smfmac_f32_16x16x64_bf16 v[124:127], v[88:91], a[112:119], v1\n"
    "ds_read_b128 v[104:107], v133 offset:20480\n"
    "v_smfmac_f32_16x16x64_bf16 v[128:131], v[88:91], a[120:127], v1\n"
    "ds_read_b128 v[108:111], v133 offset:21504\n"
    "v_smfmac_f32_16x16x64_bf16 v[124:127], v[92:95], a[128:135], v1\n"
    "ds_read_b128 v[112:115], v134 offset:20480\n"
    "v_smfmac_f32_16x16x64_bf16 v[128:131], v[92:95], a[136:143], v1\n"
    "ds_read_b128 v[116:119], v134 offset:21504\n"
    "s_waitcnt lgkmcnt(5)\n"
    "v_smfmac_f32_16x16x64_bf16 v[16:19], v[96:99], v[64:71], v1\n"
    "ds_read_b128 v[120:123], v0 offset:28672\n"
    "v_smfmac_f32_16x16x64_bf16 v[20:23], v[96:99], v[72:79], v1\n"
    "s_waitcnt lgkmcnt(5)\n"
    "v_smfmac_f32_16x16x64_bf16 v[16:19], v[100:103], v[80:87], v1\n"
    "v_cmp_eq_u32 vcc, s44, v5\n"
    "v_smfmac_f32_16x16x64_bf16 v[20:23], v[100:103], v[136:143], v1\n"
    "v_mov_b32 v79, s45\n"
    "s_waitcnt lgkmcnt(4)\n"
    "v_smfmac_f32_16x16x64_bf16 v[16:19], v[104:107], v[144:151], v1\n"
    "v_cndmask_b32 v79, v79, v0, vcc\n"
    "v_smfmac_f32_16x16x64_bf16 v[20:23], v[104:107], v[152:159], v1\n"
    "s_lshr_b32 s73, s96, 3\n"
    "s_waitcnt lgkmcnt(3)\n"
    "v_smfmac_f32_16x16x64_bf16 v[16:19], v[108:111], v[160:167], v1\n"
    "s_waitcnt lgkmcnt(0)\n"
    "s_add_u32 s73, s73, s52\n"
    "v_and_b32 v78, -2, s73\n"
    "v_smfmac_f32_16x16x64_bf16 v[20:23], v[108:111], v[168:175], v1\n"
    "buffer_store_dword v78, v79, s[40:43], s58 offen offset:3072 sc1\n"
    "s_waitcnt vmcnt(2)\n"
    "v_smfmac_f32_16x16x64_bf16 v[16:19], v[112:115], v[176:183], v1\n"
    "ds_read_b128 v[40:43], v0\n"
    "v_smfmac_f32_16x16x64_bf16 v[20:23], v[112:115], v[184:191], v1\n"
    "ds_read_b128 v[44:47], v0 offset:4096\n"
    "v_and_b32 v12, v12, v13\n"
    "v_smfmac_f32_16x16x64_bf16 v[16:19], v[116:119], v[192:199], v1\n"
    "ds_read_b128 v[48:51], v0 offset:1024\n"
    "v_add_u32 v13, -1, v12\n"
    "v_smfmac_f32_16x16x64_bf16 v[20:23], v[116:119], v[200:207], v1\n"
    "ds_read_b128 v[52:55], v0 offset:5120\n"
    "v_and_or_b32 v15, v13, s45, v5\n"
    "v_smfmac_f32_16x16x64_bf16 v[16:19], v[120:123], v[208:215], v1\n"
    "ds_read_b128 v[56:59], v0 offset:2048\n"
    "v_readfirstlane_b32 s59, v12\n"
    "v_smfmac_f32_16x16x64_bf16 v[20:23], v[120:123], v[216:223], v1\n"
    "ds_read_b128 v[60:63], v0 offset:6144\n"
    "v_smfmac_f32_16x16x64_bf16 v[24:27], v[96:99], a[0:7], v1\n"
    "ds_read_b128 v[64:67], v0 offset:3072\n"
    "v_add_f32 v16, v16, v18\n"
    "v_smfmac_f32_16x16x64_bf16 v[28:31], v[96:99], a[8:15], v1\n"
    "ds_read_b128 v[68:71], v0 offset:7168\n"
    "v_add_f32 v17, v17, v19\n"
    "v_smfmac_f32_16x16x64_bf16 v[24:27], v[100:103], a[16:23], v1\n"
    "ds_read_b128 v[88:91], v2 offset:14336\n"
    "v_add_f32 v20, v20, v22\n"
    "v_smfmac_f32_16x16x64_bf16 v[28:31], v[100:103], a[24:31], v1\n"
    "ds_read_b128 v[92:95], v2 offset:15360\n"
    "v_add_f32 v21, v21, v23\n"
    "v_smfmac_f32_16x16x64_bf16 v[24:27], v[104:107], a[32:39], v1\n"
    "v_cvt_pk_bf16_f32 v76, v16, v20\n"
    "v_smfmac_f32_16x16x64_bf16 v[28:31], v[104:107], a[40:47], v1\n"
    "v_cvt_pk_bf16_f32 v77, v17, v21\n"
    "v_smfmac_f32_16x16x64_bf16 v[24:27], v[108:111], a[48:55], v1\n"
    "ds_write_b32 v3, v76 offset:9216\n"
    "v_smfmac_f32_16x16x64_bf16 v[28:31], v[108:111], a[56:63], v1\n"
    "ds_write_b32 v3, v77 offset:9488\n"
    "v_smfmac_f32_16x16x64_bf16 v[24:27], v[112:115], a[64:71], v1\n"
    "buffer_load_dwordx4 v[16:19], v15, s[40:43], s61 offen sc1\n"
    "v_smfmac_f32_16x16x64_bf16 v[28:31], v[112:115], a[72:79], v1\n"
    "v_smfmac_f32_16x16x64_bf16 v[24:27], v[116:119], a[80:87], v1\n"
    "v_smfmac_f32_16x16x64_bf16 v[28:31], v[116:119], a[88:95], v1\n"
    "s_waitcnt lgkmcnt(0)\n"
    "s_barrier\n"
    "ds_read_b128 v[32:35], v4 offset:29696\n"
    "v_smfmac_f32_16x16x64_bf16 v[24:27], v[120:123], a[96:103], v1\n"
    "ds_read_b128 v[36:39], v4 offset:29760\n"
    "v_smfmac_f32_16x16x64_bf16 v[28:31], v[120:123], a[104:111], v1\n"
    "ds_read_b128 v[132:135], v4 offset:29824\n"
    "v_smfmac_f32_16x16x64_bf16 v[124:127], v[96:99], a[144:151], v1\n"
    "ds_read_b128 v[72:75], v4 offset:29888\n"
    "v_add_f32 v24, v24, v26\n"
    "v_add_f32 v25, v25, v27\n"
    "v_smfmac_f32_16x16x64_bf16 v[128:131], v[96:99], a[152:159], v1\n"
    "buffer_load_dwordx4 v[20:23], v15, s[40:43], s62 offen sc1\n"
    "v_add_f32 v28, v28, v30\n"
    "v_add_f32 v29, v29, v31\n"
    "v_smfmac_f32_16x16x64_bf16 v[124:127], v[100:103], a[160:167], v1\n"
    "v_cvt_pk_bf16_f32 v76, v24, v28\n"
    "v_cvt_pk_bf16_f32 v77, v25, v29\n"
    "s_waitcnt lgkmcnt(3)\n"
    "v_mfma_f32_16x16x32_bf16 v[80:83], v[40:43], v[32:35], 0\n"
    "ds_write2_b32 v3, v76, v77 offset1:68\n"
    "v_mfma_f32_16x16x32_bf16 v[84:87], v[44:47], v[32:35], 0\n"
    "s_add_u32 s73, s48, 1\n"
    "s_and_b32 s51, s73, 32\n"
    "s_and_b32 s73, s73, 31\n"
    "s_waitcnt lgkmcnt(3)\n"
    "v_mfma_f32_16x16x32_bf16 v[80:83], v[48:51], v[36:39], v[80:83]\n"
    "buffer_load_dwordx4 v[24:27], v15, s[40:43], s63 offen sc1\n"
    "v_mfma_f32_16x16x32_bf16 v[84:87], v[52:55], v[36:39], v[84:87]\n"
    "s_lshl3_add_u32 s73, s51, s73\n"
    "s_lshl2_add_u32 s73, s73, s92\n"
    "s_waitcnt lgkmcnt(2)\n"
    "v_mfma_f32_16x16x32_bf16 v[80:83], v[56:59], v[132:135], v[80:83]\n"
    "s_mov_b32 vcc_lo, 0xcccccccc\n"
    "s_mov_b32 vcc_hi, 0xcccccccc\n"
    "v_mfma_f32_16x16x32_bf16 v[84:87], v[60:63], v[132:135], v[84:87]\n"
    "s_waitcnt lgkmcnt(0)\n"
    "s_barrier\n"
    "ds_read_b128 v[40:43], v4 offset:20480\n"
    "v_mfma_f32_16x16x32_bf16 v[80:83], v[64:67], v[72:75], v[80:83]\n"
    "ds_read_b128 v[44:47], v4 offset:20544\n"
    "v_mfma_f32_16x16x32_bf16 v[84:87], v[68:71], v[72:75], v[84:87]\n"
    "ds_read_b128 v[48:51], v4 offset:20608\n"
    "v_smfmac_f32_16x16x64_bf16 v[128:131], v[100:103], a[168:175], v1\n"
    "ds_read_b128 v[52:55], v4 offset:20672\n"
    "v_smfmac_f32_16x16x64_bf16 v[124:127], v[104:107], a[176:183], v1\n"
    "ds_read_b128 v[56:59], v5 offset:8192\n"
    "v_cndmask_b32 v80, v80, v84, vcc\n"
    "v_smfmac_f32_16x16x64_bf16 v[128:131], v[104:107], a[184:191], v1\n"
    "ds_read_b128 v[60:63], v5 offset:12288\n"
    "v_cndmask_b32 v81, v81, v85, vcc\n"
    "s_waitcnt lgkmcnt(5)\n"
    "v_mfma_f32_16x16x32_bf16 v[64:67], v[40:43], v[32:35], 0\n"
    "v_cndmask_b32 v82, v82, v86, vcc\n"
    "v_cndmask_b32 v83, v83, v87, vcc\n"
    "s_waitcnt lgkmcnt(4)\n"
    "v_mfma_f32_16x16x32_bf16 v[64:67], v[44:47], v[36:39], v[64:67]\n"
    "ds_read_b128 v[96:99], v2 offset:16384\n"
    "v_maximum3_f32 v78, v80, v81, v82\n"
    "v_maximum3_f32 v78, v78, v83, v83\n"
    "s_waitcnt lgkmcnt(4)\n"
    "v_mfma_f32_16x16x32_bf16 v[64:67], v[48:51], v[132:135], v[64:67]\n"
    "ds_read_b128 v[100:103], v2 offset:17408\n"
    "v_max_f32_dpp v78, v78, v78 row_mirror row_mask:0xf bank_mask:0xf\n"
    "s_waitcnt lgkmcnt(4)\n"
    "v_mfma_f32_16x16x32_bf16 v[64:67], v[52:55], v[72:75], v[64:67]\n"
    "buffer_load_dwordx4 v[28:31], v15, s[40:43], s64 offen sc1\n"
    "v_mov_b32 v77, v78\n"
    "v_smfmac_f32_16x16x64_bf16 v[124:127], v[108:111], a[192:199], v1\n"
    "ds_read_b128 v[40:43], v2 offset:18432\n"
    "v_permlane32_swap_b32 v77, v78\n"
    "v_smfmac_f32_16x16x64_bf16 v[128:131], v[108:111], a[200:207], v1\n"
    "ds_read_b128 v[44:47], v2 offset:19456\n"
    "v_maximum3_f32 v78, v78, v77, v64\n"
    "v_maximum3_f32 v78, v78, v65, v66\n"
    "v_max_f32 v77, v78, v67\n"
    "v_max_f32 v78, v78, v67\n"
    "v_smfmac_f32_16x16x64_bf16 v[124:127], v[112:115], a[208:215], v1\n"
    "ds_read_b128 v[48:51], v2 offset:22528\n"
    "v_permlane16_swap_b32 v77, v78\n"
    "v_max_f32 v78, v78, v77\n"
    "v_mul_f32 v78, 0xbe0293ee, v78\n"
    "v_smfmac_f32_16x16x64_bf16 v[128:131], v[112:115], a[216:223], v1\n"
    "ds_read_b128 v[52:55], v2 offset:23552\n"
    "v_fmamk_f32 v80, v80, 0x3e0293ee, v78\n"
    "v_fmamk_f32 v81, v81, 0x3e0293ee, v78\n"
    "v_exp_f32 v80, v80\n"
    "v_exp_f32 v81, v81\n"
    "v_smfmac_f32_16x16x64_bf16 v[124:127], v[116:119], a[224:231], v1\n"
    "ds_read_b128 v[32:35], v2 offset:24576\n"
    "v_fmamk_f32 v82, v82, 0x3e0293ee, v78\n"
    "v_fmamk_f32 v83, v83, 0x3e0293ee, v78\n"
    "v_exp_f32 v82, v82\n"
    "v_exp_f32 v83, v83\n"
    "v_cvt_pk_bf16_f32 v80, v80, v81\n"
    "v_smfmac_f32_16x16x64_bf16 v[128:131], v[116:119], a[232:239], v1\n"
    "ds_read_b128 v[36:39], v2 offset:25600\n"
    "v_fmamk_f32 v64, v64, 0x3e0293ee, v78\n"
    "v_fmamk_f32 v65, v65, 0x3e0293ee, v78\n"
    "v_exp_f32 v64, v64\n"
    "v_exp_f32 v65, v65\n"
    "v_cvt_pk_bf16_f32 v81, v82, v83\n"
    "v_smfmac_f32_16x16x64_bf16 v[124:127], v[120:123], a[240:247], v1\n"
    "ds_read_b128 v[104:107], v2 offset:26624\n"
    "v_cvt_pk_bf16_f32 v66, v64, v65\n"
    "v_mov_b32_dpp v82, v80 row_mirror row_mask:0xf bank_mask:0xf\n"
    "v_smfmac_f32_16x16x64_bf16 v[128:131], v[120:123], a[248:255], v1\n"
    "ds_read_b128 v[108:111], v2 offset:27648\n"
    "v_mov_b32_dpp v83, v81 row_mirror row_mask:0xf bank_mask:0xf\n"
    "v_cvt_pk_bf16_f32 v67, v64, v65\n"
    "s_waitcnt lgkmcnt(11)\n"
    "v_mfma_f32_16x16x32_bf16 v[132:135], v[56:59], v[80:83], 0\n"
    "ds_read_b128 v[112:115], v2 offset:20480\n"
    "v_cvt_pk_bf16_f32 v124, v124, v125\n"
    "v_cvt_pk_bf16_f32 v125, v126, v127\n"
    "s_waitcnt lgkmcnt(11)\n"
    "v_mfma_f32_16x16x32_bf16 v[120:123], v[60:63], v[80:83], 0\n"
    "ds_read_b128 v[116:119], v2 offset:21504\n"
    "v_cvt_pk_bf16_f32 v126, v128, v129\n"
    "v_cvt_pk_bf16_f32 v127, v130, v131\n"
    "v_mfma_f32_16x16x16_bf16 v[132:135], v[124:125], v[66:67], v[132:135]\n"
    "buffer_load_dwordx4 v[12:15], v15, s[40:43], s60 offen sc1\n"
    "ds_read_b128 v[68:71], v2 offset:28672\n"
    "s_nop 0\n"
    "v_mfma_f32_16x16x16_bf16 v[120:123], v[126:127], v[66:67], v[120:123]\n"
    "ds_read_b128 v[72:75], v2 offset:29696\n"
    "buffer_load_dword v79, v0, s[40:43], s58 offen offset:3072 sc1\n"
    "v_mov_b64 v[56:57], s[46:47]\n"
    "v_mov_b64 v[58:59], s[46:47]\n"
    "v_xor_b32 v76, 0x200, v6\n"
    "v_cvt_pk_bf16_f32 v84, v132, v133\n"
    "v_cvt_pk_bf16_f32 v85, v134, v135\n"
    "v_xor_b32 v77, 0x400, v6\n"
    "s_not_b64 exec, vcc\n"
    "v_cvt_pk_bf16_f32 v86, v120, v121\n"
    "v_cvt_pk_bf16_f32 v87, v122, v123\n"
    "ds_write_b128 v6, v[84:87] offset:26880\n"
    "s_mov_b64 exec, -1\n"
    "s_cmp_eq_u32 s48, 63\n"
    "v_mfma_f32_16x16x32_bf16 v[128:131], v[80:83], v[56:59], 0\n"
    "v_mov_b32 v67, 0\n"
    "v_mov_b64 v[132:133], 0\n"
    "v_mov_b64 v[134:135], 0\n"
    "v_mov_b32_dpp v84, v84 quad_perm:[0,1,0,1] row_mask:0xf bank_mask:0xf\n"
    "v_mov_b32_dpp v85, v85 quad_perm:[0,1,0,1] row_mask:0xf bank_mask:0xf\n"
    "v_mov_b32_dpp v86, v86 quad_perm:[0,1,0,1] row_mask:0xf bank_mask:0xf\n"
    "v_mov_b32_dpp v87, v87 quad_perm:[0,1,0,1] row_mask:0xf bank_mask:0xf\n"
    "v_mfma_f32_16x16x16_bf16 v[128:131], v[66:67], v[56:57], v[128:131]\n"
    "v_mov_b64 v[80:81], 0\n"
    "v_mov_b64 v[82:83], 0\n"
    "v_smfmac_f32_16x16x64_bf16 v[132:135], v[84:87], v[88:95], v1\n"
    "v_xor_b32 v78, 0x600, v6\n"
    "s_waitcnt lgkmcnt(13)\n"
    "v_smfmac_f32_16x16x64_bf16 v[80:83], v[84:87], v[96:103], v1\n"
    "s_waitcnt lgkmcnt(0)\n"
    "s_barrier\n"
    "s_cbranch_scc1 .Lend_%=\n"
    "ds_read_b128 v[56:59], v76 offset:26880\n"
    "ds_read_b128 v[60:63], v77 offset:26880\n"
    "ds_read_b128 v[120:123], v78 offset:26880\n"
    "ds_read_b128 v[88:91], v2 offset:30720\n"
    "v_rcp_f32 v128, v128\n"
    "v_rcp_f32 v129, v129\n"
    "s_waitcnt lgkmcnt(3)\n"
    "v_smfmac_f32_16x16x64_bf16 v[132:135], v[56:59], v[40:47], v1\n"
    "ds_read_b128 v[40:43], v0 offset:26880\n"
    "v_mov_b32 v78, s73\n"
    "s_waitcnt lgkmcnt(3)\n"
    "v_smfmac_f32_16x16x64_bf16 v[132:135], v[60:63], v[48:55], v1\n"
    "ds_read_b128 v[92:95], v2 offset:31744\n"
    "s_waitcnt lgkmcnt(3)\n"
    "v_smfmac_f32_16x16x64_bf16 v[132:135], v[120:123], v[104:111], v1\n"
    "ds_read_b128 v[96:99], v2 offset:32768\n"
    "v_mov_b64 v[64:65], 0\n"
    "v_smfmac_f32_16x16x64_bf16 v[80:83], v[56:59], v[112:119], v1\n"
    "ds_read_b128 v[100:103], v2 offset:33792\n"
    "v_mov_b64 v[66:67], 0\n"
    "v_smfmac_f32_16x16x64_bf16 v[80:83], v[60:63], v[32:39], v1\n"
    "ds_read_b128 v[44:47], v0 offset:27904\n"
    "v_mul_f32 v132, v132, v128\n"
    "v_mul_f32 v133, v133, v129\n"
    "v_smfmac_f32_16x16x64_bf16 v[80:83], v[120:123], v[68:75], v1\n"
    "global_load_dword v76, v78, %1 sc1\n"
    "v_mul_f32 v134, v134, v128\n"
    "v_mul_f32 v135, v135, v129\n"
    "s_waitcnt lgkmcnt(3)\n"
    "v_smfmac_f32_16x16x64_bf16 v[64:67], v[40:43], v[88:95], v1\n"
    "global_load_dword v77, v78, %1 offset:128 sc1\n"
    "v_cvt_pk_bf16_f32 v132, v132, v133\n"
    "v_cvt_pk_bf16_f32 v133, v134, v135\n"
    "v_bfi_b32 v132, 1, s48, v132\n"
    "s_waitcnt lgkmcnt(0)\n"
    "v_smfmac_f32_16x16x64_bf16 v[64:67], v[44:47], v[96:103], v1\n"
    "v_pk_mul_f32 v[80:81], v[80:81], v[128:129]\n"
    "v_pk_mul_f32 v[82:83], v[82:83], v[128:129]\n"
    "v_cvt_pk_bf16_f32 v134, v80, v81\n"
    "v_cvt_pk_bf16_f32 v135, v82, v83\n"
    "v_bfi_b32 v134, 1, s48, v134\n"
    "buffer_store_dwordx4 v[132:135], v7, s[40:43], 0 offen sc1\n"
    "s_nop 2\n"
    "v_pk_add_f32 v[64:65], v[64:65], v[66:67]\n"
    "v_pk_mul_f32 v[64:65], v[64:65], v[128:129]\n"
    "v_cvt_pk_bf16_f32 v66, v64, v65\n"
    "v_bfi_b32 v64, 1, s48, v66\n"
    "s_nop 0\n"
    "v_mov_b32_dpp v65, v66 row_ror:8 row_mask:0xf bank_mask:0xf\n"
    "buffer_store_dwordx2 v[64:65], v8, s[40:43], 0 offen sc1\n"
    "s_memrealtime s[38:39]\n"
    "ds_write_b128 v5, v[124:127] offset:22784\n"
    "s_cmp_ge_u32 %2, 2\n"
    "s_cbranch_scc1 .Lw3_%=\n"
    "v_and_b32 v104, 0x70, v0\n"
    "ds_read_b128 v[112:115], v104 offset:31936\n"
    "s_movk_i32 s73, 0x60\n"
    "s_movk_i32 s51, 0x80\n"
    "v_lshrrev_b32 v104, 1, v0\n"
    "v_bfi_b32 v104, s73, v104, v0\n"
    "v_lshlrev_b32 v105, 2, v0\n"
    "v_bfi_b32 v104, s51, v105, v104\n"
    "s_waitcnt lgkmcnt(0)\n"
    "s_barrier\n"
    "s_waitcnt vmcnt(4)\n"
    "s_mov_b32 s56, 0\n"
    ".Lr4_a%=:\n"
    "v_subrev_u32 v78, s52, v79\n"
    "v_cmp_eq_u32 vcc, -1, v79\n"
    "s_cbranch_vccnz .Lr4_%=\n"
    ".Lr4_x%=:\n"
    "v_max_i32_dpp v78, v78, v78 row_ror:8 row_mask:0xf bank_mask:0xf\n"
    "s_nop 1\n"
    "v_max_i32_dpp v78, v78, v78 row_ror:4 row_mask:0xf bank_mask:0xf\n"
    "s_nop 1\n"
    "v_max_i32_dpp v78, v78, v78 row_ror:2 row_mask:0xf bank_mask:0xf\n"
    "s_nop 1\n"
    "v_max_i32_dpp v78, v78, v78 row_ror:1 row_mask:0xf bank_mask:0xf\n"
    "s_nop 0\n"
    "v_readlane_b32 s95, v78, 0\n"
    "v_readlane_b32 s73, v78, 16\n"
    "v_readlane_b32 s51, v78, 32\n"
    "v_readlane_b32 s53, v78, 48\n"
    "s_max_i32 s95, s95, s73\n"
    "s_max_i32 s51, s51, s53\n"
    "s_max_i32 s95, s95, s51\n"
    "s_min_i32 s95, s95, 1024\n"
    "s_add_u32 s95, s95, s52\n"
    "s_add_u32 s95, s95, s55\n"
    "s_cmp_lt_i32 s71, 0\n"
    "s_cbranch_scc1 .Lnored_%=\n"
    "s_mov_b32 s56, 0\n"
    "s_sub_u32 s53, s95, s54\n"
    "s_waitcnt lgkmcnt(0)\n"
    ".Lgl5_%=:\n"
    "s_memrealtime s[50:51]\n"
    "s_waitcnt lgkmcnt(0)\n"
    "s_sub_u32 s50, s50, s53\n"
    "s_cmp_lt_i32 s50, 0\n"
    "s_cbranch_scc1 .Lgl5_%=\n"
    "buffer_load_dwordx4 v[88:91], v104, s[40:43], s65 offen sc1\n"
    "buffer_load_dwordx4 v[92:95], v104, s[40:43], s65 offen offset:1024 sc1\n"
    "buffer_load_dwordx4 v[96:99], v104, s[40:43], s65 offen offset:2048 sc1\n"
    "buffer_load_dwordx4 v[100:103], v104, s[40:43], s65 offen offset:3072 sc1\n"
    "s_waitcnt vmcnt(6)\n"
    "v_and_b32 v77, v76, v77\n"
    "s_nop 0\n"
    "v_readfirstlane_b32 s70, v77\n"
    "ds_read_b128 v[32:35], v2\n"
    "ds_read_b128 v[36:39], v2 offset:1024\n"
    "ds_read_b128 v[40:43], v2 offset:2048\n"
    "ds_read_b128 v[44:47], v2 offset:3072\n"
    "ds_read_b128 v[48:51], v2 offset:4096\n"
    "ds_read_b128 v[52:55], v2 offset:5120\n"
    "ds_read_b128 v[56:59], v2 offset:6144\n"
    "ds_read_b128 v[60:63], v2 offset:7168\n"
    "ds_read_b128 v[64:67], v2 offset:8192\n"
    "ds_read_b128 v[68:71], v2 offset:9216\n"
    "ds_read_b128 v[72:75], v2 offset:10240\n"
    "ds_read_b128 v[76:79], v2 offset:11264\n"
    "ds_read_b128 v[80:83], v2 offset:12288\n"
    "ds_read_b128 v[84:87], v2 offset:13312\n"
    "v_mov_b64 v[124:125], 0\n"
    "v_mov_b64 v[126:127], 0\n"
    "v_mov_b64 v[128:129], 0\n"
    "v_mov_b64 v[130:131], 0\n"
    "s_mul_i32 s73, s48, 3\n"
    "s_add_u32 s73, s73, s94\n"
    "s_and_b32 s51, s73, 1\n"
    "s_lshl_b32 s51, s51, 12\n"
    "s_lshr_b32 s73, s73, 1\n"
    "s_lshl_b32 s73, s73, 14\n"
    "s_add_u32 s73, s73, s51\n"
    "s_add_u32 s66, s73, s68\n"
    "s_lshr_b32 s67, %2, 1\n"
    "s_mul_i32 s73, s48, 3\n"
    "s_add_u32 s73, s73, s67\n"
    "s_and_b32 s51, s73, 1\n"
    "s_lshl_b32 s51, s51, 12\n"
    "s_lshr_b32 s73, s73, 1\n"
    "s_lshl_b32 s73, s73, 14\n"
    "s_add_u32 s73, s73, s51\n"
    "s_add_u32 s67, s73, s68\n"
    "s_and_b32 s73, %2, 1\n"
    "s_lshl_b32 s73, s73, 11\n"
    "s_add_u32 s67, s67, s73\n"
    "s_mov_b32 s72, s58\n"
    "s_sub_u32 s73, s38, s52\n"
    "s_lshl_b32 s73, s73, 3\n"
    "s_sub_u32 s73, s73, s96\n"
    "s_ashr_i32 s73, s73, 2\n"
    "s_max_i32 s73, s73, -16\n"
    "s_min_i32 s73, s73, 16\n"
    "s_add_u32 s96, s96, s73\n"
    "s_waitcnt lgkmcnt(0)\n"
    ".Lred_%=:\n"
    "s_waitcnt vmcnt(3)\n"
    "v_mfma_f32_16x16x32_bf16 v[108:111], v[88:91], v[112:115], 0\n"
    "v_bitop3_b32 v116, v88, v90, s48 bitop3:0x7e\n"
    "s_waitcnt vmcnt(2)\n"
    "v_mfma_f32_16x16x32_bf16 v[108:111], v[92:95], v[112:115], v[108:111]\n"
    "v_bitop3_b32 v117, v92, v94, s48 bitop3:0x7e\n"
    "s_waitcnt vmcnt(1)\n"
    "v_mfma_f32_16x16x32_bf16 v[108:111], v[96:99], v[112:115], v[108:111]\n"
    "v_bitop3_b32 v118, v96, v98, s48 bitop3:0x7e\n"
    "v_or3_b32 v116, v116, v117, v118\n"
    "s_waitcnt vmcnt(0)\n"
    "v_mfma_f32_16x16x32_bf16 v[108:111], v[100:103], v[112:115], v[108:111]\n"
    "v_bitop3_b32 v119, v100, v102, s48 bitop3:0x7e\n"
    "v_or_b32 v116, v116, v119\n"
    "v_and_b32 v116, 1, v116\n"
    "v_cmp_ne_u32 vcc, 0, v116\n"
    "s_cbranch_vccnz .Lrf_%=\n"
    ".Lrx_%=:\n"
    "s_nop 3\n"
    "v_pk_add_f32 v[108:109], v[108:109], v[110:111]\n"
    "v_cvt_pk_bf16_f32 v108, v108, v109\n"
    "v_min_u32 v108, -2, v108\n"
    "buffer_store_dword v108, v9, s[40:43], s66 offen sc1\n"
    ".Lpub_%=:\n"
    "s_cmp_eq_u32 s59, 0\n"
    "s_cbranch_scc1 .Lr6_%=\n"
    ".Lr6_x%=:\n"
    "s_add_u32 s73, s48, 2\n"
    "s_and_b32 s73, s73, 63\n"
    "s_lshr_b32 s51, s73, 2\n"
    "s_xor_b32 s51, s51, s69\n"
    "s_and_b32 s51, s51, 1\n"
    "s_lshl_b32 s51, s51, 13\n"
    "s_lshl_b32 s73, s73, 5\n"
    "s_add_u32 s73, s73, %4\n"
    "s_lshl_b32 s73, s73, 14\n"
    "s_add_u32 s73, s73, s51\n"
    "s_add_u32 s60, s73, 0x6000000\n"
    "s_add_u32 s61, s73, 0x6001000\n"
    "s_add_u32 s62, s73, 0x8000000\n"
    "s_add_u32 s63, s73, 0x8001000\n"
    "s_lshl_b32 s51, %5, 12\n"
    "s_add_u32 s73, s73, s51\n"
    "s_add_u32 s64, s73, 0x4000000\n"
    "s_mul_i32 s73, s48, 3\n"
    "s_add_u32 s73, s73, 5\n"
    "s_and_b32 s51, s73, 1\n"
    "s_lshl_b32 s51, s51, 12\n"
    "s_lshr_b32 s73, s73, 1\n"
    "s_lshl_b32 s73, s73, 14\n"
    "s_add_u32 s73, s73, s51\n"
    "s_add_u32 s58, s73, s68\n"
    ".Lg_%=:\n"
    "v_xor_b32 v132, 0x800, v10\n"
    "v_xor_b32 v133, 0x1000, v10\n"
    "v_xor_b32 v134, 0x1800, v10\n"
    "v_lshrrev_b32 v135, 2, v5\n"
    "s_movk_i32 s73, 0x180\n"
    "v_lshlrev_b32 v102, 1, v0\n"
    "v_bfi_b32 v102, s73, v102, v0\n"
    "v_lshrrev_b32 v101, 2, v0\n"
    "v_bfi_b32 v102, 64, v101, v102\n"
    "v_lshlrev_b32 v103, 1, v135\n"
    "v_bfi_b32 v103, s73, v103, v135\n"
    "v_lshrrev_b32 v101, 2, v135\n"
    "v_bfi_b32 v103, 64, v101, v103\n"
    "s_mov_b32 s56, 0\n"
    "s_sub_u32 s53, s95, 0\n"
    "s_waitcnt lgkmcnt(0)\n"
    ".Lgl7_%=:\n"
    "s_memrealtime s[50:51]\n"
    "s_waitcnt lgkmcnt(0)\n"
    "s_sub_u32 s50, s50, s53\n"
    "s_cmp_lt_i32 s50, 0\n"
    "s_cbranch_scc1 .Lgl7_%=\n"
    "buffer_load_dwordx4 v[88:91], v102, s[40:43], s67 offen sc1\n"
    "buffer_load_dwordx4 v[92:95], v102, s[40:43], s67 offen offset:1024 sc1\n"
    "buffer_load_dword v96, v103, s[40:43], s72 offen sc1\n"
    "s_add_u32 s73, s48, 2\n"
    "s_and_b32 s51, s73, 32\n"
    "s_and_b32 s73, s73, 31\n"
    "s_lshl3_add_u32 s73, s51, s73\n"
    "s_lshl2_add_u32 s73, s73, s92\n"
    "s_waitcnt lgkmcnt(0)\n"
    "s_barrier\n"
    "ds_write_b128 v5, v[12:15]\n"
    "ds_write_b128 v5, v[16:19] offset:4096\n"
    "ds_write_b128 v5, v[28:31] offset:16384\n"
    "v_swap_b32 v22, v24\n"
    "v_swap_b32 v23, v25\n"
    "ds_write_b128 v5, v[20:23] offset:8192\n"
    "ds_write_b128 v5, v[24:27] offset:12288\n"
    "v_mov_b32 v14, s73\n"
    "global_load_dword v12, v14, %1 sc1\n"
    "global_load_dword v13, v14, %1 offset:128 sc1\n"
    "v_mov_b64 v[16:17], 0\n"
    "v_mov_b64 v[18:19], 0\n"
    "v_mov_b64 v[20:21], 0\n"
    "v_mov_b64 v[22:23], 0\n"
    "v_mov_b64 v[24:25], 0\n"
    "v_mov_b64 v[26:27], 0\n"
    "v_mov_b64 v[28:29], 0\n"
    "v_mov_b64 v[30:31], 0\n"
    "s_cmp_ge_u32 %2, 2\n"
    "s_cbranch_scc1 .Lcob_%=\n"
    ".Lcod_%=:\n"
    "global_load_dword v14, v14, %1 sc1\n"
    ".Lcox_%=:\n"
    "s_add_u32 s48, s48, 1\n"
    "s_branch .Lstep_%=\n"
    ".Lcob_%=:\n"
    "s_add_u32 s73, s48, 0\n"
    "s_and_b32 s51, s73, 32\n"
    "s_and_b32 s73, s73, 31\n"
    "s_lshl3_add_u32 s73, s51, s73\n"
    "s_lshl2_add_u32 s73, s73, s93\n"
    "v_mov_b32 v100, s73\n"
    "v_mov_b32 v101, 1\n"
    "s_waitcnt vmcnt(5)\n"
    "s_mov_b64 exec, 1\n"
    "ds_add_rtn_u32 v101, v11, v101 offset:31872\n"
    "s_mov_b64 exec, -1\n"
    "s_waitcnt lgkmcnt(0)\n"
    "v_readfirstlane_b32 s73, v101\n"
    "v_mov_b32 v101, 1\n"
    "s_bitcmp1_b32 s73, 0\n"
    "s_cbranch_scc0 .Lcod_%=\n"
    "s_mov_b64 exec, 1\n"
    "global_store_dword v100, v101, %1 sc1\n"
    "s_mov_b64 exec, -1\n"
    "s_branch .Lcox_%=\n"
    ".Lgf_%=:\n"
    "s_bitset1_b32 s67, 22\n"
    "s_bitset1_b32 s72, 22\n"
    "s_waitcnt lgkmcnt(0)\n"
    "buffer_load_dwordx4 v[88:91], v102, s[40:43], s67 offen sc1\n"
    "buffer_load_dwordx4 v[92:95], v102, s[40:43], s67 offen offset:1024 sc1\n"
    "buffer_load_dword v96, v103, s[40:43], s72 offen sc1\n"
    "s_waitcnt vmcnt(0)\n"
    "v_mov_b64 v[16:17], 0\n"
    "v_mov_b64 v[18:19], 0\n"
    "v_mov_b64 v[20:21], 0\n"
    "v_mov_b64 v[22:23], 0\n"
    "v_mov_b64 v[24:25], 0\n"
    "v_mov_b64 v[26:27], 0\n"
    "v_mov_b64 v[28:29], 0\n"
    "v_mov_b64 v[30:31], 0\n"
    "v_mov_b64 v[124:125], 0\n"
    "v_mov_b64 v[126:127], 0\n"
    "v_mov_b64 v[128:129], 0\n"
    "v_mov_b64 v[130:131], 0\n"
    "s_add_u32 s56, s56, 1\n"
    "s_cmp_lt_u32 s56, s57\n"
    "s_cbranch_scc1 .Lstep_%=\n"
    "s_mov_b32 s57, 0\n"
    "v_mov_b32 v135, 1\n"
    "global_store_dword v11, v135, %1 sc1\n"
    "s_branch .Lgx_%=\n"
    ".Lrf_%=:\n"
    "s_memrealtime s[50:51]\n"
    "s_waitcnt lgkmcnt(0)\n"
    "s_add_u32 s95, s50, 62\n"
    "s_bitset1_b32 s67, 22\n"
    "s_bitset1_b32 s72, 22\n"
    "buffer_load_dwordx4 v[88:91], v104, s[40:43], s65 offen sc1\n"
    "buffer_load_dwordx4 v[92:95], v104, s[40:43], s65 offen offset:1024 sc1\n"
    "buffer_load_dwordx4 v[96:99], v104, s[40:43], s65 offen offset:2048 sc1\n"
    "buffer_load_dwordx4 v[100:103], v104, s[40:43], s65 offen offset:3072 sc1\n"
    "s_add_u32 s56, s56, 1\n"
    "s_cmp_lt_u32 s56, s57\n"
    "s_cbranch_scc1 .Lred_%=\n"
    "s_mov_b32 s57, 0\n"
    "v_mov_b32 v135, 1\n"
    "global_store_dword v11, v135, %1 sc1\n"
    "s_branch .Lrx_%=\n"
    ".Lnored_%=:\n"
    "s_waitcnt vmcnt(0)\n"
    "v_and_b32 v77, v76, v77\n"
    "s_nop 0\n"
    "v_readfirstlane_b32 s70, v77\n"
    "ds_read_b128 v[32:35], v2\n"
    "ds_read_b128 v[36:39], v2 offset:1024\n"
    "ds_read_b128 v[40:43], v2 offset:2048\n"
    "ds_read_b128 v[44:47], v2 offset:3072\n"
    "ds_read_b128 v[48:51], v2 offset:4096\n"
    "ds_read_b128 v[52:55], v2 offset:5120\n"
    "ds_read_b128 v[56:59], v2 offset:6144\n"
    "ds_read_b128 v[60:63], v2 offset:7168\n"
    "ds_read_b128 v[64:67], v2 offset:8192\n"
    "ds_read_b128 v[68:71], v2 offset:9216\n"
    "ds_read_b128 v[72:75], v2 offset:10240\n"
    "ds_read_b128 v[76:79], v2 offset:11264\n"
    "ds_read_b128 v[80:83], v2 offset:12288\n"
    "ds_read_b128 v[84:87], v2 offset:13312\n"
    "v_mov_b64 v[124:125], 0\n"
    "v_mov_b64 v[126:127], 0\n"
    "v_mov_b64 v[128:129], 0\n"
    "v_mov_b64 v[130:131], 0\n"
    "s_mul_i32 s73, s48, 3\n"
    "s_add_u32 s73, s73, s94\n"
    "s_and_b32 s51, s73, 1\n"
    "s_lshl_b32 s51, s51, 12\n"
    "s_lshr_b32 s73, s73, 1\n"
    "s_lshl_b32 s73, s73, 14\n"
    "s_add_u32 s73, s73, s51\n"
    "s_add_u32 s66, s73, s68\n"
    "s_lshr_b32 s67, %2, 1\n"
    "s_mul_i32 s73, s48, 3\n"
    "s_add_u32 s73, s73, s67\n"
    "s_and_b32 s51, s73, 1\n"
    "s_lshl_b32 s51, s51, 12\n"
    "s_lshr_b32 s73, s73, 1\n"
    "s_lshl_b32 s73, s73, 14\n"
    "s_add_u32 s73, s73, s51\n"
    "s_add_u32 s67, s73, s68\n"
    "s_and_b32 s73, %2, 1\n"
    "s_lshl_b32 s73, s73, 11\n"
    "s_add_u32 s67, s67, s73\n"
    "s_mov_b32 s72, s58\n"
    "s_sub_u32 s73, s38, s52\n"
    "s_lshl_b32 s73, s73, 3\n"
    "s_sub_u32 s73, s73, s96\n"
    "s_ashr_i32 s73, s73, 2\n"
    "s_max_i32 s73, s73, -16\n"
    "s_min_i32 s73, s73, 16\n"
    "s_add_u32 s96, s96, s73\n"
    "s_waitcnt lgkmcnt(0)\n"
    "s_branch .Lpub_%=\n"
    ".Lw3_%=:\n"
    "ds_read_b128 v[32:35], v0 offset:16384\n"
    "ds_read_b128 v[48:51], v0\n"
    "ds_read_b128 v[88:91], v0 offset:4096\n"
    "ds_read_b128 v[104:107], v4 offset:20480\n"
    "ds_read_b128 v[36:39], v0 offset:17408\n"
    "ds_read_b128 v[52:55], v0 offset:1024\n"
    "ds_read_b128 v[92:95], v0 offset:5120\n"
    "ds_read_b128 v[108:111], v4 offset:20544\n"
    "ds_read_b128 v[40:43], v0 offset:18432\n"
    "ds_read_b128 v[56:59], v0 offset:2048\n"
    "ds_read_b128 v[96:99], v0 offset:6144\n"
    "ds_read_b128 v[112:115], v4 offset:20608\n"
    "s_waitcnt lgkmcnt(10)\n"
    "v_mfma_f32_16x16x32_bf16 v[120:123], v[48:51], v[32:35], 0\n"
    "s_barrier\n"
    "ds_read_b128 v[44:47], v0 offset:19456\n"
    "ds_read_b128 v[60:63], v0 offset:3072\n"
    "ds_read_b128 v[100:103], v0 offset:7168\n"
    "ds_read_b128 v[116:119], v4 offset:20672\n"
    "s_waitcnt lgkmcnt(13)\n"
    "v_mfma_f32_16x16x32_bf16 v[124:127], v[88:91], v[32:35], 0\n"
    "s_waitcnt lgkmcnt(12)\n"
    "v_mfma_f32_16x16x32_bf16 v[128:131], v[104:107], v[32:35], 0\n"
    "s_waitcnt lgkmcnt(10)\n"
    "v_mfma_f32_16x16x32_bf16 v[120:123], v[52:55], v[36:39], v[120:123]\n"
    "s_waitcnt lgkmcnt(9)\n"
    "v_mfma_f32_16x16x32_bf16 v[124:127], v[92:95], v[36:39], v[124:127]\n"
    "ds_read_b128 v[64:67], v10 offset:4096\n"
    "s_waitcnt lgkmcnt(9)\n"
    "v_mfma_f32_16x16x32_bf16 v[128:131], v[108:111], v[36:39], v[128:131]\n"
    "ds_read_b128 v[68:71], v10 offset:8192\n"
    "s_waitcnt lgkmcnt(8)\n"
    "v_mfma_f32_16x16x32_bf16 v[120:123], v[56:59], v[40:43], v[120:123]\n"
    "ds_read_b128 v[72:75], v10 offset:5120\n"
    "s_waitcnt lgkmcnt(8)\n"
    "v_mfma_f32_16x16x32_bf16 v[124:127], v[96:99], v[40:43], v[124:127]\n"
    "ds_read_b128 v[80:83], v10 offset:9216\n"
    "s_waitcnt lgkmcnt(8)\n"
    "v_mfma_f32_16x16x32_bf16 v[128:131], v[112:115], v[40:43], v[128:131]\n"
    "ds_read_b128 v[84:87], v11 offset:31888\n"
    "s_waitcnt lgkmcnt(7)\n"
    "v_mfma_f32_16x16x32_bf16 v[120:123], v[60:63], v[44:47], v[120:123]\n"
    "ds_read_b64 v[134:135], v11 offset:31904\n"
    "s_waitcnt lgkmcnt(7)\n"
    "v_mfma_f32_16x16x32_bf16 v[124:127], v[100:103], v[44:47], v[124:127]\n"
    "ds_read_b128 v[88:91], v10 offset:18688\n"
    "s_waitcnt lgkmcnt(7)\n"
    "v_mfma_f32_16x16x32_bf16 v[128:131], v[116:119], v[44:47], v[128:131]\n"
    "ds_read_b128 v[92:95], v10 offset:19712\n"
    "v_mov_b32 v132, 0xd01502f9\n"
    "s_cmp_eq_u32 s59, 0\n"
    "s_cbranch_scc1 .Lr8_%=\n"
    ".Lr8_x%=:\n"
    "ds_read_b128 v[32:35], v2\n"
    "ds_read_b128 v[36:39], v2 offset:1024\n"
    "ds_read_b128 v[40:43], v2 offset:2048\n"
    "ds_read_b128 v[44:47], v2 offset:3072\n"
    "v_cndmask_b32 v120, v132, v120, s[76:77]\n"
    "v_cndmask_b32 v121, v132, v121, s[78:79]\n"
    "v_cndmask_b32 v122, v132, v122, s[80:81]\n"
    "v_cndmask_b32 v123, v132, v123, s[82:83]\n"
    "v_cndmask_b32 v124, v132, v124, s[84:85]\n"
    "v_cndmask_b32 v125, v132, v125, s[86:87]\n"
    "v_cndmask_b32 v126, v132, v126, s[88:89]\n"
    "v_cndmask_b32 v127, v132, v127, s[90:91]\n"
    "v_maximum3_f32 v133, v128, v129, v120\n"
    "v_maximum3_f32 v133, v133, v121, v122\n"
    "v_maximum3_f32 v133, v133, v123, v124\n"
    "v_maximum3_f32 v133, v133, v125, v126\n"
    "v_maximum3_f32 v133, v133, v127, v127\n"
    "v_mov_b32 v132, v133\n"
    "ds_read_b128 v[48:51], v2 offset:4096\n"
    "ds_read_b128 v[52:55], v2 offset:5120\n"
    "v_permlane16_swap_b32 v132, v133\n"
    "v_maximum3_f32 v133, v133, v132, v132\n"
    "v_mov_b32 v132, v133\n"
    "ds_read_b128 v[56:59], v2 offset:6144\n"
    "ds_read_b128 v[60:63], v2 offset:7168\n"
    "v_permlane32_swap_b32 v132, v133\n"
    "v_maximum3_f32 v133, v133, v132, v132\n"
    "v_mul_f32 v133, 0xbe0293ee, v133\n"
    "v_fmamk_f32 v120, v120, 0x3e0293ee, v133\n"
    "v_fmamk_f32 v121, v121, 0x3e0293ee, v133\n"
    "v_fmamk_f32 v122, v122, 0x3e0293ee, v133\n"
    "v_fmamk_f32 v123, v123, 0x3e0293ee, v133\n"
    "v_fmamk_f32 v124, v124, 0x3e0293ee, v133\n"
    "v_fmamk_f32 v125, v125, 0x3e0293ee, v133\n"
    "v_fmamk_f32 v126, v126, 0x3e0293ee, v133\n"
    "v_fmamk_f32 v127, v127, 0x3e0293ee, v133\n"
    "v_fmamk_f32 v128, v128, 0x3e0293ee, v133\n"
    "v_fmamk_f32 v129, v129, 0x3e0293ee, v133\n"
    "v_exp_f32 v120, v120\n"
    "v_exp_f32 v121, v121\n"
    "v_exp_f32 v122, v122\n"
    "v_exp_f32 v123, v123\n"
    "v_exp_f32 v124, v124\n"
    "v_exp_f32 v125, v125\n"
    "v_exp_f32 v126, v126\n"
    "v_exp_f32 v127, v127\n"
    "v_exp_f32 v128, v128\n"
    "v_exp_f32 v129, v129\n"
    "v_cvt_pk_bf16_f32 v120, v120, v121\n"
    "v_cvt_pk_bf16_f32 v121, v122, v123\n"
    "v_cvt_pk_bf16_f32 v122, v124, v125\n"
    "v_cvt_pk_bf16_f32 v123, v126, v127\n"
    "v_cvt_pk_bf16_f32 v124, v128, v129\n"
    "v_cvt_pk_bf16_f32 v125, v128, v129\n"
    "s_waitcnt lgkmcnt(11)\n"
    "v_mfma_f32_16x16x32_bf16 v[100:103], v[84:87], v[120:123], 0\n"
    "v_mfma_f32_16x16x32_bf16 v[104:107], v[64:67], v[120:123], 0\n"
    "v_mfma_f32_16x16x32_bf16 v[108:111], v[68:71], v[120:123], 0\n"
    "s_waitcnt lgkmcnt(10)\n"
    "v_mfma_f32_16x16x16_bf16 v[100:103], v[134:135], v[124:125], v[100:103]\n"
    "v_mfma_f32_16x16x32_bf16 v[112:115], v[72:75], v[120:123], 0\n"
    "v_mfma_f32_16x16x32_bf16 v[116:119], v[80:83], v[120:123], 0\n"
    "s_waitcnt lgkmcnt(9)\n"
    "v_mfma_f32_16x16x16_bf16 v[104:107], v[88:89], v[124:125], v[104:107]\n"
    "ds_read_b128 v[64:67], v2 offset:8192\n"
    "v_mfma_f32_16x16x16_bf16 v[108:111], v[90:91], v[124:125], v[108:111]\n"
    "ds_read_b128 v[68:71], v2 offset:9216\n"
    "v_rcp_f32 v132, v100\n"
    "s_waitcnt lgkmcnt(10)\n"
    "v_mfma_f32_16x16x16_bf16 v[112:115], v[92:93], v[124:125], v[112:115]\n"
    "ds_read_b128 v[80:83], v2 offset:12288\n"
    "v_mfma_f32_16x16x16_bf16 v[116:119], v[94:95], v[124:125], v[116:119]\n"
    "ds_read_b128 v[84:87], v2 offset:13312\n"
    "v_pk_mul_f32 v[104:105], v[104:105], v[132:133] op_sel_hi:[1,0]\n"
    "v_pk_mul_f32 v[106:107], v[106:107], v[132:133] op_sel_hi:[1,0]\n"
    "v_pk_mul_f32 v[108:109], v[108:109], v[132:133] op_sel_hi:[1,0]\n"
    "v_pk_mul_f32 v[110:111], v[110:111], v[132:133] op_sel_hi:[1,0]\n"
    "v_cvt_pk_bf16_f32 v104, v104, v108\n"
    "v_cvt_pk_bf16_f32 v105, v105, v109\n"
    "v_cvt_pk_bf16_f32 v106, v106, v110\n"
    "v_cvt_pk_bf16_f32 v107, v107, v111\n"
    "v_pk_mul_f32 v[112:113], v[112:113], v[132:133] op_sel_hi:[1,0]\n"
    "v_pk_mul_f32 v[114:115], v[114:115], v[132:133] op_sel_hi:[1,0]\n"
    "v_pk_mul_f32 v[116:117], v[116:117], v[132:133] op_sel_hi:[1,0]\n"
    "v_pk_mul_f32 v[118:119], v[118:119], v[132:133] op_sel_hi:[1,0]\n"
    "v_cvt_pk_bf16_f32 v112, v112, v116\n"
    "v_cvt_pk_bf16_f32 v113, v113, v117\n"
    "v_cvt_pk_bf16_f32 v114, v114, v118\n"
    "v_cvt_pk_bf16_f32 v115, v115, v119\n"
    "s_waitcnt vmcnt(0)\n"
    "s_mov_b32 s56, 0\n"
    ".Lr9_a%=:\n"
    "v_subrev_u32 v78, s52, v79\n"
    "v_cmp_eq_u32 vcc, -1, v79\n"
    "s_cbranch_vccnz .Lr9_%=\n"
    ".Lr9_x%=:\n"
    "v_max_i32_dpp v78, v78, v78 row_ror:8 row_mask:0xf bank_mask:0xf\n"
    "v_mov_b64 v[124:125], 0\n"
    "v_mov_b64 v[126:127], 0\n"
    "v_max_i32_dpp v78, v78, v78 row_ror:4 row_mask:0xf bank_mask:0xf\n"
    "v_mov_b64 v[128:129], 0\n"
    "v_mov_b64 v[130:131], 0\n"
    "v_max_i32_dpp v78, v78, v78 row_ror:2 row_mask:0xf bank_mask:0xf\n"
    "s_nop 1\n"
    "v_max_i32_dpp v78, v78, v78 row_ror:1 row_mask:0xf bank_mask:0xf\n"
    "s_nop 0\n"
    "v_readlane_b32 s95, v78, 0\n"
    "v_readlane_b32 s73, v78, 16\n"
    "v_readlane_b32 s51, v78, 32\n"
    "v_readlane_b32 s53, v78, 48\n"
    "s_max_i32 s95, s95, s73\n"
    "s_max_i32 s51, s51, s53\n"
    "s_max_i32 s95, s95, s51\n"
    "s_min_i32 s95, s95, 1024\n"
    "s_add_u32 s95, s95, s52\n"
    "s_add_u32 s95, s95, s55\n"
    "ds_read_b128 v[72:75], v2 offset:10240\n"
    "ds_read_b128 v[76:79], v2 offset:11264\n"
    "s_lshr_b32 s67, %2, 1\n"
    "s_mul_i32 s73, s48, 3\n"
    "s_add_u32 s73, s73, s67\n"
    "s_and_b32 s51, s73, 1\n"
    "s_lshl_b32 s51, s51, 12\n"
    "s_lshr_b32 s73, s73, 1\n"
    "s_lshl_b32 s73, s73, 14\n"
    "s_add_u32 s73, s73, s51\n"
    "s_add_u32 s67, s73, s68\n"
    "s_and_b32 s73, %2, 1\n"
    "s_lshl_b32 s73, s73, 11\n"
    "s_add_u32 s67, s67, s73\n"
    "s_mov_b32 s72, s58\n"
    "s_cmp_eq_u32 s59, 0\n"
    "s_cbranch_scc1 .Lr10_%=\n"
    ".Lr10_x%=:\n"
    "s_add_u32 s73, s48, 2\n"
    "s_and_b32 s73, s73, 63\n"
    "s_lshr_b32 s51, s73, 2\n"
    "s_xor_b32 s51, s51, s69\n"
    "s_and_b32 s51, s51, 1\n"
    "s_lshl_b32 s51, s51, 13\n"
    "s_lshl_b32 s73, s73, 5\n"
    "s_add_u32 s73, s73, %4\n"
    "s_lshl_b32 s73, s73, 14\n"
    "s_add_u32 s73, s73, s51\n"
    "s_add_u32 s60, s73, 0x6000000\n"
    "s_add_u32 s61, s73, 0x6001000\n"
    "s_add_u32 s62, s73, 0x8000000\n"
    "s_add_u32 s63, s73, 0x8001000\n"
    "s_lshl_b32 s51, %5, 12\n"
    "s_add_u32 s73, s73, s51\n"
    "s_add_u32 s64, s73, 0x4000000\n"
    "s_mul_i32 s73, s48, 3\n"
    "s_add_u32 s73, s73, 5\n"
    "s_and_b32 s51, s73, 1\n"
    "s_lshl_b32 s51, s51, 12\n"
    "s_lshr_b32 s73, s73, 1\n"
    "s_lshl_b32 s73, s73, 14\n"
    "s_add_u32 s73, s73, s51\n"
    "s_add_u32 s58, s73, s68\n"
    "s_sub_u32 s53, s95, 16\n"
    "s_waitcnt lgkmcnt(0)\n"
    ".Lgl11_%=:\n"
    "s_memrealtime s[50:51]\n"
    "s_waitcnt lgkmcnt(0)\n"
    "s_sub_u32 s50, s50, s53\n"
    "s_cmp_lt_i32 s50, 0\n"
    "s_cbranch_scc1 .Lgl11_%=\n"
    "s_lshr_b32 s51, s48, 2\n"
    "s_xor_b32 s51, s51, s69\n"
    "s_and_b32 s51, s51, 1\n"
    "s_lshl_b32 s51, s51, 13\n"
    "s_lshl_b32 s73, s48, 19\n"
    "s_add_u32 s73, s73, s75\n"
    "s_add_u32 s73, s73, s51\n"
    "buffer_store_dwordx4 v[104:107], v10, s[40:43], s73 offen sc1\n"
    "buffer_store_dwordx4 v[112:115], v10, s[40:43], s73 offen offset:1024 sc1\n"
    "s_branch .Lg_%=\n"
    ".Lr4_%=:\n"
    "s_sleep 2\n"
    "buffer_load_dword v79, v0, s[40:43], s58 offen offset:3072 sc1\n"
    "s_waitcnt vmcnt(0)\n"
    "s_add_u32 s56, s56, 1\n"
    "s_cmp_lt_u32 s56, s57\n"
    "s_cbranch_scc1 .Lr4_a%=\n"
    "s_mov_b32 s57, 0\n"
    "v_mov_b32 v135, 1\n"
    "global_store_dword v11, v135, %1 sc1\n"
    "s_branch .Lr4_x%=\n"
    ".Lr6_%=:\n"
    "s_cmp_lg_u32 s70, 0\n"
    "s_cbranch_scc1 .Lr6_y%=\n"
    "s_waitcnt lgkmcnt(0)\n"
    "s_add_u32 s73, s48, 1\n"
    "s_and_b32 s51, s73, 32\n"
    "s_and_b32 s73, s73, 31\n"
    "s_lshl3_add_u32 s73, s51, s73\n"
    "s_lshl2_add_u32 s73, s73, s92\n"
    "s_mov_b32 s53, s73\n"
    "s_mov_b32 s56, 0\n"
    "v_mov_b32 v134, s53\n"
    ".Lpoll12_%=:\n"
    "global_load_dword v132, v134, %1 sc1\n"
    "global_load_dword v133, v134, %1 offset:128 sc1\n"
    "s_waitcnt vmcnt(0)\n"
    "v_and_b32 v133, v132, v133\n"
    "s_nop 0\n"
    "v_readfirstlane_b32 s73, v133\n"
    "s_cmp_ge_u32 s73, 1\n"
    "s_cbranch_scc1 .Lpoll12_x%=\n"
    "s_add_u32 s56, s56, 1\n"
    "s_cmp_lt_u32 s56, 3\n"
    "s_cbranch_scc1 .Lpoll12_%=\n"
    "s_sleep 8\n"
    "s_cmp_lt_u32 s56, s57\n"
    "s_cbranch_scc1 .Lpoll12_%=\n"
    "s_mov_b32 s57, 0\n"
    "v_mov_b32 v135, 1\n"
    "global_store_dword v11, v135, %1 sc1\n"
    ".Lpoll12_x%=:\n"
    ".Lr6_y%=:\n"
    "buffer_load_dwordx4 v[12:15], v5, s[40:43], s60 offen sc1\n"
    "buffer_load_dwordx4 v[16:19], v5, s[40:43], s61 offen sc1\n"
    "buffer_load_dwordx4 v[20:23], v5, s[40:43], s62 offen sc1\n"
    "buffer_load_dwordx4 v[24:27], v5, s[40:43], s63 offen sc1\n"
    "buffer_load_dwordx4 v[28:31], v5, s[40:43], s64 offen sc1\n"
    "s_waitcnt vmcnt(0)\n"
    "s_branch .Lr6_x%=\n"
    ".Lr8_%=:\n"
    "s_waitcnt vmcnt(2)\n"
    "v_and_b32 v77, v76, v77\n"
    "s_nop 0\n"
    "v_readfirstlane_b32 s59, v77\n"
    "s_cmp_eq_u32 s59, 0\n"
    "s_cbranch_scc1 .Lr8_x%=\n"
    "buffer_load_dwordx4 v[12:15], v5, s[40:43], s60 offen sc1\n"
    "buffer_load_dwordx4 v[16:19], v5, s[40:43], s61 offen sc1\n"
    "buffer_load_dwordx4 v[20:23], v5, s[40:43], s62 offen sc1\n"
    "buffer_load_dwordx4 v[24:27], v5, s[40:43], s63 offen sc1\n"
    "buffer_load_dwordx4 v[28:31], v5, s[40:43], s64 offen sc1\n"
    "s_branch .Lr8_x%=\n"
    ".Lr9_%=:\n"
    "s_sleep 2\n"
    "buffer_load_dword v79, v0, s[40:43], s58 offen offset:3072 sc1\n"
    "s_waitcnt vmcnt(0)\n"
    "s_add_u32 s56, s56, 1\n"
    "s_cmp_lt_u32 s56, s57\n"
    "s_cbranch_scc1 .Lr9_a%=\n"
    "s_mov_b32 s57, 0\n"
    "v_mov_b32 v135, 1\n"
    "global_store_dword v11, v135, %1 sc1\n"
    "s_branch .Lr9_x%=\n"
    ".Lr10_%=:\n"
    "s_waitcnt lgkmcnt(0)\n"
    "s_add_u32 s73, s48, 1\n"
    "s_and_b32 s51, s73, 32\n"
    "s_and_b32 s73, s73, 31\n"
    "s_lshl3_add_u32 s73, s51, s73\n"
    "s_lshl2_add_u32 s73, s73, s92\n"
    "s_mov_b32 s53, s73\n"
    "s_mov_b32 s56, 0\n"
    "v_mov_b32 v134, s53\n"
    ".Lpoll13_%=:\n"
    "global_load_dword v132, v134, %1 sc1\n"
    "global_load_dword v133, v134, %1 offset:128 sc1\n"
    "s_waitcnt vmcnt(0)\n"
    "v_and_b32 v133, v132, v133\n"
    "s_nop 0\n"
    "v_readfirstlane_b32 s73, v133\n"
    "s_cmp_ge_u32 s73, 1\n"
    "s_cbranch_scc1 .Lpoll13_x%=\n"
    "s_add_u32 s56, s56, 1\n"
    "s_cmp_lt_u32 s56, 3\n"
    "s_cbranch_scc1 .Lpoll13_%=\n"
    "s_sleep 8\n"
    "s_cmp_lt_u32 s56, s57\n"
    "s_cbranch_scc1 .Lpoll13_%=\n"
    "s_mov_b32 s57, 0\n"
    "v_mov_b32 v135, 1\n"
    "global_store_dword v11, v135, %1 sc1\n"
    ".Lpoll13_x%=:\n"
    ".Lr10_y%=:\n"
    "buffer_load_dwordx4 v[12:15], v5, s[40:43], s60 offen sc1\n"
    "buffer_load_dwordx4 v[16:19], v5, s[40:43], s61 offen sc1\n"
    "buffer_load_dwordx4 v[20:23], v5, s[40:43], s62 offen sc1\n"
    "buffer_load_dwordx4 v[24:27], v5, s[40:43], s63 offen sc1\n"
    "buffer_load_dwordx4 v[28:31], v5, s[40:43], s64 offen sc1\n"
    "s_waitcnt vmcnt(0)\n"
    "s_branch .Lr10_x%=\n"
    ".Lend_%=:\n"
    "ds_write_b128 v5, v[124:127] offset:22784\n"
    "s_waitcnt vmcnt(0)\n"
    "s_cmp_ge_u32 %2, 2\n"
    "s_cbranch_scc1 .Lla_%=\n"
    "s_waitcnt lgkmcnt(0)\n"
    "s_barrier\n"
    "s_branch .Lout_%=\n"
    ".Lla_%=:\n"
    "ds_read_b128 v[32:35], v0 offset:16384\n"
    "ds_read_b128 v[48:51], v0\n"
    "ds_read_b128 v[88:91], v0 offset:4096\n"
    "ds_read_b128 v[104:107], v4 offset:20480\n"
    "ds_read_b128 v[36:39], v0 offset:17408\n"
    "ds_read_b128 v[52:55], v0 offset:1024\n"
    "ds_read_b128 v[92:95], v0 offset:5120\n"
    "ds_read_b128 v[108:111], v4 offset:20544\n"
    "ds_read_b128 v[40:43], v0 offset:18432\n"
    "ds_read_b128 v[56:59], v0 offset:2048\n"
    "ds_read_b128 v[96:99], v0 offset:6144\n"
    "ds_read_b128 v[112:115], v4 offset:20608\n"
    "s_waitcnt lgkmcnt(10)\n"
    "v_mfma_f32_16x16x32_bf16 v[120:123], v[48:51], v[32:35], 0\n"
    "s_barrier\n"
    "ds_read_b128 v[44:47], v0 offset:19456\n"
    "ds_read_b128 v[60:63], v0 offset:3072\n"
    "ds_read_b128 v[100:103], v0 offset:7168\n"
    "ds_read_b128 v[116:119], v4 offset:20672\n"
    "s_waitcnt lgkmcnt(13)\n"
    "v_mfma_f32_16x16x32_bf16 v[124:127], v[88:91], v[32:35], 0\n"
    "s_waitcnt lgkmcnt(12)\n"
    "v_mfma_f32_16x16x32_bf16 v[128:131], v[104:107], v[32:35], 0\n"
    "s_waitcnt lgkmcnt(10)\n"
    "v_mfma_f32_16x16x32_bf16 v[120:123], v[52:55], v[36:39], v[120:123]\n"
    "s_waitcnt lgkmcnt(9)\n"
    "v_mfma_f32_16x16x32_bf16 v[124:127], v[92:95], v[36:39], v[124:127]\n"
    "ds_read_b128 v[64:67], v10 offset:4096\n"
    "s_waitcnt lgkmcnt(9)\n"
    "v_mfma_f32_16x16x32_bf16 v[128:131], v[108:111], v[36:39], v[128:131]\n"
    "ds_read_b128 v[68:71], v10 offset:8192\n"
    "s_waitcnt lgkmcnt(8)\n"
    "v_mfma_f32_16x16x32_bf16 v[120:123], v[56:59], v[40:43], v[120:123]\n"
    "ds_read_b128 v[72:75], v10 offset:5120\n"
    "s_waitcnt lgkmcnt(8)\n"
    "v_mfma_f32_16x16x32_bf16 v[124:127], v[96:99], v[40:43], v[124:127]\n"
    "ds_read_b128 v[80:83], v10 offset:9216\n"
    "s_waitcnt lgkmcnt(8)\n"
    "v_mfma_f32_16x16x32_bf16 v[128:131], v[112:115], v[40:43], v[128:131]\n"
    "ds_read_b128 v[84:87], v11 offset:31888\n"
    "s_waitcnt lgkmcnt(7)\n"
    "v_mfma_f32_16x16x32_bf16 v[120:123], v[60:63], v[44:47], v[120:123]\n"
    "ds_read_b64 v[134:135], v11 offset:31904\n"
    "s_waitcnt lgkmcnt(7)\n"
    "v_mfma_f32_16x16x32_bf16 v[124:127], v[100:103], v[44:47], v[124:127]\n"
    "ds_read_b128 v[88:91], v10 offset:18688\n"
    "s_waitcnt lgkmcnt(7)\n"
    "v_mfma_f32_16x16x32_bf16 v[128:131], v[116:119], v[44:47], v[128:131]\n"
    "ds_read_b128 v[92:95], v10 offset:19712\n"
    "v_mov_b32 v132, 0xd01502f9\n"
    "v_cndmask_b32 v120, v132, v120, s[76:77]\n"
    "v_cndmask_b32 v121, v132, v121, s[78:79]\n"
    "v_cndmask_b32 v122, v132, v122, s[80:81]\n"
    "v_cndmask_b32 v123, v132, v123, s[82:83]\n"
    "v_cndmask_b32 v124, v132, v124, s[84:85]\n"
    "v_cndmask_b32 v125, v132, v125, s[86:87]\n"
    "v_cndmask_b32 v126, v132, v126, s[88:89]\n"
    "v_cndmask_b32 v127, v132, v127, s[90:91]\n"
    "v_maximum3_f32 v133, v128, v129, v120\n"
    "v_maximum3_f32 v133, v133, v121, v122\n"
    "v_maximum3_f32 v133, v133, v123, v124\n"
    "v_maximum3_f32 v133, v133, v125, v126\n"
    "v_maximum3_f32 v133, v133, v127, v127\n"
    "v_mov_b32 v132, v133\n"
    "s_nop 1\n"
    "v_permlane16_swap_b32 v132, v133\n"
    "v_maximum3_f32 v133, v133, v132, v132\n"
    "v_mov_b32 v132, v133\n"
    "s_nop 1\n"
    "v_permlane32_swap_b32 v132, v133\n"
    "v_maximum3_f32 v133, v133, v132, v132\n"
    "v_mul_f32 v133, 0xbe0293ee, v133\n"
    "v_fmamk_f32 v120, v120, 0x3e0293ee, v133\n"
    "v_fmamk_f32 v121, v121, 0x3e0293ee, v133\n"
    "v_fmamk_f32 v122, v122, 0x3e0293ee, v133\n"
    "v_fmamk_f32 v123, v123, 0x3e0293ee, v133\n"
    "v_fmamk_f32 v124, v124, 0x3e0293ee, v133\n"
    "v_fmamk_f32 v125, v125, 0x3e0293ee, v133\n"
    "v_fmamk_f32 v126, v126, 0x3e0293ee, v133\n"
    "v_fmamk_f32 v127, v127, 0x3e0293ee, v133\n"
    "v_fmamk_f32 v128, v128, 0x3e0293ee, v133\n"
    "v_fmamk_f32 v129, v129, 0x3e0293ee, v133\n"
    "v_exp_f32 v120, v120\n"
    "v_exp_f32 v121, v121\n"
    "v_exp_f32 v122, v122\n"
    "v_exp_f32 v123, v123\n"
    "v_exp_f32 v124, v124\n"
    "v_exp_f32 v125, v125\n"
    "v_exp_f32 v126, v126\n"
    "v_exp_f32 v127, v127\n"
    "v_exp_f32 v128, v128\n"
    "v_exp_f32 v129, v129\n"
    "v_cvt_pk_bf16_f32 v120, v120, v121\n"
    "v_cvt_pk_bf16_f32 v121, v122, v123\n"
    "v_cvt_pk_bf16_f32 v122, v124, v125\n"
    "v_cvt_pk_bf16_f32 v123, v126, v127\n"
    "v_cvt_pk_bf16_f32 v124, v128, v129\n"
    "v_cvt_pk_bf16_f32 v125, v128, v129\n"
    "s_waitcnt lgkmcnt(3)\n"
    "v_mfma_f32_16x16x32_bf16 v[100:103], v[84:87], v[120:123], 0\n"
    "v_mfma_f32_16x16x32_bf16 v[104:107], v[64:67], v[120:123], 0\n"
    "v_mfma_f32_16x16x32_bf16 v[108:111], v[68:71], v[120:123], 0\n"
    "s_waitcnt lgkmcnt(2)\n"
    "v_mfma_f32_16x16x16_bf16 v[100:103], v[134:135], v[124:125], v[100:103]\n"
    "v_mfma_f32_16x16x32_bf16 v[112:115], v[72:75], v[120:123], 0\n"
    "v_mfma_f32_16x16x32_bf16 v[116:119], v[80:83], v[120:123], 0\n"
    "s_waitcnt lgkmcnt(1)\n"
    "v_mfma_f32_16x16x16_bf16 v[104:107], v[88:89], v[124:125], v[104:107]\n"
    "v_mfma_f32_16x16x16_bf16 v[108:111], v[90:91], v[124:125], v[108:111]\n"
    "v_rcp_f32 v132, v100\n"
    "s_waitcnt lgkmcnt(0)\n"
    "v_mfma_f32_16x16x16_bf16 v[112:115], v[92:93], v[124:125], v[112:115]\n"
    "v_mfma_f32_16x16x16_bf16 v[116:119], v[94:95], v[124:125], v[116:119]\n"
    "s_nop 1\n"
    "v_pk_mul_f32 v[104:105], v[104:105], v[132:133] op_sel_hi:[1,0]\n"
    "v_pk_mul_f32 v[106:107], v[106:107], v[132:133] op_sel_hi:[1,0]\n"
    "v_pk_mul_f32 v[108:109], v[108:109], v[132:133] op_sel_hi:[1,0]\n"
    "v_pk_mul_f32 v[110:111], v[110:111], v[132:133] op_sel_hi:[1,0]\n"
    "v_cvt_pk_bf16_f32 v104, v104, v108\n"
    "v_cvt_pk_bf16_f32 v105, v105, v109\n"
    "v_cvt_pk_bf16_f32 v106, v106, v110\n"
    "v_cvt_pk_bf16_f32 v107, v107, v111\n"
    "v_pk_mul_f32 v[112:113], v[112:113], v[132:133] op_sel_hi:[1,0]\n"
    "v_pk_mul_f32 v[114:115], v[114:115], v[132:133] op_sel_hi:[1,0]\n"
    "v_pk_mul_f32 v[116:117], v[116:117], v[132:133] op_sel_hi:[1,0]\n"
    "v_pk_mul_f32 v[118:119], v[118:119], v[132:133] op_sel_hi:[1,0]\n"
    "v_cvt_pk_bf16_f32 v112, v112, v116\n"
    "v_cvt_pk_bf16_f32 v113, v113, v117\n"
    "v_cvt_pk_bf16_f32 v114, v114, v118\n"
    "v_cvt_pk_bf16_f32 v115, v115, v119\n"
    "s_lshr_b32 s51, s48, 2\n"
    "s_xor_b32 s51, s51, s69\n"
    "s_and_b32 s51, s51, 1\n"
    "s_lshl_b32 s51, s51, 13\n"
    "s_lshl_b32 s73, s48, 19\n"
    "s_add_u32 s73, s73, s75\n"
    "s_add_u32 s73, s73, s51\n"
    "buffer_store_dwordx4 v[104:107], v10, s[40:43], s73 offen sc1\n"
    "buffer_store_dwordx4 v[112:115], v10, s[40:43], s73 offen offset:1024 sc1\n"
    "s_add_u32 s73, s48, 0\n"
    "s_and_b32 s51, s73, 32\n"
    "s_and_b32 s73, s73, 31\n"
    "s_lshl3_add_u32 s73, s51, s73\n"
    "s_lshl2_add_u32 s73, s73, s93\n"
    "v_mov_b32 v100, s73\n"
    "v_mov_b32 v101, 1\n"
    "s_waitcnt vmcnt(0)\n"
    "s_mov_b64 exec, 1\n"
    "ds_add_rtn_u32 v101, v11, v101 offset:31872\n"
    "s_mov_b64 exec, -1\n"
    "s_waitcnt lgkmcnt(0)\n"
    "v_readfirstlane_b32 s73, v101\n"
    "v_mov_b32 v101, 1\n"
    "s_bitcmp1_b32 s73, 0\n"
    "s_cbranch_scc0 .Lout_%=\n"
    "s_mov_b64 exec, 1\n"
    "global_store_dword v100, v101, %1 sc1\n"
    "s_mov_b64 exec, -1\n"
    "s_cmp_eq_u32 %3, 128\n"
    "s_cbranch_scc0 .Lout_%=\n"
    "v_mov_b32 v130, 0x20400\n"
    "global_load_dwordx2 v[132:133], v130, %1 sc1\n"
    "v_mbcnt_lo_u32_b32 v131, -1, 0\n"
    "v_min_u32 v131, 7, v131\n"
    "v_lshlrev_b32 v131, 7, v131\n"
    "v_add_u32 v131, 0x20000, v131\n"
    "global_load_dwordx2 v[134:135], v131, %1 sc1\n"
    "s_waitcnt vmcnt(0)\n"
    "v_add_u32 v132, 1, v132\n"
    "v_add_u32 v133, 1, v133\n"
    "global_store_dwordx2 v130, v[132:133], %1 sc1\n"
    ".Lout_%=:\n"
    "s_waitcnt vmcnt(0)\n"
    "s_endpgm\n"
    ".p2align 6\n"
    ".Lrole1_%=:\n"
    "s_mov_b64 s[40:41], %0\n"
    "s_and_b32 s41, s41, 0xffff\n"
    "s_mov_b32 s42, 0xc000000\n"
    "s_mov_b32 s43, 0x20000\n"
    "s_mov_b64 s[92:93], %0\n"
    "s_and_b32 s93, s93, 0xffff\n"
    "s_mov_b32 s94, 0xc000000\n"
    "s_mov_b32 s95, 0x20000\n"
    "s_mov_b32 s53, 131072\n"
    "s_mov_b64 s[56:57], 0\n"
    "v_mbcnt_lo_u32_b32 v7, -1, 0\n"
    "v_mbcnt_hi_u32_b32 v7, -1, v7\n"
    "s_lshl_b32 s50, %2, 8\n"
    "s_add_u32 s50, s50, 147456\n"
    "v_lshl_add_u32 v7, v7, 2, s50\n"
    "ds_read_b32 v1, v7\n"
    "ds_read_b32 v2, v7 offset:1024\n"
    "ds_read_b32 v3, v7 offset:2048\n"
    "ds_read_b32 v5, v7 offset:3072\n"
    "ds_read_b32 v6, v7 offset:4096\n"
    "ds_read_b32 v48, v7 offset:5120\n"
    "ds_read_b32 v49, v7 offset:6144\n"
    "ds_read_b32 v50, v7 offset:7168\n"
    "s_waitcnt lgkmcnt(0)\n"
    "s_barrier\n"
    "s_lshl_b32 s54, %2, 10\n"
    "s_add_u32 s88, s40, 32768\n"
    "s_mov_b32 s89, s41\n"
    "s_cmp_lt_u32 %2, 2\n"
    "s_cselect_b32 s96, s42, 0\n"
    "s_mov_b32 s91, s43\n"
    "s_mov_b32 s86, 0x3f803f80\n"
    "s_mov_b32 s87, 0x3f803f80\n"
    "v_mov_b64 v[76:77], s[86:87]\n"
    "v_mov_b64 v[78:79], s[86:87]\n"
    "s_mov_b64 s[44:45], %8\n"
    "s_and_b32 s45, s45, 0xffff\n"
    "s_mov_b32 s46, 0x480000\n"
    "s_mov_b32 s47, 0x20000\n"
    "s_add_u32 m0, s54, 0\n"
    "s_add_u32 s55, %6, 0\n"
    "buffer_load_dwordx4 v48, s[44:47], s55 offen lds\n"
    "s_add_u32 m0, s54, 4096\n"
    "s_add_u32 s55, %6, 1048576\n"
    "buffer_load_dwordx4 v48, s[44:47], s55 offen lds\n"
    "s_add_u32 m0, s54, 8192\n"
    "s_add_u32 s55, %6, 2097152\n"
    "buffer_load_dwordx4 v48, s[44:47], s55 offen lds\n"
    "s_add_u32 m0, s54, 12288\n"
    "s_add_u32 s55, %6, 3145728\n"
    "buffer_load_dwordx4 v48, s[44:47], s55 offen lds\n"
    "s_add_u32 m0, s54, 16384\n"
    "s_add_u32 s55, %6, 32768\n"
    "buffer_load_dwordx4 v48, s[44:47], s55 offen lds\n"
    "s_add_u32 m0, s54, 20480\n"
    "s_add_u32 s55, %6, 1081344\n"
    "buffer_load_dwordx4 v48, s[44:47], s55 offen lds\n"
    "s_add_u32 m0, s54, 24576\n"
    "s_add_u32 s55, %6, 2129920\n"
    "buffer_load_dwordx4 v48, s[44:47], s55 offen lds\n"
    "s_add_u32 m0, s54, 28672\n"
    "s_add_u32 s55, %6, 3178496\n"
    "buffer_load_dwordx4 v48, s[44:47], s55 offen lds\n"
    "s_add_u32 m0, s54, 32768\n"
    "s_add_u32 s55, %6, 262144\n"
    "buffer_load_dwordx4 v48, s[44:47], s55 offen lds\n"
    "s_add_u32 m0, s54, 36864\n"
    "s_add_u32 s55, %6, 1310720\n"
    "buffer_load_dwordx4 v48, s[44:47], s55 offen lds\n"
    "s_add_u32 m0, s54, 40960\n"
    "s_add_u32 s55, %6, 2359296\n"
    "buffer_load_dwordx4 v48, s[44:47], s55 offen lds\n"
    "s_add_u32 m0, s54, 45056\n"
    "s_add_u32 s55, %6, 3407872\n"
    "buffer_load_dwordx4 v48, s[44:47], s55 offen lds\n"
    "s_add_u32 m0, s54, 49152\n"
    "s_add_u32 s55, %6, 294912\n"
    "buffer_load_dwordx4 v48, s[44:47], s55 offen lds\n"
    "s_add_u32 m0, s54, 53248\n"
    "s_add_u32 s55, %6, 1343488\n"
    "buffer_load_dwordx4 v48, s[44:47], s55 offen lds\n"
    "s_add_u32 m0, s54, 57344\n"
    "s_add_u32 s55, %6, 2392064\n"
    "buffer_load_dwordx4 v48, s[44:47], s55 offen lds\n"
    "s_add_u32 m0, s54, 61440\n"
    "s_add_u32 s55, %6, 3440640\n"
    "buffer_load_dwordx4 v48, s[44:47], s55 offen lds\n"
    "s_add_u32 m0, s54, 65536\n"
    "s_add_u32 s55, %6, 524288\n"
    "buffer_load_dwordx4 v48, s[44:47], s55 offen lds\n"
    "s_add_u32 m0, s54, 69632\n"
    "s_add_u32 s55, %6, 1572864\n"
    "buffer_load_dwordx4 v48, s[44:47], s55 offen lds\n"
    "s_add_u32 m0, s54, 73728\n"
    "s_add_u32 s55, %6, 2621440\n"
    "buffer_load_dwordx4 v48, s[44:47], s55 offen lds\n"
    "s_add_u32 m0, s54, 77824\n"
    "s_add_u32 s55, %6, 3670016\n"
    "buffer_load_dwordx4 v48, s[44:47], s55 offen lds\n"
    "s_add_u32 m0, s54, 81920\n"
    "s_add_u32 s55, %6, 557056\n"
    "buffer_load_dwordx4 v48, s[44:47], s55 offen lds\n"
    "s_add_u32 m0, s54, 86016\n"
    "s_add_u32 s55, %6, 1605632\n"
    "buffer_load_dwordx4 v48, s[44:47], s55 offen lds\n"
    "s_add_u32 m0, s54, 90112\n"
    "s_add_u32 s55, %6, 2654208\n"
    "buffer_load_dwordx4 v48, s[44:47], s55 offen lds\n"
    "s_add_u32 m0, s54, 94208\n"
    "s_add_u32 s55, %6, 3702784\n"
    "buffer_load_dwordx4 v48, s[44:47], s55 offen lds\n"
    "s_add_u32 m0, s54, 98304\n"
    "s_add_u32 s55, %6, 786432\n"
    "buffer_load_dwordx4 v48, s[44:47], s55 offen lds\n"
    "s_add_u32 m0, s54, 102400\n"
    "s_add_u32 s55, %6, 1835008\n"
    "buffer_load_dwordx4 v48, s[44:47], s55 offen lds\n"
    "s_add_u32 m0, s54, 106496\n"
    "s_add_u32 s55, %6, 2883584\n"
    "buffer_load_dwordx4 v48, s[44:47], s55 offen lds\n"
    "s_add_u32 m0, s54, 110592\n"
    "s_add_u32 s55, %6, 3932160\n"
    "buffer_load_dwordx4 v48, s[44:47], s55 offen lds\n"
    "s_add_u32 m0, s54, 114688\n"
    "s_add_u32 s55, %6, 819200\n"
    "buffer_load_dwordx4 v48, s[44:47], s55 offen lds\n"
    "s_add_u32 m0, s54, 118784\n"
    "s_add_u32 s55, %6, 1867776\n"
    "buffer_load_dwordx4 v48, s[44:47], s55 offen lds\n"
    "s_add_u32 m0, s54, 122880\n"
    "s_add_u32 s55, %6, 2916352\n"
    "buffer_load_dwordx4 v48, s[44:47], s55 offen lds\n"
    "s_add_u32 m0, s54, 126976\n"
    "s_add_u32 s55, %6, 3964928\n"
    "buffer_load_dwordx4 v48, s[44:47], s55 offen lds\n"
    "s_add_u32 m0, s54, 131072\n"
    "s_add_u32 s55, %6, 4194304\n"
    "buffer_load_dwordx4 v48, s[44:47], s55 offen lds\n"
    "s_add_u32 m0, s54, 135168\n"
    "s_add_u32 s55, %6, 4227072\n"
    "buffer_load_dwordx4 v48, s[44:47], s55 offen lds\n"
    "s_add_u32 m0, s54, 139264\n"
    "s_add_u32 s55, %6, 4456448\n"
    "buffer_load_dwordx4 v48, s[44:47], s55 offen lds\n"
    "s_add_u32 m0, s54, 143360\n"
    "s_add_u32 s55, %6, 4489216\n"
    "buffer_load_dwordx4 v48, s[44:47], s55 offen lds\n"
    "s_xor_b32 s50, %2, 0\n"
    "s_lshl_b32 s50, s50, 12\n"
    "v_add_u32 v32, s50, v49\n"
    "v_add_u32 v33, s50, v50\n"
    "s_xor_b32 s50, %2, 1\n"
    "s_lshl_b32 s50, s50, 12\n"
    "v_add_u32 v34, s50, v49\n"
    "v_add_u32 v35, s50, v50\n"
    "s_xor_b32 s50, %2, 2\n"
    "s_lshl_b32 s50, s50, 12\n"
    "v_add_u32 v36, s50, v49\n"
    "v_add_u32 v37, s50, v50\n"
    "s_xor_b32 s50, %2, 3\n"
    "s_lshl_b32 s50, s50, 12\n"
    "v_add_u32 v38, s50, v49\n"
    "v_add_u32 v39, s50, v50\n"
    "s_waitcnt vmcnt(32) lgkmcnt(0)\n"
    "s_barrier\n"
    "ds_read_b64_tr_b16 a[0:1], v32\n"
    "ds_read_b64_tr_b16 a[4:5], v33\n"
    "ds_read_b64_tr_b16 a[32:33], v34\n"
    "ds_read_b64_tr_b16 a[36:37], v35\n"
    "ds_read_b64_tr_b16 a[64:65], v36\n"
    "ds_read_b64_tr_b16 a[68:69], v37\n"
    "ds_read_b64_tr_b16 a[96:97], v38\n"
    "ds_read_b64_tr_b16 a[100:101], v39\n"
    "s_waitcnt vmcnt(28) lgkmcnt(0)\n"
    "s_barrier\n"
    "s_mov_b64 s[44:45], %9\n"
    "s_and_b32 s45, s45, 0xffff\n"
    "s_mov_b32 s46, 0x480000\n"
    "s_mov_b32 s47, 0x20000\n"
    "s_add_u32 m0, s54, 0\n"
    "s_add_u32 s55, %6, 0\n"
    "buffer_load_dwordx4 v48, s[44:47], s55 offen lds\n"
    "s_add_u32 m0, s54, 4096\n"
    "s_add_u32 s55, %6, 1048576\n"
    "buffer_load_dwordx4 v48, s[44:47], s55 offen lds\n"
    "s_add_u32 m0, s54, 8192\n"
    "s_add_u32 s55, %6, 2097152\n"
    "buffer_load_dwordx4 v48, s[44:47], s55 offen lds\n"
    "s_add_u32 m0, s54, 12288\n"
    "s_add_u32 s55, %6, 3145728\n"
    "buffer_load_dwordx4 v48, s[44:47], s55 offen lds\n"
    "ds_read_b64_tr_b16 a[2:3], v32 offset:16384\n"
    "ds_read_b64_tr_b16 a[6:7], v33 offset:16384\n"
    "ds_read_b64_tr_b16 a[34:35], v34 offset:16384\n"
    "ds_read_b64_tr_b16 a[38:39], v35 offset:16384\n"
    "ds_read_b64_tr_b16 a[66:67], v36 offset:16384\n"
    "ds_read_b64_tr_b16 a[70:71], v37 offset:16384\n"
    "ds_read_b64_tr_b16 a[98:99], v38 offset:16384\n"
    "ds_read_b64_tr_b16 a[102:103], v39 offset:16384\n"
    "s_waitcnt vmcnt(28) lgkmcnt(0)\n"
    "s_barrier\n"
    "s_add_u32 m0, s54, 16384\n"
    "s_add_u32 s55, %6, 32768\n"
    "buffer_load_dwordx4 v48, s[44:47], s55 offen lds\n"
    "s_add_u32 m0, s54, 20480\n"
    "s_add_u32 s55, %6, 1081344\n"
    "buffer_load_dwordx4 v48, s[44:47], s55 offen lds\n"
    "s_add_u32 m0, s54, 24576\n"
    "s_add_u32 s55, %6, 2129920\n"
    "buffer_load_dwordx4 v48, s[44:47], s55 offen lds\n"
    "s_add_u32 m0, s54, 28672\n"
    "s_add_u32 s55, %6, 3178496\n"
    "buffer_load_dwordx4 v48, s[44:47], s55 offen lds\n"
    "v_mfma_f32_16x16x32_bf16 v[8:11], v[76:79], a[0:3], 0\n"
    "v_mfma_f32_16x16x32_bf16 v[12:15], v[76:79], a[4:7], 0\n"
    "v_mfma_f32_16x16x32_bf16 v[8:11], v[76:79], a[32:35], v[8:11]\n"
    "v_mfma_f32_16x16x32_bf16 v[12:15], v[76:79], a[36:39], v[12:15]\n"
    "v_mfma_f32_16x16x32_bf16 v[8:11], v[76:79], a[64:67], v[8:11]\n"
    "v_mfma_f32_16x16x32_bf16 v[12:15], v[76:79], a[68:71], v[12:15]\n"
    "v_mfma_f32_16x16x32_bf16 v[8:11], v[76:79], a[96:99], v[8:11]\n"
    "v_mfma_f32_16x16x32_bf16 v[12:15], v[76:79], a[100:103], v[12:15]\n"
    "ds_read_b64_tr_b16 a[8:9], v32 offset:32768\n"
    "ds_read_b64_tr_b16 a[12:13], v33 offset:32768\n"
    "ds_read_b64_tr_b16 a[40:41], v34 offset:32768\n"
    "ds_read_b64_tr_b16 a[44:45], v35 offset:32768\n"
    "ds_read_b64_tr_b16 a[72:73], v36 offset:32768\n"
    "ds_read_b64_tr_b16 a[76:77], v37 offset:32768\n"
    "ds_read_b64_tr_b16 a[104:105], v38 offset:32768\n"
    "ds_read_b64_tr_b16 a[108:109], v39 offset:32768\n"
    "s_waitcnt vmcnt(28) lgkmcnt(0)\n"
    "s_barrier\n"
    "s_add_u32 m0, s54, 32768\n"
    "s_add_u32 s55, %6, 262144\n"
    "buffer_load_dwordx4 v48, s[44:47], s55 offen lds\n"
    "s_add_u32 m0, s54, 36864\n"
    "s_add_u32 s55, %6, 1310720\n"
    "buffer_load_dwordx4 v48, s[44:47], s55 offen lds\n"
    "s_add_u32 m0, s54, 40960\n"
    "s_add_u32 s55, %6, 2359296\n"
    "buffer_load_dwordx4 v48, s[44:47], s55 offen lds\n"
    "s_add_u32 m0, s54, 45056\n"
    "s_add_u32 s55, %6, 3407872\n"
    "buffer_load_dwordx4 v48, s[44:47], s55 offen lds\n"
    "ds_read_b64_tr_b16 a[10:11], v32 offset:49152\n"
    "ds_read_b64_tr_b16 a[14:15], v33 offset:49152\n"
    "ds_read_b64_tr_b16 a[42:43], v34 offset:49152\n"
    "ds_read_b64_tr_b16 a[46:47], v35 offset:49152\n"
    "ds_read_b64_tr_b16 a[74:75], v36 offset:49152\n"
    "ds_read_b64_tr_b16 a[78:79], v37 offset:49152\n"
    "ds_read_b64_tr_b16 a[106:107], v38 offset:49152\n"
    "ds_read_b64_tr_b16 a[110:111], v39 offset:49152\n"
    "s_waitcnt vmcnt(28) lgkmcnt(0)\n"
    "s_barrier\n"
    "s_add_u32 m0, s54, 49152\n"
    "s_add_u32 s55, %6, 294912\n"
    "buffer_load_dwordx4 v48, s[44:47], s55 offen lds\n"
    "s_add_u32 m0, s54, 53248\n"
    "s_add_u32 s55, %6, 1343488\n"
    "buffer_load_dwordx4 v48, s[44:47], s55 offen lds\n"
    "s_add_u32 m0, s54, 57344\n"
    "s_add_u32 s55, %6, 2392064\n"
    "buffer_load_dwordx4 v48, s[44:47], s55 offen lds\n"
    "s_add_u32 m0, s54, 61440\n"
    "s_add_u32 s55, %6, 3440640\n"
    "buffer_load_dwordx4 v48, s[44:47], s55 offen lds\n"
    "v_mfma_f32_16x16x32_bf16 v[8:11], v[76:79], a[8:11], v[8:11]\n"
    "v_mfma_f32_16x16x32_bf16 v[12:15], v[76:79], a[12:15], v[12:15]\n"
    "v_mfma_f32_16x16x32_bf16 v[8:11], v[76:79], a[40:43], v[8:11]\n"
    "v_mfma_f32_16x16x32_bf16 v[12:15], v[76:79], a[44:47], v[12:15]\n"
    "v_mfma_f32_16x16x32_bf16 v[8:11], v[76:79], a[72:75], v[8:11]\n"
    "v_mfma_f32_16x16x32_bf16 v[12:15], v[76:79], a[76:79], v[12:15]\n"
    "v_mfma_f32_16x16x32_bf16 v[8:11], v[76:79], a[104:107], v[8:11]\n"
    "v_mfma_f32_16x16x32_bf16 v[12:15], v[76:79], a[108:111], v[12:15]\n"
    "v_add_u32 v71, 0x10000, v32\n"
    "ds_read_b64_tr_b16 a[16:17], v71\n"
    "v_add_u32 v70, 0x10000, v33\n"
    "ds_read_b64_tr_b16 a[20:21], v70\n"
    "v_add_u32 v69, 0x10000, v34\n"
    "ds_read_b64_tr_b16 a[48:49], v69\n"
    "v_add_u32 v68, 0x10000, v35\n"
    "ds_read_b64_tr_b16 a[52:53], v68\n"
    "v_add_u32 v67, 0x10000, v36\n"
    "ds_read_b64_tr_b16 a[80:81], v67\n"
    "v_add_u32 v66, 0x10000, v37\n"
    "ds_read_b64_tr_b16 a[84:85], v66\n"
    "v_add_u32 v65, 0x10000, v38\n"
    "ds_read_b64_tr_b16 a[112:113], v65\n"
    "v_add_u32 v64, 0x10000, v39\n"
    "ds_read_b64_tr_b16 a[116:117], v64\n"
    "s_waitcnt vmcnt(28) lgkmcnt(0)\n"
    "s_barrier\n"
    "s_add_u32 m0, s54, 65536\n"
    "s_add_u32 s55, %6, 524288\n"
    "buffer_load_dwordx4 v48, s[44:47], s55 offen lds\n"
    "s_add_u32 m0, s54, 69632\n"
    "s_add_u32 s55, %6, 1572864\n"
    "buffer_load_dwordx4 v48, s[44:47], s55 offen lds\n"
    "s_add_u32 m0, s54, 73728\n"
    "s_add_u32 s55, %6, 2621440\n"
    "buffer_load_dwordx4 v48, s[44:47], s55 offen lds\n"
    "s_add_u32 m0, s54, 77824\n"
    "s_add_u32 s55, %6, 3670016\n"
    "buffer_load_dwordx4 v48, s[44:47], s55 offen lds\n"
    "ds_read_b64_tr_b16 a[18:19], v71 offset:16384\n"
    "ds_read_b64_tr_b16 a[22:23], v70 offset:16384\n"
    "ds_read_b64_tr_b16 a[50:51], v69 offset:16384\n"
    "ds_read_b64_tr_b16 a[54:55], v68 offset:16384\n"
    "ds_read_b64_tr_b16 a[82:83], v67 offset:16384\n"
    "ds_read_b64_tr_b16 a[86:87], v66 offset:16384\n"
    "ds_read_b64_tr_b16 a[114:115], v65 offset:16384\n"
    "ds_read_b64_tr_b16 a[118:119], v64 offset:16384\n"
    "s_waitcnt vmcnt(28) lgkmcnt(0)\n"
    "s_barrier\n"
    "s_add_u32 m0, s54, 81920\n"
    "s_add_u32 s55, %6, 557056\n"
    "buffer_load_dwordx4 v48, s[44:47], s55 offen lds\n"
    "s_add_u32 m0, s54, 86016\n"
    "s_add_u32 s55, %6, 1605632\n"
    "buffer_load_dwordx4 v48, s[44:47], s55 offen lds\n"
    "s_add_u32 m0, s54, 90112\n"
    "s_add_u32 s55, %6, 2654208\n"
    "buffer_load_dwordx4 v48, s[44:47], s55 offen lds\n"
    "s_add_u32 m0, s54, 94208\n"
    "s_add_u32 s55, %6, 3702784\n"
    "buffer_load_dwordx4 v48, s[44:47], s55 offen lds\n"
    "v_mfma_f32_16x16x32_bf16 v[8:11], v[76:79], a[16:19], v[8:11]\n"
    "v_mfma_f32_16x16x32_bf16 v[12:15], v[76:79], a[20:23], v[12:15]\n"
    "v_mfma_f32_16x16x32_bf16 v[8:11], v[76:79], a[48:51], v[8:11]\n"
    "v_mfma_f32_16x16x32_bf16 v[12:15], v[76:79], a[52:55], v[12:15]\n"
    "v_mfma_f32_16x16x32_bf16 v[8:11], v[76:79], a[80:83], v[8:11]\n"
    "v_mfma_f32_16x16x32_bf16 v[12:15], v[76:79], a[84:87], v[12:15]\n"
    "v_mfma_f32_16x16x32_bf16 v[8:11], v[76:79], a[112:115], v[8:11]\n"
    "v_mfma_f32_16x16x32_bf16 v[12:15], v[76:79], a[116:119], v[12:15]\n"
    "ds_read_b64_tr_b16 a[24:25], v71 offset:32768\n"
    "ds_read_b64_tr_b16 a[28:29], v70 offset:32768\n"
    "ds_read_b64_tr_b16 a[56:57], v69 offset:32768\n"
    "ds_read_b64_tr_b16 a[60:61], v68 offset:32768\n"
    "ds_read_b64_tr_b16 a[88:89], v67 offset:32768\n"
    "ds_read_b64_tr_b16 a[92:93], v66 offset:32768\n"
    "ds_read_b64_tr_b16 a[120:121], v65 offset:32768\n"
    "ds_read_b64_tr_b16 a[124:125], v64 offset:32768\n"
    "s_waitcnt vmcnt(28) lgkmcnt(0)\n"
    "s_barrier\n"
    "s_add_u32 m0, s54, 98304\n"
    "s_add_u32 s55, %6, 786432\n"
    "buffer_load_dwordx4 v48, s[44:47], s55 offen lds\n"
    "s_add_u32 m0, s54, 102400\n"
    "s_add_u32 s55, %6, 1835008\n"
    "buffer_load_dwordx4 v48, s[44:47], s55 offen lds\n"
    "s_add_u32 m0, s54, 106496\n"
    "s_add_u32 s55, %6, 2883584\n"
    "buffer_load_dwordx4 v48, s[44:47], s55 offen lds\n"
    "s_add_u32 m0, s54, 110592\n"
    "s_add_u32 s55, %6, 3932160\n"
    "buffer_load_dwordx4 v48, s[44:47], s55 offen lds\n"
    "ds_read_b64_tr_b16 a[26:27], v71 offset:49152\n"
    "ds_read_b64_tr_b16 a[30:31], v70 offset:49152\n"
    "ds_read_b64_tr_b16 a[58:59], v69 offset:49152\n"
    "ds_read_b64_tr_b16 a[62:63], v68 offset:49152\n"
    "ds_read_b64_tr_b16 a[90:91], v67 offset:49152\n"
    "ds_read_b64_tr_b16 a[94:95], v66 offset:49152\n"
    "ds_read_b64_tr_b16 a[122:123], v65 offset:49152\n"
    "ds_read_b64_tr_b16 a[126:127], v64 offset:49152\n"
    "s_waitcnt vmcnt(28) lgkmcnt(0)\n"
    "s_barrier\n"
    "s_add_u32 m0, s54, 114688\n"
    "s_add_u32 s55, %6, 819200\n"
    "buffer_load_dwordx4 v48, s[44:47], s55 offen lds\n"
    "s_add_u32 m0, s54, 118784\n"
    "s_add_u32 s55, %6, 1867776\n"
    "buffer_load_dwordx4 v48, s[44:47], s55 offen lds\n"
    "s_add_u32 m0, s54, 122880\n"
    "s_add_u32 s55, %6, 2916352\n"
    "buffer_load_dwordx4 v48, s[44:47], s55 offen lds\n"
    "s_add_u32 m0, s54, 126976\n"
    "s_add_u32 s55, %6, 3964928\n"
    "buffer_load_dwordx4 v48, s[44:47], s55 offen lds\n"
    "v_mfma_f32_16x16x32_bf16 v[8:11], v[76:79], a[24:27], v[8:11]\n"
    "v_mfma_f32_16x16x32_bf16 v[12:15], v[76:79], a[28:31], v[12:15]\n"
    "v_mfma_f32_16x16x32_bf16 v[8:11], v[76:79], a[56:59], v[8:11]\n"
    "v_mfma_f32_16x16x32_bf16 v[12:15], v[76:79], a[60:63], v[12:15]\n"
    "v_mfma_f32_16x16x32_bf16 v[8:11], v[76:79], a[88:91], v[8:11]\n"
    "v_mfma_f32_16x16x32_bf16 v[12:15], v[76:79], a[92:95], v[12:15]\n"
    "v_mfma_f32_16x16x32_bf16 v[8:11], v[76:79], a[120:123], v[8:11]\n"
    "v_mfma_f32_16x16x32_bf16 v[12:15], v[76:79], a[124:127], v[12:15]\n"
    "v_add_u32 v63, 0x20000, v49\n"
    "ds_read_b64_tr_b16 a[128:129], v63\n"
    "v_add_u32 v62, 0x20000, v50\n"
    "ds_read_b64_tr_b16 a[132:133], v62\n"
    "ds_read_b64_tr_b16 a[130:131], v63 offset:4096\n"
    "ds_read_b64_tr_b16 a[134:135], v62 offset:4096\n"
    "ds_read_b64_tr_b16 a[136:137], v63 offset:8192\n"
    "ds_read_b64_tr_b16 a[140:141], v62 offset:8192\n"
    "ds_read_b64_tr_b16 a[138:139], v63 offset:12288\n"
    "ds_read_b64_tr_b16 a[142:143], v62 offset:12288\n"
    "s_waitcnt vmcnt(28) lgkmcnt(0)\n"
    "s_barrier\n"
    "s_add_u32 m0, s54, 131072\n"
    "s_add_u32 s55, %6, 4194304\n"
    "buffer_load_dwordx4 v48, s[44:47], s55 offen lds\n"
    "s_add_u32 m0, s54, 135168\n"
    "s_add_u32 s55, %6, 4227072\n"
    "buffer_load_dwordx4 v48, s[44:47], s55 offen lds\n"
    "s_add_u32 m0, s54, 139264\n"
    "s_add_u32 s55, %6, 4456448\n"
    "buffer_load_dwordx4 v48, s[44:47], s55 offen lds\n"
    "s_add_u32 m0, s54, 143360\n"
    "s_add_u32 s55, %6, 4489216\n"
    "buffer_load_dwordx4 v48, s[44:47], s55 offen lds\n"
    "v_mfma_f32_16x16x32_bf16 v[8:11], v[76:79], a[128:131], v[8:11]\n"
    "v_mfma_f32_16x16x32_bf16 v[12:15], v[76:79], a[132:135], v[12:15]\n"
    "v_mfma_f32_16x16x32_bf16 v[8:11], v[76:79], a[136:139], v[8:11]\n"
    "v_mfma_f32_16x16x32_bf16 v[12:15], v[76:79], a[140:143], v[12:15]\n"
    "ds_read_b64_tr_b16 a[144:145], v32\n"
    "ds_read_b64_tr_b16 a[148:149], v33\n"
    "ds_read_b64_tr_b16 a[176:177], v34\n"
    "ds_read_b64_tr_b16 a[180:181], v35\n"
    "ds_read_b64_tr_b16 a[208:209], v36\n"
    "ds_read_b64_tr_b16 a[212:213], v37\n"
    "ds_read_b64_tr_b16 a[240:241], v38\n"
    "ds_read_b64_tr_b16 a[244:245], v39\n"
    "s_waitcnt vmcnt(28) lgkmcnt(0)\n"
    "s_barrier\n"
    "s_mov_b64 s[44:45], %10\n"
    "s_and_b32 s45, s45, 0xffff\n"
    "s_mov_b32 s46, 0x480000\n"
    "s_mov_b32 s47, 0x20000\n"
    "s_add_u32 m0, s54, 0\n"
    "s_add_u32 s55, %6, 0\n"
    "buffer_load_dwordx4 v48, s[44:47], s55 offen lds\n"
    "s_add_u32 m0, s54, 4096\n"
    "s_add_u32 s55, %6, 1048576\n"
    "buffer_load_dwordx4 v48, s[44:47], s55 offen lds\n"
    "s_add_u32 m0, s54, 8192\n"
    "s_add_u32 s55, %6, 2097152\n"
    "buffer_load_dwordx4 v48, s[44:47], s55 offen lds\n"
    "s_add_u32 m0, s54, 12288\n"
    "s_add_u32 s55, %6, 3145728\n"
    "buffer_load_dwordx4 v48, s[44:47], s55 offen lds\n"
    "ds_read_b64_tr_b16 a[146:147], v32 offset:16384\n"
    "ds_read_b64_tr_b16 a[150:151], v33 offset:16384\n"
    "ds_read_b64_tr_b16 a[178:179], v34 offset:16384\n"
    "ds_read_b64_tr_b16 a[182:183], v35 offset:16384\n"
    "ds_read_b64_tr_b16 a[210:211], v36 offset:16384\n"
    "ds_read_b64_tr_b16 a[214:215], v37 offset:16384\n"
    "ds_read_b64_tr_b16 a[242:243], v38 offset:16384\n"
    "ds_read_b64_tr_b16 a[246:247], v39 offset:16384\n"
    "s_waitcnt vmcnt(28) lgkmcnt(0)\n"
    "s_barrier\n"
    "s_add_u32 m0, s54, 16384\n"
    "s_add_u32 s55, %6, 32768\n"
    "buffer_load_dwordx4 v48, s[44:47], s55 offen lds\n"
    "s_add_u32 m0, s54, 20480\n"
    "s_add_u32 s55, %6, 1081344\n"
    "buffer_load_dwordx4 v48, s[44:47], s55 offen lds\n"
    "s_add_u32 m0, s54, 24576\n"
    "s_add_u32 s55, %6, 2129920\n"
    "buffer_load_dwordx4 v48, s[44:47], s55 offen lds\n"
    "s_add_u32 m0, s54, 28672\n"
    "s_add_u32 s55, %6, 3178496\n"
    "buffer_load_dwordx4 v48, s[44:47], s55 offen lds\n"
    "v_mfma_f32_16x16x32_bf16 v[16:19], v[76:79], a[144:147], 0\n"
    "v_mfma_f32_16x16x32_bf16 v[20:23], v[76:79], a[148:151], 0\n"
    "v_mfma_f32_16x16x32_bf16 v[16:19], v[76:79], a[176:179], v[16:19]\n"
    "v_mfma_f32_16x16x32_bf16 v[20:23], v[76:79], a[180:183], v[20:23]\n"
    "v_mfma_f32_16x16x32_bf16 v[16:19], v[76:79], a[208:211], v[16:19]\n"
    "v_mfma_f32_16x16x32_bf16 v[20:23], v[76:79], a[212:215], v[20:23]\n"
    "v_mfma_f32_16x16x32_bf16 v[16:19], v[76:79], a[240:243], v[16:19]\n"
    "v_mfma_f32_16x16x32_bf16 v[20:23], v[76:79], a[244:247], v[20:23]\n"
    "ds_read_b64_tr_b16 a[152:153], v32 offset:32768\n"
    "ds_read_b64_tr_b16 a[156:157], v33 offset:32768\n"
    "ds_read_b64_tr_b16 a[184:185], v34 offset:32768\n"
    "ds_read_b64_tr_b16 a[188:189], v35 offset:32768\n"
    "ds_read_b64_tr_b16 a[216:217], v36 offset:32768\n"
    "ds_read_b64_tr_b16 a[220:221], v37 offset:32768\n"
    "ds_read_b64_tr_b16 a[248:249], v38 offset:32768\n"
    "ds_read_b64_tr_b16 a[252:253], v39 offset:32768\n"
    "s_waitcnt vmcnt(28) lgkmcnt(0)\n"
    "s_barrier\n"
    "s_add_u32 m0, s54, 32768\n"
    "s_add_u32 s55, %6, 262144\n"
    "buffer_load_dwordx4 v48, s[44:47], s55 offen lds\n"
    "s_add_u32 m0, s54, 36864\n"
    "s_add_u32 s55, %6, 1310720\n"
    "buffer_load_dwordx4 v48, s[44:47], s55 offen lds\n"
    "s_add_u32 m0, s54, 40960\n"
    "s_add_u32 s55, %6, 2359296\n"
    "buffer_load_dwordx4 v48, s[44:47], s55 offen lds\n"
    "s_add_u32 m0, s54, 45056\n"
    "s_add_u32 s55, %6, 3407872\n"
    "buffer_load_dwordx4 v48, s[44:47], s55 offen lds\n"
    "ds_read_b64_tr_b16 a[154:155], v32 offset:49152\n"
    "ds_read_b64_tr_b16 a[158:159], v33 offset:49152\n"
    "ds_read_b64_tr_b16 a[186:187], v34 offset:49152\n"
    "ds_read_b64_tr_b16 a[190:191], v35 offset:49152\n"
    "ds_read_b64_tr_b16 a[218:219], v36 offset:49152\n"
    "ds_read_b64_tr_b16 a[222:223], v37 offset:49152\n"
    "ds_read_b64_tr_b16 a[250:251], v38 offset:49152\n"
    "ds_read_b64_tr_b16 a[254:255], v39 offset:49152\n"
    "s_waitcnt vmcnt(28) lgkmcnt(0)\n"
    "s_barrier\n"
    "s_add_u32 m0, s54, 49152\n"
    "s_add_u32 s55, %6, 294912\n"
    "buffer_load_dwordx4 v48, s[44:47], s55 offen lds\n"
    "s_add_u32 m0, s54, 53248\n"
    "s_add_u32 s55, %6, 1343488\n"
    "buffer_load_dwordx4 v48, s[44:47], s55 offen lds\n"
    "s_add_u32 m0, s54, 57344\n"
    "s_add_u32 s55, %6, 2392064\n"
    "buffer_load_dwordx4 v48, s[44:47], s55 offen lds\n"
    "s_add_u32 m0, s54, 61440\n"
    "s_add_u32 s55, %6, 3440640\n"
    "buffer_load_dwordx4 v48, s[44:47], s55 offen lds\n"
    "v_mfma_f32_16x16x32_bf16 v[16:19], v[76:79], a[152:155], v[16:19]\n"
    "v_mfma_f32_16x16x32_bf16 v[20:23], v[76:79], a[156:159], v[20:23]\n"
    "v_mfma_f32_16x16x32_bf16 v[16:19], v[76:79], a[184:187], v[16:19]\n"
    "v_mfma_f32_16x16x32_bf16 v[20:23], v[76:79], a[188:191], v[20:23]\n"
    "v_mfma_f32_16x16x32_bf16 v[16:19], v[76:79], a[216:219], v[16:19]\n"
    "v_mfma_f32_16x16x32_bf16 v[20:23], v[76:79], a[220:223], v[20:23]\n"
    "v_mfma_f32_16x16x32_bf16 v[16:19], v[76:79], a[248:251], v[16:19]\n"
    "v_mfma_f32_16x16x32_bf16 v[20:23], v[76:79], a[252:255], v[20:23]\n"
    "ds_read_b64_tr_b16 a[160:161], v71\n"
    "ds_read_b64_tr_b16 a[164:165], v70\n"
    "ds_read_b64_tr_b16 a[192:193], v69\n"
    "ds_read_b64_tr_b16 a[196:197], v68\n"
    "ds_read_b64_tr_b16 a[224:225], v67\n"
    "ds_read_b64_tr_b16 a[228:229], v66\n"
    "ds_read_b64_tr_b16 v[84:85], v65\n"
    "ds_read_b64_tr_b16 v[88:89], v64\n"
    "s_waitcnt vmcnt(28) lgkmcnt(0)\n"
    "s_barrier\n"
    "s_add_u32 m0, s54, 65536\n"
    "s_add_u32 s55, %6, 524288\n"
    "buffer_load_dwordx4 v48, s[44:47], s55 offen lds\n"
    "s_add_u32 m0, s54, 69632\n"
    "s_add_u32 s55, %6, 1572864\n"
    "buffer_load_dwordx4 v48, s[44:47], s55 offen lds\n"
    "s_add_u32 m0, s54, 73728\n"
    "s_add_u32 s55, %6, 2621440\n"
    "buffer_load_dwordx4 v48, s[44:47], s55 offen lds\n"
    "s_add_u32 m0, s54, 77824\n"
    "s_add_u32 s55, %6, 3670016\n"
    "buffer_load_dwordx4 v48, s[44:47], s55 offen lds\n"
    "ds_read_b64_tr_b16 a[162:163], v71 offset:16384\n"
    "ds_read_b64_tr_b16 a[166:167], v70 offset:16384\n"
    "ds_read_b64_tr_b16 a[194:195], v69 offset:16384\n"
    "ds_read_b64_tr_b16 a[198:199], v68 offset:16384\n"
    "ds_read_b64_tr_b16 a[226:227], v67 offset:16384\n"
    "ds_read_b64_tr_b16 a[230:231], v66 offset:16384\n"
    "ds_read_b64_tr_b16 v[86:87], v65 offset:16384\n"
    "ds_read_b64_tr_b16 v[90:91], v64 offset:16384\n"
    "s_waitcnt vmcnt(28) lgkmcnt(0)\n"
    "s_barrier\n"
    "s_add_u32 m0, s54, 81920\n"
    "s_add_u32 s55, %6, 557056\n"
    "buffer_load_dwordx4 v48, s[44:47], s55 offen lds\n"
    "s_add_u32 m0, s54, 86016\n"
    "s_add_u32 s55, %6, 1605632\n"
    "buffer_load_dwordx4 v48, s[44:47], s55 offen lds\n"
    "s_add_u32 m0, s54, 90112\n"
    "s_add_u32 s55, %6, 2654208\n"
    "buffer_load_dwordx4 v48, s[44:47], s55 offen lds\n"
    "s_add_u32 m0, s54, 94208\n"
    "s_add_u32 s55, %6, 3702784\n"
    "buffer_load_dwordx4 v48, s[44:47], s55 offen lds\n"
    "v_mfma_f32_16x16x32_bf16 v[16:19], v[76:79], a[160:163], v[16:19]\n"
    "v_mfma_f32_16x16x32_bf16 v[20:23], v[76:79], a[164:167], v[20:23]\n"
    "v_mfma_f32_16x16x32_bf16 v[16:19], v[76:79], a[192:195], v[16:19]\n"
    "v_mfma_f32_16x16x32_bf16 v[20:23], v[76:79], a[196:199], v[20:23]\n"
    "v_mfma_f32_16x16x32_bf16 v[16:19], v[76:79], a[224:227], v[16:19]\n"
    "v_mfma_f32_16x16x32_bf16 v[20:23], v[76:79], a[228:231], v[20:23]\n"
    "v_mfma_f32_16x16x32_bf16 v[16:19], v[76:79], v[84:87], v[16:19]\n"
    "v_mfma_f32_16x16x32_bf16 v[20:23], v[76:79], v[88:91], v[20:23]\n"
    "ds_read_b64_tr_b16 a[168:169], v71 offset:32768\n"
    "ds_read_b64_tr_b16 a[172:173], v70 offset:32768\n"
    "ds_read_b64_tr_b16 a[200:201], v69 offset:32768\n"
    "ds_read_b64_tr_b16 a[204:205], v68 offset:32768\n"
    "ds_read_b64_tr_b16 a[232:233], v67 offset:32768\n"
    "ds_read_b64_tr_b16 a[236:237], v66 offset:32768\n"
    "ds_read_b64_tr_b16 v[92:93], v65 offset:32768\n"
    "ds_read_b64_tr_b16 v[96:97], v64 offset:32768\n"
    "s_waitcnt vmcnt(28) lgkmcnt(0)\n"
    "s_barrier\n"
    "s_add_u32 m0, s54, 98304\n"
    "s_add_u32 s55, %6, 786432\n"
    "buffer_load_dwordx4 v48, s[44:47], s55 offen lds\n"
    "s_add_u32 m0, s54, 102400\n"
    "s_add_u32 s55, %6, 1835008\n"
    "buffer_load_dwordx4 v48, s[44:47], s55 offen lds\n"
    "s_add_u32 m0, s54, 106496\n"
    "s_add_u32 s55, %6, 2883584\n"
    "buffer_load_dwordx4 v48, s[44:47], s55 offen lds\n"
    "s_add_u32 m0, s54, 110592\n"
    "s_add_u32 s55, %6, 3932160\n"
    "buffer_load_dwordx4 v48, s[44:47], s55 offen lds\n"
    "ds_read_b64_tr_b16 a[170:171], v71 offset:49152\n"
    "ds_read_b64_tr_b16 a[174:175], v70 offset:49152\n"
    "ds_read_b64_tr_b16 a[202:203], v69 offset:49152\n"
    "ds_read_b64_tr_b16 a[206:207], v68 offset:49152\n"
    "ds_read_b64_tr_b16 a[234:235], v67 offset:49152\n"
    "ds_read_b64_tr_b16 a[238:239], v66 offset:49152\n"
    "ds_read_b64_tr_b16 v[94:95], v65 offset:49152\n"
    "ds_read_b64_tr_b16 v[98:99], v64 offset:49152\n"
    "s_waitcnt vmcnt(28) lgkmcnt(0)\n"
    "s_barrier\n"
    "s_add_u32 m0, s54, 114688\n"
    "s_add_u32 s55, %6, 819200\n"
    "buffer_load_dwordx4 v48, s[44:47], s55 offen lds\n"
    "s_add_u32 m0, s54, 118784\n"
    "s_add_u32 s55, %6, 1867776\n"
    "buffer_load_dwordx4 v48, s[44:47], s55 offen lds\n"
    "s_add_u32 m0, s54, 122880\n"
    "s_add_u32 s55, %6, 2916352\n"
    "buffer_load_dwordx4 v48, s[44:47], s55 offen lds\n"
    "s_add_u32 m0, s54, 126976\n"
    "s_add_u32 s55, %6, 3964928\n"
    "buffer_load_dwordx4 v48, s[44:47], s55 offen lds\n"
    "v_mfma_f32_16x16x32_bf16 v[16:19], v[76:79], a[168:171], v[16:19]\n"
    "v_mfma_f32_16x16x32_bf16 v[20:23], v[76:79], a[172:175], v[20:23]\n"
    "v_mfma_f32_16x16x32_bf16 v[16:19], v[76:79], a[200:203], v[16:19]\n"
    "v_mfma_f32_16x16x32_bf16 v[20:23], v[76:79], a[204:207], v[20:23]\n"
    "v_mfma_f32_16x16x32_bf16 v[16:19], v[76:79], a[232:235], v[16:19]\n"
    "v_mfma_f32_16x16x32_bf16 v[20:23], v[76:79], a[236:239], v[20:23]\n"
    "v_mfma_f32_16x16x32_bf16 v[16:19], v[76:79], v[92:95], v[16:19]\n"
    "v_mfma_f32_16x16x32_bf16 v[20:23], v[76:79], v[96:99], v[20:23]\n"
    "ds_read_b64_tr_b16 v[100:101], v63\n"
    "ds_read_b64_tr_b16 v[104:105], v62\n"
    "ds_read_b64_tr_b16 v[102:103], v63 offset:4096\n"
    "ds_read_b64_tr_b16 v[106:107], v62 offset:4096\n"
    "ds_read_b64_tr_b16 v[108:109], v63 offset:8192\n"
    "ds_read_b64_tr_b16 v[112:113], v62 offset:8192\n"
    "ds_read_b64_tr_b16 v[110:111], v63 offset:12288\n"
    "ds_read_b64_tr_b16 v[114:115], v62 offset:12288\n"
    "s_waitcnt vmcnt(28) lgkmcnt(0)\n"
    "s_barrier\n"
    "s_add_u32 m0, s54, 131072\n"
    "s_add_u32 s55, %6, 4194304\n"
    "buffer_load_dwordx4 v48, s[44:47], s55 offen lds\n"
    "s_add_u32 m0, s54, 135168\n"
    "s_add_u32 s55, %6, 4227072\n"
    "buffer_load_dwordx4 v48, s[44:47], s55 offen lds\n"
    "s_add_u32 m0, s54, 139264\n"
    "s_add_u32 s55, %6, 4456448\n"
    "buffer_load_dwordx4 v48, s[44:47], s55 offen lds\n"
    "s_add_u32 m0, s54, 143360\n"
    "s_add_u32 s55, %6, 4489216\n"
    "buffer_load_dwordx4 v48, s[44:47], s55 offen lds\n"
    "v_mov_b32 v7, 0x20400\n"
    "global_load_dwordx2 v[80:81], v7, %1 sc1\n"
    "v_mbcnt_lo_u32_b32 v4, -1, 0\n"
    "v_min_u32 v4, 7, v4\n"
    "v_lshlrev_b32 v4, 7, v4\n"
    "v_add_u32 v4, 0x20000, v4\n"
    "global_load_dwordx2 v[82:83], v4, %1 sc1\n"
    "v_mfma_f32_16x16x32_bf16 v[16:19], v[76:79], v[100:103], v[16:19]\n"
    "v_mfma_f32_16x16x32_bf16 v[20:23], v[76:79], v[104:107], v[20:23]\n"
    "v_mfma_f32_16x16x32_bf16 v[16:19], v[76:79], v[108:111], v[16:19]\n"
    "v_mfma_f32_16x16x32_bf16 v[20:23], v[76:79], v[112:115], v[20:23]\n"
    "ds_read_b64_tr_b16 v[116:117], v32\n"
    "ds_read_b64_tr_b16 v[120:121], v33\n"
    "ds_read_b64_tr_b16 v[148:149], v34\n"
    "ds_read_b64_tr_b16 v[152:153], v35\n"
    "ds_read_b64_tr_b16 v[180:181], v36\n"
    "ds_read_b64_tr_b16 v[184:185], v37\n"
    "ds_read_b64_tr_b16 v[212:213], v38\n"
    "ds_read_b64_tr_b16 v[216:217], v39\n"
    "s_waitcnt vmcnt(30) lgkmcnt(0)\n"
    "s_barrier\n"
    "ds_read_b64_tr_b16 v[118:119], v32 offset:16384\n"
    "ds_read_b64_tr_b16 v[122:123], v33 offset:16384\n"
    "ds_read_b64_tr_b16 v[150:151], v34 offset:16384\n"
    "ds_read_b64_tr_b16 v[154:155], v35 offset:16384\n"
    "ds_read_b64_tr_b16 v[182:183], v36 offset:16384\n"
    "ds_read_b64_tr_b16 v[186:187], v37 offset:16384\n"
    "ds_read_b64_tr_b16 v[214:215], v38 offset:16384\n"
    "ds_read_b64_tr_b16 v[218:219], v39 offset:16384\n"
    "s_waitcnt vmcnt(26) lgkmcnt(0)\n"
    "s_barrier\n"
    "v_mfma_f32_16x16x32_bf16 v[24:27], v[76:79], v[116:119], 0\n"
    "v_mfma_f32_16x16x32_bf16 v[28:31], v[76:79], v[120:123], 0\n"
    "v_mfma_f32_16x16x32_bf16 v[24:27], v[76:79], v[148:151], v[24:27]\n"
    "v_mfma_f32_16x16x32_bf16 v[28:31], v[76:79], v[152:155], v[28:31]\n"
    "v_mfma_f32_16x16x32_bf16 v[24:27], v[76:79], v[180:183], v[24:27]\n"
    "v_mfma_f32_16x16x32_bf16 v[28:31], v[76:79], v[184:187], v[28:31]\n"
    "v_mfma_f32_16x16x32_bf16 v[24:27], v[76:79], v[212:215], v[24:27]\n"
    "v_mfma_f32_16x16x32_bf16 v[28:31], v[76:79], v[216:219], v[28:31]\n"
    "ds_read_b64_tr_b16 v[124:125], v32 offset:32768\n"
    "ds_read_b64_tr_b16 v[128:129], v33 offset:32768\n"
    "ds_read_b64_tr_b16 v[156:157], v34 offset:32768\n"
    "ds_read_b64_tr_b16 v[160:161], v35 offset:32768\n"
    "ds_read_b64_tr_b16 v[188:189], v36 offset:32768\n"
    "ds_read_b64_tr_b16 v[192:193], v37 offset:32768\n"
    "ds_read_b64_tr_b16 v[220:221], v38 offset:32768\n"
    "ds_read_b64_tr_b16 v[224:225], v39 offset:32768\n"
    "s_waitcnt vmcnt(22) lgkmcnt(0)\n"
    "s_barrier\n"
    "ds_read_b64_tr_b16 v[126:127], v32 offset:49152\n"
    "ds_read_b64_tr_b16 v[130:131], v33 offset:49152\n"
    "ds_read_b64_tr_b16 v[158:159], v34 offset:49152\n"
    "ds_read_b64_tr_b16 v[162:163], v35 offset:49152\n"
    "ds_read_b64_tr_b16 v[190:191], v36 offset:49152\n"
    "ds_read_b64_tr_b16 v[194:195], v37 offset:49152\n"
    "ds_read_b64_tr_b16 v[222:223], v38 offset:49152\n"
    "ds_read_b64_tr_b16 v[226:227], v39 offset:49152\n"
    "s_waitcnt vmcnt(18) lgkmcnt(0)\n"
    "s_barrier\n"
    "v_mfma_f32_16x16x32_bf16 v[24:27], v[76:79], v[124:127], v[24:27]\n"
    "v_mfma_f32_16x16x32_bf16 v[28:31], v[76:79], v[128:131], v[28:31]\n"
    "v_mfma_f32_16x16x32_bf16 v[24:27], v[76:79], v[156:159], v[24:27]\n"
    "v_mfma_f32_16x16x32_bf16 v[28:31], v[76:79], v[160:163], v[28:31]\n"
    "v_mfma_f32_16x16x32_bf16 v[24:27], v[76:79], v[188:191], v[24:27]\n"
    "v_mfma_f32_16x16x32_bf16 v[28:31], v[76:79], v[192:195], v[28:31]\n"
    "v_mfma_f32_16x16x32_bf16 v[24:27], v[76:79], v[220:223], v[24:27]\n"
    "v_mfma_f32_16x16x32_bf16 v[28:31], v[76:79], v[224:227], v[28:31]\n"
    "ds_read_b64_tr_b16 v[132:133], v71\n"
    "ds_read_b64_tr_b16 v[136:137], v70\n"
    "ds_read_b64_tr_b16 v[164:165], v69\n"
    "ds_read_b64_tr_b16 v[168:169], v68\n"
    "ds_read_b64_tr_b16 v[196:197], v67\n"
    "ds_read_b64_tr_b16 v[200:201], v66\n"
    "ds_read_b64_tr_b16 v[228:229], v65\n"
    "ds_read_b64_tr_b16 v[232:233], v64\n"
    "s_waitcnt vmcnt(14) lgkmcnt(0)\n"
    "s_barrier\n"
    "ds_read_b64_tr_b16 v[134:135], v71 offset:16384\n"
    "ds_read_b64_tr_b16 v[138:139], v70 offset:16384\n"
    "ds_read_b64_tr_b16 v[166:167], v69 offset:16384\n"
    "ds_read_b64_tr_b16 v[170:171], v68 offset:16384\n"
    "ds_read_b64_tr_b16 v[198:199], v67 offset:16384\n"
    "ds_read_b64_tr_b16 v[202:203], v66 offset:16384\n"
    "ds_read_b64_tr_b16 v[230:231], v65 offset:16384\n"
    "ds_read_b64_tr_b16 v[234:235], v64 offset:16384\n"
    "s_waitcnt vmcnt(10) lgkmcnt(0)\n"
    "s_barrier\n"
    "v_mfma_f32_16x16x32_bf16 v[24:27], v[76:79], v[132:135], v[24:27]\n"
    "v_mfma_f32_16x16x32_bf16 v[28:31], v[76:79], v[136:139], v[28:31]\n"
    "v_mfma_f32_16x16x32_bf16 v[24:27], v[76:79], v[164:167], v[24:27]\n"
    "v_mfma_f32_16x16x32_bf16 v[28:31], v[76:79], v[168:171], v[28:31]\n"
    "v_mfma_f32_16x16x32_bf16 v[24:27], v[76:79], v[196:199], v[24:27]\n"
    "v_mfma_f32_16x16x32_bf16 v[28:31], v[76:79], v[200:203], v[28:31]\n"
    "v_mfma_f32_16x16x32_bf16 v[24:27], v[76:79], v[228:231], v[24:27]\n"
    "v_mfma_f32_16x16x32_bf16 v[28:31], v[76:79], v[232:235], v[28:31]\n"
    "ds_read_b64_tr_b16 v[140:141], v71 offset:32768\n"
    "ds_read_b64_tr_b16 v[144:145], v70 offset:32768\n"
    "ds_read_b64_tr_b16 v[172:173], v69 offset:32768\n"
    "ds_read_b64_tr_b16 v[176:177], v68 offset:32768\n"
    "ds_read_b64_tr_b16 v[204:205], v67 offset:32768\n"
    "ds_read_b64_tr_b16 v[208:209], v66 offset:32768\n"
    "ds_read_b64_tr_b16 v[236:237], v65 offset:32768\n"
    "ds_read_b64_tr_b16 v[240:241], v64 offset:32768\n"
    "s_waitcnt vmcnt(6) lgkmcnt(0)\n"
    "s_barrier\n"
    "ds_read_b64_tr_b16 v[142:143], v71 offset:49152\n"
    "ds_read_b64_tr_b16 v[146:147], v70 offset:49152\n"
    "ds_read_b64_tr_b16 v[174:175], v69 offset:49152\n"
    "ds_read_b64_tr_b16 v[178:179], v68 offset:49152\n"
    "ds_read_b64_tr_b16 v[206:207], v67 offset:49152\n"
    "ds_read_b64_tr_b16 v[210:211], v66 offset:49152\n"
    "ds_read_b64_tr_b16 v[238:239], v65 offset:49152\n"
    "ds_read_b64_tr_b16 v[242:243], v64 offset:49152\n"
    "s_waitcnt vmcnt(2) lgkmcnt(0)\n"
    "s_barrier\n"
    "v_mfma_f32_16x16x32_bf16 v[24:27], v[76:79], v[140:143], v[24:27]\n"
    "v_mfma_f32_16x16x32_bf16 v[28:31], v[76:79], v[144:147], v[28:31]\n"
    "v_mfma_f32_16x16x32_bf16 v[24:27], v[76:79], v[172:175], v[24:27]\n"
    "v_mfma_f32_16x16x32_bf16 v[28:31], v[76:79], v[176:179], v[28:31]\n"
    "v_mfma_f32_16x16x32_bf16 v[24:27], v[76:79], v[204:207], v[24:27]\n"
    "v_mfma_f32_16x16x32_bf16 v[28:31], v[76:79], v[208:211], v[28:31]\n"
    "v_mfma_f32_16x16x32_bf16 v[24:27], v[76:79], v[236:239], v[24:27]\n"
    "v_mfma_f32_16x16x32_bf16 v[28:31], v[76:79], v[240:243], v[28:31]\n"
    "ds_read_b64_tr_b16 v[244:245], v63\n"
    "ds_read_b64_tr_b16 v[248:249], v62\n"
    "ds_read_b64_tr_b16 v[246:247], v63 offset:4096\n"
    "ds_read_b64_tr_b16 v[250:251], v62 offset:4096\n"
    "ds_read_b64_tr_b16 v[252:253], v63 offset:8192\n"
    "ds_read_b64_tr_b16 v[72:73], v62 offset:8192\n"
    "ds_read_b64_tr_b16 v[254:255], v63 offset:12288\n"
    "ds_read_b64_tr_b16 v[74:75], v62 offset:12288\n"
    "s_waitcnt lgkmcnt(0)\n"
    "s_barrier\n"
    "v_mfma_f32_16x16x32_bf16 v[24:27], v[76:79], v[244:247], v[24:27]\n"
    "v_mfma_f32_16x16x32_bf16 v[28:31], v[76:79], v[248:251], v[28:31]\n"
    "v_mfma_f32_16x16x32_bf16 v[24:27], v[76:79], v[252:255], v[24:27]\n"
    "v_mfma_f32_16x16x32_bf16 v[28:31], v[76:79], v[72:75], v[28:31]\n"
    "ds_write_b128 v2, v[72:75] offset:20480\n"
    "v_cvt_pk_bf16_f32 v9, 0, v8\n"
    "v_sub_f32 v8, v8, v9\n"
    "v_cvt_pk_bf16_f32 v10, 0, v8\n"
    "v_sub_f32 v8, v8, v10\n"
    "v_cvt_pk_bf16_f32 v9, v9, v10\n"
    "v_cvt_pk_bf16_f32 v10, v8, 0\n"
    "s_mov_b64 vcc, 0xffff\n"
    "v_cndmask_b32 v32, 0, v9, vcc\n"
    "v_cndmask_b32 v33, 0, v10, vcc\n"
    "v_cvt_pk_bf16_f32 v13, 0, v12\n"
    "v_sub_f32 v12, v12, v13\n"
    "v_cvt_pk_bf16_f32 v14, 0, v12\n"
    "v_sub_f32 v12, v12, v14\n"
    "v_cvt_pk_bf16_f32 v13, v13, v14\n"
    "v_cvt_pk_bf16_f32 v14, v12, 0\n"
    "s_mov_b64 vcc, 0xffff\n"
    "v_cndmask_b32 v34, 0, v13, vcc\n"
    "v_cndmask_b32 v35, 0, v14, vcc\n"
    "ds_write_b128 v2, v[32:35] offset:8192\n"
    "v_cvt_pk_bf16_f32 v17, 0, v16\n"
    "v_sub_f32 v16, v16, v17\n"
    "v_cvt_pk_bf16_f32 v18, 0, v16\n"
    "v_sub_f32 v16, v16, v18\n"
    "v_cvt_pk_bf16_f32 v17, v17, v18\n"
    "v_cvt_pk_bf16_f32 v18, v16, 0\n"
    "s_mov_b64 vcc, 0xffff\n"
    "v_cndmask_b32 v36, 0, v17, vcc\n"
    "v_cndmask_b32 v37, 0, v18, vcc\n"
    "v_cvt_pk_bf16_f32 v21, 0, v20\n"
    "v_sub_f32 v20, v20, v21\n"
    "v_cvt_pk_bf16_f32 v22, 0, v20\n"
    "v_sub_f32 v20, v20, v22\n"
    "v_cvt_pk_bf16_f32 v21, v21, v22\n"
    "v_cvt_pk_bf16_f32 v22, v20, 0\n"
    "s_mov_b64 vcc, 0xffff\n"
    "v_cndmask_b32 v38, 0, v21, vcc\n"
    "v_cndmask_b32 v39, 0, v22, vcc\n"
    "ds_write_b128 v2, v[36:39] offset:12288\n"
    "v_cvt_pk_bf16_f32 v25, 0, v24\n"
    "v_sub_f32 v24, v24, v25\n"
    "v_cvt_pk_bf16_f32 v26, 0, v24\n"
    "v_sub_f32 v24, v24, v26\n"
    "v_cvt_pk_bf16_f32 v25, v25, v26\n"
    "v_cvt_pk_bf16_f32 v26, v24, 0\n"
    "s_mov_b64 vcc, 0xffff\n"
    "v_cndmask_b32 v40, 0, v25, vcc\n"
    "v_cndmask_b32 v41, 0, v26, vcc\n"
    "v_cvt_pk_bf16_f32 v29, 0, v28\n"
    "v_sub_f32 v28, v28, v29\n"
    "v_cvt_pk_bf16_f32 v30, 0, v28\n"
    "v_sub_f32 v28, v28, v30\n"
    "v_cvt_pk_bf16_f32 v29, v29, v30\n"
    "v_cvt_pk_bf16_f32 v30, v28, 0\n"
    "s_mov_b64 vcc, 0xffff\n"
    "v_cndmask_b32 v42, 0, v29, vcc\n"
    "v_cndmask_b32 v43, 0, v30, vcc\n"
    "ds_write_b128 v2, v[40:43] offset:16384\n"
    "s_waitcnt vmcnt(0)\n"
    "v_add_u32 v80, 1, v80\n"
    "v_add_u32 v81, 1, v81\n"
    "s_mov_b32 s52, 1048576\n"
    ".Lhgo_a%=:\n"
    "v_cmp_ne_u64 vcc, v[80:81], v[82:83]\n"
    "s_cbranch_vccz .Lhgo_x%=\n"
    "global_load_dwordx2 v[82:83], v4, %1 sc1\n"
    "s_sub_u32 s52, s52, 1\n"
    "s_waitcnt vmcnt(0)\n"
    "s_cbranch_scc0 .Lhgo_a%=\n"
    ".Lhgo_x%=:\n"
    "v_mbcnt_lo_u32_b32 v80, -1, 0\n"
    "v_mbcnt_hi_u32_b32 v80, -1, v80\n"
    "v_min_u32 v4, 5, v80\n"
    "v_lshlrev_b32 v4, 7, v4\n"
    "v_mov_b32 v7, 0x26c00\n"
    "ds_read_b128 v[80:83], v7\n"
    "s_waitcnt lgkmcnt(0)\n"
    "v_readfirstlane_b32 s49, v81\n"
    "v_add_u32 v80, v80, v4\n"
    "s_branch .Lhchk_%=\n"
    ".Lhgo_%=:\n"
    "s_waitcnt vmcnt(2)\n"
    "v_mfma_f32_16x16x32_bf16 v[68:71], v[48:51], v[48:51], 0\n"
    "v_add_u32 v7, 32, v7\n"
    "ds_read_b128 v[80:83], v7\n"
    "v_mfma_f32_16x16x32_bf16 v[68:71], v[52:55], v[52:55], v[68:71]\n"
    "ds_write_b128 v1, v[48:51]\n"
    "ds_write_b128 v1, v[52:55] offset:1024\n"
    "v_mfma_f32_16x16x32_bf16 v[68:71], v[56:59], v[56:59], v[68:71]\n"
    "ds_write_b128 v1, v[56:59] offset:2048\n"
    "ds_write_b128 v1, v[60:63] offset:3072\n"
    "v_mfma_f32_16x16x32_bf16 v[68:71], v[60:63], v[60:63], v[68:71]\n"
    "v_mul_f32 v24, v24, v76\n"
    "v_mul_f32 v25, v25, v77\n"
    "v_mfma_f32_16x16x32_bf16 v[68:71], v[64:67], v[64:67], v[68:71]\n"
    "v_mul_f32 v26, v26, v78\n"
    "v_mul_f32 v27, v27, v79\n"
    "v_mfma_f32_16x16x32_bf16 v[8:11], a[0:3], v[48:51], 0\n"
    "v_mul_f32 v28, v28, v76\n"
    "v_mul_f32 v29, v29, v77\n"
    "v_mfma_f32_16x16x32_bf16 v[12:15], a[4:7], v[48:51], 0\n"
    "v_mul_f32 v30, v30, v78\n"
    "v_mul_f32 v31, v31, v79\n"
    "v_mfma_f32_16x16x32_bf16 v[16:19], a[144:147], v[48:51], 0\n"
    "v_mov_b64 v[76:77], s[86:87]\n"
    "v_mov_b64 v[78:79], s[86:87]\n"
    "v_mfma_f32_16x16x32_bf16 v[20:23], a[148:151], v[48:51], 0\n"
    "ds_write_b128 v1, v[64:67] offset:4096\n"
    "v_cvt_pk_bf16_f32 v24, v24, v25\n"
    "v_mfma_f32_16x16x32_bf16 v[72:75], v[76:79], v[48:51], 0\n"
    "v_cvt_pk_bf16_f32 v25, v26, v27\n"
    "v_cvt_pk_bf16_f32 v26, v28, v29\n"
    "v_mfma_f32_16x16x32_bf16 v[72:75], v[76:79], v[52:55], v[72:75]\n"
    "v_cvt_pk_bf16_f32 v27, v30, v31\n"
    "buffer_store_dwordx4 v[24:27], v2, s[40:43], s64 offen sc1\n"
    "v_mfma_f32_16x16x32_bf16 v[72:75], v[76:79], v[56:59], v[72:75]\n"
    "ds_write_b128 v1, v[68:71] offset:5120\n"
    "s_waitcnt lgkmcnt(6)\n"
    "v_readfirstlane_b32 s49, v81\n"
    "v_mfma_f32_16x16x32_bf16 v[72:75], v[76:79], v[60:63], v[72:75]\n"
    "v_readfirstlane_b32 s62, v82\n"
    "v_add_u32 v80, v80, v4\n"
    "v_mfma_f32_16x16x32_bf16 v[72:75], v[76:79], v[64:67], v[72:75]\n"
    ".Lhtop_%=:\n"
    "v_mfma_f32_16x16x32_bf16 v[24:27], v[48:51], v[116:119], 0\n"
    "global_load_dword v81, v80, %1 sc1\n"
    "s_add_u32 s63, s62, 0x2000000\n"
    "v_mfma_f32_16x16x32_bf16 v[28:31], v[48:51], v[120:123], 0\n"
    "s_add_u32 s64, s62, 0x4000000\n"
    "v_mfma_f32_16x16x32_bf16 v[8:11], a[8:11], v[52:55], v[8:11]\n"
    "ds_write_b128 v1, v[72:75] offset:6144\n"
    "v_xor_b32 v0, 0x2000, v1\n"
    "v_mfma_f32_16x16x32_bf16 v[12:15], a[12:15], v[52:55], v[12:15]\n"
    "v_mfma_f32_16x16x32_bf16 v[16:19], a[152:155], v[52:55], v[16:19]\n"
    "v_mfma_f32_16x16x32_bf16 v[20:23], a[156:159], v[52:55], v[20:23]\n"
    "v_mfma_f32_16x16x32_bf16 v[24:27], v[52:55], v[124:127], v[24:27]\n"
    "s_waitcnt lgkmcnt(0)\n"
    "s_barrier\n"
    "v_mfma_f32_16x16x32_bf16 v[28:31], v[52:55], v[128:131], v[28:31]\n"
    "ds_read_b128 v[32:35], v0\n"
    "ds_read_b128 v[36:39], v0 offset:1024\n"
    "v_mfma_f32_16x16x32_bf16 v[8:11], a[16:19], v[56:59], v[8:11]\n"
    "ds_read2st64_b32 v[68:69], v6 offset0:20 offset1:52\n"
    "ds_read2st64_b32 v[70:71], v6 offset0:84 offset1:116\n"
    "v_mfma_f32_16x16x32_bf16 v[12:15], a[20:23], v[56:59], v[12:15]\n"
    "ds_read2st64_b32 v[74:75], v6 offset0:88 offset1:120\n"
    "ds_read2st64_b32 v[72:73], v6 offset0:24 offset1:56\n"
    "v_mfma_f32_16x16x32_bf16 v[16:19], a[160:163], v[56:59], v[16:19]\n"
    "v_mov_b64 v[64:65], 0\n"
    "v_mfma_f32_16x16x32_bf16 v[20:23], a[164:167], v[56:59], v[20:23]\n"
    "v_mfma_f32_16x16x32_bf16 v[24:27], v[56:59], v[132:135], v[24:27]\n"
    "v_mfma_f32_16x16x32_bf16 v[28:31], v[56:59], v[136:139], v[28:31]\n"
    "ds_read_b128 v[48:51], v2 offset:8192\n"
    "ds_read_b128 v[52:55], v2 offset:12288\n"
    "v_mfma_f32_16x16x32_bf16 v[8:11], a[24:27], v[60:63], v[8:11]\n"
    "ds_read_b128 v[56:59], v2 offset:16384\n"
    "ds_read_b128 v[44:47], v0 offset:3072\n"
    "v_mfma_f32_16x16x32_bf16 v[12:15], a[28:31], v[60:63], v[12:15]\n"
    "ds_read_b128 v[40:43], v0 offset:2048\n"
    "v_mfma_f32_16x16x32_bf16 v[16:19], a[168:171], v[60:63], v[16:19]\n"
    "s_waitcnt lgkmcnt(5)\n"
    "v_add_f32 v72, v72, v73\n"
    "v_add_f32 v74, v74, v75\n"
    "v_mfma_f32_16x16x32_bf16 v[20:23], a[172:175], v[60:63], v[20:23]\n"
    "v_add_f32 v68, v68, v69\n"
    "v_add_f32 v70, v70, v71\n"
    "v_mfma_f32_16x16x32_bf16 v[24:27], v[60:63], v[140:143], v[24:27]\n"
    "v_add_f32 v72, v72, v74\n"
    "v_add_f32 v70, v68, v70\n"
    "v_mfma_f32_16x16x32_bf16 v[28:31], v[60:63], v[144:147], v[28:31]\n"
    "v_mul_f32 v72, 0xbae38e39, v72\n"
    "v_mul_f32 v70, 0x3ae38e39, v70\n"
    "v_mfma_f32_16x16x32_bf16 v[8:11], a[32:35], v[32:35], v[8:11]\n"
    "v_cvt_pk_bf16_f32 v62, v72, v72\n"
    "v_fma_f32 v70, -v72, v72, v70\n"
    "v_mfma_f32_16x16x32_bf16 v[12:15], a[36:39], v[32:35], v[12:15]\n"
    "v_cvt_pk_bf16_f32 v63, v72, v72\n"
    "v_max_f32 v70, 0, v70\n"
    "v_mfma_f32_16x16x32_bf16 v[16:19], a[176:179], v[32:35], v[16:19]\n"
    "v_cvt_pk_bf16_f32 v70, 0, v70\n"
    "v_add_f32 v70, 0x3727c5ac, v70\n"
    "v_mfma_f32_16x16x32_bf16 v[20:23], a[180:183], v[32:35], v[20:23]\n"
    "v_cvt_pk_bf16_f32 v70, 0, v70\n"
    "v_sqrt_f32 v70, v70\n"
    "v_mfma_f32_16x16x32_bf16 v[24:27], v[32:35], v[148:151], v[24:27]\n"
    "v_cvt_pk_bf16_f32 v70, 0, v70\n"
    "v_mfma_f32_16x16x32_bf16 v[28:31], v[32:35], v[152:155], v[28:31]\n"
    "v_rcp_f32 v70, v70\n"
    "v_mfma_f32_16x16x32_bf16 v[8:11], a[40:43], v[36:39], v[8:11]\n"
    "v_mov_b64 v[60:61], 0\n"
    "v_mfma_f32_16x16x32_bf16 v[12:15], a[44:47], v[36:39], v[12:15]\n"
    "ds_write_b32 v5, v70\n"
    "v_lshrrev_b32 v71, 4, v2\n"
    "v_mfma_f32_16x16x32_bf16 v[16:19], a[184:187], v[36:39], v[16:19]\n"
    "v_and_b32 v71, 48, v71\n"
    "ds_read_b128 v[76:79], v71 offset:24576\n"
    "v_mfma_f32_16x16x32_bf16 v[20:23], a[188:191], v[36:39], v[20:23]\n"
    "ds_read_b128 v[72:75], v2 offset:20480\n"
    "v_mfma_f32_16x16x32_bf16 v[24:27], v[36:39], v[156:159], v[24:27]\n"
    "v_xor_b32 v0, 0x4000, v1\n"
    "v_mfma_f32_16x16x32_bf16 v[28:31], v[36:39], v[160:163], v[28:31]\n"
    "s_waitcnt lgkmcnt(3)\n"
    "v_mfma_f32_16x16x32_bf16 v[8:11], a[48:51], v[40:43], v[8:11]\n"
    "ds_read_b128 v[36:39], v0 offset:1024\n"
    "ds_read_b128 v[32:35], v0\n"
    "v_mfma_f32_16x16x32_bf16 v[12:15], a[52:55], v[40:43], v[12:15]\n"
    "v_mfma_f32_16x16x32_bf16 v[16:19], a[192:195], v[40:43], v[16:19]\n"
    "v_mfma_f32_16x16x32_bf16 v[20:23], a[196:199], v[40:43], v[20:23]\n"
    "v_mfma_f32_16x16x32_bf16 v[24:27], v[40:43], v[164:167], v[24:27]\n"
    "v_mfma_f32_16x16x32_bf16 v[28:31], v[40:43], v[168:171], v[28:31]\n"
    "v_mfma_f32_16x16x32_bf16 v[8:11], v[48:51], v[62:65], v[8:11]\n"
    "v_mfma_f32_16x16x32_bf16 v[12:15], v[48:51], v[60:63], v[12:15]\n"
    "v_mfma_f32_16x16x32_bf16 v[16:19], v[52:55], v[62:65], v[16:19]\n"
    "s_waitcnt vmcnt(0)\n"
    "v_mov_b32 v82, 1\n"
    "v_mfma_f32_16x16x32_bf16 v[20:23], v[52:55], v[60:63], v[20:23]\n"
    "s_mov_b64 exec, s[56:57]\n"
    "ds_add_rtn_u32 v82, v7, v82 offset:16\n"
    "s_mov_b64 exec, -1\n"
    "v_mfma_f32_16x16x32_bf16 v[24:27], v[62:65], v[56:59], v[24:27]\n"
    "v_cmp_eq_u32 vcc, 0, v81\n"
    "s_cmp_eq_u64 vcc, 0\n"
    "v_mfma_f32_16x16x32_bf16 v[28:31], v[60:63], v[56:59], v[28:31]\n"
    "s_cselect_b32 s94, s42, 0\n"
    "s_cselect_b32 s90, s96, 0\n"
    "v_mfma_f32_16x16x32_bf16 v[8:11], a[56:59], v[44:47], v[8:11]\n"
    "global_load_dword v81, v80, %1 sc1\n"
    "v_mfma_f32_16x16x32_bf16 v[12:15], a[60:63], v[44:47], v[12:15]\n"
    "buffer_load_dwordx4 v[48:51], v3, s[92:95], s49 offen sc1\n"
    "v_mfma_f32_16x16x32_bf16 v[16:19], a[200:203], v[44:47], v[16:19]\n"
    "buffer_load_dwordx4 v[52:55], v3, s[92:95], s49 offen offset:1024 sc1\n"
    "v_mfma_f32_16x16x32_bf16 v[20:23], a[204:207], v[44:47], v[20:23]\n"
    "s_waitcnt lgkmcnt(0)\n"
    "v_readfirstlane_b32 s50, v82\n"
    "v_mfma_f32_16x16x32_bf16 v[24:27], v[44:47], v[172:175], v[24:27]\n"
    "buffer_load_dwordx4 v[56:59], v3, s[92:95], s49 offen offset:2048 sc1\n"
    "s_cmp_eq_u32 s50, 3\n"
    "v_mfma_f32_16x16x32_bf16 v[28:31], v[44:47], v[176:179], v[28:31]\n"
    "v_mfma_f32_16x16x32_bf16 v[8:11], a[64:67], v[32:35], v[8:11]\n"
    "ds_read_b128 v[44:47], v0 offset:3072\n"
    "ds_read_b128 v[40:43], v0 offset:2048\n"
    "v_mfma_f32_16x16x32_bf16 v[12:15], a[68:71], v[32:35], v[12:15]\n"
    "s_cselect_b64 exec, 1, 0\n"
    "global_store_dword v83, v82, %1 sc1\n"
    "s_mov_b64 exec, -1\n"
    "v_mfma_f32_16x16x32_bf16 v[16:19], a[208:211], v[32:35], v[16:19]\n"
    "buffer_load_dwordx4 v[60:63], v3, s[92:95], s49 offen offset:3072 sc1\n"
    "v_mfma_f32_16x16x32_bf16 v[20:23], a[212:215], v[32:35], v[20:23]\n"
    "buffer_load_dwordx4 v[64:67], v2, s[88:91], s49 offen sc1\n"
    "v_mfma_f32_16x16x32_bf16 v[24:27], v[32:35], v[180:183], v[24:27]\n"
    "v_mfma_f32_16x16x32_bf16 v[28:31], v[32:35], v[184:187], v[28:31]\n"
    "v_mfma_f32_16x16x32_bf16 v[8:11], a[72:75], v[36:39], v[8:11]\n"
    "v_mfma_f32_16x16x32_bf16 v[12:15], a[76:79], v[36:39], v[12:15]\n"
    "v_mfma_f32_16x16x32_bf16 v[16:19], a[216:219], v[36:39], v[16:19]\n"
    "v_mfma_f32_16x16x32_bf16 v[20:23], a[220:223], v[36:39], v[20:23]\n"
    "v_mfma_f32_16x16x32_bf16 v[24:27], v[36:39], v[188:191], v[24:27]\n"
    "v_xor_b32 v0, 0x6000, v1\n"
    "v_mfma_f32_16x16x32_bf16 v[28:31], v[36:39], v[192:195], v[28:31]\n"
    "s_waitcnt lgkmcnt(0)\n"
    "v_mfma_f32_16x16x32_bf16 v[8:11], a[80:83], v[40:43], v[8:11]\n"
    "ds_read_b128 v[36:39], v0 offset:1024\n"
    "ds_read_b128 v[32:35], v0\n"
    "v_mfma_f32_16x16x32_bf16 v[12:15], a[84:87], v[40:43], v[12:15]\n"
    "v_mfma_f32_16x16x32_bf16 v[16:19], a[224:227], v[40:43], v[16:19]\n"
    "v_mfma_f32_16x16x32_bf16 v[20:23], a[228:231], v[40:43], v[20:23]\n"
    "v_mfma_f32_16x16x32_bf16 v[24:27], v[40:43], v[196:199], v[24:27]\n"
    "v_mfma_f32_16x16x32_bf16 v[28:31], v[40:43], v[200:203], v[28:31]\n"
    "v_mfma_f32_16x16x32_bf16 v[8:11], a[88:91], v[44:47], v[8:11]\n"
    "v_mfma_f32_16x16x32_bf16 v[12:15], a[92:95], v[44:47], v[12:15]\n"
    "v_mfma_f32_16x16x32_bf16 v[16:19], a[232:235], v[44:47], v[16:19]\n"
    "v_mfma_f32_16x16x32_bf16 v[20:23], a[236:239], v[44:47], v[20:23]\n"
    "v_mfma_f32_16x16x32_bf16 v[24:27], v[44:47], v[204:207], v[24:27]\n"
    "v_mfma_f32_16x16x32_bf16 v[28:31], v[44:47], v[208:211], v[28:31]\n"
    "s_waitcnt lgkmcnt(0)\n"
    "v_mfma_f32_16x16x32_bf16 v[8:11], a[96:99], v[32:35], v[8:11]\n"
    "ds_read_b128 v[44:47], v0 offset:3072\n"
    "ds_read_b128 v[40:43], v0 offset:2048\n"
    "v_mfma_f32_16x16x32_bf16 v[12:15], a[100:103], v[32:35], v[12:15]\n"
    "v_mfma_f32_16x16x32_bf16 v[16:19], a[240:243], v[32:35], v[16:19]\n"
    "s_cmp_eq_u32 s94, 0\n"
    "s_cbranch_scc1 .Lhk1_%=\n"
    ".Lhk1_x%=:\n"
    "v_mfma_f32_16x16x32_bf16 v[20:23], a[244:247], v[32:35], v[20:23]\n"
    "v_mfma_f32_16x16x32_bf16 v[24:27], v[32:35], v[212:215], v[24:27]\n"
    "v_and_b32 v0, 0xffff9fff, v1\n"
    "v_mfma_f32_16x16x32_bf16 v[28:31], v[32:35], v[216:219], v[28:31]\n"
    "ds_read_b128 v[32:35], v0 offset:4096\n"
    "v_mfma_f32_16x16x32_bf16 v[8:11], a[104:107], v[36:39], v[8:11]\n"
    "v_mfma_f32_16x16x32_bf16 v[12:15], a[108:111], v[36:39], v[12:15]\n"
    "v_mfma_f32_16x16x32_bf16 v[16:19], a[248:251], v[36:39], v[16:19]\n"
    "v_mfma_f32_16x16x32_bf16 v[20:23], a[252:255], v[36:39], v[20:23]\n"
    "v_mfma_f32_16x16x32_bf16 v[24:27], v[36:39], v[220:223], v[24:27]\n"
    "v_mfma_f32_16x16x32_bf16 v[28:31], v[36:39], v[224:227], v[28:31]\n"
    "ds_read_b128 v[36:39], v0 offset:12288\n"
    "s_waitcnt lgkmcnt(2)\n"
    "v_mfma_f32_16x16x32_bf16 v[8:11], a[112:115], v[40:43], v[8:11]\n"
    "v_mfma_f32_16x16x32_bf16 v[12:15], a[116:119], v[40:43], v[12:15]\n"
    "v_xor_b32 v1, 0x8000, v1\n"
    "v_xor_b32 v6, 0x8000, v6\n"
    "v_mfma_f32_16x16x32_bf16 v[8:11], a[120:123], v[44:47], v[8:11]\n"
    "v_mfma_f32_16x16x32_bf16 v[12:15], a[124:127], v[44:47], v[12:15]\n"
    "s_waitcnt lgkmcnt(1)\n"
    "v_mfma_f32_16x16x32_bf16 v[8:11], a[128:131], v[32:35], v[8:11]\n"
    "v_mfma_f32_16x16x32_bf16 v[12:15], a[132:135], v[32:35], v[12:15]\n"
    "s_waitcnt lgkmcnt(0)\n"
    "v_mfma_f32_16x16x32_bf16 v[8:11], a[136:139], v[36:39], v[8:11]\n"
    "v_mfma_f32_16x16x32_bf16 v[12:15], a[140:143], v[36:39], v[12:15]\n"
    "v_mfma_f32_16x16x32_bf16 v[16:19], v[84:87], v[40:43], v[16:19]\n"
    "v_mfma_f32_16x16x32_bf16 v[20:23], v[88:91], v[40:43], v[20:23]\n"
    "v_mul_f32 v8, v8, v70\n"
    "v_mul_f32 v12, v12, v70\n"
    "v_mfma_f32_16x16x32_bf16 v[16:19], v[92:95], v[44:47], v[16:19]\n"
    "v_cvt_pk_bf16_f32 v8, v8, v12\n"
    "v_mul_f32 v9, v9, v70\n"
    "v_mfma_f32_16x16x32_bf16 v[20:23], v[96:99], v[44:47], v[20:23]\n"
    "v_mul_f32 v13, v13, v70\n"
    "v_cvt_pk_bf16_f32 v9, v9, v13\n"
    "v_mfma_f32_16x16x32_bf16 v[16:19], v[100:103], v[32:35], v[16:19]\n"
    "v_mul_f32 v10, v10, v70\n"
    "v_mul_f32 v14, v14, v70\n"
    "v_mfma_f32_16x16x32_bf16 v[20:23], v[104:107], v[32:35], v[20:23]\n"
    "v_cvt_pk_bf16_f32 v10, v10, v14\n"
    "v_mul_f32 v11, v11, v70\n"
    "v_mfma_f32_16x16x32_bf16 v[16:19], v[108:111], v[36:39], v[16:19]\n"
    "v_mul_f32 v15, v15, v70\n"
    "v_cvt_pk_bf16_f32 v11, v11, v15\n"
    "v_mfma_f32_16x16x32_bf16 v[20:23], v[112:115], v[36:39], v[20:23]\n"
    "buffer_store_dwordx4 v[8:11], v2, s[40:43], s62 offen sc1\n"
    "v_mfma_f32_16x16x32_bf16 v[24:27], v[40:43], v[228:231], v[24:27]\n"
    "v_mfma_f32_16x16x32_bf16 v[28:31], v[40:43], v[232:235], v[28:31]\n"
    "v_mul_f32 v16, v16, v70\n"
    "v_mul_f32 v20, v20, v70\n"
    "v_mfma_f32_16x16x32_bf16 v[24:27], v[44:47], v[236:239], v[24:27]\n"
    "v_cvt_pk_bf16_f32 v16, v16, v20\n"
    "v_mul_f32 v17, v17, v70\n"
    "v_mfma_f32_16x16x32_bf16 v[28:31], v[44:47], v[240:243], v[28:31]\n"
    "v_mul_f32 v21, v21, v70\n"
    "v_cvt_pk_bf16_f32 v17, v17, v21\n"
    "v_mfma_f32_16x16x32_bf16 v[24:27], v[32:35], v[244:247], v[24:27]\n"
    "v_mul_f32 v18, v18, v70\n"
    "v_mul_f32 v22, v22, v70\n"
    "v_mfma_f32_16x16x32_bf16 v[28:31], v[32:35], v[248:251], v[28:31]\n"
    "v_cvt_pk_bf16_f32 v18, v18, v22\n"
    "v_mul_f32 v19, v19, v70\n"
    "v_mfma_f32_16x16x32_bf16 v[24:27], v[36:39], v[252:255], v[24:27]\n"
    "v_mul_f32 v23, v23, v70\n"
    "v_cvt_pk_bf16_f32 v19, v19, v23\n"
    "v_mfma_f32_16x16x32_bf16 v[28:31], v[36:39], v[72:75], v[28:31]\n"
    "buffer_store_dwordx4 v[16:19], v2, s[40:43], s63 offen sc1\n"
    "s_mov_b32 s56, 1\n"
    "s_cmp_lg_u32 s94, 0\n"
    "s_cbranch_scc1 .Lhgo_%=\n"
    "s_nop 0\n"
    "v_mul_f32 v24, v24, v76\n"
    "v_mul_f32 v25, v25, v77\n"
    "v_cvt_pk_bf16_f32 v24, v24, v25\n"
    "v_mul_f32 v26, v26, v78\n"
    "v_mul_f32 v27, v27, v79\n"
    "v_cvt_pk_bf16_f32 v25, v26, v27\n"
    "v_mul_f32 v28, v28, v76\n"
    "v_mul_f32 v29, v29, v77\n"
    "v_cvt_pk_bf16_f32 v26, v28, v29\n"
    "v_mul_f32 v30, v30, v78\n"
    "v_mul_f32 v31, v31, v79\n"
    "v_cvt_pk_bf16_f32 v27, v30, v31\n"
    "buffer_store_dwordx4 v[24:27], v2, s[40:43], s64 offen sc1\n"
    "s_waitcnt vmcnt(3)\n"
    "v_cmp_eq_u32 vcc, 0, v81\n"
    "s_cmp_eq_u64 vcc, 0\n"
    "s_cbranch_scc1 .Lhld_%=\n"
    "ds_read_b32 v83, v7 offset:44\n"
    "global_load_dword v81, v80, %1 sc1\n"
    "s_waitcnt vmcnt(0)\n"
    "v_mov_b32 v82, 1\n"
    "s_mov_b64 exec, 1\n"
    "ds_add_rtn_u32 v82, v7, v82 offset:48\n"
    "s_mov_b64 exec, -1\n"
    "s_waitcnt lgkmcnt(0)\n"
    "v_readfirstlane_b32 s50, v82\n"
    "s_cmp_eq_u32 s50, 3\n"
    "s_cselect_b64 exec, 1, 0\n"
    "global_store_dword v83, v82, %1 sc1\n"
    "s_mov_b64 exec, -1\n"
    "s_mov_b32 s56, 0\n"
    "v_cmp_eq_u32 vcc, 0, v81\n"
    "s_cmp_eq_u64 vcc, 0\n"
    "s_cbranch_scc1 .Lhld_%=\n"
    ".Lhchk_%=:\n"
    "v_readfirstlane_b32 s50, v80\n"
    "s_cmp_lt_u32 s50, 200\n"
    "s_cbranch_scc1 .Lhend_%=\n"
    ".Lhwait_%=:\n"
    "global_load_dword v81, v80, %1 sc1\n"
    "s_mov_b32 s52, 0\n"
    ".Lhp2_%=:\n"
    "s_waitcnt vmcnt(0)\n"
    "v_cmp_eq_u32 vcc, 0, v81\n"
    "s_cmp_eq_u64 vcc, 0\n"
    "s_cbranch_scc1 .Lhp2_x%=\n"
    "s_add_u32 s52, s52, 1\n"
    "s_sleep 2\n"
    "global_load_dword v81, v80, %1 sc1\n"
    "s_cmp_lt_u32 s52, s53\n"
    "s_cbranch_scc1 .Lhp2_%=\n"
    "s_mov_b32 s53, 0\n"
    "v_mov_b32 v82, 0\n"
    "v_mov_b32 v83, 1\n"
    "global_store_dword v82, v83, %1 sc1\n"
    "s_waitcnt vmcnt(0)\n"
    ".Lhp2_x%=:\n"
    ".Lhld_%=:\n"
    "s_mov_b32 s94, s42\n"
    "s_mov_b32 s90, s96\n"
    "buffer_load_dwordx4 v[48:51], v3, s[92:95], s49 offen sc1\n"
    "buffer_load_dwordx4 v[52:55], v3, s[92:95], s49 offen offset:1024 sc1\n"
    "buffer_load_dwordx4 v[56:59], v3, s[92:95], s49 offen offset:2048 sc1\n"
    "buffer_load_dwordx4 v[60:63], v3, s[92:95], s49 offen offset:3072 sc1\n"
    "buffer_load_dwordx4 v[64:67], v2, s[88:91], s49 offen sc1\n"
    ".Lhin_%=:\n"
    "v_add_u32 v7, 32, v7\n"
    "ds_read_b128 v[80:83], v7\n"
    "s_waitcnt vmcnt(4)\n"
    "v_mfma_f32_16x16x32_bf16 v[68:71], v[48:51], v[48:51], 0\n"
    "s_waitcnt vmcnt(3)\n"
    "v_mfma_f32_16x16x32_bf16 v[68:71], v[52:55], v[52:55], v[68:71]\n"
    "ds_write_b128 v1, v[48:51]\n"
    "ds_write_b128 v1, v[52:55] offset:1024\n"
    "s_waitcnt vmcnt(2)\n"
    "v_mfma_f32_16x16x32_bf16 v[68:71], v[56:59], v[56:59], v[68:71]\n"
    "ds_write_b128 v1, v[56:59] offset:2048\n"
    "s_waitcnt vmcnt(1)\n"
    "ds_write_b128 v1, v[60:63] offset:3072\n"
    "v_mfma_f32_16x16x32_bf16 v[68:71], v[60:63], v[60:63], v[68:71]\n"
    "s_waitcnt vmcnt(0)\n"
    "v_mfma_f32_16x16x32_bf16 v[68:71], v[64:67], v[64:67], v[68:71]\n"
    "v_mfma_f32_16x16x32_bf16 v[8:11], a[0:3], v[48:51], 0\n"
    "v_mfma_f32_16x16x32_bf16 v[12:15], a[4:7], v[48:51], 0\n"
    "v_mfma_f32_16x16x32_bf16 v[16:19], a[144:147], v[48:51], 0\n"
    "v_mov_b64 v[76:77], s[86:87]\n"
    "v_mov_b64 v[78:79], s[86:87]\n"
    "v_mfma_f32_16x16x32_bf16 v[20:23], a[148:151], v[48:51], 0\n"
    "ds_write_b128 v1, v[64:67] offset:4096\n"
    "v_mfma_f32_16x16x32_bf16 v[72:75], v[76:79], v[48:51], 0\n"
    "v_mfma_f32_16x16x32_bf16 v[72:75], v[76:79], v[52:55], v[72:75]\n"
    "v_mfma_f32_16x16x32_bf16 v[72:75], v[76:79], v[56:59], v[72:75]\n"
    "ds_write_b128 v1, v[68:71] offset:5120\n"
    "s_waitcnt lgkmcnt(6)\n"
    "v_readfirstlane_b32 s49, v81\n"
    "v_mfma_f32_16x16x32_bf16 v[72:75], v[76:79], v[60:63], v[72:75]\n"
    "v_readfirstlane_b32 s62, v82\n"
    "v_add_u32 v80, v80, v4\n"
    "v_mfma_f32_16x16x32_bf16 v[72:75], v[76:79], v[64:67], v[72:75]\n"
    "s_cmp_eq_u32 s56, 0\n"
    "s_cbranch_scc1 .Lhc3_%=\n"
    "s_waitcnt vmcnt(0)\n"
    "v_mov_b32 v82, 1\n"
    "s_mov_b64 exec, 1\n"
    "ds_add_rtn_u32 v82, v7, v82 offset:16\n"
    "s_mov_b64 exec, -1\n"
    "s_waitcnt lgkmcnt(0)\n"
    "v_readfirstlane_b32 s50, v82\n"
    "s_cmp_eq_u32 s50, 3\n"
    "s_cselect_b64 exec, 1, 0\n"
    "global_store_dword v83, v82, %1 sc1\n"
    "s_mov_b64 exec, -1\n"
    "s_mov_b32 s56, 0\n"
    ".Lhc3_%=:\n"
    "s_branch .Lhtop_%=\n"
    ".Lhk1_%=:\n"
    "s_waitcnt vmcnt(5)\n"
    "v_cmp_eq_u32 vcc, 0, v81\n"
    "s_cmp_eq_u64 vcc, 0\n"
    "s_cselect_b32 s94, s42, 0\n"
    "s_cselect_b32 s90, s96, 0\n"
    "global_load_dword v81, v80, %1 sc1\n"
    "buffer_load_dwordx4 v[48:51], v3, s[92:95], s49 offen sc1\n"
    "buffer_load_dwordx4 v[52:55], v3, s[92:95], s49 offen offset:1024 sc1\n"
    "buffer_load_dwordx4 v[56:59], v3, s[92:95], s49 offen offset:2048 sc1\n"
    "buffer_load_dwordx4 v[60:63], v3, s[92:95], s49 offen offset:3072 sc1\n"
    "buffer_load_dwordx4 v[64:67], v2, s[88:91], s49 offen sc1\n"
    "s_branch .Lhk1_x%=\n"
    ".Lhpol_%=:\n"
    "v_mov_b32 v80, 64\n"
    "global_load_dword v81, v80, %1 sc1\n"
    "s_mov_b32 s52, 0\n"
    ".Lhp4_%=:\n"
    "s_waitcnt vmcnt(0)\n"
    "v_cmp_eq_u32 vcc, 0, v81\n"
    "s_cmp_eq_u64 vcc, 0\n"
    "s_cbranch_scc1 .Lhp4_x%=\n"
    "s_add_u32 s52, s52, 1\n"
    "s_sleep 2\n"
    "global_load_dword v81, v80, %1 sc1\n"
    "s_cmp_lt_u32 s52, s53\n"
    "s_cbranch_scc1 .Lhp4_%=\n"
    "s_mov_b32 s53, 0\n"
    "v_mov_b32 v82, 0\n"
    "v_mov_b32 v83, 1\n"
    "global_store_dword v82, v83, %1 sc1\n"
    "s_waitcnt vmcnt(0)\n"
    ".Lhp4_x%=:\n"
    "v_readfirstlane_b32 s50, v81\n"
    "v_mbcnt_lo_u32_b32 v80, -1, 0\n"
    "v_mbcnt_hi_u32_b32 v80, -1, v80\n"
    "s_lshl_b32 s51, %2, 6\n"
    "v_add_u32 v80, s51, v80\n"
    "v_min_u32 v80, 129, v80\n"
    "v_lshlrev_b32 v80, 5, v80\n"
    "v_add_u32 v80, 0x26c00, v80\n"
    "ds_read2_b32 v[82:83], v80 offset0:1 offset1:2\n"
    "s_and_b32 s50, s50, 1\n"
    "s_waitcnt lgkmcnt(0)\n"
    "v_and_b32 v81, s50, v82\n"
    "v_lshlrev_b32 v81, 13, v81\n"
    "v_and_b32 v82, -2, v82\n"
    "v_xor_b32 v82, v81, v82\n"
    "v_and_b32 v81, s50, v83\n"
    "v_lshlrev_b32 v81, 13, v81\n"
    "v_and_b32 v83, -2, v83\n"
    "v_xor_b32 v83, v81, v83\n"
    "ds_write2_b32 v80, v82, v83 offset0:1 offset1:2\n"
    "s_waitcnt lgkmcnt(0)\n"
    "s_barrier\n"
    "ds_read_b32 v81, v7 offset:4\n"
    "v_add_u32 v80, %4, v4\n"
    "s_waitcnt lgkmcnt(0)\n"
    "v_readfirstlane_b32 s49, v81\n"
    "s_branch .Lhwait_%=\n"
    ".Lhend_%=:\n"
    "s_cmp_eq_u32 s50, 192\n"
    "s_cbranch_scc0 .Lhpol_%=\n"
    "s_waitcnt vmcnt(0) lgkmcnt(0)\n"
    "s_endpgm\n"
    ".p2align 6\n"
    ".Lrole2_%=:\n"
    "s_mov_b64 s[40:41], %0\n"
    "s_and_b32 s41, s41, 0xffff\n"
    "s_mov_b32 s42, 0xc000000\n"
    "s_mov_b32 s43, 0x20000\n"
    "s_mov_b64 s[92:93], %0\n"
    "s_and_b32 s93, s93, 0xffff\n"
    "s_mov_b32 s94, 0xc000000\n"
    "s_mov_b32 s95, 0x20000\n"
    "s_mov_b32 s53, 131072\n"
    "s_mov_b64 s[56:57], 0\n"
    "v_mbcnt_lo_u32_b32 v7, -1, 0\n"
    "v_mbcnt_hi_u32_b32 v7, -1, v7\n"
    "s_lshl_b32 s50, %2, 8\n"
    "s_add_u32 s50, s50, 147456\n"
    "v_lshl_add_u32 v7, v7, 2, s50\n"
    "ds_read_b32 v1, v7\n"
    "ds_read_b32 v4, v7 offset:1024\n"
    "ds_read_b32 v2, v7 offset:2048\n"
    "ds_read_b32 v8, v7 offset:3072\n"
    "ds_read_b32 v9, v7 offset:4096\n"
    "ds_read_b32 v10, v7 offset:5120\n"
    "ds_read_b32 v11, v7 offset:6144\n"
    "ds_read_b32 v3, v7 offset:7168\n"
    "s_waitcnt lgkmcnt(0)\n"
    "s_barrier\n"
    "s_mul_i32 s54, %2, 6144\n"
    "v_add_u32 v5, 16384, v4\n"
    "s_xor_b32 s50, %2, 0\n"
    "s_mul_i32 s50, s50, 6144\n"
    "v_add_u32 v32, s50, v9\n"
    "v_add_u32 v33, s50, v10\n"
    "v_add_u32 v34, s50, v11\n"
    "s_xor_b32 s50, %2, 1\n"
    "s_mul_i32 s50, s50, 6144\n"
    "v_add_u32 v35, s50, v9\n"
    "v_add_u32 v36, s50, v10\n"
    "v_add_u32 v37, s50, v11\n"
    "s_xor_b32 s50, %2, 2\n"
    "s_mul_i32 s50, s50, 6144\n"
    "v_add_u32 v38, s50, v9\n"
    "v_add_u32 v39, s50, v10\n"
    "v_add_u32 v40, s50, v11\n"
    "s_xor_b32 s50, %2, 3\n"
    "s_mul_i32 s50, s50, 6144\n"
    "v_add_u32 v41, s50, v9\n"
    "v_add_u32 v42, s50, v10\n"
    "v_add_u32 v43, s50, v11\n"
    "s_mov_b64 s[44:45], %8\n"
    "s_and_b32 s45, s45, 0xffff\n"
    "s_mov_b32 s46, 0x480000\n"
    "s_mov_b32 s47, 0x20000\n"
    "s_add_u32 m0, s54, 0\n"
    "s_add_u32 s55, %6, 0\n"
    "buffer_load_dwordx4 v8, s[44:47], s55 offen lds\n"
    "s_add_u32 m0, s54, 1024\n"
    "s_add_u32 s55, %6, 18432\n"
    "buffer_load_dwordx4 v8, s[44:47], s55 offen lds\n"
    "s_add_u32 m0, s54, 2048\n"
    "s_add_u32 s55, %6, 128\n"
    "buffer_load_dwordx4 v8, s[44:47], s55 offen lds\n"
    "s_add_u32 m0, s54, 3072\n"
    "s_add_u32 s55, %6, 18560\n"
    "buffer_load_dwordx4 v8, s[44:47], s55 offen lds\n"
    "s_add_u32 m0, s54, 4096\n"
    "s_add_u32 s55, %6, 256\n"
    "buffer_load_dwordx4 v8, s[44:47], s55 offen lds\n"
    "s_add_u32 m0, s54, 5120\n"
    "s_add_u32 s55, %6, 18688\n"
    "buffer_load_dwordx4 v8, s[44:47], s55 offen lds\n"
    "s_add_u32 m0, s54, 24576\n"
    "s_add_u32 s55, %6, 2304\n"
    "buffer_load_dwordx4 v8, s[44:47], s55 offen lds\n"
    "s_add_u32 m0, s54, 25600\n"
    "s_add_u32 s55, %6, 20736\n"
    "buffer_load_dwordx4 v8, s[44:47], s55 offen lds\n"
    "s_add_u32 m0, s54, 26624\n"
    "s_add_u32 s55, %6, 2432\n"
    "buffer_load_dwordx4 v8, s[44:47], s55 offen lds\n"
    "s_add_u32 m0, s54, 27648\n"
    "s_add_u32 s55, %6, 20864\n"
    "buffer_load_dwordx4 v8, s[44:47], s55 offen lds\n"
    "s_add_u32 m0, s54, 28672\n"
    "s_add_u32 s55, %6, 2560\n"
    "buffer_load_dwordx4 v8, s[44:47], s55 offen lds\n"
    "s_add_u32 m0, s54, 29696\n"
    "s_add_u32 s55, %6, 20992\n"
    "buffer_load_dwordx4 v8, s[44:47], s55 offen lds\n"
    "s_add_u32 m0, s54, 49152\n"
    "s_add_u32 s55, %6, 36864\n"
    "buffer_load_dwordx4 v8, s[44:47], s55 offen lds\n"
    "s_add_u32 m0, s54, 50176\n"
    "s_add_u32 s55, %6, 55296\n"
    "buffer_load_dwordx4 v8, s[44:47], s55 offen lds\n"
    "s_add_u32 m0, s54, 51200\n"
    "s_add_u32 s55, %6, 36992\n"
    "buffer_load_dwordx4 v8, s[44:47], s55 offen lds\n"
    "s_add_u32 m0, s54, 52224\n"
    "s_add_u32 s55, %6, 55424\n"
    "buffer_load_dwordx4 v8, s[44:47], s55 offen lds\n"
    "s_add_u32 m0, s54, 53248\n"
    "s_add_u32 s55, %6, 37120\n"
    "buffer_load_dwordx4 v8, s[44:47], s55 offen lds\n"
    "s_add_u32 m0, s54, 54272\n"
    "s_add_u32 s55, %6, 55552\n"
    "buffer_load_dwordx4 v8, s[44:47], s55 offen lds\n"
    "s_add_u32 m0, s54, 73728\n"
    "s_add_u32 s55, %6, 39168\n"
    "buffer_load_dwordx4 v8, s[44:47], s55 offen lds\n"
    "s_add_u32 m0, s54, 74752\n"
    "s_add_u32 s55, %6, 57600\n"
    "buffer_load_dwordx4 v8, s[44:47], s55 offen lds\n"
    "s_add_u32 m0, s54, 75776\n"
    "s_add_u32 s55, %6, 39296\n"
    "buffer_load_dwordx4 v8, s[44:47], s55 offen lds\n"
    "s_add_u32 m0, s54, 76800\n"
    "s_add_u32 s55, %6, 57728\n"
    "buffer_load_dwordx4 v8, s[44:47], s55 offen lds\n"
    "s_add_u32 m0, s54, 77824\n"
    "s_add_u32 s55, %6, 39424\n"
    "buffer_load_dwordx4 v8, s[44:47], s55 offen lds\n"
    "s_add_u32 m0, s54, 78848\n"
    "s_add_u32 s55, %6, 57856\n"
    "buffer_load_dwordx4 v8, s[44:47], s55 offen lds\n"
    "s_add_u32 m0, s54, 98304\n"
    "s_add_u32 s55, %6, 73728\n"
    "buffer_load_dwordx4 v8, s[44:47], s55 offen lds\n"
    "s_add_u32 m0, s54, 99328\n"
    "s_add_u32 s55, %6, 92160\n"
    "buffer_load_dwordx4 v8, s[44:47], s55 offen lds\n"
    "s_add_u32 m0, s54, 100352\n"
    "s_add_u32 s55, %6, 73856\n"
    "buffer_load_dwordx4 v8, s[44:47], s55 offen lds\n"
    "s_add_u32 m0, s54, 101376\n"
    "s_add_u32 s55, %6, 92288\n"
    "buffer_load_dwordx4 v8, s[44:47], s55 offen lds\n"
    "s_add_u32 m0, s54, 102400\n"
    "s_add_u32 s55, %6, 73984\n"
    "buffer_load_dwordx4 v8, s[44:47], s55 offen lds\n"
    "s_add_u32 m0, s54, 103424\n"
    "s_add_u32 s55, %6, 92416\n"
    "buffer_load_dwordx4 v8, s[44:47], s55 offen lds\n"
    "s_add_u32 m0, s54, 122880\n"
    "s_add_u32 s55, %6, 76032\n"
    "buffer_load_dwordx4 v8, s[44:47], s55 offen lds\n"
    "s_add_u32 m0, s54, 123904\n"
    "s_add_u32 s55, %6, 94464\n"
    "buffer_load_dwordx4 v8, s[44:47], s55 offen lds\n"
    "s_add_u32 m0, s54, 124928\n"
    "s_add_u32 s55, %6, 76160\n"
    "buffer_load_dwordx4 v8, s[44:47], s55 offen lds\n"
    "s_add_u32 m0, s54, 125952\n"
    "s_add_u32 s55, %6, 94592\n"
    "buffer_load_dwordx4 v8, s[44:47], s55 offen lds\n"
    "s_add_u32 m0, s54, 126976\n"
    "s_add_u32 s55, %6, 76288\n"
    "buffer_load_dwordx4 v8, s[44:47], s55 offen lds\n"
    "s_add_u32 m0, s54, 128000\n"
    "s_add_u32 s55, %6, 94720\n"
    "buffer_load_dwordx4 v8, s[44:47], s55 offen lds\n"
    "s_waitcnt vmcnt(30) lgkmcnt(0)\n"
    "s_barrier\n"
    "ds_read_b64_tr_b16 a[0:1], v32\n"
    "ds_read_b64_tr_b16 a[4:5], v33\n"
    "ds_read_b64_tr_b16 a[8:9], v34\n"
    "ds_read_b64_tr_b16 a[96:97], v35\n"
    "ds_read_b64_tr_b16 a[100:101], v36\n"
    "ds_read_b64_tr_b16 a[104:105], v37\n"
    "ds_read_b64_tr_b16 a[192:193], v38\n"
    "ds_read_b64_tr_b16 a[196:197], v39\n"
    "ds_read_b64_tr_b16 a[200:201], v40\n"
    "ds_read_b64_tr_b16 v[160:161], v41\n"
    "ds_read_b64_tr_b16 v[164:165], v42\n"
    "ds_read_b64_tr_b16 v[168:169], v43\n"
    "s_waitcnt vmcnt(24) lgkmcnt(0)\n"
    "s_barrier\n"
    "s_add_u32 m0, s54, 0\n"
    "s_add_u32 s55, %6, 110592\n"
    "buffer_load_dwordx4 v8, s[44:47], s55 offen lds\n"
    "s_add_u32 m0, s54, 1024\n"
    "s_add_u32 s55, %6, 129024\n"
    "buffer_load_dwordx4 v8, s[44:47], s55 offen lds\n"
    "s_add_u32 m0, s54, 2048\n"
    "s_add_u32 s55, %6, 110720\n"
    "buffer_load_dwordx4 v8, s[44:47], s55 offen lds\n"
    "s_add_u32 m0, s54, 3072\n"
    "s_add_u32 s55, %6, 129152\n"
    "buffer_load_dwordx4 v8, s[44:47], s55 offen lds\n"
    "s_add_u32 m0, s54, 4096\n"
    "s_add_u32 s55, %6, 110848\n"
    "buffer_load_dwordx4 v8, s[44:47], s55 offen lds\n"
    "s_add_u32 m0, s54, 5120\n"
    "s_add_u32 s55, %6, 129280\n"
    "buffer_load_dwordx4 v8, s[44:47], s55 offen lds\n"
    "ds_read_b64_tr_b16 a[2:3], v32 offset:24576\n"
    "ds_read_b64_tr_b16 a[6:7], v33 offset:24576\n"
    "ds_read_b64_tr_b16 a[10:11], v34 offset:24576\n"
    "ds_read_b64_tr_b16 a[98:99], v35 offset:24576\n"
    "ds_read_b64_tr_b16 a[102:103], v36 offset:24576\n"
    "ds_read_b64_tr_b16 a[106:107], v37 offset:24576\n"
    "ds_read_b64_tr_b16 a[194:195], v38 offset:24576\n"
    "ds_read_b64_tr_b16 a[198:199], v39 offset:24576\n"
    "ds_read_b64_tr_b16 a[202:203], v40 offset:24576\n"
    "ds_read_b64_tr_b16 v[162:163], v41 offset:24576\n"
    "ds_read_b64_tr_b16 v[166:167], v42 offset:24576\n"
    "ds_read_b64_tr_b16 v[170:171], v43 offset:24576\n"
    "s_waitcnt vmcnt(24) lgkmcnt(0)\n"
    "s_barrier\n"
    "s_add_u32 m0, s54, 24576\n"
    "s_add_u32 s55, %6, 112896\n"
    "buffer_load_dwordx4 v8, s[44:47], s55 offen lds\n"
    "s_add_u32 m0, s54, 25600\n"
    "s_add_u32 s55, %6, 131328\n"
    "buffer_load_dwordx4 v8, s[44:47], s55 offen lds\n"
    "s_add_u32 m0, s54, 26624\n"
    "s_add_u32 s55, %6, 113024\n"
    "buffer_load_dwordx4 v8, s[44:47], s55 offen lds\n"
    "s_add_u32 m0, s54, 27648\n"
    "s_add_u32 s55, %6, 131456\n"
    "buffer_load_dwordx4 v8, s[44:47], s55 offen lds\n"
    "s_add_u32 m0, s54, 28672\n"
    "s_add_u32 s55, %6, 113152\n"
    "buffer_load_dwordx4 v8, s[44:47], s55 offen lds\n"
    "s_add_u32 m0, s54, 29696\n"
    "s_add_u32 s55, %6, 131584\n"
    "buffer_load_dwordx4 v8, s[44:47], s55 offen lds\n"
    "ds_read_b64_tr_b16 a[12:13], v32 offset:49152\n"
    "ds_read_b64_tr_b16 a[16:17], v33 offset:49152\n"
    "ds_read_b64_tr_b16 a[20:21], v34 offset:49152\n"
    "ds_read_b64_tr_b16 a[108:109], v35 offset:49152\n"
    "ds_read_b64_tr_b16 a[112:113], v36 offset:49152\n"
    "ds_read_b64_tr_b16 a[116:117], v37 offset:49152\n"
    "ds_read_b64_tr_b16 a[204:205], v38 offset:49152\n"
    "ds_read_b64_tr_b16 a[208:209], v39 offset:49152\n"
    "ds_read_b64_tr_b16 a[212:213], v40 offset:49152\n"
    "ds_read_b64_tr_b16 v[172:173], v41 offset:49152\n"
    "ds_read_b64_tr_b16 v[176:177], v42 offset:49152\n"
    "ds_read_b64_tr_b16 v[180:181], v43 offset:49152\n"
    "s_waitcnt vmcnt(24) lgkmcnt(0)\n"
    "s_barrier\n"
    "s_add_u32 m0, s54, 49152\n"
    "s_add_u32 s55, %6, 147456\n"
    "buffer_load_dwordx4 v8, s[44:47], s55 offen lds\n"
    "s_add_u32 m0, s54, 50176\n"
    "s_add_u32 s55, %6, 165888\n"
    "buffer_load_dwordx4 v8, s[44:47], s55 offen lds\n"
    "s_add_u32 m0, s54, 51200\n"
    "s_add_u32 s55, %6, 147584\n"
    "buffer_load_dwordx4 v8, s[44:47], s55 offen lds\n"
    "s_add_u32 m0, s54, 52224\n"
    "s_add_u32 s55, %6, 166016\n"
    "buffer_load_dwordx4 v8, s[44:47], s55 offen lds\n"
    "s_add_u32 m0, s54, 53248\n"
    "s_add_u32 s55, %6, 147712\n"
    "buffer_load_dwordx4 v8, s[44:47], s55 offen lds\n"
    "s_add_u32 m0, s54, 54272\n"
    "s_add_u32 s55, %6, 166144\n"
    "buffer_load_dwordx4 v8, s[44:47], s55 offen lds\n"
    "v_add_u32 v71, 0x10000, v32\n"
    "ds_read_b64_tr_b16 a[14:15], v71 offset:8192\n"
    "v_add_u32 v70, 0x10000, v33\n"
    "ds_read_b64_tr_b16 a[18:19], v70 offset:8192\n"
    "v_add_u32 v69, 0x10000, v34\n"
    "ds_read_b64_tr_b16 a[22:23], v69 offset:8192\n"
    "v_add_u32 v68, 0x10000, v35\n"
    "ds_read_b64_tr_b16 a[110:111], v68 offset:8192\n"
    "v_add_u32 v67, 0x10000, v36\n"
    "ds_read_b64_tr_b16 a[114:115], v67 offset:8192\n"
    "v_add_u32 v66, 0x10000, v37\n"
    "ds_read_b64_tr_b16 a[118:119], v66 offset:8192\n"
    "v_add_u32 v65, 0x10000, v38\n"
    "ds_read_b64_tr_b16 a[206:207], v65 offset:8192\n"
    "v_add_u32 v64, 0x10000, v39\n"
    "ds_read_b64_tr_b16 a[210:211], v64 offset:8192\n"
    "v_add_u32 v63, 0x10000, v40\n"
    "ds_read_b64_tr_b16 a[214:215], v63 offset:8192\n"
    "v_add_u32 v62, 0x10000, v41\n"
    "ds_read_b64_tr_b16 v[174:175], v62 offset:8192\n"
    "v_add_u32 v61, 0x10000, v42\n"
    "ds_read_b64_tr_b16 v[178:179], v61 offset:8192\n"
    "v_add_u32 v60, 0x10000, v43\n"
    "ds_read_b64_tr_b16 v[182:183], v60 offset:8192\n"
    "s_waitcnt vmcnt(24) lgkmcnt(0)\n"
    "s_barrier\n"
    "s_add_u32 m0, s54, 73728\n"
    "s_add_u32 s55, %6, 149760\n"
    "buffer_load_dwordx4 v8, s[44:47], s55 offen lds\n"
    "s_add_u32 m0, s54, 74752\n"
    "s_add_u32 s55, %6, 168192\n"
    "buffer_load_dwordx4 v8, s[44:47], s55 offen lds\n"
    "s_add_u32 m0, s54, 75776\n"
    "s_add_u32 s55, %6, 149888\n"
    "buffer_load_dwordx4 v8, s[44:47], s55 offen lds\n"
    "s_add_u32 m0, s54, 76800\n"
    "s_add_u32 s55, %6, 168320\n"
    "buffer_load_dwordx4 v8, s[44:47], s55 offen lds\n"
    "s_add_u32 m0, s54, 77824\n"
    "s_add_u32 s55, %6, 150016\n"
    "buffer_load_dwordx4 v8, s[44:47], s55 offen lds\n"
    "s_add_u32 m0, s54, 78848\n"
    "s_add_u32 s55, %6, 168448\n"
    "buffer_load_dwordx4 v8, s[44:47], s55 offen lds\n"
    "ds_read_b64_tr_b16 a[24:25], v71 offset:32768\n"
    "ds_read_b64_tr_b16 a[28:29], v70 offset:32768\n"
    "ds_read_b64_tr_b16 a[32:33], v69 offset:32768\n"
    "ds_read_b64_tr_b16 a[120:121], v68 offset:32768\n"
    "ds_read_b64_tr_b16 a[124:125], v67 offset:32768\n"
    "ds_read_b64_tr_b16 a[128:129], v66 offset:32768\n"
    "ds_read_b64_tr_b16 a[216:217], v65 offset:32768\n"
    "ds_read_b64_tr_b16 a[220:221], v64 offset:32768\n"
    "ds_read_b64_tr_b16 a[224:225], v63 offset:32768\n"
    "ds_read_b64_tr_b16 v[184:185], v62 offset:32768\n"
    "ds_read_b64_tr_b16 v[188:189], v61 offset:32768\n"
    "ds_read_b64_tr_b16 v[192:193], v60 offset:32768\n"
    "s_waitcnt vmcnt(24) lgkmcnt(0)\n"
    "s_barrier\n"
    "s_add_u32 m0, s54, 98304\n"
    "s_add_u32 s55, %6, 184320\n"
    "buffer_load_dwordx4 v8, s[44:47], s55 offen lds\n"
    "s_add_u32 m0, s54, 99328\n"
    "s_add_u32 s55, %6, 202752\n"
    "buffer_load_dwordx4 v8, s[44:47], s55 offen lds\n"
    "s_add_u32 m0, s54, 100352\n"
    "s_add_u32 s55, %6, 184448\n"
    "buffer_load_dwordx4 v8, s[44:47], s55 offen lds\n"
    "s_add_u32 m0, s54, 101376\n"
    "s_add_u32 s55, %6, 202880\n"
    "buffer_load_dwordx4 v8, s[44:47], s55 offen lds\n"
    "s_add_u32 m0, s54, 102400\n"
    "s_add_u32 s55, %6, 184576\n"
    "buffer_load_dwordx4 v8, s[44:47], s55 offen lds\n"
    "s_add_u32 m0, s54, 103424\n"
    "s_add_u32 s55, %6, 203008\n"
    "buffer_load_dwordx4 v8, s[44:47], s55 offen lds\n"
    "ds_read_b64_tr_b16 a[26:27], v71 offset:57344\n"
    "ds_read_b64_tr_b16 a[30:31], v70 offset:57344\n"
    "ds_read_b64_tr_b16 a[34:35], v69 offset:57344\n"
    "ds_read_b64_tr_b16 a[122:123], v68 offset:57344\n"
    "ds_read_b64_tr_b16 a[126:127], v67 offset:57344\n"
    "ds_read_b64_tr_b16 a[130:131], v66 offset:57344\n"
    "ds_read_b64_tr_b16 a[218:219], v65 offset:57344\n"
    "ds_read_b64_tr_b16 a[222:223], v64 offset:57344\n"
    "ds_read_b64_tr_b16 a[226:227], v63 offset:57344\n"
    "ds_read_b64_tr_b16 v[186:187], v62 offset:57344\n"
    "ds_read_b64_tr_b16 v[190:191], v61 offset:57344\n"
    "ds_read_b64_tr_b16 v[194:195], v60 offset:57344\n"
    "s_waitcnt vmcnt(24) lgkmcnt(0)\n"
    "s_barrier\n"
    "s_add_u32 m0, s54, 122880\n"
    "s_add_u32 s55, %6, 186624\n"
    "buffer_load_dwordx4 v8, s[44:47], s55 offen lds\n"
    "s_add_u32 m0, s54, 123904\n"
    "s_add_u32 s55, %6, 205056\n"
    "buffer_load_dwordx4 v8, s[44:47], s55 offen lds\n"
    "s_add_u32 m0, s54, 124928\n"
    "s_add_u32 s55, %6, 186752\n"
    "buffer_load_dwordx4 v8, s[44:47], s55 offen lds\n"
    "s_add_u32 m0, s54, 125952\n"
    "s_add_u32 s55, %6, 205184\n"
    "buffer_load_dwordx4 v8, s[44:47], s55 offen lds\n"
    "s_add_u32 m0, s54, 126976\n"
    "s_add_u32 s55, %6, 186880\n"
    "buffer_load_dwordx4 v8, s[44:47], s55 offen lds\n"
    "s_add_u32 m0, s54, 128000\n"
    "s_add_u32 s55, %6, 205312\n"
    "buffer_load_dwordx4 v8, s[44:47], s55 offen lds\n"
    "ds_read_b64_tr_b16 a[36:37], v32\n"
    "ds_read_b64_tr_b16 a[40:41], v33\n"
    "ds_read_b64_tr_b16 a[44:45], v34\n"
    "ds_read_b64_tr_b16 a[132:133], v35\n"
    "ds_read_b64_tr_b16 a[136:137], v36\n"
    "ds_read_b64_tr_b16 a[140:141], v37\n"
    "ds_read_b64_tr_b16 a[228:229], v38\n"
    "ds_read_b64_tr_b16 a[232:233], v39\n"
    "ds_read_b64_tr_b16 a[236:237], v40\n"
    "ds_read_b64_tr_b16 v[196:197], v41\n"
    "ds_read_b64_tr_b16 v[200:201], v42\n"
    "ds_read_b64_tr_b16 v[204:205], v43\n"
    "s_waitcnt vmcnt(24) lgkmcnt(0)\n"
    "s_barrier\n"
    "s_add_u32 m0, s54, 0\n"
    "s_add_u32 s55, %6, 221184\n"
    "buffer_load_dwordx4 v8, s[44:47], s55 offen lds\n"
    "s_add_u32 m0, s54, 1024\n"
    "s_add_u32 s55, %6, 239616\n"
    "buffer_load_dwordx4 v8, s[44:47], s55 offen lds\n"
    "s_add_u32 m0, s54, 2048\n"
    "s_add_u32 s55, %6, 221312\n"
    "buffer_load_dwordx4 v8, s[44:47], s55 offen lds\n"
    "s_add_u32 m0, s54, 3072\n"
    "s_add_u32 s55, %6, 239744\n"
    "buffer_load_dwordx4 v8, s[44:47], s55 offen lds\n"
    "s_add_u32 m0, s54, 4096\n"
    "s_add_u32 s55, %6, 221440\n"
    "buffer_load_dwordx4 v8, s[44:47], s55 offen lds\n"
    "s_add_u32 m0, s54, 5120\n"
    "s_add_u32 s55, %6, 239872\n"
    "buffer_load_dwordx4 v8, s[44:47], s55 offen lds\n"
    "ds_read_b64_tr_b16 a[38:39], v32 offset:24576\n"
    "ds_read_b64_tr_b16 a[42:43], v33 offset:24576\n"
    "ds_read_b64_tr_b16 a[46:47], v34 offset:24576\n"
    "ds_read_b64_tr_b16 a[134:135], v35 offset:24576\n"
    "ds_read_b64_tr_b16 a[138:139], v36 offset:24576\n"
    "ds_read_b64_tr_b16 a[142:143], v37 offset:24576\n"
    "ds_read_b64_tr_b16 a[230:231], v38 offset:24576\n"
    "ds_read_b64_tr_b16 a[234:235], v39 offset:24576\n"
    "ds_read_b64_tr_b16 a[238:239], v40 offset:24576\n"
    "ds_read_b64_tr_b16 v[198:199], v41 offset:24576\n"
    "ds_read_b64_tr_b16 v[202:203], v42 offset:24576\n"
    "ds_read_b64_tr_b16 v[206:207], v43 offset:24576\n"
    "s_waitcnt vmcnt(24) lgkmcnt(0)\n"
    "s_barrier\n"
    "s_add_u32 m0, s54, 24576\n"
    "s_add_u32 s55, %6, 223488\n"
    "buffer_load_dwordx4 v8, s[44:47], s55 offen lds\n"
    "s_add_u32 m0, s54, 25600\n"
    "s_add_u32 s55, %6, 241920\n"
    "buffer_load_dwordx4 v8, s[44:47], s55 offen lds\n"
    "s_add_u32 m0, s54, 26624\n"
    "s_add_u32 s55, %6, 223616\n"
    "buffer_load_dwordx4 v8, s[44:47], s55 offen lds\n"
    "s_add_u32 m0, s54, 27648\n"
    "s_add_u32 s55, %6, 242048\n"
    "buffer_load_dwordx4 v8, s[44:47], s55 offen lds\n"
    "s_add_u32 m0, s54, 28672\n"
    "s_add_u32 s55, %6, 223744\n"
    "buffer_load_dwordx4 v8, s[44:47], s55 offen lds\n"
    "s_add_u32 m0, s54, 29696\n"
    "s_add_u32 s55, %6, 242176\n"
    "buffer_load_dwordx4 v8, s[44:47], s55 offen lds\n"
    "ds_read_b64_tr_b16 a[48:49], v32 offset:49152\n"
    "ds_read_b64_tr_b16 a[52:53], v33 offset:49152\n"
    "ds_read_b64_tr_b16 a[56:57], v34 offset:49152\n"
    "ds_read_b64_tr_b16 a[144:145], v35 offset:49152\n"
    "ds_read_b64_tr_b16 a[148:149], v36 offset:49152\n"
    "ds_read_b64_tr_b16 a[152:153], v37 offset:49152\n"
    "ds_read_b64_tr_b16 a[240:241], v38 offset:49152\n"
    "ds_read_b64_tr_b16 a[244:245], v39 offset:49152\n"
    "ds_read_b64_tr_b16 a[248:249], v40 offset:49152\n"
    "ds_read_b64_tr_b16 v[208:209], v41 offset:49152\n"
    "ds_read_b64_tr_b16 v[212:213], v42 offset:49152\n"
    "ds_read_b64_tr_b16 v[216:217], v43 offset:49152\n"
    "s_waitcnt vmcnt(24) lgkmcnt(0)\n"
    "s_barrier\n"
    "s_add_u32 m0, s54, 49152\n"
    "s_add_u32 s55, %6, 258048\n"
    "buffer_load_dwordx4 v8, s[44:47], s55 offen lds\n"
    "s_add_u32 m0, s54, 50176\n"
    "s_add_u32 s55, %6, 276480\n"
    "buffer_load_dwordx4 v8, s[44:47], s55 offen lds\n"
    "s_add_u32 m0, s54, 51200\n"
    "s_add_u32 s55, %6, 258176\n"
    "buffer_load_dwordx4 v8, s[44:47], s55 offen lds\n"
    "s_add_u32 m0, s54, 52224\n"
    "s_add_u32 s55, %6, 276608\n"
    "buffer_load_dwordx4 v8, s[44:47], s55 offen lds\n"
    "s_add_u32 m0, s54, 53248\n"
    "s_add_u32 s55, %6, 258304\n"
    "buffer_load_dwordx4 v8, s[44:47], s55 offen lds\n"
    "s_add_u32 m0, s54, 54272\n"
    "s_add_u32 s55, %6, 276736\n"
    "buffer_load_dwordx4 v8, s[44:47], s55 offen lds\n"
    "ds_read_b64_tr_b16 a[50:51], v71 offset:8192\n"
    "ds_read_b64_tr_b16 a[54:55], v70 offset:8192\n"
    "ds_read_b64_tr_b16 a[58:59], v69 offset:8192\n"
    "ds_read_b64_tr_b16 a[146:147], v68 offset:8192\n"
    "ds_read_b64_tr_b16 a[150:151], v67 offset:8192\n"
    "ds_read_b64_tr_b16 a[154:155], v66 offset:8192\n"
    "ds_read_b64_tr_b16 a[242:243], v65 offset:8192\n"
    "ds_read_b64_tr_b16 a[246:247], v64 offset:8192\n"
    "ds_read_b64_tr_b16 a[250:251], v63 offset:8192\n"
    "ds_read_b64_tr_b16 v[210:211], v62 offset:8192\n"
    "ds_read_b64_tr_b16 v[214:215], v61 offset:8192\n"
    "ds_read_b64_tr_b16 v[218:219], v60 offset:8192\n"
    "s_waitcnt vmcnt(24) lgkmcnt(0)\n"
    "s_barrier\n"
    "s_add_u32 m0, s54, 73728\n"
    "s_add_u32 s55, %6, 260352\n"
    "buffer_load_dwordx4 v8, s[44:47], s55 offen lds\n"
    "s_add_u32 m0, s54, 74752\n"
    "s_add_u32 s55, %6, 278784\n"
    "buffer_load_dwordx4 v8, s[44:47], s55 offen lds\n"
    "s_add_u32 m0, s54, 75776\n"
    "s_add_u32 s55, %6, 260480\n"
    "buffer_load_dwordx4 v8, s[44:47], s55 offen lds\n"
    "s_add_u32 m0, s54, 76800\n"
    "s_add_u32 s55, %6, 278912\n"
    "buffer_load_dwordx4 v8, s[44:47], s55 offen lds\n"
    "s_add_u32 m0, s54, 77824\n"
    "s_add_u32 s55, %6, 260608\n"
    "buffer_load_dwordx4 v8, s[44:47], s55 offen lds\n"
    "s_add_u32 m0, s54, 78848\n"
    "s_add_u32 s55, %6, 279040\n"
    "buffer_load_dwordx4 v8, s[44:47], s55 offen lds\n"
    "v_mov_b32 v7, 0x20400\n"
    "global_load_dwordx2 v[20:21], v7, %1 sc1\n"
    "v_mbcnt_lo_u32_b32 v6, -1, 0\n"
    "v_min_u32 v6, 7, v6\n"
    "v_lshlrev_b32 v6, 7, v6\n"
    "v_add_u32 v6, 0x20000, v6\n"
    "global_load_dwordx2 v[22:23], v6, %1 sc1\n"
    "ds_read_b64_tr_b16 a[60:61], v71 offset:32768\n"
    "ds_read_b64_tr_b16 a[64:65], v70 offset:32768\n"
    "ds_read_b64_tr_b16 a[68:69], v69 offset:32768\n"
    "ds_read_b64_tr_b16 a[156:157], v68 offset:32768\n"
    "ds_read_b64_tr_b16 a[160:161], v67 offset:32768\n"
    "ds_read_b64_tr_b16 a[164:165], v66 offset:32768\n"
    "ds_read_b64_tr_b16 a[252:253], v65 offset:32768\n"
    "ds_read_b64_tr_b16 v[128:129], v64 offset:32768\n"
    "ds_read_b64_tr_b16 v[132:133], v63 offset:32768\n"
    "ds_read_b64_tr_b16 v[220:221], v62 offset:32768\n"
    "ds_read_b64_tr_b16 v[224:225], v61 offset:32768\n"
    "ds_read_b64_tr_b16 v[228:229], v60 offset:32768\n"
    "s_waitcnt vmcnt(26) lgkmcnt(0)\n"
    "s_barrier\n"
    "ds_read_b64_tr_b16 a[62:63], v71 offset:57344\n"
    "ds_read_b64_tr_b16 a[66:67], v70 offset:57344\n"
    "ds_read_b64_tr_b16 a[70:71], v69 offset:57344\n"
    "ds_read_b64_tr_b16 a[158:159], v68 offset:57344\n"
    "ds_read_b64_tr_b16 a[162:163], v67 offset:57344\n"
    "ds_read_b64_tr_b16 a[166:167], v66 offset:57344\n"
    "ds_read_b64_tr_b16 a[254:255], v65 offset:57344\n"
    "ds_read_b64_tr_b16 v[130:131], v64 offset:57344\n"
    "ds_read_b64_tr_b16 v[134:135], v63 offset:57344\n"
    "ds_read_b64_tr_b16 v[222:223], v62 offset:57344\n"
    "ds_read_b64_tr_b16 v[226:227], v61 offset:57344\n"
    "ds_read_b64_tr_b16 v[230:231], v60 offset:57344\n"
    "s_waitcnt vmcnt(20) lgkmcnt(0)\n"
    "s_barrier\n"
    "ds_read_b64_tr_b16 a[72:73], v32\n"
    "ds_read_b64_tr_b16 a[76:77], v33\n"
    "ds_read_b64_tr_b16 a[80:81], v34\n"
    "ds_read_b64_tr_b16 a[168:169], v35\n"
    "ds_read_b64_tr_b16 a[172:173], v36\n"
    "ds_read_b64_tr_b16 a[176:177], v37\n"
    "ds_read_b64_tr_b16 v[136:137], v38\n"
    "ds_read_b64_tr_b16 v[140:141], v39\n"
    "ds_read_b64_tr_b16 v[144:145], v40\n"
    "ds_read_b64_tr_b16 v[232:233], v41\n"
    "ds_read_b64_tr_b16 v[236:237], v42\n"
    "ds_read_b64_tr_b16 v[240:241], v43\n"
    "s_waitcnt vmcnt(14) lgkmcnt(0)\n"
    "s_barrier\n"
    "ds_read_b64_tr_b16 a[74:75], v32 offset:24576\n"
    "ds_read_b64_tr_b16 a[78:79], v33 offset:24576\n"
    "ds_read_b64_tr_b16 a[82:83], v34 offset:24576\n"
    "ds_read_b64_tr_b16 a[170:171], v35 offset:24576\n"
    "ds_read_b64_tr_b16 a[174:175], v36 offset:24576\n"
    "ds_read_b64_tr_b16 a[178:179], v37 offset:24576\n"
    "ds_read_b64_tr_b16 v[138:139], v38 offset:24576\n"
    "ds_read_b64_tr_b16 v[142:143], v39 offset:24576\n"
    "ds_read_b64_tr_b16 v[146:147], v40 offset:24576\n"
    "ds_read_b64_tr_b16 v[234:235], v41 offset:24576\n"
    "ds_read_b64_tr_b16 v[238:239], v42 offset:24576\n"
    "ds_read_b64_tr_b16 v[242:243], v43 offset:24576\n"
    "s_waitcnt vmcnt(8) lgkmcnt(0)\n"
    "s_barrier\n"
    "ds_read_b64_tr_b16 a[84:85], v32 offset:49152\n"
    "ds_read_b64_tr_b16 a[88:89], v33 offset:49152\n"
    "ds_read_b64_tr_b16 a[92:93], v34 offset:49152\n"
    "ds_read_b64_tr_b16 a[180:181], v35 offset:49152\n"
    "ds_read_b64_tr_b16 a[184:185], v36 offset:49152\n"
    "ds_read_b64_tr_b16 a[188:189], v37 offset:49152\n"
    "ds_read_b64_tr_b16 v[148:149], v38 offset:49152\n"
    "ds_read_b64_tr_b16 v[152:153], v39 offset:49152\n"
    "ds_read_b64_tr_b16 v[156:157], v40 offset:49152\n"
    "ds_read_b64_tr_b16 v[244:245], v41 offset:49152\n"
    "ds_read_b64_tr_b16 v[248:249], v42 offset:49152\n"
    "ds_read_b64_tr_b16 v[252:253], v43 offset:49152\n"
    "s_waitcnt vmcnt(2) lgkmcnt(0)\n"
    "s_barrier\n"
    "ds_read_b64_tr_b16 a[86:87], v71 offset:8192\n"
    "ds_read_b64_tr_b16 a[90:91], v70 offset:8192\n"
    "ds_read_b64_tr_b16 a[94:95], v69 offset:8192\n"
    "ds_read_b64_tr_b16 a[182:183], v68 offset:8192\n"
    "ds_read_b64_tr_b16 a[186:187], v67 offset:8192\n"
    "ds_read_b64_tr_b16 a[190:191], v66 offset:8192\n"
    "ds_read_b64_tr_b16 v[150:151], v65 offset:8192\n"
    "ds_read_b64_tr_b16 v[154:155], v64 offset:8192\n"
    "ds_read_b64_tr_b16 v[158:159], v63 offset:8192\n"
    "ds_read_b64_tr_b16 v[246:247], v62 offset:8192\n"
    "ds_read_b64_tr_b16 v[250:251], v61 offset:8192\n"
    "ds_read_b64_tr_b16 v[254:255], v60 offset:8192\n"
    "s_waitcnt lgkmcnt(0)\n"
    "s_barrier\n"
    "s_waitcnt vmcnt(0)\n"
    "v_add_u32 v20, 1, v20\n"
    "v_add_u32 v21, 1, v21\n"
    "s_mov_b32 s52, 1048576\n"
    ".Lwgo_a%=:\n"
    "v_cmp_ne_u64 vcc, v[20:21], v[22:23]\n"
    "s_cbranch_vccz .Lwgo_x%=\n"
    "global_load_dwordx2 v[22:23], v6, %1 sc1\n"
    "s_sub_u32 s52, s52, 1\n"
    "s_waitcnt vmcnt(0)\n"
    "s_cbranch_scc0 .Lwgo_a%=\n"
    ".Lwgo_x%=:\n"
    "v_mbcnt_lo_u32_b32 v20, -1, 0\n"
    "v_mbcnt_hi_u32_b32 v20, -1, v20\n"
    "v_min_u32 v6, 7, v20\n"
    "v_lshlrev_b32 v6, 7, v6\n"
    "v_mov_b32 v7, 0x26c00\n"
    "ds_read_b128 v[20:23], v7\n"
    "s_waitcnt lgkmcnt(0)\n"
    "v_readfirstlane_b32 s49, v21\n"
    "v_add_u32 v20, v20, v6\n"
    "s_branch .Lwchk_%=\n"
    ".Lwgo_%=:\n"
    "s_waitcnt vmcnt(1)\n"
    "v_mfma_f32_16x16x32_bf16 v[8:11], a[0:3], v[64:67], 0\n"
    "v_add_u32 v7, 32, v7\n"
    "ds_read_b128 v[20:23], v7\n"
    "v_mfma_f32_16x16x32_bf16 v[12:15], a[4:7], v[64:67], 0\n"
    "ds_write_b128 v1, v[64:67]\n"
    "ds_write_b128 v1, v[68:71] offset:1024\n"
    "v_mfma_f32_16x16x32_bf16 v[8:11], a[12:15], v[68:71], v[8:11]\n"
    "ds_write_b128 v1, v[72:75] offset:2048\n"
    "ds_write_b128 v1, v[76:79] offset:3072\n"
    "v_mfma_f32_16x16x32_bf16 v[12:15], a[16:19], v[68:71], v[12:15]\n"
    "v_cvt_pk_bf16_f32 v28, v16, v17\n"
    "v_cvt_pk_bf16_f32 v30, v16, v17\n"
    "v_mfma_f32_16x16x32_bf16 v[8:11], a[24:27], v[72:75], v[8:11]\n"
    "v_cvt_pk_bf16_f32 v29, v18, v19\n"
    "v_cvt_pk_bf16_f32 v31, v18, v19\n"
    "v_mfma_f32_16x16x32_bf16 v[12:15], a[28:31], v[72:75], v[12:15]\n"
    "ds_write_b128 v1, v[80:83] offset:4096\n"
    "ds_write_b128 v1, v[84:87] offset:5120\n"
    "v_mfma_f32_16x16x32_bf16 v[8:11], a[36:39], v[76:79], v[8:11]\n"
    "v_permlane16_swap_b32 v28, v30\n"
    "v_permlane16_swap_b32 v29, v31\n"
    "v_mfma_f32_16x16x32_bf16 v[12:15], a[40:43], v[76:79], v[12:15]\n"
    "ds_write_b128 v1, v[88:91] offset:6144\n"
    "ds_write_b128 v1, v[92:95] offset:7168\n"
    "v_mfma_f32_16x16x32_bf16 v[8:11], a[48:51], v[80:83], v[8:11]\n"
    "buffer_store_dwordx4 v[28:31], v3, s[40:43], s62 offen sc1\n"
    "v_xor_b32 v0, 0x2000, v1\n"
    "v_mfma_f32_16x16x32_bf16 v[12:15], a[52:55], v[80:83], v[12:15]\n"
    "s_waitcnt lgkmcnt(8)\n"
    "v_readfirstlane_b32 s49, v21\n"
    "v_mfma_f32_16x16x32_bf16 v[8:11], a[60:63], v[84:87], v[8:11]\n"
    "v_readfirstlane_b32 s62, v22\n"
    "v_add_u32 v20, v20, v6\n"
    "v_mfma_f32_16x16x32_bf16 v[12:15], a[64:67], v[84:87], v[12:15]\n"
    ".Lwtop_%=:\n"
    "v_mfma_f32_16x16x32_bf16 v[16:19], a[8:11], v[64:67], 0\n"
    "s_waitcnt lgkmcnt(0)\n"
    "s_barrier\n"
    "v_mfma_f32_16x16x32_bf16 v[16:19], a[20:23], v[68:71], v[16:19]\n"
    "ds_read_b128 v[32:35], v0\n"
    "ds_read_b128 v[36:39], v0 offset:1024\n"
    "v_mfma_f32_16x16x32_bf16 v[16:19], a[32:35], v[72:75], v[16:19]\n"
    "ds_read_b128 v[44:47], v0 offset:3072\n"
    "ds_read_b128 v[40:43], v0 offset:2048\n"
    "v_mfma_f32_16x16x32_bf16 v[16:19], a[44:47], v[76:79], v[16:19]\n"
    "ds_read_b128 v[52:55], v0 offset:5120\n"
    "ds_read_b128 v[48:51], v0 offset:4096\n"
    "v_mfma_f32_16x16x32_bf16 v[16:19], a[56:59], v[80:83], v[16:19]\n"
    "ds_read_b128 v[60:63], v0 offset:7168\n"
    "ds_read_b128 v[56:59], v0 offset:6144\n"
    "v_mfma_f32_16x16x32_bf16 v[16:19], a[68:71], v[84:87], v[16:19]\n"
    "global_load_dword v21, v20, %1 sc1\n"
    "v_mfma_f32_16x16x32_bf16 v[8:11], a[72:75], v[88:91], v[8:11]\n"
    "v_mfma_f32_16x16x32_bf16 v[12:15], a[76:79], v[88:91], v[12:15]\n"
    "v_mfma_f32_16x16x32_bf16 v[16:19], a[80:83], v[88:91], v[16:19]\n"
    "v_mfma_f32_16x16x32_bf16 v[8:11], a[84:87], v[92:95], v[8:11]\n"
    "v_mfma_f32_16x16x32_bf16 v[12:15], a[88:91], v[92:95], v[12:15]\n"
    "v_mfma_f32_16x16x32_bf16 v[16:19], a[92:95], v[92:95], v[16:19]\n"
    "s_waitcnt lgkmcnt(7)\n"
    "v_mfma_f32_16x16x32_bf16 v[8:11], a[96:99], v[32:35], v[8:11]\n"
    "v_mfma_f32_16x16x32_bf16 v[12:15], a[100:103], v[32:35], v[12:15]\n"
    "v_mfma_f32_16x16x32_bf16 v[16:19], a[104:107], v[32:35], v[16:19]\n"
    "s_waitcnt lgkmcnt(6)\n"
    "v_mfma_f32_16x16x32_bf16 v[8:11], a[108:111], v[36:39], v[8:11]\n"
    "v_mfma_f32_16x16x32_bf16 v[12:15], a[112:115], v[36:39], v[12:15]\n"
    "v_xor_b32 v0, 0x4000, v1\n"
    "v_mfma_f32_16x16x32_bf16 v[16:19], a[116:119], v[36:39], v[16:19]\n"
    "s_waitcnt lgkmcnt(4)\n"
    "v_mfma_f32_16x16x32_bf16 v[8:11], a[120:123], v[40:43], v[8:11]\n"
    "ds_read_b128 v[36:39], v0 offset:1024\n"
    "ds_read_b128 v[32:35], v0\n"
    "v_mfma_f32_16x16x32_bf16 v[12:15], a[124:127], v[40:43], v[12:15]\n"
    "v_mfma_f32_16x16x32_bf16 v[16:19], a[128:131], v[40:43], v[16:19]\n"
    "v_mfma_f32_16x16x32_bf16 v[8:11], a[132:135], v[44:47], v[8:11]\n"
    "v_mfma_f32_16x16x32_bf16 v[12:15], a[136:139], v[44:47], v[12:15]\n"
    "v_mfma_f32_16x16x32_bf16 v[16:19], a[140:143], v[44:47], v[16:19]\n"
    "s_waitcnt lgkmcnt(4)\n"
    "v_mfma_f32_16x16x32_bf16 v[8:11], a[144:147], v[48:51], v[8:11]\n"
    "ds_read_b128 v[44:47], v0 offset:3072\n"
    "ds_read_b128 v[40:43], v0 offset:2048\n"
    "v_mfma_f32_16x16x32_bf16 v[12:15], a[148:151], v[48:51], v[12:15]\n"
    "v_mfma_f32_16x16x32_bf16 v[16:19], a[152:155], v[48:51], v[16:19]\n"
    "v_mfma_f32_16x16x32_bf16 v[8:11], a[156:159], v[52:55], v[8:11]\n"
    "v_mfma_f32_16x16x32_bf16 v[12:15], a[160:163], v[52:55], v[12:15]\n"
    "v_mfma_f32_16x16x32_bf16 v[16:19], a[164:167], v[52:55], v[16:19]\n"
    "s_waitcnt lgkmcnt(4)\n"
    "v_mfma_f32_16x16x32_bf16 v[8:11], a[168:171], v[56:59], v[8:11]\n"
    "ds_read_b128 v[52:55], v0 offset:5120\n"
    "ds_read_b128 v[48:51], v0 offset:4096\n"
    "v_mfma_f32_16x16x32_bf16 v[12:15], a[172:175], v[56:59], v[12:15]\n"
    "v_mfma_f32_16x16x32_bf16 v[16:19], a[176:179], v[56:59], v[16:19]\n"
    "s_waitcnt vmcnt(0)\n"
    "v_mov_b32 v22, 1\n"
    "v_mfma_f32_16x16x32_bf16 v[8:11], a[180:183], v[60:63], v[8:11]\n"
    "s_mov_b64 exec, s[56:57]\n"
    "ds_add_rtn_u32 v22, v7, v22 offset:16\n"
    "s_mov_b64 exec, -1\n"
    "v_mfma_f32_16x16x32_bf16 v[12:15], a[184:187], v[60:63], v[12:15]\n"
    "v_cmp_eq_u32 vcc, 0, v21\n"
    "s_cmp_eq_u64 vcc, 0\n"
    "v_mfma_f32_16x16x32_bf16 v[16:19], a[188:191], v[60:63], v[16:19]\n"
    "s_waitcnt lgkmcnt(4)\n"
    "v_mfma_f32_16x16x32_bf16 v[8:11], a[192:195], v[32:35], v[8:11]\n"
    "ds_read_b128 v[60:63], v0 offset:7168\n"
    "ds_read_b128 v[56:59], v0 offset:6144\n"
    "v_mfma_f32_16x16x32_bf16 v[12:15], a[196:199], v[32:35], v[12:15]\n"
    "s_cselect_b32 s94, s42, 0\n"
    "v_mfma_f32_16x16x32_bf16 v[16:19], a[200:203], v[32:35], v[16:19]\n"
    "global_load_dword v21, v20, %1 sc1\n"
    "v_mfma_f32_16x16x32_bf16 v[8:11], a[204:207], v[36:39], v[8:11]\n"
    "buffer_load_dwordx4 v[64:67], v4, s[92:95], s49 offen sc1\n"
    "v_mfma_f32_16x16x32_bf16 v[12:15], a[208:211], v[36:39], v[12:15]\n"
    "v_xor_b32 v0, 0x6000, v1\n"
    "buffer_load_dwordx4 v[68:71], v4, s[92:95], s49 offen offset:1024 sc1\n"
    "v_mfma_f32_16x16x32_bf16 v[16:19], a[212:215], v[36:39], v[16:19]\n"
    "s_waitcnt lgkmcnt(4)\n"
    "v_mfma_f32_16x16x32_bf16 v[8:11], a[216:219], v[40:43], v[8:11]\n"
    "ds_read_b128 v[36:39], v0 offset:1024\n"
    "ds_read_b128 v[32:35], v0\n"
    "v_mfma_f32_16x16x32_bf16 v[12:15], a[220:223], v[40:43], v[12:15]\n"
    "s_waitcnt lgkmcnt(4)\n"
    "v_readfirstlane_b32 s50, v22\n"
    "v_mfma_f32_16x16x32_bf16 v[16:19], a[224:227], v[40:43], v[16:19]\n"
    "buffer_load_dwordx4 v[72:75], v4, s[92:95], s49 offen offset:2048 sc1\n"
    "s_cmp_eq_u32 s50, 3\n"
    "v_mfma_f32_16x16x32_bf16 v[8:11], a[228:231], v[44:47], v[8:11]\n"
    "s_cselect_b64 exec, 1, 0\n"
    "global_store_dword v23, v22, %1 sc1\n"
    "s_mov_b64 exec, -1\n"
    "v_mfma_f32_16x16x32_bf16 v[12:15], a[232:235], v[44:47], v[12:15]\n"
    "buffer_load_dwordx4 v[76:79], v4, s[92:95], s49 offen offset:3072 sc1\n"
    "v_mfma_f32_16x16x32_bf16 v[16:19], a[236:239], v[44:47], v[16:19]\n"
    "buffer_load_dwordx4 v[80:83], v5, s[92:95], s49 offen sc1\n"
    "v_mfma_f32_16x16x32_bf16 v[8:11], a[240:243], v[48:51], v[8:11]\n"
    "ds_read_b128 v[44:47], v0 offset:3072\n"
    "ds_read_b128 v[40:43], v0 offset:2048\n"
    "v_mfma_f32_16x16x32_bf16 v[12:15], a[244:247], v[48:51], v[12:15]\n"
    "buffer_load_dwordx4 v[84:87], v5, s[92:95], s49 offen offset:1024 sc1\n"
    "v_mfma_f32_16x16x32_bf16 v[16:19], a[248:251], v[48:51], v[16:19]\n"
    "buffer_load_dwordx4 v[88:91], v5, s[92:95], s49 offen offset:2048 sc1\n"
    "v_mfma_f32_16x16x32_bf16 v[8:11], a[252:255], v[52:55], v[8:11]\n"
    "buffer_load_dwordx4 v[92:95], v5, s[92:95], s49 offen offset:3072 sc1\n"
    "v_mfma_f32_16x16x32_bf16 v[12:15], v[128:131], v[52:55], v[12:15]\n"
    "v_mfma_f32_16x16x32_bf16 v[16:19], v[132:135], v[52:55], v[16:19]\n"
    "s_waitcnt lgkmcnt(4)\n"
    "v_mfma_f32_16x16x32_bf16 v[8:11], v[136:139], v[56:59], v[8:11]\n"
    "ds_read_b128 v[52:55], v0 offset:5120\n"
    "ds_read_b128 v[48:51], v0 offset:4096\n"
    "v_mfma_f32_16x16x32_bf16 v[12:15], v[140:143], v[56:59], v[12:15]\n"
    "v_mfma_f32_16x16x32_bf16 v[16:19], v[144:147], v[56:59], v[16:19]\n"
    "v_mfma_f32_16x16x32_bf16 v[8:11], v[148:151], v[60:63], v[8:11]\n"
    "v_mfma_f32_16x16x32_bf16 v[12:15], v[152:155], v[60:63], v[12:15]\n"
    "v_mfma_f32_16x16x32_bf16 v[16:19], v[156:159], v[60:63], v[16:19]\n"
    "s_waitcnt lgkmcnt(4)\n"
    "v_mfma_f32_16x16x32_bf16 v[8:11], v[160:163], v[32:35], v[8:11]\n"
    "ds_read_b128 v[60:63], v0 offset:7168\n"
    "ds_read_b128 v[56:59], v0 offset:6144\n"
    "v_mfma_f32_16x16x32_bf16 v[12:15], v[164:167], v[32:35], v[12:15]\n"
    "s_cmp_eq_u32 s94, 0\n"
    "s_cbranch_scc1 .Lwk1_%=\n"
    ".Lwk1_x%=:\n"
    "v_mfma_f32_16x16x32_bf16 v[16:19], v[168:171], v[32:35], v[16:19]\n"
    "v_mfma_f32_16x16x32_bf16 v[8:11], v[172:175], v[36:39], v[8:11]\n"
    "v_mfma_f32_16x16x32_bf16 v[12:15], v[176:179], v[36:39], v[12:15]\n"
    "v_mfma_f32_16x16x32_bf16 v[16:19], v[180:183], v[36:39], v[16:19]\n"
    "s_waitcnt lgkmcnt(4)\n"
    "v_mfma_f32_16x16x32_bf16 v[8:11], v[184:187], v[40:43], v[8:11]\n"
    "v_mfma_f32_16x16x32_bf16 v[12:15], v[188:191], v[40:43], v[12:15]\n"
    "v_mfma_f32_16x16x32_bf16 v[16:19], v[192:195], v[40:43], v[16:19]\n"
    "v_mfma_f32_16x16x32_bf16 v[8:11], v[196:199], v[44:47], v[8:11]\n"
    "v_xor_b32 v1, 0x8000, v1\n"
    "v_mfma_f32_16x16x32_bf16 v[12:15], v[200:203], v[44:47], v[12:15]\n"
    "s_waitcnt lgkmcnt(2)\n"
    "v_mfma_f32_16x16x32_bf16 v[8:11], v[208:211], v[48:51], v[8:11]\n"
    "v_mfma_f32_16x16x32_bf16 v[12:15], v[212:215], v[48:51], v[12:15]\n"
    "v_mfma_f32_16x16x32_bf16 v[8:11], v[220:223], v[52:55], v[8:11]\n"
    "v_mfma_f32_16x16x32_bf16 v[12:15], v[224:227], v[52:55], v[12:15]\n"
    "s_waitcnt lgkmcnt(0)\n"
    "v_mfma_f32_16x16x32_bf16 v[8:11], v[232:235], v[56:59], v[8:11]\n"
    "v_mfma_f32_16x16x32_bf16 v[12:15], v[236:239], v[56:59], v[12:15]\n"
    "v_mfma_f32_16x16x32_bf16 v[8:11], v[244:247], v[60:63], v[8:11]\n"
    "v_mfma_f32_16x16x32_bf16 v[12:15], v[248:251], v[60:63], v[12:15]\n"
    "v_mfma_f32_16x16x32_bf16 v[16:19], v[204:207], v[44:47], v[16:19]\n"
    "v_mfma_f32_16x16x32_bf16 v[16:19], v[216:219], v[48:51], v[16:19]\n"
    "v_cvt_pk_bf16_f32 v24, v8, v9\n"
    "v_cvt_pk_bf16_f32 v25, v10, v11\n"
    "v_mfma_f32_16x16x32_bf16 v[16:19], v[228:231], v[52:55], v[16:19]\n"
    "v_cvt_pk_bf16_f32 v26, v12, v13\n"
    "v_cvt_pk_bf16_f32 v27, v14, v15\n"
    "v_mfma_f32_16x16x32_bf16 v[16:19], v[240:243], v[56:59], v[16:19]\n"
    "buffer_store_dwordx4 v[24:27], v2, s[40:43], s62 offen sc1\n"
    "v_mfma_f32_16x16x32_bf16 v[16:19], v[252:255], v[60:63], v[16:19]\n"
    "s_mov_b32 s56, 1\n"
    "s_cmp_lg_u32 s94, 0\n"
    "s_cbranch_scc1 .Lwgo_%=\n"
    "s_nop 5\n"
    "v_cvt_pk_bf16_f32 v28, v16, v17\n"
    "v_cvt_pk_bf16_f32 v30, v16, v17\n"
    "v_cvt_pk_bf16_f32 v29, v18, v19\n"
    "v_cvt_pk_bf16_f32 v31, v18, v19\n"
    "v_permlane16_swap_b32 v28, v30\n"
    "s_nop 0\n"
    "v_permlane16_swap_b32 v29, v31\n"
    "buffer_store_dwordx4 v[28:31], v3, s[40:43], s62 offen sc1\n"
    "s_waitcnt vmcnt(2)\n"
    "v_cmp_eq_u32 vcc, 0, v21\n"
    "s_cmp_eq_u64 vcc, 0\n"
    "s_cbranch_scc1 .Lwld_%=\n"
    "ds_read_b32 v23, v7 offset:44\n"
    "global_load_dword v21, v20, %1 sc1\n"
    "s_waitcnt vmcnt(0)\n"
    "v_mov_b32 v22, 1\n"
    "s_mov_b64 exec, 1\n"
    "ds_add_rtn_u32 v22, v7, v22 offset:48\n"
    "s_mov_b64 exec, -1\n"
    "s_waitcnt lgkmcnt(0)\n"
    "v_readfirstlane_b32 s50, v22\n"
    "s_cmp_eq_u32 s50, 3\n"
    "s_cselect_b64 exec, 1, 0\n"
    "global_store_dword v23, v22, %1 sc1\n"
    "s_mov_b64 exec, -1\n"
    "s_mov_b32 s56, 0\n"
    "v_cmp_eq_u32 vcc, 0, v21\n"
    "s_cmp_eq_u64 vcc, 0\n"
    "s_cbranch_scc1 .Lwld_%=\n"
    ".Lwchk_%=:\n"
    "v_readfirstlane_b32 s50, v20\n"
    "s_cmp_lt_u32 s50, 200\n"
    "s_cbranch_scc1 .Lwend_%=\n"
    ".Lwwait_%=:\n"
    "global_load_dword v21, v20, %1 sc1\n"
    "s_mov_b32 s52, 0\n"
    ".Lwp2_%=:\n"
    "s_waitcnt vmcnt(0)\n"
    "v_cmp_eq_u32 vcc, 0, v21\n"
    "s_cmp_eq_u64 vcc, 0\n"
    "s_cbranch_scc1 .Lwp2_x%=\n"
    "s_add_u32 s52, s52, 1\n"
    "s_sleep 2\n"
    "global_load_dword v21, v20, %1 sc1\n"
    "s_cmp_lt_u32 s52, s53\n"
    "s_cbranch_scc1 .Lwp2_%=\n"
    "s_mov_b32 s53, 0\n"
    "v_mov_b32 v22, 0\n"
    "v_mov_b32 v23, 1\n"
    "global_store_dword v22, v23, %1 sc1\n"
    "s_waitcnt vmcnt(0)\n"
    ".Lwp2_x%=:\n"
    ".Lwld_%=:\n"
    "s_mov_b32 s94, s42\n"
    "buffer_load_dwordx4 v[64:67], v4, s[92:95], s49 offen sc1\n"
    "buffer_load_dwordx4 v[68:71], v4, s[92:95], s49 offen offset:1024 sc1\n"
    "buffer_load_dwordx4 v[72:75], v4, s[92:95], s49 offen offset:2048 sc1\n"
    "buffer_load_dwordx4 v[76:79], v4, s[92:95], s49 offen offset:3072 sc1\n"
    "buffer_load_dwordx4 v[80:83], v5, s[92:95], s49 offen sc1\n"
    "buffer_load_dwordx4 v[84:87], v5, s[92:95], s49 offen offset:1024 sc1\n"
    "buffer_load_dwordx4 v[88:91], v5, s[92:95], s49 offen offset:2048 sc1\n"
    "buffer_load_dwordx4 v[92:95], v5, s[92:95], s49 offen offset:3072 sc1\n"
    ".Lwin_%=:\n"
    "v_add_u32 v7, 32, v7\n"
    "ds_read_b128 v[20:23], v7\n"
    "s_waitcnt vmcnt(7)\n"
    "v_mfma_f32_16x16x32_bf16 v[8:11], a[0:3], v[64:67], 0\n"
    "v_mfma_f32_16x16x32_bf16 v[12:15], a[4:7], v[64:67], 0\n"
    "ds_write_b128 v1, v[64:67]\n"
    "s_waitcnt vmcnt(6)\n"
    "ds_write_b128 v1, v[68:71] offset:1024\n"
    "v_mfma_f32_16x16x32_bf16 v[8:11], a[12:15], v[68:71], v[8:11]\n"
    "s_waitcnt vmcnt(5)\n"
    "ds_write_b128 v1, v[72:75] offset:2048\n"
    "s_waitcnt vmcnt(4)\n"
    "ds_write_b128 v1, v[76:79] offset:3072\n"
    "v_mfma_f32_16x16x32_bf16 v[12:15], a[16:19], v[68:71], v[12:15]\n"
    "v_mfma_f32_16x16x32_bf16 v[8:11], a[24:27], v[72:75], v[8:11]\n"
    "v_mfma_f32_16x16x32_bf16 v[12:15], a[28:31], v[72:75], v[12:15]\n"
    "s_waitcnt vmcnt(3)\n"
    "ds_write_b128 v1, v[80:83] offset:4096\n"
    "s_waitcnt vmcnt(2)\n"
    "ds_write_b128 v1, v[84:87] offset:5120\n"
    "v_mfma_f32_16x16x32_bf16 v[8:11], a[36:39], v[76:79], v[8:11]\n"
    "v_mfma_f32_16x16x32_bf16 v[12:15], a[40:43], v[76:79], v[12:15]\n"
    "s_waitcnt vmcnt(1)\n"
    "ds_write_b128 v1, v[88:91] offset:6144\n"
    "s_waitcnt vmcnt(0)\n"
    "ds_write_b128 v1, v[92:95] offset:7168\n"
    "v_mfma_f32_16x16x32_bf16 v[8:11], a[48:51], v[80:83], v[8:11]\n"
    "v_xor_b32 v0, 0x2000, v1\n"
    "v_mfma_f32_16x16x32_bf16 v[12:15], a[52:55], v[80:83], v[12:15]\n"
    "s_waitcnt lgkmcnt(8)\n"
    "v_readfirstlane_b32 s49, v21\n"
    "v_mfma_f32_16x16x32_bf16 v[8:11], a[60:63], v[84:87], v[8:11]\n"
    "v_readfirstlane_b32 s62, v22\n"
    "v_add_u32 v20, v20, v6\n"
    "v_mfma_f32_16x16x32_bf16 v[12:15], a[64:67], v[84:87], v[12:15]\n"
    "s_cmp_eq_u32 s56, 0\n"
    "s_cbranch_scc1 .Lwc3_%=\n"
    "s_waitcnt vmcnt(0)\n"
    "v_mov_b32 v22, 1\n"
    "s_mov_b64 exec, 1\n"
    "ds_add_rtn_u32 v22, v7, v22 offset:16\n"
    "s_mov_b64 exec, -1\n"
    "s_waitcnt lgkmcnt(0)\n"
    "v_readfirstlane_b32 s50, v22\n"
    "s_cmp_eq_u32 s50, 3\n"
    "s_cselect_b64 exec, 1, 0\n"
    "global_store_dword v23, v22, %1 sc1\n"
    "s_mov_b64 exec, -1\n"
    "s_mov_b32 s56, 0\n"
    ".Lwc3_%=:\n"
    "s_branch .Lwtop_%=\n"
    ".Lwk1_%=:\n"
    "s_waitcnt vmcnt(8)\n"
    "v_cmp_eq_u32 vcc, 0, v21\n"
    "s_cmp_eq_u64 vcc, 0\n"
    "s_cselect_b32 s94, s42, 0\n"
    "global_load_dword v21, v20, %1 sc1\n"
    "buffer_load_dwordx4 v[64:67], v4, s[92:95], s49 offen sc1\n"
    "buffer_load_dwordx4 v[68:71], v4, s[92:95], s49 offen offset:1024 sc1\n"
    "buffer_load_dwordx4 v[72:75], v4, s[92:95], s49 offen offset:2048 sc1\n"
    "buffer_load_dwordx4 v[76:79], v4, s[92:95], s49 offen offset:3072 sc1\n"
    "buffer_load_dwordx4 v[80:83], v5, s[92:95], s49 offen sc1\n"
    "buffer_load_dwordx4 v[84:87], v5, s[92:95], s49 offen offset:1024 sc1\n"
    "buffer_load_dwordx4 v[88:91], v5, s[92:95], s49 offen offset:2048 sc1\n"
    "buffer_load_dwordx4 v[92:95], v5, s[92:95], s49 offen offset:3072 sc1\n"
    "s_branch .Lwk1_x%=\n"
    ".Lwpol_%=:\n"
    "v_mov_b32 v20, 64\n"
    "global_load_dword v21, v20, %1 sc1\n"
    "s_mov_b32 s52, 0\n"
    ".Lwp4_%=:\n"
    "s_waitcnt vmcnt(0)\n"
    "v_cmp_eq_u32 vcc, 0, v21\n"
    "s_cmp_eq_u64 vcc, 0\n"
    "s_cbranch_scc1 .Lwp4_x%=\n"
    "s_add_u32 s52, s52, 1\n"
    "s_sleep 2\n"
    "global_load_dword v21, v20, %1 sc1\n"
    "s_cmp_lt_u32 s52, s53\n"
    "s_cbranch_scc1 .Lwp4_%=\n"
    "s_mov_b32 s53, 0\n"
    "v_mov_b32 v22, 0\n"
    "v_mov_b32 v23, 1\n"
    "global_store_dword v22, v23, %1 sc1\n"
    "s_waitcnt vmcnt(0)\n"
    ".Lwp4_x%=:\n"
    "v_readfirstlane_b32 s50, v21\n"
    "v_mbcnt_lo_u32_b32 v20, -1, 0\n"
    "v_mbcnt_hi_u32_b32 v20, -1, v20\n"
    "s_lshl_b32 s51, %2, 6\n"
    "v_add_u32 v20, s51, v20\n"
    "v_min_u32 v20, 129, v20\n"
    "v_lshlrev_b32 v20, 5, v20\n"
    "v_add_u32 v20, 0x26c00, v20\n"
    "ds_read2_b32 v[22:23], v20 offset0:1 offset1:2\n"
    "s_and_b32 s50, s50, 1\n"
    "s_waitcnt lgkmcnt(0)\n"
    "v_and_b32 v21, s50, v22\n"
    "v_lshlrev_b32 v21, 13, v21\n"
    "v_and_b32 v22, -2, v22\n"
    "v_xor_b32 v22, v21, v22\n"
    "v_and_b32 v21, s50, v23\n"
    "v_lshlrev_b32 v21, 13, v21\n"
    "v_and_b32 v23, -2, v23\n"
    "v_xor_b32 v23, v21, v23\n"
    "ds_write2_b32 v20, v22, v23 offset0:1 offset1:2\n"
    "s_waitcnt lgkmcnt(0)\n"
    "s_barrier\n"
    "ds_read_b32 v21, v7 offset:4\n"
    "v_add_u32 v20, %4, v6\n"
    "s_waitcnt lgkmcnt(0)\n"
    "v_readfirstlane_b32 s49, v21\n"
    "s_branch .Lwwait_%=\n"
    ".Lwend_%=:\n"
    "s_cmp_eq_u32 s50, 192\n"
    "s_cbranch_scc0 .Lwpol_%=\n"
    "s_waitcnt vmcnt(0) lgkmcnt(0)\n"
    "s_endpgm\n"
    ".p2align 6\n"
    ".Lrole3_%=:\n"
    "s_mov_b64 s[40:41], %0\n"
    "s_and_b32 s41, s41, 0xffff\n"
    "s_mov_b32 s42, 0xc000000\n"
    "s_mov_b32 s43, 0x20000\n"
    "s_mov_b64 s[92:93], %0\n"
    "s_and_b32 s93, s93, 0xffff\n"
    "s_mov_b32 s94, 0xc000000\n"
    "s_mov_b32 s95, 0x20000\n"
    "s_mov_b32 s53, 131072\n"
    "s_mov_b64 s[56:57], 0\n"
    "v_mbcnt_lo_u32_b32 v92, -1, 0\n"
    "v_mbcnt_hi_u32_b32 v92, -1, v92\n"
    "s_lshl_b32 s50, %2, 8\n"
    "s_add_u32 s50, s50, 144384\n"
    "v_lshl_add_u32 v92, v92, 2, s50\n"
    "ds_read_b32 v0, v92\n"
    "ds_read_b32 v1, v92 offset:1024\n"
    "ds_read_b32 v2, v92 offset:2048\n"
    "ds_read_b32 v3, v92 offset:3072\n"
    "ds_read_b32 v4, v92 offset:4096\n"
    "ds_read_b32 v73, v92 offset:5120\n"
    "ds_read_b32 v6, v92 offset:6144\n"
    "ds_read_b32 v7, v92 offset:7168\n"
    "ds_read_b32 v8, v92 offset:8192\n"
    "ds_read_b32 v9, v92 offset:9216\n"
    "ds_read_b32 v10, v92 offset:10240\n"
    "ds_read_b32 v95, v92 offset:11264\n"
    "ds_read_b32 v90, v92 offset:12288\n"
    "ds_read_b32 v91, v92 offset:13312\n"
    "s_waitcnt lgkmcnt(0)\n"
    "s_barrier\n"
    "v_mbcnt_lo_u32_b32 v92, -1, 0\n"
    "v_mbcnt_hi_u32_b32 v92, -1, v92\n"
    "v_add_u32 v93, 0, v92\n"
    "v_mul_u32_u24 v94, 7282, v93\n"
    "v_lshrrev_b32 v94, 16, v94\n"
    "v_mul_u32_u24 v94, 3312, v94\n"
    "v_lshl_add_u32 v80, v93, 4, v94\n"
    "v_add_u32 v93, 64, v92\n"
    "v_mul_u32_u24 v94, 7282, v93\n"
    "v_lshrrev_b32 v94, 16, v94\n"
    "v_mul_u32_u24 v94, 3312, v94\n"
    "v_lshl_add_u32 v81, v93, 4, v94\n"
    "v_add_u32 v93, 128, v92\n"
    "v_mul_u32_u24 v94, 7282, v93\n"
    "v_lshrrev_b32 v94, 16, v94\n"
    "v_mul_u32_u24 v94, 3312, v94\n"
    "v_lshl_add_u32 v82, v93, 4, v94\n"
    "v_add_u32 v93, 192, v92\n"
    "v_mul_u32_u24 v94, 7282, v93\n"
    "v_lshrrev_b32 v94, 16, v94\n"
    "v_mul_u32_u24 v94, 3312, v94\n"
    "v_lshl_add_u32 v83, v93, 4, v94\n"
    "v_add_u32 v93, 256, v92\n"
    "v_mul_u32_u24 v94, 7282, v93\n"
    "v_lshrrev_b32 v94, 16, v94\n"
    "v_mul_u32_u24 v94, 3312, v94\n"
    "v_lshl_add_u32 v84, v93, 4, v94\n"
    "v_add_u32 v93, 320, v92\n"
    "v_mul_u32_u24 v94, 7282, v93\n"
    "v_lshrrev_b32 v94, 16, v94\n"
    "v_mul_u32_u24 v94, 3312, v94\n"
    "v_lshl_add_u32 v85, v93, 4, v94\n"
    "v_add_u32 v93, 384, v92\n"
    "v_mul_u32_u24 v94, 7282, v93\n"
    "v_lshrrev_b32 v94, 16, v94\n"
    "v_mul_u32_u24 v94, 3312, v94\n"
    "v_lshl_add_u32 v86, v93, 4, v94\n"
    "v_add_u32 v93, 448, v92\n"
    "v_mul_u32_u24 v94, 7282, v93\n"
    "v_lshrrev_b32 v94, 16, v94\n"
    "v_mul_u32_u24 v94, 3312, v94\n"
    "v_lshl_add_u32 v87, v93, 4, v94\n"
    "v_add_u32 v93, 512, v92\n"
    "v_mul_u32_u24 v94, 7282, v93\n"
    "v_lshrrev_b32 v94, 16, v94\n"
    "v_mul_u32_u24 v94, 3312, v94\n"
    "v_lshl_add_u32 v88, v93, 4, v94\n"
    "v_and_b32 v72, 48, v92\n"
    "v_add_u32 v5, 0x21c00, v73\n"
    "s_min_u32 s50, %2, 2\n"
    "s_mul_i32 s54, s50, 9216\n"
    "s_mul_i32 s50, s50, 1152\n"
    "s_add_u32 s51, s50, %6\n"
    "s_mov_b64 s[44:45], %8\n"
    "s_and_b32 s45, s45, 0xffff\n"
    "s_mov_b32 s46, 0x1e6000\n"
    "s_mov_b32 s47, 0x20000\n"
    "v_mov_b32 v56, 0x3f803f80\n"
    "v_mov_b32 v57, 0x3f803f80\n"
    "v_mov_b32 v58, 0x3f803f80\n"
    "v_mov_b32 v59, 0x3f803f80\n"
    "s_cmp_eq_u32 %2, 3\n"
    "s_cbranch_scc1 .Lac1_%=\n"
    "s_add_u32 s55, s51, 0\n"
    "s_add_u32 m0, s54, 0\n"
    "s_nop 0\n"
    "buffer_load_dwordx4 v80, s[44:47], s55 offen lds\n"
    "s_add_u32 m0, s54, 1024\n"
    "s_nop 0\n"
    "buffer_load_dwordx4 v81, s[44:47], s55 offen lds\n"
    "s_add_u32 m0, s54, 2048\n"
    "s_nop 0\n"
    "buffer_load_dwordx4 v82, s[44:47], s55 offen lds\n"
    "s_add_u32 m0, s54, 3072\n"
    "s_nop 0\n"
    "buffer_load_dwordx4 v83, s[44:47], s55 offen lds\n"
    "s_add_u32 m0, s54, 4096\n"
    "s_nop 0\n"
    "buffer_load_dwordx4 v84, s[44:47], s55 offen lds\n"
    "s_add_u32 m0, s54, 5120\n"
    "s_nop 0\n"
    "buffer_load_dwordx4 v85, s[44:47], s55 offen lds\n"
    "s_add_u32 m0, s54, 6144\n"
    "s_nop 0\n"
    "buffer_load_dwordx4 v86, s[44:47], s55 offen lds\n"
    "s_add_u32 m0, s54, 7168\n"
    "s_nop 0\n"
    "buffer_load_dwordx4 v87, s[44:47], s55 offen lds\n"
    "s_add_u32 m0, s54, 8192\n"
    "s_nop 0\n"
    "buffer_load_dwordx4 v88, s[44:47], s55 offen lds\n"
    ".Lac1_%=:\n"
    "s_cmp_eq_u32 %2, 3\n"
    "s_cbranch_scc1 .Lac2_%=\n"
    "s_add_u32 s55, s51, 221184\n"
    "s_add_u32 m0, s54, 27648\n"
    "s_nop 0\n"
    "buffer_load_dwordx4 v80, s[44:47], s55 offen lds\n"
    "s_add_u32 m0, s54, 28672\n"
    "s_nop 0\n"
    "buffer_load_dwordx4 v81, s[44:47], s55 offen lds\n"
    "s_add_u32 m0, s54, 29696\n"
    "s_nop 0\n"
    "buffer_load_dwordx4 v82, s[44:47], s55 offen lds\n"
    "s_add_u32 m0, s54, 30720\n"
    "s_nop 0\n"
    "buffer_load_dwordx4 v83, s[44:47], s55 offen lds\n"
    "s_add_u32 m0, s54, 31744\n"
    "s_nop 0\n"
    "buffer_load_dwordx4 v84, s[44:47], s55 offen lds\n"
    "s_add_u32 m0, s54, 32768\n"
    "s_nop 0\n"
    "buffer_load_dwordx4 v85, s[44:47], s55 offen lds\n"
    "s_add_u32 m0, s54, 33792\n"
    "s_nop 0\n"
    "buffer_load_dwordx4 v86, s[44:47], s55 offen lds\n"
    "s_add_u32 m0, s54, 34816\n"
    "s_nop 0\n"
    "buffer_load_dwordx4 v87, s[44:47], s55 offen lds\n"
    "s_add_u32 m0, s54, 35840\n"
    "s_nop 0\n"
    "buffer_load_dwordx4 v88, s[44:47], s55 offen lds\n"
    ".Lac2_%=:\n"
    "s_cmp_eq_u32 %2, 3\n"
    "s_cbranch_scc1 .Lac3_%=\n"
    "s_add_u32 s55, s51, 442368\n"
    "s_add_u32 m0, s54, 55296\n"
    "s_nop 0\n"
    "buffer_load_dwordx4 v80, s[44:47], s55 offen lds\n"
    "s_add_u32 m0, s54, 56320\n"
    "s_nop 0\n"
    "buffer_load_dwordx4 v81, s[44:47], s55 offen lds\n"
    "s_add_u32 m0, s54, 57344\n"
    "s_nop 0\n"
    "buffer_load_dwordx4 v82, s[44:47], s55 offen lds\n"
    "s_add_u32 m0, s54, 58368\n"
    "s_nop 0\n"
    "buffer_load_dwordx4 v83, s[44:47], s55 offen lds\n"
    "s_add_u32 m0, s54, 59392\n"
    "s_nop 0\n"
    "buffer_load_dwordx4 v84, s[44:47], s55 offen lds\n"
    "s_add_u32 m0, s54, 60416\n"
    "s_nop 0\n"
    "buffer_load_dwordx4 v85, s[44:47], s55 offen lds\n"
    "s_add_u32 m0, s54, 61440\n"
    "s_nop 0\n"
    "buffer_load_dwordx4 v86, s[44:47], s55 offen lds\n"
    "s_add_u32 m0, s54, 62464\n"
    "s_nop 0\n"
    "buffer_load_dwordx4 v87, s[44:47], s55 offen lds\n"
    "s_add_u32 m0, s54, 63488\n"
    "s_nop 0\n"
    "buffer_load_dwordx4 v88, s[44:47], s55 offen lds\n"
    ".Lac3_%=:\n"
    "s_cmp_eq_u32 %2, 3\n"
    "s_cbranch_scc1 .Lac4_%=\n"
    "s_add_u32 s55, s51, 663552\n"
    "s_add_u32 m0, s54, 82944\n"
    "s_nop 0\n"
    "buffer_load_dwordx4 v80, s[44:47], s55 offen lds\n"
    "s_add_u32 m0, s54, 83968\n"
    "s_nop 0\n"
    "buffer_load_dwordx4 v81, s[44:47], s55 offen lds\n"
    "s_add_u32 m0, s54, 84992\n"
    "s_nop 0\n"
    "buffer_load_dwordx4 v82, s[44:47], s55 offen lds\n"
    "s_add_u32 m0, s54, 86016\n"
    "s_nop 0\n"
    "buffer_load_dwordx4 v83, s[44:47], s55 offen lds\n"
    "s_add_u32 m0, s54, 87040\n"
    "s_nop 0\n"
    "buffer_load_dwordx4 v84, s[44:47], s55 offen lds\n"
    "s_add_u32 m0, s54, 88064\n"
    "s_nop 0\n"
    "buffer_load_dwordx4 v85, s[44:47], s55 offen lds\n"
    "s_add_u32 m0, s54, 89088\n"
    "s_nop 0\n"
    "buffer_load_dwordx4 v86, s[44:47], s55 offen lds\n"
    "s_add_u32 m0, s54, 90112\n"
    "s_nop 0\n"
    "buffer_load_dwordx4 v87, s[44:47], s55 offen lds\n"
    "s_add_u32 m0, s54, 91136\n"
    "s_nop 0\n"
    "buffer_load_dwordx4 v88, s[44:47], s55 offen lds\n"
    ".Lac4_%=:\n"
    "s_cmp_eq_u32 %2, 3\n"
    "s_cbranch_scc1 .Lac5_%=\n"
    "s_add_u32 s55, s51, 884736\n"
    "s_add_u32 m0, s54, 110592\n"
    "s_nop 0\n"
    "buffer_load_dwordx4 v80, s[44:47], s55 offen lds\n"
    "s_add_u32 m0, s54, 111616\n"
    "s_nop 0\n"
    "buffer_load_dwordx4 v81, s[44:47], s55 offen lds\n"
    "s_add_u32 m0, s54, 112640\n"
    "s_nop 0\n"
    "buffer_load_dwordx4 v82, s[44:47], s55 offen lds\n"
    "s_add_u32 m0, s54, 113664\n"
    "s_nop 0\n"
    "buffer_load_dwordx4 v83, s[44:47], s55 offen lds\n"
    "s_add_u32 m0, s54, 114688\n"
    "s_nop 0\n"
    "buffer_load_dwordx4 v84, s[44:47], s55 offen lds\n"
    "s_add_u32 m0, s54, 115712\n"
    "s_nop 0\n"
    "buffer_load_dwordx4 v85, s[44:47], s55 offen lds\n"
    "s_add_u32 m0, s54, 116736\n"
    "s_nop 0\n"
    "buffer_load_dwordx4 v86, s[44:47], s55 offen lds\n"
    "s_add_u32 m0, s54, 117760\n"
    "s_nop 0\n"
    "buffer_load_dwordx4 v87, s[44:47], s55 offen lds\n"
    "s_add_u32 m0, s54, 118784\n"
    "s_nop 0\n"
    "buffer_load_dwordx4 v88, s[44:47], s55 offen lds\n"
    ".Lac5_%=:\n"
    "s_waitcnt vmcnt(36) lgkmcnt(0)\n"
    "s_barrier\n"
    "ds_read_b64_tr_b16 a[0:1], v90\n"
    "ds_read_b64_tr_b16 a[2:3], v90 offset:576\n"
    "ds_read_b64_tr_b16 a[72:73], v90 offset:9216\n"
    "ds_read_b64_tr_b16 a[74:75], v90 offset:9792\n"
    "ds_read_b64_tr_b16 a[144:145], v90 offset:18432\n"
    "ds_read_b64_tr_b16 a[146:147], v90 offset:19008\n"
    "ds_read_b64_tr_b16 a[216:217], v91\n"
    "ds_read_b64_tr_b16 a[218:219], v91 offset:576\n"
    "ds_read_b64_tr_b16 a[4:5], v90 offset:4608\n"
    "ds_read_b64_tr_b16 a[6:7], v90 offset:5184\n"
    "ds_read_b64_tr_b16 a[76:77], v90 offset:13824\n"
    "ds_read_b64_tr_b16 a[78:79], v90 offset:14400\n"
    "ds_read_b64_tr_b16 a[148:149], v90 offset:23040\n"
    "ds_read_b64_tr_b16 a[150:151], v90 offset:23616\n"
    "ds_read_b64_tr_b16 a[220:221], v91 offset:4608\n"
    "ds_read_b64_tr_b16 a[222:223], v91 offset:5184\n"
    "s_waitcnt vmcnt(27) lgkmcnt(0)\n"
    "s_barrier\n"
    "s_cmp_eq_u32 %2, 3\n"
    "s_cbranch_scc1 .Lac6_%=\n"
    "s_add_u32 s55, s51, 1105920\n"
    "s_add_u32 m0, s54, 0\n"
    "s_nop 0\n"
    "buffer_load_dwordx4 v80, s[44:47], s55 offen lds\n"
    "s_add_u32 m0, s54, 1024\n"
    "s_nop 0\n"
    "buffer_load_dwordx4 v81, s[44:47], s55 offen lds\n"
    "s_add_u32 m0, s54, 2048\n"
    "s_nop 0\n"
    "buffer_load_dwordx4 v82, s[44:47], s55 offen lds\n"
    "s_add_u32 m0, s54, 3072\n"
    "s_nop 0\n"
    "buffer_load_dwordx4 v83, s[44:47], s55 offen lds\n"
    "s_add_u32 m0, s54, 4096\n"
    "s_nop 0\n"
    "buffer_load_dwordx4 v84, s[44:47], s55 offen lds\n"
    "s_add_u32 m0, s54, 5120\n"
    "s_nop 0\n"
    "buffer_load_dwordx4 v85, s[44:47], s55 offen lds\n"
    "s_add_u32 m0, s54, 6144\n"
    "s_nop 0\n"
    "buffer_load_dwordx4 v86, s[44:47], s55 offen lds\n"
    "s_add_u32 m0, s54, 7168\n"
    "s_nop 0\n"
    "buffer_load_dwordx4 v87, s[44:47], s55 offen lds\n"
    "s_add_u32 m0, s54, 8192\n"
    "s_nop 0\n"
    "buffer_load_dwordx4 v88, s[44:47], s55 offen lds\n"
    ".Lac6_%=:\n"
    "v_mfma_f32_16x16x32_bf16 v[12:15], v[56:59], a[0:3], 0\n"
    "v_mfma_f32_16x16x32_bf16 v[16:19], v[56:59], a[72:75], 0\n"
    "v_mfma_f32_16x16x32_bf16 v[20:23], v[56:59], a[144:147], 0\n"
    "v_mfma_f32_16x16x32_bf16 v[24:27], v[56:59], a[216:219], 0\n"
    "v_mfma_f32_16x16x32_bf16 v[12:15], v[56:59], a[4:7], v[12:15]\n"
    "v_mfma_f32_16x16x32_bf16 v[16:19], v[56:59], a[76:79], v[16:19]\n"
    "v_mfma_f32_16x16x32_bf16 v[20:23], v[56:59], a[148:151], v[20:23]\n"
    "v_mfma_f32_16x16x32_bf16 v[24:27], v[56:59], a[220:223], v[24:27]\n"
    "ds_read_b64_tr_b16 a[8:9], v90 offset:27648\n"
    "ds_read_b64_tr_b16 a[10:11], v90 offset:28224\n"
    "ds_read_b64_tr_b16 a[80:81], v90 offset:36864\n"
    "ds_read_b64_tr_b16 a[82:83], v90 offset:37440\n"
    "ds_read_b64_tr_b16 a[152:153], v90 offset:46080\n"
    "ds_read_b64_tr_b16 a[154:155], v90 offset:46656\n"
    "ds_read_b64_tr_b16 a[224:225], v91 offset:27648\n"
    "ds_read_b64_tr_b16 a[226:227], v91 offset:28224\n"
    "ds_read_b64_tr_b16 a[12:13], v90 offset:32256\n"
    "ds_read_b64_tr_b16 a[14:15], v90 offset:32832\n"
    "ds_read_b64_tr_b16 a[84:85], v90 offset:41472\n"
    "ds_read_b64_tr_b16 a[86:87], v90 offset:42048\n"
    "ds_read_b64_tr_b16 a[156:157], v90 offset:50688\n"
    "ds_read_b64_tr_b16 a[158:159], v90 offset:51264\n"
    "ds_read_b64_tr_b16 a[228:229], v91 offset:32256\n"
    "ds_read_b64_tr_b16 a[230:231], v91 offset:32832\n"
    "s_waitcnt vmcnt(27) lgkmcnt(0)\n"
    "s_barrier\n"
    "s_cmp_eq_u32 %2, 3\n"
    "s_cbranch_scc1 .Lac7_%=\n"
    "s_add_u32 s55, s51, 1327104\n"
    "s_add_u32 m0, s54, 27648\n"
    "s_nop 0\n"
    "buffer_load_dwordx4 v80, s[44:47], s55 offen lds\n"
    "s_add_u32 m0, s54, 28672\n"
    "s_nop 0\n"
    "buffer_load_dwordx4 v81, s[44:47], s55 offen lds\n"
    "s_add_u32 m0, s54, 29696\n"
    "s_nop 0\n"
    "buffer_load_dwordx4 v82, s[44:47], s55 offen lds\n"
    "s_add_u32 m0, s54, 30720\n"
    "s_nop 0\n"
    "buffer_load_dwordx4 v83, s[44:47], s55 offen lds\n"
    "s_add_u32 m0, s54, 31744\n"
    "s_nop 0\n"
    "buffer_load_dwordx4 v84, s[44:47], s55 offen lds\n"
    "s_add_u32 m0, s54, 32768\n"
    "s_nop 0\n"
    "buffer_load_dwordx4 v85, s[44:47], s55 offen lds\n"
    "s_add_u32 m0, s54, 33792\n"
    "s_nop 0\n"
    "buffer_load_dwordx4 v86, s[44:47], s55 offen lds\n"
    "s_add_u32 m0, s54, 34816\n"
    "s_nop 0\n"
    "buffer_load_dwordx4 v87, s[44:47], s55 offen lds\n"
    "s_add_u32 m0, s54, 35840\n"
    "s_nop 0\n"
    "buffer_load_dwordx4 v88, s[44:47], s55 offen lds\n"
    ".Lac7_%=:\n"
    "v_mfma_f32_16x16x32_bf16 v[12:15], v[56:59], a[8:11], v[12:15]\n"
    "v_mfma_f32_16x16x32_bf16 v[16:19], v[56:59], a[80:83], v[16:19]\n"
    "v_mfma_f32_16x16x32_bf16 v[20:23], v[56:59], a[152:155], v[20:23]\n"
    "v_mfma_f32_16x16x32_bf16 v[24:27], v[56:59], a[224:227], v[24:27]\n"
    "v_mfma_f32_16x16x32_bf16 v[12:15], v[56:59], a[12:15], v[12:15]\n"
    "v_mfma_f32_16x16x32_bf16 v[16:19], v[56:59], a[84:87], v[16:19]\n"
    "v_mfma_f32_16x16x32_bf16 v[20:23], v[56:59], a[156:159], v[20:23]\n"
    "v_mfma_f32_16x16x32_bf16 v[24:27], v[56:59], a[228:231], v[24:27]\n"
    "ds_read_b64_tr_b16 a[16:17], v90 offset:55296\n"
    "ds_read_b64_tr_b16 a[18:19], v90 offset:55872\n"
    "ds_read_b64_tr_b16 a[88:89], v90 offset:64512\n"
    "ds_read_b64_tr_b16 a[90:91], v90 offset:65088\n"
    "v_add_u32 v99, 0x10000, v90\n"
    "ds_read_b64_tr_b16 a[160:161], v99 offset:8192\n"
    "ds_read_b64_tr_b16 a[162:163], v99 offset:8768\n"
    "ds_read_b64_tr_b16 a[232:233], v91 offset:55296\n"
    "ds_read_b64_tr_b16 a[234:235], v91 offset:55872\n"
    "ds_read_b64_tr_b16 a[20:21], v90 offset:59904\n"
    "ds_read_b64_tr_b16 a[22:23], v90 offset:60480\n"
    "ds_read_b64_tr_b16 a[92:93], v99 offset:3584\n"
    "ds_read_b64_tr_b16 a[94:95], v99 offset:4160\n"
    "ds_read_b64_tr_b16 a[164:165], v99 offset:12800\n"
    "ds_read_b64_tr_b16 a[166:167], v99 offset:13376\n"
    "ds_read_b64_tr_b16 a[236:237], v91 offset:59904\n"
    "ds_read_b64_tr_b16 a[238:239], v91 offset:60480\n"
    "s_waitcnt vmcnt(27) lgkmcnt(0)\n"
    "s_barrier\n"
    "s_cmp_eq_u32 %2, 3\n"
    "s_cbranch_scc1 .Lac8_%=\n"
    "s_add_u32 s55, s51, 1548288\n"
    "s_add_u32 m0, s54, 55296\n"
    "s_nop 0\n"
    "buffer_load_dwordx4 v80, s[44:47], s55 offen lds\n"
    "s_add_u32 m0, s54, 56320\n"
    "s_nop 0\n"
    "buffer_load_dwordx4 v81, s[44:47], s55 offen lds\n"
    "s_add_u32 m0, s54, 57344\n"
    "s_nop 0\n"
    "buffer_load_dwordx4 v82, s[44:47], s55 offen lds\n"
    "s_add_u32 m0, s54, 58368\n"
    "s_nop 0\n"
    "buffer_load_dwordx4 v83, s[44:47], s55 offen lds\n"
    "s_add_u32 m0, s54, 59392\n"
    "s_nop 0\n"
    "buffer_load_dwordx4 v84, s[44:47], s55 offen lds\n"
    "s_add_u32 m0, s54, 60416\n"
    "s_nop 0\n"
    "buffer_load_dwordx4 v85, s[44:47], s55 offen lds\n"
    "s_add_u32 m0, s54, 61440\n"
    "s_nop 0\n"
    "buffer_load_dwordx4 v86, s[44:47], s55 offen lds\n"
    "s_add_u32 m0, s54, 62464\n"
    "s_nop 0\n"
    "buffer_load_dwordx4 v87, s[44:47], s55 offen lds\n"
    "s_add_u32 m0, s54, 63488\n"
    "s_nop 0\n"
    "buffer_load_dwordx4 v88, s[44:47], s55 offen lds\n"
    ".Lac8_%=:\n"
    "v_mfma_f32_16x16x32_bf16 v[12:15], v[56:59], a[16:19], v[12:15]\n"
    "v_mfma_f32_16x16x32_bf16 v[16:19], v[56:59], a[88:91], v[16:19]\n"
    "v_mfma_f32_16x16x32_bf16 v[20:23], v[56:59], a[160:163], v[20:23]\n"
    "v_mfma_f32_16x16x32_bf16 v[24:27], v[56:59], a[232:235], v[24:27]\n"
    "v_mfma_f32_16x16x32_bf16 v[12:15], v[56:59], a[20:23], v[12:15]\n"
    "v_mfma_f32_16x16x32_bf16 v[16:19], v[56:59], a[92:95], v[16:19]\n"
    "v_mfma_f32_16x16x32_bf16 v[20:23], v[56:59], a[164:167], v[20:23]\n"
    "v_mfma_f32_16x16x32_bf16 v[24:27], v[56:59], a[236:239], v[24:27]\n"
    "ds_read_b64_tr_b16 a[24:25], v99 offset:17408\n"
    "ds_read_b64_tr_b16 a[26:27], v99 offset:17984\n"
    "ds_read_b64_tr_b16 a[96:97], v99 offset:26624\n"
    "ds_read_b64_tr_b16 a[98:99], v99 offset:27200\n"
    "ds_read_b64_tr_b16 a[168:169], v99 offset:35840\n"
    "ds_read_b64_tr_b16 a[170:171], v99 offset:36416\n"
    "v_add_u32 v98, 0x10000, v91\n"
    "ds_read_b64_tr_b16 a[240:241], v98 offset:17408\n"
    "ds_read_b64_tr_b16 a[242:243], v98 offset:17984\n"
    "ds_read_b64_tr_b16 a[28:29], v99 offset:22016\n"
    "ds_read_b64_tr_b16 a[30:31], v99 offset:22592\n"
    "ds_read_b64_tr_b16 a[100:101], v99 offset:31232\n"
    "ds_read_b64_tr_b16 a[102:103], v99 offset:31808\n"
    "ds_read_b64_tr_b16 a[172:173], v99 offset:40448\n"
    "ds_read_b64_tr_b16 a[174:175], v99 offset:41024\n"
    "ds_read_b64_tr_b16 a[244:245], v98 offset:22016\n"
    "ds_read_b64_tr_b16 a[246:247], v98 offset:22592\n"
    "s_waitcnt vmcnt(27) lgkmcnt(0)\n"
    "s_barrier\n"
    "s_cmp_eq_u32 %2, 3\n"
    "s_cbranch_scc1 .Lac9_%=\n"
    "s_add_u32 s55, s51, 1769472\n"
    "s_add_u32 m0, s54, 82944\n"
    "s_nop 0\n"
    "buffer_load_dwordx4 v80, s[44:47], s55 offen lds\n"
    "s_add_u32 m0, s54, 83968\n"
    "s_nop 0\n"
    "buffer_load_dwordx4 v81, s[44:47], s55 offen lds\n"
    "s_add_u32 m0, s54, 84992\n"
    "s_nop 0\n"
    "buffer_load_dwordx4 v82, s[44:47], s55 offen lds\n"
    "s_add_u32 m0, s54, 86016\n"
    "s_nop 0\n"
    "buffer_load_dwordx4 v83, s[44:47], s55 offen lds\n"
    "s_add_u32 m0, s54, 87040\n"
    "s_nop 0\n"
    "buffer_load_dwordx4 v84, s[44:47], s55 offen lds\n"
    "s_add_u32 m0, s54, 88064\n"
    "s_nop 0\n"
    "buffer_load_dwordx4 v85, s[44:47], s55 offen lds\n"
    "s_add_u32 m0, s54, 89088\n"
    "s_nop 0\n"
    "buffer_load_dwordx4 v86, s[44:47], s55 offen lds\n"
    "s_add_u32 m0, s54, 90112\n"
    "s_nop 0\n"
    "buffer_load_dwordx4 v87, s[44:47], s55 offen lds\n"
    "s_add_u32 m0, s54, 91136\n"
    "s_nop 0\n"
    "buffer_load_dwordx4 v88, s[44:47], s55 offen lds\n"
    ".Lac9_%=:\n"
    "v_mov_b32 v11, 0x20400\n"
    "global_load_dwordx2 v[76:77], v11, %1 sc1\n"
    "v_mbcnt_lo_u32_b32 v220, -1, 0\n"
    "v_min_u32 v220, 7, v220\n"
    "v_lshlrev_b32 v220, 7, v220\n"
    "v_add_u32 v220, 0x20000, v220\n"
    "global_load_dwordx2 v[78:79], v220, %1 sc1\n"
    "v_mfma_f32_16x16x32_bf16 v[12:15], v[56:59], a[24:27], v[12:15]\n"
    "v_mfma_f32_16x16x32_bf16 v[16:19], v[56:59], a[96:99], v[16:19]\n"
    "v_mfma_f32_16x16x32_bf16 v[20:23], v[56:59], a[168:171], v[20:23]\n"
    "v_mfma_f32_16x16x32_bf16 v[24:27], v[56:59], a[240:243], v[24:27]\n"
    "v_mfma_f32_16x16x32_bf16 v[12:15], v[56:59], a[28:31], v[12:15]\n"
    "v_mfma_f32_16x16x32_bf16 v[16:19], v[56:59], a[100:103], v[16:19]\n"
    "v_mfma_f32_16x16x32_bf16 v[20:23], v[56:59], a[172:175], v[20:23]\n"
    "v_mfma_f32_16x16x32_bf16 v[24:27], v[56:59], a[244:247], v[24:27]\n"
    "ds_read_b64_tr_b16 a[32:33], v99 offset:45056\n"
    "ds_read_b64_tr_b16 a[34:35], v99 offset:45632\n"
    "ds_read_b64_tr_b16 a[104:105], v99 offset:54272\n"
    "ds_read_b64_tr_b16 a[106:107], v99 offset:54848\n"
    "ds_read_b64_tr_b16 a[176:177], v99 offset:63488\n"
    "ds_read_b64_tr_b16 a[178:179], v99 offset:64064\n"
    "ds_read_b64_tr_b16 a[248:249], v98 offset:45056\n"
    "ds_read_b64_tr_b16 a[250:251], v98 offset:45632\n"
    "ds_read_b64_tr_b16 a[36:37], v99 offset:49664\n"
    "ds_read_b64_tr_b16 a[38:39], v99 offset:50240\n"
    "ds_read_b64_tr_b16 a[108:109], v99 offset:58880\n"
    "ds_read_b64_tr_b16 a[110:111], v99 offset:59456\n"
    "v_add_u32 v97, 0x20000, v90\n"
    "ds_read_b64_tr_b16 a[180:181], v97 offset:2560\n"
    "ds_read_b64_tr_b16 a[182:183], v97 offset:3136\n"
    "ds_read_b64_tr_b16 a[252:253], v98 offset:49664\n"
    "ds_read_b64_tr_b16 a[254:255], v98 offset:50240\n"
    "s_waitcnt vmcnt(29) lgkmcnt(0)\n"
    "s_barrier\n"
    "v_mfma_f32_16x16x32_bf16 v[12:15], v[56:59], a[32:35], v[12:15]\n"
    "v_mfma_f32_16x16x32_bf16 v[16:19], v[56:59], a[104:107], v[16:19]\n"
    "v_mfma_f32_16x16x32_bf16 v[20:23], v[56:59], a[176:179], v[20:23]\n"
    "v_mfma_f32_16x16x32_bf16 v[24:27], v[56:59], a[248:251], v[24:27]\n"
    "v_mfma_f32_16x16x32_bf16 v[12:15], v[56:59], a[36:39], v[12:15]\n"
    "v_mfma_f32_16x16x32_bf16 v[16:19], v[56:59], a[108:111], v[16:19]\n"
    "v_mfma_f32_16x16x32_bf16 v[20:23], v[56:59], a[180:183], v[20:23]\n"
    "v_mfma_f32_16x16x32_bf16 v[24:27], v[56:59], a[252:255], v[24:27]\n"
    "ds_read_b64_tr_b16 a[40:41], v90\n"
    "ds_read_b64_tr_b16 a[42:43], v90 offset:576\n"
    "ds_read_b64_tr_b16 a[112:113], v90 offset:9216\n"
    "ds_read_b64_tr_b16 a[114:115], v90 offset:9792\n"
    "ds_read_b64_tr_b16 a[184:185], v90 offset:18432\n"
    "ds_read_b64_tr_b16 a[186:187], v90 offset:19008\n"
    "ds_read_b64_tr_b16 v[224:225], v91\n"
    "ds_read_b64_tr_b16 v[226:227], v91 offset:576\n"
    "ds_read_b64_tr_b16 a[44:45], v90 offset:4608\n"
    "ds_read_b64_tr_b16 a[46:47], v90 offset:5184\n"
    "ds_read_b64_tr_b16 a[116:117], v90 offset:13824\n"
    "ds_read_b64_tr_b16 a[118:119], v90 offset:14400\n"
    "ds_read_b64_tr_b16 a[188:189], v90 offset:23040\n"
    "ds_read_b64_tr_b16 a[190:191], v90 offset:23616\n"
    "ds_read_b64_tr_b16 v[228:229], v91 offset:4608\n"
    "ds_read_b64_tr_b16 v[230:231], v91 offset:5184\n"
    "s_waitcnt vmcnt(20) lgkmcnt(0)\n"
    "s_barrier\n"
    "v_mfma_f32_16x16x32_bf16 v[12:15], v[56:59], a[40:43], v[12:15]\n"
    "v_mfma_f32_16x16x32_bf16 v[16:19], v[56:59], a[112:115], v[16:19]\n"
    "v_mfma_f32_16x16x32_bf16 v[20:23], v[56:59], a[184:187], v[20:23]\n"
    "v_mfma_f32_16x16x32_bf16 v[24:27], v[56:59], v[224:227], v[24:27]\n"
    "v_mfma_f32_16x16x32_bf16 v[12:15], v[56:59], a[44:47], v[12:15]\n"
    "v_mfma_f32_16x16x32_bf16 v[16:19], v[56:59], a[116:119], v[16:19]\n"
    "v_mfma_f32_16x16x32_bf16 v[20:23], v[56:59], a[188:191], v[20:23]\n"
    "v_mfma_f32_16x16x32_bf16 v[24:27], v[56:59], v[228:231], v[24:27]\n"
    "ds_read_b64_tr_b16 a[48:49], v90 offset:27648\n"
    "ds_read_b64_tr_b16 a[50:51], v90 offset:28224\n"
    "ds_read_b64_tr_b16 a[120:121], v90 offset:36864\n"
    "ds_read_b64_tr_b16 a[122:123], v90 offset:37440\n"
    "ds_read_b64_tr_b16 a[192:193], v90 offset:46080\n"
    "ds_read_b64_tr_b16 a[194:195], v90 offset:46656\n"
    "ds_read_b64_tr_b16 v[232:233], v91 offset:27648\n"
    "ds_read_b64_tr_b16 v[234:235], v91 offset:28224\n"
    "ds_read_b64_tr_b16 a[52:53], v90 offset:32256\n"
    "ds_read_b64_tr_b16 a[54:55], v90 offset:32832\n"
    "ds_read_b64_tr_b16 a[124:125], v90 offset:41472\n"
    "ds_read_b64_tr_b16 a[126:127], v90 offset:42048\n"
    "ds_read_b64_tr_b16 a[196:197], v90 offset:50688\n"
    "ds_read_b64_tr_b16 a[198:199], v90 offset:51264\n"
    "ds_read_b64_tr_b16 v[236:237], v91 offset:32256\n"
    "ds_read_b64_tr_b16 v[238:239], v91 offset:32832\n"
    "s_waitcnt vmcnt(11) lgkmcnt(0)\n"
    "s_barrier\n"
    "v_mfma_f32_16x16x32_bf16 v[12:15], v[56:59], a[48:51], v[12:15]\n"
    "v_mfma_f32_16x16x32_bf16 v[16:19], v[56:59], a[120:123], v[16:19]\n"
    "v_mfma_f32_16x16x32_bf16 v[20:23], v[56:59], a[192:195], v[20:23]\n"
    "v_mfma_f32_16x16x32_bf16 v[24:27], v[56:59], v[232:235], v[24:27]\n"
    "v_mfma_f32_16x16x32_bf16 v[12:15], v[56:59], a[52:55], v[12:15]\n"
    "v_mfma_f32_16x16x32_bf16 v[16:19], v[56:59], a[124:127], v[16:19]\n"
    "v_mfma_f32_16x16x32_bf16 v[20:23], v[56:59], a[196:199], v[20:23]\n"
    "v_mfma_f32_16x16x32_bf16 v[24:27], v[56:59], v[236:239], v[24:27]\n"
    "ds_read_b64_tr_b16 a[56:57], v90 offset:55296\n"
    "ds_read_b64_tr_b16 a[58:59], v90 offset:55872\n"
    "ds_read_b64_tr_b16 a[128:129], v90 offset:64512\n"
    "ds_read_b64_tr_b16 a[130:131], v90 offset:65088\n"
    "ds_read_b64_tr_b16 a[200:201], v99 offset:8192\n"
    "ds_read_b64_tr_b16 a[202:203], v99 offset:8768\n"
    "ds_read_b64_tr_b16 v[240:241], v91 offset:55296\n"
    "ds_read_b64_tr_b16 v[242:243], v91 offset:55872\n"
    "ds_read_b64_tr_b16 a[60:61], v90 offset:59904\n"
    "ds_read_b64_tr_b16 a[62:63], v90 offset:60480\n"
    "ds_read_b64_tr_b16 a[132:133], v99 offset:3584\n"
    "ds_read_b64_tr_b16 a[134:135], v99 offset:4160\n"
    "ds_read_b64_tr_b16 a[204:205], v99 offset:12800\n"
    "ds_read_b64_tr_b16 a[206:207], v99 offset:13376\n"
    "ds_read_b64_tr_b16 v[244:245], v91 offset:59904\n"
    "ds_read_b64_tr_b16 v[246:247], v91 offset:60480\n"
    "s_waitcnt vmcnt(2) lgkmcnt(0)\n"
    "s_barrier\n"
    "v_mfma_f32_16x16x32_bf16 v[12:15], v[56:59], a[56:59], v[12:15]\n"
    "v_mfma_f32_16x16x32_bf16 v[16:19], v[56:59], a[128:131], v[16:19]\n"
    "v_mfma_f32_16x16x32_bf16 v[20:23], v[56:59], a[200:203], v[20:23]\n"
    "v_mfma_f32_16x16x32_bf16 v[24:27], v[56:59], v[240:243], v[24:27]\n"
    "v_mfma_f32_16x16x32_bf16 v[12:15], v[56:59], a[60:63], v[12:15]\n"
    "v_mfma_f32_16x16x32_bf16 v[16:19], v[56:59], a[132:135], v[16:19]\n"
    "v_mfma_f32_16x16x32_bf16 v[20:23], v[56:59], a[204:207], v[20:23]\n"
    "v_mfma_f32_16x16x32_bf16 v[24:27], v[56:59], v[244:247], v[24:27]\n"
    "ds_read_b64_tr_b16 a[64:65], v99 offset:17408\n"
    "ds_read_b64_tr_b16 a[66:67], v99 offset:17984\n"
    "ds_read_b64_tr_b16 a[136:137], v99 offset:26624\n"
    "ds_read_b64_tr_b16 a[138:139], v99 offset:27200\n"
    "ds_read_b64_tr_b16 a[208:209], v99 offset:35840\n"
    "ds_read_b64_tr_b16 a[210:211], v99 offset:36416\n"
    "ds_read_b64_tr_b16 v[248:249], v98 offset:17408\n"
    "ds_read_b64_tr_b16 v[250:251], v98 offset:17984\n"
    "ds_read_b64_tr_b16 a[68:69], v99 offset:22016\n"
    "ds_read_b64_tr_b16 a[70:71], v99 offset:22592\n"
    "ds_read_b64_tr_b16 a[140:141], v99 offset:31232\n"
    "ds_read_b64_tr_b16 a[142:143], v99 offset:31808\n"
    "ds_read_b64_tr_b16 a[212:213], v99 offset:40448\n"
    "ds_read_b64_tr_b16 a[214:215], v99 offset:41024\n"
    "ds_read_b64_tr_b16 v[252:253], v98 offset:22016\n"
    "ds_read_b64_tr_b16 v[254:255], v98 offset:22592\n"
    "s_waitcnt lgkmcnt(0)\n"
    "s_barrier\n"
    "v_mfma_f32_16x16x32_bf16 v[12:15], v[56:59], a[64:67], v[12:15]\n"
    "v_mfma_f32_16x16x32_bf16 v[16:19], v[56:59], a[136:139], v[16:19]\n"
    "v_mfma_f32_16x16x32_bf16 v[20:23], v[56:59], a[208:211], v[20:23]\n"
    "v_mfma_f32_16x16x32_bf16 v[24:27], v[56:59], v[248:251], v[24:27]\n"
    "v_mfma_f32_16x16x32_bf16 v[12:15], v[56:59], a[68:71], v[12:15]\n"
    "v_mfma_f32_16x16x32_bf16 v[16:19], v[56:59], a[140:143], v[16:19]\n"
    "v_mfma_f32_16x16x32_bf16 v[20:23], v[56:59], a[212:215], v[20:23]\n"
    "v_mfma_f32_16x16x32_bf16 v[24:27], v[56:59], v[252:255], v[24:27]\n"
    "v_mov_b32 v28, 0\n"
    "v_mov_b32 v29, 0\n"
    "v_mov_b32 v30, 0\n"
    "v_mov_b32 v31, 0\n"
    "v_mov_b32 v32, 0\n"
    "v_mov_b32 v33, 0\n"
    "v_mov_b32 v34, 0\n"
    "v_mov_b32 v35, 0\n"
    "v_mov_b32 v36, 0\n"
    "v_mov_b32 v37, 0\n"
    "v_mov_b32 v38, 0\n"
    "v_mov_b32 v39, 0\n"
    "v_mov_b32 v172, 0\n"
    "v_mov_b32 v173, 0\n"
    "v_mov_b32 v174, 0\n"
    "v_mov_b32 v175, 0\n"
    "v_mov_b32 v176, 0\n"
    "v_mov_b32 v177, 0\n"
    "v_mov_b32 v178, 0\n"
    "v_mov_b32 v199, 0\n"
    "v_mov_b32 v215, 0\n"
    "v_mov_b32 v218, 0\n"
    "v_mov_b32 v219, 0\n"
    "v_mov_b32 v94, 0xf149f2ca\n"
    "v_add_u32 v93, 7, v95\n"
    "v_cmp_gt_u32 vcc, 8, v93\n"
    "s_nop 1\n"
    "v_cndmask_b32 v44, v94, v28, vcc\n"
    "v_cmp_le_i32 vcc, 9, v95\n"
    "s_nop 1\n"
    "v_cndmask_b32 v68, v94, v28, vcc\n"
    "v_add_u32 v93, 8, v95\n"
    "v_cmp_gt_u32 vcc, 8, v93\n"
    "s_nop 1\n"
    "v_cndmask_b32 v45, v94, v28, vcc\n"
    "v_cmp_le_i32 vcc, 8, v95\n"
    "s_nop 1\n"
    "v_cndmask_b32 v69, v94, v28, vcc\n"
    "v_add_u32 v93, 9, v95\n"
    "v_cmp_gt_u32 vcc, 8, v93\n"
    "s_nop 1\n"
    "v_cndmask_b32 v46, v94, v28, vcc\n"
    "v_cmp_le_i32 vcc, 7, v95\n"
    "s_nop 1\n"
    "v_cndmask_b32 v70, v94, v28, vcc\n"
    "v_add_u32 v93, 10, v95\n"
    "v_cmp_gt_u32 vcc, 8, v93\n"
    "s_nop 1\n"
    "v_cndmask_b32 v47, v94, v28, vcc\n"
    "v_cmp_le_i32 vcc, 6, v95\n"
    "s_nop 1\n"
    "v_cndmask_b32 v71, v94, v28, vcc\n"
    "s_mov_b32 s86, 0xcccccccc\n"
    "s_mov_b32 s87, 0xcccccccc\n"
    "v_cvt_pk_bf16_f32 v13, 0, v12\n"
    "v_sub_f32 v12, v12, v13\n"
    "v_cvt_pk_bf16_f32 v14, 0, v12\n"
    "v_sub_f32 v12, v12, v14\n"
    "v_cvt_pk_bf16_f32 v13, v13, v14\n"
    "v_cvt_pk_bf16_f32 v14, v12, 0\n"
    "s_mov_b64 vcc, 0xffff\n"
    "v_cndmask_b32 v60, 0, v13, vcc\n"
    "v_cndmask_b32 v61, 0, v14, vcc\n"
    "v_cvt_pk_bf16_f32 v17, 0, v16\n"
    "v_sub_f32 v16, v16, v17\n"
    "v_cvt_pk_bf16_f32 v18, 0, v16\n"
    "v_sub_f32 v16, v16, v18\n"
    "v_cvt_pk_bf16_f32 v17, v17, v18\n"
    "v_cvt_pk_bf16_f32 v18, v16, 0\n"
    "s_mov_b64 vcc, 0xffff\n"
    "v_cndmask_b32 v62, 0, v17, vcc\n"
    "v_cndmask_b32 v63, 0, v18, vcc\n"
    "v_cvt_pk_bf16_f32 v21, 0, v20\n"
    "v_sub_f32 v20, v20, v21\n"
    "v_cvt_pk_bf16_f32 v22, 0, v20\n"
    "v_sub_f32 v20, v20, v22\n"
    "v_cvt_pk_bf16_f32 v21, v21, v22\n"
    "v_cvt_pk_bf16_f32 v22, v20, 0\n"
    "s_mov_b64 vcc, 0xffff\n"
    "v_cndmask_b32 v64, 0, v21, vcc\n"
    "v_cndmask_b32 v65, 0, v22, vcc\n"
    "v_cvt_pk_bf16_f32 v25, 0, v24\n"
    "v_sub_f32 v24, v24, v25\n"
    "v_cvt_pk_bf16_f32 v26, 0, v24\n"
    "v_sub_f32 v24, v24, v26\n"
    "v_cvt_pk_bf16_f32 v25, v25, v26\n"
    "v_cvt_pk_bf16_f32 v26, v24, 0\n"
    "s_mov_b64 vcc, 0xffff\n"
    "v_cndmask_b32 v66, 0, v25, vcc\n"
    "v_cndmask_b32 v67, 0, v26, vcc\n"
    "s_waitcnt vmcnt(0)\n"
    "v_add_u32 v76, 1, v76\n"
    "v_add_u32 v77, 1, v77\n"
    "s_mov_b32 s52, 1048576\n"
    ".Lago_a%=:\n"
    "v_cmp_ne_u64 vcc, v[76:77], v[78:79]\n"
    "s_cbranch_vccz .Lago_x%=\n"
    "global_load_dwordx2 v[78:79], v220, %1 sc1\n"
    "s_sub_u32 s52, s52, 1\n"
    "s_waitcnt vmcnt(0)\n"
    "s_cbranch_scc0 .Lago_a%=\n"
    ".Lago_x%=:\n"
    "v_mbcnt_lo_u32_b32 v76, -1, 0\n"
    "v_mbcnt_hi_u32_b32 v76, -1, v76\n"
    "v_min_u32 v220, 5, v76\n"
    "v_lshlrev_b32 v220, 7, v220\n"
    "v_mov_b32 v11, 0x26c00\n"
    "ds_read_b128 v[76:79], v11\n"
    "s_waitcnt lgkmcnt(0)\n"
    "v_readfirstlane_b32 s49, v77\n"
    "v_add_u32 v76, v76, v220\n"
    "s_branch .Lachk_%=\n"
    ".Lago_%=:\n"
    "s_waitcnt vmcnt(1)\n"
    "v_add_u32 v11, 32, v11\n"
    "ds_read_b128 v[76:79], v11\n"
    "ds_read2_b32 v[74:75], v11 offset0:5 offset1:6\n"
    "ds_write_b128 v1, v[152:155]\n"
    "v_mfma_f32_16x16x32_bf16 v[48:51], v[152:155], v[152:155], 0\n"
    "ds_write_b128 v1, v[156:159] offset:1024\n"
    "v_mfma_f32_16x16x32_bf16 v[48:51], v[156:159], v[156:159], v[48:51]\n"
    "ds_write_b128 v1, v[160:163] offset:2048\n"
    "v_mfma_f32_16x16x32_bf16 v[48:51], v[160:163], v[160:163], v[48:51]\n"
    "ds_write_b128 v1, v[164:167] offset:3072\n"
    "v_mfma_f32_16x16x32_bf16 v[48:51], v[164:167], v[164:167], v[48:51]\n"
    "ds_write_b128 v1, v[168:171] offset:4096\n"
    "v_mfma_f32_16x16x32_bf16 v[48:51], v[168:171], v[168:171], v[48:51]\n"
    "s_waitcnt lgkmcnt(5)\n"
    "v_readfirstlane_b32 s50, v75\n"
    "s_cmp_eq_u32 s50, 0\n"
    "s_cbranch_scc1 .Lac10_%=\n"
    "v_swap_b32 v28, v172\n"
    "v_swap_b32 v29, v173\n"
    "v_swap_b32 v30, v174\n"
    "v_swap_b32 v32, v175\n"
    "v_swap_b32 v33, v176\n"
    "v_swap_b32 v36, v177\n"
    "v_swap_b32 v37, v178\n"
    ".Lac10_%=:\n"
    "v_readfirstlane_b32 s49, v77\n"
    "v_readfirstlane_b32 s62, v78\n"
    "v_add_u32 v76, v76, v220\n"
    "s_waitcnt lgkmcnt(0)\n"
    ".Latop_%=:\n"
    "s_waitcnt lgkmcnt(0)\n"
    "s_barrier\n"
    "s_cmp_eq_u32 %2, 1\n"
    "s_cbranch_scc1 .Laq1_%=\n"
    "s_cmp_eq_u32 %2, 2\n"
    "s_cbranch_scc1 .Laq2_%=\n"
    "s_cmp_eq_u32 %2, 3\n"
    "s_cbranch_scc1 .Laq3_%=\n"
    "ds_read_b128 v[80:83], v0\n"
    "ds_read_b128 v[88:91], v0 offset:2048\n"
    "ds_read_b128 v[84:87], v0 offset:1024\n"
    "ds_read_b128 v[100:103], v0 offset:6144\n"
    "ds_read_b128 v[96:99], v0 offset:5120\n"
    "ds_read_b128 v[92:95], v0 offset:3072\n"
    "ds_read_b128 v[112:115], v0 offset:10240\n"
    "ds_read_b128 v[108:111], v0 offset:8192\n"
    "ds_read_b128 v[104:107], v0 offset:7168\n"
    "global_load_dword v77, v76, %1 sc1\n"
    "ds_write_b128 v73, v[48:51] offset:20480\n"
    "v_mfma_f32_16x16x32_bf16 v[52:55], v[56:59], v[152:155], 0\n"
    "v_mfma_f32_16x16x32_bf16 v[52:55], v[56:59], v[156:159], v[52:55]\n"
    "v_mfma_f32_16x16x32_bf16 v[52:55], v[56:59], v[160:163], v[52:55]\n"
    "v_mfma_f32_16x16x32_bf16 v[52:55], v[56:59], v[164:167], v[52:55]\n"
    "v_mfma_f32_16x16x32_bf16 v[52:55], v[56:59], v[168:171], v[52:55]\n"
    "s_waitcnt lgkmcnt(9)\n"
    "v_mfma_f32_16x16x32_bf16 v[24:27], a[216:219], v[80:83], 0\n"
    "ds_read_b128 v[116:119], v0 offset:11264\n"
    "ds_read_b128 v[120:123], v0 offset:12288\n"
    "s_waitcnt lgkmcnt(9)\n"
    "v_mfma_f32_16x16x32_bf16 v[24:27], a[220:223], v[84:87], v[24:27]\n"
    "ds_read_b128 v[124:127], v0 offset:13312\n"
    "ds_read_b128 v[128:131], v0 offset:15360\n"
    "v_mfma_f32_16x16x32_bf16 v[24:27], a[224:227], v[88:91], v[24:27]\n"
    "ds_write_b128 v73, v[52:55] offset:24576\n"
    "ds_read_b128 v[132:135], v0 offset:16384\n"
    "s_waitcnt lgkmcnt(10)\n"
    "v_mfma_f32_16x16x32_bf16 v[24:27], a[228:231], v[92:95], v[24:27]\n"
    "ds_read_b128 v[136:139], v0 offset:17408\n"
    "ds_read_b128 v[140:143], v0 offset:18432\n"
    "v_mfma_f32_16x16x32_bf16 v[24:27], a[232:235], v[96:99], v[24:27]\n"
    "ds_read_b128 v[144:147], v0 offset:4096\n"
    "ds_read_b128 v[148:151], v0 offset:9216\n"
    "v_mfma_f32_16x16x32_bf16 v[24:27], a[236:239], v[100:103], v[24:27]\n"
    "s_waitcnt lgkmcnt(11)\n"
    "v_mfma_f32_16x16x32_bf16 v[24:27], a[240:243], v[104:107], v[24:27]\n"
    "v_mfma_f32_16x16x32_bf16 v[24:27], a[244:247], v[108:111], v[24:27]\n"
    "s_waitcnt lgkmcnt(0)\n"
    "s_barrier\n"
    "v_mfma_f32_16x16x32_bf16 v[24:27], a[248:251], v[112:115], v[24:27]\n"
    "ds_read2st64_b32 v[48:49], v10 offset0:80 offset1:84\n"
    "ds_read2st64_b32 v[50:51], v10 offset0:88 offset1:92\n"
    "v_mfma_f32_16x16x32_bf16 v[12:15], a[0:3], v[80:83], 0\n"
    "ds_read2st64_b32 v[54:55], v10 offset0:104 offset1:108\n"
    "ds_read2st64_b32 v[52:53], v10 offset0:96 offset1:100\n"
    "v_mfma_f32_16x16x32_bf16 v[16:19], a[72:75], v[80:83], 0\n"
    "v_min_f32 v40, v74, v68\n"
    "v_min_f32 v41, v74, v69\n"
    "v_mfma_f32_16x16x32_bf16 v[12:15], a[4:7], v[84:87], v[12:15]\n"
    "v_min_f32 v42, v74, v70\n"
    "v_min_f32 v43, v74, v71\n"
    "v_mfma_f32_16x16x32_bf16 v[16:19], a[76:79], v[84:87], v[16:19]\n"
    "v_mfma_f32_16x16x32_bf16 v[12:15], a[8:11], v[88:91], v[12:15]\n"
    "s_waitcnt lgkmcnt(0)\n"
    "v_add_f32 v52, v52, v53\n"
    "v_add_f32 v54, v54, v55\n"
    "v_mfma_f32_16x16x32_bf16 v[16:19], a[80:83], v[88:91], v[16:19]\n"
    "v_add_f32 v48, v48, v49\n"
    "v_add_f32 v50, v50, v51\n"
    "v_mfma_f32_16x16x32_bf16 v[12:15], a[12:15], v[92:95], v[12:15]\n"
    "v_add_f32 v52, v52, v54\n"
    "v_add_f32 v50, v48, v50\n"
    "v_mfma_f32_16x16x32_bf16 v[16:19], a[84:87], v[92:95], v[16:19]\n"
    "v_mul_f32 v52, 0xbae38e39, v52\n"
    "v_mul_f32 v50, 0x3ae38e39, v50\n"
    "v_mfma_f32_16x16x32_bf16 v[12:15], a[16:19], v[96:99], v[12:15]\n"
    "v_cvt_pk_bf16_f32 v216, v52, v52\n"
    "v_fma_f32 v50, -v52, v52, v50\n"
    "v_mfma_f32_16x16x32_bf16 v[16:19], a[88:91], v[96:99], v[16:19]\n"
    "v_cvt_pk_bf16_f32 v217, v52, v52\n"
    "v_max_f32 v50, 0, v50\n"
    "v_mfma_f32_16x16x32_bf16 v[12:15], a[20:23], v[100:103], v[12:15]\n"
    "v_cvt_pk_bf16_f32 v50, 0, v50\n"
    "v_add_f32 v50, 0x3727c5ac, v50\n"
    "v_mfma_f32_16x16x32_bf16 v[16:19], a[92:95], v[100:103], v[16:19]\n"
    "v_mfma_f32_16x16x32_bf16 v[24:27], v[66:69], v[216:219], v[24:27]\n"
    "v_cvt_pk_bf16_f32 v50, 0, v50\n"
    "v_mfma_f32_16x16x32_bf16 v[12:15], a[24:27], v[104:107], v[12:15]\n"
    "v_sqrt_f32 v50, v50\n"
    "v_mfma_f32_16x16x32_bf16 v[16:19], a[96:99], v[104:107], v[16:19]\n"
    "v_cvt_pk_bf16_f32 v50, 0, v50\n"
    "v_rcp_f32 v50, v50\n"
    "v_mfma_f32_16x16x32_bf16 v[12:15], a[28:31], v[108:111], v[12:15]\n"
    "ds_write_b32 v9, v50\n"
    "v_mfma_f32_16x16x32_bf16 v[16:19], a[100:103], v[108:111], v[16:19]\n"
    "ds_read_b128 v[200:203], v72 offset:28672\n"
    "v_mfma_f32_16x16x32_bf16 v[12:15], v[60:63], v[216:219], v[12:15]\n"
    "v_mfma_f32_16x16x32_bf16 v[12:15], a[32:35], v[112:115], v[12:15]\n"
    "v_mfma_f32_16x16x32_bf16 v[16:19], v[62:65], v[216:219], v[16:19]\n"
    "v_mul_f32 v24, v24, v50\n"
    "v_mfma_f32_16x16x32_bf16 v[16:19], a[104:107], v[112:115], v[16:19]\n"
    "v_mul_f32 v25, v25, v50\n"
    "v_mul_f32 v26, v26, v50\n"
    "v_mfma_f32_16x16x32_bf16 v[12:15], a[36:39], v[116:119], v[12:15]\n"
    "v_mul_f32 v27, v27, v50\n"
    "ds_write_b128 v5, v[24:27]\n"
    "v_mfma_f32_16x16x32_bf16 v[16:19], a[108:111], v[116:119], v[16:19]\n"
    "v_mfma_f32_16x16x32_bf16 v[12:15], a[40:43], v[120:123], v[12:15]\n"
    "v_mfma_f32_16x16x32_bf16 v[16:19], a[112:115], v[120:123], v[16:19]\n"
    "v_mfma_f32_16x16x32_bf16 v[12:15], a[44:47], v[124:127], v[12:15]\n"
    "s_waitcnt vmcnt(0)\n"
    "v_mov_b32 v78, 1\n"
    "v_mfma_f32_16x16x32_bf16 v[16:19], a[116:119], v[124:127], v[16:19]\n"
    "s_mov_b64 exec, s[56:57]\n"
    "ds_add_rtn_u32 v78, v11, v78 offset:16\n"
    "s_mov_b64 exec, -1\n"
    "v_mfma_f32_16x16x32_bf16 v[12:15], a[48:51], v[128:131], v[12:15]\n"
    "s_waitcnt lgkmcnt(0)\n"
    "s_barrier\n"
    "v_mfma_f32_16x16x32_bf16 v[16:19], a[120:123], v[128:131], v[16:19]\n"
    "ds_read_b128 v[188:191], v8\n"
    "ds_read_b128 v[192:195], v8 offset:1024\n"
    "v_mfma_f32_16x16x32_bf16 v[12:15], a[52:55], v[132:135], v[12:15]\n"
    "ds_read2st64_b64 v[184:187], v7 offset1:2\n"
    "ds_read2st64_b64 v[180:183], v6 offset1:2\n"
    "v_cmp_eq_u32 vcc, 0, v77\n"
    "s_cmp_eq_u64 vcc, 0\n"
    "v_mfma_f32_16x16x32_bf16 v[16:19], a[124:127], v[132:135], v[16:19]\n"
    "s_cselect_b32 s94, s42, 0\n"
    "v_mfma_f32_16x16x32_bf16 v[12:15], a[56:59], v[136:139], v[12:15]\n"
    "global_load_dword v77, v76, %1 sc1\n"
    "v_mfma_f32_16x16x32_bf16 v[16:19], a[128:131], v[136:139], v[16:19]\n"
    "buffer_load_dwordx4 v[152:155], v3, s[92:95], s49 offen sc1\n"
    "v_mfma_f32_16x16x32_bf16 v[12:15], a[60:63], v[140:143], v[12:15]\n"
    "buffer_load_dwordx4 v[156:159], v3, s[92:95], s49 offen offset:1024 sc1\n"
    "v_mfma_f32_16x16x32_bf16 v[16:19], a[132:135], v[140:143], v[16:19]\n"
    "v_readfirstlane_b32 s50, v78\n"
    "v_mfma_f32_16x16x32_bf16 v[12:15], a[64:67], v[144:147], v[12:15]\n"
    "buffer_load_dwordx4 v[160:163], v3, s[92:95], s49 offen offset:2048 sc1\n"
    "s_cmp_eq_u32 s50, 3\n"
    "v_mfma_f32_16x16x32_bf16 v[16:19], a[136:139], v[144:147], v[16:19]\n"
    "s_waitcnt lgkmcnt(0)\n"
    "v_add_f32 v180, v180, v182\n"
    "v_add_f32 v181, v181, v183\n"
    "s_cselect_b64 exec, 1, 0\n"
    "global_store_dword v79, v78, %1 sc1\n"
    "s_mov_b64 exec, -1\n"
    "v_mfma_f32_16x16x32_bf16 v[12:15], a[68:71], v[148:151], v[12:15]\n"
    "v_cvt_pk_bf16_f32 v198, v180, v181\n"
    "v_add_f32 v184, v184, v186\n"
    "buffer_load_dwordx4 v[164:167], v3, s[92:95], s49 offen offset:3072 sc1\n"
    "v_mfma_f32_16x16x32_bf16 v[16:19], a[140:143], v[148:151], v[16:19]\n"
    "v_add_f32 v185, v185, v187\n"
    "v_cvt_pk_bf16_f32 v214, v184, v185\n"
    "buffer_load_dwordx4 v[168:171], v4, s[92:95], s49 offen sc1\n"
    "s_branch .Lak_%=\n"
    ".Laq1_%=:\n"
    "ds_read_b128 v[116:119], v0 offset:11264\n"
    "ds_read_b128 v[124:127], v0 offset:13312\n"
    "ds_read_b128 v[120:123], v0 offset:12288\n"
    "ds_read_b128 v[136:139], v0 offset:17408\n"
    "ds_read_b128 v[132:135], v0 offset:16384\n"
    "ds_read_b128 v[128:131], v0 offset:15360\n"
    "ds_read_b128 v[148:151], v0 offset:9216\n"
    "ds_read_b128 v[144:147], v0 offset:4096\n"
    "ds_read_b128 v[140:143], v0 offset:18432\n"
    "global_load_dword v77, v76, %1 sc1\n"
    "ds_write_b128 v73, v[48:51] offset:20480\n"
    "v_mfma_f32_16x16x32_bf16 v[52:55], v[56:59], v[152:155], 0\n"
    "v_mfma_f32_16x16x32_bf16 v[52:55], v[56:59], v[156:159], v[52:55]\n"
    "v_mfma_f32_16x16x32_bf16 v[52:55], v[56:59], v[160:163], v[52:55]\n"
    "v_mfma_f32_16x16x32_bf16 v[52:55], v[56:59], v[164:167], v[52:55]\n"
    "v_mfma_f32_16x16x32_bf16 v[52:55], v[56:59], v[168:171], v[52:55]\n"
    "s_waitcnt lgkmcnt(9)\n"
    "v_mfma_f32_16x16x32_bf16 v[24:27], a[252:255], v[116:119], 0\n"
    "ds_read_b128 v[80:83], v0\n"
    "ds_read_b128 v[84:87], v0 offset:1024\n"
    "s_waitcnt lgkmcnt(9)\n"
    "v_mfma_f32_16x16x32_bf16 v[24:27], v[224:227], v[120:123], v[24:27]\n"
    "ds_read_b128 v[88:91], v0 offset:2048\n"
    "ds_read_b128 v[92:95], v0 offset:3072\n"
    "v_mfma_f32_16x16x32_bf16 v[24:27], v[228:231], v[124:127], v[24:27]\n"
    "ds_write_b128 v73, v[52:55] offset:24576\n"
    "ds_read_b128 v[96:99], v0 offset:5120\n"
    "s_waitcnt lgkmcnt(10)\n"
    "v_mfma_f32_16x16x32_bf16 v[24:27], v[232:235], v[128:131], v[24:27]\n"
    "ds_read_b128 v[100:103], v0 offset:6144\n"
    "ds_read_b128 v[104:107], v0 offset:7168\n"
    "v_mfma_f32_16x16x32_bf16 v[24:27], v[236:239], v[132:135], v[24:27]\n"
    "ds_read_b128 v[108:111], v0 offset:8192\n"
    "ds_read_b128 v[112:115], v0 offset:10240\n"
    "v_mfma_f32_16x16x32_bf16 v[24:27], v[240:243], v[136:139], v[24:27]\n"
    "s_waitcnt lgkmcnt(11)\n"
    "v_mfma_f32_16x16x32_bf16 v[24:27], v[244:247], v[140:143], v[24:27]\n"
    "v_mfma_f32_16x16x32_bf16 v[24:27], v[248:251], v[144:147], v[24:27]\n"
    "s_waitcnt lgkmcnt(0)\n"
    "s_barrier\n"
    "v_mfma_f32_16x16x32_bf16 v[24:27], v[252:255], v[148:151], v[24:27]\n"
    "ds_read2st64_b32 v[48:49], v10 offset0:80 offset1:84\n"
    "ds_read2st64_b32 v[50:51], v10 offset0:88 offset1:92\n"
    "v_mfma_f32_16x16x32_bf16 v[12:15], a[36:39], v[116:119], 0\n"
    "ds_read2st64_b32 v[54:55], v10 offset0:104 offset1:108\n"
    "ds_read2st64_b32 v[52:53], v10 offset0:96 offset1:100\n"
    "v_mfma_f32_16x16x32_bf16 v[16:19], a[108:111], v[116:119], 0\n"
    "v_min_f32 v40, v74, v68\n"
    "v_min_f32 v41, v74, v69\n"
    "v_mfma_f32_16x16x32_bf16 v[12:15], a[40:43], v[120:123], v[12:15]\n"
    "v_min_f32 v42, v74, v70\n"
    "v_min_f32 v43, v74, v71\n"
    "v_mfma_f32_16x16x32_bf16 v[16:19], a[112:115], v[120:123], v[16:19]\n"
    "v_mfma_f32_16x16x32_bf16 v[12:15], a[44:47], v[124:127], v[12:15]\n"
    "s_waitcnt lgkmcnt(0)\n"
    "v_add_f32 v52, v52, v53\n"
    "v_add_f32 v54, v54, v55\n"
    "v_mfma_f32_16x16x32_bf16 v[16:19], a[116:119], v[124:127], v[16:19]\n"
    "v_add_f32 v48, v48, v49\n"
    "v_add_f32 v50, v50, v51\n"
    "v_mfma_f32_16x16x32_bf16 v[12:15], a[48:51], v[128:131], v[12:15]\n"
    "v_add_f32 v52, v52, v54\n"
    "v_add_f32 v50, v48, v50\n"
    "v_mfma_f32_16x16x32_bf16 v[16:19], a[120:123], v[128:131], v[16:19]\n"
    "v_mul_f32 v52, 0xbae38e39, v52\n"
    "v_mul_f32 v50, 0x3ae38e39, v50\n"
    "v_mfma_f32_16x16x32_bf16 v[12:15], a[52:55], v[132:135], v[12:15]\n"
    "v_cvt_pk_bf16_f32 v216, v52, v52\n"
    "v_fma_f32 v50, -v52, v52, v50\n"
    "v_mfma_f32_16x16x32_bf16 v[16:19], a[124:127], v[132:135], v[16:19]\n"
    "v_cvt_pk_bf16_f32 v217, v52, v52\n"
    "v_max_f32 v50, 0, v50\n"
    "v_mfma_f32_16x16x32_bf16 v[12:15], a[56:59], v[136:139], v[12:15]\n"
    "v_cvt_pk_bf16_f32 v50, 0, v50\n"
    "v_add_f32 v50, 0x3727c5ac, v50\n"
    "v_mfma_f32_16x16x32_bf16 v[16:19], a[128:131], v[136:139], v[16:19]\n"
    "v_cvt_pk_bf16_f32 v50, 0, v50\n"
    "v_sqrt_f32 v50, v50\n"
    "v_mfma_f32_16x16x32_bf16 v[12:15], a[60:63], v[140:143], v[12:15]\n"
    "v_cvt_pk_bf16_f32 v50, 0, v50\n"
    "v_mfma_f32_16x16x32_bf16 v[16:19], a[132:135], v[140:143], v[16:19]\n"
    "v_rcp_f32 v50, v50\n"
    "v_mfma_f32_16x16x32_bf16 v[12:15], a[64:67], v[144:147], v[12:15]\n"
    "ds_write_b32 v9, v50\n"
    "ds_read_b128 v[200:203], v72 offset:28672\n"
    "v_mfma_f32_16x16x32_bf16 v[16:19], a[136:139], v[144:147], v[16:19]\n"
    "v_mfma_f32_16x16x32_bf16 v[12:15], v[60:63], v[216:219], v[12:15]\n"
    "v_mfma_f32_16x16x32_bf16 v[16:19], v[62:65], v[216:219], v[16:19]\n"
    "v_mfma_f32_16x16x32_bf16 v[12:15], a[68:71], v[148:151], v[12:15]\n"
    "v_mul_f32 v24, v24, v50\n"
    "v_mul_f32 v25, v25, v50\n"
    "v_mfma_f32_16x16x32_bf16 v[16:19], a[140:143], v[148:151], v[16:19]\n"
    "v_mul_f32 v26, v26, v50\n"
    "v_mul_f32 v27, v27, v50\n"
    "v_mfma_f32_16x16x32_bf16 v[12:15], a[0:3], v[80:83], v[12:15]\n"
    "ds_write_b128 v5, v[24:27]\n"
    "v_mfma_f32_16x16x32_bf16 v[16:19], a[72:75], v[80:83], v[16:19]\n"
    "v_mfma_f32_16x16x32_bf16 v[12:15], a[4:7], v[84:87], v[12:15]\n"
    "v_mfma_f32_16x16x32_bf16 v[16:19], a[76:79], v[84:87], v[16:19]\n"
    "v_mfma_f32_16x16x32_bf16 v[12:15], a[8:11], v[88:91], v[12:15]\n"
    "s_waitcnt vmcnt(0)\n"
    "v_mov_b32 v78, 1\n"
    "v_mfma_f32_16x16x32_bf16 v[16:19], a[80:83], v[88:91], v[16:19]\n"
    "s_mov_b64 exec, s[56:57]\n"
    "ds_add_rtn_u32 v78, v11, v78 offset:16\n"
    "s_mov_b64 exec, -1\n"
    "v_mfma_f32_16x16x32_bf16 v[12:15], a[12:15], v[92:95], v[12:15]\n"
    "s_waitcnt lgkmcnt(0)\n"
    "s_barrier\n"
    "v_mfma_f32_16x16x32_bf16 v[16:19], a[84:87], v[92:95], v[16:19]\n"
    "ds_read_b128 v[188:191], v8\n"
    "ds_read_b128 v[192:195], v8 offset:1024\n"
    "v_mfma_f32_16x16x32_bf16 v[12:15], a[16:19], v[96:99], v[12:15]\n"
    "ds_read2st64_b64 v[184:187], v7 offset1:2\n"
    "ds_read2st64_b64 v[180:183], v6 offset1:2\n"
    "v_cmp_eq_u32 vcc, 0, v77\n"
    "s_cmp_eq_u64 vcc, 0\n"
    "v_mfma_f32_16x16x32_bf16 v[16:19], a[88:91], v[96:99], v[16:19]\n"
    "s_cselect_b32 s94, s42, 0\n"
    "v_mfma_f32_16x16x32_bf16 v[12:15], a[20:23], v[100:103], v[12:15]\n"
    "global_load_dword v77, v76, %1 sc1\n"
    "v_mfma_f32_16x16x32_bf16 v[16:19], a[92:95], v[100:103], v[16:19]\n"
    "buffer_load_dwordx4 v[152:155], v3, s[92:95], s49 offen sc1\n"
    "v_mfma_f32_16x16x32_bf16 v[12:15], a[24:27], v[104:107], v[12:15]\n"
    "buffer_load_dwordx4 v[156:159], v3, s[92:95], s49 offen offset:1024 sc1\n"
    "v_mfma_f32_16x16x32_bf16 v[16:19], a[96:99], v[104:107], v[16:19]\n"
    "v_readfirstlane_b32 s50, v78\n"
    "v_mfma_f32_16x16x32_bf16 v[12:15], a[28:31], v[108:111], v[12:15]\n"
    "buffer_load_dwordx4 v[160:163], v3, s[92:95], s49 offen offset:2048 sc1\n"
    "s_cmp_eq_u32 s50, 3\n"
    "v_mfma_f32_16x16x32_bf16 v[16:19], a[100:103], v[108:111], v[16:19]\n"
    "s_waitcnt lgkmcnt(0)\n"
    "v_add_f32 v180, v180, v182\n"
    "v_add_f32 v181, v181, v183\n"
    "s_cselect_b64 exec, 1, 0\n"
    "global_store_dword v79, v78, %1 sc1\n"
    "s_mov_b64 exec, -1\n"
    "v_mfma_f32_16x16x32_bf16 v[12:15], a[32:35], v[112:115], v[12:15]\n"
    "v_cvt_pk_bf16_f32 v198, v180, v181\n"
    "v_add_f32 v184, v184, v186\n"
    "buffer_load_dwordx4 v[164:167], v3, s[92:95], s49 offen offset:3072 sc1\n"
    "v_mfma_f32_16x16x32_bf16 v[16:19], a[104:107], v[112:115], v[16:19]\n"
    "v_add_f32 v185, v185, v187\n"
    "v_cvt_pk_bf16_f32 v214, v184, v185\n"
    "buffer_load_dwordx4 v[168:171], v4, s[92:95], s49 offen sc1\n"
    "s_branch .Lak_%=\n"
    ".Laq2_%=:\n"
    "ds_read_b128 v[80:83], v0\n"
    "ds_read_b128 v[88:91], v0 offset:2048\n"
    "ds_read_b128 v[84:87], v0 offset:1024\n"
    "ds_read_b128 v[100:103], v0 offset:6144\n"
    "ds_read_b128 v[96:99], v0 offset:5120\n"
    "ds_read_b128 v[92:95], v0 offset:3072\n"
    "ds_read_b128 v[112:115], v0 offset:10240\n"
    "ds_read_b128 v[108:111], v0 offset:8192\n"
    "ds_read_b128 v[104:107], v0 offset:7168\n"
    "global_load_dword v77, v76, %1 sc1\n"
    "ds_write_b128 v73, v[48:51] offset:20480\n"
    "v_mfma_f32_16x16x32_bf16 v[52:55], v[56:59], v[152:155], 0\n"
    "v_mfma_f32_16x16x32_bf16 v[52:55], v[56:59], v[156:159], v[52:55]\n"
    "v_mfma_f32_16x16x32_bf16 v[52:55], v[56:59], v[160:163], v[52:55]\n"
    "v_mfma_f32_16x16x32_bf16 v[52:55], v[56:59], v[164:167], v[52:55]\n"
    "v_mfma_f32_16x16x32_bf16 v[52:55], v[56:59], v[168:171], v[52:55]\n"
    "s_waitcnt lgkmcnt(9)\n"
    "v_mfma_f32_16x16x32_bf16 v[24:27], v[80:83], a[216:219], 0\n"
    "ds_read_b128 v[116:119], v0 offset:11264\n"
    "ds_read_b128 v[120:123], v0 offset:12288\n"
    "s_waitcnt lgkmcnt(9)\n"
    "v_mfma_f32_16x16x32_bf16 v[24:27], v[84:87], a[220:223], v[24:27]\n"
    "ds_read_b128 v[124:127], v0 offset:13312\n"
    "ds_read_b128 v[128:131], v0 offset:15360\n"
    "v_mfma_f32_16x16x32_bf16 v[24:27], v[88:91], a[224:227], v[24:27]\n"
    "ds_write_b128 v73, v[52:55] offset:24576\n"
    "ds_read_b128 v[132:135], v0 offset:16384\n"
    "s_waitcnt lgkmcnt(10)\n"
    "v_mfma_f32_16x16x32_bf16 v[24:27], v[92:95], a[228:231], v[24:27]\n"
    "ds_read_b128 v[136:139], v0 offset:17408\n"
    "ds_read_b128 v[140:143], v0 offset:18432\n"
    "v_mfma_f32_16x16x32_bf16 v[24:27], v[96:99], a[232:235], v[24:27]\n"
    "ds_read_b128 v[144:147], v0 offset:4096\n"
    "ds_read_b128 v[148:151], v0 offset:9216\n"
    "v_mfma_f32_16x16x32_bf16 v[24:27], v[100:103], a[236:239], v[24:27]\n"
    "s_waitcnt lgkmcnt(11)\n"
    "v_mfma_f32_16x16x32_bf16 v[24:27], v[104:107], a[240:243], v[24:27]\n"
    "v_mfma_f32_16x16x32_bf16 v[24:27], v[108:111], a[244:247], v[24:27]\n"
    "s_waitcnt lgkmcnt(0)\n"
    "s_barrier\n"
    "v_mfma_f32_16x16x32_bf16 v[24:27], v[112:115], a[248:251], v[24:27]\n"
    "ds_read2st64_b32 v[48:49], v10 offset0:80 offset1:84\n"
    "ds_read2st64_b32 v[50:51], v10 offset0:88 offset1:92\n"
    "v_mfma_f32_16x16x32_bf16 v[12:15], a[0:3], v[80:83], 0\n"
    "ds_read2st64_b32 v[54:55], v10 offset0:104 offset1:108\n"
    "ds_read2st64_b32 v[52:53], v10 offset0:96 offset1:100\n"
    "v_mfma_f32_16x16x32_bf16 v[16:19], a[72:75], v[80:83], 0\n"
    "v_min_f32 v40, v74, v68\n"
    "v_min_f32 v41, v74, v69\n"
    "v_mfma_f32_16x16x32_bf16 v[12:15], a[4:7], v[84:87], v[12:15]\n"
    "v_min_f32 v42, v74, v70\n"
    "v_min_f32 v43, v74, v71\n"
    "v_mfma_f32_16x16x32_bf16 v[16:19], a[76:79], v[84:87], v[16:19]\n"
    "v_mfma_f32_16x16x32_bf16 v[12:15], a[8:11], v[88:91], v[12:15]\n"
    "s_waitcnt lgkmcnt(0)\n"
    "v_add_f32 v52, v52, v53\n"
    "v_add_f32 v54, v54, v55\n"
    "v_mfma_f32_16x16x32_bf16 v[16:19], a[80:83], v[88:91], v[16:19]\n"
    "v_add_f32 v48, v48, v49\n"
    "v_add_f32 v50, v50, v51\n"
    "v_mfma_f32_16x16x32_bf16 v[12:15], a[12:15], v[92:95], v[12:15]\n"
    "v_add_f32 v52, v52, v54\n"
    "v_add_f32 v50, v48, v50\n"
    "v_mfma_f32_16x16x32_bf16 v[16:19], a[84:87], v[92:95], v[16:19]\n"
    "v_mul_f32 v52, 0xbae38e39, v52\n"
    "v_mul_f32 v50, 0x3ae38e39, v50\n"
    "v_mfma_f32_16x16x32_bf16 v[12:15], a[16:19], v[96:99], v[12:15]\n"
    "v_cvt_pk_bf16_f32 v216, v52, v52\n"
    "v_fma_f32 v50, -v52, v52, v50\n"
    "v_mfma_f32_16x16x32_bf16 v[16:19], a[88:91], v[96:99], v[16:19]\n"
    "v_cvt_pk_bf16_f32 v217, v52, v52\n"
    "v_max_f32 v50, 0, v50\n"
    "v_mfma_f32_16x16x32_bf16 v[12:15], a[20:23], v[100:103], v[12:15]\n"
    "v_cvt_pk_bf16_f32 v50, 0, v50\n"
    "v_add_f32 v50, 0x3727c5ac, v50\n"
    "v_mfma_f32_16x16x32_bf16 v[16:19], a[92:95], v[100:103], v[16:19]\n"
    "v_mfma_f32_16x16x32_bf16 v[24:27], v[216:219], v[66:69], v[24:27]\n"
    "v_cvt_pk_bf16_f32 v50, 0, v50\n"
    "v_mfma_f32_16x16x32_bf16 v[12:15], a[24:27], v[104:107], v[12:15]\n"
    "v_sqrt_f32 v50, v50\n"
    "v_mfma_f32_16x16x32_bf16 v[16:19], a[96:99], v[104:107], v[16:19]\n"
    "v_cvt_pk_bf16_f32 v50, 0, v50\n"
    "v_rcp_f32 v50, v50\n"
    "v_mfma_f32_16x16x32_bf16 v[12:15], a[28:31], v[108:111], v[12:15]\n"
    "ds_write_b32 v9, v50\n"
    "v_mfma_f32_16x16x32_bf16 v[16:19], a[100:103], v[108:111], v[16:19]\n"
    "ds_read_b128 v[200:203], v72 offset:28672\n"
    "v_mfma_f32_16x16x32_bf16 v[12:15], v[60:63], v[216:219], v[12:15]\n"
    "v_mfma_f32_16x16x32_bf16 v[12:15], a[32:35], v[112:115], v[12:15]\n"
    "v_mfma_f32_16x16x32_bf16 v[16:19], v[62:65], v[216:219], v[16:19]\n"
    "v_mfma_f32_16x16x32_bf16 v[16:19], a[104:107], v[112:115], v[16:19]\n"
    "v_mfma_f32_16x16x32_bf16 v[12:15], a[36:39], v[116:119], v[12:15]\n"
    "v_mfma_f32_16x16x32_bf16 v[16:19], a[108:111], v[116:119], v[16:19]\n"
    "s_waitcnt lgkmcnt(0)\n"
    "v_mul_f32 v24, v24, v200\n"
    "v_mul_f32 v25, v25, v201\n"
    "v_mfma_f32_16x16x32_bf16 v[12:15], a[40:43], v[120:123], v[12:15]\n"
    "v_mul_f32 v26, v26, v202\n"
    "v_mul_f32 v27, v27, v203\n"
    "v_mfma_f32_16x16x32_bf16 v[16:19], a[112:115], v[120:123], v[16:19]\n"
    "ds_write_b128 v5, v[24:27]\n"
    "v_mfma_f32_16x16x32_bf16 v[12:15], a[44:47], v[124:127], v[12:15]\n"
    "s_waitcnt vmcnt(0)\n"
    "v_mov_b32 v78, 1\n"
    "v_mfma_f32_16x16x32_bf16 v[16:19], a[116:119], v[124:127], v[16:19]\n"
    "s_mov_b64 exec, s[56:57]\n"
    "ds_add_rtn_u32 v78, v11, v78 offset:16\n"
    "s_mov_b64 exec, -1\n"
    "v_mfma_f32_16x16x32_bf16 v[12:15], a[48:51], v[128:131], v[12:15]\n"
    "s_waitcnt lgkmcnt(0)\n"
    "s_barrier\n"
    "v_mfma_f32_16x16x32_bf16 v[16:19], a[120:123], v[128:131], v[16:19]\n"
    "ds_read_b128 v[188:191], v8\n"
    "ds_read_b128 v[192:195], v8 offset:1024\n"
    "v_mfma_f32_16x16x32_bf16 v[12:15], a[52:55], v[132:135], v[12:15]\n"
    "ds_read2st64_b64 v[184:187], v7 offset1:2\n"
    "ds_read2st64_b64 v[180:183], v6 offset1:2\n"
    "v_cmp_eq_u32 vcc, 0, v77\n"
    "s_cmp_eq_u64 vcc, 0\n"
    "v_mfma_f32_16x16x32_bf16 v[16:19], a[124:127], v[132:135], v[16:19]\n"
    "s_cselect_b32 s94, s42, 0\n"
    "v_mfma_f32_16x16x32_bf16 v[12:15], a[56:59], v[136:139], v[12:15]\n"
    "global_load_dword v77, v76, %1 sc1\n"
    "v_mfma_f32_16x16x32_bf16 v[16:19], a[128:131], v[136:139], v[16:19]\n"
    "buffer_load_dwordx4 v[152:155], v3, s[92:95], s49 offen sc1\n"
    "v_mfma_f32_16x16x32_bf16 v[12:15], a[60:63], v[140:143], v[12:15]\n"
    "buffer_load_dwordx4 v[156:159], v3, s[92:95], s49 offen offset:1024 sc1\n"
    "v_mfma_f32_16x16x32_bf16 v[16:19], a[132:135], v[140:143], v[16:19]\n"
    "v_readfirstlane_b32 s50, v78\n"
    "v_mfma_f32_16x16x32_bf16 v[12:15], a[64:67], v[144:147], v[12:15]\n"
    "buffer_load_dwordx4 v[160:163], v3, s[92:95], s49 offen offset:2048 sc1\n"
    "s_cmp_eq_u32 s50, 3\n"
    "v_mfma_f32_16x16x32_bf16 v[16:19], a[136:139], v[144:147], v[16:19]\n"
    "s_waitcnt lgkmcnt(0)\n"
    "v_add_f32 v180, v180, v182\n"
    "v_add_f32 v181, v181, v183\n"
    "s_cselect_b64 exec, 1, 0\n"
    "global_store_dword v79, v78, %1 sc1\n"
    "s_mov_b64 exec, -1\n"
    "v_mfma_f32_16x16x32_bf16 v[12:15], a[68:71], v[148:151], v[12:15]\n"
    "v_cvt_pk_bf16_f32 v198, v180, v181\n"
    "v_add_f32 v184, v184, v186\n"
    "buffer_load_dwordx4 v[164:167], v3, s[92:95], s49 offen offset:3072 sc1\n"
    "v_mfma_f32_16x16x32_bf16 v[16:19], a[140:143], v[148:151], v[16:19]\n"
    "v_add_f32 v185, v185, v187\n"
    "v_cvt_pk_bf16_f32 v214, v184, v185\n"
    "buffer_load_dwordx4 v[168:171], v4, s[92:95], s49 offen sc1\n"
    "s_branch .Lak_%=\n"
    ".Laq3_%=:\n"
    "ds_read_b128 v[116:119], v0 offset:11264\n"
    "ds_read_b128 v[124:127], v0 offset:13312\n"
    "ds_read_b128 v[120:123], v0 offset:12288\n"
    "ds_read_b128 v[136:139], v0 offset:17408\n"
    "ds_read_b128 v[132:135], v0 offset:16384\n"
    "ds_read_b128 v[128:131], v0 offset:15360\n"
    "ds_read_b128 v[148:151], v0 offset:9216\n"
    "ds_read_b128 v[144:147], v0 offset:4096\n"
    "ds_read_b128 v[140:143], v0 offset:18432\n"
    "global_load_dword v77, v76, %1 sc1\n"
    "ds_write_b128 v73, v[48:51] offset:20480\n"
    "v_mfma_f32_16x16x32_bf16 v[52:55], v[56:59], v[152:155], 0\n"
    "v_mfma_f32_16x16x32_bf16 v[52:55], v[56:59], v[156:159], v[52:55]\n"
    "v_mfma_f32_16x16x32_bf16 v[52:55], v[56:59], v[160:163], v[52:55]\n"
    "v_mfma_f32_16x16x32_bf16 v[52:55], v[56:59], v[164:167], v[52:55]\n"
    "v_mfma_f32_16x16x32_bf16 v[52:55], v[56:59], v[168:171], v[52:55]\n"
    "s_waitcnt lgkmcnt(9)\n"
    "v_mfma_f32_16x16x32_bf16 v[24:27], v[116:119], a[252:255], 0\n"
    "ds_read_b128 v[80:83], v0\n"
    "ds_read_b128 v[84:87], v0 offset:1024\n"
    "s_waitcnt lgkmcnt(9)\n"
    "v_mfma_f32_16x16x32_bf16 v[24:27], v[120:123], v[224:227], v[24:27]\n"
    "ds_read_b128 v[88:91], v0 offset:2048\n"
    "ds_read_b128 v[92:95], v0 offset:3072\n"
    "v_mfma_f32_16x16x32_bf16 v[24:27], v[124:127], v[228:231], v[24:27]\n"
    "ds_write_b128 v73, v[52:55] offset:24576\n"
    "ds_read_b128 v[96:99], v0 offset:5120\n"
    "s_waitcnt lgkmcnt(10)\n"
    "v_mfma_f32_16x16x32_bf16 v[24:27], v[128:131], v[232:235], v[24:27]\n"
    "ds_read_b128 v[100:103], v0 offset:6144\n"
    "ds_read_b128 v[104:107], v0 offset:7168\n"
    "v_mfma_f32_16x16x32_bf16 v[24:27], v[132:135], v[236:239], v[24:27]\n"
    "ds_read_b128 v[108:111], v0 offset:8192\n"
    "ds_read_b128 v[112:115], v0 offset:10240\n"
    "v_mfma_f32_16x16x32_bf16 v[24:27], v[136:139], v[240:243], v[24:27]\n"
    "s_waitcnt lgkmcnt(11)\n"
    "v_mfma_f32_16x16x32_bf16 v[24:27], v[140:143], v[244:247], v[24:27]\n"
    "v_mfma_f32_16x16x32_bf16 v[24:27], v[144:147], v[248:251], v[24:27]\n"
    "s_waitcnt lgkmcnt(0)\n"
    "s_barrier\n"
    "v_mfma_f32_16x16x32_bf16 v[24:27], v[148:151], v[252:255], v[24:27]\n"
    "ds_read2st64_b32 v[48:49], v10 offset0:80 offset1:84\n"
    "ds_read2st64_b32 v[50:51], v10 offset0:88 offset1:92\n"
    "v_mfma_f32_16x16x32_bf16 v[12:15], a[36:39], v[116:119], 0\n"
    "ds_read2st64_b32 v[54:55], v10 offset0:104 offset1:108\n"
    "ds_read2st64_b32 v[52:53], v10 offset0:96 offset1:100\n"
    "v_mfma_f32_16x16x32_bf16 v[16:19], a[108:111], v[116:119], 0\n"
    "v_min_f32 v40, v74, v68\n"
    "v_min_f32 v41, v74, v69\n"
    "v_mfma_f32_16x16x32_bf16 v[12:15], a[40:43], v[120:123], v[12:15]\n"
    "v_min_f32 v42, v74, v70\n"
    "v_min_f32 v43, v74, v71\n"
    "v_mfma_f32_16x16x32_bf16 v[16:19], a[112:115], v[120:123], v[16:19]\n"
    "v_mfma_f32_16x16x32_bf16 v[12:15], a[44:47], v[124:127], v[12:15]\n"
    "s_waitcnt lgkmcnt(0)\n"
    "v_add_f32 v52, v52, v53\n"
    "v_add_f32 v54, v54, v55\n"
    "v_mfma_f32_16x16x32_bf16 v[16:19], a[116:119], v[124:127], v[16:19]\n"
    "v_add_f32 v48, v48, v49\n"
    "v_add_f32 v50, v50, v51\n"
    "v_mfma_f32_16x16x32_bf16 v[12:15], a[48:51], v[128:131], v[12:15]\n"
    "v_add_f32 v52, v52, v54\n"
    "v_add_f32 v50, v48, v50\n"
    "v_mfma_f32_16x16x32_bf16 v[16:19], a[120:123], v[128:131], v[16:19]\n"
    "v_mul_f32 v52, 0xbae38e39, v52\n"
    "v_mul_f32 v50, 0x3ae38e39, v50\n"
    "v_mfma_f32_16x16x32_bf16 v[12:15], a[52:55], v[132:135], v[12:15]\n"
    "v_cvt_pk_bf16_f32 v216, v52, v52\n"
    "v_fma_f32 v50, -v52, v52, v50\n"
    "v_mfma_f32_16x16x32_bf16 v[16:19], a[124:127], v[132:135], v[16:19]\n"
    "v_cvt_pk_bf16_f32 v217, v52, v52\n"
    "v_max_f32 v50, 0, v50\n"
    "v_mfma_f32_16x16x32_bf16 v[12:15], a[56:59], v[136:139], v[12:15]\n"
    "v_cvt_pk_bf16_f32 v50, 0, v50\n"
    "v_add_f32 v50, 0x3727c5ac, v50\n"
    "v_mfma_f32_16x16x32_bf16 v[16:19], a[128:131], v[136:139], v[16:19]\n"
    "v_cvt_pk_bf16_f32 v50, 0, v50\n"
    "v_sqrt_f32 v50, v50\n"
    "v_mfma_f32_16x16x32_bf16 v[12:15], a[60:63], v[140:143], v[12:15]\n"
    "v_cvt_pk_bf16_f32 v50, 0, v50\n"
    "v_mfma_f32_16x16x32_bf16 v[16:19], a[132:135], v[140:143], v[16:19]\n"
    "v_rcp_f32 v50, v50\n"
    "v_mfma_f32_16x16x32_bf16 v[12:15], a[64:67], v[144:147], v[12:15]\n"
    "ds_write_b32 v9, v50\n"
    "ds_read_b128 v[200:203], v72 offset:28672\n"
    "v_mfma_f32_16x16x32_bf16 v[16:19], a[136:139], v[144:147], v[16:19]\n"
    "v_mfma_f32_16x16x32_bf16 v[12:15], v[60:63], v[216:219], v[12:15]\n"
    "v_mfma_f32_16x16x32_bf16 v[16:19], v[62:65], v[216:219], v[16:19]\n"
    "v_mfma_f32_16x16x32_bf16 v[12:15], a[68:71], v[148:151], v[12:15]\n"
    "v_mfma_f32_16x16x32_bf16 v[16:19], a[140:143], v[148:151], v[16:19]\n"
    "v_mfma_f32_16x16x32_bf16 v[12:15], a[0:3], v[80:83], v[12:15]\n"
    "s_waitcnt lgkmcnt(0)\n"
    "v_mul_f32 v24, v24, v200\n"
    "v_mul_f32 v25, v25, v201\n"
    "v_mfma_f32_16x16x32_bf16 v[16:19], a[72:75], v[80:83], v[16:19]\n"
    "v_mul_f32 v26, v26, v202\n"
    "v_mul_f32 v27, v27, v203\n"
    "v_mfma_f32_16x16x32_bf16 v[12:15], a[4:7], v[84:87], v[12:15]\n"
    "ds_write_b128 v5, v[24:27]\n"
    "v_mfma_f32_16x16x32_bf16 v[16:19], a[76:79], v[84:87], v[16:19]\n"
    "v_mfma_f32_16x16x32_bf16 v[12:15], a[8:11], v[88:91], v[12:15]\n"
    "s_waitcnt vmcnt(0)\n"
    "v_mov_b32 v78, 1\n"
    "v_mfma_f32_16x16x32_bf16 v[16:19], a[80:83], v[88:91], v[16:19]\n"
    "s_mov_b64 exec, s[56:57]\n"
    "ds_add_rtn_u32 v78, v11, v78 offset:16\n"
    "s_mov_b64 exec, -1\n"
    "v_mfma_f32_16x16x32_bf16 v[12:15], a[12:15], v[92:95], v[12:15]\n"
    "s_waitcnt lgkmcnt(0)\n"
    "s_barrier\n"
    "v_mfma_f32_16x16x32_bf16 v[16:19], a[84:87], v[92:95], v[16:19]\n"
    "ds_read_b128 v[188:191], v8\n"
    "ds_read_b128 v[192:195], v8 offset:1024\n"
    "v_mfma_f32_16x16x32_bf16 v[12:15], a[16:19], v[96:99], v[12:15]\n"
    "ds_read2st64_b64 v[184:187], v7 offset1:2\n"
    "ds_read2st64_b64 v[180:183], v6 offset1:2\n"
    "v_cmp_eq_u32 vcc, 0, v77\n"
    "s_cmp_eq_u64 vcc, 0\n"
    "v_mfma_f32_16x16x32_bf16 v[16:19], a[88:91], v[96:99], v[16:19]\n"
    "s_cselect_b32 s94, s42, 0\n"
    "v_mfma_f32_16x16x32_bf16 v[12:15], a[20:23], v[100:103], v[12:15]\n"
    "global_load_dword v77, v76, %1 sc1\n"
    "v_mfma_f32_16x16x32_bf16 v[16:19], a[92:95], v[100:103], v[16:19]\n"
    "buffer_load_dwordx4 v[152:155], v3, s[92:95], s49 offen sc1\n"
    "v_mfma_f32_16x16x32_bf16 v[12:15], a[24:27], v[104:107], v[12:15]\n"
    "buffer_load_dwordx4 v[156:159], v3, s[92:95], s49 offen offset:1024 sc1\n"
    "v_mfma_f32_16x16x32_bf16 v[16:19], a[96:99], v[104:107], v[16:19]\n"
    "v_readfirstlane_b32 s50, v78\n"
    "v_mfma_f32_16x16x32_bf16 v[12:15], a[28:31], v[108:111], v[12:15]\n"
    "buffer_load_dwordx4 v[160:163], v3, s[92:95], s49 offen offset:2048 sc1\n"
    "s_cmp_eq_u32 s50, 3\n"
    "v_mfma_f32_16x16x32_bf16 v[16:19], a[100:103], v[108:111], v[16:19]\n"
    "s_waitcnt lgkmcnt(0)\n"
    "v_add_f32 v180, v180, v182\n"
    "v_add_f32 v181, v181, v183\n"
    "s_cselect_b64 exec, 1, 0\n"
    "global_store_dword v79, v78, %1 sc1\n"
    "s_mov_b64 exec, -1\n"
    "v_mfma_f32_16x16x32_bf16 v[12:15], a[32:35], v[112:115], v[12:15]\n"
    "v_cvt_pk_bf16_f32 v198, v180, v181\n"
    "v_add_f32 v184, v184, v186\n"
    "buffer_load_dwordx4 v[164:167], v3, s[92:95], s49 offen offset:3072 sc1\n"
    "v_mfma_f32_16x16x32_bf16 v[16:19], a[104:107], v[112:115], v[16:19]\n"
    "v_add_f32 v185, v185, v187\n"
    "v_cvt_pk_bf16_f32 v214, v184, v185\n"
    "buffer_load_dwordx4 v[168:171], v4, s[92:95], s49 offen sc1\n"
    ".Lak_%=:\n"
    "v_mfma_f32_16x16x32_bf16 v[20:23], v[216:219], v[64:67], 0\n"
    "v_add_f32 v188, v188, v192\n"
    "v_add_f32 v189, v189, v193\n"
    "v_mfma_f32_16x16x32_bf16 v[20:23], v[80:83], a[144:147], v[20:23]\n"
    "v_add_f32 v190, v190, v194\n"
    "v_add_f32 v191, v191, v195\n"
    "v_mfma_f32_16x16x32_bf16 v[20:23], v[84:87], a[148:151], v[20:23]\n"
    "v_mul_f32 v12, v12, v50\n"
    "v_mul_f32 v13, v13, v50\n"
    "v_mfma_f32_16x16x32_bf16 v[20:23], v[88:91], a[152:155], v[20:23]\n"
    "v_mul_f32 v14, v14, v50\n"
    "v_mul_f32 v15, v15, v50\n"
    "v_mfma_f32_16x16x32_bf16 v[20:23], v[92:95], a[156:159], v[20:23]\n"
    "v_cvt_pk_bf16_f32 v196, v12, v13\n"
    "v_cvt_pk_bf16_f32 v197, v14, v15\n"
    "v_mfma_f32_16x16x32_bf16 v[20:23], v[96:99], a[160:163], v[20:23]\n"
    "v_mul_f32 v16, v16, v50\n"
    "v_mul_f32 v17, v17, v50\n"
    "v_mfma_f32_16x16x32_bf16 v[20:23], v[100:103], a[164:167], v[20:23]\n"
    "v_mfma_f32_16x16x32_bf16 v[204:207], v[28:31], v[196:199], v[40:43]\n"
    "v_mul_f32 v18, v18, v50\n"
    "v_mfma_f32_16x16x32_bf16 v[20:23], v[104:107], a[168:171], v[20:23]\n"
    "v_mul_f32 v19, v19, v50\n"
    "v_cvt_pk_bf16_f32 v212, v16, v17\n"
    "v_mfma_f32_16x16x32_bf16 v[20:23], v[108:111], a[172:175], v[20:23]\n"
    "v_cvt_pk_bf16_f32 v213, v18, v19\n"
    "v_cvt_pk_bf16_f32 v38, v188, v189\n"
    "v_mfma_f32_16x16x32_bf16 v[20:23], v[112:115], a[176:179], v[20:23]\n"
    "v_cvt_pk_bf16_f32 v39, v190, v191\n"
    "v_mfma_f32_16x16x32_bf16 v[208:211], v[212:215], v[196:199], v[44:47]\n"
    "v_mfma_f32_16x16x32_bf16 v[20:23], v[116:119], a[180:183], v[20:23]\n"
    "v_cndmask_b32 v38, v38, v56, s[86:87]\n"
    "v_cndmask_b32 v39, v39, v56, s[86:87]\n"
    "v_mfma_f32_16x16x32_bf16 v[20:23], v[120:123], a[184:187], v[20:23]\n"
    "v_maximum3_f32 v188, v204, v205, v206\n"
    "v_mov_b32 v28, v212\n"
    "v_mfma_f32_16x16x32_bf16 v[20:23], v[124:127], a[188:191], v[20:23]\n"
    "v_mov_b32 v29, v213\n"
    "v_mov_b32 v30, v214\n"
    "v_mfma_f32_16x16x32_bf16 v[20:23], v[128:131], a[192:195], v[20:23]\n"
    "v_maximum3_f32 v188, v188, v207, v208\n"
    "v_maximum3_f32 v188, v188, v209, v210\n"
    "v_mfma_f32_16x16x32_bf16 v[20:23], v[132:135], a[196:199], v[20:23]\n"
    "v_maximum3_f32 v188, v188, v211, v211\n"
    "v_mov_b32 v189, v188\n"
    "v_mfma_f32_16x16x32_bf16 v[20:23], v[136:139], a[200:203], v[20:23]\n"
    "s_nop 0\n"
    "v_permlane16_swap_b32 v189, v188\n"
    "v_maximum3_f32 v188, v188, v189, v189\n"
    "v_mfma_f32_16x16x32_bf16 v[20:23], v[140:143], a[204:207], v[20:23]\n"
    "v_mov_b32 v189, v188\n"
    "v_mfma_f32_16x16x32_bf16 v[20:23], v[144:147], a[208:211], v[20:23]\n"
    "s_nop 0\n"
    "v_permlane32_swap_b32 v189, v188\n"
    "v_maximum3_f32 v188, v188, v189, v189\n"
    "v_mfma_f32_16x16x32_bf16 v[20:23], v[148:151], a[212:215], v[20:23]\n"
    "v_mul_f32 v188, 0xbeae1a93, v188\n"
    "v_fmamk_f32 v204, v204, 0x3eae1a93, v188\n"
    "v_fmamk_f32 v205, v205, 0x3eae1a93, v188\n"
    "v_fmamk_f32 v206, v206, 0x3eae1a93, v188\n"
    "v_fmamk_f32 v207, v207, 0x3eae1a93, v188\n"
    "v_fmamk_f32 v208, v208, 0x3eae1a93, v188\n"
    "v_fmamk_f32 v209, v209, 0x3eae1a93, v188\n"
    "v_fmamk_f32 v210, v210, 0x3eae1a93, v188\n"
    "v_fmamk_f32 v211, v211, 0x3eae1a93, v188\n"
    "v_exp_f32 v204, v204\n"
    "v_exp_f32 v205, v205\n"
    "v_exp_f32 v206, v206\n"
    "v_exp_f32 v207, v207\n"
    "v_exp_f32 v208, v208\n"
    "v_exp_f32 v209, v209\n"
    "v_exp_f32 v210, v210\n"
    "v_exp_f32 v211, v211\n"
    "v_mul_f32 v20, v20, v200\n"
    "v_mul_f32 v21, v21, v201\n"
    "v_mul_f32 v22, v22, v202\n"
    "v_mul_f32 v23, v23, v203\n"
    "v_cvt_pk_bf16_f32 v204, v204, v205\n"
    "v_cvt_pk_bf16_f32 v205, v206, v207\n"
    "v_cvt_pk_bf16_f32 v206, v208, v209\n"
    "v_cvt_pk_bf16_f32 v207, v210, v211\n"
    "v_cvt_pk_bf16_f32 v34, v20, v21\n"
    "v_cvt_pk_bf16_f32 v35, v22, v23\n"
    "v_mfma_f32_16x16x32_bf16 v[184:187], v[36:39], v[204:207], 0\n"
    "s_nop 0\n"
    "v_mfma_f32_16x16x32_bf16 v[180:183], v[32:35], v[204:207], 0\n"
    "v_mov_b64 v[36:37], v[38:39]\n"
    "v_mov_b64 v[32:33], v[34:35]\n"
    "s_nop 2\n"
    "v_rcp_f32 v188, v186\n"
    "s_nop 0\n"
    "v_mul_f32 v184, v184, v188\n"
    "v_mul_f32 v185, v185, v188\n"
    "v_cvt_pk_bf16_f32 v184, v184, v185\n"
    "v_pk_mul_f32 v[180:181], v[180:181], v[188:189] op_sel_hi:[1,0]\n"
    "v_pk_mul_f32 v[182:183], v[182:183], v[188:189] op_sel_hi:[1,0]\n"
    "v_cvt_pk_bf16_f32 v204, v180, v181\n"
    "v_cvt_pk_bf16_f32 v205, v182, v183\n"
    "v_cvt_pk_bf16_f32 v206, v180, v181\n"
    "v_cvt_pk_bf16_f32 v207, v182, v183\n"
    "s_nop 0\n"
    "v_permlane16_swap_b32 v204, v206\n"
    "v_permlane16_swap_b32 v205, v207\n"
    "s_nop 0\n"
    "v_mov_b32_dpp v204, v184 quad_perm:[0,0,0,0] row_mask:0x2 bank_mask:0xf\n"
    "v_mov_b32_dpp v205, v184 quad_perm:[1,1,1,1] row_mask:0x2 bank_mask:0xf\n"
    "v_mov_b32_dpp v206, v184 quad_perm:[2,2,2,2] row_mask:0x2 bank_mask:0xf\n"
    "v_mov_b32_dpp v207, v184 quad_perm:[3,3,3,3] row_mask:0x2 bank_mask:0xf\n"
    "buffer_store_dwordx4 v[204:207], v2, s[40:43], s62 offen sc1\n"
    "s_mov_b32 s56, 1\n"
    "s_cmp_lg_u32 s94, 0\n"
    "s_cbranch_scc1 .Lago_%=\n"
    "s_waitcnt vmcnt(1)\n"
    "v_cmp_eq_u32 vcc, 0, v77\n"
    "s_cmp_eq_u64 vcc, 0\n"
    "s_cbranch_scc1 .Lald_%=\n"
    "ds_read_b32 v79, v11 offset:44\n"
    "global_load_dword v77, v76, %1 sc1\n"
    "s_waitcnt vmcnt(0)\n"
    "v_mov_b32 v78, 1\n"
    "s_mov_b64 exec, 1\n"
    "ds_add_rtn_u32 v78, v11, v78 offset:48\n"
    "s_mov_b64 exec, -1\n"
    "s_waitcnt lgkmcnt(0)\n"
    "v_readfirstlane_b32 s50, v78\n"
    "s_cmp_eq_u32 s50, 3\n"
    "s_cselect_b64 exec, 1, 0\n"
    "global_store_dword v79, v78, %1 sc1\n"
    "s_mov_b64 exec, -1\n"
    "s_mov_b32 s56, 0\n"
    "v_cmp_eq_u32 vcc, 0, v77\n"
    "s_cmp_eq_u64 vcc, 0\n"
    "s_cbranch_scc1 .Lald_%=\n"
    ".Lachk_%=:\n"
    "v_readfirstlane_b32 s50, v76\n"
    "s_cmp_lt_u32 s50, 200\n"
    "s_cbranch_scc1 .Laend_%=\n"
    ".Lawait_%=:\n"
    "global_load_dword v77, v76, %1 sc1\n"
    "s_mov_b32 s52, 0\n"
    ".Lap11_%=:\n"
    "s_waitcnt vmcnt(0)\n"
    "v_cmp_eq_u32 vcc, 0, v77\n"
    "s_cmp_eq_u64 vcc, 0\n"
    "s_cbranch_scc1 .Lap11_x%=\n"
    "s_add_u32 s52, s52, 1\n"
    "s_sleep 2\n"
    "global_load_dword v77, v76, %1 sc1\n"
    "s_cmp_lt_u32 s52, s53\n"
    "s_cbranch_scc1 .Lap11_%=\n"
    "s_mov_b32 s53, 0\n"
    "v_mov_b32 v78, 0\n"
    "v_mov_b32 v79, 1\n"
    "global_store_dword v78, v79, %1 sc1\n"
    "s_waitcnt vmcnt(0)\n"
    ".Lap11_x%=:\n"
    ".Lald_%=:\n"
    "s_mov_b32 s94, s42\n"
    "buffer_load_dwordx4 v[152:155], v3, s[92:95], s49 offen sc1\n"
    "buffer_load_dwordx4 v[156:159], v3, s[92:95], s49 offen offset:1024 sc1\n"
    "buffer_load_dwordx4 v[160:163], v3, s[92:95], s49 offen offset:2048 sc1\n"
    "buffer_load_dwordx4 v[164:167], v3, s[92:95], s49 offen offset:3072 sc1\n"
    "buffer_load_dwordx4 v[168:171], v4, s[92:95], s49 offen sc1\n"
    ".Lain_%=:\n"
    "v_add_u32 v11, 32, v11\n"
    "ds_read_b128 v[76:79], v11\n"
    "ds_read2_b32 v[74:75], v11 offset0:5 offset1:6\n"
    "s_waitcnt vmcnt(4)\n"
    "ds_write_b128 v1, v[152:155]\n"
    "v_mfma_f32_16x16x32_bf16 v[48:51], v[152:155], v[152:155], 0\n"
    "s_waitcnt vmcnt(3)\n"
    "ds_write_b128 v1, v[156:159] offset:1024\n"
    "v_mfma_f32_16x16x32_bf16 v[48:51], v[156:159], v[156:159], v[48:51]\n"
    "s_waitcnt vmcnt(2)\n"
    "ds_write_b128 v1, v[160:163] offset:2048\n"
    "v_mfma_f32_16x16x32_bf16 v[48:51], v[160:163], v[160:163], v[48:51]\n"
    "s_waitcnt vmcnt(1)\n"
    "ds_write_b128 v1, v[164:167] offset:3072\n"
    "v_mfma_f32_16x16x32_bf16 v[48:51], v[164:167], v[164:167], v[48:51]\n"
    "s_waitcnt vmcnt(0)\n"
    "ds_write_b128 v1, v[168:171] offset:4096\n"
    "v_mfma_f32_16x16x32_bf16 v[48:51], v[168:171], v[168:171], v[48:51]\n"
    "s_waitcnt lgkmcnt(5)\n"
    "v_readfirstlane_b32 s50, v75\n"
    "s_cmp_eq_u32 s50, 0\n"
    "s_cbranch_scc1 .Lac12_%=\n"
    "v_swap_b32 v28, v172\n"
    "v_swap_b32 v29, v173\n"
    "v_swap_b32 v30, v174\n"
    "v_swap_b32 v32, v175\n"
    "v_swap_b32 v33, v176\n"
    "v_swap_b32 v36, v177\n"
    "v_swap_b32 v37, v178\n"
    ".Lac12_%=:\n"
    "v_readfirstlane_b32 s49, v77\n"
    "v_readfirstlane_b32 s62, v78\n"
    "v_add_u32 v76, v76, v220\n"
    "s_waitcnt lgkmcnt(0)\n"
    "s_cmp_eq_u32 s56, 0\n"
    "s_cbranch_scc1 .Lac13_%=\n"
    "s_waitcnt vmcnt(0)\n"
    "v_mov_b32 v78, 1\n"
    "s_mov_b64 exec, 1\n"
    "ds_add_rtn_u32 v78, v11, v78 offset:16\n"
    "s_mov_b64 exec, -1\n"
    "s_waitcnt lgkmcnt(0)\n"
    "v_readfirstlane_b32 s50, v78\n"
    "s_cmp_eq_u32 s50, 3\n"
    "s_cselect_b64 exec, 1, 0\n"
    "global_store_dword v79, v78, %1 sc1\n"
    "s_mov_b64 exec, -1\n"
    "s_mov_b32 s56, 0\n"
    ".Lac13_%=:\n"
    "s_branch .Latop_%=\n"
    ".Lapol_%=:\n"
    "v_mov_b32 v76, 64\n"
    "global_load_dword v77, v76, %1 sc1\n"
    "s_mov_b32 s52, 0\n"
    ".Lap14_%=:\n"
    "s_waitcnt vmcnt(0)\n"
    "v_cmp_eq_u32 vcc, 0, v77\n"
    "s_cmp_eq_u64 vcc, 0\n"
    "s_cbranch_scc1 .Lap14_x%=\n"
    "s_add_u32 s52, s52, 1\n"
    "s_sleep 2\n"
    "global_load_dword v77, v76, %1 sc1\n"
    "s_cmp_lt_u32 s52, s53\n"
    "s_cbranch_scc1 .Lap14_%=\n"
    "s_mov_b32 s53, 0\n"
    "v_mov_b32 v78, 0\n"
    "v_mov_b32 v79, 1\n"
    "global_store_dword v78, v79, %1 sc1\n"
    "s_waitcnt vmcnt(0)\n"
    ".Lap14_x%=:\n"
    "v_readfirstlane_b32 s50, v77\n"
    "v_mbcnt_lo_u32_b32 v76, -1, 0\n"
    "v_mbcnt_hi_u32_b32 v76, -1, v76\n"
    "s_lshl_b32 s51, %2, 6\n"
    "v_add_u32 v76, s51, v76\n"
    "v_min_u32 v76, 129, v76\n"
    "v_lshlrev_b32 v76, 5, v76\n"
    "v_add_u32 v76, 0x26c00, v76\n"
    "ds_read2_b32 v[78:79], v76 offset0:1 offset1:2\n"
    "s_and_b32 s50, s50, 1\n"
    "s_waitcnt lgkmcnt(0)\n"
    "v_and_b32 v77, s50, v78\n"
    "v_lshlrev_b32 v77, 13, v77\n"
    "v_and_b32 v78, -2, v78\n"
    "v_xor_b32 v78, v77, v78\n"
    "v_and_b32 v77, s50, v79\n"
    "v_lshlrev_b32 v77, 13, v77\n"
    "v_and_b32 v79, -2, v79\n"
    "v_xor_b32 v79, v77, v79\n"
    "ds_write2_b32 v76, v78, v79 offset0:1 offset1:2\n"
    "s_waitcnt lgkmcnt(0)\n"
    "s_barrier\n"
    "ds_read_b32 v77, v11 offset:4\n"
    "v_add_u32 v76, %4, v220\n"
    "s_waitcnt lgkmcnt(0)\n"
    "v_readfirstlane_b32 s49, v77\n"
    "s_branch .Lawait_%=\n"
    ".Laend_%=:\n"
    "s_cmp_eq_u32 s50, 192\n"
    "s_cbranch_scc0 .Lapol_%=\n"
    "s_waitcnt vmcnt(0) lgkmcnt(0)\n"
    "s_endpgm\n"
    ".p2align 6\n"
    ".Lrole4_%=:\n"
    "s_mov_b64 s[40:41], %0\n"
    "s_and_b32 s41, s41, 0xffff\n"
    "s_mov_b32 s42, 0xc000000\n"
    "s_mov_b32 s43, 0x20000\n"
    "s_mov_b64 s[92:93], %0\n"
    "s_and_b32 s93, s93, 0xffff\n"
    "s_mov_b32 s94, 0xc000000\n"
    "s_mov_b32 s95, 0x20000\n"
    "s_mov_b32 s53, 131072\n"
    "s_mov_b64 s[56:57], 0\n"
    "v_mbcnt_lo_u32_b32 v20, -1, 0\n"
    "v_mbcnt_hi_u32_b32 v20, -1, v20\n"
    "s_lshl_b32 s50, %2, 8\n"
    "s_add_u32 s50, s50, 147456\n"
    "v_lshl_add_u32 v20, v20, 2, s50\n"
    "ds_read_b32 v0, v20\n"
    "ds_read_b32 v1, v20 offset:1024\n"
    "ds_read_b32 v2, v20 offset:2048\n"
    "ds_read_b32 v3, v20 offset:3072\n"
    "ds_read_b32 v4, v20 offset:4096\n"
    "ds_read_b32 v5, v20 offset:5120\n"
    "ds_read_b32 v8, v20 offset:6144\n"
    "ds_read_b32 v9, v20 offset:7168\n"
    "ds_read_b32 v10, v20 offset:8192\n"
    "s_waitcnt lgkmcnt(0)\n"
    "s_barrier\n"
    "s_lshl_b32 s54, %2, 10\n"
    "s_mov_b64 s[44:45], %8\n"
    "s_and_b32 s45, s45, 0xffff\n"
    "s_mov_b32 s46, 0xa2000\n"
    "s_mov_b32 s47, 0x20000\n"
    "s_add_u32 m0, s54, 0\n"
    "s_add_u32 s55, %6, 0\n"
    "buffer_load_dwordx4 v8, s[44:47], s55 offen lds\n"
    "s_add_u32 m0, s54, 36864\n"
    "s_add_u32 s55, %6, 64\n"
    "buffer_load_dwordx4 v8, s[44:47], s55 offen lds\n"
    "s_add_u32 m0, s54, 73728\n"
    "s_add_u32 s55, %6, 128\n"
    "buffer_load_dwordx4 v8, s[44:47], s55 offen lds\n"
    "s_add_u32 m0, s54, 4096\n"
    "s_add_u32 s55, %6, 73728\n"
    "buffer_load_dwordx4 v8, s[44:47], s55 offen lds\n"
    "s_add_u32 m0, s54, 40960\n"
    "s_add_u32 s55, %6, 73792\n"
    "buffer_load_dwordx4 v8, s[44:47], s55 offen lds\n"
    "s_add_u32 m0, s54, 77824\n"
    "s_add_u32 s55, %6, 73856\n"
    "buffer_load_dwordx4 v8, s[44:47], s55 offen lds\n"
    "s_add_u32 m0, s54, 8192\n"
    "s_add_u32 s55, %6, 147456\n"
    "buffer_load_dwordx4 v8, s[44:47], s55 offen lds\n"
    "s_add_u32 m0, s54, 45056\n"
    "s_add_u32 s55, %6, 147520\n"
    "buffer_load_dwordx4 v8, s[44:47], s55 offen lds\n"
    "s_add_u32 m0, s54, 81920\n"
    "s_add_u32 s55, %6, 147584\n"
    "buffer_load_dwordx4 v8, s[44:47], s55 offen lds\n"
    "s_add_u32 m0, s54, 12288\n"
    "s_add_u32 s55, %6, 221184\n"
    "buffer_load_dwordx4 v8, s[44:47], s55 offen lds\n"
    "s_add_u32 m0, s54, 49152\n"
    "s_add_u32 s55, %6, 221248\n"
    "buffer_load_dwordx4 v8, s[44:47], s55 offen lds\n"
    "s_add_u32 m0, s54, 86016\n"
    "s_add_u32 s55, %6, 221312\n"
    "buffer_load_dwordx4 v8, s[44:47], s55 offen lds\n"
    "s_add_u32 m0, s54, 16384\n"
    "s_add_u32 s55, %6, 294912\n"
    "buffer_load_dwordx4 v8, s[44:47], s55 offen lds\n"
    "s_add_u32 m0, s54, 53248\n"
    "s_add_u32 s55, %6, 294976\n"
    "buffer_load_dwordx4 v8, s[44:47], s55 offen lds\n"
    "s_add_u32 m0, s54, 90112\n"
    "s_add_u32 s55, %6, 295040\n"
    "buffer_load_dwordx4 v8, s[44:47], s55 offen lds\n"
    "s_add_u32 m0, s54, 20480\n"
    "s_add_u32 s55, %6, 368640\n"
    "buffer_load_dwordx4 v8, s[44:47], s55 offen lds\n"
    "s_add_u32 m0, s54, 57344\n"
    "s_add_u32 s55, %6, 368704\n"
    "buffer_load_dwordx4 v8, s[44:47], s55 offen lds\n"
    "s_add_u32 m0, s54, 94208\n"
    "s_add_u32 s55, %6, 368768\n"
    "buffer_load_dwordx4 v8, s[44:47], s55 offen lds\n"
    "s_add_u32 m0, s54, 24576\n"
    "s_add_u32 s55, %6, 442368\n"
    "buffer_load_dwordx4 v8, s[44:47], s55 offen lds\n"
    "s_add_u32 m0, s54, 61440\n"
    "s_add_u32 s55, %6, 442432\n"
    "buffer_load_dwordx4 v8, s[44:47], s55 offen lds\n"
    "s_add_u32 m0, s54, 98304\n"
    "s_add_u32 s55, %6, 442496\n"
    "buffer_load_dwordx4 v8, s[44:47], s55 offen lds\n"
    "s_add_u32 m0, s54, 28672\n"
    "s_add_u32 s55, %6, 516096\n"
    "buffer_load_dwordx4 v8, s[44:47], s55 offen lds\n"
    "s_add_u32 m0, s54, 65536\n"
    "s_add_u32 s55, %6, 516160\n"
    "buffer_load_dwordx4 v8, s[44:47], s55 offen lds\n"
    "s_add_u32 m0, s54, 102400\n"
    "s_add_u32 s55, %6, 516224\n"
    "buffer_load_dwordx4 v8, s[44:47], s55 offen lds\n"
    "s_add_u32 m0, s54, 32768\n"
    "s_add_u32 s55, %6, 589824\n"
    "buffer_load_dwordx4 v8, s[44:47], s55 offen lds\n"
    "s_add_u32 m0, s54, 69632\n"
    "s_add_u32 s55, %6, 589888\n"
    "buffer_load_dwordx4 v8, s[44:47], s55 offen lds\n"
    "s_add_u32 m0, s54, 106496\n"
    "s_add_u32 s55, %6, 589952\n"
    "buffer_load_dwordx4 v8, s[44:47], s55 offen lds\n"
    "v_mov_b32 v7, 0x20400\n"
    "global_load_dwordx2 v[20:21], v7, %1 sc1\n"
    "v_mbcnt_lo_u32_b32 v6, -1, 0\n"
    "v_min_u32 v6, 7, v6\n"
    "v_lshlrev_b32 v6, 7, v6\n"
    "v_add_u32 v6, 0x20000, v6\n"
    "global_load_dwordx2 v[22:23], v6, %1 sc1\n"
    "s_waitcnt vmcnt(2) lgkmcnt(0)\n"
    "s_barrier\n"
    "ds_read_b64_tr_b16 a[0:1], v9\n"
    "ds_read_b64_tr_b16 a[2:3], v9 offset:256\n"
    "ds_read_b64_tr_b16 a[4:5], v9 offset:8\n"
    "ds_read_b64_tr_b16 a[6:7], v9 offset:264\n"
    "ds_read_b64_tr_b16 a[8:9], v9 offset:2304\n"
    "ds_read_b64_tr_b16 a[10:11], v9 offset:2560\n"
    "ds_read_b64_tr_b16 a[12:13], v9 offset:2312\n"
    "ds_read_b64_tr_b16 a[14:15], v9 offset:2568\n"
    "ds_read_b64_tr_b16 a[16:17], v9 offset:4608\n"
    "ds_read_b64_tr_b16 a[18:19], v9 offset:4864\n"
    "ds_read_b64_tr_b16 a[20:21], v9 offset:4616\n"
    "ds_read_b64_tr_b16 a[22:23], v9 offset:4872\n"
    "ds_read_b64_tr_b16 a[24:25], v9 offset:6912\n"
    "ds_read_b64_tr_b16 a[26:27], v9 offset:7168\n"
    "ds_read_b64_tr_b16 a[28:29], v9 offset:6920\n"
    "ds_read_b64_tr_b16 a[30:31], v9 offset:7176\n"
    "ds_read_b64_tr_b16 a[32:33], v9 offset:9216\n"
    "ds_read_b64_tr_b16 a[34:35], v9 offset:9472\n"
    "ds_read_b64_tr_b16 a[36:37], v9 offset:9224\n"
    "ds_read_b64_tr_b16 a[38:39], v9 offset:9480\n"
    "ds_read_b64_tr_b16 a[40:41], v9 offset:11520\n"
    "ds_read_b64_tr_b16 a[42:43], v9 offset:11776\n"
    "ds_read_b64_tr_b16 a[44:45], v9 offset:11528\n"
    "ds_read_b64_tr_b16 a[46:47], v9 offset:11784\n"
    "ds_read_b64_tr_b16 a[48:49], v9 offset:13824\n"
    "ds_read_b64_tr_b16 a[50:51], v9 offset:14080\n"
    "ds_read_b64_tr_b16 a[52:53], v9 offset:13832\n"
    "ds_read_b64_tr_b16 a[54:55], v9 offset:14088\n"
    "ds_read_b64_tr_b16 a[56:57], v9 offset:16128\n"
    "ds_read_b64_tr_b16 a[58:59], v9 offset:16384\n"
    "ds_read_b64_tr_b16 a[60:61], v9 offset:16136\n"
    "ds_read_b64_tr_b16 a[62:63], v9 offset:16392\n"
    "ds_read_b64_tr_b16 a[64:65], v9 offset:18432\n"
    "ds_read_b64_tr_b16 a[66:67], v9 offset:18688\n"
    "ds_read_b64_tr_b16 a[68:69], v9 offset:18440\n"
    "ds_read_b64_tr_b16 a[70:71], v9 offset:18696\n"
    "ds_read_b64_tr_b16 a[72:73], v9 offset:20736\n"
    "ds_read_b64_tr_b16 a[74:75], v9 offset:20992\n"
    "ds_read_b64_tr_b16 a[76:77], v9 offset:20744\n"
    "ds_read_b64_tr_b16 a[78:79], v9 offset:21000\n"
    "ds_read_b64_tr_b16 a[80:81], v9 offset:23040\n"
    "ds_read_b64_tr_b16 a[82:83], v9 offset:23296\n"
    "ds_read_b64_tr_b16 a[84:85], v9 offset:23048\n"
    "ds_read_b64_tr_b16 a[86:87], v9 offset:23304\n"
    "ds_read_b64_tr_b16 a[88:89], v9 offset:25344\n"
    "ds_read_b64_tr_b16 a[90:91], v9 offset:25600\n"
    "ds_read_b64_tr_b16 a[92:93], v9 offset:25352\n"
    "ds_read_b64_tr_b16 a[94:95], v9 offset:25608\n"
    "ds_read_b64_tr_b16 a[96:97], v9 offset:27648\n"
    "ds_read_b64_tr_b16 a[98:99], v9 offset:27904\n"
    "ds_read_b64_tr_b16 a[100:101], v9 offset:27656\n"
    "ds_read_b64_tr_b16 a[102:103], v9 offset:27912\n"
    "ds_read_b64_tr_b16 a[104:105], v9 offset:29952\n"
    "ds_read_b64_tr_b16 a[106:107], v9 offset:30208\n"
    "ds_read_b64_tr_b16 a[108:109], v9 offset:29960\n"
    "ds_read_b64_tr_b16 a[110:111], v9 offset:30216\n"
    "ds_read_b64_tr_b16 a[112:113], v9 offset:32256\n"
    "ds_read_b64_tr_b16 a[114:115], v9 offset:32512\n"
    "ds_read_b64_tr_b16 a[116:117], v9 offset:32264\n"
    "ds_read_b64_tr_b16 a[118:119], v9 offset:32520\n"
    "ds_read_b64_tr_b16 a[120:121], v9 offset:34560\n"
    "ds_read_b64_tr_b16 a[122:123], v9 offset:34816\n"
    "ds_read_b64_tr_b16 a[124:125], v9 offset:34568\n"
    "ds_read_b64_tr_b16 a[126:127], v9 offset:34824\n"
    "ds_read_b64_tr_b16 a[128:129], v10\n"
    "ds_read_b64_tr_b16 a[130:131], v10 offset:2304\n"
    "ds_read_b64_tr_b16 a[132:133], v10 offset:8\n"
    "ds_read_b64_tr_b16 a[134:135], v10 offset:2312\n"
    "ds_read_b64_tr_b16 a[136:137], v10 offset:18432\n"
    "ds_read_b64_tr_b16 a[138:139], v10 offset:20736\n"
    "ds_read_b64_tr_b16 a[140:141], v10 offset:18440\n"
    "ds_read_b64_tr_b16 a[142:143], v10 offset:20744\n"
    "s_waitcnt lgkmcnt(0)\n"
    "s_barrier\n"
    "s_waitcnt vmcnt(0)\n"
    "v_add_u32 v20, 1, v20\n"
    "v_add_u32 v21, 1, v21\n"
    "s_mov_b32 s52, 1048576\n"
    ".Lbgo_a%=:\n"
    "v_cmp_ne_u64 vcc, v[20:21], v[22:23]\n"
    "s_cbranch_vccz .Lbgo_x%=\n"
    "global_load_dwordx2 v[22:23], v6, %1 sc1\n"
    "s_sub_u32 s52, s52, 1\n"
    "s_waitcnt vmcnt(0)\n"
    "s_cbranch_scc0 .Lbgo_a%=\n"
    ".Lbgo_x%=:\n"
    "v_mbcnt_lo_u32_b32 v20, -1, 0\n"
    "v_mbcnt_hi_u32_b32 v20, -1, v20\n"
    "v_min_u32 v6, 7, v20\n"
    "v_lshlrev_b32 v6, 7, v6\n"
    "v_mov_b32 v7, 0x26c00\n"
    "ds_read_b128 v[20:23], v7\n"
    "s_waitcnt lgkmcnt(0)\n"
    "v_readfirstlane_b32 s49, v21\n"
    "v_add_u32 v20, v20, v6\n"
    "s_branch .Lbchk_%=\n"
    ".Lbgo_%=:\n"
    "s_waitcnt vmcnt(1)\n"
    "v_add_u32 v7, 32, v7\n"
    "ds_read_b128 v[20:23], v7\n"
    "ds_write_b128 v1, v[100:103]\n"
    "ds_write_b128 v1, v[104:107] offset:1024\n"
    "ds_write_b128 v1, v[108:111] offset:2048\n"
    "ds_write_b128 v1, v[112:115] offset:3072\n"
    "ds_write_b32 v4, v116\n"
    "ds_write_b32 v4, v117 offset:16\n"
    "ds_write_b32 v4, v118 offset:32\n"
    "ds_write_b32 v4, v119 offset:48\n"
    "v_lshlrev_b32 v8, 16, v120\n"
    "v_and_b32 v9, 0xffff0000, v120\n"
    "v_lshlrev_b32 v10, 16, v121\n"
    "v_and_b32 v11, 0xffff0000, v121\n"
    "v_lshlrev_b32 v12, 16, v122\n"
    "v_and_b32 v13, 0xffff0000, v122\n"
    "v_lshlrev_b32 v14, 16, v123\n"
    "v_and_b32 v15, 0xffff0000, v123\n"
    "v_xor_b32 v0, 0x8000, v0\n"
    "v_xor_b32 v1, 0x8000, v1\n"
    "v_xor_b32 v4, 0x8000, v4\n"
    "s_waitcnt lgkmcnt(8)\n"
    "v_readfirstlane_b32 s49, v21\n"
    "v_readfirstlane_b32 s62, v22\n"
    "v_add_u32 v20, v20, v6\n"
    "s_waitcnt lgkmcnt(0)\n"
    ".Lbtop_%=:\n"
    "s_waitcnt lgkmcnt(0)\n"
    "s_barrier\n"
    "ds_read_b128 v[24:27], v0\n"
    "ds_read_b128 v[28:31], v0 offset:1024\n"
    "ds_read_b128 v[36:39], v0 offset:3072\n"
    "ds_read_b128 v[32:35], v0 offset:2048\n"
    "ds_read_b128 v[44:47], v0 offset:5120\n"
    "ds_read_b128 v[40:43], v0 offset:4096\n"
    "global_load_dword v21, v20, %1 sc1\n"
    "s_waitcnt lgkmcnt(5)\n"
    "v_mfma_f32_16x16x32_bf16 v[8:11], a[0:3], v[24:27], v[8:11]\n"
    "ds_read_b128 v[52:55], v0 offset:7168\n"
    "ds_read_b128 v[48:51], v0 offset:6144\n"
    "v_mfma_f32_16x16x32_bf16 v[12:15], a[4:7], v[24:27], v[12:15]\n"
    "ds_read_b128 v[60:63], v0 offset:9216\n"
    "ds_read_b128 v[56:59], v0 offset:8192\n"
    "s_waitcnt lgkmcnt(8)\n"
    "v_mfma_f32_16x16x32_bf16 v[8:11], a[8:11], v[28:31], v[8:11]\n"
    "ds_read_b128 v[68:71], v0 offset:11264\n"
    "ds_read_b128 v[64:67], v0 offset:10240\n"
    "v_mfma_f32_16x16x32_bf16 v[12:15], a[12:15], v[28:31], v[12:15]\n"
    "ds_read_b128 v[76:79], v0 offset:13312\n"
    "ds_read_b128 v[72:75], v0 offset:12288\n"
    "s_waitcnt lgkmcnt(10)\n"
    "v_mfma_f32_16x16x32_bf16 v[8:11], a[16:19], v[32:35], v[8:11]\n"
    "ds_read_b128 v[84:87], v0 offset:15360\n"
    "ds_read_b128 v[80:83], v0 offset:14336\n"
    "v_mfma_f32_16x16x32_bf16 v[12:15], a[20:23], v[32:35], v[12:15]\n"
    "ds_read_b128 v[92:95], v0 offset:17408\n"
    "ds_read_b128 v[88:91], v0 offset:16384\n"
    "v_mfma_f32_16x16x32_bf16 v[8:11], a[24:27], v[36:39], v[8:11]\n"
    "v_mfma_f32_16x16x32_bf16 v[12:15], a[28:31], v[36:39], v[12:15]\n"
    "s_waitcnt lgkmcnt(12)\n"
    "v_mfma_f32_16x16x32_bf16 v[8:11], a[32:35], v[40:43], v[8:11]\n"
    "v_mfma_f32_16x16x32_bf16 v[12:15], a[36:39], v[40:43], v[12:15]\n"
    "v_mfma_f32_16x16x32_bf16 v[8:11], a[40:43], v[44:47], v[8:11]\n"
    "v_mfma_f32_16x16x32_bf16 v[12:15], a[44:47], v[44:47], v[12:15]\n"
    "s_waitcnt lgkmcnt(10)\n"
    "v_mfma_f32_16x16x32_bf16 v[8:11], a[48:51], v[48:51], v[8:11]\n"
    "v_mfma_f32_16x16x32_bf16 v[12:15], a[52:55], v[48:51], v[12:15]\n"
    "v_mfma_f32_16x16x32_bf16 v[8:11], a[56:59], v[52:55], v[8:11]\n"
    "v_mfma_f32_16x16x32_bf16 v[12:15], a[60:63], v[52:55], v[12:15]\n"
    "s_waitcnt lgkmcnt(8)\n"
    "v_mfma_f32_16x16x32_bf16 v[8:11], a[64:67], v[56:59], v[8:11]\n"
    "v_mfma_f32_16x16x32_bf16 v[12:15], a[68:71], v[56:59], v[12:15]\n"
    "v_mfma_f32_16x16x32_bf16 v[8:11], a[72:75], v[60:63], v[8:11]\n"
    "v_mfma_f32_16x16x32_bf16 v[12:15], a[76:79], v[60:63], v[12:15]\n"
    "s_waitcnt lgkmcnt(6)\n"
    "v_mfma_f32_16x16x32_bf16 v[8:11], a[80:83], v[64:67], v[8:11]\n"
    "v_mfma_f32_16x16x32_bf16 v[12:15], a[84:87], v[64:67], v[12:15]\n"
    "v_mfma_f32_16x16x32_bf16 v[8:11], a[88:91], v[68:71], v[8:11]\n"
    "v_mfma_f32_16x16x32_bf16 v[12:15], a[92:95], v[68:71], v[12:15]\n"
    "s_waitcnt lgkmcnt(4)\n"
    "v_mfma_f32_16x16x32_bf16 v[8:11], a[96:99], v[72:75], v[8:11]\n"
    "v_mfma_f32_16x16x32_bf16 v[12:15], a[100:103], v[72:75], v[12:15]\n"
    "v_mfma_f32_16x16x32_bf16 v[8:11], a[104:107], v[76:79], v[8:11]\n"
    "v_mfma_f32_16x16x32_bf16 v[12:15], a[108:111], v[76:79], v[12:15]\n"
    "s_waitcnt lgkmcnt(2)\n"
    "v_mfma_f32_16x16x32_bf16 v[8:11], a[112:115], v[80:83], v[8:11]\n"
    "v_mfma_f32_16x16x32_bf16 v[12:15], a[116:119], v[80:83], v[12:15]\n"
    "v_mfma_f32_16x16x32_bf16 v[8:11], a[120:123], v[84:87], v[8:11]\n"
    "v_mfma_f32_16x16x32_bf16 v[12:15], a[124:127], v[84:87], v[12:15]\n"
    "s_waitcnt lgkmcnt(0)\n"
    "v_mfma_f32_16x16x32_bf16 v[8:11], a[128:131], v[88:91], v[8:11]\n"
    "v_mfma_f32_16x16x32_bf16 v[12:15], a[132:135], v[88:91], v[12:15]\n"
    "v_mfma_f32_16x16x32_bf16 v[8:11], a[136:139], v[92:95], v[8:11]\n"
    "v_mfma_f32_16x16x32_bf16 v[12:15], a[140:143], v[92:95], v[12:15]\n"
    "s_nop 4\n"
    "v_cvt_pk_bf16_f32 v16, v8, v9\n"
    "v_cvt_pk_bf16_f32 v17, v10, v11\n"
    "s_nop 1\n"
    "v_cvt_pk_bf16_f32 v18, v12, v13\n"
    "v_cvt_pk_bf16_f32 v19, v14, v15\n"
    "buffer_store_dwordx4 v[16:19], v3, s[40:43], s62 offen sc1\n"
    "s_mov_b32 s56, 1\n"
    "s_waitcnt vmcnt(1)\n"
    "v_cmp_eq_u32 vcc, 0, v21\n"
    "s_cmp_eq_u64 vcc, 0\n"
    "ds_read_b32 v23, v7 offset:44\n"
    "s_cbranch_scc0 .Lbno_%=\n"
    "buffer_load_dwordx4 v[100:103], v2, s[92:95], s49 offen sc1\n"
    "buffer_load_dwordx4 v[104:107], v2, s[92:95], s49 offen offset:1024 sc1\n"
    "buffer_load_dwordx4 v[108:111], v2, s[92:95], s49 offen offset:2048 sc1\n"
    "buffer_load_dwordx4 v[112:115], v2, s[92:95], s49 offen offset:3072 sc1\n"
    "buffer_load_dwordx4 v[116:119], v5, s[92:95], s49 offen sc1\n"
    "s_sub_u32 s55, s49, 0x1000000\n"
    "buffer_load_dwordx4 v[120:123], v3, s[40:43], s55 offen sc1\n"
    "s_waitcnt vmcnt(6)\n"
    "v_mov_b32 v22, 1\n"
    "s_mov_b64 exec, 1\n"
    "ds_add_rtn_u32 v22, v7, v22 offset:48\n"
    "s_mov_b64 exec, -1\n"
    "s_waitcnt lgkmcnt(0)\n"
    "v_readfirstlane_b32 s50, v22\n"
    "s_cmp_eq_u32 s50, 3\n"
    "s_cselect_b64 exec, 1, 0\n"
    "global_store_dword v23, v22, %1 sc1\n"
    "s_mov_b64 exec, -1\n"
    "s_branch .Lbin_%=\n"
    ".Lbno_%=:\n"
    "global_load_dword v21, v20, %1 sc1\n"
    "s_waitcnt vmcnt(0)\n"
    "v_mov_b32 v22, 1\n"
    "s_mov_b64 exec, 1\n"
    "ds_add_rtn_u32 v22, v7, v22 offset:48\n"
    "s_mov_b64 exec, -1\n"
    "s_waitcnt lgkmcnt(0)\n"
    "v_readfirstlane_b32 s50, v22\n"
    "s_cmp_eq_u32 s50, 3\n"
    "s_cselect_b64 exec, 1, 0\n"
    "global_store_dword v23, v22, %1 sc1\n"
    "s_mov_b64 exec, -1\n"
    "s_mov_b32 s56, 0\n"
    "v_cmp_eq_u32 vcc, 0, v21\n"
    "s_cmp_eq_u64 vcc, 0\n"
    "s_cbranch_scc1 .Lbld_%=\n"
    ".Lbchk_%=:\n"
    "v_readfirstlane_b32 s50, v20\n"
    "s_cmp_lt_u32 s50, 200\n"
    "s_cbranch_scc1 .Lbend_%=\n"
    ".Lbwait_%=:\n"
    "global_load_dword v21, v20, %1 sc1\n"
    "s_mov_b32 s52, 0\n"
    ".Lbp1_%=:\n"
    "s_waitcnt vmcnt(0)\n"
    "v_cmp_eq_u32 vcc, 0, v21\n"
    "s_cmp_eq_u64 vcc, 0\n"
    "s_cbranch_scc1 .Lbp1_x%=\n"
    "s_add_u32 s52, s52, 1\n"
    "s_sleep 2\n"
    "global_load_dword v21, v20, %1 sc1\n"
    "s_cmp_lt_u32 s52, s53\n"
    "s_cbranch_scc1 .Lbp1_%=\n"
    "s_mov_b32 s53, 0\n"
    "v_mov_b32 v22, 0\n"
    "v_mov_b32 v23, 1\n"
    "global_store_dword v22, v23, %1 sc1\n"
    "s_waitcnt vmcnt(0)\n"
    ".Lbp1_x%=:\n"
    ".Lbld_%=:\n"
    "s_mov_b32 s94, s42\n"
    "buffer_load_dwordx4 v[100:103], v2, s[92:95], s49 offen sc1\n"
    "buffer_load_dwordx4 v[104:107], v2, s[92:95], s49 offen offset:1024 sc1\n"
    "buffer_load_dwordx4 v[108:111], v2, s[92:95], s49 offen offset:2048 sc1\n"
    "buffer_load_dwordx4 v[112:115], v2, s[92:95], s49 offen offset:3072 sc1\n"
    "buffer_load_dwordx4 v[116:119], v5, s[92:95], s49 offen sc1\n"
    "s_sub_u32 s55, s49, 0x1000000\n"
    "buffer_load_dwordx4 v[120:123], v3, s[40:43], s55 offen sc1\n"
    ".Lbin_%=:\n"
    "v_add_u32 v7, 32, v7\n"
    "ds_read_b128 v[20:23], v7\n"
    "s_waitcnt vmcnt(5)\n"
    "ds_write_b128 v1, v[100:103]\n"
    "s_waitcnt vmcnt(4)\n"
    "ds_write_b128 v1, v[104:107] offset:1024\n"
    "s_waitcnt vmcnt(3)\n"
    "ds_write_b128 v1, v[108:111] offset:2048\n"
    "s_waitcnt vmcnt(2)\n"
    "ds_write_b128 v1, v[112:115] offset:3072\n"
    "s_waitcnt vmcnt(1)\n"
    "ds_write_b32 v4, v116\n"
    "ds_write_b32 v4, v117 offset:16\n"
    "ds_write_b32 v4, v118 offset:32\n"
    "ds_write_b32 v4, v119 offset:48\n"
    "s_waitcnt vmcnt(0)\n"
    "v_lshlrev_b32 v8, 16, v120\n"
    "v_and_b32 v9, 0xffff0000, v120\n"
    "v_lshlrev_b32 v10, 16, v121\n"
    "v_and_b32 v11, 0xffff0000, v121\n"
    "v_lshlrev_b32 v12, 16, v122\n"
    "v_and_b32 v13, 0xffff0000, v122\n"
    "v_lshlrev_b32 v14, 16, v123\n"
    "v_and_b32 v15, 0xffff0000, v123\n"
    "v_xor_b32 v0, 0x8000, v0\n"
    "v_xor_b32 v1, 0x8000, v1\n"
    "v_xor_b32 v4, 0x8000, v4\n"
    "s_waitcnt lgkmcnt(8)\n"
    "v_readfirstlane_b32 s49, v21\n"
    "v_readfirstlane_b32 s62, v22\n"
    "v_add_u32 v20, v20, v6\n"
    "s_waitcnt lgkmcnt(0)\n"
    "s_branch .Lbtop_%=\n"
    ".Lbpol_%=:\n"
    "v_mov_b32 v20, 64\n"
    "global_load_dword v21, v20, %1 sc1\n"
    "s_mov_b32 s52, 0\n"
    ".Lbp2_%=:\n"
    "s_waitcnt vmcnt(0)\n"
    "v_cmp_eq_u32 vcc, 0, v21\n"
    "s_cmp_eq_u64 vcc, 0\n"
    "s_cbranch_scc1 .Lbp2_x%=\n"
    "s_add_u32 s52, s52, 1\n"
    "s_sleep 2\n"
    "global_load_dword v21, v20, %1 sc1\n"
    "s_cmp_lt_u32 s52, s53\n"
    "s_cbranch_scc1 .Lbp2_%=\n"
    "s_mov_b32 s53, 0\n"
    "v_mov_b32 v22, 0\n"
    "v_mov_b32 v23, 1\n"
    "global_store_dword v22, v23, %1 sc1\n"
    "s_waitcnt vmcnt(0)\n"
    ".Lbp2_x%=:\n"
    "v_readfirstlane_b32 s50, v21\n"
    "v_mbcnt_lo_u32_b32 v20, -1, 0\n"
    "v_mbcnt_hi_u32_b32 v20, -1, v20\n"
    "s_lshl_b32 s51, %2, 6\n"
    "v_add_u32 v20, s51, v20\n"
    "v_min_u32 v20, 129, v20\n"
    "v_lshlrev_b32 v20, 5, v20\n"
    "v_add_u32 v20, 0x26c00, v20\n"
    "ds_read2_b32 v[22:23], v20 offset0:1 offset1:2\n"
    "s_and_b32 s50, s50, 1\n"
    "s_waitcnt lgkmcnt(0)\n"
    "v_and_b32 v21, s50, v22\n"
    "v_lshlrev_b32 v21, 13, v21\n"
    "v_and_b32 v22, -2, v22\n"
    "v_xor_b32 v22, v21, v22\n"
    "v_and_b32 v21, s50, v23\n"
    "v_lshlrev_b32 v21, 13, v21\n"
    "v_and_b32 v23, -2, v23\n"
    "v_xor_b32 v23, v21, v23\n"
    "ds_write2_b32 v20, v22, v23 offset0:1 offset1:2\n"
    "s_waitcnt lgkmcnt(0)\n"
    "s_barrier\n"
    "ds_read_b32 v21, v7 offset:4\n"
    "v_add_u32 v20, %4, v6\n"
    "s_waitcnt lgkmcnt(0)\n"
    "v_readfirstlane_b32 s49, v21\n"
    "s_branch .Lbwait_%=\n"
    ".Lbend_%=:\n"
    "s_cmp_eq_u32 s50, 192\n"
    "s_cbranch_scc0 .Lbpol_%=\n"
    "s_waitcnt vmcnt(0) lgkmcnt(0)\n"
    ".Lrole5_%=:\n"
    ".Lfin:\n"
        :: "s"(wd), "s"(ws), "s"(wave), "s"(rid), "s"(P.h), "s"(P.p), "s"(P.woff), "s"(layer), "s"(w0), "s"(Wk), "s"(Wv), "s"(Wo),
           "s"(mem0), "s"(P.role) : CLOBBERS);
}

extern "C" void kernel_launch(void* const* d_in, const int* in_sizes, int n_in, void* d_out, int out_size, void* d_ws,
                              size_t ws_size, hipStream_t stream) {
    mega_kernel<<<256, 256, 0, stream>>>((const u16*)d_in[0], (const u16*)d_in[1], (const u16*)d_in[2], (const u16*)d_in[4],
                                         (const u16*)d_in[5], (const u16*)d_in[6], (const u16*)d_in[7], (const u16*)d_in[8],
                                         (u16*)d_out, (char*)d_ws);
}
```

```cpp
#include <hip/hip_runtime.h>
#include <hip/hip_bf16.h>
#include <stdint.h>

typedef unsigned short u16;
typedef unsigned u32;
typedef unsigned long long u64;
using bf16x8 = __attribute__((ext_vector_type(8))) short;
using bf16x4 = __attribute__((ext_vector_type(4))) short;
using f32x4 = __attribute__((ext_vector_type(4))) float;
using f32x2 = __attribute__((ext_vector_type(2))) float;
using v4u = __attribute__((ext_vector_type(4))) unsigned;
using v2u = __attribute__((ext_vector_type(2))) unsigned;
typedef __bf16 bfv2 __attribute__((ext_vector_type(2)));
#define GAS __attribute__((address_space(1)))
typedef GAS unsigned gu32;
typedef GAS u64 gu64;
#define RLX_AGENT __ATOMIC_RELAXED, __HIP_MEMORY_SCOPE_AGENT
typedef __amdgpu_buffer_rsrc_t rsrc_t;

constexpr int S_ = 2048, D_ = 576, NSEG = 64, SEG = 32, NB = 4096, NQA = 1728;

constexpr u32 OFF_ABORT = 0;
constexpr u32 OFF_POL = 64;
constexpr u32 OFF_RDY = 128;
constexpr u32 OFF_NEVER = 192;
constexpr u32 OFF_FA = 4096;
constexpr u32 OFF_FO = OFF_FA + 8192;
constexpr u32 OFF_FB = OFF_FO + 8192;
constexpr u32 OFF_FP = OFF_FB + 8192;
constexpr u32 OFF_COB = OFF_FP + 24576;
constexpr u32 OFF_FKV = OFF_COB + 32768;
constexpr u32 OFF_GO = 131072;
constexpr u32 OFF_EP = OFF_GO + 1024;
constexpr u32 SYNC_BYTES = OFF_EP + 128;
constexpr u32 MB2 = 2u << 20;
constexpr u32 OFF_XA = 0;
constexpr u32 OFF_XB = MB2;
constexpr u32 XB = S_ * D_ * 2;
constexpr u32 OFF_TA = 4 * MB2;
constexpr u32 OFF_TB = 8 * MB2;
constexpr u32 OFF_OA = 12 * MB2;
constexpr u32 OFF_PB = 16 * MB2;
constexpr u32 HB_ = 2 * 64 * 32 * 8192;
constexpr u32 OFF_QX = 32 * MB2;
constexpr u32 OFF_KX = OFF_QX + HB_;
constexpr u32 OFF_VX = OFF_KX + HB_;
constexpr u32 OFF_OX = OFF_VX + HB_;
constexpr u32 WS_BYTES = OFF_OX + HB_;
constexpr u32 L_TAB = 147456, L_IT = L_TAB + 11264, L_EX = 138240, L_W = 32064;
constexpr u32 O_SUM = 32768 + 2048;

#define STAMP(i, k) do {} while (0)
#define PH(k) do {} while (0)
__device__ __forceinline__ float bf2f(u16 v) { return __uint_as_float(((u32)v) << 16); }
__device__ __forceinline__ float bflo(u32 v) { return __uint_as_float(v << 16); }
__device__ __forceinline__ float bfhi(u32 v) { return __uint_as_float(v & 0xffff0000u); }
__device__ __forceinline__ u32 pack2(float a, float b) { bfv2 v = {(__bf16)a, (__bf16)b}; return __builtin_bit_cast(u32, v); }
__device__ __forceinline__ float rbf(float f) { return (float)(__bf16)f; }
__device__ __forceinline__ v4u ld16(rsrc_t r, u32 off) { return __builtin_amdgcn_raw_buffer_load_b128(r, off, 0, 16); }
__device__ __forceinline__ v2u ld8(rsrc_t r, u32 off) { return __builtin_amdgcn_raw_buffer_load_b64(r, off, 0, 16); }
__device__ __forceinline__ void st16(rsrc_t r, u32 off, v4u v) { __builtin_amdgcn_raw_buffer_store_b128(v, r, off, 0, 16); }
__device__ __forceinline__ void st8(rsrc_t r, u32 off, v2u v) { __builtin_amdgcn_raw_buffer_store_b64(v, r, off, 0, 16); }
__device__ __forceinline__ void st4(rsrc_t r, u32 off, u32 v) { __builtin_amdgcn_raw_buffer_store_b32(v, r, off, 0, 16); }
#define DRAIN() asm volatile("s_waitcnt vmcnt(0)" ::: "memory")
#define TOUCH(p) asm volatile("global_load_dword v255, %0, off" :: "v"(p) : "v255")
#define CBAR() asm volatile("" ::: "memory")
__device__ __forceinline__ u32 ldw(char* ws, u32 off) { return __hip_atomic_load((gu32*)(ws + off), RLX_AGENT); }
__device__ __forceinline__ void setw(char* ws, u32 off) { __hip_atomic_store((gu32*)(ws + off), 1u, RLX_AGENT); }
__device__ __forceinline__ u32 fl(u32 i) { return (i >> 5) * 1024 + (i & 31) * 4; }
constexpr u32 SPIN_CAP = 1u << 20;
#define SLP_W 2
#define SLP_C 8
__device__ __forceinline__ void until(u64 t) { while (__builtin_amdgcn_s_memrealtime() < t) __builtin_amdgcn_s_sleep(8); }
#define TDEL 0
__device__ __forceinline__ u64 succ(char* ws) {
    const u64 e = __hip_atomic_load((gu64*)(ws + OFF_EP), RLX_AGENT);
    return (u64)((u32)(e >> 32) + 1) << 32 | ((u32)e + 1);
}
__device__ __forceinline__ void wait_go(char* ws) {
    const u64 e = succ(ws);
    for (u32 n = 0; n < SPIN_CAP && __builtin_amdgcn_ballot_w64(__hip_atomic_load((gu64*)(ws + OFF_GO + (threadIdx.x & 7) * 128), RLX_AGENT) != e); ++n) __builtin_amdgcn_s_sleep(2);
    CBAR();
}
__device__ __forceinline__ bool spin_fail(char* ws, u32& spins) {
    ++spins;
    if ((spins & 63u) == 0u) {
        if (ldw(ws, OFF_ABORT)) return true;
        if (spins > SPIN_CAP) { __hip_atomic_store((gu32*)(ws + OFF_ABORT), 1u, RLX_AGENT); return true; }
    }
    return false;
}
__device__ __forceinline__ void wait_set(char* ws, u32 off) {
    u32 spins = 0;
    while (__builtin_amdgcn_ballot_w64(ldw(ws, off) == 0)) {
        if (spin_fail(ws, spins)) break;
        __builtin_amdgcn_s_sleep(SLP_W);
    }
    CBAR();
}
constexpr u32 OOB = 0x80000000u;
__device__ __forceinline__ u32 cgo(u32 cg) { return (cg >> 5) * 16384 + (cg & 31) * 256; }
__device__ __forceinline__ u32 sumo(u32 l) {
    const u32 lane = threadIdx.x & 63, b = l * 128 + (lane >> 5) * 64 + (lane & 15) * 4;
    return lane & 16 ? OOB : O_SUM + b / 576 * 1024 + b % 576;
}
__device__ __forceinline__ u32 sums(v4u x) {
    float s = 0.f, q = 0.f;
    __builtin_amdgcn_sched_barrier(0);
#pragma unroll
    for (int e = 0; e < 4; ++e) {
        const u32 z = x[e];
        const bfv2 y = __builtin_bit_cast(bfv2, z);
        s = __builtin_amdgcn_fdot2_f32_bf16(y, __builtin_bit_cast(bfv2, 0x3f803f80u), s, false);
        q = __builtin_amdgcn_fdot2_f32_bf16(y, y, q, false);
    }
    auto a = __builtin_amdgcn_permlane32_swap(__float_as_uint(s), __float_as_uint(q), false, false);
    const u32 r = __float_as_uint(__uint_as_float(a[0]) + __uint_as_float(a[1]));
    a = __builtin_amdgcn_permlane16_swap(r, r, false, false);
    return __float_as_uint(__uint_as_float(a[0]) + __uint_as_float(a[1]));
}

template <int N, bool FIN>
__device__ __forceinline__ void t_item(int rid, u32 i, u32 j, u32 m, u32 pol, u32 w, rsrc_t ro, char* ws, rsrc_t rs) {
    const u32 lane = threadIdx.x & 63, s = i & 63, side = (((s >> 5) ^ pol ^ m) & 1) << 13;
    v4u x[N], p[N][4];
    u32 v[N];
    wait_set(ws, OFF_FB + m * 4096 + j * 128 + fl(i));
#pragma unroll
    for (int u = 0; u < N; ++u) { v[u] = cgo(12 * j + 4 * (w + u) + (lane >> 4)) + (lane & 15) * 16; x[u] = ld16(rs, OFF_TB + (i << 16) + (FIN ? side : m << 13) + v[u]); }
    wait_set(ws, OFF_FP + (m * 3 + (j >> 1)) * 4096 + (lane & 3) * 128 + fl(i));
    STAMP(i, 0);
#pragma unroll
    for (int u = 0; u < N; ++u)
#pragma unroll
        for (int k = 0; k < 4; ++k) p[u][k] = __builtin_amdgcn_raw_buffer_load_b128(rs, v[u], OFF_PB + (i << 18) + (k << 16) + ((((i >> 3) ^ pol ^ m) & 1) << 13), 16);
#pragma unroll
    for (int u = 0; u < N; ++u) {
#pragma unroll
        for (int e = 0; e < 4; ++e) {
            f32x2 a = {bflo(x[u][e]), bfhi(x[u][e])};
#pragma unroll
            for (int k = 0; k < 4; ++k) a += f32x2{bflo(p[u][k][e]), bfhi(p[u][k][e])};
            x[u][e] = pack2(a[0], a[1]);
        }
        if (FIN) __builtin_amdgcn_raw_buffer_store_b128(x[u], ro, (s * 32 + 2 * (lane & 15) + m) * 1152 + (12 * j + 4 * (w + u) + (lane >> 4)) * 16, 0, 0);
        else {
            st16(rs, OFF_TA + ((64 + s) << 16) + side + v[u], x[u]);
            __builtin_amdgcn_raw_buffer_store_b32(sums(x[u]), rs, sumo(3 * j + w), OFF_TA + ((64 + s) << 16) + side, 16);
        }
    }
    STAMP(i, 1);
}
__device__ __forceinline__ bool t_last(u32* cnt) {
    DRAIN();
    u32 o = 0;
    if ((threadIdx.x & 63) == 0) o = __hip_atomic_fetch_add(cnt, 1u, __ATOMIC_RELAXED, __HIP_MEMORY_SCOPE_WORKGROUP);
    return __builtin_amdgcn_readfirstlane(o) == 2;
}
__device__ __forceinline__ void role_t(u64 t_in, int rid, u32 j, u32 m, const u16* x0, u16* out, char* ws, rsrc_t rs, char* smem) {
    const u32 lane = threadIdx.x & 63, w = __builtin_amdgcn_readfirstlane(threadIdx.x >> 6);
    u32* cnt = (u32*)smem + 64;
    cnt[threadIdx.x] = 0;
    __syncthreads();
    const rsrc_t ro = __builtin_amdgcn_make_buffer_rsrc(out, 0, XB, 0x00020000);
    if (w < 3) {
        const rsrc_t rx = __builtin_amdgcn_make_buffer_rsrc((void*)x0, 0, XB, 0x00020000);
        const u32 cg = 12 * j + 4 * w + (lane >> 4), vx = (2 * (lane & 15) + m) * 1152 + cg * 16, v = OFF_TA + (m << 13) + cgo(cg) + (lane & 15) * 16;
#pragma unroll 1
        for (u32 s0 = 0; s0 < 64; s0 += s0 < 4 ? 2 : 10) {
            const u32 n = s0 < 4 ? 2 : 10;
            if (s0 == 14) until(t_in + TDEL);
            v4u x[10];
#pragma unroll
            for (u32 u = 0; u < 10; ++u) x[u] = __builtin_amdgcn_raw_buffer_load_b128(rx, u < n ? vx + (s0 + u) * 36864 : OOB, 0, 0);
#pragma unroll
            for (u32 u = 0; u < 10; ++u) if (u < n) {
                st16(rs, v + ((s0 + u) << 16), x[u]);
                __builtin_amdgcn_raw_buffer_store_b32(sums(x[u]), rs, sumo(3 * j + w), OFF_TA + (m << 13) + ((s0 + u) << 16), 16);
            }
            if (s0 == 0) { wait_go(ws); __builtin_amdgcn_s_barrier(); }
            if (t_last(cnt + s0) && lane < n) setw(ws, OFF_FA + m * 4096 + j * 128 + fl(s0 + lane));
        }
    }
    else __builtin_amdgcn_s_barrier();
    CBAR();
    wait_set(ws, OFF_POL);
    const u32 pol = ldw(ws, OFF_POL);
    if (w == 3) {
        for (u32 i = 64; i < 127; ++i) t_item<3, true>(rid, i, j, m, pol, 0, ro, ws, rs);
        return;
    }
    for (u32 i = 0; i < 64; ++i) {
        t_item<1, false>(rid, i, j, m, pol, w, ro, ws, rs);
        if (t_last(cnt + 64 + i) && lane == 0) setw(ws, OFF_FA + m * 4096 + j * 128 + fl(64 + i));
    }
    t_item<1, true>(rid, 127, j, m, pol, w, ro, ws, rs);
}

#define CLOBBERS "memory", "vcc", "scc", \
    "v0", "v1", "v2", "v3", "v4", "v5", "v6", "v7", "v8", "v9", "v10", "v11", "v12", "v13", "v14", "v15", "v16", "v17", "v18", "v19", "v20", "v21", "v22", "v23", \
    "v24", "v25", "v26", "v27", "v28", "v29", "v30", "v31", "v32", "v33", "v34", "v35", "v36", "v37", "v38", "v39", "v40", "v41", "v42", "v43", "v44", "v45", "v46", "v47", \
    "v48", "v49", "v50", "v51", "v52", "v53", "v54", "v55", "v56", "v57", "v58", "v59", "v60", "v61", "v62", "v63", "v64", "v65", "v66", "v67", "v68", "v69", "v70", "v71", \
    "v72", "v73", "v74", "v75", "v76", "v77", "v78", "v79", "v80", "v81", "v82", "v83", "v84", "v85", "v86", "v87", "v88", "v89", "v90", "v91", "v92", "v93", "v94", "v95", \
    "v96", "v97", "v98", "v99", "v100", "v101", "v102", "v103", "v104", "v105", "v106", "v107", "v108", "v109", "v110", "v111", "v112", "v113", "v114", "v115", "v116", "v117", "v118", "v119", \
    "v120", "v121", "v122", "v123", "v124", "v125", "v126", "v127", "v128", "v129", "v130", "v131", "v132", "v133", "v134", "v135", "v136", "v137", "v138", "v139", "v140", "v141", "v142", "v143", \
    "v144", "v145", "v146", "v147", "v148", "v149", "v150", "v151", "v152", "v153", "v154", "v155", "v156", "v157", "v158", "v159", "v160", "v161", "v162", "v163", "v164", "v165", "v166", "v167", \
    "v168", "v169", "v170", "v171", "v172", "v173", "v174", "v175", "v176", "v177", "v178", "v179", "v180", "v181", "v182", "v183", "v184", "v185", "v186", "v187", "v188", "v189", "v190", "v191", \
    "v192", "v193", "v194", "v195", "v196", "v197", "v198", "v199", "v200", "v201", "v202", "v203", "v204", "v205", "v206", "v207", "v208", "v209", "v210", "v211", "v212", "v213", "v214", "v215", \
    "v216", "v217", "v218", "v219", "v220", "v221", "v222", "v223", "v224", "v225", "v226", "v227", "v228", "v229", "v230", "v231", "v232", "v233", "v234", "v235", "v236", "v237", "v238", "v239", \
    "v240", "v241", "v242", "v243", "v244", "v245", "v246", "v247", "v248", "v249", "v250", "v251", "v252", "v253", "v254", "v255", "a0", "a1", "a2", "a3", "a4", "a5", "a6", "a7", \
    "a8", "a9", "a10", "a11", "a12", "a13", "a14", "a15", "a16", "a17", "a18", "a19", "a20", "a21", "a22", "a23", "a24", "a25", "a26", "a27", "a28", "a29", "a30", "a31", \
    "a32", "a33", "a34", "a35", "a36", "a37", "a38", "a39", "a40", "a41", "a42", "a43", "a44", "a45", "a46", "a47", "a48", "a49", "a50", "a51", "a52", "a53", "a54", "a55", \
    "a56", "a57", "a58", "a59", "a60", "a61", "a62", "a63", "a64", "a65", "a66", "a67", "a68", "a69", "a70", "a71", "a72", "a73", "a74", "a75", "a76", "a77", "a78", "a79", \
    "a80", "a81", "a82", "a83", "a84", "a85", "a86", "a87", "a88", "a89", "a90", "a91", "a92", "a93", "a94", "a95", "a96", "a97", "a98", "a99", "a100", "a101", "a102", "a103", \
    "a104", "a105", "a106", "a107", "a108", "a109", "a110", "a111", "a112", "a113", "a114", "a115", "a116", "a117", "a118", "a119", "a120", "a121", "a122", "a123", "a124", "a125", "a126", "a127", \
    "a128", "a129", "a130", "a131", "a132", "a133", "a134", "a135", "a136", "a137", "a138", "a139", "a140", "a141", "a142", "a143", "a144", "a145", "a146", "a147", "a148", "a149", "a150", "a151", \
    "a152", "a153", "a154", "a155", "a156", "a157", "a158", "a159", "a160", "a161", "a162", "a163", "a164", "a165", "a166", "a167", "a168", "a169", "a170", "a171", "a172", "a173", "a174", "a175", \
    "a176", "a177", "a178", "a179", "a180", "a181", "a182", "a183", "a184", "a185", "a186", "a187", "a188", "a189", "a190", "a191", "a192", "a193", "a194", "a195", "a196", "a197", "a198", "a199", \
    "a200", "a201", "a202", "a203", "a204", "a205", "a206", "a207", "a208", "a209", "a210", "a211", "a212", "a213", "a214", "a215", "a216", "a217", "a218", "a219", "a220", "a221", "a222", "a223", \
    "a224", "a225", "a226", "a227", "a228", "a229", "a230", "a231", "a232", "a233", "a234", "a235", "a236", "a237", "a238", "a239", "a240", "a241", "a242", "a243", "a244", "a245", "a246", "a247", \
    "a248", "a249", "a250", "a251", "a252", "a253", "a254", "a255", "s38", "s39", "s40", "s41", "s42", "s43", "s44", "s45", "s46", "s47", "s48", "s49", "s50", "s51", "s52", "s53", \
    "s54", "s55", "s56", "s57", "s58", "s59", "s60", "s61", "s62", "s63", "s64", "s65", "s66", "s67", "s68", "s69", "s70", "s71", "s72", "s73", "s74", "s75", "s76", "s77", \
    "s78", "s79", "s80", "s81", "s82", "s83", "s84", "s85", "s86", "s87", "s88", "s89", "s90", "s91", "s92", "s93", "s94", "s95", "s96", "s97", "s98", "s99", "s100", "s101"
#define LANES() const int tid = threadIdx.x, lane = tid & 63, c = lane & 15, g = lane >> 4, q = c >> 2, p4 = c & 3; \
    const int wave = __builtin_amdgcn_readfirstlane(tid >> 6); u32* tab = (u32*)(smem + L_TAB) + tid; (void)q; (void)p4; (void)g
struct Par { u32 role, h, p, woff; };
__device__ __forceinline__ u32 rows(u32 i, u32 m) { return (i << 16) + (i < 64 ? m << 13 : (((i >> 5 ^ m) & 1) << 13) + 1); }
__device__ __forceinline__ u32 slot(u32 i) { return ((i & 63) << 19) + (((i >> 2 ^ i >> 6) & 1) << 13) + 1; }
__device__ __forceinline__ u32 part(u32 i, u32 m) { return (i << 18) + (((i >> 3 ^ m) & 1) << 13) + 1; }
template <class FS, class FD>
__device__ __forceinline__ u32 items(u32 K, u32 mk, u32 fin, u32 fout, FS src, FD dst, char* smem) {
    const u32 n = threadIdx.x - 1;
    auto idx = [&](u32 k) { const u32 t = k - K; return t < 2 * (64 - K) ? ((t & 1) ? K : 64) + (t >> 1) : k & 127; };
    if (threadIdx.x < 130) {
        v4u* row = (v4u*)(smem + L_IT) + 2 * threadIdx.x;
        row[0] = v4u{n + 1 == mk ? OFF_NEVER + 4 : n + 1 < 128 ? fin + fl(idx(n + 1)) : OFF_NEVER, src(idx(n + 1)), dst(idx(n)), fout + fl(idx(n - 1))};
        row[1] = v4u{0u, idx(n) & 63 ? 0u : 0xf149f2cau, 0x14000 + (idx(n) >> 6) * 16384, idx(n)};
    }
    return fin + fl(mk ? 64 : 0);
}
#define KA 13
#define KB 12
#define KH 11
#define KW 12

__device__ __forceinline__ Par role_h(u32 m, u32 h, char* smem) {
    LANES();
    tab[0] = 0x10000 + wave * 8192 + lane * 16;
    tab[256] = tid * 16;
    tab[2 * 256] = cgo(16 * wave) + lane * 16;
    tab[3 * 256] = 0x10000 + 5120 + lane * 4;
    tab[4 * 256] = (8 * wave + g) * 8192 + (c ^ ((g | (wave & 1) << 2) << 1)) * 16;
    tab[5 * 256] = (4 * g + q) * 256 + ((2 * wave) ^ (q | (g & 1) << 2)) * 32 + p4 * 8;
    tab[6 * 256] = tab[5 * 256] ^ 32;
    const u32 f = items(KH, 0, OFF_FB + m * 4096, OFF_FKV + (h >> 2) * 4096 + (h & 3) * 256 + m * 128, [&](u32 i) { return OFF_TB + rows(i, m); }, [&](u32 i) { return OFF_QX + h * 16384 + m * 4096 + slot(i); }, smem);
    return Par{1, f, 0, h * 256};
}

__device__ __forceinline__ Par role_w(u32 m, u32 kb, u32 nb, char* smem) {
    LANES();
    tab[0] = wave * 8192 + lane * 16;
    tab[256] = wave * 32768 + lane * 16;
    tab[512] = cgo(24 * nb + 6 * wave + g) + c * 16;
    tab[768] = (256 * wave + 4 * g + (c >> 3)) * 1152 + ((lane & 7) ^ ((g & 1) << 2)) * 16;
#pragma unroll
    for (int t = 0; t < 3; ++t) {
        const int col = wave * 96 + (t < 2 ? p4 * 16 + t * 8 : 64 + p4 * 8);
        tab[(4 + t) * 256] = (col >> 7) * 2048 + (q & 1) * 1024 + (2 * g + (q >> 1)) * 128 + ((col & 127) ^ ((g & 1) << 6));
    }
    tab[7 * 256] = (g & 1) ? OOB : cgo(24 * nb + 6 * wave + 4 + (g >> 1)) + c * 16;
    const u32 f = items(KW, 0, OFF_COB + (m * 4 + kb) * 4096, OFF_FP + (m * 3 + nb) * 4096 + kb * 128, [&](u32 i) { return OFF_OX + kb * 8 * 16384 + m * 4096 + slot(i); },
          [&](u32 i) { return OFF_PB + kb * 65536 + part(i, m); }, smem);
    return Par{2, f, 0, kb * 1024 * 1152 + nb * 384};
}

__device__ __forceinline__ Par role_a2(u32 m, u32 j, char* smem) {
    LANES();
    const int w = wave < 3 ? wave : 2;
    tab[0] = 0x8000 + lane * 16;
    tab[256] = tid * 16;
    tab[512] = cgo(16 * wave) + lane * 16;
    tab[768] = wave < 3 ? cgo(12 * j + 4 * wave + g) + c * 16 : OOB;
    tab[4 * 256] = 0x9000 + g * 256 + c * 4;
    tab[5 * 256] = wave < 3 ? sumo(3 * j + wave) : OOB;
    tab[6 * 256] = (16 * wave + (lane >> 2)) * 1152 + (lane & 3) * 16;
    tab[7 * 256] = w * 36864 + p4 * 16 + (20 * (g >> 1) + 8 * (g & 1) + q) * 64;
    tab[8 * 256] = w * 36864 + p4 * 16 + (72 * g + 16 + q) * 64;
    const u32 f = items(KB, KB, OFF_FO + m * 4096, OFF_FB + m * 4096 + j * 128, [&](u32 i) { return OFF_OA + rows(i, m); }, [&](u32 i) { return OFF_TB + rows(i, m); }, smem);
    return Par{4, f, 0, j * 192};
}

__device__ __forceinline__ Par role_a1(u32 m, u32 hg, char* smem) {
    LANES();
    const int hd = 4 * hg + wave;
    tab -= 768;
    tab[0] = wave * 8192 + lane * 16;
    tab[256] = !(g & 1) ? cgo(2 * hd + (g >> 1)) + c * 16 : g == 1 && !p4 ? 32768 + hd * 64 + c * 4 : OOB;
    tab[2 * 256] = cgo(16 * wave) + lane * 16;
    tab[3 * 256] = tid * 16;
    tab[4 * 256] = L_EX + ((wave >> 1) * 16 + c) * 16 + (wave & 1) * 8;
    tab[5 * 256] = g ? L_EX + 4096 : tab[4 * 256] + 512;
    tab[6 * 256] = L_EX + 2048 + ((wave >> 1) * 16 + 4 * g) * 16 + (2 * (wave & 1) + (c & 1)) * 4;
    tab[7 * 256] = 5120 + lane * 4;
    tab[8 * 256] = 7424 + g * 16;
    tab[9 * 256] = 4 * g - c;
    tab[10 * 256] = (4 * g + q) * 144 + 36 * wave + 4 * (wave & 1) + p4 * 8;
    tab[11 * 256] = (wave < 2 ? p4 >> 1 : 2) * 9216 + (4 * g + q) * 144 + 72 * (p4 & 1) + 32;
    if (tid < 2) *(v4u*)(smem + L_EX + 4096 + tid * 1024) = v4u{0u, 0u, 0u, 0u};
    const u32 f = items(KA, KA, OFF_FA + m * 4096, OFF_FO + m * 4096 + hg * 128, [&](u32 i) { return OFF_TA + rows(i, m); }, [&](u32 i) { return OFF_OA + rows(i, m); }, smem);
    return Par{3, f, 0, hg * 144};
}

#define SB() __builtin_amdgcn_sched_barrier(0)
__device__ __forceinline__ u32 now32() { return (u32)__builtin_amdgcn_s_memtime(); }
__device__ __forceinline__ Par role_chain(u32 h, u32 p, char* smem) {
    LANES();
    const int ro = (q & 1) + 4 * (q >> 1) + 8 * (g >> 1) + 16 * (g & 1), rr = (q & 1) | (g & 1) << 1 | (q >> 1) << 2;
    tab[0] = lane * 16;
    tab[256] = L_W + (wave * 15 + (wave == 3 ? 2 : 0)) * 2048 + lane * 16;
    tab[2 * 256] = ((g & 1) + 4 * (g >> 1) + 8 * wave) * 8192 + (c ^ ((g | (wave & 1) << 2) << 1)) * 16;
    tab[3 * 256] = (rr + 8 * (g >> 1)) * 256 + ((2 * wave) ^ rr) * 32 + p4 * 8;
    tab[4 * 256] = tab[3 * 256] ^ 32;
    tab[5 * 256] = (2 * wave + (lane >> 5)) * 1152 + ((lane & 31) ^ ((lane >> 5 | (wave & 2)) << 2)) * 16;
    tab[6 * 256] = ((q & 1) + 4 * (g >> 1) + 16 * (g & 1)) * 512 + ((2 * wave + (p4 >> 1)) ^ ((q & 1) | (g & 2))) * 64 + (q >> 1) * 16 + (p4 & 1) * 8;
    tab[7 * 256] = (32 * wave + (lane >> 2)) * 1152 + (lane & 3) * 16;
    tab[8 * 256] = ro * 64 + (wave & 1) * 32 + p4 * 8;
    return Par{0, h, p, 0};
}

#define MAP 3
constexpr int RW = 90, RA1 = 24, RA2 = MAP >= 2 ? 184 : 56, RTT = 58;
#define PFO 1
#define WDEL1 700
#define CDEL 1000
#define WDEL 700
#define NPOL 6
static_assert(OFF_GO == 131072u);
static_assert(OFF_EP == 132096u);
static_assert(OFF_POL == 64u);
static_assert(OFF_RDY == 128u);
static_assert(OFF_COB == 53248u);
static_assert(OFF_FKV == 86016u);
static_assert(OFF_XA == 0u);
static_assert(OFF_XB == 2097152u);
static_assert(OFF_TA == 8388608u);
static_assert(OFF_OA == 25165824u);
static_assert(OFF_NEVER == 192u);
static_assert(L_IT == 158720u);
static_assert(OFF_QX == 67108864u);
static_assert(OFF_KX == 100663296u);
static_assert(OFF_VX == 134217728u);
static_assert(OFF_OX == 167772160u);
static_assert(WS_BYTES == 201326592u);
static_assert(L_TAB == 147456u);
static_assert(L_EX == 138240u);
static_assert(O_SUM == 34816u);
static_assert(L_W == 32064);
__device__ __forceinline__ void measure_pol(char* ws, rsrc_t rs, char* smem) {
    const int wave = __builtin_amdgcn_readfirstlane(threadIdx.x >> 6);
    u32 best[2] = {~0u, ~0u};
    for (int n = 0; n < NPOL; ++n)
#pragma unroll
        for (int i = 0; i < 2; ++i) {
            SB();
            const u32 t0 = now32();
            SB();
            st4(rs, (wave & 1) * MB2 + (508 + 2 * i + (wave >> 1)) * 4096 + n * 128, t0);
            DRAIN();
            const u32 t = now32() - t0;
            best[i] = t < best[i] ? t : best[i];
        }
    int* d = (int*)smem;
    if ((threadIdx.x & 63) == 0) d[wave] = (int)(best[0] - best[1]);
    wait_go(ws);
    __syncthreads();
    if (threadIdx.x == 0) __hip_atomic_store((gu32*)(ws + OFF_POL), 4u | (d[0] + d[2] >= 0) | (d[1] + d[3] >= 0) << 1, RLX_AGENT);
}

__global__ __launch_bounds__(256, 1) void mega_kernel(const u16* __restrict__ x0, const u16* __restrict__ wqkv, const u16* __restrict__ woA,
                                                     const u16* __restrict__ mem0, const u16* __restrict__ Wq, const u16* __restrict__ Wk,
                                                     const u16* __restrict__ Wv, const u16* __restrict__ Wo, u16* out, char* ws) {
    __shared__ __attribute__((aligned(16))) char smem[163840];
    char* wd = (char*)(((uintptr_t)ws + SYNC_BYTES + MB2 - 1) & ~(uintptr_t)(MB2 - 1));
    const rsrc_t rs = __builtin_amdgcn_make_buffer_rsrc(wd, 0, WS_BYTES, 0x00020000);
    const u64 t_in = __builtin_amdgcn_s_memrealtime();
    {
        const char* pc; u32 n;
        asm volatile("s_getpc_b64 %0\n.Lbeg:\ns_mov_b32 %1, .Lfin-.Lbeg" : "=s"(pc), "=s"(n));
        const u32 o = ((blockIdx.x >> 3 & 3) * 256 + threadIdx.x) * 128;
        if (o < n) TOUCH(pc + o);
    }
    u32 rid = (blockIdx.x & 7) * 32 + (blockIdx.x >> 3);
    rid = rid - 24 < 8 ? rid + 120 : rid - 144 < 8 ? rid - 120 : rid;
    asm volatile("" : "+s"(rid));
    STAMP(159, 0);
    const u32 xq = (rid >> 5) & 3, t = rid & 31, layer = rid >> 7;
    const int u = t - 24;
    const int role = t < 16 ? 0 : t < 24 ? 1 : xq == 0 ? 3 : u < 2 ? 4 : xq == 1 ? 5 : 2;
    const int ix = role == 3 ? u : role == 4 ? 2 * xq - 2 + u : role == 5 ? u - 2 : (xq * 6 + u - 5) % 12;
    if (role == 5) { role_t(t_in, rid, ix, layer, x0, out, ws, rs, smem); asm volatile("s_endpgm" ::: "memory"); }
    if (role == 0 && layer) {
        const u32 i = t * 256 + threadIdx.x;
        for (u32 k = xq; k < (xq ? xq + 1 : 4); ++k) if (i < 3888) TOUCH((const char*)wqkv + (k * 3888 + i) * 128);
        if (xq && i < 1728) TOUCH((const char*)woA + (i / 3) * 1152 + (xq - 1) * 384 + (i % 3) * 128);
        for (const u16* w : {Wk, Wv})
            for (u32 k = i; k < 9216; k += 4096) TOUCH((const char*)w + (k >> 4) * 8192 + xq * 2048 + (k & 15) * 128);
        if (PFO) for (u32 k = i; k < 9216; k += 4096) TOUCH((const char*)Wo + (xq * 9216 + k) * 128);
        for (u32 k = 0; k < 18; ++k) st16(rs, (k % 3 * 3 >> 1) * MB2 + k / 3 * 262144 + (xq * 4096 + i) * 16, v4u{~0u, ~0u, ~0u, ~0u});
        DRAIN();
        wait_go(ws);
        __syncthreads();
        if (threadIdx.x == 0) __hip_atomic_fetch_add((gu32*)(ws + OFF_RDY), 1u, RLX_AGENT);
    }
    if (role == 2 && layer && ix < 8) {
        const u64 e = succ(ws);
        const rsrc_t rc = __builtin_amdgcn_make_buffer_rsrc(ws, 0, OFF_GO, 0x00020000);
        for (u32 k = 0; k < 4; ++k) st16(rc, ix * 16384 + k * 4096 + threadIdx.x * 16, v4u{0u, 0u, 0u, 0u});
        DRAIN();
        __syncthreads();
        if (threadIdx.x == 0) __hip_atomic_store((gu64*)(ws + OFF_GO + ix * 128), e, RLX_AGENT);
    }
    if (rid == RW) measure_pol(ws, rs, smem);
    until(t_in + (role == 2 ? (layer ? WDEL1 : WDEL) : role == 0 && layer ? CDEL : 0));
    Par P{5, 0, 0, 0};
    const u16* w0 = role == 2 ? Wo : role == 3 ? wqkv : role == 4 ? woA : Wq;
    if (role == 0) P = role_chain(xq * 8 + (t >> 1), t & 1, smem);
    else if (role == 1) P = role_h(layer, xq * 8 + t - 16, smem);
    else if (role == 2) P = role_w(layer, ix / 3, ix % 3, smem);
    else if (role == 3) P = role_a1(layer, ix, smem);
    else if (role == 4) P = role_a2(layer ^ (MAP >= 2), ix, smem);
    if (role == 0) P.woff = (u32)t_in;
    const int wave = __builtin_amdgcn_readfirstlane(threadIdx.x >> 6);
    asm volatile(
    "s_cmp_eq_u32 %13, 1\n"
    "s_cbranch_scc1 .Lrole1_%=\n"
    "s_cmp_eq_u32 %13, 2\n"
    "s_cbranch_scc1 .Lrole2_%=\n"
    "s_cmp_eq_u32 %13, 3\n"
    "s_cbranch_scc1 .Lrole3_%=\n"
    "s_cmp_eq_u32 %13, 4\n"
    "s_cbranch_scc1 .Lrole4_%=\n"
    "s_cmp_eq_u32 %13, 5\n"
    "s_cbranch_scc1 .Lrole5_%=\n"
    "s_mov_b64 s[40:41], %0\n"
    "s_and_b32 s41, s41, 0xffff\n"
    "s_mov_b32 s42, 0xc000000\n"
    "s_mov_b32 s43, 0x20000\n"
    "s_mov_b32 s57, 131072\n"
    "v_mbcnt_lo_u32_b32 v10, -1, 0\n"
    "v_mbcnt_hi_u32_b32 v10, -1, v10\n"
    "s_lshl_b32 s73, %2, 8\n"
    "s_add_u32 s73, s73, 147456\n"
    "v_lshl_add_u32 v10, v10, 2, s73\n"
    "ds_read_b32 v0, v10\n"
    "ds_read_b32 v2, v10 offset:1024\n"
    "ds_read_b32 v1, v10 offset:2048\n"
    "ds_read_b32 v3, v10 offset:3072\n"
    "ds_read_b32 v4, v10 offset:4096\n"
    "ds_read_b32 v5, v10 offset:5120\n"
    "ds_read_b32 v6, v10 offset:6144\n"
    "ds_read_b32 v8, v10 offset:7168\n"
    "ds_read_b32 v9, v10 offset:8192\n"
    "s_waitcnt lgkmcnt(0)\n"
    "s_barrier\n"
    "s_xor_b32 s73, %2, 0\n"
    "s_lshl_b32 s73, s73, 12\n"
    "v_add_u32 v96, s73, v3\n"
    "v_add_u32 v97, s73, v4\n"
    "s_xor_b32 s73, %2, 1\n"
    "s_lshl_b32 s73, s73, 12\n"
    "v_add_u32 v98, s73, v3\n"
    "v_add_u32 v99, s73, v4\n"
    "s_xor_b32 s73, %2, 2\n"
    "s_lshl_b32 s73, s73, 12\n"
    "v_add_u32 v100, s73, v3\n"
    "v_add_u32 v101, s73, v4\n"
    "s_xor_b32 s73, %2, 3\n"
    "s_lshl_b32 s73, s73, 12\n"
    "v_add_u32 v102, s73, v3\n"
    "v_add_u32 v103, s73, v4\n"
    "s_lshl_b32 s60, %4, 8\n"
    "s_lshl_b32 s61, %2, 10\n"
    "s_mul_i32 s63, %4, 147456\n"
    "s_mul_i32 s62, %5, 576\n"
    "s_add_u32 s63, s63, s62\n"
    "s_mov_b64 s[44:45], %11\n"
    "s_and_b32 s45, s45, 0xffff\n"
    "s_mov_b32 s46, 0x480000\n"
    "s_mov_b32 s47, 0x20000\n"
    "s_lshl_b32 s73, s61, 1\n"
    "s_add_u32 m0, s73, 0\n"
    "s_add_u32 s62, s63, 512\n"
    "buffer_load_dwordx4 v8, s[44:47], s62 offen lds\n"
    "s_lshl_b32 s73, s61, 1\n"
    "s_add_u32 m0, s73, 1024\n"
    "s_add_u32 s62, s63, 18944\n"
    "buffer_load_dwordx4 v8, s[44:47], s62 offen lds\n"
    "s_mov_b64 vcc, 0\n"
    "s_mov_b32 vcc_lo, %7\n"
    "s_cbranch_vccz .Lsl0_%=\n"
    "s_sleep 10\n"
    ".Lsl0_%=:\n"
    "s_mov_b64 s[44:45], %8\n"
    "s_and_b32 s45, s45, 0xffff\n"
    "s_mov_b32 s46, 0x480000\n"
    "s_mov_b32 s47, 0x20000\n"
    "s_add_u32 m0, s61, 16384\n"
    "s_add_u32 s62, s60, 262144\n"
    "buffer_load_dwordx4 v1, s[44:47], s62 offen lds\n"
    "s_add_u32 m0, s61, 20480\n"
    "s_add_u32 s62, s60, 1310720\n"
    "buffer_load_dwordx4 v1, s[44:47], s62 offen lds\n"
    "s_add_u32 m0, s61, 24576\n"
    "s_add_u32 s62, s60, 2359296\n"
    "buffer_load_dwordx4 v1, s[44:47], s62 offen lds\n"
    "s_add_u32 m0, s61, 28672\n"
    "s_add_u32 s62, s60, 3407872\n"
    "buffer_load_dwordx4 v1, s[44:47], s62 offen lds\n"
    "s_cbranch_vccz .Lsl1_%=\n"
    "s_sleep 10\n"
    ".Lsl1_%=:\n"
    "s_add_u32 m0, s61, 32768\n"
    "s_add_u32 s62, s60, 786432\n"
    "buffer_load_dwordx4 v1, s[44:47], s62 offen lds\n"
    "s_add_u32 m0, s61, 36864\n"
    "s_add_u32 s62, s60, 1835008\n"
    "buffer_load_dwordx4 v1, s[44:47], s62 offen lds\n"
    "s_add_u32 m0, s61, 40960\n"
    "s_add_u32 s62, s60, 2883584\n"
    "buffer_load_dwordx4 v1, s[44:47], s62 offen lds\n"
    "s_add_u32 m0, s61, 45056\n"
    "s_add_u32 s62, s60, 3932160\n"
    "buffer_load_dwordx4 v1, s[44:47], s62 offen lds\n"
    "s_cbranch_vccz .Lsl2_%=\n"
    "s_sleep 10\n"
    ".Lsl2_%=:\n"
    "s_add_u32 m0, s61, 49152\n"
    "s_add_u32 s62, s60, 278528\n"
    "buffer_load_dwordx4 v1, s[44:47], s62 offen lds\n"
    "s_add_u32 m0, s61, 53248\n"
    "s_add_u32 s62, s60, 1327104\n"
    "buffer_load_dwordx4 v1, s[44:47], s62 offen lds\n"
    "s_add_u32 m0, s61, 57344\n"
    "s_add_u32 s62, s60, 2375680\n"
    "buffer_load_dwordx4 v1, s[44:47], s62 offen lds\n"
    "s_add_u32 m0, s61, 61440\n"
    "s_add_u32 s62, s60, 3424256\n"
    "buffer_load_dwordx4 v1, s[44:47], s62 offen lds\n"
    "s_cbranch_vccz .Lsl3_%=\n"
    "s_sleep 10\n"
    ".Lsl3_%=:\n"
    "s_add_u32 m0, s61, 65536\n"
    "s_add_u32 s62, s60, 802816\n"
    "buffer_load_dwordx4 v1, s[44:47], s62 offen lds\n"
    "s_add_u32 m0, s61, 69632\n"
    "s_add_u32 s62, s60, 1851392\n"
    "buffer_load_dwordx4 v1, s[44:47], s62 offen lds\n"
    "s_add_u32 m0, s61, 73728\n"
    "s_add_u32 s62, s60, 2899968\n"
    "buffer_load_dwordx4 v1, s[44:47], s62 offen lds\n"
    "s_add_u32 m0, s61, 77824\n"
    "s_add_u32 s62, s60, 3948544\n"
    "buffer_load_dwordx4 v1, s[44:47], s62 offen lds\n"
    "s_cbranch_vccz .Lsl4_%=\n"
    "s_sleep 10\n"
    ".Lsl4_%=:\n"
    "s_add_u32 m0, s61, 81920\n"
    "s_add_u32 s62, s60, 4194304\n"
    "buffer_load_dwordx4 v1, s[44:47], s62 offen lds\n"
    "s_add_u32 m0, s61, 86016\n"
    "s_add_u32 s62, s60, 4210688\n"
    "buffer_load_dwordx4 v1, s[44:47], s62 offen lds\n"
    "s_add_u32 m0, s61, 90112\n"
    "s_add_u32 s62, s60, 4456448\n"
    "buffer_load_dwordx4 v1, s[44:47], s62 offen lds\n"
    "s_add_u32 m0, s61, 94208\n"
    "s_add_u32 s62, s60, 4472832\n"
    "buffer_load_dwordx4 v1, s[44:47], s62 offen lds\n"
    "s_cbranch_vccz .Lsl5_%=\n"
    "s_sleep 10\n"
    ".Lsl5_%=:\n"
    "s_mov_b64 s[44:45], %9\n"
    "s_and_b32 s45, s45, 0xffff\n"
    "s_mov_b32 s46, 0x480000\n"
    "s_mov_b32 s47, 0x20000\n"
    "s_add_u32 m0, s61, 98304\n"
    "s_add_u32 s62, s60, 0\n"
    "buffer_load_dwordx4 v1, s[44:47], s62 offen lds\n"
    "s_add_u32 m0, s61, 102400\n"
    "s_add_u32 s62, s60, 1048576\n"
    "buffer_load_dwordx4 v1, s[44:47], s62 offen lds\n"
    "s_add_u32 m0, s61, 106496\n"
    "s_add_u32 s62, s60, 2097152\n"
    "buffer_load_dwordx4 v1, s[44:47], s62 offen lds\n"
    "s_add_u32 m0, s61, 110592\n"
    "s_add_u32 s62, s60, 3145728\n"
    "buffer_load_dwordx4 v1, s[44:47], s62 offen lds\n"
    "s_cbranch_vccz .Lsl6_%=\n"
    "s_sleep 10\n"
    ".Lsl6_%=:\n"
    "s_add_u32 m0, s61, 114688\n"
    "s_add_u32 s62, s60, 524288\n"
    "buffer_load_dwordx4 v1, s[44:47], s62 offen lds\n"
    "s_add_u32 m0, s61, 118784\n"
    "s_add_u32 s62, s60, 1572864\n"
    "buffer_load_dwordx4 v1, s[44:47], s62 offen lds\n"
    "s_add_u32 m0, s61, 122880\n"
    "s_add_u32 s62, s60, 2621440\n"
    "buffer_load_dwordx4 v1, s[44:47], s62 offen lds\n"
    "s_add_u32 m0, s61, 126976\n"
    "s_add_u32 s62, s60, 3670016\n"
    "buffer_load_dwordx4 v1, s[44:47], s62 offen lds\n"
    "s_cbranch_vccz .Lsl7_%=\n"
    "s_sleep 10\n"
    ".Lsl7_%=:\n"
    "s_add_u32 m0, s61, 131072\n"
    "s_add_u32 s62, s60, 16384\n"
    "buffer_load_dwordx4 v1, s[44:47], s62 offen lds\n"
    "s_add_u32 m0, s61, 135168\n"
    "s_add_u32 s62, s60, 1064960\n"
    "buffer_load_dwordx4 v1, s[44:47], s62 offen lds\n"
    "s_add_u32 m0, s61, 139264\n"
    "s_add_u32 s62, s60, 2113536\n"
    "buffer_load_dwordx4 v1, s[44:47], s62 offen lds\n"
    "s_add_u32 m0, s61, 143360\n"
    "s_add_u32 s62, s60, 3162112\n"
    "buffer_load_dwordx4 v1, s[44:47], s62 offen lds\n"
    "s_cbranch_vccz .Lsl8_%=\n"
    "s_sleep 10\n"
    ".Lsl8_%=:\n"
    "s_add_u32 m0, s61, 147456\n"
    "s_add_u32 s62, s60, 540672\n"
    "buffer_load_dwordx4 v1, s[44:47], s62 offen lds\n"
    "s_add_u32 m0, s61, 151552\n"
    "s_add_u32 s62, s60, 1589248\n"
    "buffer_load_dwordx4 v1, s[44:47], s62 offen lds\n"
    "s_add_u32 m0, s61, 155648\n"
    "s_add_u32 s62, s60, 2637824\n"
    "buffer_load_dwordx4 v1, s[44:47], s62 offen lds\n"
    "s_add_u32 m0, s61, 159744\n"
    "s_add_u32 s62, s60, 3686400\n"
    "buffer_load_dwordx4 v1, s[44:47], s62 offen lds\n"
    "s_waitcnt vmcnt(32) lgkmcnt(0)\n"
    "s_barrier\n"
    "ds_read_b64_tr_b16 v[36:37], v96 offset:16384\n"
    "ds_read_b64_tr_b16 v[68:69], v98 offset:16384\n"
    "ds_read_b64_tr_b16 v[148:149], v100 offset:16384\n"
    "ds_read_b64_tr_b16 v[180:181], v102 offset:16384\n"
    "ds_read_b64_tr_b16 v[44:45], v97 offset:16384\n"
    "ds_read_b64_tr_b16 v[76:77], v99 offset:16384\n"
    "ds_read_b64_tr_b16 v[156:157], v101 offset:16384\n"
    "ds_read_b64_tr_b16 v[188:189], v103 offset:16384\n"
    "s_waitcnt vmcnt(28) lgkmcnt(0)\n"
    "s_barrier\n"
    "s_cbranch_vccz .Lsl9_%=\n"
    "s_sleep 10\n"
    ".Lsl9_%=:\n"
    "s_add_u32 m0, s61, 16384\n"
    "s_add_u32 s62, s60, 262144\n"
    "buffer_load_dwordx4 v1, s[44:47], s62 offen lds\n"
    "s_add_u32 m0, s61, 20480\n"
    "s_add_u32 s62, s60, 1310720\n"
    "buffer_load_dwordx4 v1, s[44:47], s62 offen lds\n"
    "s_add_u32 m0, s61, 24576\n"
    "s_add_u32 s62, s60, 2359296\n"
    "buffer_load_dwordx4 v1, s[44:47], s62 offen lds\n"
    "s_add_u32 m0, s61, 28672\n"
    "s_add_u32 s62, s60, 3407872\n"
    "buffer_load_dwordx4 v1, s[44:47], s62 offen lds\n"
    "ds_read_b64_tr_b16 v[52:53], v96 offset:32768\n"
    "ds_read_b64_tr_b16 v[84:85], v98 offset:32768\n"
    "ds_read_b64_tr_b16 v[164:165], v100 offset:32768\n"
    "ds_read_b64_tr_b16 v[196:197], v102 offset:32768\n"
    "ds_read_b64_tr_b16 v[60:61], v97 offset:32768\n"
    "ds_read_b64_tr_b16 v[140:141], v99 offset:32768\n"
    "ds_read_b64_tr_b16 v[172:173], v101 offset:32768\n"
    "ds_read_b64_tr_b16 v[204:205], v103 offset:32768\n"
    "s_waitcnt vmcnt(28) lgkmcnt(0)\n"
    "s_barrier\n"
    "s_cbranch_vccz .Lsl10_%=\n"
    "s_sleep 10\n"
    ".Lsl10_%=:\n"
    "s_add_u32 m0, s61, 32768\n"
    "s_add_u32 s62, s60, 786432\n"
    "buffer_load_dwordx4 v1, s[44:47], s62 offen lds\n"
    "s_add_u32 m0, s61, 36864\n"
    "s_add_u32 s62, s60, 1835008\n"
    "buffer_load_dwordx4 v1, s[44:47], s62 offen lds\n"
    "s_add_u32 m0, s61, 40960\n"
    "s_add_u32 s62, s60, 2883584\n"
    "buffer_load_dwordx4 v1, s[44:47], s62 offen lds\n"
    "s_add_u32 m0, s61, 45056\n"
    "s_add_u32 s62, s60, 3932160\n"
    "buffer_load_dwordx4 v1, s[44:47], s62 offen lds\n"
    "ds_read_b64_tr_b16 v[38:39], v96 offset:49152\n"
    "ds_read_b64_tr_b16 v[70:71], v98 offset:49152\n"
    "ds_read_b64_tr_b16 v[150:151], v100 offset:49152\n"
    "ds_read_b64_tr_b16 v[182:183], v102 offset:49152\n"
    "ds_read_b64_tr_b16 v[46:47], v97 offset:49152\n"
    "ds_read_b64_tr_b16 v[78:79], v99 offset:49152\n"
    "ds_read_b64_tr_b16 v[158:159], v101 offset:49152\n"
    "ds_read_b64_tr_b16 v[190:191], v103 offset:49152\n"
    "s_waitcnt vmcnt(28) lgkmcnt(0)\n"
    "s_barrier\n"
    "s_cbranch_vccz .Lsl11_%=\n"
    "s_sleep 10\n"
    ".Lsl11_%=:\n"
    "s_add_u32 m0, s61, 49152\n"
    "s_add_u32 s62, s60, 278528\n"
    "buffer_load_dwordx4 v1, s[44:47], s62 offen lds\n"
    "s_add_u32 m0, s61, 53248\n"
    "s_add_u32 s62, s60, 1327104\n"
    "buffer_load_dwordx4 v1, s[44:47], s62 offen lds\n"
    "s_add_u32 m0, s61, 57344\n"
    "s_add_u32 s62, s60, 2375680\n"
    "buffer_load_dwordx4 v1, s[44:47], s62 offen lds\n"
    "s_add_u32 m0, s61, 61440\n"
    "s_add_u32 s62, s60, 3424256\n"
    "buffer_load_dwordx4 v1, s[44:47], s62 offen lds\n"
    "v_add_u32 v127, 0x10000, v96\n"
    "ds_read_b64_tr_b16 v[54:55], v127\n"
    "v_add_u32 v126, 0x10000, v98\n"
    "ds_read_b64_tr_b16 v[86:87], v126\n"
    "v_add_u32 v125, 0x10000, v100\n"
    "ds_read_b64_tr_b16 v[166:167], v125\n"
    "v_add_u32 v124, 0x10000, v102\n"
    "ds_read_b64_tr_b16 v[198:199], v124\n"
    "v_add_u32 v123, 0x10000, v97\n"
    "ds_read_b64_tr_b16 v[62:63], v123\n"
    "v_add_u32 v122, 0x10000, v99\n"
    "ds_read_b64_tr_b16 v[142:143], v122\n"
    "v_add_u32 v121, 0x10000, v101\n"
    "ds_read_b64_tr_b16 v[174:175], v121\n"
    "v_add_u32 v120, 0x10000, v103\n"
    "ds_read_b64_tr_b16 v[206:207], v120\n"
    "s_waitcnt vmcnt(28) lgkmcnt(0)\n"
    "s_barrier\n"
    "s_cbranch_vccz .Lsl12_%=\n"
    "s_sleep 10\n"
    ".Lsl12_%=:\n"
    "s_add_u32 m0, s61, 65536\n"
    "s_add_u32 s62, s60, 802816\n"
    "buffer_load_dwordx4 v1, s[44:47], s62 offen lds\n"
    "s_add_u32 m0, s61, 69632\n"
    "s_add_u32 s62, s60, 1851392\n"
    "buffer_load_dwordx4 v1, s[44:47], s62 offen lds\n"
    "s_add_u32 m0, s61, 73728\n"
    "s_add_u32 s62, s60, 2899968\n"
    "buffer_load_dwordx4 v1, s[44:47], s62 offen lds\n"
    "s_add_u32 m0, s61, 77824\n"
    "s_add_u32 s62, s60, 3948544\n"
    "buffer_load_dwordx4 v1, s[44:47], s62 offen lds\n"
    "v_add_u32 v119, 0x10000, v3\n"
    "ds_read_b64_tr_b16 v[208:209], v119 offset:16384\n"
    "ds_read_b64_tr_b16 v[210:211], v119 offset:20480\n"
    "ds_read_b64_tr_b16 v[212:213], v119 offset:24576\n"
    "ds_read_b64_tr_b16 v[214:215], v119 offset:28672\n"
    "v_add_u32 v118, 0x10000, v4\n"
    "ds_read_b64_tr_b16 v[216:217], v118 offset:16384\n"
    "ds_read_b64_tr_b16 v[218:219], v118 offset:20480\n"
    "ds_read_b64_tr_b16 v[220:221], v118 offset:24576\n"
    "ds_read_b64_tr_b16 v[222:223], v118 offset:28672\n"
    "s_waitcnt vmcnt(28) lgkmcnt(0)\n"
    "s_barrier\n"
    "s_cbranch_vccz .Lsl13_%=\n"
    "s_sleep 10\n"
    ".Lsl13_%=:\n"
    "s_add_u32 m0, s61, 81920\n"
    "s_add_u32 s62, s60, 4194304\n"
    "buffer_load_dwordx4 v1, s[44:47], s62 offen lds\n"
    "s_add_u32 m0, s61, 86016\n"
    "s_add_u32 s62, s60, 4210688\n"
    "buffer_load_dwordx4 v1, s[44:47], s62 offen lds\n"
    "s_add_u32 m0, s61, 90112\n"
    "s_add_u32 s62, s60, 4456448\n"
    "buffer_load_dwordx4 v1, s[44:47], s62 offen lds\n"
    "s_add_u32 m0, s61, 94208\n"
    "s_add_u32 s62, s60, 4472832\n"
    "buffer_load_dwordx4 v1, s[44:47], s62 offen lds\n"
    "ds_read_b64_tr_b16 v[224:225], v127 offset:32768\n"
    "ds_read_b64_tr_b16 a[0:1], v126 offset:32768\n"
    "ds_read_b64_tr_b16 a[32:33], v125 offset:32768\n"
    "ds_read_b64_tr_b16 a[64:65], v124 offset:32768\n"
    "ds_read_b64_tr_b16 v[232:233], v123 offset:32768\n"
    "ds_read_b64_tr_b16 a[8:9], v122 offset:32768\n"
    "ds_read_b64_tr_b16 a[40:41], v121 offset:32768\n"
    "ds_read_b64_tr_b16 a[72:73], v120 offset:32768\n"
    "s_waitcnt vmcnt(28) lgkmcnt(0)\n"
    "s_barrier\n"
    "s_cbranch_vccz .Lsl14_%=\n"
    "s_sleep 10\n"
    ".Lsl14_%=:\n"
    "s_mov_b64 s[44:45], %10\n"
    "s_and_b32 s45, s45, 0xffff\n"
    "s_mov_b32 s46, 0x480000\n"
    "s_mov_b32 s47, 0x20000\n"
    "s_add_u32 m0, s61, 98304\n"
    "s_add_u32 s62, s60, 0\n"
    "buffer_load_dwordx4 v1, s[44:47], s62 offen lds\n"
    "s_add_u32 m0, s61, 102400\n"
    "s_add_u32 s62, s60, 1048576\n"
    "buffer_load_dwordx4 v1, s[44:47], s62 offen lds\n"
    "s_add_u32 m0, s61, 106496\n"
    "s_add_u32 s62, s60, 2097152\n"
    "buffer_load_dwordx4 v1, s[44:47], s62 offen lds\n"
    "s_add_u32 m0, s61, 110592\n"
    "s_add_u32 s62, s60, 3145728\n"
    "buffer_load_dwordx4 v1, s[44:47], s62 offen lds\n"
    "ds_read_b64_tr_b16 v[240:241], v127 offset:49152\n"
    "ds_read_b64_tr_b16 a[16:17], v126 offset:49152\n"
    "ds_read_b64_tr_b16 a[48:49], v125 offset:49152\n"
    "ds_read_b64_tr_b16 a[80:81], v124 offset:49152\n"
    "ds_read_b64_tr_b16 v[248:249], v123 offset:49152\n"
    "ds_read_b64_tr_b16 a[24:25], v122 offset:49152\n"
    "ds_read_b64_tr_b16 a[56:57], v121 offset:49152\n"
    "ds_read_b64_tr_b16 a[88:89], v120 offset:49152\n"
    "s_waitcnt vmcnt(28) lgkmcnt(0)\n"
    "s_barrier\n"
    "s_cbranch_vccz .Lsl15_%=\n"
    "s_sleep 10\n"
    ".Lsl15_%=:\n"
    "s_add_u32 m0, s61, 114688\n"
    "s_add_u32 s62, s60, 524288\n"
    "buffer_load_dwordx4 v1, s[44:47], s62 offen lds\n"
    "s_add_u32 m0, s61, 118784\n"
    "s_add_u32 s62, s60, 1572864\n"
    "buffer_load_dwordx4 v1, s[44:47], s62 offen lds\n"
    "s_add_u32 m0, s61, 122880\n"
    "s_add_u32 s62, s60, 2621440\n"
    "buffer_load_dwordx4 v1, s[44:47], s62 offen lds\n"
    "s_add_u32 m0, s61, 126976\n"
    "s_add_u32 s62, s60, 3670016\n"
    "buffer_load_dwordx4 v1, s[44:47], s62 offen lds\n"
    "v_add_u32 v117, 0x20000, v96\n"
    "ds_read_b64_tr_b16 v[226:227], v117\n"
    "v_add_u32 v116, 0x20000, v98\n"
    "ds_read_b64_tr_b16 a[2:3], v116\n"
    "v_add_u32 v115, 0x20000, v100\n"
    "ds_read_b64_tr_b16 a[34:35], v115\n"
    "v_add_u32 v114, 0x20000, v102\n"
    "ds_read_b64_tr_b16 a[66:67], v114\n"
    "v_add_u32 v113, 0x20000, v97\n"
    "ds_read_b64_tr_b16 v[234:235], v113\n"
    "v_add_u32 v112, 0x20000, v99\n"
    "ds_read_b64_tr_b16 a[10:11], v112\n"
    "v_add_u32 v111, 0x20000, v101\n"
    "ds_read_b64_tr_b16 a[42:43], v111\n"
    "v_add_u32 v110, 0x20000, v103\n"
    "ds_read_b64_tr_b16 a[74:75], v110\n"
    "s_waitcnt vmcnt(28) lgkmcnt(0)\n"
    "s_barrier\n"
    "s_cbranch_vccz .Lsl16_%=\n"
    "s_sleep 10\n"
    ".Lsl16_%=:\n"
    "s_add_u32 m0, s61, 131072\n"
    "s_add_u32 s62, s60, 16384\n"
    "buffer_load_dwordx4 v1, s[44:47], s62 offen lds\n"
    "s_add_u32 m0, s61, 135168\n"
    "s_add_u32 s62, s60, 1064960\n"
    "buffer_load_dwordx4 v1, s[44:47], s62 offen lds\n"
    "s_add_u32 m0, s61, 139264\n"
    "s_add_u32 s62, s60, 2113536\n"
    "buffer_load_dwordx4 v1, s[44:47], s62 offen lds\n"
    "s_add_u32 m0, s61, 143360\n"
    "s_add_u32 s62, s60, 3162112\n"
    "buffer_load_dwordx4 v1, s[44:47], s62 offen lds\n"
    "ds_read_b64_tr_b16 v[242:243], v117 offset:16384\n"
    "ds_read_b64_tr_b16 a[18:19], v116 offset:16384\n"
    "ds_read_b64_tr_b16 a[50:51], v115 offset:16384\n"
    "ds_read_b64_tr_b16 a[82:83], v114 offset:16384\n"
    "ds_read_b64_tr_b16 v[250:251], v113 offset:16384\n"
    "ds_read_b64_tr_b16 a[26:27], v112 offset:16384\n"
    "ds_read_b64_tr_b16 a[58:59], v111 offset:16384\n"
    "ds_read_b64_tr_b16 a[90:91], v110 offset:16384\n"
    "s_waitcnt vmcnt(28) lgkmcnt(0)\n"
    "s_barrier\n"
    "s_cbranch_vccz .Lsl17_%=\n"
    "s_sleep 10\n"
    ".Lsl17_%=:\n"
    "s_add_u32 m0, s61, 147456\n"
    "s_add_u32 s62, s60, 540672\n"
    "buffer_load_dwordx4 v1, s[44:47], s62 offen lds\n"
    "s_add_u32 m0, s61, 151552\n"
    "s_add_u32 s62, s60, 1589248\n"
    "buffer_load_dwordx4 v1, s[44:47], s62 offen lds\n"
    "s_add_u32 m0, s61, 155648\n"
    "s_add_u32 s62, s60, 2637824\n"
    "buffer_load_dwordx4 v1, s[44:47], s62 offen lds\n"
    "s_add_u32 m0, s61, 159744\n"
    "s_add_u32 s62, s60, 3686400\n"
    "buffer_load_dwordx4 v1, s[44:47], s62 offen lds\n"
    "ds_read_b64_tr_b16 v[228:229], v96 offset:16384\n"
    "ds_read_b64_tr_b16 a[4:5], v98 offset:16384\n"
    "ds_read_b64_tr_b16 a[36:37], v100 offset:16384\n"
    "ds_read_b64_tr_b16 a[68:69], v102 offset:16384\n"
    "ds_read_b64_tr_b16 v[236:237], v97 offset:16384\n"
    "ds_read_b64_tr_b16 a[12:13], v99 offset:16384\n"
    "ds_read_b64_tr_b16 a[44:45], v101 offset:16384\n"
    "ds_read_b64_tr_b16 a[76:77], v103 offset:16384\n"
    "s_waitcnt vmcnt(28) lgkmcnt(0)\n"
    "s_barrier\n"
    "s_cbranch_vccz .Lsl18_%=\n"
    "s_sleep 10\n"
    ".Lsl18_%=:\n"
    "s_add_u32 m0, s61, 16384\n"
    "s_add_u32 s62, s60, 262144\n"
    "buffer_load_dwordx4 v1, s[44:47], s62 offen lds\n"
    "s_add_u32 m0, s61, 20480\n"
    "s_add_u32 s62, s60, 1310720\n"
    "buffer_load_dwordx4 v1, s[44:47], s62 offen lds\n"
    "s_add_u32 m0, s61, 24576\n"
    "s_add_u32 s62, s60, 2359296\n"
    "buffer_load_dwordx4 v1, s[44:47], s62 offen lds\n"
    "s_add_u32 m0, s61, 28672\n"
    "s_add_u32 s62, s60, 3407872\n"
    "buffer_load_dwordx4 v1, s[44:47], s62 offen lds\n"
    "ds_read_b64_tr_b16 v[244:245], v96 offset:32768\n"
    "ds_read_b64_tr_b16 a[20:21], v98 offset:32768\n"
    "ds_read_b64_tr_b16 a[52:53], v100 offset:32768\n"
    "ds_read_b64_tr_b16 a[84:85], v102 offset:32768\n"
    "ds_read_b64_tr_b16 v[252:253], v97 offset:32768\n"
    "ds_read_b64_tr_b16 a[28:29], v99 offset:32768\n"
    "ds_read_b64_tr_b16 a[60:61], v101 offset:32768\n"
    "ds_read_b64_tr_b16 a[92:93], v103 offset:32768\n"
    "s_waitcnt vmcnt(28) lgkmcnt(0)\n"
    "s_barrier\n"
    "s_cbranch_vccz .Lsl19_%=\n"
    "s_sleep 10\n"
    ".Lsl19_%=:\n"
    "s_add_u32 m0, s61, 32768\n"
    "s_add_u32 s62, s60, 786432\n"
    "buffer_load_dwordx4 v1, s[44:47], s62 offen lds\n"
    "s_add_u32 m0, s61, 36864\n"
    "s_add_u32 s62, s60, 1835008\n"
    "buffer_load_dwordx4 v1, s[44:47], s62 offen lds\n"
    "s_add_u32 m0, s61, 40960\n"
    "s_add_u32 s62, s60, 2883584\n"
    "buffer_load_dwordx4 v1, s[44:47], s62 offen lds\n"
    "s_add_u32 m0, s61, 45056\n"
    "s_add_u32 s62, s60, 3932160\n"
    "buffer_load_dwordx4 v1, s[44:47], s62 offen lds\n"
    "ds_read_b64_tr_b16 v[230:231], v96 offset:49152\n"
    "ds_read_b64_tr_b16 a[6:7], v98 offset:49152\n"
    "ds_read_b64_tr_b16 a[38:39], v100 offset:49152\n"
    "ds_read_b64_tr_b16 a[70:71], v102 offset:49152\n"
    "ds_read_b64_tr_b16 v[238:239], v97 offset:49152\n"
    "ds_read_b64_tr_b16 a[14:15], v99 offset:49152\n"
    "ds_read_b64_tr_b16 a[46:47], v101 offset:49152\n"
    "ds_read_b64_tr_b16 a[78:79], v103 offset:49152\n"
    "s_waitcnt vmcnt(28) lgkmcnt(0)\n"
    "s_barrier\n"
    "s_cbranch_vccz .Lsl20_%=\n"
    "s_sleep 10\n"
    ".Lsl20_%=:\n"
    "s_add_u32 m0, s61, 49152\n"
    "s_add_u32 s62, s60, 278528\n"
    "buffer_load_dwordx4 v1, s[44:47], s62 offen lds\n"
    "s_add_u32 m0, s61, 53248\n"
    "s_add_u32 s62, s60, 1327104\n"
    "buffer_load_dwordx4 v1, s[44:47], s62 offen lds\n"
    "s_add_u32 m0, s61, 57344\n"
    "s_add_u32 s62, s60, 2375680\n"
    "buffer_load_dwordx4 v1, s[44:47], s62 offen lds\n"
    "s_add_u32 m0, s61, 61440\n"
    "s_add_u32 s62, s60, 3424256\n"
    "buffer_load_dwordx4 v1, s[44:47], s62 offen lds\n"
    "ds_read_b64_tr_b16 v[246:247], v127\n"
    "ds_read_b64_tr_b16 a[22:23], v126\n"
    "ds_read_b64_tr_b16 a[54:55], v125\n"
    "ds_read_b64_tr_b16 a[86:87], v124\n"
    "ds_read_b64_tr_b16 v[254:255], v123\n"
    "ds_read_b64_tr_b16 a[30:31], v122\n"
    "ds_read_b64_tr_b16 a[62:63], v121\n"
    "ds_read_b64_tr_b16 a[94:95], v120\n"
    "s_waitcnt vmcnt(28) lgkmcnt(0)\n"
    "s_barrier\n"
    "s_cbranch_vccz .Lsl21_%=\n"
    "s_sleep 10\n"
    ".Lsl21_%=:\n"
    "s_add_u32 m0, s61, 65536\n"
    "s_add_u32 s62, s60, 802816\n"
    "buffer_load_dwordx4 v1, s[44:47], s62 offen lds\n"
    "s_add_u32 m0, s61, 69632\n"
    "s_add_u32 s62, s60, 1851392\n"
    "buffer_load_dwordx4 v1, s[44:47], s62 offen lds\n"
    "s_add_u32 m0, s61, 73728\n"
    "s_add_u32 s62, s60, 2899968\n"
    "buffer_load_dwordx4 v1, s[44:47], s62 offen lds\n"
    "s_add_u32 m0, s61, 77824\n"
    "s_add_u32 s62, s60, 3948544\n"
    "buffer_load_dwordx4 v1, s[44:47], s62 offen lds\n"
    "ds_read_b64_tr_b16 a[96:97], v119 offset:16384\n"
    "ds_read_b64_tr_b16 a[98:99], v119 offset:20480\n"
    "ds_read_b64_tr_b16 a[100:101], v119 offset:24576\n"
    "ds_read_b64_tr_b16 a[102:103], v119 offset:28672\n"
    "ds_read_b64_tr_b16 a[104:105], v118 offset:16384\n"
    "ds_read_b64_tr_b16 a[106:107], v118 offset:20480\n"
    "ds_read_b64_tr_b16 a[108:109], v118 offset:24576\n"
    "ds_read_b64_tr_b16 a[110:111], v118 offset:28672\n"
    "s_waitcnt vmcnt(28) lgkmcnt(0)\n"
    "s_barrier\n"
    "s_cbranch_vccz .Lsl22_%=\n"
    "s_sleep 10\n"
    ".Lsl22_%=:\n"
    "s_add_u32 m0, s61, 81920\n"
    "s_add_u32 s62, s60, 4194304\n"
    "buffer_load_dwordx4 v1, s[44:47], s62 offen lds\n"
    "s_add_u32 m0, s61, 86016\n"
    "s_add_u32 s62, s60, 4210688\n"
    "buffer_load_dwordx4 v1, s[44:47], s62 offen lds\n"
    "s_add_u32 m0, s61, 90112\n"
    "s_add_u32 s62, s60, 4456448\n"
    "buffer_load_dwordx4 v1, s[44:47], s62 offen lds\n"
    "s_add_u32 m0, s61, 94208\n"
    "s_add_u32 s62, s60, 4472832\n"
    "buffer_load_dwordx4 v1, s[44:47], s62 offen lds\n"
    "ds_read_b64_tr_b16 a[112:113], v127 offset:32768\n"
    "ds_read_b64_tr_b16 a[144:145], v126 offset:32768\n"
    "ds_read_b64_tr_b16 a[176:177], v125 offset:32768\n"
    "ds_read_b64_tr_b16 a[208:209], v124 offset:32768\n"
    "ds_read_b64_tr_b16 a[120:121], v123 offset:32768\n"
    "ds_read_b64_tr_b16 a[152:153], v122 offset:32768\n"
    "ds_read_b64_tr_b16 a[184:185], v121 offset:32768\n"
    "ds_read_b64_tr_b16 a[216:217], v120 offset:32768\n"
    "s_waitcnt vmcnt(28) lgkmcnt(0)\n"
    "s_barrier\n"
    "s_cbranch_vccz .Lsl23_%=\n"
    "s_sleep 10\n"
    ".Lsl23_%=:\n"
    "s_mov_b64 s[44:45], %8\n"
    "s_and_b32 s45, s45, 0xffff\n"
    "s_mov_b32 s46, 0x480000\n"
    "s_mov_b32 s47, 0x20000\n"
    "s_add_u32 m0, s61, 98304\n"
    "s_add_u32 s62, s60, 0\n"
    "buffer_load_dwordx4 v1, s[44:47], s62 offen lds\n"
    "s_add_u32 m0, s61, 102400\n"
    "s_add_u32 s62, s60, 1048576\n"
    "buffer_load_dwordx4 v1, s[44:47], s62 offen lds\n"
    "s_add_u32 m0, s61, 106496\n"
    "s_add_u32 s62, s60, 2097152\n"
    "buffer_load_dwordx4 v1, s[44:47], s62 offen lds\n"
    "s_add_u32 m0, s61, 110592\n"
    "s_add_u32 s62, s60, 3145728\n"
    "buffer_load_dwordx4 v1, s[44:47], s62 offen lds\n"
    "ds_read_b64_tr_b16 a[128:129], v127 offset:49152\n"
    "ds_read_b64_tr_b16 a[160:161], v126 offset:49152\n"
    "ds_read_b64_tr_b16 a[192:193], v125 offset:49152\n"
    "ds_read_b64_tr_b16 a[224:225], v124 offset:49152\n"
    "ds_read_b64_tr_b16 a[136:137], v123 offset:49152\n"
    "ds_read_b64_tr_b16 a[168:169], v122 offset:49152\n"
    "ds_read_b64_tr_b16 a[200:201], v121 offset:49152\n"
    "ds_read_b64_tr_b16 a[232:233], v120 offset:49152\n"
    "s_waitcnt vmcnt(28) lgkmcnt(0)\n"
    "s_barrier\n"
    "s_cbranch_vccz .Lsl24_%=\n"
    "s_sleep 10\n"
    ".Lsl24_%=:\n"
    "s_add_u32 m0, s61, 114688\n"
    "s_add_u32 s62, s60, 524288\n"
    "buffer_load_dwordx4 v1, s[44:47], s62 offen lds\n"
    "s_add_u32 m0, s61, 118784\n"
    "s_add_u32 s62, s60, 1572864\n"
    "buffer_load_dwordx4 v1, s[44:47], s62 offen lds\n"
    "s_add_u32 m0, s61, 122880\n"
    "s_add_u32 s62, s60, 2621440\n"
    "buffer_load_dwordx4 v1, s[44:47], s62 offen lds\n"
    "s_add_u32 m0, s61, 126976\n"
    "s_add_u32 s62, s60, 3670016\n"
    "buffer_load_dwordx4 v1, s[44:47], s62 offen lds\n"
    "ds_read_b64_tr_b16 a[114:115], v117\n"
    "ds_read_b64_tr_b16 a[146:147], v116\n"
    "ds_read_b64_tr_b16 a[178:179], v115\n"
    "ds_read_b64_tr_b16 a[210:211], v114\n"
    "ds_read_b64_tr_b16 a[122:123], v113\n"
    "ds_read_b64_tr_b16 a[154:155], v112\n"
    "ds_read_b64_tr_b16 a[186:187], v111\n"
    "ds_read_b64_tr_b16 a[218:219], v110\n"
    "s_waitcnt vmcnt(28) lgkmcnt(0)\n"
    "s_barrier\n"
    "s_cbranch_vccz .Lsl25_%=\n"
    "s_sleep 10\n"
    ".Lsl25_%=:\n"
    "s_add_u32 m0, s61, 131072\n"
    "s_add_u32 s62, s60, 16384\n"
    "buffer_load_dwordx4 v1, s[44:47], s62 offen lds\n"
    "s_add_u32 m0, s61, 135168\n"
    "s_add_u32 s62, s60, 1064960\n"
    "buffer_load_dwordx4 v1, s[44:47], s62 offen lds\n"
    "s_add_u32 m0, s61, 139264\n"
    "s_add_u32 s62, s60, 2113536\n"
    "buffer_load_dwordx4 v1, s[44:47], s62 offen lds\n"
    "s_add_u32 m0, s61, 143360\n"
    "s_add_u32 s62, s60, 3162112\n"
    "buffer_load_dwordx4 v1, s[44:47], s62 offen lds\n"
    "ds_read_b64_tr_b16 a[130:131], v117 offset:16384\n"
    "ds_read_b64_tr_b16 a[162:163], v116 offset:16384\n"
    "ds_read_b64_tr_b16 a[194:195], v115 offset:16384\n"
    "ds_read_b64_tr_b16 a[226:227], v114 offset:16384\n"
    "ds_read_b64_tr_b16 a[138:139], v113 offset:16384\n"
    "ds_read_b64_tr_b16 a[170:171], v112 offset:16384\n"
    "ds_read_b64_tr_b16 a[202:203], v111 offset:16384\n"
    "ds_read_b64_tr_b16 a[234:235], v110 offset:16384\n"
    "s_waitcnt vmcnt(28) lgkmcnt(0)\n"
    "s_barrier\n"
    "s_cbranch_vccz .Lsl26_%=\n"
    "s_sleep 10\n"
    ".Lsl26_%=:\n"
    "s_add_u32 m0, s61, 147456\n"
    "s_add_u32 s62, s60, 540672\n"
    "buffer_load_dwordx4 v1, s[44:47], s62 offen lds\n"
    "s_add_u32 m0, s61, 151552\n"
    "s_add_u32 s62, s60, 1589248\n"
    "buffer_load_dwordx4 v1, s[44:47], s62 offen lds\n"
    "s_add_u32 m0, s61, 155648\n"
    "s_add_u32 s62, s60, 2637824\n"
    "buffer_load_dwordx4 v1, s[44:47], s62 offen lds\n"
    "s_add_u32 m0, s61, 159744\n"
    "s_add_u32 s62, s60, 3686400\n"
    "buffer_load_dwordx4 v1, s[44:47], s62 offen lds\n"
    "ds_read_b64_tr_b16 a[116:117], v96 offset:16384\n"
    "ds_read_b64_tr_b16 a[148:149], v98 offset:16384\n"
    "ds_read_b64_tr_b16 a[180:181], v100 offset:16384\n"
    "ds_read_b64_tr_b16 a[212:213], v102 offset:16384\n"
    "ds_read_b64_tr_b16 a[124:125], v97 offset:16384\n"
    "ds_read_b64_tr_b16 a[156:157], v99 offset:16384\n"
    "ds_read_b64_tr_b16 a[188:189], v101 offset:16384\n"
    "ds_read_b64_tr_b16 a[220:221], v103 offset:16384\n"
    "s_waitcnt vmcnt(28) lgkmcnt(0)\n"
    "s_barrier\n"
    "s_cbranch_vccz .Lsl27_%=\n"
    "s_sleep 10\n"
    ".Lsl27_%=:\n"
    "s_mov_b64 s[44:45], %11\n"
    "s_and_b32 s45, s45, 0xffff\n"
    "s_mov_b32 s46, 0x480000\n"
    "s_mov_b32 s47, 0x20000\n"
    "s_add_u32 m0, s61, 16384\n"
    "s_add_u32 s62, s63, 0\n"
    "buffer_load_dwordx4 v5, s[44:47], s62 offen lds\n"
    "s_add_u32 m0, s61, 20480\n"
    "s_add_u32 s62, s63, 9216\n"
    "buffer_load_dwordx4 v5, s[44:47], s62 offen lds\n"
    "s_add_u32 m0, s61, 24576\n"
    "s_add_u32 s62, s63, 18432\n"
    "buffer_load_dwordx4 v5, s[44:47], s62 offen lds\n"
    "s_add_u32 m0, s61, 28672\n"
    "s_add_u32 s62, s63, 27648\n"
    "buffer_load_dwordx4 v5, s[44:47], s62 offen lds\n"
    "ds_read_b64_tr_b16 a[132:133], v96 offset:32768\n"
    "ds_read_b64_tr_b16 a[164:165], v98 offset:32768\n"
    "ds_read_b64_tr_b16 a[196:197], v100 offset:32768\n"
    "ds_read_b64_tr_b16 a[228:229], v102 offset:32768\n"
    "ds_read_b64_tr_b16 a[140:141], v97 offset:32768\n"
    "ds_read_b64_tr_b16 a[172:173], v99 offset:32768\n"
    "ds_read_b64_tr_b16 a[204:205], v101 offset:32768\n"
    "ds_read_b64_tr_b16 a[236:237], v103 offset:32768\n"
    "s_waitcnt vmcnt(28) lgkmcnt(0)\n"
    "s_barrier\n"
    "s_cbranch_vccz .Lsl28_%=\n"
    "s_sleep 10\n"
    ".Lsl28_%=:\n"
    "s_add_u32 m0, s61, 32768\n"
    "s_add_u32 s62, s63, 36864\n"
    "buffer_load_dwordx4 v5, s[44:47], s62 offen lds\n"
    "s_add_u32 m0, s61, 36864\n"
    "s_add_u32 s62, s63, 46080\n"
    "buffer_load_dwordx4 v5, s[44:47], s62 offen lds\n"
    "s_add_u32 m0, s61, 40960\n"
    "s_add_u32 s62, s63, 55296\n"
    "buffer_load_dwordx4 v5, s[44:47], s62 offen lds\n"
    "s_add_u32 m0, s61, 45056\n"
    "s_add_u32 s62, s63, 64512\n"
    "buffer_load_dwordx4 v5, s[44:47], s62 offen lds\n"
    "ds_read_b64_tr_b16 a[118:119], v96 offset:49152\n"
    "ds_read_b64_tr_b16 a[150:151], v98 offset:49152\n"
    "ds_read_b64_tr_b16 a[182:183], v100 offset:49152\n"
    "ds_read_b64_tr_b16 a[214:215], v102 offset:49152\n"
    "ds_read_b64_tr_b16 a[126:127], v97 offset:49152\n"
    "ds_read_b64_tr_b16 a[158:159], v99 offset:49152\n"
    "ds_read_b64_tr_b16 a[190:191], v101 offset:49152\n"
    "ds_read_b64_tr_b16 a[222:223], v103 offset:49152\n"
    "s_waitcnt vmcnt(28) lgkmcnt(0)\n"
    "s_barrier\n"
    "s_cbranch_vccz .Lsl29_%=\n"
    "s_sleep 10\n"
    ".Lsl29_%=:\n"
    "s_add_u32 m0, s61, 49152\n"
    "s_add_u32 s62, s63, 73728\n"
    "buffer_load_dwordx4 v5, s[44:47], s62 offen lds\n"
    "s_add_u32 m0, s61, 53248\n"
    "s_add_u32 s62, s63, 82944\n"
    "buffer_load_dwordx4 v5, s[44:47], s62 offen lds\n"
    "s_add_u32 m0, s61, 57344\n"
    "s_add_u32 s62, s63, 92160\n"
    "buffer_load_dwordx4 v5, s[44:47], s62 offen lds\n"
    "s_add_u32 m0, s61, 61440\n"
    "s_add_u32 s62, s63, 101376\n"
    "buffer_load_dwordx4 v5, s[44:47], s62 offen lds\n"
    "ds_read_b64_tr_b16 a[134:135], v127\n"
    "ds_read_b64_tr_b16 a[166:167], v126\n"
    "ds_read_b64_tr_b16 a[198:199], v125\n"
    "ds_read_b64_tr_b16 a[230:231], v124\n"
    "ds_read_b64_tr_b16 a[142:143], v123\n"
    "ds_read_b64_tr_b16 a[174:175], v122\n"
    "ds_read_b64_tr_b16 a[206:207], v121\n"
    "ds_read_b64_tr_b16 a[238:239], v120\n"
    "s_waitcnt vmcnt(28) lgkmcnt(0)\n"
    "s_barrier\n"
    "s_cbranch_vccz .Lsl30_%=\n"
    "s_sleep 10\n"
    ".Lsl30_%=:\n"
    "s_add_u32 m0, s61, 65536\n"
    "s_add_u32 s62, s63, 110592\n"
    "buffer_load_dwordx4 v5, s[44:47], s62 offen lds\n"
    "s_add_u32 m0, s61, 69632\n"
    "s_add_u32 s62, s63, 119808\n"
    "buffer_load_dwordx4 v5, s[44:47], s62 offen lds\n"
    "s_add_u32 m0, s61, 73728\n"
    "s_add_u32 s62, s63, 129024\n"
    "buffer_load_dwordx4 v5, s[44:47], s62 offen lds\n"
    "s_add_u32 m0, s61, 77824\n"
    "s_add_u32 s62, s63, 138240\n"
    "buffer_load_dwordx4 v5, s[44:47], s62 offen lds\n"
    "ds_read_b64_tr_b16 a[240:241], v119 offset:16384\n"
    "ds_read_b64_tr_b16 a[242:243], v119 offset:20480\n"
    "ds_read_b64_tr_b16 a[244:245], v119 offset:24576\n"
    "ds_read_b64_tr_b16 a[246:247], v119 offset:28672\n"
    "ds_read_b64_tr_b16 a[248:249], v118 offset:16384\n"
    "ds_read_b64_tr_b16 a[250:251], v118 offset:20480\n"
    "ds_read_b64_tr_b16 a[252:253], v118 offset:24576\n"
    "ds_read_b64_tr_b16 a[254:255], v118 offset:28672\n"
    "s_waitcnt vmcnt(28) lgkmcnt(0)\n"
    "s_barrier\n"
    "ds_read_b64_tr_b16 v[32:33], v127 offset:32768\n"
    "ds_read_b64_tr_b16 v[64:65], v126 offset:32768\n"
    "ds_read_b64_tr_b16 v[144:145], v125 offset:32768\n"
    "ds_read_b64_tr_b16 v[176:177], v124 offset:32768\n"
    "ds_read_b64_tr_b16 v[40:41], v123 offset:32768\n"
    "ds_read_b64_tr_b16 v[72:73], v122 offset:32768\n"
    "ds_read_b64_tr_b16 v[152:153], v121 offset:32768\n"
    "ds_read_b64_tr_b16 v[184:185], v120 offset:32768\n"
    "s_waitcnt vmcnt(24) lgkmcnt(0)\n"
    "s_barrier\n"
    "ds_read_b64_tr_b16 v[48:49], v127 offset:49152\n"
    "ds_read_b64_tr_b16 v[80:81], v126 offset:49152\n"
    "ds_read_b64_tr_b16 v[160:161], v125 offset:49152\n"
    "ds_read_b64_tr_b16 v[192:193], v124 offset:49152\n"
    "ds_read_b64_tr_b16 v[56:57], v123 offset:49152\n"
    "ds_read_b64_tr_b16 v[136:137], v122 offset:49152\n"
    "ds_read_b64_tr_b16 v[168:169], v121 offset:49152\n"
    "ds_read_b64_tr_b16 v[200:201], v120 offset:49152\n"
    "s_waitcnt vmcnt(20) lgkmcnt(0)\n"
    "s_barrier\n"
    "ds_read_b64_tr_b16 v[34:35], v117\n"
    "ds_read_b64_tr_b16 v[66:67], v116\n"
    "ds_read_b64_tr_b16 v[146:147], v115\n"
    "ds_read_b64_tr_b16 v[178:179], v114\n"
    "ds_read_b64_tr_b16 v[42:43], v113\n"
    "ds_read_b64_tr_b16 v[74:75], v112\n"
    "ds_read_b64_tr_b16 v[154:155], v111\n"
    "ds_read_b64_tr_b16 v[186:187], v110\n"
    "s_waitcnt vmcnt(16) lgkmcnt(0)\n"
    "s_barrier\n"
    "ds_read_b64_tr_b16 v[50:51], v117 offset:16384\n"
    "ds_read_b64_tr_b16 v[82:83], v116 offset:16384\n"
    "ds_read_b64_tr_b16 v[162:163], v115 offset:16384\n"
    "ds_read_b64_tr_b16 v[194:195], v114 offset:16384\n"
    "ds_read_b64_tr_b16 v[58:59], v113 offset:16384\n"
    "ds_read_b64_tr_b16 v[138:139], v112 offset:16384\n"
    "ds_read_b64_tr_b16 v[170:171], v111 offset:16384\n"
    "ds_read_b64_tr_b16 v[202:203], v110 offset:16384\n"
    "s_waitcnt vmcnt(12) lgkmcnt(0)\n"
    "s_barrier\n"
    "ds_read_b64_tr_b16 v[88:89], v6 offset:16384\n"
    "ds_read_b64_tr_b16 v[90:91], v6 offset:17408\n"
    "ds_read_b64_tr_b16 v[92:93], v6 offset:20480\n"
    "ds_read_b64_tr_b16 v[94:95], v6 offset:21504\n"
    "ds_read_b64_tr_b16 v[96:97], v6 offset:16416\n"
    "ds_read_b64_tr_b16 v[98:99], v6 offset:17440\n"
    "ds_read_b64_tr_b16 v[100:101], v6 offset:20512\n"
    "ds_read_b64_tr_b16 v[102:103], v6 offset:21536\n"
    "s_waitcnt vmcnt(8) lgkmcnt(0)\n"
    "s_barrier\n"
    "ds_read_b64_tr_b16 v[104:105], v6 offset:32768\n"
    "ds_read_b64_tr_b16 v[106:107], v6 offset:33792\n"
    "ds_read_b64_tr_b16 v[108:109], v6 offset:36864\n"
    "ds_read_b64_tr_b16 v[110:111], v6 offset:37888\n"
    "ds_read_b64_tr_b16 v[112:113], v6 offset:32800\n"
    "ds_read_b64_tr_b16 v[114:115], v6 offset:33824\n"
    "ds_read_b64_tr_b16 v[116:117], v6 offset:36896\n"
    "ds_read_b64_tr_b16 v[118:119], v6 offset:37920\n"
    "s_waitcnt vmcnt(4) lgkmcnt(0)\n"
    "s_barrier\n"
    "ds_read_b64_tr_b16 v[120:121], v6 offset:49152\n"
    "ds_read_b64_tr_b16 v[122:123], v6 offset:50176\n"
    "ds_read_b64_tr_b16 v[124:125], v6 offset:53248\n"
    "ds_read_b64_tr_b16 v[126:127], v6 offset:54272\n"
    "ds_read_b64_tr_b16 v[128:129], v6 offset:49184\n"
    "ds_read_b64_tr_b16 v[130:131], v6 offset:50208\n"
    "ds_read_b64_tr_b16 v[132:133], v6 offset:53280\n"
    "ds_read_b64_tr_b16 v[134:135], v6 offset:54304\n"
    "s_waitcnt vmcnt(0) lgkmcnt(0)\n"
    "s_barrier\n"
    "v_add_u32 v31, 0x10000, v6\n"
    "ds_read_b64_tr_b16 v[12:13], v31\n"
    "ds_read_b64_tr_b16 v[14:15], v31 offset:1024\n"
    "ds_read_b64_tr_b16 v[16:17], v31 offset:4096\n"
    "ds_read_b64_tr_b16 v[18:19], v31 offset:5120\n"
    "ds_read_b64_tr_b16 v[20:21], v31 offset:32\n"
    "ds_read_b64_tr_b16 v[22:23], v31 offset:1056\n"
    "ds_read_b64_tr_b16 v[24:25], v31 offset:4128\n"
    "ds_read_b64_tr_b16 v[26:27], v31 offset:5152\n"
    "s_waitcnt lgkmcnt(0)\n"
    "s_barrier\n"
    "s_xor_b32 s73, %2, 0\n"
    "s_lshl_b32 s73, s73, 12\n"
    "v_add_u32 v6, s73, v2\n"
    "ds_write_b128 v6, v[88:91] offset:14336\n"
    "ds_write_b128 v6, v[92:95] offset:15360\n"
    "ds_write_b128 v6, v[96:99] offset:16384\n"
    "ds_write_b128 v6, v[100:103] offset:17408\n"
    "s_xor_b32 s73, %2, 1\n"
    "s_lshl_b32 s73, s73, 12\n"
    "v_add_u32 v6, s73, v2\n"
    "ds_write_b128 v6, v[104:107] offset:14336\n"
    "ds_write_b128 v6, v[108:111] offset:15360\n"
    "ds_write_b128 v6, v[112:115] offset:16384\n"
    "ds_write_b128 v6, v[116:119] offset:17408\n"
    "s_xor_b32 s73, %2, 2\n"
    "s_lshl_b32 s73, s73, 12\n"
    "v_add_u32 v6, s73, v2\n"
    "ds_write_b128 v6, v[120:123] offset:14336\n"
    "ds_write_b128 v6, v[124:127] offset:15360\n"
    "ds_write_b128 v6, v[128:131] offset:16384\n"
    "ds_write_b128 v6, v[132:135] offset:17408\n"
    "s_xor_b32 s73, %2, 3\n"
    "s_lshl_b32 s73, s73, 12\n"
    "v_add_u32 v6, s73, v2\n"
    "ds_write_b128 v6, v[12:15] offset:14336\n"
    "ds_write_b128 v6, v[16:19] offset:15360\n"
    "ds_write_b128 v6, v[20:23] offset:16384\n"
    "ds_write_b128 v6, v[24:27] offset:17408\n"
    "ds_read_b64_tr_b16 v[88:89], v9\n"
    "ds_read_b64_tr_b16 v[90:91], v9 offset:128\n"
    "ds_read_b64_tr_b16 v[92:93], v9 offset:2048\n"
    "ds_read_b64_tr_b16 v[94:95], v9 offset:2176\n"
    "ds_read_b64_tr_b16 v[96:97], v9 offset:4096\n"
    "ds_read_b64_tr_b16 v[98:99], v9 offset:4224\n"
    "ds_read_b64_tr_b16 v[100:101], v9 offset:6144\n"
    "ds_read_b64_tr_b16 v[102:103], v9 offset:6272\n"
    "ds_write_b128 v2, v[32:35]\n"
    "ds_write_b128 v2, v[36:39] offset:1024\n"
    "ds_write_b128 v2, v[40:43] offset:2048\n"
    "ds_write_b128 v2, v[44:47] offset:3072\n"
    "ds_write_b128 v2, v[48:51] offset:4096\n"
    "ds_write_b128 v2, v[52:55] offset:5120\n"
    "ds_write_b128 v2, v[56:59] offset:6144\n"
    "ds_write_b128 v2, v[60:63] offset:7168\n"
    "ds_write_b128 v2, v[64:67] offset:8192\n"
    "ds_write_b128 v2, v[68:71] offset:9216\n"
    "ds_write_b128 v2, v[72:75] offset:10240\n"
    "ds_write_b128 v2, v[76:79] offset:11264\n"
    "ds_write_b128 v2, v[80:83] offset:12288\n"
    "ds_write_b128 v2, v[84:87] offset:13312\n"
    "v_lshrrev_b32 v112, 4, v0\n"
    "v_and_b32 v113, 15, v112\n"
    "v_lshrrev_b32 v114, 4, v112\n"
    "v_mov_b32 v11, 0\n"
    "v_lshrrev_b32 v115, 1, v113\n"
    "v_bfi_b32 v111, 1, v113, v115\n"
    "v_and_b32 v115, 1, v115\n"
    "v_lshl_add_u32 v115, v114, 1, v115\n"
    "v_mul_u32_u24 v117, 1152, v111\n"
    "v_lshl_add_u32 v117, v115, 4, v117\n"
    "v_and_b32 v115, 1, v114\n"
    "v_lshrrev_b32 v116, 1, v114\n"
    "v_mov_b32 v120, 0\n"
    "v_mov_b32 v121, 0\n"
    "v_mov_b32 v122, 0\n"
    "v_mov_b32 v123, 0\n"
    "ds_write_b128 v11, v[120:123] offset:31872\n"
    "v_mov_b32 v124, 0x3f803f80\n"
    "v_mov_b32 v125, 0x3f803f80\n"
    "v_mov_b32 v126, 0x3f803f80\n"
    "v_mov_b32 v127, 0x3f803f80\n"
    "ds_write_b128 v11, v[124:127] offset:31888\n"
    "v_mov_b32 v128, 0x3f003f00\n"
    "v_mov_b32 v129, 0x3f003f00\n"
    "v_mov_b32 v130, 0x3f003f00\n"
    "v_mov_b32 v131, 0x3f003f00\n"
    "ds_write_b128 v11, v[128:131] offset:31904\n"
    "v_mul_u32_u24 v118, 57, v112\n"
    "v_lshrrev_b32 v118, 9, v118\n"
    "v_mul_u32_u24 v118, 9, v118\n"
    "v_cmp_eq_u32 vcc, v118, v112\n"
    "v_lshlrev_b32 v118, 1, v112\n"
    "s_nop 0\n"
    "v_cndmask_b32 v120, v11, v124, vcc\n"
    "ds_write_b16 v118, v120 offset:31936\n"
    "s_waitcnt lgkmcnt(0)\n"
    "s_cmp_lt_u32 %2, 2\n"
    "s_cbranch_scc1 .Lsk_%=\n"
    "ds_write_b128 v2, v[88:91] offset:30720\n"
    "ds_write_b128 v2, v[92:95] offset:31744\n"
    "ds_write_b128 v2, v[96:99] offset:32768\n"
    "ds_write_b128 v2, v[100:103] offset:33792\n"
    ".Lsk_%=:\n"
    "s_cmp_eq_u32 %2, 3\n"
    "s_cbranch_scc1 .Lsm_%=\n"
    "s_mov_b64 s[44:45], %12\n"
    "s_and_b32 s45, s45, 0xffff\n"
    "s_mov_b32 s46, 0x2400\n"
    "s_mov_b32 s47, 0x20000\n"
    "s_mul_i32 s62, %2, 384\n"
    "s_mul_i32 s61, %2, 3072\n"
    "s_add_u32 m0, s61, 20480\n"
    "s_add_u32 s73, s62, 0\n"
    "buffer_load_dwordx4 v117, s[44:47], s73 offen lds\n"
    "s_add_u32 m0, s61, 21504\n"
    "s_add_u32 s73, s62, 128\n"
    "buffer_load_dwordx4 v117, s[44:47], s73 offen lds\n"
    "s_add_u32 m0, s61, 22528\n"
    "s_add_u32 s73, s62, 256\n"
    "buffer_load_dwordx4 v117, s[44:47], s73 offen lds\n"
    ".Lsm_%=:\n"
    "v_mov_b32 v130, 0x20400\n"
    "global_load_dwordx2 v[132:133], v130, %1 sc1\n"
    "v_mbcnt_lo_u32_b32 v131, -1, 0\n"
    "v_min_u32 v131, 7, v131\n"
    "v_lshlrev_b32 v131, 7, v131\n"
    "v_add_u32 v131, 0x20000, v131\n"
    "global_load_dwordx2 v[134:135], v131, %1 sc1\n"
    "s_waitcnt vmcnt(0) lgkmcnt(0)\n"
    "s_barrier\n"
    "v_mov_b32 v1, 0x4444\n"
    "v_mov_b32 v118, 0xeeee\n"
    "v_and_b32 v4, 2, v113\n"
    "v_cmp_eq_u32 vcc, 0, v4\n"
    "s_nop 1\n"
    "v_cndmask_b32 v1, v118, v1, vcc\n"
    "v_xor_b32 v118, 7, v111\n"
    "v_cndmask_b32 v4, v118, v111, vcc\n"
    "s_lshl_b32 s73, %2, 10\n"
    "v_add_u32 v5, s73, v0\n"
    "v_and_b32 v119, 7, v113\n"
    "v_lshrrev_b32 v120, 3, v113\n"
    "v_mul_u32_u24 v3, 544, v114\n"
    "v_lshl_add_u32 v3, v113, 2, v3\n"
    "s_lshl_b32 s73, %2, 6\n"
    "s_add_u32 s73, s73, 20480\n"
    "v_add_u32 v3, s73, v3\n"
    "v_mul_u32_u24 v4, 272, v4\n"
    "v_lshl_add_u32 v4, v114, 4, v4\n"
    "v_and_b32 v6, 13, v113\n"
    "v_lshl_add_u32 v6, v116, 4, v6\n"
    "v_lshl_add_u32 v6, v115, 1, v6\n"
    "s_lshl_b32 s73, %2, 9\n"
    "v_lshl_add_u32 v6, v6, 4, s73\n"
    "s_waitcnt vmcnt(0)\n"
    "v_add_u32 v132, 1, v132\n"
    "v_add_u32 v133, 1, v133\n"
    "s_mov_b32 s56, 1048576\n"
    ".Lcgo_a%=:\n"
    "v_cmp_ne_u64 vcc, v[132:133], v[134:135]\n"
    "s_cbranch_vccz .Lcgo_x%=\n"
    "global_load_dwordx2 v[134:135], v131, %1 sc1\n"
    "s_sub_u32 s56, s56, 1\n"
    "s_waitcnt vmcnt(0)\n"
    "s_cbranch_scc0 .Lcgo_a%=\n"
    ".Lcgo_x%=:\n"
    "s_mov_b32 s56, 0\n"
    "v_mov_b32 v134, 64\n"
    ".Lpoll1_%=:\n"
    "global_load_dword v135, v134, %1 sc1\n"
    "s_waitcnt vmcnt(0)\n"
    "v_readfirstlane_b32 s73, v135\n"
    "s_cmp_ge_u32 s73, 4\n"
    "s_cbranch_scc1 .Lpoll1_x%=\n"
    "s_add_u32 s56, s56, 1\n"
    "s_cmp_lt_u32 s56, 3\n"
    "s_cbranch_scc1 .Lpoll1_%=\n"
    "s_sleep 8\n"
    "s_cmp_lt_u32 s56, s57\n"
    "s_cbranch_scc1 .Lpoll1_%=\n"
    "s_mov_b32 s57, 0\n"
    "v_mov_b32 v135, 1\n"
    "global_store_dword v11, v135, %1 sc1\n"
    ".Lpoll1_x%=:\n"
    "s_xor_b32 s69, s73, %7\n"
    "s_lshr_b32 s51, s73, 1\n"
    "s_xor_b32 s51, s51, %7\n"
    "s_and_b32 s51, s51, 1\n"
    "s_lshl_b32 s51, s51, 13\n"
    "s_add_u32 s68, s51, 0x200000\n"
    "s_and_b32 s60, s69, 1\n"
    "s_lshl_b32 s60, s60, 13\n"
    "v_lshl_add_u32 v8, v116, 2, v115\n"
    "v_lshlrev_b32 v8, 12, v8\n"
    "v_lshl_add_u32 v8, v119, 4, v8\n"
    "v_lshl_add_u32 v7, v120, 15, v8\n"
    "v_lshl_add_u32 v8, v120, 30, v8\n"
    "s_mul_i32 s73, %5, 294912\n"
    "s_lshl_b32 s51, %4, 7\n"
    "s_add_u32 s73, s73, s51\n"
    "s_add_u32 s73, s73, s60\n"
    "s_lshl_b32 s51, %2, 16\n"
    "s_add_u32 s51, s51, s73\n"
    "v_add_u32 v7, s51, v7\n"
    "s_lshl3_add_u32 s51, %2, s73\n"
    "s_add_u32 s51, s51, 262128\n"
    "s_cmp_lt_u32 %2, 2\n"
    "s_cselect_b32 s51, 0x80000000, s51\n"
    "v_add_u32 v8, s51, v8\n"
    "s_lshl_b32 s61, %4, 1\n"
    "s_add_u32 s61, s61, %5\n"
    "s_lshl4_add_u32 s44, s61, 1024\n"
    "s_lshr_b32 s73, s61, 3\n"
    "s_add_u32 s73, s73, 64\n"
    "s_and_b32 s51, s61, 7\n"
    "s_add_u32 s51, s51, %2\n"
    "s_cmp_eq_u32 s51, 1\n"
    "s_cselect_b32 s71, s73, -1\n"
    "s_cmp_eq_u32 %2, 0\n"
    "s_cselect_b32 s71, s61, s71\n"
    "s_max_i32 s61, s71, 0\n"
    "s_lshr_b32 s73, s61, 1\n"
    "s_lshl_b32 s73, s73, 14\n"
    "s_and_b32 s51, s61, 1\n"
    "s_lshl_b32 s51, s51, 12\n"
    "s_add_u32 s65, s73, s51\n"
    "s_add_u32 s65, s65, s60\n"
    "s_lshr_b32 s94, s61, 5\n"
    "s_and_b32 s73, s61, 31\n"
    "s_lshl_b32 s73, s73, 7\n"
    "v_lshl_add_u32 v9, v114, 2, s73\n"
    "v_lshl_add_u32 v9, v119, 4, v9\n"
    "v_lshl_add_u32 v9, v120, 22, v9\n"
    "s_lshl_b32 s73, %2, 11\n"
    "v_add_u32 v10, s73, v0\n"
    "s_cmp_ge_u32 %2, 2\n"
    "s_cbranch_scc0 .Lnw3_%=\n"
    "v_lshl_add_u32 v9, v113, 1, %5\n"
    "v_lshlrev_b32 v118, 3, v114\n"
    "v_sub_u32 v9, v9, v118\n"
    "v_cmp_le_i32 s[76:77], 0, v9\n"
    "v_cmp_le_i32 s[78:79], 2, v9\n"
    "v_cmp_le_i32 s[80:81], 4, v9\n"
    "v_cmp_le_i32 s[82:83], 6, v9\n"
    "v_cmp_le_i32 s[84:85], 1, v9\n"
    "v_cmp_le_i32 s[86:87], 3, v9\n"
    "v_cmp_le_i32 s[88:89], 5, v9\n"
    "v_cmp_le_i32 s[90:91], 7, v9\n"
    ".Lnw3_%=:\n"
    "s_lshr_b32 s73, %4, 2\n"
    "s_lshl1_add_u32 s73, s73, %7\n"
    "s_and_b32 s51, %4, 3\n"
    "s_lshl3_add_u32 s73, s73, s51\n"
    "s_lshl_b32 s73, s73, 8\n"
    "s_add_u32 s92, s73, 86016\n"
    "s_lshr_b32 s51, %4, 3\n"
    "s_lshl2_add_u32 s73, %5, s51\n"
    "s_lshl1_add_u32 s73, s73, %7\n"
    "s_and_b32 s51, %4, 7\n"
    "s_lshl4_add_u32 s73, s73, s51\n"
    "s_lshl_b32 s73, s73, 7\n"
    "s_add_u32 s93, s73, 53248\n"
    "s_lshl_b32 s73, %5, 12\n"
    "s_lshl_b32 s51, %4, 14\n"
    "s_add_u32 s73, s73, s51\n"
    "s_add_u32 s75, s73, 0x9fff000\n"
    "s_brev_b32 s45, 1\n"
    "s_mov_b32 s46, 0x3f803f80\n"
    "s_mov_b32 s47, s46\n"
    "s_mov_b32 s48, 0\n"
    "s_mov_b32 s54, 60\n"
    "s_mov_b32 s55, 84\n"
    "s_mov_b32 s96, 672\n"
    "s_mov_b32 s56, 0\n"
    "v_mov_b32 v134, 128\n"
    ".Lpoll2_%=:\n"
    "global_load_dword v135, v134, %1 sc1\n"
    "s_waitcnt vmcnt(0)\n"
    "v_readfirstlane_b32 s73, v135\n"
    "s_cmp_ge_u32 s73, 64\n"
    "s_cbranch_scc1 .Lpoll2_x%=\n"
    "s_add_u32 s56, s56, 1\n"
    "s_cmp_lt_u32 s56, 3\n"
    "s_cbranch_scc1 .Lpoll2_%=\n"
    "s_sleep 8\n"
    "s_cmp_lt_u32 s56, s57\n"
    "s_cbranch_scc1 .Lpoll2_%=\n"
    "s_mov_b32 s57, 0\n"
    "v_mov_b32 v135, 1\n"
    "global_store_dword v11, v135, %1 sc1\n"
    ".Lpoll2_x%=:\n"
    "ds_read_b128 v[32:35], v2\n"
    "ds_read_b128 v[36:39], v2 offset:1024\n"
    "ds_read_b128 v[40:43], v2 offset:2048\n"
    "ds_read_b128 v[44:47], v2 offset:3072\n"
    "ds_read_b128 v[48:51], v2 offset:4096\n"
    "ds_read_b128 v[52:55], v2 offset:5120\n"
    "ds_read_b128 v[56:59], v2 offset:6144\n"
    "ds_read_b128 v[60:63], v2 offset:7168\n"
    "ds_read_b128 v[64:67], v2 offset:8192\n"
    "ds_read_b128 v[68:71], v2 offset:9216\n"
    "ds_read_b128 v[72:75], v2 offset:10240\n"
    "ds_read_b128 v[76:79], v2 offset:11264\n"
    "ds_read_b128 v[80:83], v2 offset:12288\n"
    "ds_read_b128 v[84:87], v2 offset:13312\n"
    "v_mov_b64 v[124:125], 0\n"
    "v_mov_b64 v[126:127], 0\n"
    "v_mov_b64 v[128:129], 0\n"
    "v_mov_b64 v[130:131], 0\n"
    "s_add_u32 s73, s48, 0\n"
    "s_and_b32 s73, s73, 63\n"
    "s_lshr_b32 s51, s73, 2\n"
    "s_xor_b32 s51, s51, s69\n"
    "s_and_b32 s51, s51, 1\n"
    "s_lshl_b32 s51, s51, 13\n"
    "s_lshl_b32 s73, s73, 5\n"
    "s_add_u32 s73, s73, %4\n"
    "s_lshl_b32 s73, s73, 14\n"
    "s_add_u32 s73, s73, s51\n"
    "s_add_u32 s60, s73, 0x6000000\n"
    "s_add_u32 s61, s73, 0x6001000\n"
    "s_add_u32 s62, s73, 0x8000000\n"
    "s_add_u32 s63, s73, 0x8001000\n"
    "s_lshl_b32 s51, %5, 12\n"
    "s_add_u32 s73, s73, s51\n"
    "s_add_u32 s64, s73, 0x4000000\n"
    "s_mul_i32 s73, s48, 3\n"
    "s_add_u32 s73, s73, 2\n"
    "s_and_b32 s51, s73, 1\n"
    "s_lshl_b32 s51, s51, 12\n"
    "s_lshr_b32 s73, s73, 1\n"
    "s_lshl_b32 s73, s73, 14\n"
    "s_add_u32 s73, s73, s51\n"
    "s_add_u32 s58, s73, s68\n"
    "s_waitcnt lgkmcnt(0)\n"
    "s_mov_b32 s56, 0\n"
    "v_mov_b32 v134, s92\n"
    ".Lpoll3_%=:\n"
    "global_load_dword v132, v134, %1 sc1\n"
    "global_load_dword v133, v134, %1 offset:128 sc1\n"
    "s_waitcnt vmcnt(0)\n"
    "v_and_b32 v133, v132, v133\n"
    "s_nop 0\n"
    "v_readfirstlane_b32 s73, v133\n"
    "s_cmp_ge_u32 s73, 1\n"
    "s_cbranch_scc1 .Lpoll3_x%=\n"
    "s_add_u32 s56, s56, 1\n"
    "s_cmp_lt_u32 s56, 3\n"
    "s_cbranch_scc1 .Lpoll3_%=\n"
    "s_sleep 8\n"
    "s_cmp_lt_u32 s56, s57\n"
    "s_cbranch_scc1 .Lpoll3_%=\n"
    "s_mov_b32 s57, 0\n"
    "v_mov_b32 v135, 1\n"
    "global_store_dword v11, v135, %1 sc1\n"
    ".Lpoll3_x%=:\n"
    "buffer_load_dwordx4 v[12:15], v5, s[40:43], s60 offen sc1\n"
    "buffer_load_dwordx4 v[16:19], v5, s[40:43], s61 offen sc1\n"
    "buffer_load_dwordx4 v[20:23], v5, s[40:43], s62 offen sc1\n"
    "buffer_load_dwordx4 v[24:27], v5, s[40:43], s63 offen sc1\n"
    "buffer_load_dwordx4 v[28:31], v5, s[40:43], s64 offen sc1\n"
    "v_xor_b32 v132, 0x800, v10\n"
    "v_xor_b32 v133, 0x1000, v10\n"
    "v_xor_b32 v134, 0x1800, v10\n"
    "v_lshrrev_b32 v135, 2, v5\n"
    "s_movk_i32 s73, 0x180\n"
    "v_lshlrev_b32 v120, 1, v0\n"
    "v_bfi_b32 v120, s73, v120, v0\n"
    "v_lshrrev_b32 v122, 2, v0\n"
    "v_bfi_b32 v120, 64, v122, v120\n"
    "v_lshlrev_b32 v121, 1, v135\n"
    "v_bfi_b32 v121, s73, v121, v135\n"
    "v_lshrrev_b32 v122, 2, v135\n"
    "v_bfi_b32 v121, 64, v122, v121\n"
    "ds_read_b128 v[88:91], v10 offset:20480\n"
    "ds_read_b128 v[92:95], v10 offset:21504\n"
    "ds_read_b32 v96, v135 offset:28672\n"
    "s_add_u32 s73, s48, 1\n"
    "s_and_b32 s73, s73, 63\n"
    "s_lshr_b32 s51, s73, 2\n"
    "s_xor_b32 s51, s51, s69\n"
    "s_and_b32 s51, s51, 1\n"
    "s_lshl_b32 s51, s51, 13\n"
    "s_lshl_b32 s73, s73, 5\n"
    "s_add_u32 s73, s73, %4\n"
    "s_lshl_b32 s73, s73, 14\n"
    "s_add_u32 s73, s73, s51\n"
    "s_add_u32 s60, s73, 0x6000000\n"
    "s_add_u32 s61, s73, 0x6001000\n"
    "s_add_u32 s62, s73, 0x8000000\n"
    "s_add_u32 s63, s73, 0x8001000\n"
    "s_lshl_b32 s51, %5, 12\n"
    "s_add_u32 s73, s73, s51\n"
    "s_add_u32 s64, s73, 0x4000000\n"
    "s_add_u32 s73, s48, 1\n"
    "s_and_b32 s51, s73, 32\n"
    "s_and_b32 s73, s73, 31\n"
    "s_lshl3_add_u32 s73, s51, s73\n"
    "s_lshl2_add_u32 s73, s73, s92\n"
    "s_waitcnt lgkmcnt(2)\n"
    "v_min_u32 v88, -2, v88\n"
    "v_min_u32 v89, -2, v89\n"
    "v_min_u32 v90, -2, v90\n"
    "v_min_u32 v91, -2, v91\n"
    "s_waitcnt lgkmcnt(1)\n"
    "v_min_u32 v92, -2, v92\n"
    "v_min_u32 v93, -2, v93\n"
    "v_min_u32 v94, -2, v94\n"
    "v_min_u32 v95, -2, v95\n"
    "s_waitcnt lgkmcnt(0)\n"
    "v_min_u32 v96, -2, v96\n"
    "s_waitcnt vmcnt(4)\n"
    "ds_write_b128 v5, v[12:15]\n"
    "s_waitcnt vmcnt(3)\n"
    "ds_write_b128 v5, v[16:19] offset:4096\n"
    "s_waitcnt vmcnt(0)\n"
    "ds_write_b128 v5, v[28:31] offset:16384\n"
    "v_swap_b32 v22, v24\n"
    "v_swap_b32 v23, v25\n"
    "ds_write_b128 v5, v[20:23] offset:8192\n"
    "ds_write_b128 v5, v[24:27] offset:12288\n"
    "v_mov_b32 v14, s73\n"
    "global_load_dword v12, v14, %1 sc1\n"
    "global_load_dword v13, v14, %1 offset:128 sc1\n"
    "global_load_dword v14, v14, %1 sc1\n"
    "v_mov_b64 v[16:17], 0\n"
    "v_mov_b64 v[18:19], 0\n"
    "v_mov_b64 v[20:21], 0\n"
    "v_mov_b64 v[22:23], 0\n"
    "v_mov_b64 v[24:25], 0\n"
    "v_mov_b64 v[26:27], 0\n"
    "v_mov_b64 v[28:29], 0\n"
    "v_mov_b64 v[30:31], 0\n"
    ".Lstep_%=:\n"
    "s_waitcnt vmcnt(5)\n"
    "v_smfmac_f32_16x16x64_bf16 v[16:19], v[88:91], v[32:39], v1\n"
    "ds_write_b128 v10, v[88:91] offset:20480\n"
    "v_smfmac_f32_16x16x64_bf16 v[20:23], v[88:91], v[40:47], v1\n"
    "s_waitcnt vmcnt(4)\n"
    "ds_write_b128 v10, v[92:95] offset:21504\n"
    "v_max3_u32 v100, v88, v89, v90\n"
    "v_smfmac_f32_16x16x64_bf16 v[16:19], v[92:95], v[48:55], v1\n"
    "s_waitcnt vmcnt(3)\n"
    "ds_write_b32 v135, v96 offset:28672\n"
    "v_max3_u32 v100, v100, v91, v92\n"
    "v_smfmac_f32_16x16x64_bf16 v[20:23], v[92:95], v[56:63], v1\n"
    "v_max3_u32 v100, v100, v93, v94\n"
    "v_smfmac_f32_16x16x64_bf16 v[24:27], v[88:91], v[224:231], v1\n"
    "v_max3_u32 v100, v100, v95, v96\n"
    "v_smfmac_f32_16x16x64_bf16 v[28:31], v[88:91], v[232:239], v1\n"
    "v_cmp_eq_u32 vcc, -1, v100\n"
    "v_smfmac_f32_16x16x64_bf16 v[24:27], v[92:95], v[240:247], v1\n"
    "s_cbranch_vccnz .Lgf_%=\n"
    ".Lgx_%=:\n"
    "s_waitcnt lgkmcnt(0)\n"
    "s_barrier\n"
    "s_memrealtime s[52:53]\n"
    "ds_read_b128 v[96:99], v132 offset:20480\n"
    "v_smfmac_f32_16x16x64_bf16 v[28:31], v[92:95], v[248:255], v1\n"
    "ds_read_b128 v[100:103], v132 offset:21504\n"
    "v_smfmac_f32_16x16x64_bf16 v[124:127], v[88:91], a[112:119], v1\n"
    "ds_read_b128 v[104:107], v133 offset:20480\n"
    "v_smfmac_f32_16x16x64_bf16 v[128:131], v[88:91], a[120:127], v1\n"
    "ds_read_b128 v[108:111], v133 offset:21504\n"
    "v_smfmac_f32_16x16x64_bf16 v[124:127], v[92:95], a[128:135], v1\n"
    "ds_read_b128 v[112:115], v134 offset:20480\n"
    "v_smfmac_f32_16x16x64_bf16 v[128:131], v[92:95], a[136:143], v1\n"
    "ds_read_b128 v[116:119], v134 offset:21504\n"
    "s_waitcnt lgkmcnt(5)\n"
    "v_smfmac_f32_16x16x64_bf16 v[16:19], v[96:99], v[64:71], v1\n"
    "ds_read_b128 v[120:123], v0 offset:28672\n"
    "v_smfmac_f32_16x16x64_bf16 v[20:23], v[96:99], v[72:79], v1\n"
    "s_waitcnt lgkmcnt(5)\n"
    "v_smfmac_f32_16x16x64_bf16 v[16:19], v[100:103], v[80:87], v1\n"
    "v_cmp_eq_u32 vcc, s44, v5\n"
    "v_smfmac_f32_16x16x64_bf16 v[20:23], v[100:103], v[136:143], v1\n"
    "v_mov_b32 v79, s45\n"
    "s_waitcnt lgkmcnt(4)\n"
    "v_smfmac_f32_16x16x64_bf16 v[16:19], v[104:107], v[144:151], v1\n"
    "v_cndmask_b32 v79, v79, v0, vcc\n"
    "v_smfmac_f32_16x16x64_bf16 v[20:23], v[104:107], v[152:159], v1\n"
    "s_lshr_b32 s73, s96, 3\n"
    "s_waitcnt lgkmcnt(3)\n"
    "v_smfmac_f32_16x16x64_bf16 v[16:19], v[108:111], v[160:167], v1\n"
    "s_waitcnt lgkmcnt(0)\n"
    "s_add_u32 s73, s73, s52\n"
    "v_and_b32 v78, -2, s73\n"
    "v_smfmac_f32_16x16x64_bf16 v[20:23], v[108:111], v[168:175], v1\n"
    "buffer_store_dword v78, v79, s[40:43], s58 offen offset:3072 sc1\n"
    "s_waitcnt vmcnt(2)\n"
    "v_smfmac_f32_16x16x64_bf16 v[16:19], v[112:115], v[176:183], v1\n"
    "ds_read_b128 v[40:43], v0\n"
    "v_smfmac_f32_16x16x64_bf16 v[20:23], v[112:115], v[184:191], v1\n"
    "ds_read_b128 v[44:47], v0 offset:4096\n"
    "v_and_b32 v12, v12, v13\n"
    "v_smfmac_f32_16x16x64_bf16 v[16:19], v[116:119], v[192:199], v1\n"
    "ds_read_b128 v[48:51], v0 offset:1024\n"
    "v_add_u32 v13, -1, v12\n"
    "v_smfmac_f32_16x16x64_bf16 v[20:23], v[116:119], v[200:207], v1\n"
    "ds_read_b128 v[52:55], v0 offset:5120\n"
    "v_and_or_b32 v15, v13, s45, v5\n"
    "v_smfmac_f32_16x16x64_bf16 v[16:19], v[120:123], v[208:215], v1\n"
    "ds_read_b128 v[56:59], v0 offset:2048\n"
    "v_readfirstlane_b32 s59, v12\n"
    "v_smfmac_f32_16x16x64_bf16 v[20:23], v[120:123], v[216:223], v1\n"
    "ds_read_b128 v[60:63], v0 offset:6144\n"
    "v_smfmac_f32_16x16x64_bf16 v[24:27], v[96:99], a[0:7], v1\n"
    "ds_read_b128 v[64:67], v0 offset:3072\n"
    "v_add_f32 v16, v16, v18\n"
    "v_smfmac_f32_16x16x64_bf16 v[28:31], v[96:99], a[8:15], v1\n"
    "ds_read_b128 v[68:71], v0 offset:7168\n"
    "v_add_f32 v17, v17, v19\n"
    "v_smfmac_f32_16x16x64_bf16 v[24:27], v[100:103], a[16:23], v1\n"
    "ds_read_b128 v[88:91], v2 offset:14336\n"
    "v_add_f32 v20, v20, v22\n"
    "v_smfmac_f32_16x16x64_bf16 v[28:31], v[100:103], a[24:31], v1\n"
    "ds_read_b128 v[92:95], v2 offset:15360\n"
    "v_add_f32 v21, v21, v23\n"
    "v_smfmac_f32_16x16x64_bf16 v[24:27], v[104:107], a[32:39], v1\n"
    "v_cvt_pk_bf16_f32 v76, v16, v20\n"
    "v_smfmac_f32_16x16x64_bf16 v[28:31], v[104:107], a[40:47], v1\n"
    "v_cvt_pk_bf16_f32 v77, v17, v21\n"
    "v_smfmac_f32_16x16x64_bf16 v[24:27], v[108:111], a[48:55], v1\n"
    "ds_write_b32 v3, v76 offset:9216\n"
    "v_smfmac_f32_16x16x64_bf16 v[28:31], v[108:111], a[56:63], v1\n"
    "ds_write_b32 v3, v77 offset:9488\n"
    "v_smfmac_f32_16x16x64_bf16 v[24:27], v[112:115], a[64:71], v1\n"
    "buffer_load_dwordx4 v[16:19], v15, s[40:43], s61 offen sc1\n"
    "v_smfmac_f32_16x16x64_bf16 v[28:31], v[112:115], a[72:79], v1\n"
    "v_smfmac_f32_16x16x64_bf16 v[24:27], v[116:119], a[80:87], v1\n"
    "v_smfmac_f32_16x16x64_bf16 v[28:31], v[116:119], a[88:95], v1\n"
    "s_waitcnt lgkmcnt(0)\n"
    "s_barrier\n"
    "ds_read_b128 v[32:35], v4 offset:29696\n"
    "v_smfmac_f32_16x16x64_bf16 v[24:27], v[120:123], a[96:103], v1\n"
    "ds_read_b128 v[36:39], v4 offset:29760\n"
    "v_smfmac_f32_16x16x64_bf16 v[28:31], v[120:123], a[104:111], v1\n"
    "ds_read_b128 v[132:135], v4 offset:29824\n"
    "v_smfmac_f32_16x16x64_bf16 v[124:127], v[96:99], a[144:151], v1\n"
    "ds_read_b128 v[72:75], v4 offset:29888\n"
    "v_add_f32 v24, v24, v26\n"
    "v_add_f32 v25, v25, v27\n"
    "v_smfmac_f32_16x16x64_bf16 v[128:131], v[96:99], a[152:159], v1\n"
    "buffer_load_dwordx4 v[20:23], v15, s[40:43], s62 offen sc1\n"
    "v_add_f32 v28, v28, v30\n"
    "v_add_f32 v29, v29, v31\n"
    "v_smfmac_f32_16x16x64_bf16 v[124:127], v[100:103], a[160:167], v1\n"
    "v_cvt_pk_bf16_f32 v76, v24, v28\n"
    "v_cvt_pk_bf16_f32 v77, v25, v29\n"
    "s_waitcnt lgkmcnt(3)\n"
    "v_mfma_f32_16x16x32_bf16 v[80:83], v[40:43], v[32:35], 0\n"
    "ds_write2_b32 v3, v76, v77 offset1:68\n"
    "v_mfma_f32_16x16x32_bf16 v[84:87], v[44:47], v[32:35], 0\n"
    "s_add_u32 s73, s48, 1\n"
    "s_and_b32 s51, s73, 32\n"
    "s_and_b32 s73, s73, 31\n"
    "s_waitcnt lgkmcnt(3)\n"
    "v_mfma_f32_16x16x32_bf16 v[80:83], v[48:51], v[36:39], v[80:83]\n"
    "buffer_load_dwordx4 v[24:27], v15, s[40:43], s63 offen sc1\n"
    "v_mfma_f32_16x16x32_bf16 v[84:87], v[52:55], v[36:39], v[84:87]\n"
    "s_lshl3_add_u32 s73, s51, s73\n"
    "s_lshl2_add_u32 s73, s73, s92\n"
    "s_waitcnt lgkmcnt(2)\n"
    "v_mfma_f32_16x16x32_bf16 v[80:83], v[56:59], v[132:135], v[80:83]\n"
    "s_mov_b32 vcc_lo, 0xcccccccc\n"
    "s_mov_b32 vcc_hi, 0xcccccccc\n"
    "v_mfma_f32_16x16x32_bf16 v[84:87], v[60:63], v[132:135], v[84:87]\n"
    "s_waitcnt lgkmcnt(0)\n"
    "s_barrier\n"
    "ds_read_b128 v[40:43], v4 offset:20480\n"
    "v_mfma_f32_16x16x32_bf16 v[80:83], v[64:67], v[72:75], v[80:83]\n"
    "ds_read_b128 v[44:47], v4 offset:20544\n"
    "v_mfma_f32_16x16x32_bf16 v[84:87], v[68:71], v[72:75], v[84:87]\n"
    "ds_read_b128 v[48:51], v4 offset:20608\n"
    "v_smfmac_f32_16x16x64_bf16 v[128:131], v[100:103], a[168:175], v1\n"
    "ds_read_b128 v[52:55], v4 offset:20672\n"
    "v_smfmac_f32_16x16x64_bf16 v[124:127], v[104:107], a[176:183], v1\n"
    "ds_read_b128 v[56:59], v5 offset:8192\n"
    "v_cndmask_b32 v80, v80, v84, vcc\n"
    "v_smfmac_f32_16x16x64_bf16 v[128:131], v[104:107], a[184:191], v1\n"
    "ds_read_b128 v[60:63], v5 offset:12288\n"
    "v_cndmask_b32 v81, v81, v85, vcc\n"
    "s_waitcnt lgkmcnt(5)\n"
    "v_mfma_f32_16x16x32_bf16 v[64:67], v[40:43], v[32:35], 0\n"
    "v_cndmask_b32 v82, v82, v86, vcc\n"
    "v_cndmask_b32 v83, v83, v87, vcc\n"
    "s_waitcnt lgkmcnt(4)\n"
    "v_mfma_f32_16x16x32_bf16 v[64:67], v[44:47], v[36:39], v[64:67]\n"
    "ds_read_b128 v[96:99], v2 offset:16384\n"
    "v_maximum3_f32 v78, v80, v81, v82\n"
    "v_maximum3_f32 v78, v78, v83, v83\n"
    "s_waitcnt lgkmcnt(4)\n"
    "v_mfma_f32_16x16x32_bf16 v[64:67], v[48:51], v[132:135], v[64:67]\n"
    "ds_read_b128 v[100:103], v2 offset:17408\n"
    "v_max_f32_dpp v78, v78, v78 row_mirror row_mask:0xf bank_mask:0xf\n"
    "s_waitcnt lgkmcnt(4)\n"
    "v_mfma_f32_16x16x32_bf16 v[64:67], v[52:55], v[72:75], v[64:67]\n"
    "buffer_load_dwordx4 v[28:31], v15, s[40:43], s64 offen sc1\n"
    "v_mov_b32 v77, v78\n"
    "v_smfmac_f32_16x16x64_bf16 v[124:127], v[108:111], a[192:199], v1\n"
    "ds_read_b128 v[40:43], v2 offset:18432\n"
    "v_permlane32_swap_b32 v77, v78\n"
    "v_smfmac_f32_16x16x64_bf16 v[128:131], v[108:111], a[200:207], v1\n"
    "ds_read_b128 v[44:47], v2 offset:19456\n"
    "v_maximum3_f32 v78, v78, v77, v64\n"
    "v_maximum3_f32 v78, v78, v65, v66\n"
    "v_max_f32 v77, v78, v67\n"
    "v_max_f32 v78, v78, v67\n"
    "v_smfmac_f32_16x16x64_bf16 v[124:127], v[112:115], a[208:215], v1\n"
    "ds_read_b128 v[48:51], v2 offset:22528\n"
    "v_permlane16_swap_b32 v77, v78\n"
    "v_max_f32 v78, v78, v77\n"
    "v_mul_f32 v78, 0xbe0293ee, v78\n"
    "v_smfmac_f32_16x16x64_bf16 v[128:131], v[112:115], a[216:223], v1\n"
    "ds_read_b128 v[52:55], v2 offset:23552\n"
    "v_fmamk_f32 v80, v80, 0x3e0293ee, v78\n"
    "v_fmamk_f32 v81, v81, 0x3e0293ee, v78\n"
    "v_exp_f32 v80, v80\n"
    "v_exp_f32 v81, v81\n"
    "v_smfmac_f32_16x16x64_bf16 v[124:127], v[116:119], a[224:231], v1\n"
    "ds_read_b128 v[32:35], v2 offset:24576\n"
    "v_fmamk_f32 v82, v82, 0x3e0293ee, v78\n"
    "v_fmamk_f32 v83, v83, 0x3e0293ee, v78\n"
    "v_exp_f32 v82, v82\n"
    "v_exp_f32 v83, v83\n"
    "v_cvt_pk_bf16_f32 v80, v80, v81\n"
    "v_smfmac_f32_16x16x64_bf16 v[128:131], v[116:119], a[232:239], v1\n"
    "ds_read_b128 v[36:39], v2 offset:25600\n"
    "v_fmamk_f32 v64, v64, 0x3e0293ee, v78\n"
    "v_fmamk_f32 v65, v65, 0x3e0293ee, v78\n"
    "v_exp_f32 v64, v64\n"
    "v_exp_f32 v65, v65\n"
    "v_cvt_pk_bf16_f32 v81, v82, v83\n"
    "v_smfmac_f32_16x16x64_bf16 v[124:127], v[120:123], a[240:247], v1\n"
    "ds_read_b128 v[104:107], v2 offset:26624\n"
    "v_cvt_pk_bf16_f32 v66, v64, v65\n"
    "v_mov_b32_dpp v82, v80 row_mirror row_mask:0xf bank_mask:0xf\n"
    "v_smfmac_f32_16x16x64_bf16 v[128:131], v[120:123], a[248:255], v1\n"
    "ds_read_b128 v[108:111], v2 offset:27648\n"
    "v_mov_b32_dpp v83, v81 row_mirror row_mask:0xf bank_mask:0xf\n"
    "v_cvt_pk_bf16_f32 v67, v64, v65\n"
    "s_waitcnt lgkmcnt(11)\n"
    "v_mfma_f32_16x16x32_bf16 v[132:135], v[56:59], v[80:83], 0\n"
    "ds_read_b128 v[112:115], v2 offset:20480\n"
    "v_cvt_pk_bf16_f32 v124, v124, v125\n"
    "v_cvt_pk_bf16_f32 v125, v126, v127\n"
    "s_waitcnt lgkmcnt(11)\n"
    "v_mfma_f32_16x16x32_bf16 v[120:123], v[60:63], v[80:83], 0\n"
    "ds_read_b128 v[116:119], v2 offset:21504\n"
    "v_cvt_pk_bf16_f32 v126, v128, v129\n"
    "v_cvt_pk_bf16_f32 v127, v130, v131\n"
    "v_mfma_f32_16x16x16_bf16 v[132:135], v[124:125], v[66:67], v[132:135]\n"
    "buffer_load_dwordx4 v[12:15], v15, s[40:43], s60 offen sc1\n"
    "ds_read_b128 v[68:71], v2 offset:28672\n"
    "s_nop 0\n"
    "v_mfma_f32_16x16x16_bf16 v[120:123], v[126:127], v[66:67], v[120:123]\n"
    "ds_read_b128 v[72:75], v2 offset:29696\n"
    "buffer_load_dword v79, v0, s[40:43], s58 offen offset:3072 sc1\n"
    "v_mov_b64 v[56:57], s[46:47]\n"
    "v_mov_b64 v[58:59], s[46:47]\n"
    "v_xor_b32 v76, 0x200, v6\n"
    "v_cvt_pk_bf16_f32 v84, v132, v133\n"
    "v_cvt_pk_bf16_f32 v85, v134, v135\n"
    "v_xor_b32 v77, 0x400, v6\n"
    "s_not_b64 exec, vcc\n"
    "v_cvt_pk_bf16_f32 v86, v120, v121\n"
    "v_cvt_pk_bf16_f32 v87, v122, v123\n"
    "ds_write_b128 v6, v[84:87] offset:26880\n"
    "s_mov_b64 exec, -1\n"
    "s_cmp_eq_u32 s48, 63\n"
    "v_mfma_f32_16x16x32_bf16 v[128:131], v[80:83], v[56:59], 0\n"
    "v_mov_b32 v67, 0\n"
    "v_mov_b64 v[132:133], 0\n"
    "v_mov_b64 v[134:135], 0\n"
    "v_mov_b32_dpp v84, v84 quad_perm:[0,1,0,1] row_mask:0xf bank_mask:0xf\n"
    "v_mov_b32_dpp v85, v85 quad_perm:[0,1,0,1] row_mask:0xf bank_mask:0xf\n"
    "v_mov_b32_dpp v86, v86 quad_perm:[0,1,0,1] row_mask:0xf bank_mask:0xf\n"
    "v_mov_b32_dpp v87, v87 quad_perm:[0,1,0,1] row_mask:0xf bank_mask:0xf\n"
    "v_mfma_f32_16x16x16_bf16 v[128:131], v[66:67], v[56:57], v[128:131]\n"
    "v_mov_b64 v[80:81], 0\n"
    "v_mov_b64 v[82:83], 0\n"
    "v_smfmac_f32_16x16x64_bf16 v[132:135], v[84:87], v[88:95], v1\n"
    "v_xor_b32 v78, 0x600, v6\n"
    "s_waitcnt lgkmcnt(13)\n"
    "v_smfmac_f32_16x16x64_bf16 v[80:83], v[84:87], v[96:103], v1\n"
    "s_waitcnt lgkmcnt(0)\n"
    "s_barrier\n"
    "s_cbranch_scc1 .Lend_%=\n"
    "ds_read_b128 v[56:59], v76 offset:26880\n"
    "ds_read_b128 v[60:63], v77 offset:26880\n"
    "ds_read_b128 v[120:123], v78 offset:26880\n"
    "ds_read_b128 v[88:91], v2 offset:30720\n"
    "v_rcp_f32 v128, v128\n"
    "v_rcp_f32 v129, v129\n"
    "s_waitcnt lgkmcnt(3)\n"
    "v_smfmac_f32_16x16x64_bf16 v[132:135], v[56:59], v[40:47], v1\n"
    "ds_read_b128 v[40:43], v0 offset:26880\n"
    "v_mov_b32 v78, s73\n"
    "s_waitcnt lgkmcnt(3)\n"
    "v_smfmac_f32_16x16x64_bf16 v[132:135], v[60:63], v[48:55], v1\n"
    "ds_read_b128 v[92:95], v2 offset:31744\n"
    "s_waitcnt lgkmcnt(3)\n"
    "v_smfmac_f32_16x16x64_bf16 v[132:135], v[120:123], v[104:111], v1\n"
    "ds_read_b128 v[96:99], v2 offset:32768\n"
    "v_mov_b64 v[64:65], 0\n"
    "v_smfmac_f32_16x16x64_bf16 v[80:83], v[56:59], v[112:119], v1\n"
    "ds_read_b128 v[100:103], v2 offset:33792\n"
    "v_mov_b64 v[66:67], 0\n"
    "v_smfmac_f32_16x16x64_bf16 v[80:83], v[60:63], v[32:39], v1\n"
    "ds_read_b128 v[44:47], v0 offset:27904\n"
    "v_mul_f32 v132, v132, v128\n"
    "v_mul_f32 v133, v133, v129\n"
    "v_smfmac_f32_16x16x64_bf16 v[80:83], v[120:123], v[68:75], v1\n"
    "global_load_dword v76, v78, %1 sc1\n"
    "v_mul_f32 v134, v134, v128\n"
    "v_mul_f32 v135, v135, v129\n"
    "s_waitcnt lgkmcnt(3)\n"
    "v_smfmac_f32_16x16x64_bf16 v[64:67], v[40:43], v[88:95], v1\n"
    "global_load_dword v77, v78, %1 offset:128 sc1\n"
    "v_cvt_pk_bf16_f32 v132, v132, v133\n"
    "v_cvt_pk_bf16_f32 v133, v134, v135\n"
    "v_bfi_b32 v132, 1, s48, v132\n"
    "s_waitcnt lgkmcnt(0)\n"
    "v_smfmac_f32_16x16x64_bf16 v[64:67], v[44:47], v[96:103], v1\n"
    "v_pk_mul_f32 v[80:81], v[80:81], v[128:129]\n"
    "v_pk_mul_f32 v[82:83], v[82:83], v[128:129]\n"
    "v_cvt_pk_bf16_f32 v134, v80, v81\n"
    "v_cvt_pk_bf16_f32 v135, v82, v83\n"
    "v_bfi_b32 v134, 1, s48, v134\n"
    "buffer_store_dwordx4 v[132:135], v7, s[40:43], 0 offen sc1\n"
    "s_nop 2\n"
    "v_pk_add_f32 v[64:65], v[64:65], v[66:67]\n"
    "v_pk_mul_f32 v[64:65], v[64:65], v[128:129]\n"
    "v_cvt_pk_bf16_f32 v66, v64, v65\n"
    "v_bfi_b32 v64, 1, s48, v66\n"
    "s_nop 0\n"
    "v_mov_b32_dpp v65, v66 row_ror:8 row_mask:0xf bank_mask:0xf\n"
    "buffer_store_dwordx2 v[64:65], v8, s[40:43], 0 offen sc1\n"
    "s_memrealtime s[38:39]\n"
    "ds_write_b128 v5, v[124:127] offset:22784\n"
    "s_cmp_ge_u32 %2, 2\n"
    "s_cbranch_scc1 .Lw3_%=\n"
    "v_and_b32 v104, 0x70, v0\n"
    "ds_read_b128 v[112:115], v104 offset:31936\n"
    "s_movk_i32 s73, 0x60\n"
    "s_movk_i32 s51, 0x80\n"
    "v_lshrrev_b32 v104, 1, v0\n"
    "v_bfi_b32 v104, s73, v104, v0\n"
    "v_lshlrev_b32 v105, 2, v0\n"
    "v_bfi_b32 v104, s51, v105, v104\n"
    "s_waitcnt lgkmcnt(0)\n"
    "s_barrier\n"
    "s_waitcnt vmcnt(4)\n"
    "s_mov_b32 s56, 0\n"
    ".Lr4_a%=:\n"
    "v_subrev_u32 v78, s52, v79\n"
    "v_cmp_eq_u32 vcc, -1, v79\n"
    "s_cbranch_vccnz .Lr4_%=\n"
    ".Lr4_x%=:\n"
    "v_max_i32_dpp v78, v78, v78 row_ror:8 row_mask:0xf bank_mask:0xf\n"
    "s_nop 1\n"
    "v_max_i32_dpp v78, v78, v78 row_ror:4 row_mask:0xf bank_mask:0xf\n"
    "s_nop 1\n"
    "v_max_i32_dpp v78, v78, v78 row_ror:2 row_mask:0xf bank_mask:0xf\n"
    "s_nop 1\n"
    "v_max_i32_dpp v78, v78, v78 row_ror:1 row_mask:0xf bank_mask:0xf\n"
    "s_nop 0\n"
    "v_readlane_b32 s95, v78, 0\n"
    "v_readlane_b32 s73, v78, 16\n"
    "v_readlane_b32 s51, v78, 32\n"
    "v_readlane_b32 s53, v78, 48\n"
    "s_max_i32 s95, s95, s73\n"
    "s_max_i32 s51, s51, s53\n"
    "s_max_i32 s95, s95, s51\n"
    "s_min_i32 s95, s95, 1024\n"
    "s_add_u32 s95, s95, s52\n"
    "s_add_u32 s95, s95, s55\n"
    "s_cmp_eq_u32 %2, 0\n"
    "s_cselect_b64 exec, 1, 0\n"
    "v_and_b32 v106, -2, s95\n"
    "ds_write_b32 v11, v106 offset:31876\n"
    "s_mov_b64 exec, -1\n"
    "v_xor_b32 v132, 0x800, v10\n"
    "v_xor_b32 v133, 0x1000, v10\n"
    "v_xor_b32 v134, 0x1800, v10\n"
    "v_lshrrev_b32 v135, 2, v5\n"
    "s_movk_i32 s73, 0x180\n"
    "v_lshlrev_b32 v120, 1, v0\n"
    "v_bfi_b32 v120, s73, v120, v0\n"
    "v_lshrrev_b32 v122, 2, v0\n"
    "v_bfi_b32 v120, 64, v122, v120\n"
    "v_lshlrev_b32 v121, 1, v135\n"
    "v_bfi_b32 v121, s73, v121, v135\n"
    "v_lshrrev_b32 v122, 2, v135\n"
    "v_bfi_b32 v121, 64, v122, v121\n"
    "s_mul_i32 s73, s48, 3\n"
    "s_add_u32 s73, s73, s94\n"
    "s_and_b32 s51, s73, 1\n"
    "s_lshl_b32 s51, s51, 12\n"
    "s_lshr_b32 s73, s73, 1\n"
    "s_lshl_b32 s73, s73, 14\n"
    "s_add_u32 s73, s73, s51\n"
    "s_add_u32 s66, s73, s68\n"
    "s_lshr_b32 s67, %2, 1\n"
    "s_mul_i32 s73, s48, 3\n"
    "s_add_u32 s73, s73, s67\n"
    "s_and_b32 s51, s73, 1\n"
    "s_lshl_b32 s51, s51, 12\n"
    "s_lshr_b32 s73, s73, 1\n"
    "s_lshl_b32 s73, s73, 14\n"
    "s_add_u32 s73, s73, s51\n"
    "s_add_u32 s67, s73, s68\n"
    "s_and_b32 s73, %2, 1\n"
    "s_lshl_b32 s73, s73, 11\n"
    "s_add_u32 s67, s67, s73\n"
    "s_mov_b32 s72, s58\n"
    "s_mul_i32 s73, s48, 3\n"
    "s_add_u32 s73, s73, 5\n"
    "s_and_b32 s51, s73, 1\n"
    "s_lshl_b32 s51, s51, 12\n"
    "s_lshr_b32 s73, s73, 1\n"
    "s_lshl_b32 s73, s73, 14\n"
    "s_add_u32 s73, s73, s51\n"
    "s_add_u32 s58, s73, s68\n"
    "s_cmp_lt_i32 s71, 0\n"
    "s_cbranch_scc1 .Lnored_%=\n"
    "s_mov_b32 s56, 0\n"
    "s_sub_u32 s53, s95, s54\n"
    "s_waitcnt lgkmcnt(0)\n"
    ".Lgl5_%=:\n"
    "s_memrealtime s[50:51]\n"
    "s_waitcnt lgkmcnt(0)\n"
    "s_sub_u32 s50, s50, s53\n"
    "s_cmp_lt_i32 s50, 0\n"
    "s_cbranch_scc1 .Lgl5_%=\n"
    "buffer_load_dwordx4 v[88:91], v104, s[40:43], s65 offen sc1\n"
    "buffer_load_dwordx4 v[92:95], v104, s[40:43], s65 offen offset:1024 sc1\n"
    "buffer_load_dwordx4 v[96:99], v104, s[40:43], s65 offen offset:2048 sc1\n"
    "buffer_load_dwordx4 v[100:103], v104, s[40:43], s65 offen offset:3072 sc1\n"
    "s_waitcnt vmcnt(6)\n"
    "v_and_b32 v77, v76, v77\n"
    "s_nop 0\n"
    "v_readfirstlane_b32 s70, v77\n"
    "ds_read_b128 v[32:35], v2\n"
    "ds_read_b128 v[36:39], v2 offset:1024\n"
    "ds_read_b128 v[40:43], v2 offset:2048\n"
    "ds_read_b128 v[44:47], v2 offset:3072\n"
    "ds_read_b128 v[48:51], v2 offset:4096\n"
    "ds_read_b128 v[52:55], v2 offset:5120\n"
    "ds_read_b128 v[56:59], v2 offset:6144\n"
    "ds_read_b128 v[60:63], v2 offset:7168\n"
    "ds_read_b128 v[64:67], v2 offset:8192\n"
    "ds_read_b128 v[68:71], v2 offset:9216\n"
    "ds_read_b128 v[72:75], v2 offset:10240\n"
    "ds_read_b128 v[76:79], v2 offset:11264\n"
    "ds_read_b128 v[80:83], v2 offset:12288\n"
    "ds_read_b128 v[84:87], v2 offset:13312\n"
    "v_mov_b64 v[124:125], 0\n"
    "v_mov_b64 v[126:127], 0\n"
    "v_mov_b64 v[128:129], 0\n"
    "v_mov_b64 v[130:131], 0\n"
    "s_sub_u32 s73, s38, s52\n"
    "s_lshl_b32 s73, s73, 3\n"
    "s_sub_u32 s73, s73, s96\n"
    "s_ashr_i32 s73, s73, 2\n"
    "s_max_i32 s73, s73, -16\n"
    "s_min_i32 s73, s73, 16\n"
    "s_add_u32 s96, s96, s73\n"
    "s_waitcnt lgkmcnt(0)\n"
    ".Lred_%=:\n"
    "s_waitcnt vmcnt(3)\n"
    "v_mfma_f32_16x16x32_bf16 v[108:111], v[88:91], v[112:115], 0\n"
    "v_bitop3_b32 v116, v88, v90, s48 bitop3:0x7e\n"
    "s_waitcnt vmcnt(2)\n"
    "v_mfma_f32_16x16x32_bf16 v[108:111], v[92:95], v[112:115], v[108:111]\n"
    "v_bitop3_b32 v117, v92, v94, s48 bitop3:0x7e\n"
    "s_waitcnt vmcnt(1)\n"
    "v_mfma_f32_16x16x32_bf16 v[108:111], v[96:99], v[112:115], v[108:111]\n"
    "v_bitop3_b32 v118, v96, v98, s48 bitop3:0x7e\n"
    "v_or3_b32 v116, v116, v117, v118\n"
    "s_waitcnt vmcnt(0)\n"
    "v_mfma_f32_16x16x32_bf16 v[108:111], v[100:103], v[112:115], v[108:111]\n"
    "v_bitop3_b32 v119, v100, v102, s48 bitop3:0x7e\n"
    "v_or_b32 v116, v116, v119\n"
    "v_and_b32 v116, 1, v116\n"
    "v_cmp_ne_u32 vcc, 0, v116\n"
    "s_cbranch_vccnz .Lrf_%=\n"
    ".Lrx_%=:\n"
    "s_nop 3\n"
    "v_pk_add_f32 v[108:109], v[108:109], v[110:111]\n"
    "v_cvt_pk_bf16_f32 v108, v108, v109\n"
    "v_min_u32 v108, -2, v108\n"
    "buffer_store_dword v108, v9, s[40:43], s66 offen sc1\n"
    ".Lpub_%=:\n"
    "s_cmp_eq_u32 s59, 0\n"
    "s_cbranch_scc1 .Lr6_%=\n"
    ".Lr6_x%=:\n"
    "s_add_u32 s73, s48, 2\n"
    "s_and_b32 s73, s73, 63\n"
    "s_lshr_b32 s51, s73, 2\n"
    "s_xor_b32 s51, s51, s69\n"
    "s_and_b32 s51, s51, 1\n"
    "s_lshl_b32 s51, s51, 13\n"
    "s_lshl_b32 s73, s73, 5\n"
    "s_add_u32 s73, s73, %4\n"
    "s_lshl_b32 s73, s73, 14\n"
    "s_add_u32 s73, s73, s51\n"
    "s_add_u32 s60, s73, 0x6000000\n"
    "s_add_u32 s61, s73, 0x6001000\n"
    "s_add_u32 s62, s73, 0x8000000\n"
    "s_add_u32 s63, s73, 0x8001000\n"
    "s_lshl_b32 s51, %5, 12\n"
    "s_add_u32 s73, s73, s51\n"
    "s_add_u32 s64, s73, 0x4000000\n"
    "s_add_u32 s73, s48, 2\n"
    "s_and_b32 s51, s73, 32\n"
    "s_and_b32 s73, s73, 31\n"
    "s_lshl3_add_u32 s73, s51, s73\n"
    "s_lshl2_add_u32 s73, s73, s92\n"
    "v_mov_b32 v101, s73\n"
    ".Lg_%=:\n"
    "s_waitcnt lgkmcnt(0)\n"
    "s_barrier\n"
    "ds_read_b32 v97, v11 offset:31876\n"
    "s_mov_b32 s56, 0\n"
    "s_waitcnt lgkmcnt(0)\n"
    "v_readfirstlane_b32 s95, v97\n"
    "s_and_b32 s73, s95, 1\n"
    "s_lshl_b32 s73, s73, 22\n"
    "s_or_b32 s67, s67, s73\n"
    "s_or_b32 s72, s72, s73\n"
    "s_sub_u32 s53, s95, 0\n"
    "s_waitcnt lgkmcnt(0)\n"
    ".Lgl7_%=:\n"
    "s_memrealtime s[50:51]\n"
    "s_waitcnt lgkmcnt(0)\n"
    "s_sub_u32 s50, s50, s53\n"
    "s_cmp_lt_i32 s50, 0\n"
    "s_cbranch_scc1 .Lgl7_%=\n"
    "buffer_load_dwordx4 v[88:91], v120, s[40:43], s67 offen sc1\n"
    "buffer_load_dwordx4 v[92:95], v120, s[40:43], s67 offen offset:1024 sc1\n"
    "buffer_load_dword v96, v121, s[40:43], s72 offen sc1\n"
    "ds_write_b128 v5, v[12:15]\n"
    "ds_write_b128 v5, v[16:19] offset:4096\n"
    "ds_write_b128 v5, v[28:31] offset:16384\n"
    "v_swap_b32 v22, v24\n"
    "v_swap_b32 v23, v25\n"
    "ds_write_b128 v5, v[20:23] offset:8192\n"
    "ds_write_b128 v5, v[24:27] offset:12288\n"
    "v_mov_b64 v[16:17], 0\n"
    "v_mov_b64 v[18:19], 0\n"
    "v_mov_b64 v[20:21], 0\n"
    "v_mov_b64 v[22:23], 0\n"
    "v_mov_b64 v[24:25], 0\n"
    "v_mov_b64 v[26:27], 0\n"
    "v_mov_b64 v[28:29], 0\n"
    "v_mov_b64 v[30:31], 0\n"
    "v_mov_b32 v14, v101\n"
    "global_load_dword v12, v14, %1 sc1\n"
    "global_load_dword v13, v14, %1 offset:128 sc1\n"
    "s_cmp_ge_u32 %2, 2\n"
    "s_cbranch_scc1 .Lcob_%=\n"
    ".Lcod_%=:\n"
    "global_load_dword v14, v14, %1 sc1\n"
    ".Lcox_%=:\n"
    "s_add_u32 s48, s48, 1\n"
    "s_branch .Lstep_%=\n"
    ".Lcob_%=:\n"
    "s_add_u32 s73, s48, 0\n"
    "s_and_b32 s51, s73, 32\n"
    "s_and_b32 s73, s73, 31\n"
    "s_lshl3_add_u32 s73, s51, s73\n"
    "s_lshl2_add_u32 s73, s73, s93\n"
    "v_mov_b32 v100, s73\n"
    "v_mov_b32 v101, 1\n"
    "s_waitcnt vmcnt(5)\n"
    "s_mov_b64 exec, 1\n"
    "ds_add_rtn_u32 v101, v11, v101 offset:31872\n"
    "s_mov_b64 exec, -1\n"
    "s_waitcnt lgkmcnt(0)\n"
    "v_readfirstlane_b32 s73, v101\n"
    "v_mov_b32 v101, 1\n"
    "s_bitcmp1_b32 s73, 0\n"
    "s_cbranch_scc0 .Lcod_%=\n"
    "s_mov_b64 exec, 1\n"
    "global_store_dword v100, v101, %1 sc1\n"
    "s_mov_b64 exec, -1\n"
    "s_branch .Lcox_%=\n"
    ".Lgf_%=:\n"
    "s_bitset1_b32 s67, 22\n"
    "s_bitset1_b32 s72, 22\n"
    "s_waitcnt lgkmcnt(0)\n"
    "buffer_load_dwordx4 v[88:91], v120, s[40:43], s67 offen sc1\n"
    "buffer_load_dwordx4 v[92:95], v120, s[40:43], s67 offen offset:1024 sc1\n"
    "buffer_load_dword v96, v121, s[40:43], s72 offen sc1\n"
    "s_waitcnt vmcnt(0)\n"
    "v_mov_b64 v[16:17], 0\n"
    "v_mov_b64 v[18:19], 0\n"
    "v_mov_b64 v[20:21], 0\n"
    "v_mov_b64 v[22:23], 0\n"
    "v_mov_b64 v[24:25], 0\n"
    "v_mov_b64 v[26:27], 0\n"
    "v_mov_b64 v[28:29], 0\n"
    "v_mov_b64 v[30:31], 0\n"
    "v_mov_b64 v[124:125], 0\n"
    "v_mov_b64 v[126:127], 0\n"
    "v_mov_b64 v[128:129], 0\n"
    "v_mov_b64 v[130:131], 0\n"
    "s_add_u32 s56, s56, 1\n"
    "s_cmp_lt_u32 s56, s57\n"
    "s_cbranch_scc1 .Lstep_%=\n"
    "s_mov_b32 s57, 0\n"
    "v_mov_b32 v135, 1\n"
    "global_store_dword v11, v135, %1 sc1\n"
    "s_branch .Lgx_%=\n"
    ".Lrf_%=:\n"
    "s_memrealtime s[50:51]\n"
    "s_waitcnt lgkmcnt(0)\n"
    "s_add_u32 s95, s50, 62\n"
    "v_or_b32 v106, 1, s95\n"
    "ds_write_b32 v11, v106 offset:31876\n"
    "buffer_load_dwordx4 v[88:91], v104, s[40:43], s65 offen sc1\n"
    "buffer_load_dwordx4 v[92:95], v104, s[40:43], s65 offen offset:1024 sc1\n"
    "buffer_load_dwordx4 v[96:99], v104, s[40:43], s65 offen offset:2048 sc1\n"
    "buffer_load_dwordx4 v[100:103], v104, s[40:43], s65 offen offset:3072 sc1\n"
    "s_add_u32 s56, s56, 1\n"
    "s_cmp_lt_u32 s56, s57\n"
    "s_cbranch_scc1 .Lred_%=\n"
    "s_mov_b32 s57, 0\n"
    "v_mov_b32 v135, 1\n"
    "global_store_dword v11, v135, %1 sc1\n"
    "s_branch .Lrx_%=\n"
    ".Lnored_%=:\n"
    "s_waitcnt vmcnt(0)\n"
    "v_and_b32 v77, v76, v77\n"
    "s_nop 0\n"
    "v_readfirstlane_b32 s70, v77\n"
    "ds_read_b128 v[32:35], v2\n"
    "ds_read_b128 v[36:39], v2 offset:1024\n"
    "ds_read_b128 v[40:43], v2 offset:2048\n"
    "ds_read_b128 v[44:47], v2 offset:3072\n"
    "ds_read_b128 v[48:51], v2 offset:4096\n"
    "ds_read_b128 v[52:55], v2 offset:5120\n"
    "ds_read_b128 v[56:59], v2 offset:6144\n"
    "ds_read_b128 v[60:63], v2 offset:7168\n"
    "ds_read_b128 v[64:67], v2 offset:8192\n"
    "ds_read_b128 v[68:71], v2 offset:9216\n"
    "ds_read_b128 v[72:75], v2 offset:10240\n"
    "ds_read_b128 v[76:79], v2 offset:11264\n"
    "ds_read_b128 v[80:83], v2 offset:12288\n"
    "ds_read_b128 v[84:87], v2 offset:13312\n"
    "v_mov_b64 v[124:125], 0\n"
    "v_mov_b64 v[126:127], 0\n"
    "v_mov_b64 v[128:129], 0\n"
    "v_mov_b64 v[130:131], 0\n"
    "s_sub_u32 s73, s38, s52\n"
    "s_lshl_b32 s73, s73, 3\n"
    "s_sub_u32 s73, s73, s96\n"
    "s_ashr_i32 s73, s73, 2\n"
    "s_max_i32 s73, s73, -16\n"
    "s_min_i32 s73, s73, 16\n"
    "s_add_u32 s96, s96, s73\n"
    "s_waitcnt lgkmcnt(0)\n"
    "s_branch .Lpub_%=\n"
    ".Lw3_%=:\n"
    "ds_read_b128 v[32:35], v0 offset:16384\n"
    "ds_read_b128 v[48:51], v0\n"
    "ds_read_b128 v[88:91], v0 offset:4096\n"
    "ds_read_b128 v[104:107], v4 offset:20480\n"
    "ds_read_b128 v[36:39], v0 offset:17408\n"
    "ds_read_b128 v[52:55], v0 offset:1024\n"
    "ds_read_b128 v[92:95], v0 offset:5120\n"
    "ds_read_b128 v[108:111], v4 offset:20544\n"
    "ds_read_b128 v[40:43], v0 offset:18432\n"
    "ds_read_b128 v[56:59], v0 offset:2048\n"
    "ds_read_b128 v[96:99], v0 offset:6144\n"
    "ds_read_b128 v[112:115], v4 offset:20608\n"
    "s_waitcnt lgkmcnt(10)\n"
    "v_mfma_f32_16x16x32_bf16 v[120:123], v[48:51], v[32:35], 0\n"
    "s_barrier\n"
    "ds_read_b128 v[44:47], v0 offset:19456\n"
    "ds_read_b128 v[60:63], v0 offset:3072\n"
    "ds_read_b128 v[100:103], v0 offset:7168\n"
    "ds_read_b128 v[116:119], v4 offset:20672\n"
    "s_waitcnt lgkmcnt(13)\n"
    "v_mfma_f32_16x16x32_bf16 v[124:127], v[88:91], v[32:35], 0\n"
    "s_waitcnt lgkmcnt(12)\n"
    "v_mfma_f32_16x16x32_bf16 v[128:131], v[104:107], v[32:35], 0\n"
    "s_waitcnt lgkmcnt(10)\n"
    "v_mfma_f32_16x16x32_bf16 v[120:123], v[52:55], v[36:39], v[120:123]\n"
    "s_waitcnt lgkmcnt(9)\n"
    "v_mfma_f32_16x16x32_bf16 v[124:127], v[92:95], v[36:39], v[124:127]\n"
    "ds_read_b128 v[64:67], v10 offset:4096\n"
    "s_waitcnt lgkmcnt(9)\n"
    "v_mfma_f32_16x16x32_bf16 v[128:131], v[108:111], v[36:39], v[128:131]\n"
    "ds_read_b128 v[68:71], v10 offset:8192\n"
    "s_waitcnt lgkmcnt(8)\n"
    "v_mfma_f32_16x16x32_bf16 v[120:123], v[56:59], v[40:43], v[120:123]\n"
    "ds_read_b128 v[72:75], v10 offset:5120\n"
    "s_waitcnt lgkmcnt(8)\n"
    "v_mfma_f32_16x16x32_bf16 v[124:127], v[96:99], v[40:43], v[124:127]\n"
    "ds_read_b128 v[80:83], v10 offset:9216\n"
    "s_waitcnt lgkmcnt(8)\n"
    "v_mfma_f32_16x16x32_bf16 v[128:131], v[112:115], v[40:43], v[128:131]\n"
    "ds_read_b128 v[84:87], v11 offset:31888\n"
    "s_waitcnt lgkmcnt(7)\n"
    "v_mfma_f32_16x16x32_bf16 v[120:123], v[60:63], v[44:47], v[120:123]\n"
    "ds_read_b64 v[134:135], v11 offset:31904\n"
    "s_waitcnt lgkmcnt(7)\n"
    "v_mfma_f32_16x16x32_bf16 v[124:127], v[100:103], v[44:47], v[124:127]\n"
    "ds_read_b128 v[88:91], v10 offset:18688\n"
    "s_waitcnt lgkmcnt(7)\n"
    "v_mfma_f32_16x16x32_bf16 v[128:131], v[116:119], v[44:47], v[128:131]\n"
    "ds_read_b128 v[92:95], v10 offset:19712\n"
    "v_mov_b32 v132, 0xd01502f9\n"
    "s_cmp_eq_u32 s59, 0\n"
    "s_cbranch_scc1 .Lr8_%=\n"
    ".Lr8_x%=:\n"
    "ds_read_b128 v[32:35], v2\n"
    "ds_read_b128 v[36:39], v2 offset:1024\n"
    "ds_read_b128 v[40:43], v2 offset:2048\n"
    "ds_read_b128 v[44:47], v2 offset:3072\n"
    "v_cndmask_b32 v120, v132, v120, s[76:77]\n"
    "v_cndmask_b32 v121, v132, v121, s[78:79]\n"
    "v_cndmask_b32 v122, v132, v122, s[80:81]\n"
    "v_cndmask_b32 v123, v132, v123, s[82:83]\n"
    "v_cndmask_b32 v124, v132, v124, s[84:85]\n"
    "v_cndmask_b32 v125, v132, v125, s[86:87]\n"
    "v_cndmask_b32 v126, v132, v126, s[88:89]\n"
    "v_cndmask_b32 v127, v132, v127, s[90:91]\n"
    "v_maximum3_f32 v133, v128, v129, v120\n"
    "v_maximum3_f32 v133, v133, v121, v122\n"
    "v_maximum3_f32 v133, v133, v123, v124\n"
    "v_maximum3_f32 v133, v133, v125, v126\n"
    "v_maximum3_f32 v133, v133, v127, v127\n"
    "v_mov_b32 v132, v133\n"
    "ds_read_b128 v[48:51], v2 offset:4096\n"
    "ds_read_b128 v[52:55], v2 offset:5120\n"
    "v_permlane16_swap_b32 v132, v133\n"
    "v_maximum3_f32 v133, v133, v132, v132\n"
    "v_mov_b32 v132, v133\n"
    "ds_read_b128 v[56:59], v2 offset:6144\n"
    "ds_read_b128 v[60:63], v2 offset:7168\n"
    "v_permlane32_swap_b32 v132, v133\n"
    "v_maximum3_f32 v133, v133, v132, v132\n"
    "v_mul_f32 v133, 0xbe0293ee, v133\n"
    "v_fmamk_f32 v120, v120, 0x3e0293ee, v133\n"
    "v_fmamk_f32 v121, v121, 0x3e0293ee, v133\n"
    "v_fmamk_f32 v122, v122, 0x3e0293ee, v133\n"
    "v_fmamk_f32 v123, v123, 0x3e0293ee, v133\n"
    "v_fmamk_f32 v124, v124, 0x3e0293ee, v133\n"
    "v_fmamk_f32 v125, v125, 0x3e0293ee, v133\n"
    "v_fmamk_f32 v126, v126, 0x3e0293ee, v133\n"
    "v_fmamk_f32 v127, v127, 0x3e0293ee, v133\n"
    "v_fmamk_f32 v128, v128, 0x3e0293ee, v133\n"
    "v_fmamk_f32 v129, v129, 0x3e0293ee, v133\n"
    "v_exp_f32 v120, v120\n"
    "v_exp_f32 v121, v121\n"
    "v_exp_f32 v122, v122\n"
    "v_exp_f32 v123, v123\n"
    "v_exp_f32 v124, v124\n"
    "v_exp_f32 v125, v125\n"
    "v_exp_f32 v126, v126\n"
    "v_exp_f32 v127, v127\n"
    "v_exp_f32 v128, v128\n"
    "v_exp_f32 v129, v129\n"
    "v_cvt_pk_bf16_f32 v120, v120, v121\n"
    "v_cvt_pk_bf16_f32 v121, v122, v123\n"
    "v_cvt_pk_bf16_f32 v122, v124, v125\n"
    "v_cvt_pk_bf16_f32 v123, v126, v127\n"
    "v_cvt_pk_bf16_f32 v124, v128, v129\n"
    "v_cvt_pk_bf16_f32 v125, v128, v129\n"
    "s_waitcnt lgkmcnt(11)\n"
    "v_mfma_f32_16x16x32_bf16 v[100:103], v[84:87], v[120:123], 0\n"
    "v_mfma_f32_16x16x32_bf16 v[104:107], v[64:67], v[120:123], 0\n"
    "v_mfma_f32_16x16x32_bf16 v[108:111], v[68:71], v[120:123], 0\n"
    "s_waitcnt lgkmcnt(10)\n"
    "v_mfma_f32_16x16x16_bf16 v[100:103], v[134:135], v[124:125], v[100:103]\n"
    "v_mfma_f32_16x16x32_bf16 v[112:115], v[72:75], v[120:123], 0\n"
    "v_mfma_f32_16x16x32_bf16 v[116:119], v[80:83], v[120:123], 0\n"
    "s_waitcnt lgkmcnt(9)\n"
    "v_mfma_f32_16x16x16_bf16 v[104:107], v[88:89], v[124:125], v[104:107]\n"
    "ds_read_b128 v[64:67], v2 offset:8192\n"
    "v_mfma_f32_16x16x16_bf16 v[108:111], v[90:91], v[124:125], v[108:111]\n"
    "ds_read_b128 v[68:71], v2 offset:9216\n"
    "v_rcp_f32 v132, v100\n"
    "s_waitcnt lgkmcnt(10)\n"
    "v_mfma_f32_16x16x16_bf16 v[112:115], v[92:93], v[124:125], v[112:115]\n"
    "ds_read_b128 v[80:83], v2 offset:12288\n"
    "v_mfma_f32_16x16x16_bf16 v[116:119], v[94:95], v[124:125], v[116:119]\n"
    "ds_read_b128 v[84:87], v2 offset:13312\n"
    "v_pk_mul_f32 v[104:105], v[104:105], v[132:133] op_sel_hi:[1,0]\n"
    "v_pk_mul_f32 v[106:107], v[106:107], v[132:133] op_sel_hi:[1,0]\n"
    "v_pk_mul_f32 v[108:109], v[108:109], v[132:133] op_sel_hi:[1,0]\n"
    "v_pk_mul_f32 v[110:111], v[110:111], v[132:133] op_sel_hi:[1,0]\n"
    "v_cvt_pk_bf16_f32 v104, v104, v108\n"
    "v_cvt_pk_bf16_f32 v105, v105, v109\n"
    "v_cvt_pk_bf16_f32 v106, v106, v110\n"
    "v_cvt_pk_bf16_f32 v107, v107, v111\n"
    "v_pk_mul_f32 v[112:113], v[112:113], v[132:133] op_sel_hi:[1,0]\n"
    "v_pk_mul_f32 v[114:115], v[114:115], v[132:133] op_sel_hi:[1,0]\n"
    "v_pk_mul_f32 v[116:117], v[116:117], v[132:133] op_sel_hi:[1,0]\n"
    "v_pk_mul_f32 v[118:119], v[118:119], v[132:133] op_sel_hi:[1,0]\n"
    "v_cvt_pk_bf16_f32 v112, v112, v116\n"
    "v_cvt_pk_bf16_f32 v113, v113, v117\n"
    "v_cvt_pk_bf16_f32 v114, v114, v118\n"
    "v_cvt_pk_bf16_f32 v115, v115, v119\n"
    "s_lshr_b32 s51, s48, 2\n"
    "s_xor_b32 s51, s51, s69\n"
    "s_and_b32 s51, s51, 1\n"
    "s_lshl_b32 s51, s51, 13\n"
    "s_lshl_b32 s73, s48, 19\n"
    "s_add_u32 s73, s73, s75\n"
    "s_add_u32 s73, s73, s51\n"
    "buffer_store_dwordx4 v[104:107], v10, s[40:43], s73 offen sc1\n"
    "buffer_store_dwordx4 v[112:115], v10, s[40:43], s73 offen offset:1024 sc1\n"
    "s_waitcnt vmcnt(2)\n"
    "ds_read_b128 v[72:75], v2 offset:10240\n"
    "ds_read_b128 v[76:79], v2 offset:11264\n"
    "v_mov_b64 v[124:125], 0\n"
    "v_mov_b64 v[126:127], 0\n"
    "v_mov_b64 v[128:129], 0\n"
    "v_mov_b64 v[130:131], 0\n"
    "v_xor_b32 v132, 0x800, v10\n"
    "v_xor_b32 v133, 0x1000, v10\n"
    "v_xor_b32 v134, 0x1800, v10\n"
    "v_lshrrev_b32 v135, 2, v5\n"
    "s_movk_i32 s73, 0x180\n"
    "v_lshlrev_b32 v120, 1, v0\n"
    "v_bfi_b32 v120, s73, v120, v0\n"
    "v_lshrrev_b32 v122, 2, v0\n"
    "v_bfi_b32 v120, 64, v122, v120\n"
    "v_lshlrev_b32 v121, 1, v135\n"
    "v_bfi_b32 v121, s73, v121, v135\n"
    "v_lshrrev_b32 v122, 2, v135\n"
    "v_bfi_b32 v121, 64, v122, v121\n"
    "s_lshr_b32 s67, %2, 1\n"
    "s_mul_i32 s73, s48, 3\n"
    "s_add_u32 s73, s73, s67\n"
    "s_and_b32 s51, s73, 1\n"
    "s_lshl_b32 s51, s51, 12\n"
    "s_lshr_b32 s73, s73, 1\n"
    "s_lshl_b32 s73, s73, 14\n"
    "s_add_u32 s73, s73, s51\n"
    "s_add_u32 s67, s73, s68\n"
    "s_and_b32 s73, %2, 1\n"
    "s_lshl_b32 s73, s73, 11\n"
    "s_add_u32 s67, s67, s73\n"
    "s_mov_b32 s72, s58\n"
    "s_mul_i32 s73, s48, 3\n"
    "s_add_u32 s73, s73, 5\n"
    "s_and_b32 s51, s73, 1\n"
    "s_lshl_b32 s51, s51, 12\n"
    "s_lshr_b32 s73, s73, 1\n"
    "s_lshl_b32 s73, s73, 14\n"
    "s_add_u32 s73, s73, s51\n"
    "s_add_u32 s58, s73, s68\n"
    "s_cmp_eq_u32 s59, 0\n"
    "s_cbranch_scc1 .Lr9_%=\n"
    ".Lr9_x%=:\n"
    "s_add_u32 s73, s48, 2\n"
    "s_and_b32 s73, s73, 63\n"
    "s_lshr_b32 s51, s73, 2\n"
    "s_xor_b32 s51, s51, s69\n"
    "s_and_b32 s51, s51, 1\n"
    "s_lshl_b32 s51, s51, 13\n"
    "s_lshl_b32 s73, s73, 5\n"
    "s_add_u32 s73, s73, %4\n"
    "s_lshl_b32 s73, s73, 14\n"
    "s_add_u32 s73, s73, s51\n"
    "s_add_u32 s60, s73, 0x6000000\n"
    "s_add_u32 s61, s73, 0x6001000\n"
    "s_add_u32 s62, s73, 0x8000000\n"
    "s_add_u32 s63, s73, 0x8001000\n"
    "s_lshl_b32 s51, %5, 12\n"
    "s_add_u32 s73, s73, s51\n"
    "s_add_u32 s64, s73, 0x4000000\n"
    "s_add_u32 s73, s48, 2\n"
    "s_and_b32 s51, s73, 32\n"
    "s_and_b32 s73, s73, 31\n"
    "s_lshl3_add_u32 s73, s51, s73\n"
    "s_lshl2_add_u32 s73, s73, s92\n"
    "v_mov_b32 v101, s73\n"
    "s_branch .Lg_%=\n"
    ".Lr4_%=:\n"
    "s_sleep 2\n"
    "buffer_load_dword v79, v0, s[40:43], s58 offen offset:3072 sc1\n"
    "s_waitcnt vmcnt(0)\n"
    "s_add_u32 s56, s56, 1\n"
    "s_cmp_lt_u32 s56, s57\n"
    "s_cbranch_scc1 .Lr4_a%=\n"
    "s_mov_b32 s57, 0\n"
    "v_mov_b32 v135, 1\n"
    "global_store_dword v11, v135, %1 sc1\n"
    "s_branch .Lr4_x%=\n"
    ".Lr6_%=:\n"
    "s_cmp_lg_u32 s70, 0\n"
    "s_cbranch_scc1 .Lr6_y%=\n"
    "s_waitcnt lgkmcnt(0)\n"
    "s_add_u32 s73, s48, 1\n"
    "s_and_b32 s51, s73, 32\n"
    "s_and_b32 s73, s73, 31\n"
    "s_lshl3_add_u32 s73, s51, s73\n"
    "s_lshl2_add_u32 s73, s73, s92\n"
    "s_mov_b32 s53, s73\n"
    "s_mov_b32 s56, 0\n"
    "v_mov_b32 v134, s53\n"
    ".Lpoll10_%=:\n"
    "global_load_dword v132, v134, %1 sc1\n"
    "global_load_dword v133, v134, %1 offset:128 sc1\n"
    "s_waitcnt vmcnt(0)\n"
    "v_and_b32 v133, v132, v133\n"
    "s_nop 0\n"
    "v_readfirstlane_b32 s73, v133\n"
    "s_cmp_ge_u32 s73, 1\n"
    "s_cbranch_scc1 .Lpoll10_x%=\n"
    "s_add_u32 s56, s56, 1\n"
    "s_cmp_lt_u32 s56, 3\n"
    "s_cbranch_scc1 .Lpoll10_%=\n"
    "s_sleep 8\n"
    "s_cmp_lt_u32 s56, s57\n"
    "s_cbranch_scc1 .Lpoll10_%=\n"
    "s_mov_b32 s57, 0\n"
    "v_mov_b32 v135, 1\n"
    "global_store_dword v11, v135, %1 sc1\n"
    ".Lpoll10_x%=:\n"
    ".Lr6_y%=:\n"
    "buffer_load_dwordx4 v[12:15], v5, s[40:43], s60 offen sc1\n"
    "buffer_load_dwordx4 v[16:19], v5, s[40:43], s61 offen sc1\n"
    "buffer_load_dwordx4 v[20:23], v5, s[40:43], s62 offen sc1\n"
    "buffer_load_dwordx4 v[24:27], v5, s[40:43], s63 offen sc1\n"
    "buffer_load_dwordx4 v[28:31], v5, s[40:43], s64 offen sc1\n"
    "s_waitcnt vmcnt(0)\n"
    "v_xor_b32 v132, 0x800, v10\n"
    "v_xor_b32 v133, 0x1000, v10\n"
    "v_xor_b32 v134, 0x1800, v10\n"
    "v_lshrrev_b32 v135, 2, v5\n"
    "s_movk_i32 s73, 0x180\n"
    "v_lshlrev_b32 v120, 1, v0\n"
    "v_bfi_b32 v120, s73, v120, v0\n"
    "v_lshrrev_b32 v122, 2, v0\n"
    "v_bfi_b32 v120, 64, v122, v120\n"
    "v_lshlrev_b32 v121, 1, v135\n"
    "v_bfi_b32 v121, s73, v121, v135\n"
    "v_lshrrev_b32 v122, 2, v135\n"
    "v_bfi_b32 v121, 64, v122, v121\n"
    "s_branch .Lr6_x%=\n"
    ".Lr8_%=:\n"
    "s_waitcnt vmcnt(2)\n"
    "v_and_b32 v77, v76, v77\n"
    "s_nop 0\n"
    "v_readfirstlane_b32 s59, v77\n"
    "s_cmp_eq_u32 s59, 0\n"
    "s_cbranch_scc1 .Lr8_x%=\n"
    "buffer_load_dwordx4 v[12:15], v5, s[40:43], s60 offen sc1\n"
    "buffer_load_dwordx4 v[16:19], v5, s[40:43], s61 offen sc1\n"
    "buffer_load_dwordx4 v[20:23], v5, s[40:43], s62 offen sc1\n"
    "buffer_load_dwordx4 v[24:27], v5, s[40:43], s63 offen sc1\n"
    "buffer_load_dwordx4 v[28:31], v5, s[40:43], s64 offen sc1\n"
    "s_branch .Lr8_x%=\n"
    ".Lr9_%=:\n"
    "s_waitcnt lgkmcnt(0)\n"
    "s_add_u32 s73, s48, 1\n"
    "s_and_b32 s51, s73, 32\n"
    "s_and_b32 s73, s73, 31\n"
    "s_lshl3_add_u32 s73, s51, s73\n"
    "s_lshl2_add_u32 s73, s73, s92\n"
    "s_mov_b32 s53, s73\n"
    "s_mov_b32 s56, 0\n"
    "v_mov_b32 v134, s53\n"
    ".Lpoll11_%=:\n"
    "global_load_dword v132, v134, %1 sc1\n"
    "global_load_dword v133, v134, %1 offset:128 sc1\n"
    "s_waitcnt vmcnt(0)\n"
    "v_and_b32 v133, v132, v133\n"
    "s_nop 0\n"
    "v_readfirstlane_b32 s73, v133\n"
    "s_cmp_ge_u32 s73, 1\n"
    "s_cbranch_scc1 .Lpoll11_x%=\n"
    "s_add_u32 s56, s56, 1\n"
    "s_cmp_lt_u32 s56, 3\n"
    "s_cbranch_scc1 .Lpoll11_%=\n"
    "s_sleep 8\n"
    "s_cmp_lt_u32 s56, s57\n"
    "s_cbranch_scc1 .Lpoll11_%=\n"
    "s_mov_b32 s57, 0\n"
    "v_mov_b32 v135, 1\n"
    "global_store_dword v11, v135, %1 sc1\n"
    ".Lpoll11_x%=:\n"
    ".Lr9_y%=:\n"
    "buffer_load_dwordx4 v[12:15], v5, s[40:43], s60 offen sc1\n"
    "buffer_load_dwordx4 v[16:19], v5, s[40:43], s61 offen sc1\n"
    "buffer_load_dwordx4 v[20:23], v5, s[40:43], s62 offen sc1\n"
    "buffer_load_dwordx4 v[24:27], v5, s[40:43], s63 offen sc1\n"
    "buffer_load_dwordx4 v[28:31], v5, s[40:43], s64 offen sc1\n"
    "s_waitcnt vmcnt(0)\n"
    "v_xor_b32 v132, 0x800, v10\n"
    "v_xor_b32 v133, 0x1000, v10\n"
    "v_xor_b32 v134, 0x1800, v10\n"
    "v_lshrrev_b32 v135, 2, v5\n"
    "s_movk_i32 s73, 0x180\n"
    "v_lshlrev_b32 v120, 1, v0\n"
    "v_bfi_b32 v120, s73, v120, v0\n"
    "v_lshrrev_b32 v122, 2, v0\n"
    "v_bfi_b32 v120, 64, v122, v120\n"
    "v_lshlrev_b32 v121, 1, v135\n"
    "v_bfi_b32 v121, s73, v121, v135\n"
    "v_lshrrev_b32 v122, 2, v135\n"
    "v_bfi_b32 v121, 64, v122, v121\n"
    "s_branch .Lr9_x%=\n"
    ".Lend_%=:\n"
    "ds_write_b128 v5, v[124:127] offset:22784\n"
    "s_waitcnt vmcnt(0)\n"
    "s_cmp_ge_u32 %2, 2\n"
    "s_cbranch_scc1 .Lla_%=\n"
    "s_waitcnt lgkmcnt(0)\n"
    "s_barrier\n"
    "s_branch .Lout_%=\n"
    ".Lla_%=:\n"
    "ds_read_b128 v[32:35], v0 offset:16384\n"
    "ds_read_b128 v[48:51], v0\n"
    "ds_read_b128 v[88:91], v0 offset:4096\n"
    "ds_read_b128 v[104:107], v4 offset:20480\n"
    "ds_read_b128 v[36:39], v0 offset:17408\n"
    "ds_read_b128 v[52:55], v0 offset:1024\n"
    "ds_read_b128 v[92:95], v0 offset:5120\n"
    "ds_read_b128 v[108:111], v4 offset:20544\n"
    "ds_read_b128 v[40:43], v0 offset:18432\n"
    "ds_read_b128 v[56:59], v0 offset:2048\n"
    "ds_read_b128 v[96:99], v0 offset:6144\n"
    "ds_read_b128 v[112:115], v4 offset:20608\n"
    "s_waitcnt lgkmcnt(10)\n"
    "v_mfma_f32_16x16x32_bf16 v[120:123], v[48:51], v[32:35], 0\n"
    "s_barrier\n"
    "ds_read_b128 v[44:47], v0 offset:19456\n"
    "ds_read_b128 v[60:63], v0 offset:3072\n"
    "ds_read_b128 v[100:103], v0 offset:7168\n"
    "ds_read_b128 v[116:119], v4 offset:20672\n"
    "s_waitcnt lgkmcnt(13)\n"
    "v_mfma_f32_16x16x32_bf16 v[124:127], v[88:91], v[32:35], 0\n"
    "s_waitcnt lgkmcnt(12)\n"
    "v_mfma_f32_16x16x32_bf16 v[128:131], v[104:107], v[32:35], 0\n"
    "s_waitcnt lgkmcnt(10)\n"
    "v_mfma_f32_16x16x32_bf16 v[120:123], v[52:55], v[36:39], v[120:123]\n"
    "s_waitcnt lgkmcnt(9)\n"
    "v_mfma_f32_16x16x32_bf16 v[124:127], v[92:95], v[36:39], v[124:127]\n"
    "ds_read_b128 v[64:67], v10 offset:4096\n"
    "s_waitcnt lgkmcnt(9)\n"
    "v_mfma_f32_16x16x32_bf16 v[128:131], v[108:111], v[36:39], v[128:131]\n"
    "ds_read_b128 v[68:71], v10 offset:8192\n"
    "s_waitcnt lgkmcnt(8)\n"
    "v_mfma_f32_16x16x32_bf16 v[120:123], v[56:59], v[40:43], v[120:123]\n"
    "ds_read_b128 v[72:75], v10 offset:5120\n"
    "s_waitcnt lgkmcnt(8)\n"
    "v_mfma_f32_16x16x32_bf16 v[124:127], v[96:99], v[40:43], v[124:127]\n"
    "ds_read_b128 v[80:83], v10 offset:9216\n"
    "s_waitcnt lgkmcnt(8)\n"
    "v_mfma_f32_16x16x32_bf16 v[128:131], v[112:115], v[40:43], v[128:131]\n"
    "ds_read_b128 v[84:87], v11 offset:31888\n"
    "s_waitcnt lgkmcnt(7)\n"
    "v_mfma_f32_16x16x32_bf16 v[120:123], v[60:63], v[44:47], v[120:123]\n"
    "ds_read_b64 v[134:135], v11 offset:31904\n"
    "s_waitcnt lgkmcnt(7)\n"
    "v_mfma_f32_16x16x32_bf16 v[124:127], v[100:103], v[44:47], v[124:127]\n"
    "ds_read_b128 v[88:91], v10 offset:18688\n"
    "s_waitcnt lgkmcnt(7)\n"
    "v_mfma_f32_16x16x32_bf16 v[128:131], v[116:119], v[44:47], v[128:131]\n"
    "ds_read_b128 v[92:95], v10 offset:19712\n"
    "v_mov_b32 v132, 0xd01502f9\n"
    "v_cndmask_b32 v120, v132, v120, s[76:77]\n"
    "v_cndmask_b32 v121, v132, v121, s[78:79]\n"
    "v_cndmask_b32 v122, v132, v122, s[80:81]\n"
    "v_cndmask_b32 v123, v132, v123, s[82:83]\n"
    "v_cndmask_b32 v124, v132, v124, s[84:85]\n"
    "v_cndmask_b32 v125, v132, v125, s[86:87]\n"
    "v_cndmask_b32 v126, v132, v126, s[88:89]\n"
    "v_cndmask_b32 v127, v132, v127, s[90:91]\n"
    "v_maximum3_f32 v133, v128, v129, v120\n"
    "v_maximum3_f32 v133, v133, v121, v122\n"
    "v_maximum3_f32 v133, v133, v123, v124\n"
    "v_maximum3_f32 v133, v133, v125, v126\n"
    "v_maximum3_f32 v133, v133, v127, v127\n"
    "v_mov_b32 v132, v133\n"
    "s_nop 1\n"
    "v_permlane16_swap_b32 v132, v133\n"
    "v_maximum3_f32 v133, v133, v132, v132\n"
    "v_mov_b32 v132, v133\n"
    "s_nop 1\n"
    "v_permlane32_swap_b32 v132, v133\n"
    "v_maximum3_f32 v133, v133, v132, v132\n"
    "v_mul_f32 v133, 0xbe0293ee, v133\n"
    "v_fmamk_f32 v120, v120, 0x3e0293ee, v133\n"
    "v_fmamk_f32 v121, v121, 0x3e0293ee, v133\n"
    "v_fmamk_f32 v122, v122, 0x3e0293ee, v133\n"
    "v_fmamk_f32 v123, v123, 0x3e0293ee, v133\n"
    "v_fmamk_f32 v124, v124, 0x3e0293ee, v133\n"
    "v_fmamk_f32 v125, v125, 0x3e0293ee, v133\n"
    "v_fmamk_f32 v126, v126, 0x3e0293ee, v133\n"
    "v_fmamk_f32 v127, v127, 0x3e0293ee, v133\n"
    "v_fmamk_f32 v128, v128, 0x3e0293ee, v133\n"
    "v_fmamk_f32 v129, v129, 0x3e0293ee, v133\n"
    "v_exp_f32 v120, v120\n"
    "v_exp_f32 v121, v121\n"
    "v_exp_f32 v122, v122\n"
    "v_exp_f32 v123, v123\n"
    "v_exp_f32 v124, v124\n"
    "v_exp_f32 v125, v125\n"
    "v_exp_f32 v126, v126\n"
    "v_exp_f32 v127, v127\n"
    "v_exp_f32 v128, v128\n"
    "v_exp_f32 v129, v129\n"
    "v_cvt_pk_bf16_f32 v120, v120, v121\n"
    "v_cvt_pk_bf16_f32 v121, v122, v123\n"
    "v_cvt_pk_bf16_f32 v122, v124, v125\n"
    "v_cvt_pk_bf16_f32 v123, v126, v127\n"
    "v_cvt_pk_bf16_f32 v124, v128, v129\n"
    "v_cvt_pk_bf16_f32 v125, v128, v129\n"
    "s_waitcnt lgkmcnt(3)\n"
    "v_mfma_f32_16x16x32_bf16 v[100:103], v[84:87], v[120:123], 0\n"
    "v_mfma_f32_16x16x32_bf16 v[104:107], v[64:67], v[120:123], 0\n"
    "v_mfma_f32_16x16x32_bf16 v[108:111], v[68:71], v[120:123], 0\n"
    "s_waitcnt lgkmcnt(2)\n"
    "v_mfma_f32_16x16x16_bf16 v[100:103], v[134:135], v[124:125], v[100:103]\n"
    "v_mfma_f32_16x16x32_bf16 v[112:115], v[72:75], v[120:123], 0\n"
    "v_mfma_f32_16x16x32_bf16 v[116:119], v[80:83], v[120:123], 0\n"
    "s_waitcnt lgkmcnt(1)\n"
    "v_mfma_f32_16x16x16_bf16 v[104:107], v[88:89], v[124:125], v[104:107]\n"
    "v_mfma_f32_16x16x16_bf16 v[108:111], v[90:91], v[124:125], v[108:111]\n"
    "v_rcp_f32 v132, v100\n"
    "s_waitcnt lgkmcnt(0)\n"
    "v_mfma_f32_16x16x16_bf16 v[112:115], v[92:93], v[124:125], v[112:115]\n"
    "v_mfma_f32_16x16x16_bf16 v[116:119], v[94:95], v[124:125], v[116:119]\n"
    "s_nop 1\n"
    "v_pk_mul_f32 v[104:105], v[104:105], v[132:133] op_sel_hi:[1,0]\n"
    "v_pk_mul_f32 v[106:107], v[106:107], v[132:133] op_sel_hi:[1,0]\n"
    "v_pk_mul_f32 v[108:109], v[108:109], v[132:133] op_sel_hi:[1,0]\n"
    "v_pk_mul_f32 v[110:111], v[110:111], v[132:133] op_sel_hi:[1,0]\n"
    "v_cvt_pk_bf16_f32 v104, v104, v108\n"
    "v_cvt_pk_bf16_f32 v105, v105, v109\n"
    "v_cvt_pk_bf16_f32 v106, v106, v110\n"
    "v_cvt_pk_bf16_f32 v107, v107, v111\n"
    "v_pk_mul_f32 v[112:113], v[112:113], v[132:133] op_sel_hi:[1,0]\n"
    "v_pk_mul_f32 v[114:115], v[114:115], v[132:133] op_sel_hi:[1,0]\n"
    "v_pk_mul_f32 v[116:117], v[116:117], v[132:133] op_sel_hi:[1,0]\n"
    "v_pk_mul_f32 v[118:119], v[118:119], v[132:133] op_sel_hi:[1,0]\n"
    "v_cvt_pk_bf16_f32 v112, v112, v116\n"
    "v_cvt_pk_bf16_f32 v113, v113, v117\n"
    "v_cvt_pk_bf16_f32 v114, v114, v118\n"
    "v_cvt_pk_bf16_f32 v115, v115, v119\n"
    "s_lshr_b32 s51, s48, 2\n"
    "s_xor_b32 s51, s51, s69\n"
    "s_and_b32 s51, s51, 1\n"
    "s_lshl_b32 s51, s51, 13\n"
    "s_lshl_b32 s73, s48, 19\n"
    "s_add_u32 s73, s73, s75\n"
    "s_add_u32 s73, s73, s51\n"
    "buffer_store_dwordx4 v[104:107], v10, s[40:43], s73 offen sc1\n"
    "buffer_store_dwordx4 v[112:115], v10, s[40:43], s73 offen offset:1024 sc1\n"
    "s_add_u32 s73, s48, 0\n"
    "s_and_b32 s51, s73, 32\n"
    "s_and_b32 s73, s73, 31\n"
    "s_lshl3_add_u32 s73, s51, s73\n"
    "s_lshl2_add_u32 s73, s73, s93\n"
    "v_mov_b32 v100, s73\n"
    "v_mov_b32 v101, 1\n"
    "s_waitcnt vmcnt(0)\n"
    "s_mov_b64 exec, 1\n"
    "ds_add_rtn_u32 v101, v11, v101 offset:31872\n"
    "s_mov_b64 exec, -1\n"
    "s_waitcnt lgkmcnt(0)\n"
    "v_readfirstlane_b32 s73, v101\n"
    "v_mov_b32 v101, 1\n"
    "s_bitcmp1_b32 s73, 0\n"
    "s_cbranch_scc0 .Lout_%=\n"
    "s_mov_b64 exec, 1\n"
    "global_store_dword v100, v101, %1 sc1\n"
    "s_mov_b64 exec, -1\n"
    "s_cmp_eq_u32 %3, 128\n"
    "s_cbranch_scc0 .Lout_%=\n"
    "v_mov_b32 v130, 0x20400\n"
    "global_load_dwordx2 v[132:133], v130, %1 sc1\n"
    "v_mbcnt_lo_u32_b32 v131, -1, 0\n"
    "v_min_u32 v131, 7, v131\n"
    "v_lshlrev_b32 v131, 7, v131\n"
    "v_add_u32 v131, 0x20000, v131\n"
    "global_load_dwordx2 v[134:135], v131, %1 sc1\n"
    "s_waitcnt vmcnt(0)\n"
    "v_add_u32 v132, 1, v132\n"
    "v_add_u32 v133, 1, v133\n"
    "global_store_dwordx2 v130, v[132:133], %1 sc1\n"
    ".Lout_%=:\n"
    "s_waitcnt vmcnt(0)\n"
    "s_endpgm\n"
    ".p2align 6\n"
    ".Lrole1_%=:\n"
    "s_mov_b64 s[40:41], %0\n"
    "s_and_b32 s41, s41, 0xffff\n"
    "s_mov_b32 s42, 0xc000000\n"
    "s_mov_b32 s43, 0x20000\n"
    "s_mov_b64 s[92:93], %0\n"
    "s_and_b32 s93, s93, 0xffff\n"
    "s_mov_b32 s94, 0xc000000\n"
    "s_mov_b32 s95, 0x20000\n"
    "s_mov_b32 s53, 131072\n"
    "s_mov_b64 s[56:57], 0\n"
    "v_mbcnt_lo_u32_b32 v7, -1, 0\n"
    "v_mbcnt_hi_u32_b32 v7, -1, v7\n"
    "s_lshl_b32 s50, %2, 8\n"
    "s_add_u32 s50, s50, 147456\n"
    "v_lshl_add_u32 v7, v7, 2, s50\n"
    "ds_read_b32 v1, v7\n"
    "ds_read_b32 v2, v7 offset:1024\n"
    "ds_read_b32 v3, v7 offset:2048\n"
    "ds_read_b32 v5, v7 offset:3072\n"
    "ds_read_b32 v48, v7 offset:4096\n"
    "ds_read_b32 v49, v7 offset:5120\n"
    "ds_read_b32 v50, v7 offset:6144\n"
    "s_waitcnt lgkmcnt(0)\n"
    "s_barrier\n"
    "s_movk_i32 s86, 255\n"
    "s_lshl_b32 s54, %2, 10\n"
    "s_lshl_b32 s58, %2, 13\n"
    "s_add_u32 s58, s58, 65536\n"
    "s_add_u32 s88, s40, 32768\n"
    "s_mov_b32 s89, s41\n"
    "s_cmp_lt_u32 %2, 2\n"
    "s_cselect_b32 s96, s42, 0\n"
    "s_mov_b32 s91, s43\n"
    "s_mul_i32 s59, %2, 576\n"
    "s_add_u32 s59, s59, 68608\n"
    "s_bfm_b64 s[60:61], 36, 0\n"
    "v_mov_b32 v72, 0x3f803f80\n"
    "v_mov_b32 v73, 0x3f803f80\n"
    "v_mov_b32 v74, 0x3f803f80\n"
    "v_mov_b32 v75, 0x3f803f80\n"
    "s_mov_b64 s[44:45], %8\n"
    "s_and_b32 s45, s45, 0xffff\n"
    "s_mov_b32 s46, 0x480000\n"
    "s_mov_b32 s47, 0x20000\n"
    "s_add_u32 m0, s54, 0\n"
    "s_add_u32 s55, %6, 0\n"
    "buffer_load_dwordx4 v48, s[44:47], s55 offen lds\n"
    "s_add_u32 m0, s54, 4096\n"
    "s_add_u32 s55, %6, 1048576\n"
    "buffer_load_dwordx4 v48, s[44:47], s55 offen lds\n"
    "s_add_u32 m0, s54, 8192\n"
    "s_add_u32 s55, %6, 2097152\n"
    "buffer_load_dwordx4 v48, s[44:47], s55 offen lds\n"
    "s_add_u32 m0, s54, 12288\n"
    "s_add_u32 s55, %6, 3145728\n"
    "buffer_load_dwordx4 v48, s[44:47], s55 offen lds\n"
    "s_add_u32 m0, s54, 16384\n"
    "s_add_u32 s55, %6, 32768\n"
    "buffer_load_dwordx4 v48, s[44:47], s55 offen lds\n"
    "s_add_u32 m0, s54, 20480\n"
    "s_add_u32 s55, %6, 1081344\n"
    "buffer_load_dwordx4 v48, s[44:47], s55 offen lds\n"
    "s_add_u32 m0, s54, 24576\n"
    "s_add_u32 s55, %6, 2129920\n"
    "buffer_load_dwordx4 v48, s[44:47], s55 offen lds\n"
    "s_add_u32 m0, s54, 28672\n"
    "s_add_u32 s55, %6, 3178496\n"
    "buffer_load_dwordx4 v48, s[44:47], s55 offen lds\n"
    "s_add_u32 m0, s54, 32768\n"
    "s_add_u32 s55, %6, 262144\n"
    "buffer_load_dwordx4 v48, s[44:47], s55 offen lds\n"
    "s_add_u32 m0, s54, 36864\n"
    "s_add_u32 s55, %6, 1310720\n"
    "buffer_load_dwordx4 v48, s[44:47], s55 offen lds\n"
    "s_add_u32 m0, s54, 40960\n"
    "s_add_u32 s55, %6, 2359296\n"
    "buffer_load_dwordx4 v48, s[44:47], s55 offen lds\n"
    "s_add_u32 m0, s54, 45056\n"
    "s_add_u32 s55, %6, 3407872\n"
    "buffer_load_dwordx4 v48, s[44:47], s55 offen lds\n"
    "s_add_u32 m0, s54, 49152\n"
    "s_add_u32 s55, %6, 294912\n"
    "buffer_load_dwordx4 v48, s[44:47], s55 offen lds\n"
    "s_add_u32 m0, s54, 53248\n"
    "s_add_u32 s55, %6, 1343488\n"
    "buffer_load_dwordx4 v48, s[44:47], s55 offen lds\n"
    "s_add_u32 m0, s54, 57344\n"
    "s_add_u32 s55, %6, 2392064\n"
    "buffer_load_dwordx4 v48, s[44:47], s55 offen lds\n"
    "s_add_u32 m0, s54, 61440\n"
    "s_add_u32 s55, %6, 3440640\n"
    "buffer_load_dwordx4 v48, s[44:47], s55 offen lds\n"
    "s_add_u32 m0, s54, 65536\n"
    "s_add_u32 s55, %6, 524288\n"
    "buffer_load_dwordx4 v48, s[44:47], s55 offen lds\n"
    "s_add_u32 m0, s54, 69632\n"
    "s_add_u32 s55, %6, 1572864\n"
    "buffer_load_dwordx4 v48, s[44:47], s55 offen lds\n"
    "s_add_u32 m0, s54, 73728\n"
    "s_add_u32 s55, %6, 2621440\n"
    "buffer_load_dwordx4 v48, s[44:47], s55 offen lds\n"
    "s_add_u32 m0, s54, 77824\n"
    "s_add_u32 s55, %6, 3670016\n"
    "buffer_load_dwordx4 v48, s[44:47], s55 offen lds\n"
    "s_add_u32 m0, s54, 81920\n"
    "s_add_u32 s55, %6, 557056\n"
    "buffer_load_dwordx4 v48, s[44:47], s55 offen lds\n"
    "s_add_u32 m0, s54, 86016\n"
    "s_add_u32 s55, %6, 1605632\n"
    "buffer_load_dwordx4 v48, s[44:47], s55 offen lds\n"
    "s_add_u32 m0, s54, 90112\n"
    "s_add_u32 s55, %6, 2654208\n"
    "buffer_load_dwordx4 v48, s[44:47], s55 offen lds\n"
    "s_add_u32 m0, s54, 94208\n"
    "s_add_u32 s55, %6, 3702784\n"
    "buffer_load_dwordx4 v48, s[44:47], s55 offen lds\n"
    "s_add_u32 m0, s54, 98304\n"
    "s_add_u32 s55, %6, 786432\n"
    "buffer_load_dwordx4 v48, s[44:47], s55 offen lds\n"
    "s_add_u32 m0, s54, 102400\n"
    "s_add_u32 s55, %6, 1835008\n"
    "buffer_load_dwordx4 v48, s[44:47], s55 offen lds\n"
    "s_add_u32 m0, s54, 106496\n"
    "s_add_u32 s55, %6, 2883584\n"
    "buffer_load_dwordx4 v48, s[44:47], s55 offen lds\n"
    "s_add_u32 m0, s54, 110592\n"
    "s_add_u32 s55, %6, 3932160\n"
    "buffer_load_dwordx4 v48, s[44:47], s55 offen lds\n"
    "s_add_u32 m0, s54, 114688\n"
    "s_add_u32 s55, %6, 819200\n"
    "buffer_load_dwordx4 v48, s[44:47], s55 offen lds\n"
    "s_add_u32 m0, s54, 118784\n"
    "s_add_u32 s55, %6, 1867776\n"
    "buffer_load_dwordx4 v48, s[44:47], s55 offen lds\n"
    "s_add_u32 m0, s54, 122880\n"
    "s_add_u32 s55, %6, 2916352\n"
    "buffer_load_dwordx4 v48, s[44:47], s55 offen lds\n"
    "s_add_u32 m0, s54, 126976\n"
    "s_add_u32 s55, %6, 3964928\n"
    "buffer_load_dwordx4 v48, s[44:47], s55 offen lds\n"
    "s_add_u32 m0, s54, 131072\n"
    "s_add_u32 s55, %6, 4194304\n"
    "buffer_load_dwordx4 v48, s[44:47], s55 offen lds\n"
    "s_add_u32 m0, s54, 135168\n"
    "s_add_u32 s55, %6, 4227072\n"
    "buffer_load_dwordx4 v48, s[44:47], s55 offen lds\n"
    "s_add_u32 m0, s54, 139264\n"
    "s_add_u32 s55, %6, 4456448\n"
    "buffer_load_dwordx4 v48, s[44:47], s55 offen lds\n"
    "s_add_u32 m0, s54, 143360\n"
    "s_add_u32 s55, %6, 4489216\n"
    "buffer_load_dwordx4 v48, s[44:47], s55 offen lds\n"
    "s_xor_b32 s50, %2, 0\n"
    "s_lshl_b32 s50, s50, 12\n"
    "v_add_u32 v32, s50, v49\n"
    "v_add_u32 v33, s50, v50\n"
    "s_xor_b32 s50, %2, 1\n"
    "s_lshl_b32 s50, s50, 12\n"
    "v_add_u32 v34, s50, v49\n"
    "v_add_u32 v35, s50, v50\n"
    "s_xor_b32 s50, %2, 2\n"
    "s_lshl_b32 s50, s50, 12\n"
    "v_add_u32 v36, s50, v49\n"
    "v_add_u32 v37, s50, v50\n"
    "s_xor_b32 s50, %2, 3\n"
    "s_lshl_b32 s50, s50, 12\n"
    "v_add_u32 v38, s50, v49\n"
    "v_add_u32 v39, s50, v50\n"
    "s_waitcnt vmcnt(32) lgkmcnt(0)\n"
    "s_barrier\n"
    "ds_read_b64_tr_b16 a[0:1], v32\n"
    "ds_read_b64_tr_b16 a[4:5], v33\n"
    "ds_read_b64_tr_b16 a[32:33], v34\n"
    "ds_read_b64_tr_b16 a[36:37], v35\n"
    "ds_read_b64_tr_b16 a[64:65], v36\n"
    "ds_read_b64_tr_b16 a[68:69], v37\n"
    "ds_read_b64_tr_b16 a[96:97], v38\n"
    "ds_read_b64_tr_b16 a[100:101], v39\n"
    "s_waitcnt vmcnt(28) lgkmcnt(0)\n"
    "s_barrier\n"
    "s_mov_b64 s[44:45], %9\n"
    "s_and_b32 s45, s45, 0xffff\n"
    "s_mov_b32 s46, 0x480000\n"
    "s_mov_b32 s47, 0x20000\n"
    "s_add_u32 m0, s54, 0\n"
    "s_add_u32 s55, %6, 0\n"
    "buffer_load_dwordx4 v48, s[44:47], s55 offen lds\n"
    "s_add_u32 m0, s54, 4096\n"
    "s_add_u32 s55, %6, 1048576\n"
    "buffer_load_dwordx4 v48, s[44:47], s55 offen lds\n"
    "s_add_u32 m0, s54, 8192\n"
    "s_add_u32 s55, %6, 2097152\n"
    "buffer_load_dwordx4 v48, s[44:47], s55 offen lds\n"
    "s_add_u32 m0, s54, 12288\n"
    "s_add_u32 s55, %6, 3145728\n"
    "buffer_load_dwordx4 v48, s[44:47], s55 offen lds\n"
    "ds_read_b64_tr_b16 a[2:3], v32 offset:16384\n"
    "ds_read_b64_tr_b16 a[6:7], v33 offset:16384\n"
    "ds_read_b64_tr_b16 a[34:35], v34 offset:16384\n"
    "ds_read_b64_tr_b16 a[38:39], v35 offset:16384\n"
    "ds_read_b64_tr_b16 a[66:67], v36 offset:16384\n"
    "ds_read_b64_tr_b16 a[70:71], v37 offset:16384\n"
    "ds_read_b64_tr_b16 a[98:99], v38 offset:16384\n"
    "ds_read_b64_tr_b16 a[102:103], v39 offset:16384\n"
    "s_waitcnt vmcnt(28) lgkmcnt(0)\n"
    "s_barrier\n"
    "s_add_u32 m0, s54, 16384\n"
    "s_add_u32 s55, %6, 32768\n"
    "buffer_load_dwordx4 v48, s[44:47], s55 offen lds\n"
    "s_add_u32 m0, s54, 20480\n"
    "s_add_u32 s55, %6, 1081344\n"
    "buffer_load_dwordx4 v48, s[44:47], s55 offen lds\n"
    "s_add_u32 m0, s54, 24576\n"
    "s_add_u32 s55, %6, 2129920\n"
    "buffer_load_dwordx4 v48, s[44:47], s55 offen lds\n"
    "s_add_u32 m0, s54, 28672\n"
    "s_add_u32 s55, %6, 3178496\n"
    "buffer_load_dwordx4 v48, s[44:47], s55 offen lds\n"
    "v_mfma_f32_16x16x32_bf16 v[8:11], a[0:3], v[72:75], 0\n"
    "v_mfma_f32_16x16x32_bf16 v[12:15], a[4:7], v[72:75], 0\n"
    "v_mfma_f32_16x16x32_bf16 v[8:11], a[32:35], v[72:75], v[8:11]\n"
    "v_mfma_f32_16x16x32_bf16 v[12:15], a[36:39], v[72:75], v[12:15]\n"
    "v_mfma_f32_16x16x32_bf16 v[8:11], a[64:67], v[72:75], v[8:11]\n"
    "v_mfma_f32_16x16x32_bf16 v[12:15], a[68:71], v[72:75], v[12:15]\n"
    "v_mfma_f32_16x16x32_bf16 v[8:11], a[96:99], v[72:75], v[8:11]\n"
    "v_mfma_f32_16x16x32_bf16 v[12:15], a[100:103], v[72:75], v[12:15]\n"
    "ds_read_b64_tr_b16 a[8:9], v32 offset:32768\n"
    "ds_read_b64_tr_b16 a[12:13], v33 offset:32768\n"
    "ds_read_b64_tr_b16 a[40:41], v34 offset:32768\n"
    "ds_read_b64_tr_b16 a[44:45], v35 offset:32768\n"
    "ds_read_b64_tr_b16 a[72:73], v36 offset:32768\n"
    "ds_read_b64_tr_b16 a[76:77], v37 offset:32768\n"
    "ds_read_b64_tr_b16 a[104:105], v38 offset:32768\n"
    "ds_read_b64_tr_b16 a[108:109], v39 offset:32768\n"
    "s_waitcnt vmcnt(28) lgkmcnt(0)\n"
    "s_barrier\n"
    "s_add_u32 m0, s54, 32768\n"
    "s_add_u32 s55, %6, 262144\n"
    "buffer_load_dwordx4 v48, s[44:47], s55 offen lds\n"
    "s_add_u32 m0, s54, 36864\n"
    "s_add_u32 s55, %6, 1310720\n"
    "buffer_load_dwordx4 v48, s[44:47], s55 offen lds\n"
    "s_add_u32 m0, s54, 40960\n"
    "s_add_u32 s55, %6, 2359296\n"
    "buffer_load_dwordx4 v48, s[44:47], s55 offen lds\n"
    "s_add_u32 m0, s54, 45056\n"
    "s_add_u32 s55, %6, 3407872\n"
    "buffer_load_dwordx4 v48, s[44:47], s55 offen lds\n"
    "ds_read_b64_tr_b16 a[10:11], v32 offset:49152\n"
    "ds_read_b64_tr_b16 a[14:15], v33 offset:49152\n"
    "ds_read_b64_tr_b16 a[42:43], v34 offset:49152\n"
    "ds_read_b64_tr_b16 a[46:47], v35 offset:49152\n"
    "ds_read_b64_tr_b16 a[74:75], v36 offset:49152\n"
    "ds_read_b64_tr_b16 a[78:79], v37 offset:49152\n"
    "ds_read_b64_tr_b16 a[106:107], v38 offset:49152\n"
    "ds_read_b64_tr_b16 a[110:111], v39 offset:49152\n"
    "s_waitcnt vmcnt(28) lgkmcnt(0)\n"
    "s_barrier\n"
    "s_add_u32 m0, s54, 49152\n"
    "s_add_u32 s55, %6, 294912\n"
    "buffer_load_dwordx4 v48, s[44:47], s55 offen lds\n"
    "s_add_u32 m0, s54, 53248\n"
    "s_add_u32 s55, %6, 1343488\n"
    "buffer_load_dwordx4 v48, s[44:47], s55 offen lds\n"
    "s_add_u32 m0, s54, 57344\n"
    "s_add_u32 s55, %6, 2392064\n"
    "buffer_load_dwordx4 v48, s[44:47], s55 offen lds\n"
    "s_add_u32 m0, s54, 61440\n"
    "s_add_u32 s55, %6, 3440640\n"
    "buffer_load_dwordx4 v48, s[44:47], s55 offen lds\n"
    "v_mfma_f32_16x16x32_bf16 v[8:11], a[8:11], v[72:75], v[8:11]\n"
    "v_mfma_f32_16x16x32_bf16 v[12:15], a[12:15], v[72:75], v[12:15]\n"
    "v_mfma_f32_16x16x32_bf16 v[8:11], a[40:43], v[72:75], v[8:11]\n"
    "v_mfma_f32_16x16x32_bf16 v[12:15], a[44:47], v[72:75], v[12:15]\n"
    "v_mfma_f32_16x16x32_bf16 v[8:11], a[72:75], v[72:75], v[8:11]\n"
    "v_mfma_f32_16x16x32_bf16 v[12:15], a[76:79], v[72:75], v[12:15]\n"
    "v_mfma_f32_16x16x32_bf16 v[8:11], a[104:107], v[72:75], v[8:11]\n"
    "v_mfma_f32_16x16x32_bf16 v[12:15], a[108:111], v[72:75], v[12:15]\n"
    "v_add_u32 v71, 0x10000, v32\n"
    "ds_read_b64_tr_b16 a[16:17], v71\n"
    "v_add_u32 v70, 0x10000, v33\n"
    "ds_read_b64_tr_b16 a[20:21], v70\n"
    "v_add_u32 v69, 0x10000, v34\n"
    "ds_read_b64_tr_b16 a[48:49], v69\n"
    "v_add_u32 v68, 0x10000, v35\n"
    "ds_read_b64_tr_b16 a[52:53], v68\n"
    "v_add_u32 v67, 0x10000, v36\n"
    "ds_read_b64_tr_b16 a[80:81], v67\n"
    "v_add_u32 v66, 0x10000, v37\n"
    "ds_read_b64_tr_b16 a[84:85], v66\n"
    "v_add_u32 v65, 0x10000, v38\n"
    "ds_read_b64_tr_b16 a[112:113], v65\n"
    "v_add_u32 v64, 0x10000, v39\n"
    "ds_read_b64_tr_b16 a[116:117], v64\n"
    "s_waitcnt vmcnt(28) lgkmcnt(0)\n"
    "s_barrier\n"
    "s_add_u32 m0, s54, 65536\n"
    "s_add_u32 s55, %6, 524288\n"
    "buffer_load_dwordx4 v48, s[44:47], s55 offen lds\n"
    "s_add_u32 m0, s54, 69632\n"
    "s_add_u32 s55, %6, 1572864\n"
    "buffer_load_dwordx4 v48, s[44:47], s55 offen lds\n"
    "s_add_u32 m0, s54, 73728\n"
    "s_add_u32 s55, %6, 2621440\n"
    "buffer_load_dwordx4 v48, s[44:47], s55 offen lds\n"
    "s_add_u32 m0, s54, 77824\n"
    "s_add_u32 s55, %6, 3670016\n"
    "buffer_load_dwordx4 v48, s[44:47], s55 offen lds\n"
    "ds_read_b64_tr_b16 a[18:19], v71 offset:16384\n"
    "ds_read_b64_tr_b16 a[22:23], v70 offset:16384\n"
    "ds_read_b64_tr_b16 a[50:51], v69 offset:16384\n"
    "ds_read_b64_tr_b16 a[54:55], v68 offset:16384\n"
    "ds_read_b64_tr_b16 a[82:83], v67 offset:16384\n"
    "ds_read_b64_tr_b16 a[86:87], v66 offset:16384\n"
    "ds_read_b64_tr_b16 a[114:115], v65 offset:16384\n"
    "ds_read_b64_tr_b16 a[118:119], v64 offset:16384\n"
    "s_waitcnt vmcnt(28) lgkmcnt(0)\n"
    "s_barrier\n"
    "s_add_u32 m0, s54, 81920\n"
    "s_add_u32 s55, %6, 557056\n"
    "buffer_load_dwordx4 v48, s[44:47], s55 offen lds\n"
    "s_add_u32 m0, s54, 86016\n"
    "s_add_u32 s55, %6, 1605632\n"
    "buffer_load_dwordx4 v48, s[44:47], s55 offen lds\n"
    "s_add_u32 m0, s54, 90112\n"
    "s_add_u32 s55, %6, 2654208\n"
    "buffer_load_dwordx4 v48, s[44:47], s55 offen lds\n"
    "s_add_u32 m0, s54, 94208\n"
    "s_add_u32 s55, %6, 3702784\n"
    "buffer_load_dwordx4 v48, s[44:47], s55 offen lds\n"
    "v_mfma_f32_16x16x32_bf16 v[8:11], a[16:19], v[72:75], v[8:11]\n"
    "v_mfma_f32_16x16x32_bf16 v[12:15], a[20:23], v[72:75], v[12:15]\n"
    "v_mfma_f32_16x16x32_bf16 v[8:11], a[48:51], v[72:75], v[8:11]\n"
    "v_mfma_f32_16x16x32_bf16 v[12:15], a[52:55], v[72:75], v[12:15]\n"
    "v_mfma_f32_16x16x32_bf16 v[8:11], a[80:83], v[72:75], v[8:11]\n"
    "v_mfma_f32_16x16x32_bf16 v[12:15], a[84:87], v[72:75], v[12:15]\n"
    "v_mfma_f32_16x16x32_bf16 v[8:11], a[112:115], v[72:75], v[8:11]\n"
    "v_mfma_f32_16x16x32_bf16 v[12:15], a[116:119], v[72:75], v[12:15]\n"
    "ds_read_b64_tr_b16 a[24:25], v71 offset:32768\n"
    "ds_read_b64_tr_b16 a[28:29], v70 offset:32768\n"
    "ds_read_b64_tr_b16 a[56:57], v69 offset:32768\n"
    "ds_read_b64_tr_b16 a[60:61], v68 offset:32768\n"
    "ds_read_b64_tr_b16 a[88:89], v67 offset:32768\n"
    "ds_read_b64_tr_b16 a[92:93], v66 offset:32768\n"
    "ds_read_b64_tr_b16 a[120:121], v65 offset:32768\n"
    "ds_read_b64_tr_b16 a[124:125], v64 offset:32768\n"
    "s_waitcnt vmcnt(28) lgkmcnt(0)\n"
    "s_barrier\n"
    "s_add_u32 m0, s54, 98304\n"
    "s_add_u32 s55, %6, 786432\n"
    "buffer_load_dwordx4 v48, s[44:47], s55 offen lds\n"
    "s_add_u32 m0, s54, 102400\n"
    "s_add_u32 s55, %6, 1835008\n"
    "buffer_load_dwordx4 v48, s[44:47], s55 offen lds\n"
    "s_add_u32 m0, s54, 106496\n"
    "s_add_u32 s55, %6, 2883584\n"
    "buffer_load_dwordx4 v48, s[44:47], s55 offen lds\n"
    "s_add_u32 m0, s54, 110592\n"
    "s_add_u32 s55, %6, 3932160\n"
    "buffer_load_dwordx4 v48, s[44:47], s55 offen lds\n"
    "ds_read_b64_tr_b16 a[26:27], v71 offset:49152\n"
    "ds_read_b64_tr_b16 a[30:31], v70 offset:49152\n"
    "ds_read_b64_tr_b16 a[58:59], v69 offset:49152\n"
    "ds_read_b64_tr_b16 a[62:63], v68 offset:49152\n"
    "ds_read_b64_tr_b16 a[90:91], v67 offset:49152\n"
    "ds_read_b64_tr_b16 a[94:95], v66 offset:49152\n"
    "ds_read_b64_tr_b16 a[122:123], v65 offset:49152\n"
    "ds_read_b64_tr_b16 a[126:127], v64 offset:49152\n"
    "s_waitcnt vmcnt(28) lgkmcnt(0)\n"
    "s_barrier\n"
    "s_add_u32 m0, s54, 114688\n"
    "s_add_u32 s55, %6, 819200\n"
    "buffer_load_dwordx4 v48, s[44:47], s55 offen lds\n"
    "s_add_u32 m0, s54, 118784\n"
    "s_add_u32 s55, %6, 1867776\n"
    "buffer_load_dwordx4 v48, s[44:47], s55 offen lds\n"
    "s_add_u32 m0, s54, 122880\n"
    "s_add_u32 s55, %6, 2916352\n"
    "buffer_load_dwordx4 v48, s[44:47], s55 offen lds\n"
    "s_add_u32 m0, s54, 126976\n"
    "s_add_u32 s55, %6, 3964928\n"
    "buffer_load_dwordx4 v48, s[44:47], s55 offen lds\n"
    "v_mfma_f32_16x16x32_bf16 v[8:11], a[24:27], v[72:75], v[8:11]\n"
    "v_mfma_f32_16x16x32_bf16 v[12:15], a[28:31], v[72:75], v[12:15]\n"
    "v_mfma_f32_16x16x32_bf16 v[8:11], a[56:59], v[72:75], v[8:11]\n"
    "v_mfma_f32_16x16x32_bf16 v[12:15], a[60:63], v[72:75], v[12:15]\n"
    "v_mfma_f32_16x16x32_bf16 v[8:11], a[88:91], v[72:75], v[8:11]\n"
    "v_mfma_f32_16x16x32_bf16 v[12:15], a[92:95], v[72:75], v[12:15]\n"
    "v_mfma_f32_16x16x32_bf16 v[8:11], a[120:123], v[72:75], v[8:11]\n"
    "v_mfma_f32_16x16x32_bf16 v[12:15], a[124:127], v[72:75], v[12:15]\n"
    "v_add_u32 v63, 0x20000, v49\n"
    "ds_read_b64_tr_b16 a[128:129], v63\n"
    "v_add_u32 v62, 0x20000, v50\n"
    "ds_read_b64_tr_b16 a[132:133], v62\n"
    "ds_read_b64_tr_b16 a[130:131], v63 offset:4096\n"
    "ds_read_b64_tr_b16 a[134:135], v62 offset:4096\n"
    "ds_read_b64_tr_b16 a[136:137], v63 offset:8192\n"
    "ds_read_b64_tr_b16 a[140:141], v62 offset:8192\n"
    "ds_read_b64_tr_b16 a[138:139], v63 offset:12288\n"
    "ds_read_b64_tr_b16 a[142:143], v62 offset:12288\n"
    "s_waitcnt vmcnt(28) lgkmcnt(0)\n"
    "s_barrier\n"
    "s_add_u32 m0, s54, 131072\n"
    "s_add_u32 s55, %6, 4194304\n"
    "buffer_load_dwordx4 v48, s[44:47], s55 offen lds\n"
    "s_add_u32 m0, s54, 135168\n"
    "s_add_u32 s55, %6, 4227072\n"
    "buffer_load_dwordx4 v48, s[44:47], s55 offen lds\n"
    "s_add_u32 m0, s54, 139264\n"
    "s_add_u32 s55, %6, 4456448\n"
    "buffer_load_dwordx4 v48, s[44:47], s55 offen lds\n"
    "s_add_u32 m0, s54, 143360\n"
    "s_add_u32 s55, %6, 4489216\n"
    "buffer_load_dwordx4 v48, s[44:47], s55 offen lds\n"
    "v_mfma_f32_16x16x32_bf16 v[8:11], a[128:131], v[72:75], v[8:11]\n"
    "v_mfma_f32_16x16x32_bf16 v[12:15], a[132:135], v[72:75], v[12:15]\n"
    "v_mfma_f32_16x16x32_bf16 v[8:11], a[136:139], v[72:75], v[8:11]\n"
    "v_mfma_f32_16x16x32_bf16 v[12:15], a[140:143], v[72:75], v[12:15]\n"
    "ds_read_b64_tr_b16 a[144:145], v32\n"
    "ds_read_b64_tr_b16 a[148:149], v33\n"
    "ds_read_b64_tr_b16 a[176:177], v34\n"
    "ds_read_b64_tr_b16 a[180:181], v35\n"
    "ds_read_b64_tr_b16 a[208:209], v36\n"
    "ds_read_b64_tr_b16 a[212:213], v37\n"
    "ds_read_b64_tr_b16 a[240:241], v38\n"
    "ds_read_b64_tr_b16 a[244:245], v39\n"
    "s_waitcnt vmcnt(28) lgkmcnt(0)\n"
    "s_barrier\n"
    "s_mov_b64 s[44:45], %10\n"
    "s_and_b32 s45, s45, 0xffff\n"
    "s_mov_b32 s46, 0x480000\n"
    "s_mov_b32 s47, 0x20000\n"
    "s_add_u32 m0, s54, 0\n"
    "s_add_u32 s55, %6, 0\n"
    "buffer_load_dwordx4 v48, s[44:47], s55 offen lds\n"
    "s_add_u32 m0, s54, 4096\n"
    "s_add_u32 s55, %6, 1048576\n"
    "buffer_load_dwordx4 v48, s[44:47], s55 offen lds\n"
    "s_add_u32 m0, s54, 8192\n"
    "s_add_u32 s55, %6, 2097152\n"
    "buffer_load_dwordx4 v48, s[44:47], s55 offen lds\n"
    "s_add_u32 m0, s54, 12288\n"
    "s_add_u32 s55, %6, 3145728\n"
    "buffer_load_dwordx4 v48, s[44:47], s55 offen lds\n"
    "ds_read_b64_tr_b16 a[146:147], v32 offset:16384\n"
    "ds_read_b64_tr_b16 a[150:151], v33 offset:16384\n"
    "ds_read_b64_tr_b16 a[178:179], v34 offset:16384\n"
    "ds_read_b64_tr_b16 a[182:183], v35 offset:16384\n"
    "ds_read_b64_tr_b16 a[210:211], v36 offset:16384\n"
    "ds_read_b64_tr_b16 a[214:215], v37 offset:16384\n"
    "ds_read_b64_tr_b16 a[242:243], v38 offset:16384\n"
    "ds_read_b64_tr_b16 a[246:247], v39 offset:16384\n"
    "s_waitcnt vmcnt(28) lgkmcnt(0)\n"
    "s_barrier\n"
    "s_add_u32 m0, s54, 16384\n"
    "s_add_u32 s55, %6, 32768\n"
    "buffer_load_dwordx4 v48, s[44:47], s55 offen lds\n"
    "s_add_u32 m0, s54, 20480\n"
    "s_add_u32 s55, %6, 1081344\n"
    "buffer_load_dwordx4 v48, s[44:47], s55 offen lds\n"
    "s_add_u32 m0, s54, 24576\n"
    "s_add_u32 s55, %6, 2129920\n"
    "buffer_load_dwordx4 v48, s[44:47], s55 offen lds\n"
    "s_add_u32 m0, s54, 28672\n"
    "s_add_u32 s55, %6, 3178496\n"
    "buffer_load_dwordx4 v48, s[44:47], s55 offen lds\n"
    "v_mfma_f32_16x16x32_bf16 v[16:19], a[144:147], v[72:75], 0\n"
    "v_mfma_f32_16x16x32_bf16 v[20:23], a[148:151], v[72:75], 0\n"
    "v_mfma_f32_16x16x32_bf16 v[16:19], a[176:179], v[72:75], v[16:19]\n"
    "v_mfma_f32_16x16x32_bf16 v[20:23], a[180:183], v[72:75], v[20:23]\n"
    "v_mfma_f32_16x16x32_bf16 v[16:19], a[208:211], v[72:75], v[16:19]\n"
    "v_mfma_f32_16x16x32_bf16 v[20:23], a[212:215], v[72:75], v[20:23]\n"
    "v_mfma_f32_16x16x32_bf16 v[16:19], a[240:243], v[72:75], v[16:19]\n"
    "v_mfma_f32_16x16x32_bf16 v[20:23], a[244:247], v[72:75], v[20:23]\n"
    "ds_read_b64_tr_b16 a[152:153], v32 offset:32768\n"
    "ds_read_b64_tr_b16 a[156:157], v33 offset:32768\n"
    "ds_read_b64_tr_b16 a[184:185], v34 offset:32768\n"
    "ds_read_b64_tr_b16 a[188:189], v35 offset:32768\n"
    "ds_read_b64_tr_b16 a[216:217], v36 offset:32768\n"
    "ds_read_b64_tr_b16 a[220:221], v37 offset:32768\n"
    "ds_read_b64_tr_b16 a[248:249], v38 offset:32768\n"
    "ds_read_b64_tr_b16 a[252:253], v39 offset:32768\n"
    "s_waitcnt vmcnt(28) lgkmcnt(0)\n"
    "s_barrier\n"
    "s_add_u32 m0, s54, 32768\n"
    "s_add_u32 s55, %6, 262144\n"
    "buffer_load_dwordx4 v48, s[44:47], s55 offen lds\n"
    "s_add_u32 m0, s54, 36864\n"
    "s_add_u32 s55, %6, 1310720\n"
    "buffer_load_dwordx4 v48, s[44:47], s55 offen lds\n"
    "s_add_u32 m0, s54, 40960\n"
    "s_add_u32 s55, %6, 2359296\n"
    "buffer_load_dwordx4 v48, s[44:47], s55 offen lds\n"
    "s_add_u32 m0, s54, 45056\n"
    "s_add_u32 s55, %6, 3407872\n"
    "buffer_load_dwordx4 v48, s[44:47], s55 offen lds\n"
    "ds_read_b64_tr_b16 a[154:155], v32 offset:49152\n"
    "ds_read_b64_tr_b16 a[158:159], v33 offset:49152\n"
    "ds_read_b64_tr_b16 a[186:187], v34 offset:49152\n"
    "ds_read_b64_tr_b16 a[190:191], v35 offset:49152\n"
    "ds_read_b64_tr_b16 a[218:219], v36 offset:49152\n"
    "ds_read_b64_tr_b16 a[222:223], v37 offset:49152\n"
    "ds_read_b64_tr_b16 a[250:251], v38 offset:49152\n"
    "ds_read_b64_tr_b16 a[254:255], v39 offset:49152\n"
    "s_waitcnt vmcnt(28) lgkmcnt(0)\n"
    "s_barrier\n"
    "s_add_u32 m0, s54, 49152\n"
    "s_add_u32 s55, %6, 294912\n"
    "buffer_load_dwordx4 v48, s[44:47], s55 offen lds\n"
    "s_add_u32 m0, s54, 53248\n"
    "s_add_u32 s55, %6, 1343488\n"
    "buffer_load_dwordx4 v48, s[44:47], s55 offen lds\n"
    "s_add_u32 m0, s54, 57344\n"
    "s_add_u32 s55, %6, 2392064\n"
    "buffer_load_dwordx4 v48, s[44:47], s55 offen lds\n"
    "s_add_u32 m0, s54, 61440\n"
    "s_add_u32 s55, %6, 3440640\n"
    "buffer_load_dwordx4 v48, s[44:47], s55 offen lds\n"
    "v_mfma_f32_16x16x32_bf16 v[16:19], a[152:155], v[72:75], v[16:19]\n"
    "v_mfma_f32_16x16x32_bf16 v[20:23], a[156:159], v[72:75], v[20:23]\n"
    "v_mfma_f32_16x16x32_bf16 v[16:19], a[184:187], v[72:75], v[16:19]\n"
    "v_mfma_f32_16x16x32_bf16 v[20:23], a[188:191], v[72:75], v[20:23]\n"
    "v_mfma_f32_16x16x32_bf16 v[16:19], a[216:219], v[72:75], v[16:19]\n"
    "v_mfma_f32_16x16x32_bf16 v[20:23], a[220:223], v[72:75], v[20:23]\n"
    "v_mfma_f32_16x16x32_bf16 v[16:19], a[248:251], v[72:75], v[16:19]\n"
    "v_mfma_f32_16x16x32_bf16 v[20:23], a[252:255], v[72:75], v[20:23]\n"
    "ds_read_b64_tr_b16 a[160:161], v71\n"
    "ds_read_b64_tr_b16 a[164:165], v70\n"
    "ds_read_b64_tr_b16 a[192:193], v69\n"
    "ds_read_b64_tr_b16 a[196:197], v68\n"
    "ds_read_b64_tr_b16 a[224:225], v67\n"
    "ds_read_b64_tr_b16 a[228:229], v66\n"
    "ds_read_b64_tr_b16 v[84:85], v65\n"
    "ds_read_b64_tr_b16 v[88:89], v64\n"
    "s_waitcnt vmcnt(28) lgkmcnt(0)\n"
    "s_barrier\n"
    "s_add_u32 m0, s54, 65536\n"
    "s_add_u32 s55, %6, 524288\n"
    "buffer_load_dwordx4 v48, s[44:47], s55 offen lds\n"
    "s_add_u32 m0, s54, 69632\n"
    "s_add_u32 s55, %6, 1572864\n"
    "buffer_load_dwordx4 v48, s[44:47], s55 offen lds\n"
    "s_add_u32 m0, s54, 73728\n"
    "s_add_u32 s55, %6, 2621440\n"
    "buffer_load_dwordx4 v48, s[44:47], s55 offen lds\n"
    "s_add_u32 m0, s54, 77824\n"
    "s_add_u32 s55, %6, 3670016\n"
    "buffer_load_dwordx4 v48, s[44:47], s55 offen lds\n"
    "ds_read_b64_tr_b16 a[162:163], v71 offset:16384\n"
    "ds_read_b64_tr_b16 a[166:167], v70 offset:16384\n"
    "ds_read_b64_tr_b16 a[194:195], v69 offset:16384\n"
    "ds_read_b64_tr_b16 a[198:199], v68 offset:16384\n"
    "ds_read_b64_tr_b16 a[226:227], v67 offset:16384\n"
    "ds_read_b64_tr_b16 a[230:231], v66 offset:16384\n"
    "ds_read_b64_tr_b16 v[86:87], v65 offset:16384\n"
    "ds_read_b64_tr_b16 v[90:91], v64 offset:16384\n"
    "s_waitcnt vmcnt(28) lgkmcnt(0)\n"
    "s_barrier\n"
    "s_add_u32 m0, s54, 81920\n"
    "s_add_u32 s55, %6, 557056\n"
    "buffer_load_dwordx4 v48, s[44:47], s55 offen lds\n"
    "s_add_u32 m0, s54, 86016\n"
    "s_add_u32 s55, %6, 1605632\n"
    "buffer_load_dwordx4 v48, s[44:47], s55 offen lds\n"
    "s_add_u32 m0, s54, 90112\n"
    "s_add_u32 s55, %6, 2654208\n"
    "buffer_load_dwordx4 v48, s[44:47], s55 offen lds\n"
    "s_add_u32 m0, s54, 94208\n"
    "s_add_u32 s55, %6, 3702784\n"
    "buffer_load_dwordx4 v48, s[44:47], s55 offen lds\n"
    "v_mfma_f32_16x16x32_bf16 v[16:19], a[160:163], v[72:75], v[16:19]\n"
    "v_mfma_f32_16x16x32_bf16 v[20:23], a[164:167], v[72:75], v[20:23]\n"
    "v_mfma_f32_16x16x32_bf16 v[16:19], a[192:195], v[72:75], v[16:19]\n"
    "v_mfma_f32_16x16x32_bf16 v[20:23], a[196:199], v[72:75], v[20:23]\n"
    "v_mfma_f32_16x16x32_bf16 v[16:19], a[224:227], v[72:75], v[16:19]\n"
    "v_mfma_f32_16x16x32_bf16 v[20:23], a[228:231], v[72:75], v[20:23]\n"
    "v_mfma_f32_16x16x32_bf16 v[16:19], v[84:87], v[72:75], v[16:19]\n"
    "v_mfma_f32_16x16x32_bf16 v[20:23], v[88:91], v[72:75], v[20:23]\n"
    "ds_read_b64_tr_b16 a[168:169], v71 offset:32768\n"
    "ds_read_b64_tr_b16 a[172:173], v70 offset:32768\n"
    "ds_read_b64_tr_b16 a[200:201], v69 offset:32768\n"
    "ds_read_b64_tr_b16 a[204:205], v68 offset:32768\n"
    "ds_read_b64_tr_b16 a[232:233], v67 offset:32768\n"
    "ds_read_b64_tr_b16 a[236:237], v66 offset:32768\n"
    "ds_read_b64_tr_b16 v[92:93], v65 offset:32768\n"
    "ds_read_b64_tr_b16 v[96:97], v64 offset:32768\n"
    "s_waitcnt vmcnt(28) lgkmcnt(0)\n"
    "s_barrier\n"
    "s_add_u32 m0, s54, 98304\n"
    "s_add_u32 s55, %6, 786432\n"
    "buffer_load_dwordx4 v48, s[44:47], s55 offen lds\n"
    "s_add_u32 m0, s54, 102400\n"
    "s_add_u32 s55, %6, 1835008\n"
    "buffer_load_dwordx4 v48, s[44:47], s55 offen lds\n"
    "s_add_u32 m0, s54, 106496\n"
    "s_add_u32 s55, %6, 2883584\n"
    "buffer_load_dwordx4 v48, s[44:47], s55 offen lds\n"
    "s_add_u32 m0, s54, 110592\n"
    "s_add_u32 s55, %6, 3932160\n"
    "buffer_load_dwordx4 v48, s[44:47], s55 offen lds\n"
    "ds_read_b64_tr_b16 a[170:171], v71 offset:49152\n"
    "ds_read_b64_tr_b16 a[174:175], v70 offset:49152\n"
    "ds_read_b64_tr_b16 a[202:203], v69 offset:49152\n"
    "ds_read_b64_tr_b16 a[206:207], v68 offset:49152\n"
    "ds_read_b64_tr_b16 a[234:235], v67 offset:49152\n"
    "ds_read_b64_tr_b16 a[238:239], v66 offset:49152\n"
    "ds_read_b64_tr_b16 v[94:95], v65 offset:49152\n"
    "ds_read_b64_tr_b16 v[98:99], v64 offset:49152\n"
    "s_waitcnt vmcnt(28) lgkmcnt(0)\n"
    "s_barrier\n"
    "s_add_u32 m0, s54, 114688\n"
    "s_add_u32 s55, %6, 819200\n"
    "buffer_load_dwordx4 v48, s[44:47], s55 offen lds\n"
    "s_add_u32 m0, s54, 118784\n"
    "s_add_u32 s55, %6, 1867776\n"
    "buffer_load_dwordx4 v48, s[44:47], s55 offen lds\n"
    "s_add_u32 m0, s54, 122880\n"
    "s_add_u32 s55, %6, 2916352\n"
    "buffer_load_dwordx4 v48, s[44:47], s55 offen lds\n"
    "s_add_u32 m0, s54, 126976\n"
    "s_add_u32 s55, %6, 3964928\n"
    "buffer_load_dwordx4 v48, s[44:47], s55 offen lds\n"
    "v_mfma_f32_16x16x32_bf16 v[16:19], a[168:171], v[72:75], v[16:19]\n"
    "v_mfma_f32_16x16x32_bf16 v[20:23], a[172:175], v[72:75], v[20:23]\n"
    "v_mfma_f32_16x16x32_bf16 v[16:19], a[200:203], v[72:75], v[16:19]\n"
    "v_mfma_f32_16x16x32_bf16 v[20:23], a[204:207], v[72:75], v[20:23]\n"
    "v_mfma_f32_16x16x32_bf16 v[16:19], a[232:235], v[72:75], v[16:19]\n"
    "v_mfma_f32_16x16x32_bf16 v[20:23], a[236:239], v[72:75], v[20:23]\n"
    "v_mfma_f32_16x16x32_bf16 v[16:19], v[92:95], v[72:75], v[16:19]\n"
    "v_mfma_f32_16x16x32_bf16 v[20:23], v[96:99], v[72:75], v[20:23]\n"
    "ds_read_b64_tr_b16 v[100:101], v63\n"
    "ds_read_b64_tr_b16 v[104:105], v62\n"
    "ds_read_b64_tr_b16 v[102:103], v63 offset:4096\n"
    "ds_read_b64_tr_b16 v[106:107], v62 offset:4096\n"
    "ds_read_b64_tr_b16 v[108:109], v63 offset:8192\n"
    "ds_read_b64_tr_b16 v[112:113], v62 offset:8192\n"
    "ds_read_b64_tr_b16 v[110:111], v63 offset:12288\n"
    "ds_read_b64_tr_b16 v[114:115], v62 offset:12288\n"
    "s_waitcnt vmcnt(28) lgkmcnt(0)\n"
    "s_barrier\n"
    "s_add_u32 m0, s54, 131072\n"
    "s_add_u32 s55, %6, 4194304\n"
    "buffer_load_dwordx4 v48, s[44:47], s55 offen lds\n"
    "s_add_u32 m0, s54, 135168\n"
    "s_add_u32 s55, %6, 4227072\n"
    "buffer_load_dwordx4 v48, s[44:47], s55 offen lds\n"
    "s_add_u32 m0, s54, 139264\n"
    "s_add_u32 s55, %6, 4456448\n"
    "buffer_load_dwordx4 v48, s[44:47], s55 offen lds\n"
    "s_add_u32 m0, s54, 143360\n"
    "s_add_u32 s55, %6, 4489216\n"
    "buffer_load_dwordx4 v48, s[44:47], s55 offen lds\n"
    "v_mov_b32 v7, 0x20400\n"
    "global_load_dwordx2 v[80:81], v7, %1 sc1\n"
    "v_mbcnt_lo_u32_b32 v4, -1, 0\n"
    "v_min_u32 v4, 7, v4\n"
    "v_lshlrev_b32 v4, 7, v4\n"
    "v_add_u32 v4, 0x20000, v4\n"
    "global_load_dwordx2 v[82:83], v4, %1 sc1\n"
    "v_mfma_f32_16x16x32_bf16 v[16:19], v[100:103], v[72:75], v[16:19]\n"
    "v_mfma_f32_16x16x32_bf16 v[20:23], v[104:107], v[72:75], v[20:23]\n"
    "v_mfma_f32_16x16x32_bf16 v[16:19], v[108:111], v[72:75], v[16:19]\n"
    "v_mfma_f32_16x16x32_bf16 v[20:23], v[112:115], v[72:75], v[20:23]\n"
    "ds_read_b64_tr_b16 v[116:117], v32\n"
    "ds_read_b64_tr_b16 v[120:121], v33\n"
    "ds_read_b64_tr_b16 v[148:149], v34\n"
    "ds_read_b64_tr_b16 v[152:153], v35\n"
    "ds_read_b64_tr_b16 v[180:181], v36\n"
    "ds_read_b64_tr_b16 v[184:185], v37\n"
    "ds_read_b64_tr_b16 v[212:213], v38\n"
    "ds_read_b64_tr_b16 v[216:217], v39\n"
    "s_waitcnt vmcnt(30) lgkmcnt(0)\n"
    "s_barrier\n"
    "ds_read_b64_tr_b16 v[118:119], v32 offset:16384\n"
    "ds_read_b64_tr_b16 v[122:123], v33 offset:16384\n"
    "ds_read_b64_tr_b16 v[150:151], v34 offset:16384\n"
    "ds_read_b64_tr_b16 v[154:155], v35 offset:16384\n"
    "ds_read_b64_tr_b16 v[182:183], v36 offset:16384\n"
    "ds_read_b64_tr_b16 v[186:187], v37 offset:16384\n"
    "ds_read_b64_tr_b16 v[214:215], v38 offset:16384\n"
    "ds_read_b64_tr_b16 v[218:219], v39 offset:16384\n"
    "s_waitcnt vmcnt(26) lgkmcnt(0)\n"
    "s_barrier\n"
    "v_mfma_f32_16x16x32_bf16 v[24:27], v[72:75], v[116:119], 0\n"
    "v_mfma_f32_16x16x32_bf16 v[28:31], v[72:75], v[120:123], 0\n"
    "v_mfma_f32_16x16x32_bf16 v[24:27], v[72:75], v[148:151], v[24:27]\n"
    "v_mfma_f32_16x16x32_bf16 v[28:31], v[72:75], v[152:155], v[28:31]\n"
    "v_mfma_f32_16x16x32_bf16 v[24:27], v[72:75], v[180:183], v[24:27]\n"
    "v_mfma_f32_16x16x32_bf16 v[28:31], v[72:75], v[184:187], v[28:31]\n"
    "v_mfma_f32_16x16x32_bf16 v[24:27], v[72:75], v[212:215], v[24:27]\n"
    "v_mfma_f32_16x16x32_bf16 v[28:31], v[72:75], v[216:219], v[28:31]\n"
    "ds_read_b64_tr_b16 v[124:125], v32 offset:32768\n"
    "ds_read_b64_tr_b16 v[128:129], v33 offset:32768\n"
    "ds_read_b64_tr_b16 v[156:157], v34 offset:32768\n"
    "ds_read_b64_tr_b16 v[160:161], v35 offset:32768\n"
    "ds_read_b64_tr_b16 v[188:189], v36 offset:32768\n"
    "ds_read_b64_tr_b16 v[192:193], v37 offset:32768\n"
    "ds_read_b64_tr_b16 v[220:221], v38 offset:32768\n"
    "ds_read_b64_tr_b16 v[224:225], v39 offset:32768\n"
    "s_waitcnt vmcnt(22) lgkmcnt(0)\n"
    "s_barrier\n"
    "ds_read_b64_tr_b16 v[126:127], v32 offset:49152\n"
    "ds_read_b64_tr_b16 v[130:131], v33 offset:49152\n"
    "ds_read_b64_tr_b16 v[158:159], v34 offset:49152\n"
    "ds_read_b64_tr_b16 v[162:163], v35 offset:49152\n"
    "ds_read_b64_tr_b16 v[190:191], v36 offset:49152\n"
    "ds_read_b64_tr_b16 v[194:195], v37 offset:49152\n"
    "ds_read_b64_tr_b16 v[222:223], v38 offset:49152\n"
    "ds_read_b64_tr_b16 v[226:227], v39 offset:49152\n"
    "s_waitcnt vmcnt(18) lgkmcnt(0)\n"
    "s_barrier\n"
    "v_mfma_f32_16x16x32_bf16 v[24:27], v[72:75], v[124:127], v[24:27]\n"
    "v_mfma_f32_16x16x32_bf16 v[28:31], v[72:75], v[128:131], v[28:31]\n"
    "v_mfma_f32_16x16x32_bf16 v[24:27], v[72:75], v[156:159], v[24:27]\n"
    "v_mfma_f32_16x16x32_bf16 v[28:31], v[72:75], v[160:163], v[28:31]\n"
    "v_mfma_f32_16x16x32_bf16 v[24:27], v[72:75], v[188:191], v[24:27]\n"
    "v_mfma_f32_16x16x32_bf16 v[28:31], v[72:75], v[192:195], v[28:31]\n"
    "v_mfma_f32_16x16x32_bf16 v[24:27], v[72:75], v[220:223], v[24:27]\n"
    "v_mfma_f32_16x16x32_bf16 v[28:31], v[72:75], v[224:227], v[28:31]\n"
    "ds_read_b64_tr_b16 v[132:133], v71\n"
    "ds_read_b64_tr_b16 v[136:137], v70\n"
    "ds_read_b64_tr_b16 v[164:165], v69\n"
    "ds_read_b64_tr_b16 v[168:169], v68\n"
    "ds_read_b64_tr_b16 v[196:197], v67\n"
    "ds_read_b64_tr_b16 v[200:201], v66\n"
    "ds_read_b64_tr_b16 v[228:229], v65\n"
    "ds_read_b64_tr_b16 v[232:233], v64\n"
    "s_waitcnt vmcnt(14) lgkmcnt(0)\n"
    "s_barrier\n"
    "ds_read_b64_tr_b16 v[134:135], v71 offset:16384\n"
    "ds_read_b64_tr_b16 v[138:139], v70 offset:16384\n"
    "ds_read_b64_tr_b16 v[166:167], v69 offset:16384\n"
    "ds_read_b64_tr_b16 v[170:171], v68 offset:16384\n"
    "ds_read_b64_tr_b16 v[198:199], v67 offset:16384\n"
    "ds_read_b64_tr_b16 v[202:203], v66 offset:16384\n"
    "ds_read_b64_tr_b16 v[230:231], v65 offset:16384\n"
    "ds_read_b64_tr_b16 v[234:235], v64 offset:16384\n"
    "s_waitcnt vmcnt(10) lgkmcnt(0)\n"
    "s_barrier\n"
    "v_mfma_f32_16x16x32_bf16 v[24:27], v[72:75], v[132:135], v[24:27]\n"
    "v_mfma_f32_16x16x32_bf16 v[28:31], v[72:75], v[136:139], v[28:31]\n"
    "v_mfma_f32_16x16x32_bf16 v[24:27], v[72:75], v[164:167], v[24:27]\n"
    "v_mfma_f32_16x16x32_bf16 v[28:31], v[72:75], v[168:171], v[28:31]\n"
    "v_mfma_f32_16x16x32_bf16 v[24:27], v[72:75], v[196:199], v[24:27]\n"
    "v_mfma_f32_16x16x32_bf16 v[28:31], v[72:75], v[200:203], v[28:31]\n"
    "v_mfma_f32_16x16x32_bf16 v[24:27], v[72:75], v[228:231], v[24:27]\n"
    "v_mfma_f32_16x16x32_bf16 v[28:31], v[72:75], v[232:235], v[28:31]\n"
    "ds_read_b64_tr_b16 v[140:141], v71 offset:32768\n"
    "ds_read_b64_tr_b16 v[144:145], v70 offset:32768\n"
    "ds_read_b64_tr_b16 v[172:173], v69 offset:32768\n"
    "ds_read_b64_tr_b16 v[176:177], v68 offset:32768\n"
    "ds_read_b64_tr_b16 v[204:205], v67 offset:32768\n"
    "ds_read_b64_tr_b16 v[208:209], v66 offset:32768\n"
    "ds_read_b64_tr_b16 v[236:237], v65 offset:32768\n"
    "ds_read_b64_tr_b16 v[240:241], v64 offset:32768\n"
    "s_waitcnt vmcnt(6) lgkmcnt(0)\n"
    "s_barrier\n"
    "ds_read_b64_tr_b16 v[142:143], v71 offset:49152\n"
    "ds_read_b64_tr_b16 v[146:147], v70 offset:49152\n"
    "ds_read_b64_tr_b16 v[174:175], v69 offset:49152\n"
    "ds_read_b64_tr_b16 v[178:179], v68 offset:49152\n"
    "ds_read_b64_tr_b16 v[206:207], v67 offset:49152\n"
    "ds_read_b64_tr_b16 v[210:211], v66 offset:49152\n"
    "ds_read_b64_tr_b16 v[238:239], v65 offset:49152\n"
    "ds_read_b64_tr_b16 v[242:243], v64 offset:49152\n"
    "s_waitcnt vmcnt(2) lgkmcnt(0)\n"
    "s_barrier\n"
    "v_mfma_f32_16x16x32_bf16 v[24:27], v[72:75], v[140:143], v[24:27]\n"
    "v_mfma_f32_16x16x32_bf16 v[28:31], v[72:75], v[144:147], v[28:31]\n"
    "v_mfma_f32_16x16x32_bf16 v[24:27], v[72:75], v[172:175], v[24:27]\n"
    "v_mfma_f32_16x16x32_bf16 v[28:31], v[72:75], v[176:179], v[28:31]\n"
    "v_mfma_f32_16x16x32_bf16 v[24:27], v[72:75], v[204:207], v[24:27]\n"
    "v_mfma_f32_16x16x32_bf16 v[28:31], v[72:75], v[208:211], v[28:31]\n"
    "v_mfma_f32_16x16x32_bf16 v[24:27], v[72:75], v[236:239], v[24:27]\n"
    "v_mfma_f32_16x16x32_bf16 v[28:31], v[72:75], v[240:243], v[28:31]\n"
    "ds_read_b64_tr_b16 v[244:245], v63\n"
    "ds_read_b64_tr_b16 v[248:249], v62\n"
    "ds_read_b64_tr_b16 v[246:247], v63 offset:4096\n"
    "ds_read_b64_tr_b16 v[250:251], v62 offset:4096\n"
    "ds_read_b64_tr_b16 v[252:253], v63 offset:8192\n"
    "ds_read_b64_tr_b16 v[76:77], v62 offset:8192\n"
    "ds_read_b64_tr_b16 v[254:255], v63 offset:12288\n"
    "ds_read_b64_tr_b16 v[78:79], v62 offset:12288\n"
    "s_waitcnt lgkmcnt(0)\n"
    "s_barrier\n"
    "v_mfma_f32_16x16x32_bf16 v[24:27], v[72:75], v[244:247], v[24:27]\n"
    "v_mfma_f32_16x16x32_bf16 v[28:31], v[72:75], v[248:251], v[28:31]\n"
    "v_mfma_f32_16x16x32_bf16 v[24:27], v[72:75], v[252:255], v[24:27]\n"
    "v_mfma_f32_16x16x32_bf16 v[28:31], v[72:75], v[76:79], v[28:31]\n"
    "ds_write_b128 v2, v[8:11] offset:8192\n"
    "ds_write_b128 v2, v[12:15] offset:12288\n"
    "ds_write_b128 v2, v[16:19] offset:16384\n"
    "ds_write_b128 v2, v[20:23] offset:20480\n"
    "s_nop 0\n"
    "v_mov_b32 v74, v24\n"
    "s_nop 2\n"
    "v_mov_b32 v75, v28\n"
    "s_waitcnt vmcnt(0)\n"
    "v_add_u32 v80, 1, v80\n"
    "v_add_u32 v81, 1, v81\n"
    "s_mov_b32 s52, 1048576\n"
    ".Lhgo_a%=:\n"
    "v_cmp_ne_u64 vcc, v[80:81], v[82:83]\n"
    "s_cbranch_vccz .Lhgo_x%=\n"
    "global_load_dwordx2 v[82:83], v4, %1 sc1\n"
    "s_sub_u32 s52, s52, 1\n"
    "s_waitcnt vmcnt(0)\n"
    "s_cbranch_scc0 .Lhgo_a%=\n"
    ".Lhgo_x%=:\n"
    "v_mbcnt_lo_u32_b32 v80, -1, 0\n"
    "v_mbcnt_hi_u32_b32 v80, -1, v80\n"
    "v_min_u32 v4, 5, v80\n"
    "v_lshlrev_b32 v4, 7, v4\n"
    "v_mov_b32 v7, 0x26c00\n"
    "ds_read_b128 v[80:83], v7\n"
    "v_mov_b32 v6, 0\n"
    "s_waitcnt lgkmcnt(0)\n"
    "v_readfirstlane_b32 s49, v81\n"
    "v_add_u32 v80, v80, v4\n"
    "s_branch .Lhchk_%=\n"
    ".Lhgo_%=:\n"
    "s_waitcnt lgkmcnt(1)\n"
    "v_mfma_f32_16x16x32_bf16 v[8:11], a[0:3], v[48:51], 0\n"
    "s_waitcnt vmcnt(2)\n"
    "ds_read_b128 v[56:59], v1 offset:2048\n"
    "v_add_u32 v7, 32, v7\n"
    "v_mfma_f32_16x16x32_bf16 v[12:15], a[4:7], v[48:51], 0\n"
    "ds_read_b128 v[80:83], v7\n"
    "v_mfma_f32_16x16x32_bf16 v[16:19], a[144:147], v[48:51], 0\n"
    "ds_read_b128 v[60:63], v1 offset:3072\n"
    "v_mul_f32 v24, v24, v68\n"
    "v_mfma_f32_16x16x32_bf16 v[20:23], a[148:151], v[48:51], 0\n"
    "v_mul_f32 v25, v25, v69\n"
    "v_cvt_pk_bf16_f32 v24, v24, v25\n"
    "s_waitcnt lgkmcnt(3)\n"
    "v_mfma_f32_16x16x32_bf16 v[8:11], a[8:11], v[52:55], v[8:11]\n"
    "v_mul_f32 v26, v26, v70\n"
    "v_mul_f32 v27, v27, v71\n"
    "v_mfma_f32_16x16x32_bf16 v[12:15], a[12:15], v[52:55], v[12:15]\n"
    "v_cvt_pk_bf16_f32 v25, v26, v27\n"
    "v_mul_f32 v28, v28, v68\n"
    "v_mfma_f32_16x16x32_bf16 v[16:19], a[152:155], v[52:55], v[16:19]\n"
    "v_mul_f32 v29, v29, v69\n"
    "v_cvt_pk_bf16_f32 v26, v28, v29\n"
    "v_mfma_f32_16x16x32_bf16 v[20:23], a[156:159], v[52:55], v[20:23]\n"
    "v_mul_f32 v30, v30, v70\n"
    "v_mul_f32 v31, v31, v71\n"
    "s_waitcnt lgkmcnt(2)\n"
    "v_mfma_f32_16x16x32_bf16 v[8:11], a[16:19], v[56:59], v[8:11]\n"
    "v_cvt_pk_bf16_f32 v27, v30, v31\n"
    "v_mfma_f32_16x16x32_bf16 v[12:15], a[20:23], v[56:59], v[12:15]\n"
    "buffer_store_dwordx4 v[24:27], v2, s[40:43], s64 offen sc1\n"
    "s_waitcnt lgkmcnt(1)\n"
    "v_readfirstlane_b32 s49, v81\n"
    "v_mfma_f32_16x16x32_bf16 v[16:19], a[160:163], v[56:59], v[16:19]\n"
    "v_readfirstlane_b32 s62, v82\n"
    "v_add_u32 v80, v80, v4\n"
    ".Lhtop_%=:\n"
    "v_mfma_f32_16x16x32_bf16 v[20:23], a[164:167], v[56:59], v[20:23]\n"
    "global_load_dword v81, v80, %1 sc1\n"
    "v_xor_b32 v0, 0x2000, v1\n"
    "v_mfma_f32_16x16x32_bf16 v[24:27], v[48:51], v[116:119], 0\n"
    "s_barrier\n"
    "ds_read_b128 v[32:35], v0\n"
    "v_mfma_f32_16x16x32_bf16 v[28:31], v[48:51], v[120:123], 0\n"
    "ds_read2st64_b32 v[64:65], v5 offset1:1\n"
    "v_cmp_eq_u32 vcc, 0, v6\n"
    "s_cmp_eq_u64 vcc, 0\n"
    "v_mfma_f32_16x16x32_bf16 v[24:27], v[52:55], v[124:127], v[24:27]\n"
    "ds_read2st64_b32 v[66:67], v5 offset0:2 offset1:3\n"
    "s_cselect_b32 s94, s42, 0\n"
    "s_cselect_b32 s90, s96, 0\n"
    "v_mfma_f32_16x16x32_bf16 v[28:31], v[52:55], v[128:131], v[28:31]\n"
    "ds_read2st64_b32 v[68:69], v5 offset0:4 offset1:5\n"
    "s_xor_b32 s58, s58, 0x8000\n"
    "v_mfma_f32_16x16x32_bf16 v[24:27], v[56:59], v[132:135], v[24:27]\n"
    "ds_read2st64_b32 v[70:71], v5 offset0:6 offset1:7\n"
    "s_xor_b32 s59, s59, 0x8000\n"
    "v_mfma_f32_16x16x32_bf16 v[28:31], v[56:59], v[136:139], v[28:31]\n"
    "ds_read_b32 v72, v5 offset:2048\n"
    "s_add_u32 s63, s62, 0x2000000\n"
    "s_waitcnt lgkmcnt(6)\n"
    "v_mfma_f32_16x16x32_bf16 v[8:11], a[24:27], v[60:63], v[8:11]\n"
    "ds_read_b128 v[36:39], v0 offset:1024\n"
    "s_add_u32 s64, s62, 0x4000000\n"
    "v_mfma_f32_16x16x32_bf16 v[12:15], a[28:31], v[60:63], v[12:15]\n"
    "ds_read_b128 v[40:43], v0 offset:2048\n"
    "v_mfma_f32_16x16x32_bf16 v[16:19], a[168:171], v[60:63], v[16:19]\n"
    "ds_read_b128 v[44:47], v0 offset:3072\n"
    "s_mov_b32 m0, s58\n"
    "v_mfma_f32_16x16x32_bf16 v[20:23], a[172:175], v[60:63], v[20:23]\n"
    "buffer_load_dwordx4 v3, s[92:95], s49 offen sc1 lds\n"
    "v_mfma_f32_16x16x32_bf16 v[24:27], v[60:63], v[140:143], v[24:27]\n"
    "s_waitcnt lgkmcnt(7)\n"
    "v_add_f32 v64, v64, v65\n"
    "s_waitcnt lgkmcnt(6)\n"
    "v_add_f32 v66, v66, v67\n"
    "v_mfma_f32_16x16x32_bf16 v[28:31], v[60:63], v[144:147], v[28:31]\n"
    "s_waitcnt lgkmcnt(5)\n"
    "v_add_f32 v68, v68, v69\n"
    "s_waitcnt lgkmcnt(4)\n"
    "v_add_f32 v70, v70, v71\n"
    "v_mfma_f32_16x16x32_bf16 v[8:11], a[32:35], v[32:35], v[8:11]\n"
    "v_add_f32 v64, v64, v66\n"
    "v_add_f32 v68, v68, v70\n"
    "v_mfma_f32_16x16x32_bf16 v[12:15], a[36:39], v[32:35], v[12:15]\n"
    "buffer_load_dwordx4 v3, s[92:95], s49 offen offset:1024 sc1 lds\n"
    "s_waitcnt lgkmcnt(3)\n"
    "v_add_f32 v64, v64, v72\n"
    "v_mfma_f32_16x16x32_bf16 v[16:19], a[176:179], v[32:35], v[16:19]\n"
    "v_add_f32 v72, v64, v68\n"
    "v_add_f32 v64, v64, v68\n"
    "v_mfma_f32_16x16x32_bf16 v[20:23], a[180:183], v[32:35], v[20:23]\n"
    "v_xor_b32 v0, 0x4000, v1\n"
    "v_permlane32_swap_b32 v64, v72\n"
    "v_mfma_f32_16x16x32_bf16 v[24:27], v[32:35], v[148:151], v[24:27]\n"
    "v_add_f32 v65, v64, v72\n"
    "v_mfma_f32_16x16x32_bf16 v[28:31], v[32:35], v[152:155], v[28:31]\n"
    "ds_read_b128 v[32:35], v0\n"
    "v_add_f32 v72, v72, v64\n"
    "s_waitcnt lgkmcnt(3)\n"
    "v_mfma_f32_16x16x32_bf16 v[8:11], a[40:43], v[36:39], v[8:11]\n"
    "buffer_load_dwordx4 v3, s[92:95], s49 offen offset:2048 sc1 lds\n"
    "v_permlane16_swap_b32 v65, v72\n"
    "v_mfma_f32_16x16x32_bf16 v[12:15], a[44:47], v[36:39], v[12:15]\n"
    "v_mul_f32 v64, 0xbae38e39, v65\n"
    "v_mfma_f32_16x16x32_bf16 v[16:19], a[184:187], v[36:39], v[16:19]\n"
    "v_mul_f32 v72, 0x3ae38e39, v72\n"
    "v_cvt_pk_bf16_f32 v73, 0, v64\n"
    "v_mfma_f32_16x16x32_bf16 v[20:23], a[188:191], v[36:39], v[20:23]\n"
    "v_fma_f32 v72, -v64, v64, v72\n"
    "v_max_f32 v72, 0, v72\n"
    "v_mfma_f32_16x16x32_bf16 v[24:27], v[36:39], v[156:159], v[24:27]\n"
    "buffer_load_dwordx4 v3, s[92:95], s49 offen offset:3072 sc1 lds\n"
    "v_cvt_pk_bf16_f32 v72, 0, v72\n"
    "v_mfma_f32_16x16x32_bf16 v[28:31], v[36:39], v[160:163], v[28:31]\n"
    "ds_read_b128 v[36:39], v0 offset:1024\n"
    "v_add_f32 v72, 0x3727c5ac, v72\n"
    "s_waitcnt lgkmcnt(3)\n"
    "v_mfma_f32_16x16x32_bf16 v[8:11], a[48:51], v[40:43], v[8:11]\n"
    "v_cvt_pk_bf16_f32 v72, 0, v72\n"
    "v_sqrt_f32 v72, v72\n"
    "v_mfma_f32_16x16x32_bf16 v[12:15], a[52:55], v[40:43], v[12:15]\n"
    "s_bitset1_b32 m0, 12\n"
    "v_lshrrev_b32 v68, 2, v5\n"
    "v_mfma_f32_16x16x32_bf16 v[16:19], a[192:195], v[40:43], v[16:19]\n"
    "buffer_load_dwordx4 v2, s[88:91], s49 offen sc1 lds\n"
    "v_cvt_pk_bf16_f32 v72, 0, v72\n"
    "v_mfma_f32_16x16x32_bf16 v[20:23], a[196:199], v[40:43], v[20:23]\n"
    "v_rcp_f32 v72, v72\n"
    "v_and_b32 v68, 48, v68\n"
    "v_mfma_f32_16x16x32_bf16 v[24:27], v[40:43], v[164:167], v[24:27]\n"
    "ds_write2st64_b32 v5, v73, v72 offset0:9 offset1:10\n"
    "v_bfi_b32 v68, s86, v68, v5\n"
    "v_mfma_f32_16x16x32_bf16 v[28:31], v[40:43], v[168:171], v[28:31]\n"
    "ds_read_b128 v[40:43], v0 offset:2048\n"
    "s_mov_b32 m0, s59\n"
    "s_mov_b32 s55, s49\n"
    "s_bitset1_b32 s55, 15\n"
    "s_waitcnt lgkmcnt(4)\n"
    "v_mfma_f32_16x16x32_bf16 v[8:11], a[56:59], v[44:47], v[8:11]\n"
    "s_mov_b64 exec, s[60:61]\n"
    "buffer_load_dwordx4 v2, s[92:95], s55 offen offset:2048 sc1 lds\n"
    "s_mov_b64 exec, -1\n"
    "v_mfma_f32_16x16x32_bf16 v[12:15], a[60:63], v[44:47], v[12:15]\n"
    "ds_read_b128 v[64:67], v68 offset:2304\n"
    "v_mfma_f32_16x16x32_bf16 v[16:19], a[200:203], v[44:47], v[16:19]\n"
    "ds_read_b128 v[68:71], v68 offset:2560\n"
    "v_mfma_f32_16x16x32_bf16 v[20:23], a[204:207], v[44:47], v[20:23]\n"
    "ds_read_b128 v[48:51], v2 offset:8192\n"
    "v_mfma_f32_16x16x32_bf16 v[24:27], v[44:47], v[172:175], v[24:27]\n"
    "ds_read_b128 v[52:55], v2 offset:12288\n"
    "v_mfma_f32_16x16x32_bf16 v[28:31], v[44:47], v[176:179], v[28:31]\n"
    "ds_read_b128 v[44:47], v0 offset:3072\n"
    "s_waitcnt lgkmcnt(8)\n"
    "v_mfma_f32_16x16x32_bf16 v[8:11], a[64:67], v[32:35], v[8:11]\n"
    "ds_read_b128 v[56:59], v2 offset:16384\n"
    "v_mfma_f32_16x16x32_bf16 v[12:15], a[68:71], v[32:35], v[12:15]\n"
    "ds_read_b128 v[60:63], v2 offset:20480\n"
    "v_mfma_f32_16x16x32_bf16 v[16:19], a[208:211], v[32:35], v[16:19]\n"
    "s_waitcnt vmcnt(6)\n"
    "v_mov_b32 v82, 1\n"
    "v_mfma_f32_16x16x32_bf16 v[20:23], a[212:215], v[32:35], v[20:23]\n"
    "v_xor_b32 v0, 0x6000, v1\n"
    "v_mfma_f32_16x16x32_bf16 v[24:27], v[32:35], v[180:183], v[24:27]\n"
    "s_mov_b64 exec, s[56:57]\n"
    "ds_add_rtn_u32 v82, v7, v82 offset:16\n"
    "s_mov_b64 exec, -1\n"
    "v_mfma_f32_16x16x32_bf16 v[28:31], v[32:35], v[184:187], v[28:31]\n"
    "ds_read_b128 v[32:35], v0\n"
    "s_waitcnt lgkmcnt(10)\n"
    "v_mfma_f32_16x16x32_bf16 v[8:11], a[72:75], v[36:39], v[8:11]\n"
    "ds_read_b32 v6, v7 offset:32\n"
    "v_mfma_f32_16x16x32_bf16 v[12:15], a[76:79], v[36:39], v[12:15]\n"
    "v_mfma_f32_16x16x32_bf16 v[16:19], a[216:219], v[36:39], v[16:19]\n"
    "s_waitcnt lgkmcnt(8)\n"
    "v_fmac_f32 v24, v64, v74\n"
    "v_fmac_f32 v25, v65, v74\n"
    "v_mfma_f32_16x16x32_bf16 v[20:23], a[220:223], v[36:39], v[20:23]\n"
    "s_waitcnt lgkmcnt(6)\n"
    "v_fmac_f32 v8, v48, v73\n"
    "v_fmac_f32 v9, v49, v73\n"
    "v_mfma_f32_16x16x32_bf16 v[24:27], v[36:39], v[188:191], v[24:27]\n"
    "s_waitcnt lgkmcnt(2)\n"
    "v_readfirstlane_b32 s50, v82\n"
    "v_fmac_f32 v10, v50, v73\n"
    "v_mfma_f32_16x16x32_bf16 v[28:31], v[36:39], v[192:195], v[28:31]\n"
    "ds_read_b128 v[36:39], v0 offset:1024\n"
    "s_cmp_eq_u32 s50, 3\n"
    "v_mfma_f32_16x16x32_bf16 v[8:11], a[80:83], v[40:43], v[8:11]\n"
    "s_cselect_b64 exec, 1, 0\n"
    "global_store_dword v83, v82, %1 sc1\n"
    "s_mov_b64 exec, -1\n"
    "v_mfma_f32_16x16x32_bf16 v[12:15], a[84:87], v[40:43], v[12:15]\n"
    "s_cmp_eq_u32 s94, 0\n"
    "s_cbranch_scc1 .Lhk1_%=\n"
    ".Lhk1_x%=:\n"
    "v_mfma_f32_16x16x32_bf16 v[16:19], a[224:227], v[40:43], v[16:19]\n"
    "s_waitcnt lgkmcnt(1)\n"
    "v_add_u32 v6, v6, v4\n"
    "v_fmac_f32 v26, v66, v74\n"
    "v_mfma_f32_16x16x32_bf16 v[20:23], a[228:231], v[40:43], v[20:23]\n"
    "global_load_dword v6, v6, %1 sc1\n"
    "v_fmac_f32 v11, v51, v73\n"
    "v_mfma_f32_16x16x32_bf16 v[24:27], v[40:43], v[196:199], v[24:27]\n"
    "v_fmac_f32 v12, v52, v73\n"
    "v_fmac_f32 v13, v53, v73\n"
    "v_mfma_f32_16x16x32_bf16 v[28:31], v[40:43], v[200:203], v[28:31]\n"
    "ds_read_b128 v[40:43], v0 offset:2048\n"
    "v_fmac_f32 v14, v54, v73\n"
    "v_mfma_f32_16x16x32_bf16 v[8:11], a[88:91], v[44:47], v[8:11]\n"
    "v_fmac_f32 v16, v56, v73\n"
    "v_fmac_f32 v17, v57, v73\n"
    "v_mfma_f32_16x16x32_bf16 v[12:15], a[92:95], v[44:47], v[12:15]\n"
    "v_fmac_f32 v20, v60, v73\n"
    "v_fmac_f32 v21, v61, v73\n"
    "v_mfma_f32_16x16x32_bf16 v[16:19], a[232:235], v[44:47], v[16:19]\n"
    "v_fmac_f32 v27, v67, v74\n"
    "v_fmac_f32 v28, v64, v75\n"
    "v_mfma_f32_16x16x32_bf16 v[20:23], a[236:239], v[44:47], v[20:23]\n"
    "v_fmac_f32 v29, v65, v75\n"
    "v_fmac_f32 v30, v66, v75\n"
    "v_mfma_f32_16x16x32_bf16 v[24:27], v[44:47], v[204:207], v[24:27]\n"
    "v_fmac_f32 v15, v55, v73\n"
    "v_mfma_f32_16x16x32_bf16 v[28:31], v[44:47], v[208:211], v[28:31]\n"
    "ds_read_b128 v[44:47], v0 offset:3072\n"
    "v_fmac_f32 v18, v58, v73\n"
    "v_mfma_f32_16x16x32_bf16 v[8:11], a[96:99], v[32:35], v[8:11]\n"
    "v_fmac_f32 v19, v59, v73\n"
    "v_fmac_f32 v22, v62, v73\n"
    "v_mfma_f32_16x16x32_bf16 v[12:15], a[100:103], v[32:35], v[12:15]\n"
    "v_fmac_f32 v23, v63, v73\n"
    "v_mfma_f32_16x16x32_bf16 v[16:19], a[240:243], v[32:35], v[16:19]\n"
    "v_fmac_f32 v31, v67, v75\n"
    "v_mfma_f32_16x16x32_bf16 v[20:23], a[244:247], v[32:35], v[20:23]\n"
    "v_and_b32 v0, 0xffff9fff, v1\n"
    "v_mfma_f32_16x16x32_bf16 v[24:27], v[32:35], v[212:215], v[24:27]\n"
    "v_mfma_f32_16x16x32_bf16 v[28:31], v[32:35], v[216:219], v[28:31]\n"
    "ds_read_b128 v[32:35], v0 offset:4096\n"
    "s_waitcnt lgkmcnt(3)\n"
    "v_mfma_f32_16x16x32_bf16 v[8:11], a[104:107], v[36:39], v[8:11]\n"
    "v_mfma_f32_16x16x32_bf16 v[12:15], a[108:111], v[36:39], v[12:15]\n"
    "v_mfma_f32_16x16x32_bf16 v[16:19], a[248:251], v[36:39], v[16:19]\n"
    "s_cmp_eq_u32 s94, 0\n"
    "s_cbranch_scc1 .Lhk2_%=\n"
    ".Lhk2_x%=:\n"
    "v_mfma_f32_16x16x32_bf16 v[20:23], a[252:255], v[36:39], v[20:23]\n"
    "v_mfma_f32_16x16x32_bf16 v[24:27], v[36:39], v[220:223], v[24:27]\n"
    "v_mfma_f32_16x16x32_bf16 v[28:31], v[36:39], v[224:227], v[28:31]\n"
    "ds_read_b128 v[36:39], v0 offset:12288\n"
    "s_waitcnt lgkmcnt(3)\n"
    "v_mfma_f32_16x16x32_bf16 v[8:11], a[112:115], v[40:43], v[8:11]\n"
    "v_mfma_f32_16x16x32_bf16 v[12:15], a[116:119], v[40:43], v[12:15]\n"
    "v_xor_b32 v1, 0x8000, v1\n"
    "v_xor_b32 v5, 0x8000, v5\n"
    "s_waitcnt lgkmcnt(2)\n"
    "v_mfma_f32_16x16x32_bf16 v[8:11], a[120:123], v[44:47], v[8:11]\n"
    "v_mfma_f32_16x16x32_bf16 v[12:15], a[124:127], v[44:47], v[12:15]\n"
    "s_waitcnt lgkmcnt(1)\n"
    "v_mfma_f32_16x16x32_bf16 v[8:11], a[128:131], v[32:35], v[8:11]\n"
    "v_mfma_f32_16x16x32_bf16 v[12:15], a[132:135], v[32:35], v[12:15]\n"
    "s_waitcnt lgkmcnt(0)\n"
    "v_mfma_f32_16x16x32_bf16 v[8:11], a[136:139], v[36:39], v[8:11]\n"
    "v_mfma_f32_16x16x32_bf16 v[12:15], a[140:143], v[36:39], v[12:15]\n"
    "v_mfma_f32_16x16x32_bf16 v[16:19], v[84:87], v[40:43], v[16:19]\n"
    "s_waitcnt vmcnt(4)\n"
    "ds_read_b128 v[48:51], v1\n"
    "v_mfma_f32_16x16x32_bf16 v[20:23], v[88:91], v[40:43], v[20:23]\n"
    "v_mul_f32 v8, v8, v72\n"
    "v_mul_f32 v12, v12, v72\n"
    "v_mfma_f32_16x16x32_bf16 v[16:19], v[92:95], v[44:47], v[16:19]\n"
    "v_cvt_pk_bf16_f32 v8, v8, v12\n"
    "v_mul_f32 v9, v9, v72\n"
    "v_mfma_f32_16x16x32_bf16 v[20:23], v[96:99], v[44:47], v[20:23]\n"
    "v_mul_f32 v13, v13, v72\n"
    "v_cvt_pk_bf16_f32 v9, v9, v13\n"
    "v_mfma_f32_16x16x32_bf16 v[16:19], v[100:103], v[32:35], v[16:19]\n"
    "v_mul_f32 v10, v10, v72\n"
    "v_mul_f32 v14, v14, v72\n"
    "v_mfma_f32_16x16x32_bf16 v[20:23], v[104:107], v[32:35], v[20:23]\n"
    "v_cvt_pk_bf16_f32 v10, v10, v14\n"
    "v_mul_f32 v11, v11, v72\n"
    "v_mfma_f32_16x16x32_bf16 v[16:19], v[108:111], v[36:39], v[16:19]\n"
    "v_mul_f32 v15, v15, v72\n"
    "v_cvt_pk_bf16_f32 v11, v11, v15\n"
    "v_mfma_f32_16x16x32_bf16 v[20:23], v[112:115], v[36:39], v[20:23]\n"
    "buffer_store_dwordx4 v[8:11], v2, s[40:43], s62 offen sc1\n"
    "v_mfma_f32_16x16x32_bf16 v[24:27], v[40:43], v[228:231], v[24:27]\n"
    "ds_read_b128 v[52:55], v1 offset:1024\n"
    "v_mfma_f32_16x16x32_bf16 v[28:31], v[40:43], v[232:235], v[28:31]\n"
    "v_mul_f32 v16, v16, v72\n"
    "v_mul_f32 v20, v20, v72\n"
    "v_mfma_f32_16x16x32_bf16 v[24:27], v[44:47], v[236:239], v[24:27]\n"
    "v_cvt_pk_bf16_f32 v16, v16, v20\n"
    "v_mul_f32 v17, v17, v72\n"
    "v_mfma_f32_16x16x32_bf16 v[28:31], v[44:47], v[240:243], v[28:31]\n"
    "v_mul_f32 v21, v21, v72\n"
    "v_cvt_pk_bf16_f32 v17, v17, v21\n"
    "v_mfma_f32_16x16x32_bf16 v[24:27], v[32:35], v[244:247], v[24:27]\n"
    "v_mul_f32 v18, v18, v72\n"
    "v_mul_f32 v22, v22, v72\n"
    "v_mfma_f32_16x16x32_bf16 v[28:31], v[32:35], v[248:251], v[28:31]\n"
    "v_cvt_pk_bf16_f32 v18, v18, v22\n"
    "v_mul_f32 v19, v19, v72\n"
    "v_mfma_f32_16x16x32_bf16 v[24:27], v[36:39], v[252:255], v[24:27]\n"
    "v_mul_f32 v23, v23, v72\n"
    "v_cvt_pk_bf16_f32 v19, v19, v23\n"
    "v_mfma_f32_16x16x32_bf16 v[28:31], v[36:39], v[76:79], v[28:31]\n"
    "buffer_store_dwordx4 v[16:19], v2, s[40:43], s63 offen sc1\n"
    "s_mov_b32 s56, 1\n"
    "s_cmp_lg_u32 s94, 0\n"
    "s_cbranch_scc1 .Lhgo_%=\n"
    "s_nop 0\n"
    "v_mul_f32 v24, v24, v68\n"
    "v_mul_f32 v25, v25, v69\n"
    "v_cvt_pk_bf16_f32 v24, v24, v25\n"
    "v_mul_f32 v26, v26, v70\n"
    "v_mul_f32 v27, v27, v71\n"
    "v_cvt_pk_bf16_f32 v25, v26, v27\n"
    "v_mul_f32 v28, v28, v68\n"
    "v_mul_f32 v29, v29, v69\n"
    "v_cvt_pk_bf16_f32 v26, v28, v29\n"
    "v_mul_f32 v30, v30, v70\n"
    "v_mul_f32 v31, v31, v71\n"
    "v_cvt_pk_bf16_f32 v27, v30, v31\n"
    "buffer_store_dwordx4 v[24:27], v2, s[40:43], s64 offen sc1\n"
    "s_waitcnt vmcnt(3)\n"
    "v_cmp_eq_u32 vcc, 0, v81\n"
    "s_cmp_eq_u64 vcc, 0\n"
    "s_cbranch_scc1 .Lhld_%=\n"
    "ds_read_b32 v83, v7 offset:44\n"
    "global_load_dword v81, v80, %1 sc1\n"
    "s_waitcnt vmcnt(0)\n"
    "v_mov_b32 v82, 1\n"
    "s_mov_b64 exec, 1\n"
    "ds_add_rtn_u32 v82, v7, v82 offset:48\n"
    "s_mov_b64 exec, -1\n"
    "s_waitcnt lgkmcnt(0)\n"
    "v_readfirstlane_b32 s50, v82\n"
    "s_cmp_eq_u32 s50, 3\n"
    "s_cselect_b64 exec, 1, 0\n"
    "global_store_dword v83, v82, %1 sc1\n"
    "s_mov_b64 exec, -1\n"
    "s_mov_b32 s56, 0\n"
    "v_cmp_eq_u32 vcc, 0, v81\n"
    "s_cmp_eq_u64 vcc, 0\n"
    "s_cbranch_scc1 .Lhld_%=\n"
    ".Lhchk_%=:\n"
    "v_readfirstlane_b32 s50, v80\n"
    "s_cmp_lt_u32 s50, 200\n"
    "s_cbranch_scc1 .Lhend_%=\n"
    ".Lhwait_%=:\n"
    "global_load_dword v81, v80, %1 sc1\n"
    "s_mov_b32 s52, 0\n"
    ".Lhp3_%=:\n"
    "s_waitcnt vmcnt(0)\n"
    "v_cmp_eq_u32 vcc, 0, v81\n"
    "s_cmp_eq_u64 vcc, 0\n"
    "s_cbranch_scc1 .Lhp3_x%=\n"
    "s_add_u32 s52, s52, 1\n"
    "s_sleep 2\n"
    "global_load_dword v81, v80, %1 sc1\n"
    "s_cmp_lt_u32 s52, s53\n"
    "s_cbranch_scc1 .Lhp3_%=\n"
    "s_mov_b32 s53, 0\n"
    "v_mov_b32 v82, 0\n"
    "v_mov_b32 v83, 1\n"
    "global_store_dword v82, v83, %1 sc1\n"
    "s_waitcnt vmcnt(0)\n"
    ".Lhp3_x%=:\n"
    ".Lhld_%=:\n"
    "s_mov_b32 s94, s42\n"
    "s_mov_b32 s90, s96\n"
    "s_mov_b32 m0, s58\n"
    "s_nop 0\n"
    "buffer_load_dwordx4 v3, s[92:95], s49 offen sc1 lds\n"
    "buffer_load_dwordx4 v3, s[92:95], s49 offen offset:1024 sc1 lds\n"
    "buffer_load_dwordx4 v3, s[92:95], s49 offen offset:2048 sc1 lds\n"
    "buffer_load_dwordx4 v3, s[92:95], s49 offen offset:3072 sc1 lds\n"
    "s_bitset1_b32 m0, 12\n"
    "s_nop 0\n"
    "buffer_load_dwordx4 v2, s[88:91], s49 offen sc1 lds\n"
    "s_mov_b32 m0, s59\n"
    "s_mov_b32 s55, s49\n"
    "s_bitset1_b32 s55, 15\n"
    "s_mov_b64 exec, s[60:61]\n"
    "buffer_load_dwordx4 v2, s[92:95], s55 offen offset:2048 sc1 lds\n"
    "s_mov_b64 exec, -1\n"
    "s_waitcnt vmcnt(4)\n"
    "ds_read_b128 v[48:51], v1\n"
    "ds_read_b128 v[52:55], v1 offset:1024\n"
    ".Lhin_%=:\n"
    "s_waitcnt lgkmcnt(1)\n"
    "v_mfma_f32_16x16x32_bf16 v[8:11], a[0:3], v[48:51], 0\n"
    "s_waitcnt vmcnt(0)\n"
    "ds_read_b128 v[56:59], v1 offset:2048\n"
    "v_add_u32 v7, 32, v7\n"
    "v_mfma_f32_16x16x32_bf16 v[12:15], a[4:7], v[48:51], 0\n"
    "ds_read_b128 v[80:83], v7\n"
    "v_mfma_f32_16x16x32_bf16 v[16:19], a[144:147], v[48:51], 0\n"
    "ds_read_b128 v[60:63], v1 offset:3072\n"
    "v_mfma_f32_16x16x32_bf16 v[20:23], a[148:151], v[48:51], 0\n"
    "s_waitcnt lgkmcnt(3)\n"
    "v_mfma_f32_16x16x32_bf16 v[8:11], a[8:11], v[52:55], v[8:11]\n"
    "v_mfma_f32_16x16x32_bf16 v[12:15], a[12:15], v[52:55], v[12:15]\n"
    "v_mfma_f32_16x16x32_bf16 v[16:19], a[152:155], v[52:55], v[16:19]\n"
    "v_mfma_f32_16x16x32_bf16 v[20:23], a[156:159], v[52:55], v[20:23]\n"
    "s_waitcnt lgkmcnt(2)\n"
    "v_mfma_f32_16x16x32_bf16 v[8:11], a[16:19], v[56:59], v[8:11]\n"
    "v_mfma_f32_16x16x32_bf16 v[12:15], a[20:23], v[56:59], v[12:15]\n"
    "s_waitcnt lgkmcnt(1)\n"
    "v_readfirstlane_b32 s49, v81\n"
    "v_readfirstlane_b32 s62, v82\n"
    "v_mfma_f32_16x16x32_bf16 v[16:19], a[160:163], v[56:59], v[16:19]\n"
    "v_add_u32 v80, v80, v4\n"
    "s_cmp_eq_u32 s56, 0\n"
    "s_cbranch_scc1 .Lhc4_%=\n"
    "s_waitcnt vmcnt(0)\n"
    "v_mov_b32 v82, 1\n"
    "s_mov_b64 exec, 1\n"
    "ds_add_rtn_u32 v82, v7, v82 offset:16\n"
    "s_mov_b64 exec, -1\n"
    "s_waitcnt lgkmcnt(0)\n"
    "v_readfirstlane_b32 s50, v82\n"
    "s_cmp_eq_u32 s50, 3\n"
    "s_cselect_b64 exec, 1, 0\n"
    "global_store_dword v83, v82, %1 sc1\n"
    "s_mov_b64 exec, -1\n"
    "s_mov_b32 s56, 0\n"
    ".Lhc4_%=:\n"
    "s_branch .Lhtop_%=\n"
    ".Lhk1_%=:\n"
    "s_waitcnt vmcnt(6)\n"
    "v_cmp_eq_u32 vcc, 0, v81\n"
    "s_cmp_eq_u64 vcc, 0\n"
    "s_cselect_b32 s94, s42, 0\n"
    "s_cselect_b32 s90, s96, 0\n"
    "global_load_dword v81, v80, %1 sc1\n"
    "s_mov_b32 m0, s58\n"
    "s_nop 0\n"
    "buffer_load_dwordx4 v3, s[92:95], s49 offen sc1 lds\n"
    "buffer_load_dwordx4 v3, s[92:95], s49 offen offset:1024 sc1 lds\n"
    "buffer_load_dwordx4 v3, s[92:95], s49 offen offset:2048 sc1 lds\n"
    "buffer_load_dwordx4 v3, s[92:95], s49 offen offset:3072 sc1 lds\n"
    "s_bitset1_b32 m0, 12\n"
    "s_nop 0\n"
    "buffer_load_dwordx4 v2, s[88:91], s49 offen sc1 lds\n"
    "s_mov_b32 m0, s59\n"
    "s_mov_b32 s55, s49\n"
    "s_bitset1_b32 s55, 15\n"
    "s_mov_b64 exec, s[60:61]\n"
    "buffer_load_dwordx4 v2, s[92:95], s55 offen offset:2048 sc1 lds\n"
    "s_mov_b64 exec, -1\n"
    "s_branch .Lhk1_x%=\n"
    ".Lhk2_%=:\n"
    "s_waitcnt vmcnt(6)\n"
    "v_cmp_eq_u32 vcc, 0, v81\n"
    "s_cmp_eq_u64 vcc, 0\n"
    "s_cselect_b32 s94, s42, 0\n"
    "s_cselect_b32 s90, s96, 0\n"
    "global_load_dword v81, v80, %1 sc1\n"
    "s_mov_b32 m0, s58\n"
    "s_nop 0\n"
    "buffer_load_dwordx4 v3, s[92:95], s49 offen sc1 lds\n"
    "buffer_load_dwordx4 v3, s[92:95], s49 offen offset:1024 sc1 lds\n"
    "buffer_load_dwordx4 v3, s[92:95], s49 offen offset:2048 sc1 lds\n"
    "buffer_load_dwordx4 v3, s[92:95], s49 offen offset:3072 sc1 lds\n"
    "s_bitset1_b32 m0, 12\n"
    "s_nop 0\n"
    "buffer_load_dwordx4 v2, s[88:91], s49 offen sc1 lds\n"
    "s_mov_b32 m0, s59\n"
    "s_mov_b32 s55, s49\n"
    "s_bitset1_b32 s55, 15\n"
    "s_mov_b64 exec, s[60:61]\n"
    "buffer_load_dwordx4 v2, s[92:95], s55 offen offset:2048 sc1 lds\n"
    "s_mov_b64 exec, -1\n"
    "s_branch .Lhk2_x%=\n"
    ".Lhpol_%=:\n"
    "v_mov_b32 v80, 64\n"
    "global_load_dword v81, v80, %1 sc1\n"
    "s_mov_b32 s52, 0\n"
    ".Lhp5_%=:\n"
    "s_waitcnt vmcnt(0)\n"
    "v_cmp_eq_u32 vcc, 0, v81\n"
    "s_cmp_eq_u64 vcc, 0\n"
    "s_cbranch_scc1 .Lhp5_x%=\n"
    "s_add_u32 s52, s52, 1\n"
    "s_sleep 2\n"
    "global_load_dword v81, v80, %1 sc1\n"
    "s_cmp_lt_u32 s52, s53\n"
    "s_cbranch_scc1 .Lhp5_%=\n"
    "s_mov_b32 s53, 0\n"
    "v_mov_b32 v82, 0\n"
    "v_mov_b32 v83, 1\n"
    "global_store_dword v82, v83, %1 sc1\n"
    "s_waitcnt vmcnt(0)\n"
    ".Lhp5_x%=:\n"
    "v_readfirstlane_b32 s50, v81\n"
    "v_mbcnt_lo_u32_b32 v80, -1, 0\n"
    "v_mbcnt_hi_u32_b32 v80, -1, v80\n"
    "s_lshl_b32 s51, %2, 6\n"
    "v_add_u32 v80, s51, v80\n"
    "v_min_u32 v80, 129, v80\n"
    "v_lshlrev_b32 v80, 5, v80\n"
    "v_add_u32 v80, 0x26c00, v80\n"
    "ds_read2_b32 v[82:83], v80 offset0:1 offset1:2\n"
    "s_and_b32 s50, s50, 1\n"
    "s_waitcnt lgkmcnt(0)\n"
    "v_and_b32 v81, s50, v82\n"
    "v_lshlrev_b32 v81, 13, v81\n"
    "v_and_b32 v82, -2, v82\n"
    "v_xor_b32 v82, v81, v82\n"
    "v_and_b32 v81, s50, v83\n"
    "v_lshlrev_b32 v81, 13, v81\n"
    "v_and_b32 v83, -2, v83\n"
    "v_xor_b32 v83, v81, v83\n"
    "ds_write2_b32 v80, v82, v83 offset0:1 offset1:2\n"
    "s_waitcnt lgkmcnt(0)\n"
    "s_barrier\n"
    "ds_read_b32 v81, v7 offset:4\n"
    "v_add_u32 v80, %4, v4\n"
    "s_waitcnt lgkmcnt(0)\n"
    "v_readfirstlane_b32 s49, v81\n"
    "s_branch .Lhwait_%=\n"
    ".Lhend_%=:\n"
    "s_cmp_eq_u32 s50, 192\n"
    "s_cbranch_scc0 .Lhpol_%=\n"
    "s_waitcnt vmcnt(0) lgkmcnt(0)\n"
    "s_endpgm\n"
    ".p2align 6\n"
    ".Lrole2_%=:\n"
    "s_mov_b64 s[40:41], %0\n"
    "s_and_b32 s41, s41, 0xffff\n"
    "s_mov_b32 s42, 0xc000000\n"
    "s_mov_b32 s43, 0x20000\n"
    "s_mov_b64 s[92:93], %0\n"
    "s_and_b32 s93, s93, 0xffff\n"
    "s_mov_b32 s94, 0xc000000\n"
    "s_mov_b32 s95, 0x20000\n"
    "s_mov_b32 s53, 131072\n"
    "s_mov_b64 s[56:57], 0\n"
    "v_mbcnt_lo_u32_b32 v7, -1, 0\n"
    "v_mbcnt_hi_u32_b32 v7, -1, v7\n"
    "s_lshl_b32 s50, %2, 8\n"
    "s_add_u32 s50, s50, 147456\n"
    "v_lshl_add_u32 v7, v7, 2, s50\n"
    "ds_read_b32 v1, v7\n"
    "ds_read_b32 v4, v7 offset:1024\n"
    "ds_read_b32 v2, v7 offset:2048\n"
    "ds_read_b32 v8, v7 offset:3072\n"
    "ds_read_b32 v9, v7 offset:4096\n"
    "ds_read_b32 v10, v7 offset:5120\n"
    "ds_read_b32 v11, v7 offset:6144\n"
    "ds_read_b32 v3, v7 offset:7168\n"
    "s_waitcnt lgkmcnt(0)\n"
    "s_barrier\n"
    "s_mul_i32 s54, %2, 6144\n"
    "v_add_u32 v5, 16384, v4\n"
    "s_lshl_b32 s58, %2, 13\n"
    "s_add_u32 s58, s58, 0\n"
    "s_xor_b32 s50, %2, 0\n"
    "s_mul_i32 s50, s50, 6144\n"
    "v_add_u32 v32, s50, v9\n"
    "v_add_u32 v33, s50, v10\n"
    "v_add_u32 v34, s50, v11\n"
    "s_xor_b32 s50, %2, 1\n"
    "s_mul_i32 s50, s50, 6144\n"
    "v_add_u32 v35, s50, v9\n"
    "v_add_u32 v36, s50, v10\n"
    "v_add_u32 v37, s50, v11\n"
    "s_xor_b32 s50, %2, 2\n"
    "s_mul_i32 s50, s50, 6144\n"
    "v_add_u32 v38, s50, v9\n"
    "v_add_u32 v39, s50, v10\n"
    "v_add_u32 v40, s50, v11\n"
    "s_xor_b32 s50, %2, 3\n"
    "s_mul_i32 s50, s50, 6144\n"
    "v_add_u32 v41, s50, v9\n"
    "v_add_u32 v42, s50, v10\n"
    "v_add_u32 v43, s50, v11\n"
    "s_mov_b64 s[44:45], %8\n"
    "s_and_b32 s45, s45, 0xffff\n"
    "s_mov_b32 s46, 0x480000\n"
    "s_mov_b32 s47, 0x20000\n"
    "s_add_u32 m0, s54, 0\n"
    "s_add_u32 s55, %6, 0\n"
    "buffer_load_dwordx4 v8, s[44:47], s55 offen lds\n"
    "s_add_u32 m0, s54, 1024\n"
    "s_add_u32 s55, %6, 18432\n"
    "buffer_load_dwordx4 v8, s[44:47], s55 offen lds\n"
    "s_add_u32 m0, s54, 2048\n"
    "s_add_u32 s55, %6, 128\n"
    "buffer_load_dwordx4 v8, s[44:47], s55 offen lds\n"
    "s_add_u32 m0, s54, 3072\n"
    "s_add_u32 s55, %6, 18560\n"
    "buffer_load_dwordx4 v8, s[44:47], s55 offen lds\n"
    "s_add_u32 m0, s54, 4096\n"
    "s_add_u32 s55, %6, 256\n"
    "buffer_load_dwordx4 v8, s[44:47], s55 offen lds\n"
    "s_add_u32 m0, s54, 5120\n"
    "s_add_u32 s55, %6, 18688\n"
    "buffer_load_dwordx4 v8, s[44:47], s55 offen lds\n"
    "s_add_u32 m0, s54, 24576\n"
    "s_add_u32 s55, %6, 2304\n"
    "buffer_load_dwordx4 v8, s[44:47], s55 offen lds\n"
    "s_add_u32 m0, s54, 25600\n"
    "s_add_u32 s55, %6, 20736\n"
    "buffer_load_dwordx4 v8, s[44:47], s55 offen lds\n"
    "s_add_u32 m0, s54, 26624\n"
    "s_add_u32 s55, %6, 2432\n"
    "buffer_load_dwordx4 v8, s[44:47], s55 offen lds\n"
    "s_add_u32 m0, s54, 27648\n"
    "s_add_u32 s55, %6, 20864\n"
    "buffer_load_dwordx4 v8, s[44:47], s55 offen lds\n"
    "s_add_u32 m0, s54, 28672\n"
    "s_add_u32 s55, %6, 2560\n"
    "buffer_load_dwordx4 v8, s[44:47], s55 offen lds\n"
    "s_add_u32 m0, s54, 29696\n"
    "s_add_u32 s55, %6, 20992\n"
    "buffer_load_dwordx4 v8, s[44:47], s55 offen lds\n"
    "s_add_u32 m0, s54, 49152\n"
    "s_add_u32 s55, %6, 36864\n"
    "buffer_load_dwordx4 v8, s[44:47], s55 offen lds\n"
    "s_add_u32 m0, s54, 50176\n"
    "s_add_u32 s55, %6, 55296\n"
    "buffer_load_dwordx4 v8, s[44:47], s55 offen lds\n"
    "s_add_u32 m0, s54, 51200\n"
    "s_add_u32 s55, %6, 36992\n"
    "buffer_load_dwordx4 v8, s[44:47], s55 offen lds\n"
    "s_add_u32 m0, s54, 52224\n"
    "s_add_u32 s55, %6, 55424\n"
    "buffer_load_dwordx4 v8, s[44:47], s55 offen lds\n"
    "s_add_u32 m0, s54, 53248\n"
    "s_add_u32 s55, %6, 37120\n"
    "buffer_load_dwordx4 v8, s[44:47], s55 offen lds\n"
    "s_add_u32 m0, s54, 54272\n"
    "s_add_u32 s55, %6, 55552\n"
    "buffer_load_dwordx4 v8, s[44:47], s55 offen lds\n"
    "s_add_u32 m0, s54, 73728\n"
    "s_add_u32 s55, %6, 39168\n"
    "buffer_load_dwordx4 v8, s[44:47], s55 offen lds\n"
    "s_add_u32 m0, s54, 74752\n"
    "s_add_u32 s55, %6, 57600\n"
    "buffer_load_dwordx4 v8, s[44:47], s55 offen lds\n"
    "s_add_u32 m0, s54, 75776\n"
    "s_add_u32 s55, %6, 39296\n"
    "buffer_load_dwordx4 v8, s[44:47], s55 offen lds\n"
    "s_add_u32 m0, s54, 76800\n"
    "s_add_u32 s55, %6, 57728\n"
    "buffer_load_dwordx4 v8, s[44:47], s55 offen lds\n"
    "s_add_u32 m0, s54, 77824\n"
    "s_add_u32 s55, %6, 39424\n"
    "buffer_load_dwordx4 v8, s[44:47], s55 offen lds\n"
    "s_add_u32 m0, s54, 78848\n"
    "s_add_u32 s55, %6, 57856\n"
    "buffer_load_dwordx4 v8, s[44:47], s55 offen lds\n"
    "s_add_u32 m0, s54, 98304\n"
    "s_add_u32 s55, %6, 73728\n"
    "buffer_load_dwordx4 v8, s[44:47], s55 offen lds\n"
    "s_add_u32 m0, s54, 99328\n"
    "s_add_u32 s55, %6, 92160\n"
    "buffer_load_dwordx4 v8, s[44:47], s55 offen lds\n"
    "s_add_u32 m0, s54, 100352\n"
    "s_add_u32 s55, %6, 73856\n"
    "buffer_load_dwordx4 v8, s[44:47], s55 offen lds\n"
    "s_add_u32 m0, s54, 101376\n"
    "s_add_u32 s55, %6, 92288\n"
    "buffer_load_dwordx4 v8, s[44:47], s55 offen lds\n"
    "s_add_u32 m0, s54, 102400\n"
    "s_add_u32 s55, %6, 73984\n"
    "buffer_load_dwordx4 v8, s[44:47], s55 offen lds\n"
    "s_add_u32 m0, s54, 103424\n"
    "s_add_u32 s55, %6, 92416\n"
    "buffer_load_dwordx4 v8, s[44:47], s55 offen lds\n"
    "s_add_u32 m0, s54, 122880\n"
    "s_add_u32 s55, %6, 76032\n"
    "buffer_load_dwordx4 v8, s[44:47], s55 offen lds\n"
    "s_add_u32 m0, s54, 123904\n"
    "s_add_u32 s55, %6, 94464\n"
    "buffer_load_dwordx4 v8, s[44:47], s55 offen lds\n"
    "s_add_u32 m0, s54, 124928\n"
    "s_add_u32 s55, %6, 76160\n"
    "buffer_load_dwordx4 v8, s[44:47], s55 offen lds\n"
    "s_add_u32 m0, s54, 125952\n"
    "s_add_u32 s55, %6, 94592\n"
    "buffer_load_dwordx4 v8, s[44:47], s55 offen lds\n"
    "s_add_u32 m0, s54, 126976\n"
    "s_add_u32 s55, %6, 76288\n"
    "buffer_load_dwordx4 v8, s[44:47], s55 offen lds\n"
    "s_add_u32 m0, s54, 128000\n"
    "s_add_u32 s55, %6, 94720\n"
    "buffer_load_dwordx4 v8, s[44:47], s55 offen lds\n"
    "s_waitcnt vmcnt(30) lgkmcnt(0)\n"
    "s_barrier\n"
    "ds_read_b64_tr_b16 a[0:1], v32\n"
    "ds_read_b64_tr_b16 a[4:5], v33\n"
    "ds_read_b64_tr_b16 a[8:9], v34\n"
    "ds_read_b64_tr_b16 a[96:97], v35\n"
    "ds_read_b64_tr_b16 a[100:101], v36\n"
    "ds_read_b64_tr_b16 a[104:105], v37\n"
    "ds_read_b64_tr_b16 a[192:193], v38\n"
    "ds_read_b64_tr_b16 a[196:197], v39\n"
    "ds_read_b64_tr_b16 a[200:201], v40\n"
    "ds_read_b64_tr_b16 v[160:161], v41\n"
    "ds_read_b64_tr_b16 v[164:165], v42\n"
    "ds_read_b64_tr_b16 v[168:169], v43\n"
    "s_waitcnt vmcnt(24) lgkmcnt(0)\n"
    "s_barrier\n"
    "s_add_u32 m0, s54, 0\n"
    "s_add_u32 s55, %6, 110592\n"
    "buffer_load_dwordx4 v8, s[44:47], s55 offen lds\n"
    "s_add_u32 m0, s54, 1024\n"
    "s_add_u32 s55, %6, 129024\n"
    "buffer_load_dwordx4 v8, s[44:47], s55 offen lds\n"
    "s_add_u32 m0, s54, 2048\n"
    "s_add_u32 s55, %6, 110720\n"
    "buffer_load_dwordx4 v8, s[44:47], s55 offen lds\n"
    "s_add_u32 m0, s54, 3072\n"
    "s_add_u32 s55, %6, 129152\n"
    "buffer_load_dwordx4 v8, s[44:47], s55 offen lds\n"
    "s_add_u32 m0, s54, 4096\n"
    "s_add_u32 s55, %6, 110848\n"
    "buffer_load_dwordx4 v8, s[44:47], s55 offen lds\n"
    "s_add_u32 m0, s54, 5120\n"
    "s_add_u32 s55, %6, 129280\n"
    "buffer_load_dwordx4 v8, s[44:47], s55 offen lds\n"
    "ds_read_b64_tr_b16 a[2:3], v32 offset:24576\n"
    "ds_read_b64_tr_b16 a[6:7], v33 offset:24576\n"
    "ds_read_b64_tr_b16 a[10:11], v34 offset:24576\n"
    "ds_read_b64_tr_b16 a[98:99], v35 offset:24576\n"
    "ds_read_b64_tr_b16 a[102:103], v36 offset:24576\n"
    "ds_read_b64_tr_b16 a[106:107], v37 offset:24576\n"
    "ds_read_b64_tr_b16 a[194:195], v38 offset:24576\n"
    "ds_read_b64_tr_b16 a[198:199], v39 offset:24576\n"
    "ds_read_b64_tr_b16 a[202:203], v40 offset:24576\n"
    "ds_read_b64_tr_b16 v[162:163], v41 offset:24576\n"
    "ds_read_b64_tr_b16 v[166:167], v42 offset:24576\n"
    "ds_read_b64_tr_b16 v[170:171], v43 offset:24576\n"
    "s_waitcnt vmcnt(24) lgkmcnt(0)\n"
    "s_barrier\n"
    "s_add_u32 m0, s54, 24576\n"
    "s_add_u32 s55, %6, 112896\n"
    "buffer_load_dwordx4 v8, s[44:47], s55 offen lds\n"
    "s_add_u32 m0, s54, 25600\n"
    "s_add_u32 s55, %6, 131328\n"
    "buffer_load_dwordx4 v8, s[44:47], s55 offen lds\n"
    "s_add_u32 m0, s54, 26624\n"
    "s_add_u32 s55, %6, 113024\n"
    "buffer_load_dwordx4 v8, s[44:47], s55 offen lds\n"
    "s_add_u32 m0, s54, 27648\n"
    "s_add_u32 s55, %6, 131456\n"
    "buffer_load_dwordx4 v8, s[44:47], s55 offen lds\n"
    "s_add_u32 m0, s54, 28672\n"
    "s_add_u32 s55, %6, 113152\n"
    "buffer_load_dwordx4 v8, s[44:47], s55 offen lds\n"
    "s_add_u32 m0, s54, 29696\n"
    "s_add_u32 s55, %6, 131584\n"
    "buffer_load_dwordx4 v8, s[44:47], s55 offen lds\n"
    "ds_read_b64_tr_b16 a[12:13], v32 offset:49152\n"
    "ds_read_b64_tr_b16 a[16:17], v33 offset:49152\n"
    "ds_read_b64_tr_b16 a[20:21], v34 offset:49152\n"
    "ds_read_b64_tr_b16 a[108:109], v35 offset:49152\n"
    "ds_read_b64_tr_b16 a[112:113], v36 offset:49152\n"
    "ds_read_b64_tr_b16 a[116:117], v37 offset:49152\n"
    "ds_read_b64_tr_b16 a[204:205], v38 offset:49152\n"
    "ds_read_b64_tr_b16 a[208:209], v39 offset:49152\n"
    "ds_read_b64_tr_b16 a[212:213], v40 offset:49152\n"
    "ds_read_b64_tr_b16 v[172:173], v41 offset:49152\n"
    "ds_read_b64_tr_b16 v[176:177], v42 offset:49152\n"
    "ds_read_b64_tr_b16 v[180:181], v43 offset:49152\n"
    "s_waitcnt vmcnt(24) lgkmcnt(0)\n"
    "s_barrier\n"
    "s_add_u32 m0, s54, 49152\n"
    "s_add_u32 s55, %6, 147456\n"
    "buffer_load_dwordx4 v8, s[44:47], s55 offen lds\n"
    "s_add_u32 m0, s54, 50176\n"
    "s_add_u32 s55, %6, 165888\n"
    "buffer_load_dwordx4 v8, s[44:47], s55 offen lds\n"
    "s_add_u32 m0, s54, 51200\n"
    "s_add_u32 s55, %6, 147584\n"
    "buffer_load_dwordx4 v8, s[44:47], s55 offen lds\n"
    "s_add_u32 m0, s54, 52224\n"
    "s_add_u32 s55, %6, 166016\n"
    "buffer_load_dwordx4 v8, s[44:47], s55 offen lds\n"
    "s_add_u32 m0, s54, 53248\n"
    "s_add_u32 s55, %6, 147712\n"
    "buffer_load_dwordx4 v8, s[44:47], s55 offen lds\n"
    "s_add_u32 m0, s54, 54272\n"
    "s_add_u32 s55, %6, 166144\n"
    "buffer_load_dwordx4 v8, s[44:47], s55 offen lds\n"
    "v_add_u32 v71, 0x10000, v32\n"
    "ds_read_b64_tr_b16 a[14:15], v71 offset:8192\n"
    "v_add_u32 v70, 0x10000, v33\n"
    "ds_read_b64_tr_b16 a[18:19], v70 offset:8192\n"
    "v_add_u32 v69, 0x10000, v34\n"
    "ds_read_b64_tr_b16 a[22:23], v69 offset:8192\n"
    "v_add_u32 v68, 0x10000, v35\n"
    "ds_read_b64_tr_b16 a[110:111], v68 offset:8192\n"
    "v_add_u32 v67, 0x10000, v36\n"
    "ds_read_b64_tr_b16 a[114:115], v67 offset:8192\n"
    "v_add_u32 v66, 0x10000, v37\n"
    "ds_read_b64_tr_b16 a[118:119], v66 offset:8192\n"
    "v_add_u32 v65, 0x10000, v38\n"
    "ds_read_b64_tr_b16 a[206:207], v65 offset:8192\n"
    "v_add_u32 v64, 0x10000, v39\n"
    "ds_read_b64_tr_b16 a[210:211], v64 offset:8192\n"
    "v_add_u32 v63, 0x10000, v40\n"
    "ds_read_b64_tr_b16 a[214:215], v63 offset:8192\n"
    "v_add_u32 v62, 0x10000, v41\n"
    "ds_read_b64_tr_b16 v[174:175], v62 offset:8192\n"
    "v_add_u32 v61, 0x10000, v42\n"
    "ds_read_b64_tr_b16 v[178:179], v61 offset:8192\n"
    "v_add_u32 v60, 0x10000, v43\n"
    "ds_read_b64_tr_b16 v[182:183], v60 offset:8192\n"
    "s_waitcnt vmcnt(24) lgkmcnt(0)\n"
    "s_barrier\n"
    "s_add_u32 m0, s54, 73728\n"
    "s_add_u32 s55, %6, 149760\n"
    "buffer_load_dwordx4 v8, s[44:47], s55 offen lds\n"
    "s_add_u32 m0, s54, 74752\n"
    "s_add_u32 s55, %6, 168192\n"
    "buffer_load_dwordx4 v8, s[44:47], s55 offen lds\n"
    "s_add_u32 m0, s54, 75776\n"
    "s_add_u32 s55, %6, 149888\n"
    "buffer_load_dwordx4 v8, s[44:47], s55 offen lds\n"
    "s_add_u32 m0, s54, 76800\n"
    "s_add_u32 s55, %6, 168320\n"
    "buffer_load_dwordx4 v8, s[44:47], s55 offen lds\n"
    "s_add_u32 m0, s54, 77824\n"
    "s_add_u32 s55, %6, 150016\n"
    "buffer_load_dwordx4 v8, s[44:47], s55 offen lds\n"
    "s_add_u32 m0, s54, 78848\n"
    "s_add_u32 s55, %6, 168448\n"
    "buffer_load_dwordx4 v8, s[44:47], s55 offen lds\n"
    "ds_read_b64_tr_b16 a[24:25], v71 offset:32768\n"
    "ds_read_b64_tr_b16 a[28:29], v70 offset:32768\n"
    "ds_read_b64_tr_b16 a[32:33], v69 offset:32768\n"
    "ds_read_b64_tr_b16 a[120:121], v68 offset:32768\n"
    "ds_read_b64_tr_b16 a[124:125], v67 offset:32768\n"
    "ds_read_b64_tr_b16 a[128:129], v66 offset:32768\n"
    "ds_read_b64_tr_b16 a[216:217], v65 offset:32768\n"
    "ds_read_b64_tr_b16 a[220:221], v64 offset:32768\n"
    "ds_read_b64_tr_b16 a[224:225], v63 offset:32768\n"
    "ds_read_b64_tr_b16 v[184:185], v62 offset:32768\n"
    "ds_read_b64_tr_b16 v[188:189], v61 offset:32768\n"
    "ds_read_b64_tr_b16 v[192:193], v60 offset:32768\n"
    "s_waitcnt vmcnt(24) lgkmcnt(0)\n"
    "s_barrier\n"
    "s_add_u32 m0, s54, 98304\n"
    "s_add_u32 s55, %6, 184320\n"
    "buffer_load_dwordx4 v8, s[44:47], s55 offen lds\n"
    "s_add_u32 m0, s54, 99328\n"
    "s_add_u32 s55, %6, 202752\n"
    "buffer_load_dwordx4 v8, s[44:47], s55 offen lds\n"
    "s_add_u32 m0, s54, 100352\n"
    "s_add_u32 s55, %6, 184448\n"
    "buffer_load_dwordx4 v8, s[44:47], s55 offen lds\n"
    "s_add_u32 m0, s54, 101376\n"
    "s_add_u32 s55, %6, 202880\n"
    "buffer_load_dwordx4 v8, s[44:47], s55 offen lds\n"
    "s_add_u32 m0, s54, 102400\n"
    "s_add_u32 s55, %6, 184576\n"
    "buffer_load_dwordx4 v8, s[44:47], s55 offen lds\n"
    "s_add_u32 m0, s54, 103424\n"
    "s_add_u32 s55, %6, 203008\n"
    "buffer_load_dwordx4 v8, s[44:47], s55 offen lds\n"
    "ds_read_b64_tr_b16 a[26:27], v71 offset:57344\n"
    "ds_read_b64_tr_b16 a[30:31], v70 offset:57344\n"
    "ds_read_b64_tr_b16 a[34:35], v69 offset:57344\n"
    "ds_read_b64_tr_b16 a[122:123], v68 offset:57344\n"
    "ds_read_b64_tr_b16 a[126:127], v67 offset:57344\n"
    "ds_read_b64_tr_b16 a[130:131], v66 offset:57344\n"
    "ds_read_b64_tr_b16 a[218:219], v65 offset:57344\n"
    "ds_read_b64_tr_b16 a[222:223], v64 offset:57344\n"
    "ds_read_b64_tr_b16 a[226:227], v63 offset:57344\n"
    "ds_read_b64_tr_b16 v[186:187], v62 offset:57344\n"
    "ds_read_b64_tr_b16 v[190:191], v61 offset:57344\n"
    "ds_read_b64_tr_b16 v[194:195], v60 offset:57344\n"
    "s_waitcnt vmcnt(24) lgkmcnt(0)\n"
    "s_barrier\n"
    "s_add_u32 m0, s54, 122880\n"
    "s_add_u32 s55, %6, 186624\n"
    "buffer_load_dwordx4 v8, s[44:47], s55 offen lds\n"
    "s_add_u32 m0, s54, 123904\n"
    "s_add_u32 s55, %6, 205056\n"
    "buffer_load_dwordx4 v8, s[44:47], s55 offen lds\n"
    "s_add_u32 m0, s54, 124928\n"
    "s_add_u32 s55, %6, 186752\n"
    "buffer_load_dwordx4 v8, s[44:47], s55 offen lds\n"
    "s_add_u32 m0, s54, 125952\n"
    "s_add_u32 s55, %6, 205184\n"
    "buffer_load_dwordx4 v8, s[44:47], s55 offen lds\n"
    "s_add_u32 m0, s54, 126976\n"
    "s_add_u32 s55, %6, 186880\n"
    "buffer_load_dwordx4 v8, s[44:47], s55 offen lds\n"
    "s_add_u32 m0, s54, 128000\n"
    "s_add_u32 s55, %6, 205312\n"
    "buffer_load_dwordx4 v8, s[44:47], s55 offen lds\n"
    "ds_read_b64_tr_b16 a[36:37], v32\n"
    "ds_read_b64_tr_b16 a[40:41], v33\n"
    "ds_read_b64_tr_b16 a[44:45], v34\n"
    "ds_read_b64_tr_b16 a[132:133], v35\n"
    "ds_read_b64_tr_b16 a[136:137], v36\n"
    "ds_read_b64_tr_b16 a[140:141], v37\n"
    "ds_read_b64_tr_b16 a[228:229], v38\n"
    "ds_read_b64_tr_b16 a[232:233], v39\n"
    "ds_read_b64_tr_b16 a[236:237], v40\n"
    "ds_read_b64_tr_b16 v[196:197], v41\n"
    "ds_read_b64_tr_b16 v[200:201], v42\n"
    "ds_read_b64_tr_b16 v[204:205], v43\n"
    "s_waitcnt vmcnt(24) lgkmcnt(0)\n"
    "s_barrier\n"
    "s_add_u32 m0, s54, 0\n"
    "s_add_u32 s55, %6, 221184\n"
    "buffer_load_dwordx4 v8, s[44:47], s55 offen lds\n"
    "s_add_u32 m0, s54, 1024\n"
    "s_add_u32 s55, %6, 239616\n"
    "buffer_load_dwordx4 v8, s[44:47], s55 offen lds\n"
    "s_add_u32 m0, s54, 2048\n"
    "s_add_u32 s55, %6, 221312\n"
    "buffer_load_dwordx4 v8, s[44:47], s55 offen lds\n"
    "s_add_u32 m0, s54, 3072\n"
    "s_add_u32 s55, %6, 239744\n"
    "buffer_load_dwordx4 v8, s[44:47], s55 offen lds\n"
    "s_add_u32 m0, s54, 4096\n"
    "s_add_u32 s55, %6, 221440\n"
    "buffer_load_dwordx4 v8, s[44:47], s55 offen lds\n"
    "s_add_u32 m0, s54, 5120\n"
    "s_add_u32 s55, %6, 239872\n"
    "buffer_load_dwordx4 v8, s[44:47], s55 offen lds\n"
    "ds_read_b64_tr_b16 a[38:39], v32 offset:24576\n"
    "ds_read_b64_tr_b16 a[42:43], v33 offset:24576\n"
    "ds_read_b64_tr_b16 a[46:47], v34 offset:24576\n"
    "ds_read_b64_tr_b16 a[134:135], v35 offset:24576\n"
    "ds_read_b64_tr_b16 a[138:139], v36 offset:24576\n"
    "ds_read_b64_tr_b16 a[142:143], v37 offset:24576\n"
    "ds_read_b64_tr_b16 a[230:231], v38 offset:24576\n"
    "ds_read_b64_tr_b16 a[234:235], v39 offset:24576\n"
    "ds_read_b64_tr_b16 a[238:239], v40 offset:24576\n"
    "ds_read_b64_tr_b16 v[198:199], v41 offset:24576\n"
    "ds_read_b64_tr_b16 v[202:203], v42 offset:24576\n"
    "ds_read_b64_tr_b16 v[206:207], v43 offset:24576\n"
    "s_waitcnt vmcnt(24) lgkmcnt(0)\n"
    "s_barrier\n"
    "s_add_u32 m0, s54, 24576\n"
    "s_add_u32 s55, %6, 223488\n"
    "buffer_load_dwordx4 v8, s[44:47], s55 offen lds\n"
    "s_add_u32 m0, s54, 25600\n"
    "s_add_u32 s55, %6, 241920\n"
    "buffer_load_dwordx4 v8, s[44:47], s55 offen lds\n"
    "s_add_u32 m0, s54, 26624\n"
    "s_add_u32 s55, %6, 223616\n"
    "buffer_load_dwordx4 v8, s[44:47], s55 offen lds\n"
    "s_add_u32 m0, s54, 27648\n"
    "s_add_u32 s55, %6, 242048\n"
    "buffer_load_dwordx4 v8, s[44:47], s55 offen lds\n"
    "s_add_u32 m0, s54, 28672\n"
    "s_add_u32 s55, %6, 223744\n"
    "buffer_load_dwordx4 v8, s[44:47], s55 offen lds\n"
    "s_add_u32 m0, s54, 29696\n"
    "s_add_u32 s55, %6, 242176\n"
    "buffer_load_dwordx4 v8, s[44:47], s55 offen lds\n"
    "ds_read_b64_tr_b16 a[48:49], v32 offset:49152\n"
    "ds_read_b64_tr_b16 a[52:53], v33 offset:49152\n"
    "ds_read_b64_tr_b16 a[56:57], v34 offset:49152\n"
    "ds_read_b64_tr_b16 a[144:145], v35 offset:49152\n"
    "ds_read_b64_tr_b16 a[148:149], v36 offset:49152\n"
    "ds_read_b64_tr_b16 a[152:153], v37 offset:49152\n"
    "ds_read_b64_tr_b16 a[240:241], v38 offset:49152\n"
    "ds_read_b64_tr_b16 a[244:245], v39 offset:49152\n"
    "ds_read_b64_tr_b16 a[248:249], v40 offset:49152\n"
    "ds_read_b64_tr_b16 v[208:209], v41 offset:49152\n"
    "ds_read_b64_tr_b16 v[212:213], v42 offset:49152\n"
    "ds_read_b64_tr_b16 v[216:217], v43 offset:49152\n"
    "s_waitcnt vmcnt(24) lgkmcnt(0)\n"
    "s_barrier\n"
    "s_add_u32 m0, s54, 49152\n"
    "s_add_u32 s55, %6, 258048\n"
    "buffer_load_dwordx4 v8, s[44:47], s55 offen lds\n"
    "s_add_u32 m0, s54, 50176\n"
    "s_add_u32 s55, %6, 276480\n"
    "buffer_load_dwordx4 v8, s[44:47], s55 offen lds\n"
    "s_add_u32 m0, s54, 51200\n"
    "s_add_u32 s55, %6, 258176\n"
    "buffer_load_dwordx4 v8, s[44:47], s55 offen lds\n"
    "s_add_u32 m0, s54, 52224\n"
    "s_add_u32 s55, %6, 276608\n"
    "buffer_load_dwordx4 v8, s[44:47], s55 offen lds\n"
    "s_add_u32 m0, s54, 53248\n"
    "s_add_u32 s55, %6, 258304\n"
    "buffer_load_dwordx4 v8, s[44:47], s55 offen lds\n"
    "s_add_u32 m0, s54, 54272\n"
    "s_add_u32 s55, %6, 276736\n"
    "buffer_load_dwordx4 v8, s[44:47], s55 offen lds\n"
    "ds_read_b64_tr_b16 a[50:51], v71 offset:8192\n"
    "ds_read_b64_tr_b16 a[54:55], v70 offset:8192\n"
    "ds_read_b64_tr_b16 a[58:59], v69 offset:8192\n"
    "ds_read_b64_tr_b16 a[146:147], v68 offset:8192\n"
    "ds_read_b64_tr_b16 a[150:151], v67 offset:8192\n"
    "ds_read_b64_tr_b16 a[154:155], v66 offset:8192\n"
    "ds_read_b64_tr_b16 a[242:243], v65 offset:8192\n"
    "ds_read_b64_tr_b16 a[246:247], v64 offset:8192\n"
    "ds_read_b64_tr_b16 a[250:251], v63 offset:8192\n"
    "ds_read_b64_tr_b16 v[210:211], v62 offset:8192\n"
    "ds_read_b64_tr_b16 v[214:215], v61 offset:8192\n"
    "ds_read_b64_tr_b16 v[218:219], v60 offset:8192\n"
    "s_waitcnt vmcnt(24) lgkmcnt(0)\n"
    "s_barrier\n"
    "s_add_u32 m0, s54, 73728\n"
    "s_add_u32 s55, %6, 260352\n"
    "buffer_load_dwordx4 v8, s[44:47], s55 offen lds\n"
    "s_add_u32 m0, s54, 74752\n"
    "s_add_u32 s55, %6, 278784\n"
    "buffer_load_dwordx4 v8, s[44:47], s55 offen lds\n"
    "s_add_u32 m0, s54, 75776\n"
    "s_add_u32 s55, %6, 260480\n"
    "buffer_load_dwordx4 v8, s[44:47], s55 offen lds\n"
    "s_add_u32 m0, s54, 76800\n"
    "s_add_u32 s55, %6, 278912\n"
    "buffer_load_dwordx4 v8, s[44:47], s55 offen lds\n"
    "s_add_u32 m0, s54, 77824\n"
    "s_add_u32 s55, %6, 260608\n"
    "buffer_load_dwordx4 v8, s[44:47], s55 offen lds\n"
    "s_add_u32 m0, s54, 78848\n"
    "s_add_u32 s55, %6, 279040\n"
    "buffer_load_dwordx4 v8, s[44:47], s55 offen lds\n"
    "v_mov_b32 v7, 0x20400\n"
    "global_load_dwordx2 v[20:21], v7, %1 sc1\n"
    "v_mbcnt_lo_u32_b32 v6, -1, 0\n"
    "v_min_u32 v6, 7, v6\n"
    "v_lshlrev_b32 v6, 7, v6\n"
    "v_add_u32 v6, 0x20000, v6\n"
    "global_load_dwordx2 v[22:23], v6, %1 sc1\n"
    "ds_read_b64_tr_b16 a[60:61], v71 offset:32768\n"
    "ds_read_b64_tr_b16 a[64:65], v70 offset:32768\n"
    "ds_read_b64_tr_b16 a[68:69], v69 offset:32768\n"
    "ds_read_b64_tr_b16 a[156:157], v68 offset:32768\n"
    "ds_read_b64_tr_b16 a[160:161], v67 offset:32768\n"
    "ds_read_b64_tr_b16 a[164:165], v66 offset:32768\n"
    "ds_read_b64_tr_b16 a[252:253], v65 offset:32768\n"
    "ds_read_b64_tr_b16 v[128:129], v64 offset:32768\n"
    "ds_read_b64_tr_b16 v[132:133], v63 offset:32768\n"
    "ds_read_b64_tr_b16 v[220:221], v62 offset:32768\n"
    "ds_read_b64_tr_b16 v[224:225], v61 offset:32768\n"
    "ds_read_b64_tr_b16 v[228:229], v60 offset:32768\n"
    "s_waitcnt vmcnt(26) lgkmcnt(0)\n"
    "s_barrier\n"
    "ds_read_b64_tr_b16 a[62:63], v71 offset:57344\n"
    "ds_read_b64_tr_b16 a[66:67], v70 offset:57344\n"
    "ds_read_b64_tr_b16 a[70:71], v69 offset:57344\n"
    "ds_read_b64_tr_b16 a[158:159], v68 offset:57344\n"
    "ds_read_b64_tr_b16 a[162:163], v67 offset:57344\n"
    "ds_read_b64_tr_b16 a[166:167], v66 offset:57344\n"
    "ds_read_b64_tr_b16 a[254:255], v65 offset:57344\n"
    "ds_read_b64_tr_b16 v[130:131], v64 offset:57344\n"
    "ds_read_b64_tr_b16 v[134:135], v63 offset:57344\n"
    "ds_read_b64_tr_b16 v[222:223], v62 offset:57344\n"
    "ds_read_b64_tr_b16 v[226:227], v61 offset:57344\n"
    "ds_read_b64_tr_b16 v[230:231], v60 offset:57344\n"
    "s_waitcnt vmcnt(20) lgkmcnt(0)\n"
    "s_barrier\n"
    "ds_read_b64_tr_b16 a[72:73], v32\n"
    "ds_read_b64_tr_b16 a[76:77], v33\n"
    "ds_read_b64_tr_b16 a[80:81], v34\n"
    "ds_read_b64_tr_b16 a[168:169], v35\n"
    "ds_read_b64_tr_b16 a[172:173], v36\n"
    "ds_read_b64_tr_b16 a[176:177], v37\n"
    "ds_read_b64_tr_b16 v[136:137], v38\n"
    "ds_read_b64_tr_b16 v[140:141], v39\n"
    "ds_read_b64_tr_b16 v[144:145], v40\n"
    "ds_read_b64_tr_b16 v[232:233], v41\n"
    "ds_read_b64_tr_b16 v[236:237], v42\n"
    "ds_read_b64_tr_b16 v[240:241], v43\n"
    "s_waitcnt vmcnt(14) lgkmcnt(0)\n"
    "s_barrier\n"
    "ds_read_b64_tr_b16 a[74:75], v32 offset:24576\n"
    "ds_read_b64_tr_b16 a[78:79], v33 offset:24576\n"
    "ds_read_b64_tr_b16 a[82:83], v34 offset:24576\n"
    "ds_read_b64_tr_b16 a[170:171], v35 offset:24576\n"
    "ds_read_b64_tr_b16 a[174:175], v36 offset:24576\n"
    "ds_read_b64_tr_b16 a[178:179], v37 offset:24576\n"
    "ds_read_b64_tr_b16 v[138:139], v38 offset:24576\n"
    "ds_read_b64_tr_b16 v[142:143], v39 offset:24576\n"
    "ds_read_b64_tr_b16 v[146:147], v40 offset:24576\n"
    "ds_read_b64_tr_b16 v[234:235], v41 offset:24576\n"
    "ds_read_b64_tr_b16 v[238:239], v42 offset:24576\n"
    "ds_read_b64_tr_b16 v[242:243], v43 offset:24576\n"
    "s_waitcnt vmcnt(8) lgkmcnt(0)\n"
    "s_barrier\n"
    "ds_read_b64_tr_b16 a[84:85], v32 offset:49152\n"
    "ds_read_b64_tr_b16 a[88:89], v33 offset:49152\n"
    "ds_read_b64_tr_b16 a[92:93], v34 offset:49152\n"
    "ds_read_b64_tr_b16 a[180:181], v35 offset:49152\n"
    "ds_read_b64_tr_b16 a[184:185], v36 offset:49152\n"
    "ds_read_b64_tr_b16 a[188:189], v37 offset:49152\n"
    "ds_read_b64_tr_b16 v[148:149], v38 offset:49152\n"
    "ds_read_b64_tr_b16 v[152:153], v39 offset:49152\n"
    "ds_read_b64_tr_b16 v[156:157], v40 offset:49152\n"
    "ds_read_b64_tr_b16 v[244:245], v41 offset:49152\n"
    "ds_read_b64_tr_b16 v[248:249], v42 offset:49152\n"
    "ds_read_b64_tr_b16 v[252:253], v43 offset:49152\n"
    "s_waitcnt vmcnt(2) lgkmcnt(0)\n"
    "s_barrier\n"
    "ds_read_b64_tr_b16 a[86:87], v71 offset:8192\n"
    "ds_read_b64_tr_b16 a[90:91], v70 offset:8192\n"
    "ds_read_b64_tr_b16 a[94:95], v69 offset:8192\n"
    "ds_read_b64_tr_b16 a[182:183], v68 offset:8192\n"
    "ds_read_b64_tr_b16 a[186:187], v67 offset:8192\n"
    "ds_read_b64_tr_b16 a[190:191], v66 offset:8192\n"
    "ds_read_b64_tr_b16 v[150:151], v65 offset:8192\n"
    "ds_read_b64_tr_b16 v[154:155], v64 offset:8192\n"
    "ds_read_b64_tr_b16 v[158:159], v63 offset:8192\n"
    "ds_read_b64_tr_b16 v[246:247], v62 offset:8192\n"
    "ds_read_b64_tr_b16 v[250:251], v61 offset:8192\n"
    "ds_read_b64_tr_b16 v[254:255], v60 offset:8192\n"
    "s_waitcnt lgkmcnt(0)\n"
    "s_barrier\n"
    "s_waitcnt vmcnt(0)\n"
    "v_add_u32 v20, 1, v20\n"
    "v_add_u32 v21, 1, v21\n"
    "s_mov_b32 s52, 1048576\n"
    ".Lwgo_a%=:\n"
    "v_cmp_ne_u64 vcc, v[20:21], v[22:23]\n"
    "s_cbranch_vccz .Lwgo_x%=\n"
    "global_load_dwordx2 v[22:23], v6, %1 sc1\n"
    "s_sub_u32 s52, s52, 1\n"
    "s_waitcnt vmcnt(0)\n"
    "s_cbranch_scc0 .Lwgo_a%=\n"
    ".Lwgo_x%=:\n"
    "v_mbcnt_lo_u32_b32 v20, -1, 0\n"
    "v_mbcnt_hi_u32_b32 v20, -1, v20\n"
    "v_min_u32 v6, 7, v20\n"
    "v_lshlrev_b32 v6, 7, v6\n"
    "v_mov_b32 v7, 0x26c00\n"
    "ds_read_b128 v[20:23], v7\n"
    "v_mov_b32 v96, 0\n"
    "s_waitcnt lgkmcnt(0)\n"
    "v_readfirstlane_b32 s49, v21\n"
    "v_add_u32 v20, v20, v6\n"
    "s_branch .Lwchk_%=\n"
    ".Lwgo_%=:\n"
    "s_waitcnt lgkmcnt(3)\n"
    "v_mfma_f32_16x16x32_bf16 v[8:11], a[0:3], v[64:67], 0\n"
    "v_add_u32 v7, 32, v7\n"
    "v_mfma_f32_16x16x32_bf16 v[12:15], a[4:7], v[64:67], 0\n"
    "s_waitcnt vmcnt(1)\n"
    "ds_read_b128 v[20:23], v7\n"
    "s_waitcnt lgkmcnt(3)\n"
    "v_mfma_f32_16x16x32_bf16 v[8:11], a[12:15], v[68:71], v[8:11]\n"
    "v_cvt_pk_bf16_f32 v28, v16, v17\n"
    "v_cvt_pk_bf16_f32 v30, v16, v17\n"
    "v_mfma_f32_16x16x32_bf16 v[12:15], a[16:19], v[68:71], v[12:15]\n"
    "v_cvt_pk_bf16_f32 v29, v18, v19\n"
    "v_cvt_pk_bf16_f32 v31, v18, v19\n"
    "s_waitcnt lgkmcnt(2)\n"
    "v_mfma_f32_16x16x32_bf16 v[8:11], a[24:27], v[72:75], v[8:11]\n"
    "ds_read_b128 v[80:83], v1 offset:4096\n"
    "v_permlane16_swap_b32 v28, v30\n"
    "v_mfma_f32_16x16x32_bf16 v[12:15], a[28:31], v[72:75], v[12:15]\n"
    "ds_read_b128 v[84:87], v1 offset:5120\n"
    "v_permlane16_swap_b32 v29, v31\n"
    "s_waitcnt lgkmcnt(3)\n"
    "v_mfma_f32_16x16x32_bf16 v[8:11], a[36:39], v[76:79], v[8:11]\n"
    "buffer_store_dwordx4 v[28:31], v3, s[40:43], s62 offen sc1\n"
    "v_mfma_f32_16x16x32_bf16 v[12:15], a[40:43], v[76:79], v[12:15]\n"
    "ds_read_b128 v[88:91], v1 offset:6144\n"
    "s_waitcnt lgkmcnt(3)\n"
    "v_readfirstlane_b32 s49, v21\n"
    "v_mfma_f32_16x16x32_bf16 v[16:19], a[8:11], v[64:67], 0\n"
    "ds_read_b128 v[92:95], v1 offset:7168\n"
    "v_readfirstlane_b32 s62, v22\n"
    "v_mfma_f32_16x16x32_bf16 v[16:19], a[20:23], v[68:71], v[16:19]\n"
    "v_add_u32 v20, v20, v6\n"
    ".Lwtop_%=:\n"
    "v_mfma_f32_16x16x32_bf16 v[16:19], a[32:35], v[72:75], v[16:19]\n"
    "global_load_dword v21, v20, %1 sc1\n"
    "v_xor_b32 v0, 0x2000, v1\n"
    "v_mfma_f32_16x16x32_bf16 v[16:19], a[44:47], v[76:79], v[16:19]\n"
    "s_xor_b32 s58, s58, 0x8000\n"
    "s_waitcnt lgkmcnt(3)\n"
    "v_mfma_f32_16x16x32_bf16 v[8:11], a[48:51], v[80:83], v[8:11]\n"
    "s_barrier\n"
    "ds_read_b128 v[32:35], v0\n"
    "v_mfma_f32_16x16x32_bf16 v[12:15], a[52:55], v[80:83], v[12:15]\n"
    "v_cmp_eq_u32 vcc, 0, v96\n"
    "s_cmp_eq_u64 vcc, 0\n"
    "s_cselect_b32 s94, s42, 0\n"
    "v_mfma_f32_16x16x32_bf16 v[16:19], a[56:59], v[80:83], v[16:19]\n"
    "ds_read_b128 v[36:39], v0 offset:1024\n"
    "s_waitcnt lgkmcnt(4)\n"
    "v_mfma_f32_16x16x32_bf16 v[8:11], a[60:63], v[84:87], v[8:11]\n"
    "ds_read_b128 v[40:43], v0 offset:2048\n"
    "v_mfma_f32_16x16x32_bf16 v[12:15], a[64:67], v[84:87], v[12:15]\n"
    "ds_read_b128 v[44:47], v0 offset:3072\n"
    "v_mfma_f32_16x16x32_bf16 v[16:19], a[68:71], v[84:87], v[16:19]\n"
    "ds_read_b128 v[48:51], v0 offset:4096\n"
    "s_waitcnt lgkmcnt(6)\n"
    "v_mfma_f32_16x16x32_bf16 v[8:11], a[72:75], v[88:91], v[8:11]\n"
    "ds_read_b128 v[52:55], v0 offset:5120\n"
    "v_mfma_f32_16x16x32_bf16 v[12:15], a[76:79], v[88:91], v[12:15]\n"
    "ds_read_b128 v[56:59], v0 offset:6144\n"
    "v_mfma_f32_16x16x32_bf16 v[16:19], a[80:83], v[88:91], v[16:19]\n"
    "ds_read_b128 v[60:63], v0 offset:7168\n"
    "s_mov_b32 m0, s58\n"
    "s_waitcnt lgkmcnt(8)\n"
    "v_mfma_f32_16x16x32_bf16 v[8:11], a[84:87], v[92:95], v[8:11]\n"
    "buffer_load_dwordx4 v4, s[92:95], s49 offen sc1 lds\n"
    "v_mfma_f32_16x16x32_bf16 v[12:15], a[88:91], v[92:95], v[12:15]\n"
    "v_mfma_f32_16x16x32_bf16 v[16:19], a[92:95], v[92:95], v[16:19]\n"
    "s_waitcnt lgkmcnt(7)\n"
    "v_mfma_f32_16x16x32_bf16 v[8:11], a[96:99], v[32:35], v[8:11]\n"
    "v_mfma_f32_16x16x32_bf16 v[12:15], a[100:103], v[32:35], v[12:15]\n"
    "buffer_load_dwordx4 v4, s[92:95], s49 offen offset:1024 sc1 lds\n"
    "v_xor_b32 v0, 0x4000, v1\n"
    "v_mfma_f32_16x16x32_bf16 v[16:19], a[104:107], v[32:35], v[16:19]\n"
    "ds_read_b128 v[32:35], v0\n"
    "s_waitcnt lgkmcnt(7)\n"
    "v_mfma_f32_16x16x32_bf16 v[8:11], a[108:111], v[36:39], v[8:11]\n"
    "v_mfma_f32_16x16x32_bf16 v[12:15], a[112:115], v[36:39], v[12:15]\n"
    "v_mfma_f32_16x16x32_bf16 v[16:19], a[116:119], v[36:39], v[16:19]\n"
    "ds_read_b128 v[36:39], v0 offset:1024\n"
    "s_waitcnt lgkmcnt(7)\n"
    "v_mfma_f32_16x16x32_bf16 v[8:11], a[120:123], v[40:43], v[8:11]\n"
    "buffer_load_dwordx4 v4, s[92:95], s49 offen offset:2048 sc1 lds\n"
    "v_mfma_f32_16x16x32_bf16 v[12:15], a[124:127], v[40:43], v[12:15]\n"
    "v_mfma_f32_16x16x32_bf16 v[16:19], a[128:131], v[40:43], v[16:19]\n"
    "ds_read_b128 v[40:43], v0 offset:2048\n"
    "s_waitcnt lgkmcnt(7)\n"
    "v_mfma_f32_16x16x32_bf16 v[8:11], a[132:135], v[44:47], v[8:11]\n"
    "v_mfma_f32_16x16x32_bf16 v[12:15], a[136:139], v[44:47], v[12:15]\n"
    "buffer_load_dwordx4 v4, s[92:95], s49 offen offset:3072 sc1 lds\n"
    "v_mfma_f32_16x16x32_bf16 v[16:19], a[140:143], v[44:47], v[16:19]\n"
    "ds_read_b128 v[44:47], v0 offset:3072\n"
    "s_waitcnt lgkmcnt(7)\n"
    "v_mfma_f32_16x16x32_bf16 v[8:11], a[144:147], v[48:51], v[8:11]\n"
    "v_mfma_f32_16x16x32_bf16 v[12:15], a[148:151], v[48:51], v[12:15]\n"
    "v_mfma_f32_16x16x32_bf16 v[16:19], a[152:155], v[48:51], v[16:19]\n"
    "ds_read_b128 v[48:51], v0 offset:4096\n"
    "s_bitset1_b32 m0, 12\n"
    "s_waitcnt lgkmcnt(7)\n"
    "v_mfma_f32_16x16x32_bf16 v[8:11], a[156:159], v[52:55], v[8:11]\n"
    "buffer_load_dwordx4 v5, s[92:95], s49 offen sc1 lds\n"
    "v_mfma_f32_16x16x32_bf16 v[12:15], a[160:163], v[52:55], v[12:15]\n"
    "v_mfma_f32_16x16x32_bf16 v[16:19], a[164:167], v[52:55], v[16:19]\n"
    "ds_read_b128 v[52:55], v0 offset:5120\n"
    "s_waitcnt lgkmcnt(7)\n"
    "v_mfma_f32_16x16x32_bf16 v[8:11], a[168:171], v[56:59], v[8:11]\n"
    "v_mfma_f32_16x16x32_bf16 v[12:15], a[172:175], v[56:59], v[12:15]\n"
    "buffer_load_dwordx4 v5, s[92:95], s49 offen offset:1024 sc1 lds\n"
    "v_mfma_f32_16x16x32_bf16 v[16:19], a[176:179], v[56:59], v[16:19]\n"
    "ds_read_b128 v[56:59], v0 offset:6144\n"
    "s_waitcnt vmcnt(6)\n"
    "v_mov_b32 v22, 1\n"
    "s_waitcnt lgkmcnt(7)\n"
    "v_mfma_f32_16x16x32_bf16 v[8:11], a[180:183], v[60:63], v[8:11]\n"
    "s_mov_b64 exec, s[56:57]\n"
    "ds_add_rtn_u32 v22, v7, v22 offset:16\n"
    "s_mov_b64 exec, -1\n"
    "v_mfma_f32_16x16x32_bf16 v[12:15], a[184:187], v[60:63], v[12:15]\n"
    "ds_read_b32 v96, v7 offset:32\n"
    "v_mfma_f32_16x16x32_bf16 v[16:19], a[188:191], v[60:63], v[16:19]\n"
    "ds_read_b128 v[60:63], v0 offset:7168\n"
    "s_waitcnt lgkmcnt(8)\n"
    "v_mfma_f32_16x16x32_bf16 v[8:11], a[192:195], v[32:35], v[8:11]\n"
    "buffer_load_dwordx4 v5, s[92:95], s49 offen offset:2048 sc1 lds\n"
    "v_mfma_f32_16x16x32_bf16 v[12:15], a[196:199], v[32:35], v[12:15]\n"
    "v_xor_b32 v0, 0x6000, v1\n"
    "v_mfma_f32_16x16x32_bf16 v[16:19], a[200:203], v[32:35], v[16:19]\n"
    "ds_read_b128 v[32:35], v0\n"
    "s_waitcnt lgkmcnt(8)\n"
    "v_mfma_f32_16x16x32_bf16 v[8:11], a[204:207], v[36:39], v[8:11]\n"
    "s_waitcnt lgkmcnt(3)\n"
    "v_readfirstlane_b32 s50, v22\n"
    "v_mfma_f32_16x16x32_bf16 v[12:15], a[208:211], v[36:39], v[12:15]\n"
    "buffer_load_dwordx4 v5, s[92:95], s49 offen offset:3072 sc1 lds\n"
    "s_cmp_eq_u32 s50, 3\n"
    "v_mfma_f32_16x16x32_bf16 v[16:19], a[212:215], v[36:39], v[16:19]\n"
    "ds_read_b128 v[36:39], v0 offset:1024\n"
    "v_mfma_f32_16x16x32_bf16 v[8:11], a[216:219], v[40:43], v[8:11]\n"
    "s_cselect_b64 exec, 1, 0\n"
    "global_store_dword v23, v22, %1 sc1\n"
    "s_mov_b64 exec, -1\n"
    "v_mfma_f32_16x16x32_bf16 v[12:15], a[220:223], v[40:43], v[12:15]\n"
    "s_cmp_eq_u32 s94, 0\n"
    "s_cbranch_scc1 .Lwk1_%=\n"
    ".Lwk1_x%=:\n"
    "v_mfma_f32_16x16x32_bf16 v[16:19], a[224:227], v[40:43], v[16:19]\n"
    "ds_read_b128 v[40:43], v0 offset:2048\n"
    "s_waitcnt lgkmcnt(4)\n"
    "v_add_u32 v96, v96, v6\n"
    "v_mfma_f32_16x16x32_bf16 v[8:11], a[228:231], v[44:47], v[8:11]\n"
    "global_load_dword v96, v96, %1 sc1\n"
    "v_mfma_f32_16x16x32_bf16 v[12:15], a[232:235], v[44:47], v[12:15]\n"
    "v_mfma_f32_16x16x32_bf16 v[16:19], a[236:239], v[44:47], v[16:19]\n"
    "ds_read_b128 v[44:47], v0 offset:3072\n"
    "v_mfma_f32_16x16x32_bf16 v[8:11], a[240:243], v[48:51], v[8:11]\n"
    "v_mfma_f32_16x16x32_bf16 v[12:15], a[244:247], v[48:51], v[12:15]\n"
    "v_mfma_f32_16x16x32_bf16 v[16:19], a[248:251], v[48:51], v[16:19]\n"
    "ds_read_b128 v[48:51], v0 offset:4096\n"
    "v_mfma_f32_16x16x32_bf16 v[8:11], a[252:255], v[52:55], v[8:11]\n"
    "v_mfma_f32_16x16x32_bf16 v[12:15], v[128:131], v[52:55], v[12:15]\n"
    "v_mfma_f32_16x16x32_bf16 v[16:19], v[132:135], v[52:55], v[16:19]\n"
    "ds_read_b128 v[52:55], v0 offset:5120\n"
    "v_mfma_f32_16x16x32_bf16 v[8:11], v[136:139], v[56:59], v[8:11]\n"
    "v_mfma_f32_16x16x32_bf16 v[12:15], v[140:143], v[56:59], v[12:15]\n"
    "v_mfma_f32_16x16x32_bf16 v[16:19], v[144:147], v[56:59], v[16:19]\n"
    "ds_read_b128 v[56:59], v0 offset:6144\n"
    "s_waitcnt lgkmcnt(7)\n"
    "v_mfma_f32_16x16x32_bf16 v[8:11], v[148:151], v[60:63], v[8:11]\n"
    "v_mfma_f32_16x16x32_bf16 v[12:15], v[152:155], v[60:63], v[12:15]\n"
    "v_mfma_f32_16x16x32_bf16 v[16:19], v[156:159], v[60:63], v[16:19]\n"
    "ds_read_b128 v[60:63], v0 offset:7168\n"
    "s_waitcnt lgkmcnt(7)\n"
    "v_mfma_f32_16x16x32_bf16 v[8:11], v[160:163], v[32:35], v[8:11]\n"
    "s_cmp_eq_u32 s94, 0\n"
    "s_cbranch_scc1 .Lwk2_%=\n"
    ".Lwk2_x%=:\n"
    "v_mfma_f32_16x16x32_bf16 v[12:15], v[164:167], v[32:35], v[12:15]\n"
    "v_mfma_f32_16x16x32_bf16 v[16:19], v[168:171], v[32:35], v[16:19]\n"
    "s_waitcnt lgkmcnt(6)\n"
    "v_mfma_f32_16x16x32_bf16 v[8:11], v[172:175], v[36:39], v[8:11]\n"
    "v_mfma_f32_16x16x32_bf16 v[12:15], v[176:179], v[36:39], v[12:15]\n"
    "v_mfma_f32_16x16x32_bf16 v[16:19], v[180:183], v[36:39], v[16:19]\n"
    "s_waitcnt lgkmcnt(5)\n"
    "v_mfma_f32_16x16x32_bf16 v[8:11], v[184:187], v[40:43], v[8:11]\n"
    "v_mfma_f32_16x16x32_bf16 v[12:15], v[188:191], v[40:43], v[12:15]\n"
    "v_mfma_f32_16x16x32_bf16 v[16:19], v[192:195], v[40:43], v[16:19]\n"
    "s_waitcnt lgkmcnt(4)\n"
    "v_mfma_f32_16x16x32_bf16 v[8:11], v[196:199], v[44:47], v[8:11]\n"
    "v_xor_b32 v1, 0x8000, v1\n"
    "v_mfma_f32_16x16x32_bf16 v[12:15], v[200:203], v[44:47], v[12:15]\n"
    "s_waitcnt lgkmcnt(3)\n"
    "v_mfma_f32_16x16x32_bf16 v[8:11], v[208:211], v[48:51], v[8:11]\n"
    "v_mfma_f32_16x16x32_bf16 v[12:15], v[212:215], v[48:51], v[12:15]\n"
    "s_waitcnt lgkmcnt(2)\n"
    "v_mfma_f32_16x16x32_bf16 v[8:11], v[220:223], v[52:55], v[8:11]\n"
    "v_mfma_f32_16x16x32_bf16 v[12:15], v[224:227], v[52:55], v[12:15]\n"
    "s_waitcnt lgkmcnt(1)\n"
    "v_mfma_f32_16x16x32_bf16 v[8:11], v[232:235], v[56:59], v[8:11]\n"
    "s_waitcnt vmcnt(4)\n"
    "ds_read_b128 v[64:67], v1\n"
    "v_mfma_f32_16x16x32_bf16 v[12:15], v[236:239], v[56:59], v[12:15]\n"
    "ds_read_b128 v[68:71], v1 offset:1024\n"
    "s_waitcnt lgkmcnt(2)\n"
    "v_mfma_f32_16x16x32_bf16 v[8:11], v[244:247], v[60:63], v[8:11]\n"
    "ds_read_b128 v[72:75], v1 offset:2048\n"
    "v_mfma_f32_16x16x32_bf16 v[12:15], v[248:251], v[60:63], v[12:15]\n"
    "ds_read_b128 v[76:79], v1 offset:3072\n"
    "v_mfma_f32_16x16x32_bf16 v[16:19], v[204:207], v[44:47], v[16:19]\n"
    "v_mfma_f32_16x16x32_bf16 v[16:19], v[216:219], v[48:51], v[16:19]\n"
    "v_cvt_pk_bf16_f32 v24, v8, v9\n"
    "v_cvt_pk_bf16_f32 v25, v10, v11\n"
    "v_mfma_f32_16x16x32_bf16 v[16:19], v[228:231], v[52:55], v[16:19]\n"
    "v_cvt_pk_bf16_f32 v26, v12, v13\n"
    "v_cvt_pk_bf16_f32 v27, v14, v15\n"
    "v_mfma_f32_16x16x32_bf16 v[16:19], v[240:243], v[56:59], v[16:19]\n"
    "buffer_store_dwordx4 v[24:27], v2, s[40:43], s62 offen sc1\n"
    "v_mfma_f32_16x16x32_bf16 v[16:19], v[252:255], v[60:63], v[16:19]\n"
    "s_mov_b32 s56, 1\n"
    "s_cmp_lg_u32 s94, 0\n"
    "s_cbranch_scc1 .Lwgo_%=\n"
    "s_nop 5\n"
    "v_cvt_pk_bf16_f32 v28, v16, v17\n"
    "v_cvt_pk_bf16_f32 v30, v16, v17\n"
    "v_cvt_pk_bf16_f32 v29, v18, v19\n"
    "v_cvt_pk_bf16_f32 v31, v18, v19\n"
    "v_permlane16_swap_b32 v28, v30\n"
    "s_nop 0\n"
    "v_permlane16_swap_b32 v29, v31\n"
    "buffer_store_dwordx4 v[28:31], v3, s[40:43], s62 offen sc1\n"
    "s_waitcnt vmcnt(2)\n"
    "v_cmp_eq_u32 vcc, 0, v21\n"
    "s_cmp_eq_u64 vcc, 0\n"
    "s_cbranch_scc1 .Lwld_%=\n"
    "ds_read_b32 v23, v7 offset:44\n"
    "global_load_dword v21, v20, %1 sc1\n"
    "s_waitcnt vmcnt(0)\n"
    "v_mov_b32 v22, 1\n"
    "s_mov_b64 exec, 1\n"
    "ds_add_rtn_u32 v22, v7, v22 offset:48\n"
    "s_mov_b64 exec, -1\n"
    "s_waitcnt lgkmcnt(0)\n"
    "v_readfirstlane_b32 s50, v22\n"
    "s_cmp_eq_u32 s50, 3\n"
    "s_cselect_b64 exec, 1, 0\n"
    "global_store_dword v23, v22, %1 sc1\n"
    "s_mov_b64 exec, -1\n"
    "s_mov_b32 s56, 0\n"
    "v_cmp_eq_u32 vcc, 0, v21\n"
    "s_cmp_eq_u64 vcc, 0\n"
    "s_cbranch_scc1 .Lwld_%=\n"
    ".Lwchk_%=:\n"
    "v_readfirstlane_b32 s50, v20\n"
    "s_cmp_lt_u32 s50, 200\n"
    "s_cbranch_scc1 .Lwend_%=\n"
    ".Lwwait_%=:\n"
    "global_load_dword v21, v20, %1 sc1\n"
    "s_mov_b32 s52, 0\n"
    ".Lwp3_%=:\n"
    "s_waitcnt vmcnt(0)\n"
    "v_cmp_eq_u32 vcc, 0, v21\n"
    "s_cmp_eq_u64 vcc, 0\n"
    "s_cbranch_scc1 .Lwp3_x%=\n"
    "s_add_u32 s52, s52, 1\n"
    "s_sleep 2\n"
    "global_load_dword v21, v20, %1 sc1\n"
    "s_cmp_lt_u32 s52, s53\n"
    "s_cbranch_scc1 .Lwp3_%=\n"
    "s_mov_b32 s53, 0\n"
    "v_mov_b32 v22, 0\n"
    "v_mov_b32 v23, 1\n"
    "global_store_dword v22, v23, %1 sc1\n"
    "s_waitcnt vmcnt(0)\n"
    ".Lwp3_x%=:\n"
    ".Lwld_%=:\n"
    "s_mov_b32 s94, s42\n"
    "s_mov_b32 m0, s58\n"
    "s_nop 0\n"
    "buffer_load_dwordx4 v4, s[92:95], s49 offen sc1 lds\n"
    "buffer_load_dwordx4 v4, s[92:95], s49 offen offset:1024 sc1 lds\n"
    "buffer_load_dwordx4 v4, s[92:95], s49 offen offset:2048 sc1 lds\n"
    "buffer_load_dwordx4 v4, s[92:95], s49 offen offset:3072 sc1 lds\n"
    "s_bitset1_b32 m0, 12\n"
    "s_nop 0\n"
    "buffer_load_dwordx4 v5, s[92:95], s49 offen sc1 lds\n"
    "buffer_load_dwordx4 v5, s[92:95], s49 offen offset:1024 sc1 lds\n"
    "buffer_load_dwordx4 v5, s[92:95], s49 offen offset:2048 sc1 lds\n"
    "buffer_load_dwordx4 v5, s[92:95], s49 offen offset:3072 sc1 lds\n"
    "s_waitcnt vmcnt(4)\n"
    "ds_read_b128 v[64:67], v1\n"
    "ds_read_b128 v[68:71], v1 offset:1024\n"
    "ds_read_b128 v[72:75], v1 offset:2048\n"
    "ds_read_b128 v[76:79], v1 offset:3072\n"
    ".Lwin_%=:\n"
    "s_waitcnt lgkmcnt(3)\n"
    "v_mfma_f32_16x16x32_bf16 v[8:11], a[0:3], v[64:67], 0\n"
    "v_add_u32 v7, 32, v7\n"
    "v_mfma_f32_16x16x32_bf16 v[12:15], a[4:7], v[64:67], 0\n"
    "s_waitcnt vmcnt(0)\n"
    "ds_read_b128 v[20:23], v7\n"
    "s_waitcnt lgkmcnt(3)\n"
    "v_mfma_f32_16x16x32_bf16 v[8:11], a[12:15], v[68:71], v[8:11]\n"
    "v_mfma_f32_16x16x32_bf16 v[12:15], a[16:19], v[68:71], v[12:15]\n"
    "s_waitcnt lgkmcnt(2)\n"
    "v_mfma_f32_16x16x32_bf16 v[8:11], a[24:27], v[72:75], v[8:11]\n"
    "ds_read_b128 v[80:83], v1 offset:4096\n"
    "v_mfma_f32_16x16x32_bf16 v[12:15], a[28:31], v[72:75], v[12:15]\n"
    "ds_read_b128 v[84:87], v1 offset:5120\n"
    "s_waitcnt lgkmcnt(3)\n"
    "v_mfma_f32_16x16x32_bf16 v[8:11], a[36:39], v[76:79], v[8:11]\n"
    "v_mfma_f32_16x16x32_bf16 v[12:15], a[40:43], v[76:79], v[12:15]\n"
    "ds_read_b128 v[88:91], v1 offset:6144\n"
    "s_waitcnt lgkmcnt(3)\n"
    "v_readfirstlane_b32 s49, v21\n"
    "v_mfma_f32_16x16x32_bf16 v[16:19], a[8:11], v[64:67], 0\n"
    "ds_read_b128 v[92:95], v1 offset:7168\n"
    "v_readfirstlane_b32 s62, v22\n"
    "v_mfma_f32_16x16x32_bf16 v[16:19], a[20:23], v[68:71], v[16:19]\n"
    "v_add_u32 v20, v20, v6\n"
    "s_cmp_eq_u32 s56, 0\n"
    "s_cbranch_scc1 .Lwc4_%=\n"
    "s_waitcnt vmcnt(0)\n"
    "v_mov_b32 v22, 1\n"
    "s_mov_b64 exec, 1\n"
    "ds_add_rtn_u32 v22, v7, v22 offset:16\n"
    "s_mov_b64 exec, -1\n"
    "s_waitcnt lgkmcnt(0)\n"
    "v_readfirstlane_b32 s50, v22\n"
    "s_cmp_eq_u32 s50, 3\n"
    "s_cselect_b64 exec, 1, 0\n"
    "global_store_dword v23, v22, %1 sc1\n"
    "s_mov_b64 exec, -1\n"
    "s_mov_b32 s56, 0\n"
    ".Lwc4_%=:\n"
    "s_branch .Lwtop_%=\n"
    ".Lwk1_%=:\n"
    "s_waitcnt vmcnt(8)\n"
    "v_cmp_eq_u32 vcc, 0, v21\n"
    "s_cmp_eq_u64 vcc, 0\n"
    "s_cselect_b32 s94, s42, 0\n"
    "global_load_dword v21, v20, %1 sc1\n"
    "s_mov_b32 m0, s58\n"
    "s_nop 0\n"
    "buffer_load_dwordx4 v4, s[92:95], s49 offen sc1 lds\n"
    "buffer_load_dwordx4 v4, s[92:95], s49 offen offset:1024 sc1 lds\n"
    "buffer_load_dwordx4 v4, s[92:95], s49 offen offset:2048 sc1 lds\n"
    "buffer_load_dwordx4 v4, s[92:95], s49 offen offset:3072 sc1 lds\n"
    "s_bitset1_b32 m0, 12\n"
    "s_nop 0\n"
    "buffer_load_dwordx4 v5, s[92:95], s49 offen sc1 lds\n"
    "buffer_load_dwordx4 v5, s[92:95], s49 offen offset:1024 sc1 lds\n"
    "buffer_load_dwordx4 v5, s[92:95], s49 offen offset:2048 sc1 lds\n"
    "buffer_load_dwordx4 v5, s[92:95], s49 offen offset:3072 sc1 lds\n"
    "s_branch .Lwk1_x%=\n"
    ".Lwk2_%=:\n"
    "s_waitcnt vmcnt(8)\n"
    "v_cmp_eq_u32 vcc, 0, v21\n"
    "s_cmp_eq_u64 vcc, 0\n"
    "s_cselect_b32 s94, s42, 0\n"
    "global_load_dword v21, v20, %1 sc1\n"
    "s_mov_b32 m0, s58\n"
    "s_nop 0\n"
    "buffer_load_dwordx4 v4, s[92:95], s49 offen sc1 lds\n"
    "buffer_load_dwordx4 v4, s[92:95], s49 offen offset:1024 sc1 lds\n"
    "buffer_load_dwordx4 v4, s[92:95], s49 offen offset:2048 sc1 lds\n"
    "buffer_load_dwordx4 v4, s[92:95], s49 offen offset:3072 sc1 lds\n"
    "s_bitset1_b32 m0, 12\n"
    "s_nop 0\n"
    "buffer_load_dwordx4 v5, s[92:95], s49 offen sc1 lds\n"
    "buffer_load_dwordx4 v5, s[92:95], s49 offen offset:1024 sc1 lds\n"
    "buffer_load_dwordx4 v5, s[92:95], s49 offen offset:2048 sc1 lds\n"
    "buffer_load_dwordx4 v5, s[92:95], s49 offen offset:3072 sc1 lds\n"
    "s_branch .Lwk2_x%=\n"
    ".Lwpol_%=:\n"
    "v_mov_b32 v20, 64\n"
    "global_load_dword v21, v20, %1 sc1\n"
    "s_mov_b32 s52, 0\n"
    ".Lwp5_%=:\n"
    "s_waitcnt vmcnt(0)\n"
    "v_cmp_eq_u32 vcc, 0, v21\n"
    "s_cmp_eq_u64 vcc, 0\n"
    "s_cbranch_scc1 .Lwp5_x%=\n"
    "s_add_u32 s52, s52, 1\n"
    "s_sleep 2\n"
    "global_load_dword v21, v20, %1 sc1\n"
    "s_cmp_lt_u32 s52, s53\n"
    "s_cbranch_scc1 .Lwp5_%=\n"
    "s_mov_b32 s53, 0\n"
    "v_mov_b32 v22, 0\n"
    "v_mov_b32 v23, 1\n"
    "global_store_dword v22, v23, %1 sc1\n"
    "s_waitcnt vmcnt(0)\n"
    ".Lwp5_x%=:\n"
    "v_readfirstlane_b32 s50, v21\n"
    "v_mbcnt_lo_u32_b32 v20, -1, 0\n"
    "v_mbcnt_hi_u32_b32 v20, -1, v20\n"
    "s_lshl_b32 s51, %2, 6\n"
    "v_add_u32 v20, s51, v20\n"
    "v_min_u32 v20, 129, v20\n"
    "v_lshlrev_b32 v20, 5, v20\n"
    "v_add_u32 v20, 0x26c00, v20\n"
    "ds_read2_b32 v[22:23], v20 offset0:1 offset1:2\n"
    "s_and_b32 s50, s50, 1\n"
    "s_waitcnt lgkmcnt(0)\n"
    "v_and_b32 v21, s50, v22\n"
    "v_lshlrev_b32 v21, 13, v21\n"
    "v_and_b32 v22, -2, v22\n"
    "v_xor_b32 v22, v21, v22\n"
    "v_and_b32 v21, s50, v23\n"
    "v_lshlrev_b32 v21, 13, v21\n"
    "v_and_b32 v23, -2, v23\n"
    "v_xor_b32 v23, v21, v23\n"
    "ds_write2_b32 v20, v22, v23 offset0:1 offset1:2\n"
    "s_waitcnt lgkmcnt(0)\n"
    "s_barrier\n"
    "ds_read_b32 v21, v7 offset:4\n"
    "v_add_u32 v20, %4, v6\n"
    "s_waitcnt lgkmcnt(0)\n"
    "v_readfirstlane_b32 s49, v21\n"
    "s_branch .Lwwait_%=\n"
    ".Lwend_%=:\n"
    "s_cmp_eq_u32 s50, 192\n"
    "s_cbranch_scc0 .Lwpol_%=\n"
    "s_waitcnt vmcnt(0) lgkmcnt(0)\n"
    "s_endpgm\n"
    ".p2align 6\n"
    ".Lrole3_%=:\n"
    "s_mov_b64 s[40:41], %0\n"
    "s_and_b32 s41, s41, 0xffff\n"
    "s_mov_b32 s42, 0xc000000\n"
    "s_mov_b32 s43, 0x20000\n"
    "s_mov_b64 s[92:93], %0\n"
    "s_and_b32 s93, s93, 0xffff\n"
    "s_mov_b32 s94, 0xc000000\n"
    "s_mov_b32 s95, 0x20000\n"
    "s_mov_b32 s53, 131072\n"
    "s_mov_b64 s[56:57], 0\n"
    "v_mbcnt_lo_u32_b32 v100, -1, 0\n"
    "v_mbcnt_hi_u32_b32 v100, -1, v100\n"
    "s_lshl_b32 s50, %2, 8\n"
    "s_add_u32 s50, s50, 144384\n"
    "v_lshl_add_u32 v100, v100, 2, s50\n"
    "ds_read_b32 v0, v100\n"
    "ds_read_b32 v1, v100 offset:1024\n"
    "ds_read_b32 v2, v100 offset:2048\n"
    "ds_read_b32 v3, v100 offset:3072\n"
    "ds_read_b32 v4, v100 offset:4096\n"
    "ds_read_b32 v5, v100 offset:5120\n"
    "ds_read_b32 v6, v100 offset:6144\n"
    "ds_read_b32 v7, v100 offset:7168\n"
    "ds_read_b32 v8, v100 offset:8192\n"
    "ds_read_b32 v152, v100 offset:9216\n"
    "ds_read_b32 v98, v100 offset:10240\n"
    "ds_read_b32 v99, v100 offset:11264\n"
    "s_waitcnt lgkmcnt(0)\n"
    "s_barrier\n"
    "v_mbcnt_lo_u32_b32 v100, -1, 0\n"
    "v_mbcnt_hi_u32_b32 v100, -1, v100\n"
    "v_add_u32 v101, 0, v100\n"
    "v_mul_u32_u24 v102, 7282, v101\n"
    "v_lshrrev_b32 v102, 16, v102\n"
    "v_and_b32 v103, 12, v102\n"
    "v_and_b32 v104, 48, v102\n"
    "v_mul_i32_i24 v104, 24192, v104\n"
    "v_mul_u32_u24 v103, 3456, v103\n"
    "v_mul_u32_u24 v102, 3312, v102\n"
    "v_add3_u32 v102, v102, v103, v104\n"
    "v_lshl_add_u32 v80, v101, 4, v102\n"
    "v_add_u32 v101, 64, v100\n"
    "v_mul_u32_u24 v102, 7282, v101\n"
    "v_lshrrev_b32 v102, 16, v102\n"
    "v_and_b32 v103, 12, v102\n"
    "v_and_b32 v104, 48, v102\n"
    "v_mul_i32_i24 v104, 24192, v104\n"
    "v_mul_u32_u24 v103, 3456, v103\n"
    "v_mul_u32_u24 v102, 3312, v102\n"
    "v_add3_u32 v102, v102, v103, v104\n"
    "v_lshl_add_u32 v81, v101, 4, v102\n"
    "v_add_u32 v101, 128, v100\n"
    "v_mul_u32_u24 v102, 7282, v101\n"
    "v_lshrrev_b32 v102, 16, v102\n"
    "v_and_b32 v103, 12, v102\n"
    "v_and_b32 v104, 48, v102\n"
    "v_mul_i32_i24 v104, 24192, v104\n"
    "v_mul_u32_u24 v103, 3456, v103\n"
    "v_mul_u32_u24 v102, 3312, v102\n"
    "v_add3_u32 v102, v102, v103, v104\n"
    "v_lshl_add_u32 v82, v101, 4, v102\n"
    "v_add_u32 v101, 192, v100\n"
    "v_mul_u32_u24 v102, 7282, v101\n"
    "v_lshrrev_b32 v102, 16, v102\n"
    "v_and_b32 v103, 12, v102\n"
    "v_and_b32 v104, 48, v102\n"
    "v_mul_i32_i24 v104, 24192, v104\n"
    "v_mul_u32_u24 v103, 3456, v103\n"
    "v_mul_u32_u24 v102, 3312, v102\n"
    "v_add3_u32 v102, v102, v103, v104\n"
    "v_lshl_add_u32 v83, v101, 4, v102\n"
    "v_add_u32 v101, 256, v100\n"
    "v_mul_u32_u24 v102, 7282, v101\n"
    "v_lshrrev_b32 v102, 16, v102\n"
    "v_and_b32 v103, 12, v102\n"
    "v_and_b32 v104, 48, v102\n"
    "v_mul_i32_i24 v104, 24192, v104\n"
    "v_mul_u32_u24 v103, 3456, v103\n"
    "v_mul_u32_u24 v102, 3312, v102\n"
    "v_add3_u32 v102, v102, v103, v104\n"
    "v_lshl_add_u32 v84, v101, 4, v102\n"
    "v_add_u32 v101, 320, v100\n"
    "v_mul_u32_u24 v102, 7282, v101\n"
    "v_lshrrev_b32 v102, 16, v102\n"
    "v_and_b32 v103, 12, v102\n"
    "v_and_b32 v104, 48, v102\n"
    "v_mul_i32_i24 v104, 24192, v104\n"
    "v_mul_u32_u24 v103, 3456, v103\n"
    "v_mul_u32_u24 v102, 3312, v102\n"
    "v_add3_u32 v102, v102, v103, v104\n"
    "v_lshl_add_u32 v85, v101, 4, v102\n"
    "v_add_u32 v101, 384, v100\n"
    "v_mul_u32_u24 v102, 7282, v101\n"
    "v_lshrrev_b32 v102, 16, v102\n"
    "v_and_b32 v103, 12, v102\n"
    "v_and_b32 v104, 48, v102\n"
    "v_mul_i32_i24 v104, 24192, v104\n"
    "v_mul_u32_u24 v103, 3456, v103\n"
    "v_mul_u32_u24 v102, 3312, v102\n"
    "v_add3_u32 v102, v102, v103, v104\n"
    "v_lshl_add_u32 v86, v101, 4, v102\n"
    "v_add_u32 v101, 448, v100\n"
    "v_mul_u32_u24 v102, 7282, v101\n"
    "v_lshrrev_b32 v102, 16, v102\n"
    "v_and_b32 v103, 12, v102\n"
    "v_and_b32 v104, 48, v102\n"
    "v_mul_i32_i24 v104, 24192, v104\n"
    "v_mul_u32_u24 v103, 3456, v103\n"
    "v_mul_u32_u24 v102, 3312, v102\n"
    "v_add3_u32 v102, v102, v103, v104\n"
    "v_lshl_add_u32 v87, v101, 4, v102\n"
    "v_add_u32 v101, 512, v100\n"
    "v_mul_u32_u24 v102, 7282, v101\n"
    "v_lshrrev_b32 v102, 16, v102\n"
    "v_and_b32 v103, 12, v102\n"
    "v_and_b32 v104, 48, v102\n"
    "v_mul_i32_i24 v104, 24192, v104\n"
    "v_mul_u32_u24 v103, 3456, v103\n"
    "v_mul_u32_u24 v102, 3312, v102\n"
    "v_add3_u32 v102, v102, v103, v104\n"
    "v_lshl_add_u32 v88, v101, 4, v102\n"
    "s_min_u32 s50, %2, 2\n"
    "s_mul_i32 s54, s50, 9216\n"
    "s_mul_i32 s50, s50, 1152\n"
    "s_add_u32 s51, s50, %6\n"
    "s_mov_b64 s[44:45], %8\n"
    "s_and_b32 s45, s45, 0xffff\n"
    "s_mov_b32 s46, 0x1e6000\n"
    "s_mov_b32 s47, 0x20000\n"
    "v_add_u32 v101, 0, v100\n"
    "v_mul_u32_u24 v102, 7282, v101\n"
    "v_lshrrev_b32 v102, 16, v102\n"
    "v_and_b32 v103, 12, v102\n"
    "v_and_b32 v104, 16, v102\n"
    "v_mul_i32_i24 v104, -2592, v104\n"
    "v_mul_u32_u24 v103, 3456, v103\n"
    "v_mul_u32_u24 v102, 3312, v102\n"
    "v_add3_u32 v102, v102, v103, v104\n"
    "v_lshl_add_u32 v89, v101, 4, v102\n"
    "v_add_u32 v101, 64, v100\n"
    "v_mul_u32_u24 v102, 7282, v101\n"
    "v_lshrrev_b32 v102, 16, v102\n"
    "v_and_b32 v103, 12, v102\n"
    "v_and_b32 v104, 16, v102\n"
    "v_mul_i32_i24 v104, -2592, v104\n"
    "v_mul_u32_u24 v103, 3456, v103\n"
    "v_mul_u32_u24 v102, 3312, v102\n"
    "v_add3_u32 v102, v102, v103, v104\n"
    "v_lshl_add_u32 v90, v101, 4, v102\n"
    "v_add_u32 v101, 128, v100\n"
    "v_mul_u32_u24 v102, 7282, v101\n"
    "v_lshrrev_b32 v102, 16, v102\n"
    "v_and_b32 v103, 12, v102\n"
    "v_and_b32 v104, 16, v102\n"
    "v_mul_i32_i24 v104, -2592, v104\n"
    "v_mul_u32_u24 v103, 3456, v103\n"
    "v_mul_u32_u24 v102, 3312, v102\n"
    "v_add3_u32 v102, v102, v103, v104\n"
    "v_lshl_add_u32 v91, v101, 4, v102\n"
    "v_add_u32 v101, 192, v100\n"
    "v_mul_u32_u24 v102, 7282, v101\n"
    "v_lshrrev_b32 v102, 16, v102\n"
    "v_and_b32 v103, 12, v102\n"
    "v_and_b32 v104, 16, v102\n"
    "v_mul_i32_i24 v104, -2592, v104\n"
    "v_mul_u32_u24 v103, 3456, v103\n"
    "v_mul_u32_u24 v102, 3312, v102\n"
    "v_add3_u32 v102, v102, v103, v104\n"
    "v_lshl_add_u32 v92, v101, 4, v102\n"
    "v_add_u32 v101, 256, v100\n"
    "v_mul_u32_u24 v102, 7282, v101\n"
    "v_lshrrev_b32 v102, 16, v102\n"
    "v_and_b32 v103, 12, v102\n"
    "v_and_b32 v104, 16, v102\n"
    "v_mul_i32_i24 v104, -2592, v104\n"
    "v_mul_u32_u24 v103, 3456, v103\n"
    "v_mul_u32_u24 v102, 3312, v102\n"
    "v_add3_u32 v102, v102, v103, v104\n"
    "v_lshl_add_u32 v93, v101, 4, v102\n"
    "v_add_u32 v101, 320, v100\n"
    "v_mul_u32_u24 v102, 7282, v101\n"
    "v_lshrrev_b32 v102, 16, v102\n"
    "v_and_b32 v103, 12, v102\n"
    "v_and_b32 v104, 16, v102\n"
    "v_mul_i32_i24 v104, -2592, v104\n"
    "v_mul_u32_u24 v103, 3456, v103\n"
    "v_mul_u32_u24 v102, 3312, v102\n"
    "v_add3_u32 v102, v102, v103, v104\n"
    "v_lshl_add_u32 v94, v101, 4, v102\n"
    "v_add_u32 v101, 384, v100\n"
    "v_mul_u32_u24 v102, 7282, v101\n"
    "v_lshrrev_b32 v102, 16, v102\n"
    "v_and_b32 v103, 12, v102\n"
    "v_and_b32 v104, 16, v102\n"
    "v_mul_i32_i24 v104, -2592, v104\n"
    "v_mul_u32_u24 v103, 3456, v103\n"
    "v_mul_u32_u24 v102, 3312, v102\n"
    "v_add3_u32 v102, v102, v103, v104\n"
    "v_lshl_add_u32 v95, v101, 4, v102\n"
    "v_add_u32 v101, 448, v100\n"
    "v_mul_u32_u24 v102, 7282, v101\n"
    "v_lshrrev_b32 v102, 16, v102\n"
    "v_and_b32 v103, 12, v102\n"
    "v_and_b32 v104, 16, v102\n"
    "v_mul_i32_i24 v104, -2592, v104\n"
    "v_mul_u32_u24 v103, 3456, v103\n"
    "v_mul_u32_u24 v102, 3312, v102\n"
    "v_add3_u32 v102, v102, v103, v104\n"
    "v_lshl_add_u32 v96, v101, 4, v102\n"
    "v_add_u32 v101, 512, v100\n"
    "v_mul_u32_u24 v102, 7282, v101\n"
    "v_lshrrev_b32 v102, 16, v102\n"
    "v_and_b32 v103, 12, v102\n"
    "v_and_b32 v104, 16, v102\n"
    "v_mul_i32_i24 v104, -2592, v104\n"
    "v_mul_u32_u24 v103, 3456, v103\n"
    "v_mul_u32_u24 v102, 3312, v102\n"
    "v_add3_u32 v102, v102, v103, v104\n"
    "v_lshl_add_u32 v97, v101, 4, v102\n"
    "v_add_u32 v9, 0x21c00, v3\n"
    "v_add_u32 v10, 1024, v6\n"
    "s_lshl_b32 s58, %2, 13\n"
    "s_add_u32 s58, s58, 0\n"
    "s_add_u32 s88, s40, 32768\n"
    "s_mov_b32 s89, s41\n"
    "s_cmp_lt_u32 %2, 2\n"
    "s_cselect_b32 s96, s42, 0\n"
    "s_mov_b32 s91, s43\n"
    "s_mul_i32 s59, %2, 576\n"
    "s_add_u32 s59, s59, 3072\n"
    "s_bfm_b64 s[60:61], 36, 0\n"
    "s_xor_b32 s50, %2, 0\n"
    "s_mul_i32 s50, s50, 2304\n"
    "v_add_u32 v106, s50, v98\n"
    "v_add_u32 v107, s50, v99\n"
    "s_xor_b32 s50, %2, 1\n"
    "s_mul_i32 s50, s50, 2304\n"
    "v_add_u32 v108, s50, v98\n"
    "v_add_u32 v109, s50, v99\n"
    "s_xor_b32 s50, %2, 2\n"
    "s_mul_i32 s50, s50, 2304\n"
    "v_add_u32 v110, s50, v98\n"
    "v_add_u32 v111, s50, v99\n"
    "s_xor_b32 s50, %2, 3\n"
    "s_mul_i32 s50, s50, 2304\n"
    "v_add_u32 v112, s50, v98\n"
    "v_add_u32 v113, s50, v99\n"
    "s_xor_b32 s50, %2, 4\n"
    "s_and_b32 s50, s50, 1\n"
    "s_lshl_b32 s50, s50, 1\n"
    "s_mul_i32 s50, s50, 2304\n"
    "v_add_u32 v114, s50, v98\n"
    "v_add_u32 v115, s50, v99\n"
    "s_xor_b32 s50, %2, 5\n"
    "s_and_b32 s50, s50, 1\n"
    "s_lshl_b32 s50, s50, 1\n"
    "s_mul_i32 s50, s50, 2304\n"
    "v_add_u32 v116, s50, v98\n"
    "v_add_u32 v117, s50, v99\n"
    "v_mov_b32 v56, 0x3f803f80\n"
    "v_mov_b32 v57, 0x3f803f80\n"
    "v_mov_b32 v58, 0x3f803f80\n"
    "v_mov_b32 v59, 0x3f803f80\n"
    "s_cmp_eq_u32 %2, 3\n"
    "s_cbranch_scc1 .Lac1_%=\n"
    "s_add_u32 s55, s51, 0\n"
    "s_add_u32 m0, s54, 0\n"
    "s_nop 0\n"
    "buffer_load_dwordx4 v80, s[44:47], s55 offen lds\n"
    "s_add_u32 m0, s54, 1024\n"
    "s_nop 0\n"
    "buffer_load_dwordx4 v81, s[44:47], s55 offen lds\n"
    "s_add_u32 m0, s54, 2048\n"
    "s_nop 0\n"
    "buffer_load_dwordx4 v82, s[44:47], s55 offen lds\n"
    "s_add_u32 m0, s54, 3072\n"
    "s_nop 0\n"
    "buffer_load_dwordx4 v83, s[44:47], s55 offen lds\n"
    "s_add_u32 m0, s54, 4096\n"
    "s_nop 0\n"
    "buffer_load_dwordx4 v84, s[44:47], s55 offen lds\n"
    "s_add_u32 m0, s54, 5120\n"
    "s_nop 0\n"
    "buffer_load_dwordx4 v85, s[44:47], s55 offen lds\n"
    "s_add_u32 m0, s54, 6144\n"
    "s_nop 0\n"
    "buffer_load_dwordx4 v86, s[44:47], s55 offen lds\n"
    "s_add_u32 m0, s54, 7168\n"
    "s_nop 0\n"
    "buffer_load_dwordx4 v87, s[44:47], s55 offen lds\n"
    "s_add_u32 m0, s54, 8192\n"
    "s_nop 0\n"
    "buffer_load_dwordx4 v88, s[44:47], s55 offen lds\n"
    ".Lac1_%=:\n"
    "s_cmp_eq_u32 %2, 3\n"
    "s_cbranch_scc1 .Lac2_%=\n"
    "s_add_u32 s55, s51, 13824\n"
    "s_add_u32 m0, s54, 27648\n"
    "s_nop 0\n"
    "buffer_load_dwordx4 v80, s[44:47], s55 offen lds\n"
    "s_add_u32 m0, s54, 28672\n"
    "s_nop 0\n"
    "buffer_load_dwordx4 v81, s[44:47], s55 offen lds\n"
    "s_add_u32 m0, s54, 29696\n"
    "s_nop 0\n"
    "buffer_load_dwordx4 v82, s[44:47], s55 offen lds\n"
    "s_add_u32 m0, s54, 30720\n"
    "s_nop 0\n"
    "buffer_load_dwordx4 v83, s[44:47], s55 offen lds\n"
    "s_add_u32 m0, s54, 31744\n"
    "s_nop 0\n"
    "buffer_load_dwordx4 v84, s[44:47], s55 offen lds\n"
    "s_add_u32 m0, s54, 32768\n"
    "s_nop 0\n"
    "buffer_load_dwordx4 v85, s[44:47], s55 offen lds\n"
    "s_add_u32 m0, s54, 33792\n"
    "s_nop 0\n"
    "buffer_load_dwordx4 v86, s[44:47], s55 offen lds\n"
    "s_add_u32 m0, s54, 34816\n"
    "s_nop 0\n"
    "buffer_load_dwordx4 v87, s[44:47], s55 offen lds\n"
    "s_add_u32 m0, s54, 35840\n"
    "s_nop 0\n"
    "buffer_load_dwordx4 v88, s[44:47], s55 offen lds\n"
    ".Lac2_%=:\n"
    "s_cmp_eq_u32 %2, 3\n"
    "s_cbranch_scc1 .Lac3_%=\n"
    "s_add_u32 s55, s51, 110592\n"
    "s_add_u32 m0, s54, 55296\n"
    "s_nop 0\n"
    "buffer_load_dwordx4 v80, s[44:47], s55 offen lds\n"
    "s_add_u32 m0, s54, 56320\n"
    "s_nop 0\n"
    "buffer_load_dwordx4 v81, s[44:47], s55 offen lds\n"
    "s_add_u32 m0, s54, 57344\n"
    "s_nop 0\n"
    "buffer_load_dwordx4 v82, s[44:47], s55 offen lds\n"
    "s_add_u32 m0, s54, 58368\n"
    "s_nop 0\n"
    "buffer_load_dwordx4 v83, s[44:47], s55 offen lds\n"
    "s_add_u32 m0, s54, 59392\n"
    "s_nop 0\n"
    "buffer_load_dwordx4 v84, s[44:47], s55 offen lds\n"
    "s_add_u32 m0, s54, 60416\n"
    "s_nop 0\n"
    "buffer_load_dwordx4 v85, s[44:47], s55 offen lds\n"
    "s_add_u32 m0, s54, 61440\n"
    "s_nop 0\n"
    "buffer_load_dwordx4 v86, s[44:47], s55 offen lds\n"
    "s_add_u32 m0, s54, 62464\n"
    "s_nop 0\n"
    "buffer_load_dwordx4 v87, s[44:47], s55 offen lds\n"
    "s_add_u32 m0, s54, 63488\n"
    "s_nop 0\n"
    "buffer_load_dwordx4 v88, s[44:47], s55 offen lds\n"
    ".Lac3_%=:\n"
    "s_cmp_eq_u32 %2, 3\n"
    "s_cbranch_scc1 .Lac4_%=\n"
    "s_add_u32 s55, s51, 124416\n"
    "s_add_u32 m0, s54, 82944\n"
    "s_nop 0\n"
    "buffer_load_dwordx4 v80, s[44:47], s55 offen lds\n"
    "s_add_u32 m0, s54, 83968\n"
    "s_nop 0\n"
    "buffer_load_dwordx4 v81, s[44:47], s55 offen lds\n"
    "s_add_u32 m0, s54, 84992\n"
    "s_nop 0\n"
    "buffer_load_dwordx4 v82, s[44:47], s55 offen lds\n"
    "s_add_u32 m0, s54, 86016\n"
    "s_nop 0\n"
    "buffer_load_dwordx4 v83, s[44:47], s55 offen lds\n"
    "s_add_u32 m0, s54, 87040\n"
    "s_nop 0\n"
    "buffer_load_dwordx4 v84, s[44:47], s55 offen lds\n"
    "s_add_u32 m0, s54, 88064\n"
    "s_nop 0\n"
    "buffer_load_dwordx4 v85, s[44:47], s55 offen lds\n"
    "s_add_u32 m0, s54, 89088\n"
    "s_nop 0\n"
    "buffer_load_dwordx4 v86, s[44:47], s55 offen lds\n"
    "s_add_u32 m0, s54, 90112\n"
    "s_nop 0\n"
    "buffer_load_dwordx4 v87, s[44:47], s55 offen lds\n"
    "s_add_u32 m0, s54, 91136\n"
    "s_nop 0\n"
    "buffer_load_dwordx4 v88, s[44:47], s55 offen lds\n"
    ".Lac4_%=:\n"
    "s_cmp_eq_u32 %2, 3\n"
    "s_cbranch_scc1 .Lac5_%=\n"
    "s_add_u32 s55, s51, 221184\n"
    "s_add_u32 m0, s54, 110592\n"
    "s_nop 0\n"
    "buffer_load_dwordx4 v80, s[44:47], s55 offen lds\n"
    "s_add_u32 m0, s54, 111616\n"
    "s_nop 0\n"
    "buffer_load_dwordx4 v81, s[44:47], s55 offen lds\n"
    "s_add_u32 m0, s54, 112640\n"
    "s_nop 0\n"
    "buffer_load_dwordx4 v82, s[44:47], s55 offen lds\n"
    "s_add_u32 m0, s54, 113664\n"
    "s_nop 0\n"
    "buffer_load_dwordx4 v83, s[44:47], s55 offen lds\n"
    "s_add_u32 m0, s54, 114688\n"
    "s_nop 0\n"
    "buffer_load_dwordx4 v84, s[44:47], s55 offen lds\n"
    "s_add_u32 m0, s54, 115712\n"
    "s_nop 0\n"
    "buffer_load_dwordx4 v85, s[44:47], s55 offen lds\n"
    "s_add_u32 m0, s54, 116736\n"
    "s_nop 0\n"
    "buffer_load_dwordx4 v86, s[44:47], s55 offen lds\n"
    "s_add_u32 m0, s54, 117760\n"
    "s_nop 0\n"
    "buffer_load_dwordx4 v87, s[44:47], s55 offen lds\n"
    "s_add_u32 m0, s54, 118784\n"
    "s_nop 0\n"
    "buffer_load_dwordx4 v88, s[44:47], s55 offen lds\n"
    ".Lac5_%=:\n"
    "s_waitcnt vmcnt(36) lgkmcnt(0)\n"
    "s_barrier\n"
    "ds_read_b64_tr_b16 a[0:1], v106\n"
    "ds_read_b64_tr_b16 a[72:73], v106 offset:9216\n"
    "ds_read_b64_tr_b16 a[144:145], v106 offset:18432\n"
    "ds_read_b64_tr_b16 a[216:217], v107\n"
    "ds_read_b64_tr_b16 a[16:17], v108\n"
    "ds_read_b64_tr_b16 a[88:89], v108 offset:9216\n"
    "ds_read_b64_tr_b16 a[160:161], v108 offset:18432\n"
    "ds_read_b64_tr_b16 a[32:33], v110\n"
    "ds_read_b64_tr_b16 a[104:105], v110 offset:9216\n"
    "ds_read_b64_tr_b16 a[176:177], v110 offset:18432\n"
    "ds_read_b64_tr_b16 a[232:233], v111\n"
    "ds_read_b64_tr_b16 a[48:49], v112\n"
    "ds_read_b64_tr_b16 a[120:121], v112 offset:9216\n"
    "ds_read_b64_tr_b16 a[192:193], v112 offset:18432\n"
    "s_waitcnt vmcnt(27) lgkmcnt(0)\n"
    "s_barrier\n"
    "s_cmp_eq_u32 %2, 3\n"
    "s_cbranch_scc1 .Lac6_%=\n"
    "s_add_u32 s55, s51, 235008\n"
    "s_add_u32 m0, s54, 0\n"
    "s_nop 0\n"
    "buffer_load_dwordx4 v80, s[44:47], s55 offen lds\n"
    "s_add_u32 m0, s54, 1024\n"
    "s_nop 0\n"
    "buffer_load_dwordx4 v81, s[44:47], s55 offen lds\n"
    "s_add_u32 m0, s54, 2048\n"
    "s_nop 0\n"
    "buffer_load_dwordx4 v82, s[44:47], s55 offen lds\n"
    "s_add_u32 m0, s54, 3072\n"
    "s_nop 0\n"
    "buffer_load_dwordx4 v83, s[44:47], s55 offen lds\n"
    "s_add_u32 m0, s54, 4096\n"
    "s_nop 0\n"
    "buffer_load_dwordx4 v84, s[44:47], s55 offen lds\n"
    "s_add_u32 m0, s54, 5120\n"
    "s_nop 0\n"
    "buffer_load_dwordx4 v85, s[44:47], s55 offen lds\n"
    "s_add_u32 m0, s54, 6144\n"
    "s_nop 0\n"
    "buffer_load_dwordx4 v86, s[44:47], s55 offen lds\n"
    "s_add_u32 m0, s54, 7168\n"
    "s_nop 0\n"
    "buffer_load_dwordx4 v87, s[44:47], s55 offen lds\n"
    "s_add_u32 m0, s54, 8192\n"
    "s_nop 0\n"
    "buffer_load_dwordx4 v88, s[44:47], s55 offen lds\n"
    ".Lac6_%=:\n"
    "ds_read_b64_tr_b16 a[2:3], v106 offset:27648\n"
    "ds_read_b64_tr_b16 a[74:75], v106 offset:36864\n"
    "ds_read_b64_tr_b16 a[146:147], v106 offset:46080\n"
    "ds_read_b64_tr_b16 a[218:219], v107 offset:27648\n"
    "ds_read_b64_tr_b16 a[18:19], v108 offset:27648\n"
    "ds_read_b64_tr_b16 a[90:91], v108 offset:36864\n"
    "ds_read_b64_tr_b16 a[162:163], v108 offset:46080\n"
    "ds_read_b64_tr_b16 a[34:35], v110 offset:27648\n"
    "ds_read_b64_tr_b16 a[106:107], v110 offset:36864\n"
    "ds_read_b64_tr_b16 a[178:179], v110 offset:46080\n"
    "ds_read_b64_tr_b16 a[234:235], v111 offset:27648\n"
    "ds_read_b64_tr_b16 a[50:51], v112 offset:27648\n"
    "ds_read_b64_tr_b16 a[122:123], v112 offset:36864\n"
    "ds_read_b64_tr_b16 a[194:195], v112 offset:46080\n"
    "s_waitcnt vmcnt(27) lgkmcnt(0)\n"
    "s_barrier\n"
    "s_cmp_eq_u32 %2, 3\n"
    "s_cbranch_scc1 .Lac7_%=\n"
    "s_add_u32 s55, s51, 331776\n"
    "s_add_u32 m0, s54, 27648\n"
    "s_nop 0\n"
    "buffer_load_dwordx4 v80, s[44:47], s55 offen lds\n"
    "s_add_u32 m0, s54, 28672\n"
    "s_nop 0\n"
    "buffer_load_dwordx4 v81, s[44:47], s55 offen lds\n"
    "s_add_u32 m0, s54, 29696\n"
    "s_nop 0\n"
    "buffer_load_dwordx4 v82, s[44:47], s55 offen lds\n"
    "s_add_u32 m0, s54, 30720\n"
    "s_nop 0\n"
    "buffer_load_dwordx4 v83, s[44:47], s55 offen lds\n"
    "s_add_u32 m0, s54, 31744\n"
    "s_nop 0\n"
    "buffer_load_dwordx4 v84, s[44:47], s55 offen lds\n"
    "s_add_u32 m0, s54, 32768\n"
    "s_nop 0\n"
    "buffer_load_dwordx4 v85, s[44:47], s55 offen lds\n"
    "s_add_u32 m0, s54, 33792\n"
    "s_nop 0\n"
    "buffer_load_dwordx4 v86, s[44:47], s55 offen lds\n"
    "s_add_u32 m0, s54, 34816\n"
    "s_nop 0\n"
    "buffer_load_dwordx4 v87, s[44:47], s55 offen lds\n"
    "s_add_u32 m0, s54, 35840\n"
    "s_nop 0\n"
    "buffer_load_dwordx4 v88, s[44:47], s55 offen lds\n"
    ".Lac7_%=:\n"
    "v_mfma_f32_16x16x32_bf16 v[12:15], v[56:59], a[0:3], 0\n"
    "v_mfma_f32_16x16x32_bf16 v[16:19], v[56:59], a[72:75], 0\n"
    "v_mfma_f32_16x16x32_bf16 v[20:23], v[56:59], a[144:147], 0\n"
    "v_mfma_f32_16x16x32_bf16 v[24:27], v[56:59], a[216:219], 0\n"
    "v_mfma_f32_16x16x32_bf16 v[12:15], v[56:59], a[16:19], v[12:15]\n"
    "v_mfma_f32_16x16x32_bf16 v[16:19], v[56:59], a[88:91], v[16:19]\n"
    "v_mfma_f32_16x16x32_bf16 v[20:23], v[56:59], a[160:163], v[20:23]\n"
    "v_mfma_f32_16x16x32_bf16 v[12:15], v[56:59], a[32:35], v[12:15]\n"
    "v_mfma_f32_16x16x32_bf16 v[16:19], v[56:59], a[104:107], v[16:19]\n"
    "v_mfma_f32_16x16x32_bf16 v[20:23], v[56:59], a[176:179], v[20:23]\n"
    "v_mfma_f32_16x16x32_bf16 v[24:27], v[56:59], a[232:235], v[24:27]\n"
    "v_mfma_f32_16x16x32_bf16 v[12:15], v[56:59], a[48:51], v[12:15]\n"
    "v_mfma_f32_16x16x32_bf16 v[16:19], v[56:59], a[120:123], v[16:19]\n"
    "v_mfma_f32_16x16x32_bf16 v[20:23], v[56:59], a[192:195], v[20:23]\n"
    "ds_read_b64_tr_b16 a[4:5], v106 offset:55296\n"
    "ds_read_b64_tr_b16 a[76:77], v106 offset:64512\n"
    "v_add_u32 v149, 0x10000, v106\n"
    "ds_read_b64_tr_b16 a[148:149], v149 offset:8192\n"
    "ds_read_b64_tr_b16 a[220:221], v107 offset:55296\n"
    "ds_read_b64_tr_b16 a[20:21], v108 offset:55296\n"
    "ds_read_b64_tr_b16 a[92:93], v108 offset:64512\n"
    "v_add_u32 v148, 0x10000, v108\n"
    "ds_read_b64_tr_b16 a[164:165], v148 offset:8192\n"
    "ds_read_b64_tr_b16 a[36:37], v110 offset:55296\n"
    "ds_read_b64_tr_b16 a[108:109], v110 offset:64512\n"
    "v_add_u32 v147, 0x10000, v110\n"
    "ds_read_b64_tr_b16 a[180:181], v147 offset:8192\n"
    "ds_read_b64_tr_b16 a[236:237], v111 offset:55296\n"
    "ds_read_b64_tr_b16 a[52:53], v112 offset:55296\n"
    "ds_read_b64_tr_b16 a[124:125], v112 offset:64512\n"
    "v_add_u32 v146, 0x10000, v112\n"
    "ds_read_b64_tr_b16 a[196:197], v146 offset:8192\n"
    "s_waitcnt vmcnt(27) lgkmcnt(0)\n"
    "s_barrier\n"
    "s_cmp_eq_u32 %2, 3\n"
    "s_cbranch_scc1 .Lac8_%=\n"
    "s_add_u32 s55, s51, 345600\n"
    "s_add_u32 m0, s54, 55296\n"
    "s_nop 0\n"
    "buffer_load_dwordx4 v80, s[44:47], s55 offen lds\n"
    "s_add_u32 m0, s54, 56320\n"
    "s_nop 0\n"
    "buffer_load_dwordx4 v81, s[44:47], s55 offen lds\n"
    "s_add_u32 m0, s54, 57344\n"
    "s_nop 0\n"
    "buffer_load_dwordx4 v82, s[44:47], s55 offen lds\n"
    "s_add_u32 m0, s54, 58368\n"
    "s_nop 0\n"
    "buffer_load_dwordx4 v83, s[44:47], s55 offen lds\n"
    "s_add_u32 m0, s54, 59392\n"
    "s_nop 0\n"
    "buffer_load_dwordx4 v84, s[44:47], s55 offen lds\n"
    "s_add_u32 m0, s54, 60416\n"
    "s_nop 0\n"
    "buffer_load_dwordx4 v85, s[44:47], s55 offen lds\n"
    "s_add_u32 m0, s54, 61440\n"
    "s_nop 0\n"
    "buffer_load_dwordx4 v86, s[44:47], s55 offen lds\n"
    "s_add_u32 m0, s54, 62464\n"
    "s_nop 0\n"
    "buffer_load_dwordx4 v87, s[44:47], s55 offen lds\n"
    "s_add_u32 m0, s54, 63488\n"
    "s_nop 0\n"
    "buffer_load_dwordx4 v88, s[44:47], s55 offen lds\n"
    ".Lac8_%=:\n"
    "ds_read_b64_tr_b16 a[6:7], v149 offset:17408\n"
    "ds_read_b64_tr_b16 a[78:79], v149 offset:26624\n"
    "ds_read_b64_tr_b16 a[150:151], v149 offset:35840\n"
    "v_add_u32 v145, 0x10000, v107\n"
    "ds_read_b64_tr_b16 a[222:223], v145 offset:17408\n"
    "ds_read_b64_tr_b16 a[22:23], v148 offset:17408\n"
    "ds_read_b64_tr_b16 a[94:95], v148 offset:26624\n"
    "ds_read_b64_tr_b16 a[166:167], v148 offset:35840\n"
    "ds_read_b64_tr_b16 a[38:39], v147 offset:17408\n"
    "ds_read_b64_tr_b16 a[110:111], v147 offset:26624\n"
    "ds_read_b64_tr_b16 a[182:183], v147 offset:35840\n"
    "v_add_u32 v144, 0x10000, v111\n"
    "ds_read_b64_tr_b16 a[238:239], v144 offset:17408\n"
    "ds_read_b64_tr_b16 a[54:55], v146 offset:17408\n"
    "ds_read_b64_tr_b16 a[126:127], v146 offset:26624\n"
    "ds_read_b64_tr_b16 a[198:199], v146 offset:35840\n"
    "s_waitcnt vmcnt(27) lgkmcnt(0)\n"
    "s_barrier\n"
    "s_cmp_eq_u32 %2, 3\n"
    "s_cbranch_scc1 .Lac9_%=\n"
    "s_add_u32 s55, s51, 1769472\n"
    "s_add_u32 m0, s54, 82944\n"
    "s_nop 0\n"
    "buffer_load_dwordx4 v89, s[44:47], s55 offen lds\n"
    "s_add_u32 m0, s54, 83968\n"
    "s_nop 0\n"
    "buffer_load_dwordx4 v90, s[44:47], s55 offen lds\n"
    "s_add_u32 m0, s54, 84992\n"
    "s_nop 0\n"
    "buffer_load_dwordx4 v91, s[44:47], s55 offen lds\n"
    "s_add_u32 m0, s54, 86016\n"
    "s_nop 0\n"
    "buffer_load_dwordx4 v92, s[44:47], s55 offen lds\n"
    "s_add_u32 m0, s54, 87040\n"
    "s_nop 0\n"
    "buffer_load_dwordx4 v93, s[44:47], s55 offen lds\n"
    "s_add_u32 m0, s54, 88064\n"
    "s_nop 0\n"
    "buffer_load_dwordx4 v94, s[44:47], s55 offen lds\n"
    "s_add_u32 m0, s54, 89088\n"
    "s_nop 0\n"
    "buffer_load_dwordx4 v95, s[44:47], s55 offen lds\n"
    "s_add_u32 m0, s54, 90112\n"
    "s_nop 0\n"
    "buffer_load_dwordx4 v96, s[44:47], s55 offen lds\n"
    "s_add_u32 m0, s54, 91136\n"
    "s_nop 0\n"
    "buffer_load_dwordx4 v97, s[44:47], s55 offen lds\n"
    ".Lac9_%=:\n"
    "v_mov_b32 v11, 0x20400\n"
    "global_load_dwordx2 v[76:77], v11, %1 sc1\n"
    "v_mbcnt_lo_u32_b32 v72, -1, 0\n"
    "v_min_u32 v72, 7, v72\n"
    "v_lshlrev_b32 v72, 7, v72\n"
    "v_add_u32 v72, 0x20000, v72\n"
    "global_load_dwordx2 v[78:79], v72, %1 sc1\n"
    "v_mfma_f32_16x16x32_bf16 v[12:15], v[56:59], a[4:7], v[12:15]\n"
    "v_mfma_f32_16x16x32_bf16 v[16:19], v[56:59], a[76:79], v[16:19]\n"
    "v_mfma_f32_16x16x32_bf16 v[20:23], v[56:59], a[148:151], v[20:23]\n"
    "v_mfma_f32_16x16x32_bf16 v[24:27], v[56:59], a[220:223], v[24:27]\n"
    "v_mfma_f32_16x16x32_bf16 v[12:15], v[56:59], a[20:23], v[12:15]\n"
    "v_mfma_f32_16x16x32_bf16 v[16:19], v[56:59], a[92:95], v[16:19]\n"
    "v_mfma_f32_16x16x32_bf16 v[20:23], v[56:59], a[164:167], v[20:23]\n"
    "v_mfma_f32_16x16x32_bf16 v[12:15], v[56:59], a[36:39], v[12:15]\n"
    "v_mfma_f32_16x16x32_bf16 v[16:19], v[56:59], a[108:111], v[16:19]\n"
    "v_mfma_f32_16x16x32_bf16 v[20:23], v[56:59], a[180:183], v[20:23]\n"
    "v_mfma_f32_16x16x32_bf16 v[24:27], v[56:59], a[236:239], v[24:27]\n"
    "v_mfma_f32_16x16x32_bf16 v[12:15], v[56:59], a[52:55], v[12:15]\n"
    "v_mfma_f32_16x16x32_bf16 v[16:19], v[56:59], a[124:127], v[16:19]\n"
    "v_mfma_f32_16x16x32_bf16 v[20:23], v[56:59], a[196:199], v[20:23]\n"
    "ds_read_b64_tr_b16 a[8:9], v149 offset:45056\n"
    "ds_read_b64_tr_b16 a[80:81], v149 offset:54272\n"
    "ds_read_b64_tr_b16 a[152:153], v149 offset:63488\n"
    "ds_read_b64_tr_b16 a[224:225], v145 offset:45056\n"
    "ds_read_b64_tr_b16 a[24:25], v148 offset:45056\n"
    "ds_read_b64_tr_b16 a[96:97], v148 offset:54272\n"
    "ds_read_b64_tr_b16 a[168:169], v148 offset:63488\n"
    "ds_read_b64_tr_b16 a[40:41], v147 offset:45056\n"
    "ds_read_b64_tr_b16 a[112:113], v147 offset:54272\n"
    "ds_read_b64_tr_b16 a[184:185], v147 offset:63488\n"
    "ds_read_b64_tr_b16 a[240:241], v144 offset:45056\n"
    "ds_read_b64_tr_b16 a[56:57], v146 offset:45056\n"
    "ds_read_b64_tr_b16 a[128:129], v146 offset:54272\n"
    "ds_read_b64_tr_b16 a[200:201], v146 offset:63488\n"
    "s_waitcnt vmcnt(29) lgkmcnt(0)\n"
    "s_barrier\n"
    "ds_read_b64_tr_b16 a[10:11], v106\n"
    "ds_read_b64_tr_b16 a[82:83], v106 offset:9216\n"
    "ds_read_b64_tr_b16 a[154:155], v106 offset:18432\n"
    "ds_read_b64_tr_b16 a[226:227], v107\n"
    "ds_read_b64_tr_b16 a[26:27], v108\n"
    "ds_read_b64_tr_b16 a[98:99], v108 offset:9216\n"
    "ds_read_b64_tr_b16 a[170:171], v108 offset:18432\n"
    "ds_read_b64_tr_b16 a[42:43], v110\n"
    "ds_read_b64_tr_b16 a[114:115], v110 offset:9216\n"
    "ds_read_b64_tr_b16 a[186:187], v110 offset:18432\n"
    "ds_read_b64_tr_b16 a[242:243], v111\n"
    "ds_read_b64_tr_b16 a[58:59], v112\n"
    "ds_read_b64_tr_b16 a[130:131], v112 offset:9216\n"
    "ds_read_b64_tr_b16 a[202:203], v112 offset:18432\n"
    "s_waitcnt vmcnt(20) lgkmcnt(0)\n"
    "s_barrier\n"
    "v_mfma_f32_16x16x32_bf16 v[12:15], v[56:59], a[8:11], v[12:15]\n"
    "v_mfma_f32_16x16x32_bf16 v[16:19], v[56:59], a[80:83], v[16:19]\n"
    "v_mfma_f32_16x16x32_bf16 v[20:23], v[56:59], a[152:155], v[20:23]\n"
    "v_mfma_f32_16x16x32_bf16 v[24:27], v[56:59], a[224:227], v[24:27]\n"
    "v_mfma_f32_16x16x32_bf16 v[12:15], v[56:59], a[24:27], v[12:15]\n"
    "v_mfma_f32_16x16x32_bf16 v[16:19], v[56:59], a[96:99], v[16:19]\n"
    "v_mfma_f32_16x16x32_bf16 v[20:23], v[56:59], a[168:171], v[20:23]\n"
    "v_mfma_f32_16x16x32_bf16 v[12:15], v[56:59], a[40:43], v[12:15]\n"
    "v_mfma_f32_16x16x32_bf16 v[16:19], v[56:59], a[112:115], v[16:19]\n"
    "v_mfma_f32_16x16x32_bf16 v[20:23], v[56:59], a[184:187], v[20:23]\n"
    "v_mfma_f32_16x16x32_bf16 v[24:27], v[56:59], a[240:243], v[24:27]\n"
    "v_mfma_f32_16x16x32_bf16 v[12:15], v[56:59], a[56:59], v[12:15]\n"
    "v_mfma_f32_16x16x32_bf16 v[16:19], v[56:59], a[128:131], v[16:19]\n"
    "v_mfma_f32_16x16x32_bf16 v[20:23], v[56:59], a[200:203], v[20:23]\n"
    "ds_read_b64_tr_b16 a[12:13], v106 offset:27648\n"
    "ds_read_b64_tr_b16 a[84:85], v106 offset:36864\n"
    "ds_read_b64_tr_b16 a[156:157], v106 offset:46080\n"
    "ds_read_b64_tr_b16 a[228:229], v107 offset:27648\n"
    "ds_read_b64_tr_b16 a[28:29], v108 offset:27648\n"
    "ds_read_b64_tr_b16 a[100:101], v108 offset:36864\n"
    "ds_read_b64_tr_b16 a[172:173], v108 offset:46080\n"
    "ds_read_b64_tr_b16 a[44:45], v110 offset:27648\n"
    "ds_read_b64_tr_b16 a[116:117], v110 offset:36864\n"
    "ds_read_b64_tr_b16 a[188:189], v110 offset:46080\n"
    "ds_read_b64_tr_b16 a[244:245], v111 offset:27648\n"
    "ds_read_b64_tr_b16 a[60:61], v112 offset:27648\n"
    "ds_read_b64_tr_b16 a[132:133], v112 offset:36864\n"
    "ds_read_b64_tr_b16 a[204:205], v112 offset:46080\n"
    "s_waitcnt vmcnt(11) lgkmcnt(0)\n"
    "s_barrier\n"
    "ds_read_b64_tr_b16 a[14:15], v106 offset:55296\n"
    "ds_read_b64_tr_b16 a[86:87], v106 offset:64512\n"
    "ds_read_b64_tr_b16 a[158:159], v149 offset:8192\n"
    "ds_read_b64_tr_b16 a[230:231], v107 offset:55296\n"
    "ds_read_b64_tr_b16 a[30:31], v108 offset:55296\n"
    "ds_read_b64_tr_b16 a[102:103], v108 offset:64512\n"
    "ds_read_b64_tr_b16 a[174:175], v148 offset:8192\n"
    "ds_read_b64_tr_b16 a[46:47], v110 offset:55296\n"
    "ds_read_b64_tr_b16 a[118:119], v110 offset:64512\n"
    "ds_read_b64_tr_b16 a[190:191], v147 offset:8192\n"
    "ds_read_b64_tr_b16 a[246:247], v111 offset:55296\n"
    "ds_read_b64_tr_b16 a[62:63], v112 offset:55296\n"
    "ds_read_b64_tr_b16 a[134:135], v112 offset:64512\n"
    "ds_read_b64_tr_b16 a[206:207], v146 offset:8192\n"
    "s_waitcnt vmcnt(2) lgkmcnt(0)\n"
    "s_barrier\n"
    "v_mfma_f32_16x16x32_bf16 v[12:15], v[56:59], a[12:15], v[12:15]\n"
    "v_mfma_f32_16x16x32_bf16 v[16:19], v[56:59], a[84:87], v[16:19]\n"
    "v_mfma_f32_16x16x32_bf16 v[20:23], v[56:59], a[156:159], v[20:23]\n"
    "v_mfma_f32_16x16x32_bf16 v[24:27], v[56:59], a[228:231], v[24:27]\n"
    "v_mfma_f32_16x16x32_bf16 v[12:15], v[56:59], a[28:31], v[12:15]\n"
    "v_mfma_f32_16x16x32_bf16 v[16:19], v[56:59], a[100:103], v[16:19]\n"
    "v_mfma_f32_16x16x32_bf16 v[20:23], v[56:59], a[172:175], v[20:23]\n"
    "v_mfma_f32_16x16x32_bf16 v[12:15], v[56:59], a[44:47], v[12:15]\n"
    "v_mfma_f32_16x16x32_bf16 v[16:19], v[56:59], a[116:119], v[16:19]\n"
    "v_mfma_f32_16x16x32_bf16 v[20:23], v[56:59], a[188:191], v[20:23]\n"
    "v_mfma_f32_16x16x32_bf16 v[24:27], v[56:59], a[244:247], v[24:27]\n"
    "v_mfma_f32_16x16x32_bf16 v[12:15], v[56:59], a[60:63], v[12:15]\n"
    "v_mfma_f32_16x16x32_bf16 v[16:19], v[56:59], a[132:135], v[16:19]\n"
    "v_mfma_f32_16x16x32_bf16 v[20:23], v[56:59], a[204:207], v[20:23]\n"
    "v_add_u32 v143, 0x10000, v114\n"
    "ds_read_b64_tr_b16 a[64:65], v143 offset:17408\n"
    "ds_read_b64_tr_b16 a[136:137], v143 offset:26624\n"
    "ds_read_b64_tr_b16 a[208:209], v143 offset:35840\n"
    "v_add_u32 v142, 0x10000, v115\n"
    "ds_read_b64_tr_b16 a[248:249], v142 offset:17408\n"
    "ds_read_b64_tr_b16 a[66:67], v143 offset:19712\n"
    "ds_read_b64_tr_b16 a[138:139], v143 offset:28928\n"
    "ds_read_b64_tr_b16 a[210:211], v143 offset:38144\n"
    "ds_read_b64_tr_b16 a[250:251], v142 offset:19712\n"
    "v_add_u32 v141, 0x10000, v116\n"
    "ds_read_b64_tr_b16 a[68:69], v141 offset:17408\n"
    "ds_read_b64_tr_b16 a[140:141], v141 offset:26624\n"
    "ds_read_b64_tr_b16 a[212:213], v141 offset:35840\n"
    "ds_read_b64_tr_b16 a[70:71], v141 offset:19712\n"
    "ds_read_b64_tr_b16 a[142:143], v141 offset:28928\n"
    "ds_read_b64_tr_b16 a[214:215], v141 offset:38144\n"
    "s_waitcnt lgkmcnt(0)\n"
    "s_barrier\n"
    "v_mfma_f32_16x16x32_bf16 v[12:15], v[56:59], a[64:67], v[12:15]\n"
    "v_mfma_f32_16x16x32_bf16 v[16:19], v[56:59], a[136:139], v[16:19]\n"
    "v_mfma_f32_16x16x32_bf16 v[20:23], v[56:59], a[208:211], v[20:23]\n"
    "v_mfma_f32_16x16x32_bf16 v[24:27], v[56:59], a[248:251], v[24:27]\n"
    "v_mfma_f32_16x16x32_bf16 v[12:15], v[56:59], a[68:71], v[12:15]\n"
    "v_mfma_f32_16x16x32_bf16 v[16:19], v[56:59], a[140:143], v[16:19]\n"
    "v_mfma_f32_16x16x32_bf16 v[20:23], v[56:59], a[212:215], v[20:23]\n"
    "v_mov_b32 v28, 0\n"
    "v_mov_b32 v29, 0\n"
    "v_mov_b32 v30, 0\n"
    "v_mov_b32 v31, 0\n"
    "v_mov_b32 v199, 0\n"
    "v_mov_b32 v215, 0\n"
    "v_mov_b32 v218, 0\n"
    "v_mov_b32 v219, 0\n"
    "v_add_u32 v73, 0x14000, v3\n"
    "ds_write_b128 v73, v[28:31]\n"
    "ds_write_b128 v73, v[28:31] offset:4096\n"
    "ds_write_b128 v73, v[28:31] offset:8192\n"
    "ds_write_b128 v73, v[28:31] offset:16384\n"
    "ds_write_b128 v73, v[28:31] offset:20480\n"
    "ds_write_b128 v73, v[28:31] offset:24576\n"
    "v_mov_b32 v153, 0xf149f2ca\n"
    "v_add_u32 v103, 7, v152\n"
    "v_cmp_gt_u32 vcc, 8, v103\n"
    "s_nop 1\n"
    "v_cndmask_b32 v44, v153, v28, vcc\n"
    "v_cmp_le_i32 vcc, 9, v152\n"
    "s_nop 1\n"
    "v_cndmask_b32 v68, v153, v28, vcc\n"
    "v_add_u32 v103, 8, v152\n"
    "v_cmp_gt_u32 vcc, 8, v103\n"
    "s_nop 1\n"
    "v_cndmask_b32 v45, v153, v28, vcc\n"
    "v_cmp_le_i32 vcc, 8, v152\n"
    "s_nop 1\n"
    "v_cndmask_b32 v69, v153, v28, vcc\n"
    "v_add_u32 v103, 9, v152\n"
    "v_cmp_gt_u32 vcc, 8, v103\n"
    "s_nop 1\n"
    "v_cndmask_b32 v46, v153, v28, vcc\n"
    "v_cmp_le_i32 vcc, 7, v152\n"
    "s_nop 1\n"
    "v_cndmask_b32 v70, v153, v28, vcc\n"
    "v_add_u32 v103, 10, v152\n"
    "v_cmp_gt_u32 vcc, 8, v103\n"
    "s_nop 1\n"
    "v_cndmask_b32 v47, v153, v28, vcc\n"
    "v_cmp_le_i32 vcc, 6, v152\n"
    "s_nop 1\n"
    "v_cndmask_b32 v71, v153, v28, vcc\n"
    "s_mov_b32 s86, 0xcccccccc\n"
    "s_mov_b32 s87, 0xcccccccc\n"
    "v_cvt_pk_bf16_f32 v13, 0, v12\n"
    "v_sub_f32 v12, v12, v13\n"
    "v_cvt_pk_bf16_f32 v14, 0, v12\n"
    "v_sub_f32 v12, v12, v14\n"
    "v_cvt_pk_bf16_f32 v13, v13, v14\n"
    "v_cvt_pk_bf16_f32 v14, v12, 0\n"
    "s_mov_b64 vcc, 0xffff\n"
    "v_cndmask_b32 v60, 0, v13, vcc\n"
    "v_cndmask_b32 v61, 0, v14, vcc\n"
    "v_cvt_pk_bf16_f32 v17, 0, v16\n"
    "v_sub_f32 v16, v16, v17\n"
    "v_cvt_pk_bf16_f32 v18, 0, v16\n"
    "v_sub_f32 v16, v16, v18\n"
    "v_cvt_pk_bf16_f32 v17, v17, v18\n"
    "v_cvt_pk_bf16_f32 v18, v16, 0\n"
    "s_mov_b64 vcc, 0xffff\n"
    "v_cndmask_b32 v62, 0, v17, vcc\n"
    "v_cndmask_b32 v63, 0, v18, vcc\n"
    "v_cvt_pk_bf16_f32 v21, 0, v20\n"
    "v_sub_f32 v20, v20, v21\n"
    "v_cvt_pk_bf16_f32 v22, 0, v20\n"
    "v_sub_f32 v20, v20, v22\n"
    "v_cvt_pk_bf16_f32 v21, v21, v22\n"
    "v_cvt_pk_bf16_f32 v22, v20, 0\n"
    "s_mov_b64 vcc, 0xffff\n"
    "v_cndmask_b32 v64, 0, v21, vcc\n"
    "v_cndmask_b32 v65, 0, v22, vcc\n"
    "v_cvt_pk_bf16_f32 v25, 0, v24\n"
    "v_sub_f32 v24, v24, v25\n"
    "v_cvt_pk_bf16_f32 v26, 0, v24\n"
    "v_sub_f32 v24, v24, v26\n"
    "v_cvt_pk_bf16_f32 v25, v25, v26\n"
    "v_cvt_pk_bf16_f32 v26, v24, 0\n"
    "s_mov_b64 vcc, 0xffff\n"
    "v_cndmask_b32 v66, 0, v25, vcc\n"
    "v_cndmask_b32 v67, 0, v26, vcc\n"
    "s_waitcnt vmcnt(0)\n"
    "v_add_u32 v76, 1, v76\n"
    "v_add_u32 v77, 1, v77\n"
    "s_mov_b32 s52, 1048576\n"
    ".Lago_a%=:\n"
    "v_cmp_ne_u64 vcc, v[76:77], v[78:79]\n"
    "s_cbranch_vccz .Lago_x%=\n"
    "global_load_dwordx2 v[78:79], v72, %1 sc1\n"
    "s_sub_u32 s52, s52, 1\n"
    "s_waitcnt vmcnt(0)\n"
    "s_cbranch_scc0 .Lago_a%=\n"
    ".Lago_x%=:\n"
    "v_mbcnt_lo_u32_b32 v76, -1, 0\n"
    "v_mbcnt_hi_u32_b32 v76, -1, v76\n"
    "v_min_u32 v72, 5, v76\n"
    "v_lshlrev_b32 v72, 7, v72\n"
    "v_mov_b32 v11, 0x26c00\n"
    "ds_read_b128 v[76:79], v11\n"
    "v_mov_b32 v55, 0\n"
    "s_waitcnt lgkmcnt(0)\n"
    "v_readfirstlane_b32 s49, v77\n"
    "v_add_u32 v76, v76, v72\n"
    "s_branch .Lachk_%=\n"
    ".Lago_%=:\n"
    "s_waitcnt vmcnt(1)\n"
    "v_add_u32 v11, 32, v11\n"
    "ds_read_b128 v[76:79], v11\n"
    "ds_read2_b32 v[74:75], v11 offset0:5 offset1:6\n"
    ".Latop_%=:\n"
    "s_waitcnt lgkmcnt(5)\n"
    "v_mfma_f32_16x16x32_bf16 v[24:27], a[216:219], v[80:83], 0\n"
    "v_xor_b32 v50, 0x4000, v0\n"
    "v_xor_b32 v49, 0x2000, v0\n"
    "s_waitcnt lgkmcnt(4)\n"
    "v_mfma_f32_16x16x32_bf16 v[24:27], a[220:223], v[84:87], v[24:27]\n"
    "v_xor_b32 v51, 0x6000, v0\n"
    "v_and_b32 v52, 0xffffbfff, v0\n"
    "s_waitcnt lgkmcnt(3)\n"
    "v_mfma_f32_16x16x32_bf16 v[24:27], a[224:227], v[88:91], v[24:27]\n"
    "v_xor_b32 v53, 0x2000, v52\n"
    "s_waitcnt lgkmcnt(2)\n"
    "v_mfma_f32_16x16x32_bf16 v[24:27], a[228:231], v[92:95], v[24:27]\n"
    "s_barrier\n"
    "ds_read_b128 v[112:115], v50\n"
    "v_mfma_f32_16x16x32_bf16 v[12:15], a[0:3], v[80:83], 0\n"
    "ds_read2st64_b32 v[160:161], v7 offset1:1\n"
    "s_waitcnt lgkmcnt(3)\n"
    "v_readfirstlane_b32 s49, v77\n"
    "v_mfma_f32_16x16x32_bf16 v[16:19], a[72:75], v[80:83], 0\n"
    "ds_read2st64_b32 v[162:163], v7 offset0:2 offset1:3\n"
    "v_readfirstlane_b32 s62, v78\n"
    "v_mfma_f32_16x16x32_bf16 v[12:15], a[4:7], v[84:87], v[12:15]\n"
    "ds_read2st64_b32 v[164:165], v7 offset0:4 offset1:5\n"
    "v_add_u32 v76, v76, v72\n"
    "v_mfma_f32_16x16x32_bf16 v[16:19], a[76:79], v[84:87], v[16:19]\n"
    "ds_read2st64_b32 v[166:167], v7 offset0:6 offset1:7\n"
    "s_xor_b32 s58, s58, 0x8000\n"
    "v_mfma_f32_16x16x32_bf16 v[12:15], a[8:11], v[88:91], v[12:15]\n"
    "ds_read_b32 v168, v7 offset:2048\n"
    "s_xor_b32 s59, s59, 0x8000\n"
    "v_mfma_f32_16x16x32_bf16 v[16:19], a[80:83], v[88:91], v[16:19]\n"
    "ds_read_b128 v[116:119], v50 offset:1024\n"
    "v_cmp_eq_u32 vcc, 0, v55\n"
    "s_cmp_eq_u64 vcc, 0\n"
    "v_mfma_f32_16x16x32_bf16 v[12:15], a[12:15], v[92:95], v[12:15]\n"
    "global_load_dword v77, v76, %1 sc1\n"
    "s_cselect_b32 s94, s42, 0\n"
    "s_cselect_b32 s90, s96, 0\n"
    "v_mfma_f32_16x16x32_bf16 v[16:19], a[84:87], v[92:95], v[16:19]\n"
    "ds_read_b128 v[120:123], v50 offset:2048\n"
    "s_mov_b32 m0, s58\n"
    "s_waitcnt lgkmcnt(7)\n"
    "v_mfma_f32_16x16x32_bf16 v[24:27], a[232:235], v[112:115], v[24:27]\n"
    "buffer_load_dwordx4 v2, s[92:95], s49 offen sc1 lds\n"
    "v_add_u32 v73, v75, v3\n"
    "s_waitcnt lgkmcnt(6)\n"
    "v_add_f32 v160, v160, v161\n"
    "v_mfma_f32_16x16x32_bf16 v[12:15], a[32:35], v[112:115], v[12:15]\n"
    "ds_read_b128 v[124:127], v50 offset:3072\n"
    "s_waitcnt lgkmcnt(6)\n"
    "v_add_f32 v162, v162, v163\n"
    "s_waitcnt lgkmcnt(5)\n"
    "v_add_f32 v164, v164, v165\n"
    "v_mfma_f32_16x16x32_bf16 v[16:19], a[104:107], v[112:115], v[16:19]\n"
    "ds_read_b128 v[144:147], v52 offset:4096\n"
    "s_waitcnt lgkmcnt(5)\n"
    "v_add_f32 v166, v166, v167\n"
    "v_add_f32 v160, v160, v162\n"
    "s_waitcnt lgkmcnt(3)\n"
    "v_mfma_f32_16x16x32_bf16 v[24:27], a[236:239], v[116:119], v[24:27]\n"
    "ds_read_b128 v[96:99], v49\n"
    "v_add_f32 v164, v164, v166\n"
    "v_add_f32 v160, v160, v168\n"
    "v_mfma_f32_16x16x32_bf16 v[12:15], a[36:39], v[116:119], v[12:15]\n"
    "buffer_load_dwordx4 v2, s[92:95], s49 offen offset:1024 sc1 lds\n"
    "v_add_f32 v168, v160, v164\n"
    "v_add_f32 v160, v160, v164\n"
    "v_mfma_f32_16x16x32_bf16 v[16:19], a[108:111], v[116:119], v[16:19]\n"
    "ds_read_b128 v[100:103], v49 offset:1024\n"
    "v_permlane32_swap_b32 v160, v168\n"
    "s_waitcnt lgkmcnt(4)\n"
    "v_mfma_f32_16x16x32_bf16 v[24:27], a[240:243], v[120:123], v[24:27]\n"
    "ds_read_b128 v[104:107], v49 offset:2048\n"
    "v_add_f32 v161, v160, v168\n"
    "v_add_f32 v168, v168, v160\n"
    "v_mfma_f32_16x16x32_bf16 v[12:15], a[40:43], v[120:123], v[12:15]\n"
    "ds_read_b128 v[108:111], v49 offset:3072\n"
    "v_permlane16_swap_b32 v161, v168\n"
    "v_mfma_f32_16x16x32_bf16 v[16:19], a[112:115], v[120:123], v[16:19]\n"
    "buffer_load_dwordx4 v2, s[92:95], s49 offen offset:2048 sc1 lds\n"
    "v_mul_f32 v160, 0xbae38e39, v161\n"
    "v_mul_f32 v168, 0x3ae38e39, v168\n"
    "s_waitcnt lgkmcnt(5)\n"
    "v_mfma_f32_16x16x32_bf16 v[24:27], a[244:247], v[124:127], v[24:27]\n"
    "ds_read_b128 v[128:131], v51\n"
    "v_cvt_pk_bf16_f32 v216, v160, v160\n"
    "v_cvt_pk_bf16_f32 v217, v160, v160\n"
    "v_mfma_f32_16x16x32_bf16 v[12:15], a[44:47], v[124:127], v[12:15]\n"
    "ds_read_b128 v[132:135], v51 offset:1024\n"
    "v_fma_f32 v168, -v160, v160, v168\n"
    "v_max_f32 v168, 0, v168\n"
    "v_mfma_f32_16x16x32_bf16 v[16:19], a[116:119], v[124:127], v[16:19]\n"
    "ds_read_b128 v[136:139], v51 offset:2048\n"
    "v_mfma_f32_16x16x32_bf16 v[12:15], v[60:63], v[216:219], v[12:15]\n"
    "v_mfma_f32_16x16x32_bf16 v[16:19], v[62:65], v[216:219], v[16:19]\n"
    "s_waitcnt lgkmcnt(7)\n"
    "v_mfma_f32_16x16x32_bf16 v[24:27], a[248:251], v[144:147], v[24:27]\n"
    "buffer_load_dwordx4 v2, s[92:95], s49 offen offset:3072 sc1 lds\n"
    "v_mfma_f32_16x16x32_bf16 v[24:27], v[66:69], v[216:219], v[24:27]\n"
    "v_cvt_pk_bf16_f32 v168, 0, v168\n"
    "v_mfma_f32_16x16x32_bf16 v[12:15], a[64:67], v[144:147], v[12:15]\n"
    "ds_read_b128 v[140:143], v51 offset:3072\n"
    "v_add_f32 v168, 0x3727c5ac, v168\n"
    "v_cvt_pk_bf16_f32 v168, 0, v168\n"
    "v_mfma_f32_16x16x32_bf16 v[16:19], a[136:139], v[144:147], v[16:19]\n"
    "ds_read_b128 v[148:151], v53 offset:4096\n"
    "v_sqrt_f32 v168, v168\n"
    "s_waitcnt lgkmcnt(8)\n"
    "v_mfma_f32_16x16x32_bf16 v[12:15], a[16:19], v[96:99], v[12:15]\n"
    "ds_read_b128 v[28:31], v73\n"
    "s_bitset1_b32 m0, 12\n"
    "v_cvt_pk_bf16_f32 v168, 0, v168\n"
    "v_mfma_f32_16x16x32_bf16 v[16:19], a[88:91], v[96:99], v[16:19]\n"
    "buffer_load_dwordx4 v3, s[88:91], s49 offen sc1 lds\n"
    "v_rcp_f32 v168, v168\n"
    "s_waitcnt lgkmcnt(8)\n"
    "v_mfma_f32_16x16x32_bf16 v[12:15], a[20:23], v[100:103], v[12:15]\n"
    "ds_read_b64 v[32:33], v73 offset:4096\n"
    "v_mul_f32 v24, v24, v168\n"
    "v_mul_f32 v25, v25, v168\n"
    "v_mfma_f32_16x16x32_bf16 v[16:19], a[92:95], v[100:103], v[16:19]\n"
    "ds_read_b64 v[36:37], v73 offset:8192\n"
    "v_mul_f32 v26, v26, v168\n"
    "v_mul_f32 v27, v27, v168\n"
    "s_waitcnt lgkmcnt(9)\n"
    "v_mfma_f32_16x16x32_bf16 v[12:15], a[24:27], v[104:107], v[12:15]\n"
    "ds_write_b128 v9, v[24:27]\n"
    "s_mov_b32 m0, s59\n"
    "s_mov_b32 s55, s49\n"
    "s_bitset1_b32 s55, 15\n"
    "v_mfma_f32_16x16x32_bf16 v[16:19], a[96:99], v[104:107], v[16:19]\n"
    "s_mov_b64 exec, s[60:61]\n"
    "buffer_load_dwordx4 v3, s[92:95], s55 offen offset:2048 sc1 lds\n"
    "s_mov_b64 exec, -1\n"
    "s_waitcnt lgkmcnt(9)\n"
    "v_mfma_f32_16x16x32_bf16 v[12:15], a[28:31], v[108:111], v[12:15]\n"
    "ds_write_b32 v7, v168 offset:2304\n"
    "v_mfma_f32_16x16x32_bf16 v[16:19], a[100:103], v[108:111], v[16:19]\n"
    "ds_read_b128 v[200:203], v8\n"
    "s_waitcnt lgkmcnt(10)\n"
    "v_mfma_f32_16x16x32_bf16 v[12:15], a[48:51], v[128:131], v[12:15]\n"
    "v_min_f32 v40, v74, v68\n"
    "v_min_f32 v41, v74, v69\n"
    "v_mfma_f32_16x16x32_bf16 v[16:19], a[120:123], v[128:131], v[16:19]\n"
    "v_min_f32 v42, v74, v70\n"
    "v_min_f32 v43, v74, v71\n"
    "s_waitcnt lgkmcnt(9)\n"
    "v_mfma_f32_16x16x32_bf16 v[12:15], a[52:55], v[132:135], v[12:15]\n"
    "s_waitcnt lgkmcnt(0)\n"
    "s_barrier\n"
    "ds_read2st64_b64 v[180:183], v4 offset1:2\n"
    "v_mfma_f32_16x16x32_bf16 v[16:19], a[124:127], v[132:135], v[16:19]\n"
    "ds_read2st64_b64 v[184:187], v5 offset1:2\n"
    "v_mfma_f32_16x16x32_bf16 v[12:15], a[56:59], v[136:139], v[12:15]\n"
    "ds_read2_b32 v[188:189], v6 offset1:4\n"
    "v_mfma_f32_16x16x32_bf16 v[16:19], a[128:131], v[136:139], v[16:19]\n"
    "ds_read2_b32 v[190:191], v6 offset0:8 offset1:12\n"
    "s_waitcnt vmcnt(6)\n"
    "v_mov_b32 v54, 1\n"
    "v_mfma_f32_16x16x32_bf16 v[12:15], a[60:63], v[140:143], v[12:15]\n"
    "s_mov_b64 exec, s[56:57]\n"
    "ds_add_rtn_u32 v54, v11, v54 offset:16\n"
    "s_mov_b64 exec, -1\n"
    "v_mfma_f32_16x16x32_bf16 v[16:19], a[132:135], v[140:143], v[16:19]\n"
    "ds_read_b32 v55, v11 offset:32\n"
    "v_xor_b32 v0, 0x8000, v0\n"
    "v_mfma_f32_16x16x32_bf16 v[12:15], a[68:71], v[148:151], v[12:15]\n"
    "ds_read2_b32 v[192:193], v10 offset1:4\n"
    "v_xor_b32 v7, 0x8000, v7\n"
    "v_mfma_f32_16x16x32_bf16 v[16:19], a[140:143], v[148:151], v[16:19]\n"
    "ds_read2_b32 v[194:195], v10 offset0:8 offset1:12\n"
    "v_xor_b32 v8, 0x8000, v8\n"
    "v_mfma_f32_16x16x32_bf16 v[20:23], v[216:219], v[64:67], 0\n"
    "v_mfma_f32_16x16x32_bf16 v[20:23], v[80:83], a[144:147], v[20:23]\n"
    "v_mfma_f32_16x16x32_bf16 v[20:23], v[84:87], a[148:151], v[20:23]\n"
    "s_waitcnt lgkmcnt(3)\n"
    "v_readfirstlane_b32 s50, v54\n"
    "v_add_f32 v180, v180, v182\n"
    "v_mfma_f32_16x16x32_bf16 v[20:23], v[88:91], a[152:155], v[20:23]\n"
    "s_cmp_eq_u32 s50, 3\n"
    "v_add_f32 v181, v181, v183\n"
    "v_mfma_f32_16x16x32_bf16 v[20:23], v[92:95], a[156:159], v[20:23]\n"
    "s_cselect_b64 exec, 1, 0\n"
    "global_store_dword v79, v54, %1 sc1\n"
    "s_mov_b64 exec, -1\n"
    "v_mfma_f32_16x16x32_bf16 v[20:23], v[96:99], a[160:163], v[20:23]\n"
    "s_cmp_eq_u32 s94, 0\n"
    "s_cbranch_scc1 .Lak10_%=\n"
    ".Lak10_x%=:\n"
    "v_mfma_f32_16x16x32_bf16 v[20:23], v[100:103], a[164:167], v[20:23]\n"
    "s_waitcnt lgkmcnt(2)\n"
    "v_add_u32 v55, v55, v72\n"
    "v_cvt_pk_bf16_f32 v198, v180, v181\n"
    "v_mfma_f32_16x16x32_bf16 v[20:23], v[104:107], a[168:171], v[20:23]\n"
    "global_load_dword v55, v55, %1 sc1\n"
    "v_add_f32 v184, v184, v186\n"
    "v_mfma_f32_16x16x32_bf16 v[20:23], v[108:111], a[172:175], v[20:23]\n"
    "v_add_f32 v185, v185, v187\n"
    "v_cvt_pk_bf16_f32 v214, v184, v185\n"
    "v_mfma_f32_16x16x32_bf16 v[20:23], v[112:115], a[176:179], v[20:23]\n"
    "v_mul_f32 v12, v12, v168\n"
    "v_mul_f32 v13, v13, v168\n"
    "v_mul_f32 v14, v14, v168\n"
    "v_mul_f32 v15, v15, v168\n"
    "v_mfma_f32_16x16x32_bf16 v[20:23], v[116:119], a[180:183], v[20:23]\n"
    "v_cvt_pk_bf16_f32 v196, v12, v13\n"
    "v_cvt_pk_bf16_f32 v197, v14, v15\n"
    "v_mul_f32 v16, v16, v168\n"
    "v_mul_f32 v17, v17, v168\n"
    "v_mfma_f32_16x16x32_bf16 v[20:23], v[120:123], a[184:187], v[20:23]\n"
    "v_mfma_f32_16x16x32_bf16 v[204:207], v[28:31], v[196:199], v[40:43]\n"
    "v_mul_f32 v18, v18, v168\n"
    "v_mul_f32 v19, v19, v168\n"
    "v_cvt_pk_bf16_f32 v212, v16, v17\n"
    "v_mfma_f32_16x16x32_bf16 v[20:23], v[124:127], a[188:191], v[20:23]\n"
    "v_cvt_pk_bf16_f32 v213, v18, v19\n"
    "s_waitcnt lgkmcnt(1)\n"
    "v_add_f32 v188, v188, v192\n"
    "v_add_f32 v189, v189, v193\n"
    "v_mfma_f32_16x16x32_bf16 v[208:211], v[212:215], v[196:199], v[44:47]\n"
    "v_mfma_f32_16x16x32_bf16 v[20:23], v[128:131], a[192:195], v[20:23]\n"
    "ds_write_b128 v73, v[212:215]\n"
    "s_waitcnt lgkmcnt(1)\n"
    "v_add_f32 v190, v190, v194\n"
    "v_add_f32 v191, v191, v195\n"
    "v_maximum3_f32 v40, v204, v205, v206\n"
    "v_mfma_f32_16x16x32_bf16 v[20:23], v[132:135], a[196:199], v[20:23]\n"
    "v_cvt_pk_bf16_f32 v38, v188, v189\n"
    "v_cvt_pk_bf16_f32 v39, v190, v191\n"
    "v_cndmask_b32 v38, v38, v56, s[86:87]\n"
    "v_cndmask_b32 v39, v39, v56, s[86:87]\n"
    "v_mfma_f32_16x16x32_bf16 v[20:23], v[136:139], a[200:203], v[20:23]\n"
    "v_maximum3_f32 v188, v40, v207, v208\n"
    "v_maximum3_f32 v188, v188, v209, v210\n"
    "v_maximum3_f32 v188, v188, v211, v211\n"
    "v_mov_b32 v189, v188\n"
    "v_mfma_f32_16x16x32_bf16 v[20:23], v[140:143], a[204:207], v[20:23]\n"
    "s_nop 0\n"
    "v_permlane16_swap_b32 v189, v188\n"
    "v_maximum3_f32 v188, v188, v189, v189\n"
    "v_mov_b32 v189, v188\n"
    "v_mfma_f32_16x16x32_bf16 v[20:23], v[144:147], a[208:211], v[20:23]\n"
    "s_nop 0\n"
    "v_permlane32_swap_b32 v189, v188\n"
    "v_maximum3_f32 v188, v188, v189, v189\n"
    "v_mul_f32 v188, 0xbeae1a93, v188\n"
    "v_fmamk_f32 v204, v204, 0x3eae1a93, v188\n"
    "v_mfma_f32_16x16x32_bf16 v[20:23], v[148:151], a[212:215], v[20:23]\n"
    "v_fmamk_f32 v205, v205, 0x3eae1a93, v188\n"
    "v_fmamk_f32 v206, v206, 0x3eae1a93, v188\n"
    "v_fmamk_f32 v207, v207, 0x3eae1a93, v188\n"
    "v_fmamk_f32 v208, v208, 0x3eae1a93, v188\n"
    "v_fmamk_f32 v209, v209, 0x3eae1a93, v188\n"
    "v_fmamk_f32 v210, v210, 0x3eae1a93, v188\n"
    "v_fmamk_f32 v211, v211, 0x3eae1a93, v188\n"
    "v_exp_f32 v204, v204\n"
    "v_exp_f32 v205, v205\n"
    "v_exp_f32 v206, v206\n"
    "v_exp_f32 v207, v207\n"
    "v_exp_f32 v208, v208\n"
    "v_exp_f32 v209, v209\n"
    "v_exp_f32 v210, v210\n"
    "v_exp_f32 v211, v211\n"
    "v_cvt_pk_bf16_f32 v204, v204, v205\n"
    "v_cvt_pk_bf16_f32 v205, v206, v207\n"
    "v_cvt_pk_bf16_f32 v206, v208, v209\n"
    "v_cvt_pk_bf16_f32 v207, v210, v211\n"
    "s_nop 1\n"
    "v_mfma_f32_16x16x32_bf16 v[184:187], v[36:39], v[204:207], 0\n"
    "v_mul_f32 v20, v20, v200\n"
    "v_mul_f32 v21, v21, v201\n"
    "v_mul_f32 v22, v22, v202\n"
    "v_mul_f32 v23, v23, v203\n"
    "v_cvt_pk_bf16_f32 v34, v20, v21\n"
    "v_cvt_pk_bf16_f32 v35, v22, v23\n"
    "s_nop 1\n"
    "v_mfma_f32_16x16x32_bf16 v[180:183], v[32:35], v[204:207], 0\n"
    "ds_write2st64_b64 v73, v[34:35], v[38:39] offset0:8 offset1:16\n"
    "v_rcp_f32 v188, v186\n"
    "s_waitcnt vmcnt(2)\n"
    "ds_read_b128 v[80:83], v0\n"
    "ds_read_b128 v[84:87], v0 offset:1024\n"
    "ds_read_b128 v[88:91], v0 offset:2048\n"
    "ds_read_b128 v[92:95], v0 offset:3072\n"
    "v_mul_f32 v184, v184, v188\n"
    "v_mul_f32 v185, v185, v188\n"
    "v_cvt_pk_bf16_f32 v184, v184, v185\n"
    "v_pk_mul_f32 v[180:181], v[180:181], v[188:189] op_sel_hi:[1,0]\n"
    "v_pk_mul_f32 v[182:183], v[182:183], v[188:189] op_sel_hi:[1,0]\n"
    "v_cvt_pk_bf16_f32 v204, v180, v181\n"
    "v_cvt_pk_bf16_f32 v205, v182, v183\n"
    "v_cvt_pk_bf16_f32 v206, v180, v181\n"
    "v_cvt_pk_bf16_f32 v207, v182, v183\n"
    "s_nop 0\n"
    "v_permlane16_swap_b32 v204, v206\n"
    "v_permlane16_swap_b32 v205, v207\n"
    "s_nop 0\n"
    "v_mov_b32_dpp v204, v184 quad_perm:[0,0,0,0] row_mask:0x2 bank_mask:0xf\n"
    "v_mov_b32_dpp v205, v184 quad_perm:[1,1,1,1] row_mask:0x2 bank_mask:0xf\n"
    "v_mov_b32_dpp v206, v184 quad_perm:[2,2,2,2] row_mask:0x2 bank_mask:0xf\n"
    "v_mov_b32_dpp v207, v184 quad_perm:[3,3,3,3] row_mask:0x2 bank_mask:0xf\n"
    "buffer_store_dwordx4 v[204:207], v1, s[40:43], s62 offen sc1\n"
    "s_mov_b32 s56, 1\n"
    "s_cmp_lg_u32 s94, 0\n"
    "s_cbranch_scc1 .Lago_%=\n"
    "s_waitcnt vmcnt(1)\n"
    "v_cmp_eq_u32 vcc, 0, v77\n"
    "s_cmp_eq_u64 vcc, 0\n"
    "s_cbranch_scc1 .Lald_%=\n"
    "ds_read_b32 v79, v11 offset:44\n"
    "global_load_dword v77, v76, %1 sc1\n"
    "s_waitcnt vmcnt(0)\n"
    "v_mov_b32 v54, 1\n"
    "s_mov_b64 exec, 1\n"
    "ds_add_rtn_u32 v54, v11, v54 offset:48\n"
    "s_mov_b64 exec, -1\n"
    "s_waitcnt lgkmcnt(0)\n"
    "v_readfirstlane_b32 s50, v54\n"
    "s_cmp_eq_u32 s50, 3\n"
    "s_cselect_b64 exec, 1, 0\n"
    "global_store_dword v79, v54, %1 sc1\n"
    "s_mov_b64 exec, -1\n"
    "s_mov_b32 s56, 0\n"
    "v_cmp_eq_u32 vcc, 0, v77\n"
    "s_cmp_eq_u64 vcc, 0\n"
    "s_cbranch_scc1 .Lald_%=\n"
    ".Lachk_%=:\n"
    "v_readfirstlane_b32 s50, v76\n"
    "s_cmp_lt_u32 s50, 200\n"
    "s_cbranch_scc1 .Laend_%=\n"
    ".Lawait_%=:\n"
    "global_load_dword v77, v76, %1 sc1\n"
    "s_mov_b32 s52, 0\n"
    ".Lap11_%=:\n"
    "s_waitcnt vmcnt(0)\n"
    "v_cmp_eq_u32 vcc, 0, v77\n"
    "s_cmp_eq_u64 vcc, 0\n"
    "s_cbranch_scc1 .Lap11_x%=\n"
    "s_add_u32 s52, s52, 1\n"
    "s_sleep 2\n"
    "global_load_dword v77, v76, %1 sc1\n"
    "s_cmp_lt_u32 s52, s53\n"
    "s_cbranch_scc1 .Lap11_%=\n"
    "s_mov_b32 s53, 0\n"
    "v_mov_b32 v78, 0\n"
    "v_mov_b32 v79, 1\n"
    "global_store_dword v78, v79, %1 sc1\n"
    "s_waitcnt vmcnt(0)\n"
    ".Lap11_x%=:\n"
    ".Lald_%=:\n"
    "s_mov_b32 s94, s42\n"
    "s_mov_b32 s90, s96\n"
    "s_mov_b32 m0, s58\n"
    "s_nop 0\n"
    "buffer_load_dwordx4 v2, s[92:95], s49 offen sc1 lds\n"
    "buffer_load_dwordx4 v2, s[92:95], s49 offen offset:1024 sc1 lds\n"
    "buffer_load_dwordx4 v2, s[92:95], s49 offen offset:2048 sc1 lds\n"
    "buffer_load_dwordx4 v2, s[92:95], s49 offen offset:3072 sc1 lds\n"
    "s_bitset1_b32 m0, 12\n"
    "s_nop 0\n"
    "buffer_load_dwordx4 v3, s[88:91], s49 offen sc1 lds\n"
    "s_mov_b32 m0, s59\n"
    "s_mov_b32 s55, s49\n"
    "s_bitset1_b32 s55, 15\n"
    "s_mov_b64 exec, s[60:61]\n"
    "buffer_load_dwordx4 v3, s[92:95], s55 offen offset:2048 sc1 lds\n"
    "s_mov_b64 exec, -1\n"
    "s_waitcnt vmcnt(2)\n"
    "ds_read_b128 v[80:83], v0\n"
    "ds_read_b128 v[84:87], v0 offset:1024\n"
    "ds_read_b128 v[88:91], v0 offset:2048\n"
    "ds_read_b128 v[92:95], v0 offset:3072\n"
    ".Lain_%=:\n"
    "s_waitcnt vmcnt(0)\n"
    "v_add_u32 v11, 32, v11\n"
    "ds_read_b128 v[76:79], v11\n"
    "ds_read2_b32 v[74:75], v11 offset0:5 offset1:6\n"
    "s_waitcnt lgkmcnt(0)\n"
    "s_cmp_eq_u32 s56, 0\n"
    "s_cbranch_scc1 .Lac12_%=\n"
    "s_waitcnt vmcnt(0)\n"
    "v_mov_b32 v54, 1\n"
    "s_mov_b64 exec, 1\n"
    "ds_add_rtn_u32 v54, v11, v54 offset:16\n"
    "s_mov_b64 exec, -1\n"
    "s_waitcnt lgkmcnt(0)\n"
    "v_readfirstlane_b32 s50, v54\n"
    "s_cmp_eq_u32 s50, 3\n"
    "s_cselect_b64 exec, 1, 0\n"
    "global_store_dword v79, v54, %1 sc1\n"
    "s_mov_b64 exec, -1\n"
    "s_mov_b32 s56, 0\n"
    ".Lac12_%=:\n"
    "s_branch .Latop_%=\n"
    ".Lak10_%=:\n"
    "s_waitcnt vmcnt(6)\n"
    "v_cmp_eq_u32 vcc, 0, v77\n"
    "s_cmp_eq_u64 vcc, 0\n"
    "s_cselect_b32 s94, s42, 0\n"
    "s_cselect_b32 s90, s96, 0\n"
    "global_load_dword v77, v76, %1 sc1\n"
    "s_mov_b32 m0, s58\n"
    "s_nop 0\n"
    "buffer_load_dwordx4 v2, s[92:95], s49 offen sc1 lds\n"
    "buffer_load_dwordx4 v2, s[92:95], s49 offen offset:1024 sc1 lds\n"
    "buffer_load_dwordx4 v2, s[92:95], s49 offen offset:2048 sc1 lds\n"
    "buffer_load_dwordx4 v2, s[92:95], s49 offen offset:3072 sc1 lds\n"
    "s_bitset1_b32 m0, 12\n"
    "s_nop 0\n"
    "buffer_load_dwordx4 v3, s[88:91], s49 offen sc1 lds\n"
    "s_mov_b32 m0, s59\n"
    "s_mov_b32 s55, s49\n"
    "s_bitset1_b32 s55, 15\n"
    "s_mov_b64 exec, s[60:61]\n"
    "buffer_load_dwordx4 v3, s[92:95], s55 offen offset:2048 sc1 lds\n"
    "s_mov_b64 exec, -1\n"
    "s_branch .Lak10_x%=\n"
    ".Lapol_%=:\n"
    "v_mov_b32 v76, 64\n"
    "global_load_dword v77, v76, %1 sc1\n"
    "s_mov_b32 s52, 0\n"
    ".Lap13_%=:\n"
    "s_waitcnt vmcnt(0)\n"
    "v_cmp_eq_u32 vcc, 0, v77\n"
    "s_cmp_eq_u64 vcc, 0\n"
    "s_cbranch_scc1 .Lap13_x%=\n"
    "s_add_u32 s52, s52, 1\n"
    "s_sleep 2\n"
    "global_load_dword v77, v76, %1 sc1\n"
    "s_cmp_lt_u32 s52, s53\n"
    "s_cbranch_scc1 .Lap13_%=\n"
    "s_mov_b32 s53, 0\n"
    "v_mov_b32 v78, 0\n"
    "v_mov_b32 v79, 1\n"
    "global_store_dword v78, v79, %1 sc1\n"
    "s_waitcnt vmcnt(0)\n"
    ".Lap13_x%=:\n"
    "v_readfirstlane_b32 s50, v77\n"
    "v_mbcnt_lo_u32_b32 v76, -1, 0\n"
    "v_mbcnt_hi_u32_b32 v76, -1, v76\n"
    "s_lshl_b32 s51, %2, 6\n"
    "v_add_u32 v76, s51, v76\n"
    "v_min_u32 v76, 129, v76\n"
    "v_lshlrev_b32 v76, 5, v76\n"
    "v_add_u32 v76, 0x26c00, v76\n"
    "ds_read2_b32 v[78:79], v76 offset0:1 offset1:2\n"
    "s_and_b32 s50, s50, 1\n"
    "s_waitcnt lgkmcnt(0)\n"
    "v_and_b32 v77, s50, v78\n"
    "v_lshlrev_b32 v77, 13, v77\n"
    "v_and_b32 v78, -2, v78\n"
    "v_xor_b32 v78, v77, v78\n"
    "v_and_b32 v77, s50, v79\n"
    "v_lshlrev_b32 v77, 13, v77\n"
    "v_and_b32 v79, -2, v79\n"
    "v_xor_b32 v79, v77, v79\n"
    "ds_write2_b32 v76, v78, v79 offset0:1 offset1:2\n"
    "s_waitcnt lgkmcnt(0)\n"
    "s_barrier\n"
    "ds_read_b32 v77, v11 offset:4\n"
    "v_add_u32 v76, %4, v72\n"
    "s_waitcnt lgkmcnt(0)\n"
    "v_readfirstlane_b32 s49, v77\n"
    "s_branch .Lawait_%=\n"
    ".Laend_%=:\n"
    "s_cmp_eq_u32 s50, 192\n"
    "s_cbranch_scc0 .Lapol_%=\n"
    "s_waitcnt vmcnt(0) lgkmcnt(0)\n"
    "s_endpgm\n"
    ".p2align 6\n"
    ".Lrole4_%=:\n"
    "s_mov_b64 s[40:41], %0\n"
    "s_and_b32 s41, s41, 0xffff\n"
    "s_mov_b32 s42, 0xc000000\n"
    "s_mov_b32 s43, 0x20000\n"
    "s_mov_b64 s[92:93], %0\n"
    "s_and_b32 s93, s93, 0xffff\n"
    "s_mov_b32 s94, 0xc000000\n"
    "s_mov_b32 s95, 0x20000\n"
    "s_mov_b32 s53, 131072\n"
    "s_mov_b64 s[56:57], 0\n"
    "v_mbcnt_lo_u32_b32 v20, -1, 0\n"
    "v_mbcnt_hi_u32_b32 v20, -1, v20\n"
    "s_lshl_b32 s50, %2, 8\n"
    "s_add_u32 s50, s50, 147456\n"
    "v_lshl_add_u32 v20, v20, 2, s50\n"
    "ds_read_b32 v0, v20\n"
    "ds_read_b32 v1, v20 offset:1024\n"
    "ds_read_b32 v2, v20 offset:2048\n"
    "ds_read_b32 v3, v20 offset:3072\n"
    "ds_read_b32 v4, v20 offset:4096\n"
    "ds_read_b32 v5, v20 offset:5120\n"
    "ds_read_b32 v8, v20 offset:6144\n"
    "ds_read_b32 v9, v20 offset:7168\n"
    "ds_read_b32 v10, v20 offset:8192\n"
    "s_waitcnt lgkmcnt(0)\n"
    "s_barrier\n"
    "s_lshl_b32 s54, %2, 10\n"
    "s_lshl_b32 s58, %2, 13\n"
    "s_add_u32 s58, s58, 0\n"
    "s_add_u32 s88, s40, 32768\n"
    "s_mov_b32 s89, s41\n"
    "s_cmp_lt_u32 %2, 2\n"
    "s_cselect_b32 s96, s42, 0\n"
    "s_mov_b32 s91, s43\n"
    "s_mul_i32 s59, %2, 576\n"
    "s_add_u32 s59, s59, 3072\n"
    "s_bfm_b64 s[60:61], 36, 0\n"
    "s_mov_b32 s86, 0x3f803f80\n"
    "s_mov_b64 s[44:45], %8\n"
    "s_and_b32 s45, s45, 0xffff\n"
    "s_mov_b32 s46, 0xa2000\n"
    "s_mov_b32 s47, 0x20000\n"
    "s_add_u32 m0, s54, 0\n"
    "s_add_u32 s55, %6, 0\n"
    "buffer_load_dwordx4 v8, s[44:47], s55 offen lds\n"
    "s_add_u32 m0, s54, 36864\n"
    "s_add_u32 s55, %6, 64\n"
    "buffer_load_dwordx4 v8, s[44:47], s55 offen lds\n"
    "s_add_u32 m0, s54, 73728\n"
    "s_add_u32 s55, %6, 128\n"
    "buffer_load_dwordx4 v8, s[44:47], s55 offen lds\n"
    "s_add_u32 m0, s54, 4096\n"
    "s_add_u32 s55, %6, 73728\n"
    "buffer_load_dwordx4 v8, s[44:47], s55 offen lds\n"
    "s_add_u32 m0, s54, 40960\n"
    "s_add_u32 s55, %6, 73792\n"
    "buffer_load_dwordx4 v8, s[44:47], s55 offen lds\n"
    "s_add_u32 m0, s54, 77824\n"
    "s_add_u32 s55, %6, 73856\n"
    "buffer_load_dwordx4 v8, s[44:47], s55 offen lds\n"
    "s_add_u32 m0, s54, 8192\n"
    "s_add_u32 s55, %6, 147456\n"
    "buffer_load_dwordx4 v8, s[44:47], s55 offen lds\n"
    "s_add_u32 m0, s54, 45056\n"
    "s_add_u32 s55, %6, 147520\n"
    "buffer_load_dwordx4 v8, s[44:47], s55 offen lds\n"
    "s_add_u32 m0, s54, 81920\n"
    "s_add_u32 s55, %6, 147584\n"
    "buffer_load_dwordx4 v8, s[44:47], s55 offen lds\n"
    "s_add_u32 m0, s54, 12288\n"
    "s_add_u32 s55, %6, 221184\n"
    "buffer_load_dwordx4 v8, s[44:47], s55 offen lds\n"
    "s_add_u32 m0, s54, 49152\n"
    "s_add_u32 s55, %6, 221248\n"
    "buffer_load_dwordx4 v8, s[44:47], s55 offen lds\n"
    "s_add_u32 m0, s54, 86016\n"
    "s_add_u32 s55, %6, 221312\n"
    "buffer_load_dwordx4 v8, s[44:47], s55 offen lds\n"
    "s_add_u32 m0, s54, 16384\n"
    "s_add_u32 s55, %6, 294912\n"
    "buffer_load_dwordx4 v8, s[44:47], s55 offen lds\n"
    "s_add_u32 m0, s54, 53248\n"
    "s_add_u32 s55, %6, 294976\n"
    "buffer_load_dwordx4 v8, s[44:47], s55 offen lds\n"
    "s_add_u32 m0, s54, 90112\n"
    "s_add_u32 s55, %6, 295040\n"
    "buffer_load_dwordx4 v8, s[44:47], s55 offen lds\n"
    "s_add_u32 m0, s54, 20480\n"
    "s_add_u32 s55, %6, 368640\n"
    "buffer_load_dwordx4 v8, s[44:47], s55 offen lds\n"
    "s_add_u32 m0, s54, 57344\n"
    "s_add_u32 s55, %6, 368704\n"
    "buffer_load_dwordx4 v8, s[44:47], s55 offen lds\n"
    "s_add_u32 m0, s54, 94208\n"
    "s_add_u32 s55, %6, 368768\n"
    "buffer_load_dwordx4 v8, s[44:47], s55 offen lds\n"
    "s_add_u32 m0, s54, 24576\n"
    "s_add_u32 s55, %6, 442368\n"
    "buffer_load_dwordx4 v8, s[44:47], s55 offen lds\n"
    "s_add_u32 m0, s54, 61440\n"
    "s_add_u32 s55, %6, 442432\n"
    "buffer_load_dwordx4 v8, s[44:47], s55 offen lds\n"
    "s_add_u32 m0, s54, 98304\n"
    "s_add_u32 s55, %6, 442496\n"
    "buffer_load_dwordx4 v8, s[44:47], s55 offen lds\n"
    "s_add_u32 m0, s54, 28672\n"
    "s_add_u32 s55, %6, 516096\n"
    "buffer_load_dwordx4 v8, s[44:47], s55 offen lds\n"
    "s_add_u32 m0, s54, 65536\n"
    "s_add_u32 s55, %6, 516160\n"
    "buffer_load_dwordx4 v8, s[44:47], s55 offen lds\n"
    "s_add_u32 m0, s54, 102400\n"
    "s_add_u32 s55, %6, 516224\n"
    "buffer_load_dwordx4 v8, s[44:47], s55 offen lds\n"
    "s_add_u32 m0, s54, 32768\n"
    "s_add_u32 s55, %6, 589824\n"
    "buffer_load_dwordx4 v8, s[44:47], s55 offen lds\n"
    "s_add_u32 m0, s54, 69632\n"
    "s_add_u32 s55, %6, 589888\n"
    "buffer_load_dwordx4 v8, s[44:47], s55 offen lds\n"
    "s_add_u32 m0, s54, 106496\n"
    "s_add_u32 s55, %6, 589952\n"
    "buffer_load_dwordx4 v8, s[44:47], s55 offen lds\n"
    "v_mov_b32 v7, 0x20400\n"
    "global_load_dwordx2 v[20:21], v7, %1 sc1\n"
    "v_mbcnt_lo_u32_b32 v6, -1, 0\n"
    "v_min_u32 v6, 7, v6\n"
    "v_lshlrev_b32 v6, 7, v6\n"
    "v_add_u32 v6, 0x20000, v6\n"
    "global_load_dwordx2 v[22:23], v6, %1 sc1\n"
    "s_waitcnt vmcnt(2) lgkmcnt(0)\n"
    "s_barrier\n"
    "ds_read_b64_tr_b16 a[0:1], v9\n"
    "ds_read_b64_tr_b16 a[2:3], v9 offset:256\n"
    "ds_read_b64_tr_b16 a[4:5], v9 offset:8\n"
    "ds_read_b64_tr_b16 a[6:7], v9 offset:264\n"
    "ds_read_b64_tr_b16 a[8:9], v9 offset:2304\n"
    "ds_read_b64_tr_b16 a[10:11], v9 offset:2560\n"
    "ds_read_b64_tr_b16 a[12:13], v9 offset:2312\n"
    "ds_read_b64_tr_b16 a[14:15], v9 offset:2568\n"
    "ds_read_b64_tr_b16 a[16:17], v9 offset:4608\n"
    "ds_read_b64_tr_b16 a[18:19], v9 offset:4864\n"
    "ds_read_b64_tr_b16 a[20:21], v9 offset:4616\n"
    "ds_read_b64_tr_b16 a[22:23], v9 offset:4872\n"
    "ds_read_b64_tr_b16 a[24:25], v9 offset:6912\n"
    "ds_read_b64_tr_b16 a[26:27], v9 offset:7168\n"
    "ds_read_b64_tr_b16 a[28:29], v9 offset:6920\n"
    "ds_read_b64_tr_b16 a[30:31], v9 offset:7176\n"
    "ds_read_b64_tr_b16 a[32:33], v9 offset:9216\n"
    "ds_read_b64_tr_b16 a[34:35], v9 offset:9472\n"
    "ds_read_b64_tr_b16 a[36:37], v9 offset:9224\n"
    "ds_read_b64_tr_b16 a[38:39], v9 offset:9480\n"
    "ds_read_b64_tr_b16 a[40:41], v9 offset:11520\n"
    "ds_read_b64_tr_b16 a[42:43], v9 offset:11776\n"
    "ds_read_b64_tr_b16 a[44:45], v9 offset:11528\n"
    "ds_read_b64_tr_b16 a[46:47], v9 offset:11784\n"
    "ds_read_b64_tr_b16 a[48:49], v9 offset:13824\n"
    "ds_read_b64_tr_b16 a[50:51], v9 offset:14080\n"
    "ds_read_b64_tr_b16 a[52:53], v9 offset:13832\n"
    "ds_read_b64_tr_b16 a[54:55], v9 offset:14088\n"
    "ds_read_b64_tr_b16 a[56:57], v9 offset:16128\n"
    "ds_read_b64_tr_b16 a[58:59], v9 offset:16384\n"
    "ds_read_b64_tr_b16 a[60:61], v9 offset:16136\n"
    "ds_read_b64_tr_b16 a[62:63], v9 offset:16392\n"
    "ds_read_b64_tr_b16 a[64:65], v9 offset:18432\n"
    "ds_read_b64_tr_b16 a[66:67], v9 offset:18688\n"
    "ds_read_b64_tr_b16 a[68:69], v9 offset:18440\n"
    "ds_read_b64_tr_b16 a[70:71], v9 offset:18696\n"
    "ds_read_b64_tr_b16 a[72:73], v9 offset:20736\n"
    "ds_read_b64_tr_b16 a[74:75], v9 offset:20992\n"
    "ds_read_b64_tr_b16 a[76:77], v9 offset:20744\n"
    "ds_read_b64_tr_b16 a[78:79], v9 offset:21000\n"
    "ds_read_b64_tr_b16 a[80:81], v9 offset:23040\n"
    "ds_read_b64_tr_b16 a[82:83], v9 offset:23296\n"
    "ds_read_b64_tr_b16 a[84:85], v9 offset:23048\n"
    "ds_read_b64_tr_b16 a[86:87], v9 offset:23304\n"
    "ds_read_b64_tr_b16 a[88:89], v9 offset:25344\n"
    "ds_read_b64_tr_b16 a[90:91], v9 offset:25600\n"
    "ds_read_b64_tr_b16 a[92:93], v9 offset:25352\n"
    "ds_read_b64_tr_b16 a[94:95], v9 offset:25608\n"
    "ds_read_b64_tr_b16 a[96:97], v9 offset:27648\n"
    "ds_read_b64_tr_b16 a[98:99], v9 offset:27904\n"
    "ds_read_b64_tr_b16 a[100:101], v9 offset:27656\n"
    "ds_read_b64_tr_b16 a[102:103], v9 offset:27912\n"
    "ds_read_b64_tr_b16 a[104:105], v9 offset:29952\n"
    "ds_read_b64_tr_b16 a[106:107], v9 offset:30208\n"
    "ds_read_b64_tr_b16 a[108:109], v9 offset:29960\n"
    "ds_read_b64_tr_b16 a[110:111], v9 offset:30216\n"
    "ds_read_b64_tr_b16 a[112:113], v9 offset:32256\n"
    "ds_read_b64_tr_b16 a[114:115], v9 offset:32512\n"
    "ds_read_b64_tr_b16 a[116:117], v9 offset:32264\n"
    "ds_read_b64_tr_b16 a[118:119], v9 offset:32520\n"
    "ds_read_b64_tr_b16 a[120:121], v9 offset:34560\n"
    "ds_read_b64_tr_b16 a[122:123], v9 offset:34816\n"
    "ds_read_b64_tr_b16 a[124:125], v9 offset:34568\n"
    "ds_read_b64_tr_b16 a[126:127], v9 offset:34824\n"
    "ds_read_b64_tr_b16 a[128:129], v10\n"
    "ds_read_b64_tr_b16 a[130:131], v10 offset:2304\n"
    "ds_read_b64_tr_b16 a[132:133], v10 offset:8\n"
    "ds_read_b64_tr_b16 a[134:135], v10 offset:2312\n"
    "ds_read_b64_tr_b16 a[136:137], v10 offset:18432\n"
    "ds_read_b64_tr_b16 a[138:139], v10 offset:20736\n"
    "ds_read_b64_tr_b16 a[140:141], v10 offset:18440\n"
    "ds_read_b64_tr_b16 a[142:143], v10 offset:20744\n"
    "s_waitcnt lgkmcnt(0)\n"
    "s_barrier\n"
    "s_waitcnt vmcnt(0)\n"
    "v_add_u32 v20, 1, v20\n"
    "v_add_u32 v21, 1, v21\n"
    "s_mov_b32 s52, 1048576\n"
    ".Lbgo_a%=:\n"
    "v_cmp_ne_u64 vcc, v[20:21], v[22:23]\n"
    "s_cbranch_vccz .Lbgo_x%=\n"
    "global_load_dwordx2 v[22:23], v6, %1 sc1\n"
    "s_sub_u32 s52, s52, 1\n"
    "s_waitcnt vmcnt(0)\n"
    "s_cbranch_scc0 .Lbgo_a%=\n"
    ".Lbgo_x%=:\n"
    "v_mbcnt_lo_u32_b32 v20, -1, 0\n"
    "v_mbcnt_hi_u32_b32 v20, -1, v20\n"
    "v_min_u32 v6, 7, v20\n"
    "v_lshlrev_b32 v6, 7, v6\n"
    "v_mov_b32 v7, 0x26c00\n"
    "ds_read_b128 v[20:23], v7\n"
    "s_waitcnt lgkmcnt(0)\n"
    "v_readfirstlane_b32 s49, v21\n"
    "v_add_u32 v20, v20, v6\n"
    "s_branch .Lbchk_%=\n"
    ".Lbgo_%=:\n"
    "v_xor_b32 v0, 0x8000, v0\n"
    "v_xor_b32 v4, 0x8000, v4\n"
    "v_add_u32 v7, 32, v7\n"
    "ds_read_b128 v[20:23], v7\n"
    "s_xor_b32 s58, s58, 0x8000\n"
    "s_waitcnt lgkmcnt(0)\n"
    "v_readfirstlane_b32 s49, v21\n"
    "v_readfirstlane_b32 s62, v22\n"
    "v_add_u32 v20, v20, v6\n"
    "v_add_u32 v104, 8192, v4\n"
    ".Lbtop_%=:\n"
    "s_barrier\n"
    "ds_read_b128 v[24:27], v0\n"
    "ds_read_b128 v[28:31], v0 offset:1024\n"
    "ds_read_b128 v[32:35], v0 offset:2048\n"
    "ds_read_b128 v[36:39], v0 offset:3072\n"
    "ds_read_b128 v[40:43], v0 offset:8192\n"
    "ds_read_b128 v[44:47], v0 offset:9216\n"
    "global_load_dword v21, v20, %1 sc1\n"
    "v_lshlrev_b32 v8, 16, v100\n"
    "v_and_b32 v9, 0xffff0000, v100\n"
    "v_lshlrev_b32 v10, 16, v101\n"
    "v_and_b32 v11, 0xffff0000, v101\n"
    "v_lshlrev_b32 v12, 16, v102\n"
    "v_and_b32 v13, 0xffff0000, v102\n"
    "v_lshlrev_b32 v14, 16, v103\n"
    "v_and_b32 v15, 0xffff0000, v103\n"
    "s_waitcnt lgkmcnt(5)\n"
    "v_mfma_f32_16x16x32_bf16 v[8:11], a[0:3], v[24:27], v[8:11]\n"
    "ds_read_b128 v[48:51], v0 offset:10240\n"
    "v_mfma_f32_16x16x32_bf16 v[12:15], a[4:7], v[24:27], v[12:15]\n"
    "ds_read_b128 v[52:55], v0 offset:11264\n"
    "s_waitcnt lgkmcnt(6)\n"
    "v_mfma_f32_16x16x32_bf16 v[8:11], a[8:11], v[28:31], v[8:11]\n"
    "ds_read_b128 v[56:59], v0 offset:16384\n"
    "v_mfma_f32_16x16x32_bf16 v[12:15], a[12:15], v[28:31], v[12:15]\n"
    "ds_read_b128 v[60:63], v0 offset:17408\n"
    "s_waitcnt lgkmcnt(7)\n"
    "v_mfma_f32_16x16x32_bf16 v[8:11], a[16:19], v[32:35], v[8:11]\n"
    "ds_read_b128 v[64:67], v0 offset:18432\n"
    "v_mfma_f32_16x16x32_bf16 v[12:15], a[20:23], v[32:35], v[12:15]\n"
    "ds_read_b128 v[68:71], v0 offset:19456\n"
    "s_waitcnt lgkmcnt(8)\n"
    "v_mfma_f32_16x16x32_bf16 v[8:11], a[24:27], v[36:39], v[8:11]\n"
    "ds_read_b128 v[72:75], v0 offset:24576\n"
    "v_mfma_f32_16x16x32_bf16 v[12:15], a[28:31], v[36:39], v[12:15]\n"
    "ds_read_b128 v[76:79], v0 offset:25600\n"
    "s_waitcnt lgkmcnt(9)\n"
    "v_mfma_f32_16x16x32_bf16 v[8:11], a[32:35], v[40:43], v[8:11]\n"
    "ds_read_b128 v[80:83], v0 offset:26624\n"
    "v_mfma_f32_16x16x32_bf16 v[12:15], a[36:39], v[40:43], v[12:15]\n"
    "ds_read_b128 v[84:87], v0 offset:27648\n"
    "s_waitcnt lgkmcnt(10)\n"
    "v_mfma_f32_16x16x32_bf16 v[8:11], a[40:43], v[44:47], v[8:11]\n"
    "ds_read2_b32 v[88:89], v4 offset1:16\n"
    "v_mfma_f32_16x16x32_bf16 v[12:15], a[44:47], v[44:47], v[12:15]\n"
    "ds_read2_b32 v[90:91], v4 offset0:32 offset1:48\n"
    "s_waitcnt lgkmcnt(11)\n"
    "v_mfma_f32_16x16x32_bf16 v[8:11], a[48:51], v[48:51], v[8:11]\n"
    "ds_read2_b32 v[92:93], v104 offset1:16\n"
    "v_mfma_f32_16x16x32_bf16 v[12:15], a[52:55], v[48:51], v[12:15]\n"
    "ds_read2_b32 v[94:95], v104 offset0:32 offset1:48\n"
    "s_waitcnt lgkmcnt(12)\n"
    "v_mfma_f32_16x16x32_bf16 v[8:11], a[56:59], v[52:55], v[8:11]\n"
    "v_mfma_f32_16x16x32_bf16 v[12:15], a[60:63], v[52:55], v[12:15]\n"
    "s_waitcnt lgkmcnt(11)\n"
    "v_mfma_f32_16x16x32_bf16 v[8:11], a[64:67], v[56:59], v[8:11]\n"
    "v_mfma_f32_16x16x32_bf16 v[12:15], a[68:71], v[56:59], v[12:15]\n"
    "s_waitcnt lgkmcnt(10)\n"
    "v_mfma_f32_16x16x32_bf16 v[8:11], a[72:75], v[60:63], v[8:11]\n"
    "v_mfma_f32_16x16x32_bf16 v[12:15], a[76:79], v[60:63], v[12:15]\n"
    "s_waitcnt lgkmcnt(9)\n"
    "v_mfma_f32_16x16x32_bf16 v[8:11], a[80:83], v[64:67], v[8:11]\n"
    "v_mfma_f32_16x16x32_bf16 v[12:15], a[84:87], v[64:67], v[12:15]\n"
    "s_waitcnt lgkmcnt(8)\n"
    "v_mfma_f32_16x16x32_bf16 v[8:11], a[88:91], v[68:71], v[8:11]\n"
    "v_mfma_f32_16x16x32_bf16 v[12:15], a[92:95], v[68:71], v[12:15]\n"
    "s_waitcnt lgkmcnt(7)\n"
    "v_mfma_f32_16x16x32_bf16 v[8:11], a[96:99], v[72:75], v[8:11]\n"
    "v_mfma_f32_16x16x32_bf16 v[12:15], a[100:103], v[72:75], v[12:15]\n"
    "s_waitcnt lgkmcnt(6)\n"
    "v_mfma_f32_16x16x32_bf16 v[8:11], a[104:107], v[76:79], v[8:11]\n"
    "v_mfma_f32_16x16x32_bf16 v[12:15], a[108:111], v[76:79], v[12:15]\n"
    "s_waitcnt lgkmcnt(5)\n"
    "v_mfma_f32_16x16x32_bf16 v[8:11], a[112:115], v[80:83], v[8:11]\n"
    "v_mfma_f32_16x16x32_bf16 v[12:15], a[116:119], v[80:83], v[12:15]\n"
    "s_waitcnt lgkmcnt(4)\n"
    "v_mfma_f32_16x16x32_bf16 v[8:11], a[120:123], v[84:87], v[8:11]\n"
    "v_mfma_f32_16x16x32_bf16 v[12:15], a[124:127], v[84:87], v[12:15]\n"
    "s_waitcnt lgkmcnt(2)\n"
    "v_mfma_f32_16x16x32_bf16 v[8:11], a[128:131], v[88:91], v[8:11]\n"
    "v_mfma_f32_16x16x32_bf16 v[12:15], a[132:135], v[88:91], v[12:15]\n"
    "s_waitcnt lgkmcnt(0)\n"
    "v_mfma_f32_16x16x32_bf16 v[8:11], a[136:139], v[92:95], v[8:11]\n"
    "v_mfma_f32_16x16x32_bf16 v[12:15], a[140:143], v[92:95], v[12:15]\n"
    "s_nop 4\n"
    "v_cvt_pk_bf16_f32 v16, v8, v9\n"
    "v_cvt_pk_bf16_f32 v17, v10, v11\n"
    "s_nop 1\n"
    "v_cvt_pk_bf16_f32 v18, v12, v13\n"
    "v_cvt_pk_bf16_f32 v19, v14, v15\n"
    "buffer_store_dwordx4 v[16:19], v3, s[40:43], s62 offen sc1\n"
    "v_dot2_f32_bf16 v8, v16, s86, 0\n"
    "v_dot2_f32_bf16 v9, v16, v16, 0\n"
    "v_dot2_f32_bf16 v8, v17, s86, v8\n"
    "v_dot2_f32_bf16 v9, v17, v17, v9\n"
    "v_dot2_f32_bf16 v8, v18, s86, v8\n"
    "v_dot2_f32_bf16 v9, v18, v18, v9\n"
    "v_dot2_f32_bf16 v8, v19, s86, v8\n"
    "v_dot2_f32_bf16 v9, v19, v19, v9\n"
    "s_nop 2\n"
    "v_permlane32_swap_b32 v8, v9\n"
    "v_add_f32 v10, v8, v9\n"
    "v_add_f32 v8, v8, v9\n"
    "s_nop 1\n"
    "v_permlane16_swap_b32 v10, v8\n"
    "v_add_f32 v8, v8, v10\n"
    "buffer_store_dword v8, v5, s[40:43], s62 offen sc1\n"
    "s_mov_b32 s56, 1\n"
    "s_waitcnt vmcnt(2)\n"
    "v_cmp_eq_u32 vcc, 0, v21\n"
    "s_cmp_eq_u64 vcc, 0\n"
    "ds_read_b32 v23, v7 offset:44\n"
    "s_cbranch_scc0 .Lbno_%=\n"
    "s_mov_b32 m0, s58\n"
    "s_nop 0\n"
    "buffer_load_dwordx4 v2, s[92:95], s49 offen sc1 lds\n"
    "buffer_load_dwordx4 v2, s[92:95], s49 offen offset:1024 sc1 lds\n"
    "buffer_load_dwordx4 v2, s[92:95], s49 offen offset:2048 sc1 lds\n"
    "buffer_load_dwordx4 v2, s[92:95], s49 offen offset:3072 sc1 lds\n"
    "s_bitset1_b32 m0, 12\n"
    "s_nop 0\n"
    "buffer_load_dwordx4 v1, s[88:91], s49 offen sc1 lds\n"
    "s_sub_u32 s55, s49, 0x1000000\n"
    "buffer_load_dwordx4 v[100:103], v3, s[40:43], s55 offen sc1\n"
    "s_waitcnt vmcnt(6)\n"
    "v_mov_b32 v22, 1\n"
    "s_mov_b64 exec, 1\n"
    "ds_add_rtn_u32 v22, v7, v22 offset:48\n"
    "s_mov_b64 exec, -1\n"
    "s_waitcnt lgkmcnt(0)\n"
    "v_readfirstlane_b32 s50, v22\n"
    "s_cmp_eq_u32 s50, 3\n"
    "s_cbranch_scc0 .Lbin0_%=\n"
    "s_cselect_b64 exec, 1, 0\n"
    "global_store_dword v23, v22, %1 sc1\n"
    "s_mov_b64 exec, -1\n"
    "s_waitcnt vmcnt(1)\n"
    "s_branch .Lbin_%=\n"
    ".Lbno_%=:\n"
    "global_load_dword v21, v20, %1 sc1\n"
    "s_waitcnt vmcnt(0)\n"
    "v_mov_b32 v22, 1\n"
    "s_mov_b64 exec, 1\n"
    "ds_add_rtn_u32 v22, v7, v22 offset:48\n"
    "s_mov_b64 exec, -1\n"
    "s_waitcnt lgkmcnt(0)\n"
    "v_readfirstlane_b32 s50, v22\n"
    "s_cmp_eq_u32 s50, 3\n"
    "s_cselect_b64 exec, 1, 0\n"
    "global_store_dword v23, v22, %1 sc1\n"
    "s_mov_b64 exec, -1\n"
    "s_mov_b32 s56, 0\n"
    "v_cmp_eq_u32 vcc, 0, v21\n"
    "s_cmp_eq_u64 vcc, 0\n"
    "s_cbranch_scc1 .Lbld_%=\n"
    ".Lbchk_%=:\n"
    "v_readfirstlane_b32 s50, v20\n"
    "s_cmp_lt_u32 s50, 200\n"
    "s_cbranch_scc1 .Lbend_%=\n"
    ".Lbwait_%=:\n"
    "global_load_dword v21, v20, %1 sc1\n"
    "s_mov_b32 s52, 0\n"
    ".Lbp1_%=:\n"
    "s_waitcnt vmcnt(0)\n"
    "v_cmp_eq_u32 vcc, 0, v21\n"
    "s_cmp_eq_u64 vcc, 0\n"
    "s_cbranch_scc1 .Lbp1_x%=\n"
    "s_add_u32 s52, s52, 1\n"
    "s_sleep 2\n"
    "global_load_dword v21, v20, %1 sc1\n"
    "s_cmp_lt_u32 s52, s53\n"
    "s_cbranch_scc1 .Lbp1_%=\n"
    "s_mov_b32 s53, 0\n"
    "v_mov_b32 v22, 0\n"
    "v_mov_b32 v23, 1\n"
    "global_store_dword v22, v23, %1 sc1\n"
    "s_waitcnt vmcnt(0)\n"
    ".Lbp1_x%=:\n"
    ".Lbld_%=:\n"
    "s_mov_b32 s94, s42\n"
    "s_mov_b32 s90, s96\n"
    "s_mov_b32 m0, s58\n"
    "s_nop 0\n"
    "buffer_load_dwordx4 v2, s[92:95], s49 offen sc1 lds\n"
    "buffer_load_dwordx4 v2, s[92:95], s49 offen offset:1024 sc1 lds\n"
    "buffer_load_dwordx4 v2, s[92:95], s49 offen offset:2048 sc1 lds\n"
    "buffer_load_dwordx4 v2, s[92:95], s49 offen offset:3072 sc1 lds\n"
    "s_bitset1_b32 m0, 12\n"
    "s_nop 0\n"
    "buffer_load_dwordx4 v1, s[88:91], s49 offen sc1 lds\n"
    "s_sub_u32 s55, s49, 0x1000000\n"
    "buffer_load_dwordx4 v[100:103], v3, s[40:43], s55 offen sc1\n"
    ".Lbin0_%=:\n"
    "s_waitcnt vmcnt(0)\n"
    ".Lbin_%=:\n"
    "v_xor_b32 v0, 0x8000, v0\n"
    "v_xor_b32 v4, 0x8000, v4\n"
    "v_add_u32 v7, 32, v7\n"
    "ds_read_b128 v[20:23], v7\n"
    "s_xor_b32 s58, s58, 0x8000\n"
    "s_waitcnt lgkmcnt(0)\n"
    "v_readfirstlane_b32 s49, v21\n"
    "v_readfirstlane_b32 s62, v22\n"
    "v_add_u32 v20, v20, v6\n"
    "v_add_u32 v104, 8192, v4\n"
    "s_branch .Lbtop_%=\n"
    ".Lbpol_%=:\n"
    "v_mov_b32 v20, 64\n"
    "global_load_dword v21, v20, %1 sc1\n"
    "s_mov_b32 s52, 0\n"
    ".Lbp2_%=:\n"
    "s_waitcnt vmcnt(0)\n"
    "v_cmp_eq_u32 vcc, 0, v21\n"
    "s_cmp_eq_u64 vcc, 0\n"
    "s_cbranch_scc1 .Lbp2_x%=\n"
    "s_add_u32 s52, s52, 1\n"
    "s_sleep 2\n"
    "global_load_dword v21, v20, %1 sc1\n"
    "s_cmp_lt_u32 s52, s53\n"
    "s_cbranch_scc1 .Lbp2_%=\n"
    "s_mov_b32 s53, 0\n"
    "v_mov_b32 v22, 0\n"
    "v_mov_b32 v23, 1\n"
    "global_store_dword v22, v23, %1 sc1\n"
    "s_waitcnt vmcnt(0)\n"
    ".Lbp2_x%=:\n"
    "v_readfirstlane_b32 s50, v21\n"
    "v_mbcnt_lo_u32_b32 v20, -1, 0\n"
    "v_mbcnt_hi_u32_b32 v20, -1, v20\n"
    "s_lshl_b32 s51, %2, 6\n"
    "v_add_u32 v20, s51, v20\n"
    "v_min_u32 v20, 129, v20\n"
    "v_lshlrev_b32 v20, 5, v20\n"
    "v_add_u32 v20, 0x26c00, v20\n"
    "ds_read2_b32 v[22:23], v20 offset0:1 offset1:2\n"
    "s_and_b32 s50, s50, 1\n"
    "s_waitcnt lgkmcnt(0)\n"
    "v_and_b32 v21, s50, v22\n"
    "v_lshlrev_b32 v21, 13, v21\n"
    "v_and_b32 v22, -2, v22\n"
    "v_xor_b32 v22, v21, v22\n"
    "v_and_b32 v21, s50, v23\n"
    "v_lshlrev_b32 v21, 13, v21\n"
    "v_and_b32 v23, -2, v23\n"
    "v_xor_b32 v23, v21, v23\n"
    "ds_write2_b32 v20, v22, v23 offset0:1 offset1:2\n"
    "s_waitcnt lgkmcnt(0)\n"
    "s_barrier\n"
    "ds_read_b32 v21, v7 offset:4\n"
    "v_add_u32 v20, %4, v6\n"
    "s_waitcnt lgkmcnt(0)\n"
    "v_readfirstlane_b32 s49, v21\n"
    "s_branch .Lbwait_%=\n"
    ".Lbend_%=:\n"
    "s_cmp_eq_u32 s50, 192\n"
    "s_cbranch_scc0 .Lbpol_%=\n"
    "s_waitcnt vmcnt(0) lgkmcnt(0)\n"
    ".Lrole5_%=:\n"
    ".Lfin:\n"
        :: "s"(wd), "s"(ws), "s"(wave), "s"(rid), "s"(P.h), "s"(P.p), "s"(P.woff), "s"(layer), "s"(w0), "s"(Wk), "s"(Wv), "s"(Wo),
           "s"(mem0), "s"(P.role) : CLOBBERS);
}

extern "C" void kernel_launch(void* const* d_in, const int* in_sizes, int n_in, void* d_out, int out_size, void* d_ws,
                              size_t ws_size, hipStream_t stream) {
    mega_kernel<<<256, 256, 0, stream>>>((const u16*)d_in[0], (const u16*)d_in[1], (const u16*)d_in[2], (const u16*)d_in[4],
                                         (const u16*)d_in[5], (const u16*)d_in[6], (const u16*)d_in[7], (const u16*)d_in[8],
                                         (u16*)d_out, (char*)d_ws);
}
```

```cpp
#include <hip/hip_runtime.h>
#include <hip/hip_bf16.h>
#include <stdint.h>

typedef unsigned short u16;
typedef unsigned u32;
typedef unsigned long long u64;
using bf16x8 = __attribute__((ext_vector_type(8))) short;
using bf16x4 = __attribute__((ext_vector_type(4))) short;
using f32x4 = __attribute__((ext_vector_type(4))) float;
using f32x2 = __attribute__((ext_vector_type(2))) float;
using v4u = __attribute__((ext_vector_type(4))) unsigned;
using v2u = __attribute__((ext_vector_type(2))) unsigned;
typedef __bf16 bfv2 __attribute__((ext_vector_type(2)));
#define GAS __attribute__((address_space(1)))
typedef GAS unsigned gu32;
typedef GAS u64 gu64;
#define RLX_AGENT __ATOMIC_RELAXED, __HIP_MEMORY_SCOPE_AGENT
typedef __amdgpu_buffer_rsrc_t rsrc_t;

constexpr int S_ = 2048, D_ = 576, NSEG = 64, SEG = 32, NB = 4096, NQA = 1728;

constexpr u32 OFF_ABORT = 0;
constexpr u32 OFF_POL = 64;
constexpr u32 OFF_RDY = 128;
constexpr u32 OFF_NEVER = 192;
constexpr u32 OFF_FA = 4096;
constexpr u32 OFF_FO = OFF_FA + 8192;
constexpr u32 OFF_FB = OFF_FO + 8192;
constexpr u32 OFF_FP = OFF_FB + 8192;
constexpr u32 OFF_COB = OFF_FP + 24576;
constexpr u32 OFF_FKV = OFF_COB + 32768;
constexpr u32 OFF_GO = 131072;
constexpr u32 OFF_EP = OFF_GO + 1024;
constexpr u32 SYNC_BYTES = OFF_EP + 128;
constexpr u32 MB2 = 2u << 20;
constexpr u32 OFF_XA = 0;
constexpr u32 OFF_XB = MB2;
constexpr u32 XB = S_ * D_ * 2;
constexpr u32 OFF_TA = 4 * MB2;
constexpr u32 OFF_TB = 8 * MB2;
constexpr u32 OFF_OA = 12 * MB2;
constexpr u32 OFF_PB = 16 * MB2;
constexpr u32 HB_ = 2 * 64 * 32 * 8192;
constexpr u32 OFF_QX = 32 * MB2;
constexpr u32 OFF_KX = OFF_QX + HB_;
constexpr u32 OFF_VX = OFF_KX + HB_;
constexpr u32 OFF_OX = OFF_VX + HB_;
constexpr u32 WS_BYTES = OFF_OX + HB_;
constexpr u32 L_TAB = 147456, L_IT = L_TAB + 11264, L_EX = 138240, L_W = 32064;
constexpr u32 O_SUM = 32768 + 2048;

#define STAMP(i, k) do {} while (0)
#define PH(k) do {} while (0)
__device__ __forceinline__ float bf2f(u16 v) { return __uint_as_float(((u32)v) << 16); }
__device__ __forceinline__ float bflo(u32 v) { return __uint_as_float(v << 16); }
__device__ __forceinline__ float bfhi(u32 v) { return __uint_as_float(v & 0xffff0000u); }
__device__ __forceinline__ u32 pack2(float a, float b) { bfv2 v = {(__bf16)a, (__bf16)b}; return __builtin_bit_cast(u32, v); }
__device__ __forceinline__ float rbf(float f) { return (float)(__bf16)f; }
__device__ __forceinline__ v4u ld16(rsrc_t r, u32 off) { return __builtin_amdgcn_raw_buffer_load_b128(r, off, 0, 16); }
__device__ __forceinline__ v2u ld8(rsrc_t r, u32 off) { return __builtin_amdgcn_raw_buffer_load_b64(r, off, 0, 16); }
__device__ __forceinline__ void st16(rsrc_t r, u32 off, v4u v) { __builtin_amdgcn_raw_buffer_store_b128(v, r, off, 0, 16); }
__device__ __forceinline__ void st8(rsrc_t r, u32 off, v2u v) { __builtin_amdgcn_raw_buffer_store_b64(v, r, off, 0, 16); }
__device__ __forceinline__ void st4(rsrc_t r, u32 off, u32 v) { __builtin_amdgcn_raw_buffer_store_b32(v, r, off, 0, 16); }
#define DRAIN() asm volatile("s_waitcnt vmcnt(0)" ::: "memory")
#define TOUCH(p) asm volatile("global_load_dword v255, %0, off" :: "v"(p) : "v255")
#define CBAR() asm volatile("" ::: "memory")
__device__ __forceinline__ u32 ldw(char* ws, u32 off) { return __hip_atomic_load((gu32*)(ws + off), RLX_AGENT); }
__device__ __forceinline__ void setw(char* ws, u32 off) { __hip_atomic_store((gu32*)(ws + off), 1u, RLX_AGENT); }
__device__ __forceinline__ u32 fl(u32 i) { return (i >> 5) * 1024 + (i & 31) * 4; }
constexpr u32 SPIN_CAP = 1u << 20;
#define SLP_W 2
#define SLP_C 8
__device__ __forceinline__ void until(u64 t) { while (__builtin_amdgcn_s_memrealtime() < t) __builtin_amdgcn_s_sleep(8); }
#define TDEL 0
__device__ __forceinline__ u64 succ(char* ws) {
    const u64 e = __hip_atomic_load((gu64*)(ws + OFF_EP), RLX_AGENT);
    return (u64)((u32)(e >> 32) + 1) << 32 | ((u32)e + 1);
}
__device__ __forceinline__ void wait_go(char* ws) {
    const u64 e = succ(ws);
    for (u32 n = 0; n < SPIN_CAP && __builtin_amdgcn_ballot_w64(__hip_atomic_load((gu64*)(ws + OFF_GO + (threadIdx.x & 7) * 128), RLX_AGENT) != e); ++n) __builtin_amdgcn_s_sleep(2);
    CBAR();
}
__device__ __forceinline__ bool spin_fail(char* ws, u32& spins) {
    ++spins;
    if ((spins & 63u) == 0u) {
        if (ldw(ws, OFF_ABORT)) return true;
        if (spins > SPIN_CAP) { __hip_atomic_store((gu32*)(ws + OFF_ABORT), 1u, RLX_AGENT); return true; }
    }
    return false;
}
__device__ __forceinline__ void wait_set(char* ws, u32 off) {
    u32 spins = 0;
    while (__builtin_amdgcn_ballot_w64(ldw(ws, off) == 0)) {
        if (spin_fail(ws, spins)) break;
        __builtin_amdgcn_s_sleep(SLP_W);
    }
    CBAR();
}
constexpr u32 OOB = 0x80000000u;
__device__ __forceinline__ u32 cgo(u32 cg) { return (cg >> 5) * 16384 + (cg & 31) * 256; }
__device__ __forceinline__ u32 sumo(u32 l) {
    const u32 lane = threadIdx.x & 63, b = l * 128 + (lane >> 5) * 64 + (lane & 15) * 4;
    return lane & 16 ? OOB : O_SUM + b / 576 * 1024 + b % 576;
}
__device__ __forceinline__ u32 sums(v4u x) {
    float s = 0.f, q = 0.f;
    __builtin_amdgcn_sched_barrier(0);
#pragma unroll
    for (int e = 0; e < 4; ++e) {
        const u32 z = x[e];
        const bfv2 y = __builtin_bit_cast(bfv2, z);
        s = __builtin_amdgcn_fdot2_f32_bf16(y, __builtin_bit_cast(bfv2, 0x3f803f80u), s, false);
        q = __builtin_amdgcn_fdot2_f32_bf16(y, y, q, false);
    }
    auto a = __builtin_amdgcn_permlane32_swap(__float_as_uint(s), __float_as_uint(q), false, false);
    const u32 r = __float_as_uint(__uint_as_float(a[0]) + __uint_as_float(a[1]));
    a = __builtin_amdgcn_permlane16_swap(r, r, false, false);
    return __float_as_uint(__uint_as_float(a[0]) + __uint_as_float(a[1]));
}

template <int N, bool FIN>
__device__ __forceinline__ void t_item(int rid, u32 i, u32 j, u32 m, u32 pol, u32 w, rsrc_t ro, char* ws, rsrc_t rs) {
    const u32 lane = threadIdx.x & 63, s = i & 63, side = (((s >> 5) ^ pol ^ m) & 1) << 13;
    v4u x[N], p[N][4];
    u32 v[N];
    wait_set(ws, OFF_FB + m * 4096 + j * 128 + fl(i));
#pragma unroll
    for (int u = 0; u < N; ++u) { v[u] = cgo(12 * j + 4 * (w + u) + (lane >> 4)) + (lane & 15) * 16; x[u] = ld16(rs, OFF_TB + (i << 16) + (FIN ? side : m << 13) + v[u]); }
    wait_set(ws, OFF_FP + (m * 3 + (j >> 1)) * 4096 + (lane & 3) * 128 + fl(i));
    STAMP(i, 0);
#pragma unroll
    for (int u = 0; u < N; ++u)
#pragma unroll
        for (int k = 0; k < 4; ++k) p[u][k] = __builtin_amdgcn_raw_buffer_load_b128(rs, v[u], OFF_PB + (i << 18) + (k << 16) + ((((i >> 3) ^ pol ^ m) & 1) << 13), 16);
#pragma unroll
    for (int u = 0; u < N; ++u) {
#pragma unroll
        for (int e = 0; e < 4; ++e) {
            f32x2 a = {bflo(x[u][e]), bfhi(x[u][e])};
#pragma unroll
            for (int k = 0; k < 4; ++k) a += f32x2{bflo(p[u][k][e]), bfhi(p[u][k][e])};
            x[u][e] = pack2(a[0], a[1]);
        }
        if (FIN) __builtin_amdgcn_raw_buffer_store_b128(x[u], ro, (s * 32 + 2 * (lane & 15) + m) * 1152 + (12 * j + 4 * (w + u) + (lane >> 4)) * 16, 0, 0);
        else {
            st16(rs, OFF_TA + ((64 + s) << 16) + side + v[u], x[u]);
            __builtin_amdgcn_raw_buffer_store_b32(sums(x[u]), rs, sumo(3 * j + w), OFF_TA + ((64 + s) << 16) + side, 16);
        }
    }
    STAMP(i, 1);
}
__device__ __forceinline__ bool t_last(u32* cnt) {
    DRAIN();
    u32 o = 0;
    if ((threadIdx.x & 63) == 0) o = __hip_atomic_fetch_add(cnt, 1u, __ATOMIC_RELAXED, __HIP_MEMORY_SCOPE_WORKGROUP);
    return __builtin_amdgcn_readfirstlane(o) == 2;
}
__device__ __forceinline__ void role_t(u64 t_in, int rid, u32 j, u32 m, const u16* x0, u16* out, char* ws, rsrc_t rs, char* smem) {
    const u32 lane = threadIdx.x & 63, w = __builtin_amdgcn_readfirstlane(threadIdx.x >> 6);
    u32* cnt = (u32*)smem + 64;
    cnt[threadIdx.x] = 0;
    __syncthreads();
    const rsrc_t ro = __builtin_amdgcn_make_buffer_rsrc(out, 0, XB, 0x00020000);
    if (w < 3) {
        const rsrc_t rx = __builtin_amdgcn_make_buffer_rsrc((void*)x0, 0, XB, 0x00020000);
        const u32 cg = 12 * j + 4 * w + (lane >> 4), vx = (2 * (lane & 15) + m) * 1152 + cg * 16, v = OFF_TA + (m << 13) + cgo(cg) + (lane & 15) * 16;
#pragma unroll 1
        for (u32 s0 = 0; s0 < 64; s0 += s0 < 4 ? 2 : 10) {
            const u32 n = s0 < 4 ? 2 : 10;
            if (s0 == 14) until(t_in + TDEL);
            v4u x[10];
#pragma unroll
            for (u32 u = 0; u < 10; ++u) x[u] = __builtin_amdgcn_raw_buffer_load_b128(rx, u < n ? vx + (s0 + u) * 36864 : OOB, 0, 0);
#pragma unroll
            for (u32 u = 0; u < 10; ++u) if (u < n) {
                st16(rs, v + ((s0 + u) << 16), x[u]);
                __builtin_amdgcn_raw_buffer_store_b32(sums(x[u]), rs, sumo(3 * j + w), OFF_TA + (m << 13) + ((s0 + u) << 16), 16);
            }
            if (s0 == 0) { wait_go(ws); __builtin_amdgcn_s_barrier(); }
            if (t_last(cnt + s0) && lane < n) setw(ws, OFF_FA + m * 4096 + j * 128 + fl(s0 + lane));
        }
    }
    else __builtin_amdgcn_s_barrier();
    CBAR();
    wait_set(ws, OFF_POL);
    const u32 pol = ldw(ws, OFF_POL);
    if (w == 3) {
        for (u32 i = 64; i < 127; ++i) t_item<3, true>(rid, i, j, m, pol, 0, ro, ws, rs);
        return;
    }
    for (u32 i = 0; i < 64; ++i) {
        t_item<1, false>(rid, i, j, m, pol, w, ro, ws, rs);
        if (t_last(cnt + 64 + i) && lane == 0) setw(ws, OFF_FA + m * 4096 + j * 128 + fl(64 + i));
    }
    t_item<1, true>(rid, 127, j, m, pol, w, ro, ws, rs);
}

#define CLOBBERS "memory", "vcc", "scc", \
    "v0", "v1", "v2", "v3", "v4", "v5", "v6", "v7", "v8", "v9", "v10", "v11", "v12", "v13", "v14", "v15", "v16", "v17", "v18", "v19", "v20", "v21", "v22", "v23", \
    "v24", "v25", "v26", "v27", "v28", "v29", "v30", "v31", "v32", "v33", "v34", "v35", "v36", "v37", "v38", "v39", "v40", "v41", "v42", "v43", "v44", "v45", "v46", "v47", \
    "v48", "v49", "v50", "v51", "v52", "v53", "v54", "v55", "v56", "v57", "v58", "v59", "v60", "v61", "v62", "v63", "v64", "v65", "v66", "v67", "v68", "v69", "v70", "v71", \
    "v72", "v73", "v74", "v75", "v76", "v77", "v78", "v79", "v80", "v81", "v82", "v83", "v84", "v85", "v86", "v87", "v88", "v89", "v90", "v91", "v92", "v93", "v94", "v95", \
    "v96", "v97", "v98", "v99", "v100", "v101", "v102", "v103", "v104", "v105", "v106", "v107", "v108", "v109", "v110", "v111", "v112", "v113", "v114", "v115", "v116", "v117", "v118", "v119", \
    "v120", "v121", "v122", "v123", "v124", "v125", "v126", "v127", "v128", "v129", "v130", "v131", "v132", "v133", "v134", "v135", "v136", "v137", "v138", "v139", "v140", "v141", "v142", "v143", \
    "v144", "v145", "v146", "v147", "v148", "v149", "v150", "v151", "v152", "v153", "v154", "v155", "v156", "v157", "v158", "v159", "v160", "v161", "v162", "v163", "v164", "v165", "v166", "v167", \
    "v168", "v169", "v170", "v171", "v172", "v173", "v174", "v175", "v176", "v177", "v178", "v179", "v180", "v181", "v182", "v183", "v184", "v185", "v186", "v187", "v188", "v189", "v190", "v191", \
    "v192", "v193", "v194", "v195", "v196", "v197", "v198", "v199", "v200", "v201", "v202", "v203", "v204", "v205", "v206", "v207", "v208", "v209", "v210", "v211", "v212", "v213", "v214", "v215", \
    "v216", "v217", "v218", "v219", "v220", "v221", "v222", "v223", "v224", "v225", "v226", "v227", "v228", "v229", "v230", "v231", "v232", "v233", "v234", "v235", "v236", "v237", "v238", "v239", \
    "v240", "v241", "v242", "v243", "v244", "v245", "v246", "v247", "v248", "v249", "v250", "v251", "v252", "v253", "v254", "v255", "a0", "a1", "a2", "a3", "a4", "a5", "a6", "a7", \
    "a8", "a9", "a10", "a11", "a12", "a13", "a14", "a15", "a16", "a17", "a18", "a19", "a20", "a21", "a22", "a23", "a24", "a25", "a26", "a27", "a28", "a29", "a30", "a31", \
    "a32", "a33", "a34", "a35", "a36", "a37", "a38", "a39", "a40", "a41", "a42", "a43", "a44", "a45", "a46", "a47", "a48", "a49", "a50", "a51", "a52", "a53", "a54", "a55", \
    "a56", "a57", "a58", "a59", "a60", "a61", "a62", "a63", "a64", "a65", "a66", "a67", "a68", "a69", "a70", "a71", "a72", "a73", "a74", "a75", "a76", "a77", "a78", "a79", \
    "a80", "a81", "a82", "a83", "a84", "a85", "a86", "a87", "a88", "a89", "a90", "a91", "a92", "a93", "a94", "a95", "a96", "a97", "a98", "a99", "a100", "a101", "a102", "a103", \
    "a104", "a105", "a106", "a107", "a108", "a109", "a110", "a111", "a112", "a113", "a114", "a115", "a116", "a117", "a118", "a119", "a120", "a121", "a122", "a123", "a124", "a125", "a126", "a127", \
    "a128", "a129", "a130", "a131", "a132", "a133", "a134", "a135", "a136", "a137", "a138", "a139", "a140", "a141", "a142", "a143", "a144", "a145", "a146", "a147", "a148", "a149", "a150", "a151", \
    "a152", "a153", "a154", "a155", "a156", "a157", "a158", "a159", "a160", "a161", "a162", "a163", "a164", "a165", "a166", "a167", "a168", "a169", "a170", "a171", "a172", "a173", "a174", "a175", \
    "a176", "a177", "a178", "a179", "a180", "a181", "a182", "a183", "a184", "a185", "a186", "a187", "a188", "a189", "a190", "a191", "a192", "a193", "a194", "a195", "a196", "a197", "a198", "a199", \
    "a200", "a201", "a202", "a203", "a204", "a205", "a206", "a207", "a208", "a209", "a210", "a211", "a212", "a213", "a214", "a215", "a216", "a217", "a218", "a219", "a220", "a221", "a222", "a223", \
    "a224", "a225", "a226", "a227", "a228", "a229", "a230", "a231", "a232", "a233", "a234", "a235", "a236", "a237", "a238", "a239", "a240", "a241", "a242", "a243", "a244", "a245", "a246", "a247", \
    "a248", "a249", "a250", "a251", "a252", "a253", "a254", "a255", "s38", "s39", "s40", "s41", "s42", "s43", "s44", "s45", "s46", "s47", "s48", "s49", "s50", "s51", "s52", "s53", \
    "s54", "s55", "s56", "s57", "s58", "s59", "s60", "s61", "s62", "s63", "s64", "s65", "s66", "s67", "s68", "s69", "s70", "s71", "s72", "s73", "s74", "s75", "s76", "s77", \
    "s78", "s79", "s80", "s81", "s82", "s83", "s84", "s85", "s86", "s87", "s88", "s89", "s90", "s91", "s92", "s93", "s94", "s95", "s96", "s97", "s98", "s99", "s100", "s101"
#define LANES() const int tid = threadIdx.x, lane = tid & 63, c = lane & 15, g = lane >> 4, q = c >> 2, p4 = c & 3; \
    const int wave = __builtin_amdgcn_readfirstlane(tid >> 6); u32* tab = (u32*)(smem + L_TAB) + tid; (void)q; (void)p4; (void)g
struct Par { u32 role, h, p, woff; };
__device__ __forceinline__ u32 rows(u32 i, u32 m) { return (i << 16) + (i < 64 ? m << 13 : (((i >> 5 ^ m) & 1) << 13) + 1); }
__device__ __forceinline__ u32 slot(u32 i) { return ((i & 63) << 19) + (((i >> 2 ^ i >> 6) & 1) << 13) + 1; }
__device__ __forceinline__ u32 part(u32 i, u32 m) { return (i << 18) + (((i >> 3 ^ m) & 1) << 13) + 1; }
template <class FS, class FD>
__device__ __forceinline__ u32 items(u32 K, u32 mk, u32 fin, u32 fout, FS src, FD dst, char* smem) {
    const u32 n = threadIdx.x - 1;
    auto idx = [&](u32 k) { const u32 t = k - K; return t < 2 * (64 - K) ? ((t & 1) ? K : 64) + (t >> 1) : k & 127; };
    if (threadIdx.x < 130) {
        v4u* row = (v4u*)(smem + L_IT) + 2 * threadIdx.x;
        row[0] = v4u{n + 1 == mk ? OFF_NEVER + 4 : n + 1 < 128 ? fin + fl(idx(n + 1)) : OFF_NEVER, src(idx(n + 1)), dst(idx(n)), fout + fl(idx(n - 1))};
        row[1] = v4u{0u, idx(n) & 63 ? 0u : 0xf149f2cau, 0x14000 + (idx(n) >> 6) * 16384, idx(n)};
    }
    return fin + fl(mk ? 64 : 0);
}
#define KA 15
#define KB 14
#define KH 13
#define KW 12

__device__ __forceinline__ Par role_h(u32 m, u32 h, char* smem) {
    LANES();
    tab[0] = 0x10000 + wave * 8192 + lane * 16;
    tab[256] = tid * 16;
    tab[2 * 256] = cgo(16 * wave) + lane * 16;
    tab[3 * 256] = 0x10000 + 5120 + lane * 4;
    tab[4 * 256] = (8 * wave + g) * 8192 + (c ^ ((g | (wave & 1) << 2) << 1)) * 16;
    tab[5 * 256] = (4 * g + q) * 256 + ((2 * wave) ^ (q | (g & 1) << 2)) * 32 + p4 * 8;
    tab[6 * 256] = tab[5 * 256] ^ 32;
    const u32 f = items(KH, 0, OFF_FB + m * 4096, OFF_FKV + (h >> 2) * 4096 + (h & 3) * 256 + m * 128, [&](u32 i) { return OFF_TB + rows(i, m); }, [&](u32 i) { return OFF_QX + h * 16384 + m * 4096 + slot(i); }, smem);
    return Par{1, f, 0, h * 256};
}

__device__ __forceinline__ Par role_w(u32 m, u32 kb, u32 nb, char* smem) {
    LANES();
    tab[0] = wave * 8192 + lane * 16;
    tab[256] = wave * 32768 + lane * 16;
    tab[512] = cgo(24 * nb + 6 * wave + g) + c * 16;
    tab[768] = (256 * wave + 4 * g + (c >> 3)) * 1152 + ((lane & 7) ^ ((g & 1) << 2)) * 16;
#pragma unroll
    for (int t = 0; t < 3; ++t) {
        const int col = wave * 96 + (t < 2 ? p4 * 16 + t * 8 : 64 + p4 * 8);
        tab[(4 + t) * 256] = (col >> 7) * 2048 + (q & 1) * 1024 + (2 * g + (q >> 1)) * 128 + ((col & 127) ^ ((g & 1) << 6));
    }
    tab[7 * 256] = (g & 1) ? OOB : cgo(24 * nb + 6 * wave + 4 + (g >> 1)) + c * 16;
    const u32 f = items(KW, 0, OFF_COB + (m * 4 + kb) * 4096, OFF_FP + (m * 3 + nb) * 4096 + kb * 128, [&](u32 i) { return OFF_OX + kb * 8 * 16384 + m * 4096 + slot(i); },
          [&](u32 i) { return OFF_PB + kb * 65536 + part(i, m); }, smem);
    return Par{2, f, 0, kb * 1024 * 1152 + nb * 384};
}

__device__ __forceinline__ Par role_a2(u32 m, u32 j, char* smem) {
    LANES();
    const int w = wave < 3 ? wave : 2;
    tab[0] = 0x8000 + lane * 16;
    tab[256] = tid * 16;
    tab[512] = cgo(16 * wave) + lane * 16;
    tab[768] = wave < 3 ? cgo(12 * j + 4 * wave + g) + c * 16 : OOB;
    tab[4 * 256] = 0x9000 + g * 256 + c * 4;
    tab[5 * 256] = wave < 3 ? sumo(3 * j + wave) : OOB;
    tab[6 * 256] = (16 * wave + (lane >> 2)) * 1152 + (lane & 3) * 16;
    tab[7 * 256] = w * 36864 + p4 * 16 + (20 * (g >> 1) + 8 * (g & 1) + q) * 64;
    tab[8 * 256] = w * 36864 + p4 * 16 + (72 * g + 16 + q) * 64;
    const u32 f = items(KB, KB, OFF_FO + m * 4096, OFF_FB + m * 4096 + j * 128, [&](u32 i) { return OFF_OA + rows(i, m); }, [&](u32 i) { return OFF_TB + rows(i, m); }, smem);
    return Par{4, f, 0, j * 192};
}

__device__ __forceinline__ Par role_a1(u32 m, u32 hg, char* smem) {
    LANES();
    const int hd = 4 * hg + wave;
    tab -= 768;
    tab[0] = wave * 8192 + lane * 16;
    tab[256] = !(g & 1) ? cgo(2 * hd + (g >> 1)) + c * 16 : g == 1 && !p4 ? 32768 + hd * 64 + c * 4 : OOB;
    tab[2 * 256] = cgo(16 * wave) + lane * 16;
    tab[3 * 256] = tid * 16;
    tab[4 * 256] = L_EX + ((wave >> 1) * 16 + c) * 16 + (wave & 1) * 8;
    tab[5 * 256] = g ? L_EX + 4096 : tab[4 * 256] + 512;
    tab[6 * 256] = L_EX + 2048 + ((wave >> 1) * 16 + 4 * g) * 16 + (2 * (wave & 1) + (c & 1)) * 4;
    tab[7 * 256] = 5120 + lane * 4;
    tab[8 * 256] = 7424 + g * 16;
    tab[9 * 256] = 4 * g - c;
    tab[10 * 256] = (4 * g + q) * 144 + 36 * wave + 4 * (wave & 1) + p4 * 8;
    tab[11 * 256] = (wave < 2 ? p4 >> 1 : 2) * 9216 + (4 * g + q) * 144 + 72 * (p4 & 1) + 32;
    if (tid < 2) *(v4u*)(smem + L_EX + 4096 + tid * 1024) = v4u{0u, 0u, 0u, 0u};
    const u32 f = items(KA, KA, OFF_FA + m * 4096, OFF_FO + m * 4096 + hg * 128, [&](u32 i) { return OFF_TA + rows(i, m); }, [&](u32 i) { return OFF_OA + rows(i, m); }, smem);
    return Par{3, f, 0, hg * 144};
}

#define SB() __builtin_amdgcn_sched_barrier(0)
__device__ __forceinline__ u32 now32() { return (u32)__builtin_amdgcn_s_memtime(); }
__device__ __forceinline__ Par role_chain(u32 h, u32 p, char* smem) {
    LANES();
    const int ro = (q & 1) + 4 * (q >> 1) + 8 * (g >> 1) + 16 * (g & 1), rr = (q & 1) | (g & 1) << 1 | (q >> 1) << 2;
    tab[0] = lane * 16;
    tab[256] = L_W + (wave * 15 + (wave == 3 ? 2 : 0)) * 2048 + lane * 16;
    tab[2 * 256] = ((g & 1) + 4 * (g >> 1) + 8 * wave) * 8192 + (c ^ ((g | (wave & 1) << 2) << 1)) * 16;
    tab[3 * 256] = (rr + 8 * (g >> 1)) * 256 + ((2 * wave) ^ rr) * 32 + p4 * 8;
    tab[4 * 256] = tab[3 * 256] ^ 32;
    tab[5 * 256] = (2 * wave + (lane >> 5)) * 1152 + ((lane & 31) ^ ((lane >> 5 | (wave & 2)) << 2)) * 16;
    tab[6 * 256] = ((q & 1) + 4 * (g >> 1) + 16 * (g & 1)) * 512 + ((2 * wave + (p4 >> 1)) ^ ((q & 1) | (g & 2))) * 64 + (q >> 1) * 16 + (p4 & 1) * 8;
    tab[7 * 256] = (32 * wave + (lane >> 2)) * 1152 + (lane & 3) * 16;
    tab[8 * 256] = ro * 64 + (wave & 1) * 32 + p4 * 8;
    return Par{0, h, p, 0};
}

#define MAP 4
#define DED 3
#define YX 0
constexpr int RW = MAP == 4 ? (YX & 2 ? 94 : 93) : 90, RA1 = 24, RA2 = MAP >= 2 ? 184 : 56, RTT = 58;
#define PFO 1
#define WDEL1 700
#define CDEL 1000
#define WDEL 700
#define NPOL 6
static_assert(OFF_GO == 131072u);
static_assert(OFF_EP == 132096u);
static_assert(OFF_POL == 64u);
static_assert(OFF_RDY == 128u);
static_assert(OFF_COB == 53248u);
static_assert(OFF_FKV == 86016u);
static_assert(OFF_XA == 0u);
static_assert(OFF_XB == 2097152u);
static_assert(OFF_TA == 8388608u);
static_assert(OFF_OA == 25165824u);
static_assert(OFF_NEVER == 192u);
static_assert(L_IT == 158720u);
static_assert(OFF_QX == 67108864u);
static_assert(OFF_KX == 100663296u);
static_assert(OFF_VX == 134217728u);
static_assert(OFF_OX == 167772160u);
static_assert(WS_BYTES == 201326592u);
static_assert(L_TAB == 147456u);
static_assert(L_EX == 138240u);
static_assert(O_SUM == 34816u);
static_assert(L_W == 32064);
__device__ __forceinline__ void measure_pol(char* ws, rsrc_t rs, char* smem) {
    const int wave = __builtin_amdgcn_readfirstlane(threadIdx.x >> 6);
    u32 best[2] = {~0u, ~0u};
    for (int n = 0; n < NPOL; ++n)
#pragma unroll
        for (int i = 0; i < 2; ++i) {
            SB();
            const u32 t0 = now32();
            SB();
            st4(rs, (wave & 1) * MB2 + (508 + 2 * i + (wave >> 1)) * 4096 + n * 128, t0);
            DRAIN();
            const u32 t = now32() - t0;
            best[i] = t < best[i] ? t : best[i];
        }
    int* d = (int*)smem;
    if ((threadIdx.x & 63) == 0) d[wave] = (int)(best[0] - best[1]);
    wait_go(ws);
    __syncthreads();
    if (threadIdx.x == 0) __hip_atomic_store((gu32*)(ws + OFF_POL), 4u | (d[0] + d[2] >= 0) | (d[1] + d[3] >= 0) << 1, RLX_AGENT);
}

__global__ __launch_bounds__(256, 1) void mega_kernel(const u16* __restrict__ x0, const u16* __restrict__ wqkv, const u16* __restrict__ woA,
                                                     const u16* __restrict__ mem0, const u16* __restrict__ Wq, const u16* __restrict__ Wk,
                                                     const u16* __restrict__ Wv, const u16* __restrict__ Wo, u16* out, char* ws) {
    __shared__ __attribute__((aligned(16))) char smem[163840];
    char* wd = (char*)(((uintptr_t)ws + SYNC_BYTES + MB2 - 1) & ~(uintptr_t)(MB2 - 1));
    const rsrc_t rs = __builtin_amdgcn_make_buffer_rsrc(wd, 0, WS_BYTES, 0x00020000);
    const u64 t_in = __builtin_amdgcn_s_memrealtime();
    {
        const char* pc; u32 n;
        asm volatile("s_getpc_b64 %0\n.Lbeg:\ns_mov_b32 %1, .Lfin-.Lbeg" : "=s"(pc), "=s"(n));
        const u32 o = ((blockIdx.x >> 3 & 3) * 256 + threadIdx.x) * 128;
        if (o < n) TOUCH(pc + o);
    }
    u32 rid = (blockIdx.x & 7) * 32 + (blockIdx.x >> 3);
    rid = (rid & 143) | (rid & 48) << 1 | (rid & 64) >> 2 ^ (DED & 16);
    if (rid & 16) {
        const u32 y = (rid >> 6) ^ YX, sl = blockIdx.x >> 3, m = sl & 1, e = sl >> 1;
        auto rw = [](u32 i) { const u32 v = 12 + (i + 3) % 12; return v / 6 * 32 + v % 6 + 26; };
        auto rh = [](u32 h) { return (h >> 3) * 32 + 16 + (h & 7); };
        if (y < 2) rid = e < 10 ? m * 128 + rh(y * 10 + e) : e < 12 ? m * 128 + rw((2 * y + e - 10) * 3) : y * 128 + (e < 13 ? 62 + m : (e - 12) * 32 + 24 + m);
        else rid = m * 128 + (y == 2 ? (e < 8 ? 24 + e : rw((e & 3) * 3 + (e >> 2) - 1)) : e < 12 ? rh(20 + e) : 46 + e);
    }
    asm volatile("" : "+s"(rid));
    STAMP(159, 0);
    const u32 xq = (rid >> 5) & 3, t = rid & 31, layer = rid >> 7;
    const int u = t - 24;
    const int role = t < 16 ? 0 : t < 24 ? 1 : xq == 0 ? 3 : u < 2 ? 4 : xq == 1 ? 5 : 2;
    const int ix = role == 3 ? u : role == 4 ? 2 * xq - 2 + u : role == 5 ? u - 2 : (xq * 6 + u - 5) % 12;
    if (role == 5) { role_t(t_in, rid, ix, layer, x0, out, ws, rs, smem); asm volatile("s_endpgm" ::: "memory"); }
    if (role == 3) for (u32 k = (layer * 8 + ix) * 256 + threadIdx.x; k < 15552; k += 4096) TOUCH((const char*)wqkv + k * 128);
    if (role == 0 && layer) {
        const u32 i = t * 256 + threadIdx.x;
        if ((DED & 2) && i < 3888) TOUCH((const char*)wqkv + (xq * 3888 + i) * 128);
        if (xq && i < 1728) TOUCH((const char*)woA + (i / 3) * 1152 + (xq - 1) * 384 + (i % 3) * 128);
        for (const u16* w : {Wk, Wv})
            for (u32 k = i; k < 9216; k += 4096) TOUCH((const char*)w + (k >> 4) * 8192 + xq * 2048 + (k & 15) * 128);
        if (PFO) for (u32 k = i; k < 9216; k += 4096) TOUCH((const char*)Wo + (xq * 9216 + k) * 128);
        for (u32 k = 0; k < 18; ++k) st16(rs, (k % 3 * 3 >> 1) * MB2 + k / 3 * 262144 + (xq * 4096 + i) * 16, v4u{~0u, ~0u, ~0u, ~0u});
        DRAIN();
        wait_go(ws);
        __syncthreads();
        if (threadIdx.x == 0) __hip_atomic_fetch_add((gu32*)(ws + OFF_RDY), 1u, RLX_AGENT);
    }
    if (role == 2 && layer && ix < 8) {
        const u64 e = succ(ws);
        const rsrc_t rc = __builtin_amdgcn_make_buffer_rsrc(ws, 0, OFF_GO, 0x00020000);
        for (u32 k = 0; k < 4; ++k) st16(rc, ix * 16384 + k * 4096 + threadIdx.x * 16, v4u{0u, 0u, 0u, 0u});
        DRAIN();
        __syncthreads();
        if (threadIdx.x == 0) __hip_atomic_store((gu64*)(ws + OFF_GO + ix * 128), e, RLX_AGENT);
    }
    if (rid == RW) measure_pol(ws, rs, smem);
    until(t_in + (role == 2 ? (layer ? WDEL1 : WDEL) : role == 0 && layer ? CDEL : 0));
    Par P{5, 0, 0, 0};
    const u16* w0 = role == 2 ? Wo : role == 3 ? wqkv : role == 4 ? woA : Wq;
    if (role == 0) P = role_chain(xq * 8 + (t >> 1), t & 1, smem);
    else if (role == 1) P = role_h(layer, xq * 8 + t - 16, smem);
    else if (role == 2) P = role_w(layer, ix / 3, ix % 3, smem);
    else if (role == 3) P = role_a1(layer, ix, smem);
    else if (role == 4) P = role_a2(layer ^ (MAP >= 2), ix, smem);
    if (role == 0) P.woff = (u32)t_in;
    const int wave = __builtin_amdgcn_readfirstlane(threadIdx.x >> 6);
    asm volatile(
    "s_cmp_eq_u32 %13, 1\n"
    "s_cbranch_scc1 .Lrole1_%=\n"
    "s_cmp_eq_u32 %13, 2\n"
    "s_cbranch_scc1 .Lrole2_%=\n"
    "s_cmp_eq_u32 %13, 3\n"
    "s_cbranch_scc1 .Lrole3_%=\n"
    "s_cmp_eq_u32 %13, 4\n"
    "s_cbranch_scc1 .Lrole4_%=\n"
    "s_cmp_eq_u32 %13, 5\n"
    "s_cbranch_scc1 .Lrole5_%=\n"
    "s_mov_b64 s[40:41], %0\n"
    "s_and_b32 s41, s41, 0xffff\n"
    "s_mov_b32 s42, 0xc000000\n"
    "s_mov_b32 s43, 0x20000\n"
    "s_mov_b32 s57, 131072\n"
    "v_mbcnt_lo_u32_b32 v10, -1, 0\n"
    "v_mbcnt_hi_u32_b32 v10, -1, v10\n"
    "s_lshl_b32 s73, %2, 8\n"
    "s_add_u32 s73, s73, 147456\n"
    "v_lshl_add_u32 v10, v10, 2, s73\n"
    "ds_read_b32 v0, v10\n"
    "ds_read_b32 v2, v10 offset:1024\n"
    "ds_read_b32 v1, v10 offset:2048\n"
    "ds_read_b32 v3, v10 offset:3072\n"
    "ds_read_b32 v4, v10 offset:4096\n"
    "ds_read_b32 v5, v10 offset:5120\n"
    "ds_read_b32 v6, v10 offset:6144\n"
    "ds_read_b32 v8, v10 offset:7168\n"
    "ds_read_b32 v9, v10 offset:8192\n"
    "s_waitcnt lgkmcnt(0)\n"
    "s_barrier\n"
    "s_xor_b32 s73, %2, 0\n"
    "s_lshl_b32 s73, s73, 12\n"
    "v_add_u32 v96, s73, v3\n"
    "v_add_u32 v97, s73, v4\n"
    "s_xor_b32 s73, %2, 1\n"
    "s_lshl_b32 s73, s73, 12\n"
    "v_add_u32 v98, s73, v3\n"
    "v_add_u32 v99, s73, v4\n"
    "s_xor_b32 s73, %2, 2\n"
    "s_lshl_b32 s73, s73, 12\n"
    "v_add_u32 v100, s73, v3\n"
    "v_add_u32 v101, s73, v4\n"
    "s_xor_b32 s73, %2, 3\n"
    "s_lshl_b32 s73, s73, 12\n"
    "v_add_u32 v102, s73, v3\n"
    "v_add_u32 v103, s73, v4\n"
    "s_lshl_b32 s60, %4, 8\n"
    "s_lshl_b32 s61, %2, 10\n"
    "s_mul_i32 s63, %4, 147456\n"
    "s_mul_i32 s62, %5, 576\n"
    "s_add_u32 s63, s63, s62\n"
    "s_mov_b64 s[44:45], %11\n"
    "s_and_b32 s45, s45, 0xffff\n"
    "s_mov_b32 s46, 0x480000\n"
    "s_mov_b32 s47, 0x20000\n"
    "s_lshl_b32 s73, s61, 1\n"
    "s_add_u32 m0, s73, 0\n"
    "s_add_u32 s62, s63, 512\n"
    "buffer_load_dwordx4 v8, s[44:47], s62 offen lds\n"
    "s_lshl_b32 s73, s61, 1\n"
    "s_add_u32 m0, s73, 1024\n"
    "s_add_u32 s62, s63, 18944\n"
    "buffer_load_dwordx4 v8, s[44:47], s62 offen lds\n"
    "s_mov_b64 vcc, 0\n"
    "s_mov_b32 vcc_lo, %7\n"
    "s_cbranch_vccz .Lsl0_%=\n"
    "s_sleep 10\n"
    ".Lsl0_%=:\n"
    "s_mov_b64 s[44:45], %8\n"
    "s_and_b32 s45, s45, 0xffff\n"
    "s_mov_b32 s46, 0x480000\n"
    "s_mov_b32 s47, 0x20000\n"
    "s_add_u32 m0, s61, 16384\n"
    "s_add_u32 s62, s60, 262144\n"
    "buffer_load_dwordx4 v1, s[44:47], s62 offen lds\n"
    "s_add_u32 m0, s61, 20480\n"
    "s_add_u32 s62, s60, 1310720\n"
    "buffer_load_dwordx4 v1, s[44:47], s62 offen lds\n"
    "s_add_u32 m0, s61, 24576\n"
    "s_add_u32 s62, s60, 2359296\n"
    "buffer_load_dwordx4 v1, s[44:47], s62 offen lds\n"
    "s_add_u32 m0, s61, 28672\n"
    "s_add_u32 s62, s60, 3407872\n"
    "buffer_load_dwordx4 v1, s[44:47], s62 offen lds\n"
    "s_cbranch_vccz .Lsl1_%=\n"
    "s_sleep 10\n"
    ".Lsl1_%=:\n"
    "s_add_u32 m0, s61, 32768\n"
    "s_add_u32 s62, s60, 786432\n"
    "buffer_load_dwordx4 v1, s[44:47], s62 offen lds\n"
    "s_add_u32 m0, s61, 36864\n"
    "s_add_u32 s62, s60, 1835008\n"
    "buffer_load_dwordx4 v1, s[44:47], s62 offen lds\n"
    "s_add_u32 m0, s61, 40960\n"
    "s_add_u32 s62, s60, 2883584\n"
    "buffer_load_dwordx4 v1, s[44:47], s62 offen lds\n"
    "s_add_u32 m0, s61, 45056\n"
    "s_add_u32 s62, s60, 3932160\n"
    "buffer_load_dwordx4 v1, s[44:47], s62 offen lds\n"
    "s_cbranch_vccz .Lsl2_%=\n"
    "s_sleep 10\n"
    ".Lsl2_%=:\n"
    "s_add_u32 m0, s61, 49152\n"
    "s_add_u32 s62, s60, 278528\n"
    "buffer_load_dwordx4 v1, s[44:47], s62 offen lds\n"
    "s_add_u32 m0, s61, 53248\n"
    "s_add_u32 s62, s60, 1327104\n"
    "buffer_load_dwordx4 v1, s[44:47], s62 offen lds\n"
    "s_add_u32 m0, s61, 57344\n"
    "s_add_u32 s62, s60, 2375680\n"
    "buffer_load_dwordx4 v1, s[44:47], s62 offen lds\n"
    "s_add_u32 m0, s61, 61440\n"
    "s_add_u32 s62, s60, 3424256\n"
    "buffer_load_dwordx4 v1, s[44:47], s62 offen lds\n"
    "s_cbranch_vccz .Lsl3_%=\n"
    "s_sleep 10\n"
    ".Lsl3_%=:\n"
    "s_add_u32 m0, s61, 65536\n"
    "s_add_u32 s62, s60, 802816\n"
    "buffer_load_dwordx4 v1, s[44:47], s62 offen lds\n"
    "s_add_u32 m0, s61, 69632\n"
    "s_add_u32 s62, s60, 1851392\n"
    "buffer_load_dwordx4 v1, s[44:47], s62 offen lds\n"
    "s_add_u32 m0, s61, 73728\n"
    "s_add_u32 s62, s60, 2899968\n"
    "buffer_load_dwordx4 v1, s[44:47], s62 offen lds\n"
    "s_add_u32 m0, s61, 77824\n"
    "s_add_u32 s62, s60, 3948544\n"
    "buffer_load_dwordx4 v1, s[44:47], s62 offen lds\n"
    "s_cbranch_vccz .Lsl4_%=\n"
    "s_sleep 10\n"
    ".Lsl4_%=:\n"
    "s_add_u32 m0, s61, 81920\n"
    "s_add_u32 s62, s60, 4194304\n"
    "buffer_load_dwordx4 v1, s[44:47], s62 offen lds\n"
    "s_add_u32 m0, s61, 86016\n"
    "s_add_u32 s62, s60, 4210688\n"
    "buffer_load_dwordx4 v1, s[44:47], s62 offen lds\n"
    "s_add_u32 m0, s61, 90112\n"
    "s_add_u32 s62, s60, 4456448\n"
    "buffer_load_dwordx4 v1, s[44:47], s62 offen lds\n"
    "s_add_u32 m0, s61, 94208\n"
    "s_add_u32 s62, s60, 4472832\n"
    "buffer_load_dwordx4 v1, s[44:47], s62 offen lds\n"
    "s_cbranch_vccz .Lsl5_%=\n"
    "s_sleep 10\n"
    ".Lsl5_%=:\n"
    "s_mov_b64 s[44:45], %9\n"
    "s_and_b32 s45, s45, 0xffff\n"
    "s_mov_b32 s46, 0x480000\n"
    "s_mov_b32 s47, 0x20000\n"
    "s_add_u32 m0, s61, 98304\n"
    "s_add_u32 s62, s60, 0\n"
    "buffer_load_dwordx4 v1, s[44:47], s62 offen lds\n"
    "s_add_u32 m0, s61, 102400\n"
    "s_add_u32 s62, s60, 1048576\n"
    "buffer_load_dwordx4 v1, s[44:47], s62 offen lds\n"
    "s_add_u32 m0, s61, 106496\n"
    "s_add_u32 s62, s60, 2097152\n"
    "buffer_load_dwordx4 v1, s[44:47], s62 offen lds\n"
    "s_add_u32 m0, s61, 110592\n"
    "s_add_u32 s62, s60, 3145728\n"
    "buffer_load_dwordx4 v1, s[44:47], s62 offen lds\n"
    "s_cbranch_vccz .Lsl6_%=\n"
    "s_sleep 10\n"
    ".Lsl6_%=:\n"
    "s_add_u32 m0, s61, 114688\n"
    "s_add_u32 s62, s60, 524288\n"
    "buffer_load_dwordx4 v1, s[44:47], s62 offen lds\n"
    "s_add_u32 m0, s61, 118784\n"
    "s_add_u32 s62, s60, 1572864\n"
    "buffer_load_dwordx4 v1, s[44:47], s62 offen lds\n"
    "s_add_u32 m0, s61, 122880\n"
    "s_add_u32 s62, s60, 2621440\n"
    "buffer_load_dwordx4 v1, s[44:47], s62 offen lds\n"
    "s_add_u32 m0, s61, 126976\n"
    "s_add_u32 s62, s60, 3670016\n"
    "buffer_load_dwordx4 v1, s[44:47], s62 offen lds\n"
    "s_cbranch_vccz .Lsl7_%=\n"
    "s_sleep 10\n"
    ".Lsl7_%=:\n"
    "s_add_u32 m0, s61, 131072\n"
    "s_add_u32 s62, s60, 16384\n"
    "buffer_load_dwordx4 v1, s[44:47], s62 offen lds\n"
    "s_add_u32 m0, s61, 135168\n"
    "s_add_u32 s62, s60, 1064960\n"
    "buffer_load_dwordx4 v1, s[44:47], s62 offen lds\n"
    "s_add_u32 m0, s61, 139264\n"
    "s_add_u32 s62, s60, 2113536\n"
    "buffer_load_dwordx4 v1, s[44:47], s62 offen lds\n"
    "s_add_u32 m0, s61, 143360\n"
    "s_add_u32 s62, s60, 3162112\n"
    "buffer_load_dwordx4 v1, s[44:47], s62 offen lds\n"
    "s_cbranch_vccz .Lsl8_%=\n"
    "s_sleep 10\n"
    ".Lsl8_%=:\n"
    "s_add_u32 m0, s61, 147456\n"
    "s_add_u32 s62, s60, 540672\n"
    "buffer_load_dwordx4 v1, s[44:47], s62 offen lds\n"
    "s_add_u32 m0, s61, 151552\n"
    "s_add_u32 s62, s60, 1589248\n"
    "buffer_load_dwordx4 v1, s[44:47], s62 offen lds\n"
    "s_add_u32 m0, s61, 155648\n"
    "s_add_u32 s62, s60, 2637824\n"
    "buffer_load_dwordx4 v1, s[44:47], s62 offen lds\n"
    "s_add_u32 m0, s61, 159744\n"
    "s_add_u32 s62, s60, 3686400\n"
    "buffer_load_dwordx4 v1, s[44:47], s62 offen lds\n"
    "s_waitcnt vmcnt(32) lgkmcnt(0)\n"
    "s_barrier\n"
    "ds_read_b64_tr_b16 v[36:37], v96 offset:16384\n"
    "ds_read_b64_tr_b16 v[68:69], v98 offset:16384\n"
    "ds_read_b64_tr_b16 v[148:149], v100 offset:16384\n"
    "ds_read_b64_tr_b16 v[180:181], v102 offset:16384\n"
    "ds_read_b64_tr_b16 v[44:45], v97 offset:16384\n"
    "ds_read_b64_tr_b16 v[76:77], v99 offset:16384\n"
    "ds_read_b64_tr_b16 v[156:157], v101 offset:16384\n"
    "ds_read_b64_tr_b16 v[188:189], v103 offset:16384\n"
    "s_waitcnt vmcnt(28) lgkmcnt(0)\n"
    "s_barrier\n"
    "s_cbranch_vccz .Lsl9_%=\n"
    "s_sleep 10\n"
    ".Lsl9_%=:\n"
    "s_add_u32 m0, s61, 16384\n"
    "s_add_u32 s62, s60, 262144\n"
    "buffer_load_dwordx4 v1, s[44:47], s62 offen lds\n"
    "s_add_u32 m0, s61, 20480\n"
    "s_add_u32 s62, s60, 1310720\n"
    "buffer_load_dwordx4 v1, s[44:47], s62 offen lds\n"
    "s_add_u32 m0, s61, 24576\n"
    "s_add_u32 s62, s60, 2359296\n"
    "buffer_load_dwordx4 v1, s[44:47], s62 offen lds\n"
    "s_add_u32 m0, s61, 28672\n"
    "s_add_u32 s62, s60, 3407872\n"
    "buffer_load_dwordx4 v1, s[44:47], s62 offen lds\n"
    "ds_read_b64_tr_b16 v[52:53], v96 offset:32768\n"
    "ds_read_b64_tr_b16 v[84:85], v98 offset:32768\n"
    "ds_read_b64_tr_b16 v[164:165], v100 offset:32768\n"
    "ds_read_b64_tr_b16 v[196:197], v102 offset:32768\n"
    "ds_read_b64_tr_b16 v[60:61], v97 offset:32768\n"
    "ds_read_b64_tr_b16 v[140:141], v99 offset:32768\n"
    "ds_read_b64_tr_b16 v[172:173], v101 offset:32768\n"
    "ds_read_b64_tr_b16 v[204:205], v103 offset:32768\n"
    "s_waitcnt vmcnt(28) lgkmcnt(0)\n"
    "s_barrier\n"
    "s_cbranch_vccz .Lsl10_%=\n"
    "s_sleep 10\n"
    ".Lsl10_%=:\n"
    "s_add_u32 m0, s61, 32768\n"
    "s_add_u32 s62, s60, 786432\n"
    "buffer_load_dwordx4 v1, s[44:47], s62 offen lds\n"
    "s_add_u32 m0, s61, 36864\n"
    "s_add_u32 s62, s60, 1835008\n"
    "buffer_load_dwordx4 v1, s[44:47], s62 offen lds\n"
    "s_add_u32 m0, s61, 40960\n"
    "s_add_u32 s62, s60, 2883584\n"
    "buffer_load_dwordx4 v1, s[44:47], s62 offen lds\n"
    "s_add_u32 m0, s61, 45056\n"
    "s_add_u32 s62, s60, 3932160\n"
    "buffer_load_dwordx4 v1, s[44:47], s62 offen lds\n"
    "ds_read_b64_tr_b16 v[38:39], v96 offset:49152\n"
    "ds_read_b64_tr_b16 v[70:71], v98 offset:49152\n"
    "ds_read_b64_tr_b16 v[150:151], v100 offset:49152\n"
    "ds_read_b64_tr_b16 v[182:183], v102 offset:49152\n"
    "ds_read_b64_tr_b16 v[46:47], v97 offset:49152\n"
    "ds_read_b64_tr_b16 v[78:79], v99 offset:49152\n"
    "ds_read_b64_tr_b16 v[158:159], v101 offset:49152\n"
    "ds_read_b64_tr_b16 v[190:191], v103 offset:49152\n"
    "s_waitcnt vmcnt(28) lgkmcnt(0)\n"
    "s_barrier\n"
    "s_cbranch_vccz .Lsl11_%=\n"
    "s_sleep 10\n"
    ".Lsl11_%=:\n"
    "s_add_u32 m0, s61, 49152\n"
    "s_add_u32 s62, s60, 278528\n"
    "buffer_load_dwordx4 v1, s[44:47], s62 offen lds\n"
    "s_add_u32 m0, s61, 53248\n"
    "s_add_u32 s62, s60, 1327104\n"
    "buffer_load_dwordx4 v1, s[44:47], s62 offen lds\n"
    "s_add_u32 m0, s61, 57344\n"
    "s_add_u32 s62, s60, 2375680\n"
    "buffer_load_dwordx4 v1, s[44:47], s62 offen lds\n"
    "s_add_u32 m0, s61, 61440\n"
    "s_add_u32 s62, s60, 3424256\n"
    "buffer_load_dwordx4 v1, s[44:47], s62 offen lds\n"
    "v_add_u32 v127, 0x10000, v96\n"
    "ds_read_b64_tr_b16 v[54:55], v127\n"
    "v_add_u32 v126, 0x10000, v98\n"
    "ds_read_b64_tr_b16 v[86:87], v126\n"
    "v_add_u32 v125, 0x10000, v100\n"
    "ds_read_b64_tr_b16 v[166:167], v125\n"
    "v_add_u32 v124, 0x10000, v102\n"
    "ds_read_b64_tr_b16 v[198:199], v124\n"
    "v_add_u32 v123, 0x10000, v97\n"
    "ds_read_b64_tr_b16 v[62:63], v123\n"
    "v_add_u32 v122, 0x10000, v99\n"
    "ds_read_b64_tr_b16 v[142:143], v122\n"
    "v_add_u32 v121, 0x10000, v101\n"
    "ds_read_b64_tr_b16 v[174:175], v121\n"
    "v_add_u32 v120, 0x10000, v103\n"
    "ds_read_b64_tr_b16 v[206:207], v120\n"
    "s_waitcnt vmcnt(28) lgkmcnt(0)\n"
    "s_barrier\n"
    "s_cbranch_vccz .Lsl12_%=\n"
    "s_sleep 10\n"
    ".Lsl12_%=:\n"
    "s_add_u32 m0, s61, 65536\n"
    "s_add_u32 s62, s60, 802816\n"
    "buffer_load_dwordx4 v1, s[44:47], s62 offen lds\n"
    "s_add_u32 m0, s61, 69632\n"
    "s_add_u32 s62, s60, 1851392\n"
    "buffer_load_dwordx4 v1, s[44:47], s62 offen lds\n"
    "s_add_u32 m0, s61, 73728\n"
    "s_add_u32 s62, s60, 2899968\n"
    "buffer_load_dwordx4 v1, s[44:47], s62 offen lds\n"
    "s_add_u32 m0, s61, 77824\n"
    "s_add_u32 s62, s60, 3948544\n"
    "buffer_load_dwordx4 v1, s[44:47], s62 offen lds\n"
    "v_add_u32 v119, 0x10000, v3\n"
    "ds_read_b64_tr_b16 v[208:209], v119 offset:16384\n"
    "ds_read_b64_tr_b16 v[210:211], v119 offset:20480\n"
    "ds_read_b64_tr_b16 v[212:213], v119 offset:24576\n"
    "ds_read_b64_tr_b16 v[214:215], v119 offset:28672\n"
    "v_add_u32 v118, 0x10000, v4\n"
    "ds_read_b64_tr_b16 v[216:217], v118 offset:16384\n"
    "ds_read_b64_tr_b16 v[218:219], v118 offset:20480\n"
    "ds_read_b64_tr_b16 v[220:221], v118 offset:24576\n"
    "ds_read_b64_tr_b16 v[222:223], v118 offset:28672\n"
    "s_waitcnt vmcnt(28) lgkmcnt(0)\n"
    "s_barrier\n"
    "s_cbranch_vccz .Lsl13_%=\n"
    "s_sleep 10\n"
    ".Lsl13_%=:\n"
    "s_add_u32 m0, s61, 81920\n"
    "s_add_u32 s62, s60, 4194304\n"
    "buffer_load_dwordx4 v1, s[44:47], s62 offen lds\n"
    "s_add_u32 m0, s61, 86016\n"
    "s_add_u32 s62, s60, 4210688\n"
    "buffer_load_dwordx4 v1, s[44:47], s62 offen lds\n"
    "s_add_u32 m0, s61, 90112\n"
    "s_add_u32 s62, s60, 4456448\n"
    "buffer_load_dwordx4 v1, s[44:47], s62 offen lds\n"
    "s_add_u32 m0, s61, 94208\n"
    "s_add_u32 s62, s60, 4472832\n"
    "buffer_load_dwordx4 v1, s[44:47], s62 offen lds\n"
    "ds_read_b64_tr_b16 v[224:225], v127 offset:32768\n"
    "ds_read_b64_tr_b16 a[0:1], v126 offset:32768\n"
    "ds_read_b64_tr_b16 a[32:33], v125 offset:32768\n"
    "ds_read_b64_tr_b16 a[64:65], v124 offset:32768\n"
    "ds_read_b64_tr_b16 v[232:233], v123 offset:32768\n"
    "ds_read_b64_tr_b16 a[8:9], v122 offset:32768\n"
    "ds_read_b64_tr_b16 a[40:41], v121 offset:32768\n"
    "ds_read_b64_tr_b16 a[72:73], v120 offset:32768\n"
    "s_waitcnt vmcnt(28) lgkmcnt(0)\n"
    "s_barrier\n"
    "s_cbranch_vccz .Lsl14_%=\n"
    "s_sleep 10\n"
    ".Lsl14_%=:\n"
    "s_mov_b64 s[44:45], %10\n"
    "s_and_b32 s45, s45, 0xffff\n"
    "s_mov_b32 s46, 0x480000\n"
    "s_mov_b32 s47, 0x20000\n"
    "s_add_u32 m0, s61, 98304\n"
    "s_add_u32 s62, s60, 0\n"
    "buffer_load_dwordx4 v1, s[44:47], s62 offen lds\n"
    "s_add_u32 m0, s61, 102400\n"
    "s_add_u32 s62, s60, 1048576\n"
    "buffer_load_dwordx4 v1, s[44:47], s62 offen lds\n"
    "s_add_u32 m0, s61, 106496\n"
    "s_add_u32 s62, s60, 2097152\n"
    "buffer_load_dwordx4 v1, s[44:47], s62 offen lds\n"
    "s_add_u32 m0, s61, 110592\n"
    "s_add_u32 s62, s60, 3145728\n"
    "buffer_load_dwordx4 v1, s[44:47], s62 offen lds\n"
    "ds_read_b64_tr_b16 v[240:241], v127 offset:49152\n"
    "ds_read_b64_tr_b16 a[16:17], v126 offset:49152\n"
    "ds_read_b64_tr_b16 a[48:49], v125 offset:49152\n"
    "ds_read_b64_tr_b16 a[80:81], v124 offset:49152\n"
    "ds_read_b64_tr_b16 v[248:249], v123 offset:49152\n"
    "ds_read_b64_tr_b16 a[24:25], v122 offset:49152\n"
    "ds_read_b64_tr_b16 a[56:57], v121 offset:49152\n"
    "ds_read_b64_tr_b16 a[88:89], v120 offset:49152\n"
    "s_waitcnt vmcnt(28) lgkmcnt(0)\n"
    "s_barrier\n"
    "s_cbranch_vccz .Lsl15_%=\n"
    "s_sleep 10\n"
    ".Lsl15_%=:\n"
    "s_add_u32 m0, s61, 114688\n"
    "s_add_u32 s62, s60, 524288\n"
    "buffer_load_dwordx4 v1, s[44:47], s62 offen lds\n"
    "s_add_u32 m0, s61, 118784\n"
    "s_add_u32 s62, s60, 1572864\n"
    "buffer_load_dwordx4 v1, s[44:47], s62 offen lds\n"
    "s_add_u32 m0, s61, 122880\n"
    "s_add_u32 s62, s60, 2621440\n"
    "buffer_load_dwordx4 v1, s[44:47], s62 offen lds\n"
    "s_add_u32 m0, s61, 126976\n"
    "s_add_u32 s62, s60, 3670016\n"
    "buffer_load_dwordx4 v1, s[44:47], s62 offen lds\n"
    "v_add_u32 v117, 0x20000, v96\n"
    "ds_read_b64_tr_b16 v[226:227], v117\n"
    "v_add_u32 v116, 0x20000, v98\n"
    "ds_read_b64_tr_b16 a[2:3], v116\n"
    "v_add_u32 v115, 0x20000, v100\n"
    "ds_read_b64_tr_b16 a[34:35], v115\n"
    "v_add_u32 v114, 0x20000, v102\n"
    "ds_read_b64_tr_b16 a[66:67], v114\n"
    "v_add_u32 v113, 0x20000, v97\n"
    "ds_read_b64_tr_b16 v[234:235], v113\n"
    "v_add_u32 v112, 0x20000, v99\n"
    "ds_read_b64_tr_b16 a[10:11], v112\n"
    "v_add_u32 v111, 0x20000, v101\n"
    "ds_read_b64_tr_b16 a[42:43], v111\n"
    "v_add_u32 v110, 0x20000, v103\n"
    "ds_read_b64_tr_b16 a[74:75], v110\n"
    "s_waitcnt vmcnt(28) lgkmcnt(0)\n"
    "s_barrier\n"
    "s_cbranch_vccz .Lsl16_%=\n"
    "s_sleep 10\n"
    ".Lsl16_%=:\n"
    "s_add_u32 m0, s61, 131072\n"
    "s_add_u32 s62, s60, 16384\n"
    "buffer_load_dwordx4 v1, s[44:47], s62 offen lds\n"
    "s_add_u32 m0, s61, 135168\n"
    "s_add_u32 s62, s60, 1064960\n"
    "buffer_load_dwordx4 v1, s[44:47], s62 offen lds\n"
    "s_add_u32 m0, s61, 139264\n"
    "s_add_u32 s62, s60, 2113536\n"
    "buffer_load_dwordx4 v1, s[44:47], s62 offen lds\n"
    "s_add_u32 m0, s61, 143360\n"
    "s_add_u32 s62, s60, 3162112\n"
    "buffer_load_dwordx4 v1, s[44:47], s62 offen lds\n"
    "ds_read_b64_tr_b16 v[242:243], v117 offset:16384\n"
    "ds_read_b64_tr_b16 a[18:19], v116 offset:16384\n"
    "ds_read_b64_tr_b16 a[50:51], v115 offset:16384\n"
    "ds_read_b64_tr_b16 a[82:83], v114 offset:16384\n"
    "ds_read_b64_tr_b16 v[250:251], v113 offset:16384\n"
    "ds_read_b64_tr_b16 a[26:27], v112 offset:16384\n"
    "ds_read_b64_tr_b16 a[58:59], v111 offset:16384\n"
    "ds_read_b64_tr_b16 a[90:91], v110 offset:16384\n"
    "s_waitcnt vmcnt(28) lgkmcnt(0)\n"
    "s_barrier\n"
    "s_cbranch_vccz .Lsl17_%=\n"
    "s_sleep 10\n"
    ".Lsl17_%=:\n"
    "s_add_u32 m0, s61, 147456\n"
    "s_add_u32 s62, s60, 540672\n"
    "buffer_load_dwordx4 v1, s[44:47], s62 offen lds\n"
    "s_add_u32 m0, s61, 151552\n"
    "s_add_u32 s62, s60, 1589248\n"
    "buffer_load_dwordx4 v1, s[44:47], s62 offen lds\n"
    "s_add_u32 m0, s61, 155648\n"
    "s_add_u32 s62, s60, 2637824\n"
    "buffer_load_dwordx4 v1, s[44:47], s62 offen lds\n"
    "s_add_u32 m0, s61, 159744\n"
    "s_add_u32 s62, s60, 3686400\n"
    "buffer_load_dwordx4 v1, s[44:47], s62 offen lds\n"
    "ds_read_b64_tr_b16 v[228:229], v96 offset:16384\n"
    "ds_read_b64_tr_b16 a[4:5], v98 offset:16384\n"
    "ds_read_b64_tr_b16 a[36:37], v100 offset:16384\n"
    "ds_read_b64_tr_b16 a[68:69], v102 offset:16384\n"
    "ds_read_b64_tr_b16 v[236:237], v97 offset:16384\n"
    "ds_read_b64_tr_b16 a[12:13], v99 offset:16384\n"
    "ds_read_b64_tr_b16 a[44:45], v101 offset:16384\n"
    "ds_read_b64_tr_b16 a[76:77], v103 offset:16384\n"
    "s_waitcnt vmcnt(28) lgkmcnt(0)\n"
    "s_barrier\n"
    "s_cbranch_vccz .Lsl18_%=\n"
    "s_sleep 10\n"
    ".Lsl18_%=:\n"
    "s_add_u32 m0, s61, 16384\n"
    "s_add_u32 s62, s60, 262144\n"
    "buffer_load_dwordx4 v1, s[44:47], s62 offen lds\n"
    "s_add_u32 m0, s61, 20480\n"
    "s_add_u32 s62, s60, 1310720\n"
    "buffer_load_dwordx4 v1, s[44:47], s62 offen lds\n"
    "s_add_u32 m0, s61, 24576\n"
    "s_add_u32 s62, s60, 2359296\n"
    "buffer_load_dwordx4 v1, s[44:47], s62 offen lds\n"
    "s_add_u32 m0, s61, 28672\n"
    "s_add_u32 s62, s60, 3407872\n"
    "buffer_load_dwordx4 v1, s[44:47], s62 offen lds\n"
    "ds_read_b64_tr_b16 v[244:245], v96 offset:32768\n"
    "ds_read_b64_tr_b16 a[20:21], v98 offset:32768\n"
    "ds_read_b64_tr_b16 a[52:53], v100 offset:32768\n"
    "ds_read_b64_tr_b16 a[84:85], v102 offset:32768\n"
    "ds_read_b64_tr_b16 v[252:253], v97 offset:32768\n"
    "ds_read_b64_tr_b16 a[28:29], v99 offset:32768\n"
    "ds_read_b64_tr_b16 a[60:61], v101 offset:32768\n"
    "ds_read_b64_tr_b16 a[92:93], v103 offset:32768\n"
    "s_waitcnt vmcnt(28) lgkmcnt(0)\n"
    "s_barrier\n"
    "s_cbranch_vccz .Lsl19_%=\n"
    "s_sleep 10\n"
    ".Lsl19_%=:\n"
    "s_add_u32 m0, s61, 32768\n"
    "s_add_u32 s62, s60, 786432\n"
    "buffer_load_dwordx4 v1, s[44:47], s62 offen lds\n"
    "s_add_u32 m0, s61, 36864\n"
    "s_add_u32 s62, s60, 1835008\n"
    "buffer_load_dwordx4 v1, s[44:47], s62 offen lds\n"
    "s_add_u32 m0, s61, 40960\n"
    "s_add_u32 s62, s60, 2883584\n"
    "buffer_load_dwordx4 v1, s[44:47], s62 offen lds\n"
    "s_add_u32 m0, s61, 45056\n"
    "s_add_u32 s62, s60, 3932160\n"
    "buffer_load_dwordx4 v1, s[44:47], s62 offen lds\n"
    "ds_read_b64_tr_b16 v[230:231], v96 offset:49152\n"
    "ds_read_b64_tr_b16 a[6:7], v98 offset:49152\n"
    "ds_read_b64_tr_b16 a[38:39], v100 offset:49152\n"
    "ds_read_b64_tr_b16 a[70:71], v102 offset:49152\n"
    "ds_read_b64_tr_b16 v[238:239], v97 offset:49152\n"
    "ds_read_b64_tr_b16 a[14:15], v99 offset:49152\n"
    "ds_read_b64_tr_b16 a[46:47], v101 offset:49152\n"
    "ds_read_b64_tr_b16 a[78:79], v103 offset:49152\n"
    "s_waitcnt vmcnt(28) lgkmcnt(0)\n"
    "s_barrier\n"
    "s_cbranch_vccz .Lsl20_%=\n"
    "s_sleep 10\n"
    ".Lsl20_%=:\n"
    "s_add_u32 m0, s61, 49152\n"
    "s_add_u32 s62, s60, 278528\n"
    "buffer_load_dwordx4 v1, s[44:47], s62 offen lds\n"
    "s_add_u32 m0, s61, 53248\n"
    "s_add_u32 s62, s60, 1327104\n"
    "buffer_load_dwordx4 v1, s[44:47], s62 offen lds\n"
    "s_add_u32 m0, s61, 57344\n"
    "s_add_u32 s62, s60, 2375680\n"
    "buffer_load_dwordx4 v1, s[44:47], s62 offen lds\n"
    "s_add_u32 m0, s61, 61440\n"
    "s_add_u32 s62, s60, 3424256\n"
    "buffer_load_dwordx4 v1, s[44:47], s62 offen lds\n"
    "ds_read_b64_tr_b16 v[246:247], v127\n"
    "ds_read_b64_tr_b16 a[22:23], v126\n"
    "ds_read_b64_tr_b16 a[54:55], v125\n"
    "ds_read_b64_tr_b16 a[86:87], v124\n"
    "ds_read_b64_tr_b16 v[254:255], v123\n"
    "ds_read_b64_tr_b16 a[30:31], v122\n"
    "ds_read_b64_tr_b16 a[62:63], v121\n"
    "ds_read_b64_tr_b16 a[94:95], v120\n"
    "s_waitcnt vmcnt(28) lgkmcnt(0)\n"
    "s_barrier\n"
    "s_cbranch_vccz .Lsl21_%=\n"
    "s_sleep 10\n"
    ".Lsl21_%=:\n"
    "s_add_u32 m0, s61, 65536\n"
    "s_add_u32 s62, s60, 802816\n"
    "buffer_load_dwordx4 v1, s[44:47], s62 offen lds\n"
    "s_add_u32 m0, s61, 69632\n"
    "s_add_u32 s62, s60, 1851392\n"
    "buffer_load_dwordx4 v1, s[44:47], s62 offen lds\n"
    "s_add_u32 m0, s61, 73728\n"
    "s_add_u32 s62, s60, 2899968\n"
    "buffer_load_dwordx4 v1, s[44:47], s62 offen lds\n"
    "s_add_u32 m0, s61, 77824\n"
    "s_add_u32 s62, s60, 3948544\n"
    "buffer_load_dwordx4 v1, s[44:47], s62 offen lds\n"
    "ds_read_b64_tr_b16 a[96:97], v119 offset:16384\n"
    "ds_read_b64_tr_b16 a[98:99], v119 offset:20480\n"
    "ds_read_b64_tr_b16 a[100:101], v119 offset:24576\n"
    "ds_read_b64_tr_b16 a[102:103], v119 offset:28672\n"
    "ds_read_b64_tr_b16 a[104:105], v118 offset:16384\n"
    "ds_read_b64_tr_b16 a[106:107], v118 offset:20480\n"
    "ds_read_b64_tr_b16 a[108:109], v118 offset:24576\n"
    "ds_read_b64_tr_b16 a[110:111], v118 offset:28672\n"
    "s_waitcnt vmcnt(28) lgkmcnt(0)\n"
    "s_barrier\n"
    "s_cbranch_vccz .Lsl22_%=\n"
    "s_sleep 10\n"
    ".Lsl22_%=:\n"
    "s_add_u32 m0, s61, 81920\n"
    "s_add_u32 s62, s60, 4194304\n"
    "buffer_load_dwordx4 v1, s[44:47], s62 offen lds\n"
    "s_add_u32 m0, s61, 86016\n"
    "s_add_u32 s62, s60, 4210688\n"
    "buffer_load_dwordx4 v1, s[44:47], s62 offen lds\n"
    "s_add_u32 m0, s61, 90112\n"
    "s_add_u32 s62, s60, 4456448\n"
    "buffer_load_dwordx4 v1, s[44:47], s62 offen lds\n"
    "s_add_u32 m0, s61, 94208\n"
    "s_add_u32 s62, s60, 4472832\n"
    "buffer_load_dwordx4 v1, s[44:47], s62 offen lds\n"
    "ds_read_b64_tr_b16 a[112:113], v127 offset:32768\n"
    "ds_read_b64_tr_b16 a[144:145], v126 offset:32768\n"
    "ds_read_b64_tr_b16 a[176:177], v125 offset:32768\n"
    "ds_read_b64_tr_b16 a[208:209], v124 offset:32768\n"
    "ds_read_b64_tr_b16 a[120:121], v123 offset:32768\n"
    "ds_read_b64_tr_b16 a[152:153], v122 offset:32768\n"
    "ds_read_b64_tr_b16 a[184:185], v121 offset:32768\n"
    "ds_read_b64_tr_b16 a[216:217], v120 offset:32768\n"
    "s_waitcnt vmcnt(28) lgkmcnt(0)\n"
    "s_barrier\n"
    "s_cbranch_vccz .Lsl23_%=\n"
    "s_sleep 10\n"
    ".Lsl23_%=:\n"
    "s_mov_b64 s[44:45], %8\n"
    "s_and_b32 s45, s45, 0xffff\n"
    "s_mov_b32 s46, 0x480000\n"
    "s_mov_b32 s47, 0x20000\n"
    "s_add_u32 m0, s61, 98304\n"
    "s_add_u32 s62, s60, 0\n"
    "buffer_load_dwordx4 v1, s[44:47], s62 offen lds\n"
    "s_add_u32 m0, s61, 102400\n"
    "s_add_u32 s62, s60, 1048576\n"
    "buffer_load_dwordx4 v1, s[44:47], s62 offen lds\n"
    "s_add_u32 m0, s61, 106496\n"
    "s_add_u32 s62, s60, 2097152\n"
    "buffer_load_dwordx4 v1, s[44:47], s62 offen lds\n"
    "s_add_u32 m0, s61, 110592\n"
    "s_add_u32 s62, s60, 3145728\n"
    "buffer_load_dwordx4 v1, s[44:47], s62 offen lds\n"
    "ds_read_b64_tr_b16 a[128:129], v127 offset:49152\n"
    "ds_read_b64_tr_b16 a[160:161], v126 offset:49152\n"
    "ds_read_b64_tr_b16 a[192:193], v125 offset:49152\n"
    "ds_read_b64_tr_b16 a[224:225], v124 offset:49152\n"
    "ds_read_b64_tr_b16 a[136:137], v123 offset:49152\n"
    "ds_read_b64_tr_b16 a[168:169], v122 offset:49152\n"
    "ds_read_b64_tr_b16 a[200:201], v121 offset:49152\n"
    "ds_read_b64_tr_b16 a[232:233], v120 offset:49152\n"
    "s_waitcnt vmcnt(28) lgkmcnt(0)\n"
    "s_barrier\n"
    "s_cbranch_vccz .Lsl24_%=\n"
    "s_sleep 10\n"
    ".Lsl24_%=:\n"
    "s_add_u32 m0, s61, 114688\n"
    "s_add_u32 s62, s60, 524288\n"
    "buffer_load_dwordx4 v1, s[44:47], s62 offen lds\n"
    "s_add_u32 m0, s61, 118784\n"
    "s_add_u32 s62, s60, 1572864\n"
    "buffer_load_dwordx4 v1, s[44:47], s62 offen lds\n"
    "s_add_u32 m0, s61, 122880\n"
    "s_add_u32 s62, s60, 2621440\n"
    "buffer_load_dwordx4 v1, s[44:47], s62 offen lds\n"
    "s_add_u32 m0, s61, 126976\n"
    "s_add_u32 s62, s60, 3670016\n"
    "buffer_load_dwordx4 v1, s[44:47], s62 offen lds\n"
    "ds_read_b64_tr_b16 a[114:115], v117\n"
    "ds_read_b64_tr_b16 a[146:147], v116\n"
    "ds_read_b64_tr_b16 a[178:179], v115\n"
    "ds_read_b64_tr_b16 a[210:211], v114\n"
    "ds_read_b64_tr_b16 a[122:123], v113\n"
    "ds_read_b64_tr_b16 a[154:155], v112\n"
    "ds_read_b64_tr_b16 a[186:187], v111\n"
    "ds_read_b64_tr_b16 a[218:219], v110\n"
    "s_waitcnt vmcnt(28) lgkmcnt(0)\n"
    "s_barrier\n"
    "s_cbranch_vccz .Lsl25_%=\n"
    "s_sleep 10\n"
    ".Lsl25_%=:\n"
    "s_add_u32 m0, s61, 131072\n"
    "s_add_u32 s62, s60, 16384\n"
    "buffer_load_dwordx4 v1, s[44:47], s62 offen lds\n"
    "s_add_u32 m0, s61, 135168\n"
    "s_add_u32 s62, s60, 1064960\n"
    "buffer_load_dwordx4 v1, s[44:47], s62 offen lds\n"
    "s_add_u32 m0, s61, 139264\n"
    "s_add_u32 s62, s60, 2113536\n"
    "buffer_load_dwordx4 v1, s[44:47], s62 offen lds\n"
    "s_add_u32 m0, s61, 143360\n"
    "s_add_u32 s62, s60, 3162112\n"
    "buffer_load_dwordx4 v1, s[44:47], s62 offen lds\n"
    "ds_read_b64_tr_b16 a[130:131], v117 offset:16384\n"
    "ds_read_b64_tr_b16 a[162:163], v116 offset:16384\n"
    "ds_read_b64_tr_b16 a[194:195], v115 offset:16384\n"
    "ds_read_b64_tr_b16 a[226:227], v114 offset:16384\n"
    "ds_read_b64_tr_b16 a[138:139], v113 offset:16384\n"
    "ds_read_b64_tr_b16 a[170:171], v112 offset:16384\n"
    "ds_read_b64_tr_b16 a[202:203], v111 offset:16384\n"
    "ds_read_b64_tr_b16 a[234:235], v110 offset:16384\n"
    "s_waitcnt vmcnt(28) lgkmcnt(0)\n"
    "s_barrier\n"
    "s_cbranch_vccz .Lsl26_%=\n"
    "s_sleep 10\n"
    ".Lsl26_%=:\n"
    "s_add_u32 m0, s61, 147456\n"
    "s_add_u32 s62, s60, 540672\n"
    "buffer_load_dwordx4 v1, s[44:47], s62 offen lds\n"
    "s_add_u32 m0, s61, 151552\n"
    "s_add_u32 s62, s60, 1589248\n"
    "buffer_load_dwordx4 v1, s[44:47], s62 offen lds\n"
    "s_add_u32 m0, s61, 155648\n"
    "s_add_u32 s62, s60, 2637824\n"
    "buffer_load_dwordx4 v1, s[44:47], s62 offen lds\n"
    "s_add_u32 m0, s61, 159744\n"
    "s_add_u32 s62, s60, 3686400\n"
    "buffer_load_dwordx4 v1, s[44:47], s62 offen lds\n"
    "ds_read_b64_tr_b16 a[116:117], v96 offset:16384\n"
    "ds_read_b64_tr_b16 a[148:149], v98 offset:16384\n"
    "ds_read_b64_tr_b16 a[180:181], v100 offset:16384\n"
    "ds_read_b64_tr_b16 a[212:213], v102 offset:16384\n"
    "ds_read_b64_tr_b16 a[124:125], v97 offset:16384\n"
    "ds_read_b64_tr_b16 a[156:157], v99 offset:16384\n"
    "ds_read_b64_tr_b16 a[188:189], v101 offset:16384\n"
    "ds_read_b64_tr_b16 a[220:221], v103 offset:16384\n"
    "s_waitcnt vmcnt(28) lgkmcnt(0)\n"
    "s_barrier\n"
    "s_cbranch_vccz .Lsl27_%=\n"
    "s_sleep 10\n"
    ".Lsl27_%=:\n"
    "s_mov_b64 s[44:45], %11\n"
    "s_and_b32 s45, s45, 0xffff\n"
    "s_mov_b32 s46, 0x480000\n"
    "s_mov_b32 s47, 0x20000\n"
    "s_add_u32 m0, s61, 16384\n"
    "s_add_u32 s62, s63, 0\n"
    "buffer_load_dwordx4 v5, s[44:47], s62 offen lds\n"
    "s_add_u32 m0, s61, 20480\n"
    "s_add_u32 s62, s63, 9216\n"
    "buffer_load_dwordx4 v5, s[44:47], s62 offen lds\n"
    "s_add_u32 m0, s61, 24576\n"
    "s_add_u32 s62, s63, 18432\n"
    "buffer_load_dwordx4 v5, s[44:47], s62 offen lds\n"
    "s_add_u32 m0, s61, 28672\n"
    "s_add_u32 s62, s63, 27648\n"
    "buffer_load_dwordx4 v5, s[44:47], s62 offen lds\n"
    "ds_read_b64_tr_b16 a[132:133], v96 offset:32768\n"
    "ds_read_b64_tr_b16 a[164:165], v98 offset:32768\n"
    "ds_read_b64_tr_b16 a[196:197], v100 offset:32768\n"
    "ds_read_b64_tr_b16 a[228:229], v102 offset:32768\n"
    "ds_read_b64_tr_b16 a[140:141], v97 offset:32768\n"
    "ds_read_b64_tr_b16 a[172:173], v99 offset:32768\n"
    "ds_read_b64_tr_b16 a[204:205], v101 offset:32768\n"
    "ds_read_b64_tr_b16 a[236:237], v103 offset:32768\n"
    "s_waitcnt vmcnt(28) lgkmcnt(0)\n"
    "s_barrier\n"
    "s_cbranch_vccz .Lsl28_%=\n"
    "s_sleep 10\n"
    ".Lsl28_%=:\n"
    "s_add_u32 m0, s61, 32768\n"
    "s_add_u32 s62, s63, 36864\n"
    "buffer_load_dwordx4 v5, s[44:47], s62 offen lds\n"
    "s_add_u32 m0, s61, 36864\n"
    "s_add_u32 s62, s63, 46080\n"
    "buffer_load_dwordx4 v5, s[44:47], s62 offen lds\n"
    "s_add_u32 m0, s61, 40960\n"
    "s_add_u32 s62, s63, 55296\n"
    "buffer_load_dwordx4 v5, s[44:47], s62 offen lds\n"
    "s_add_u32 m0, s61, 45056\n"
    "s_add_u32 s62, s63, 64512\n"
    "buffer_load_dwordx4 v5, s[44:47], s62 offen lds\n"
    "ds_read_b64_tr_b16 a[118:119], v96 offset:49152\n"
    "ds_read_b64_tr_b16 a[150:151], v98 offset:49152\n"
    "ds_read_b64_tr_b16 a[182:183], v100 offset:49152\n"
    "ds_read_b64_tr_b16 a[214:215], v102 offset:49152\n"
    "ds_read_b64_tr_b16 a[126:127], v97 offset:49152\n"
    "ds_read_b64_tr_b16 a[158:159], v99 offset:49152\n"
    "ds_read_b64_tr_b16 a[190:191], v101 offset:49152\n"
    "ds_read_b64_tr_b16 a[222:223], v103 offset:49152\n"
    "s_waitcnt vmcnt(28) lgkmcnt(0)\n"
    "s_barrier\n"
    "s_cbranch_vccz .Lsl29_%=\n"
    "s_sleep 10\n"
    ".Lsl29_%=:\n"
    "s_add_u32 m0, s61, 49152\n"
    "s_add_u32 s62, s63, 73728\n"
    "buffer_load_dwordx4 v5, s[44:47], s62 offen lds\n"
    "s_add_u32 m0, s61, 53248\n"
    "s_add_u32 s62, s63, 82944\n"
    "buffer_load_dwordx4 v5, s[44:47], s62 offen lds\n"
    "s_add_u32 m0, s61, 57344\n"
    "s_add_u32 s62, s63, 92160\n"
    "buffer_load_dwordx4 v5, s[44:47], s62 offen lds\n"
    "s_add_u32 m0, s61, 61440\n"
    "s_add_u32 s62, s63, 101376\n"
    "buffer_load_dwordx4 v5, s[44:47], s62 offen lds\n"
    "ds_read_b64_tr_b16 a[134:135], v127\n"
    "ds_read_b64_tr_b16 a[166:167], v126\n"
    "ds_read_b64_tr_b16 a[198:199], v125\n"
    "ds_read_b64_tr_b16 a[230:231], v124\n"
    "ds_read_b64_tr_b16 a[142:143], v123\n"
    "ds_read_b64_tr_b16 a[174:175], v122\n"
    "ds_read_b64_tr_b16 a[206:207], v121\n"
    "ds_read_b64_tr_b16 a[238:239], v120\n"
    "s_waitcnt vmcnt(28) lgkmcnt(0)\n"
    "s_barrier\n"
    "s_cbranch_vccz .Lsl30_%=\n"
    "s_sleep 10\n"
    ".Lsl30_%=:\n"
    "s_add_u32 m0, s61, 65536\n"
    "s_add_u32 s62, s63, 110592\n"
    "buffer_load_dwordx4 v5, s[44:47], s62 offen lds\n"
    "s_add_u32 m0, s61, 69632\n"
    "s_add_u32 s62, s63, 119808\n"
    "buffer_load_dwordx4 v5, s[44:47], s62 offen lds\n"
    "s_add_u32 m0, s61, 73728\n"
    "s_add_u32 s62, s63, 129024\n"
    "buffer_load_dwordx4 v5, s[44:47], s62 offen lds\n"
    "s_add_u32 m0, s61, 77824\n"
    "s_add_u32 s62, s63, 138240\n"
    "buffer_load_dwordx4 v5, s[44:47], s62 offen lds\n"
    "ds_read_b64_tr_b16 a[240:241], v119 offset:16384\n"
    "ds_read_b64_tr_b16 a[242:243], v119 offset:20480\n"
    "ds_read_b64_tr_b16 a[244:245], v119 offset:24576\n"
    "ds_read_b64_tr_b16 a[246:247], v119 offset:28672\n"
    "ds_read_b64_tr_b16 a[248:249], v118 offset:16384\n"
    "ds_read_b64_tr_b16 a[250:251], v118 offset:20480\n"
    "ds_read_b64_tr_b16 a[252:253], v118 offset:24576\n"
    "ds_read_b64_tr_b16 a[254:255], v118 offset:28672\n"
    "s_waitcnt vmcnt(28) lgkmcnt(0)\n"
    "s_barrier\n"
    "ds_read_b64_tr_b16 v[32:33], v127 offset:32768\n"
    "ds_read_b64_tr_b16 v[64:65], v126 offset:32768\n"
    "ds_read_b64_tr_b16 v[144:145], v125 offset:32768\n"
    "ds_read_b64_tr_b16 v[176:177], v124 offset:32768\n"
    "ds_read_b64_tr_b16 v[40:41], v123 offset:32768\n"
    "ds_read_b64_tr_b16 v[72:73], v122 offset:32768\n"
    "ds_read_b64_tr_b16 v[152:153], v121 offset:32768\n"
    "ds_read_b64_tr_b16 v[184:185], v120 offset:32768\n"
    "s_waitcnt vmcnt(24) lgkmcnt(0)\n"
    "s_barrier\n"
    "ds_read_b64_tr_b16 v[48:49], v127 offset:49152\n"
    "ds_read_b64_tr_b16 v[80:81], v126 offset:49152\n"
    "ds_read_b64_tr_b16 v[160:161], v125 offset:49152\n"
    "ds_read_b64_tr_b16 v[192:193], v124 offset:49152\n"
    "ds_read_b64_tr_b16 v[56:57], v123 offset:49152\n"
    "ds_read_b64_tr_b16 v[136:137], v122 offset:49152\n"
    "ds_read_b64_tr_b16 v[168:169], v121 offset:49152\n"
    "ds_read_b64_tr_b16 v[200:201], v120 offset:49152\n"
    "s_waitcnt vmcnt(20) lgkmcnt(0)\n"
    "s_barrier\n"
    "ds_read_b64_tr_b16 v[34:35], v117\n"
    "ds_read_b64_tr_b16 v[66:67], v116\n"
    "ds_read_b64_tr_b16 v[146:147], v115\n"
    "ds_read_b64_tr_b16 v[178:179], v114\n"
    "ds_read_b64_tr_b16 v[42:43], v113\n"
    "ds_read_b64_tr_b16 v[74:75], v112\n"
    "ds_read_b64_tr_b16 v[154:155], v111\n"
    "ds_read_b64_tr_b16 v[186:187], v110\n"
    "s_waitcnt vmcnt(16) lgkmcnt(0)\n"
    "s_barrier\n"
    "ds_read_b64_tr_b16 v[50:51], v117 offset:16384\n"
    "ds_read_b64_tr_b16 v[82:83], v116 offset:16384\n"
    "ds_read_b64_tr_b16 v[162:163], v115 offset:16384\n"
    "ds_read_b64_tr_b16 v[194:195], v114 offset:16384\n"
    "ds_read_b64_tr_b16 v[58:59], v113 offset:16384\n"
    "ds_read_b64_tr_b16 v[138:139], v112 offset:16384\n"
    "ds_read_b64_tr_b16 v[170:171], v111 offset:16384\n"
    "ds_read_b64_tr_b16 v[202:203], v110 offset:16384\n"
    "s_waitcnt vmcnt(12) lgkmcnt(0)\n"
    "s_barrier\n"
    "ds_read_b64_tr_b16 v[88:89], v6 offset:16384\n"
    "ds_read_b64_tr_b16 v[90:91], v6 offset:17408\n"
    "ds_read_b64_tr_b16 v[92:93], v6 offset:20480\n"
    "ds_read_b64_tr_b16 v[94:95], v6 offset:21504\n"
    "ds_read_b64_tr_b16 v[96:97], v6 offset:16416\n"
    "ds_read_b64_tr_b16 v[98:99], v6 offset:17440\n"
    "ds_read_b64_tr_b16 v[100:101], v6 offset:20512\n"
    "ds_read_b64_tr_b16 v[102:103], v6 offset:21536\n"
    "s_waitcnt vmcnt(8) lgkmcnt(0)\n"
    "s_barrier\n"
    "ds_read_b64_tr_b16 v[104:105], v6 offset:32768\n"
    "ds_read_b64_tr_b16 v[106:107], v6 offset:33792\n"
    "ds_read_b64_tr_b16 v[108:109], v6 offset:36864\n"
    "ds_read_b64_tr_b16 v[110:111], v6 offset:37888\n"
    "ds_read_b64_tr_b16 v[112:113], v6 offset:32800\n"
    "ds_read_b64_tr_b16 v[114:115], v6 offset:33824\n"
    "ds_read_b64_tr_b16 v[116:117], v6 offset:36896\n"
    "ds_read_b64_tr_b16 v[118:119], v6 offset:37920\n"
    "s_waitcnt vmcnt(4) lgkmcnt(0)\n"
    "s_barrier\n"
    "ds_read_b64_tr_b16 v[120:121], v6 offset:49152\n"
    "ds_read_b64_tr_b16 v[122:123], v6 offset:50176\n"
    "ds_read_b64_tr_b16 v[124:125], v6 offset:53248\n"
    "ds_read_b64_tr_b16 v[126:127], v6 offset:54272\n"
    "ds_read_b64_tr_b16 v[128:129], v6 offset:49184\n"
    "ds_read_b64_tr_b16 v[130:131], v6 offset:50208\n"
    "ds_read_b64_tr_b16 v[132:133], v6 offset:53280\n"
    "ds_read_b64_tr_b16 v[134:135], v6 offset:54304\n"
    "s_waitcnt vmcnt(0) lgkmcnt(0)\n"
    "s_barrier\n"
    "v_add_u32 v31, 0x10000, v6\n"
    "ds_read_b64_tr_b16 v[12:13], v31\n"
    "ds_read_b64_tr_b16 v[14:15], v31 offset:1024\n"
    "ds_read_b64_tr_b16 v[16:17], v31 offset:4096\n"
    "ds_read_b64_tr_b16 v[18:19], v31 offset:5120\n"
    "ds_read_b64_tr_b16 v[20:21], v31 offset:32\n"
    "ds_read_b64_tr_b16 v[22:23], v31 offset:1056\n"
    "ds_read_b64_tr_b16 v[24:25], v31 offset:4128\n"
    "ds_read_b64_tr_b16 v[26:27], v31 offset:5152\n"
    "s_waitcnt lgkmcnt(0)\n"
    "s_barrier\n"
    "s_xor_b32 s73, %2, 0\n"
    "s_lshl_b32 s73, s73, 12\n"
    "v_add_u32 v6, s73, v2\n"
    "ds_write_b128 v6, v[88:91] offset:14336\n"
    "ds_write_b128 v6, v[92:95] offset:15360\n"
    "ds_write_b128 v6, v[96:99] offset:16384\n"
    "ds_write_b128 v6, v[100:103] offset:17408\n"
    "s_xor_b32 s73, %2, 1\n"
    "s_lshl_b32 s73, s73, 12\n"
    "v_add_u32 v6, s73, v2\n"
    "ds_write_b128 v6, v[104:107] offset:14336\n"
    "ds_write_b128 v6, v[108:111] offset:15360\n"
    "ds_write_b128 v6, v[112:115] offset:16384\n"
    "ds_write_b128 v6, v[116:119] offset:17408\n"
    "s_xor_b32 s73, %2, 2\n"
    "s_lshl_b32 s73, s73, 12\n"
    "v_add_u32 v6, s73, v2\n"
    "ds_write_b128 v6, v[120:123] offset:14336\n"
    "ds_write_b128 v6, v[124:127] offset:15360\n"
    "ds_write_b128 v6, v[128:131] offset:16384\n"
    "ds_write_b128 v6, v[132:135] offset:17408\n"
    "s_xor_b32 s73, %2, 3\n"
    "s_lshl_b32 s73, s73, 12\n"
    "v_add_u32 v6, s73, v2\n"
    "ds_write_b128 v6, v[12:15] offset:14336\n"
    "ds_write_b128 v6, v[16:19] offset:15360\n"
    "ds_write_b128 v6, v[20:23] offset:16384\n"
    "ds_write_b128 v6, v[24:27] offset:17408\n"
    "ds_read_b64_tr_b16 v[88:89], v9\n"
    "ds_read_b64_tr_b16 v[90:91], v9 offset:128\n"
    "ds_read_b64_tr_b16 v[92:93], v9 offset:2048\n"
    "ds_read_b64_tr_b16 v[94:95], v9 offset:2176\n"
    "ds_read_b64_tr_b16 v[96:97], v9 offset:4096\n"
    "ds_read_b64_tr_b16 v[98:99], v9 offset:4224\n"
    "ds_read_b64_tr_b16 v[100:101], v9 offset:6144\n"
    "ds_read_b64_tr_b16 v[102:103], v9 offset:6272\n"
    "ds_write_b128 v2, v[32:35]\n"
    "ds_write_b128 v2, v[36:39] offset:1024\n"
    "ds_write_b128 v2, v[40:43] offset:2048\n"
    "ds_write_b128 v2, v[44:47] offset:3072\n"
    "ds_write_b128 v2, v[48:51] offset:4096\n"
    "ds_write_b128 v2, v[52:55] offset:5120\n"
    "ds_write_b128 v2, v[56:59] offset:6144\n"
    "ds_write_b128 v2, v[60:63] offset:7168\n"
    "ds_write_b128 v2, v[64:67] offset:8192\n"
    "ds_write_b128 v2, v[68:71] offset:9216\n"
    "ds_write_b128 v2, v[72:75] offset:10240\n"
    "ds_write_b128 v2, v[76:79] offset:11264\n"
    "ds_write_b128 v2, v[80:83] offset:12288\n"
    "ds_write_b128 v2, v[84:87] offset:13312\n"
    "v_lshrrev_b32 v112, 4, v0\n"
    "v_and_b32 v113, 15, v112\n"
    "v_lshrrev_b32 v114, 4, v112\n"
    "v_mov_b32 v11, 0\n"
    "v_lshrrev_b32 v115, 1, v113\n"
    "v_bfi_b32 v111, 1, v113, v115\n"
    "v_and_b32 v115, 1, v115\n"
    "v_lshl_add_u32 v115, v114, 1, v115\n"
    "v_mul_u32_u24 v117, 1152, v111\n"
    "v_lshl_add_u32 v117, v115, 4, v117\n"
    "v_and_b32 v115, 1, v114\n"
    "v_lshrrev_b32 v116, 1, v114\n"
    "v_mov_b32 v120, 0\n"
    "v_mov_b32 v121, 0\n"
    "v_mov_b32 v122, 0\n"
    "v_mov_b32 v123, 0\n"
    "ds_write_b128 v11, v[120:123] offset:31872\n"
    "v_mov_b32 v124, 0x3f803f80\n"
    "v_mov_b32 v125, 0x3f803f80\n"
    "v_mov_b32 v126, 0x3f803f80\n"
    "v_mov_b32 v127, 0x3f803f80\n"
    "ds_write_b128 v11, v[124:127] offset:31888\n"
    "v_mov_b32 v128, 0x3f003f00\n"
    "v_mov_b32 v129, 0x3f003f00\n"
    "v_mov_b32 v130, 0x3f003f00\n"
    "v_mov_b32 v131, 0x3f003f00\n"
    "ds_write_b128 v11, v[128:131] offset:31904\n"
    "v_mul_u32_u24 v118, 57, v112\n"
    "v_lshrrev_b32 v118, 9, v118\n"
    "v_mul_u32_u24 v118, 9, v118\n"
    "v_cmp_eq_u32 vcc, v118, v112\n"
    "v_lshlrev_b32 v118, 1, v112\n"
    "s_nop 0\n"
    "v_cndmask_b32 v120, v11, v124, vcc\n"
    "ds_write_b16 v118, v120 offset:31936\n"
    "s_waitcnt lgkmcnt(0)\n"
    "s_cmp_lt_u32 %2, 2\n"
    "s_cbranch_scc1 .Lsk_%=\n"
    "ds_write_b128 v2, v[88:91] offset:30720\n"
    "ds_write_b128 v2, v[92:95] offset:31744\n"
    "ds_write_b128 v2, v[96:99] offset:32768\n"
    "ds_write_b128 v2, v[100:103] offset:33792\n"
    ".Lsk_%=:\n"
    "s_cmp_eq_u32 %2, 3\n"
    "s_cbranch_scc1 .Lsm_%=\n"
    "s_mov_b64 s[44:45], %12\n"
    "s_and_b32 s45, s45, 0xffff\n"
    "s_mov_b32 s46, 0x2400\n"
    "s_mov_b32 s47, 0x20000\n"
    "s_mul_i32 s62, %2, 384\n"
    "s_mul_i32 s61, %2, 3072\n"
    "s_add_u32 m0, s61, 20480\n"
    "s_add_u32 s73, s62, 0\n"
    "buffer_load_dwordx4 v117, s[44:47], s73 offen lds\n"
    "s_add_u32 m0, s61, 21504\n"
    "s_add_u32 s73, s62, 128\n"
    "buffer_load_dwordx4 v117, s[44:47], s73 offen lds\n"
    "s_add_u32 m0, s61, 22528\n"
    "s_add_u32 s73, s62, 256\n"
    "buffer_load_dwordx4 v117, s[44:47], s73 offen lds\n"
    ".Lsm_%=:\n"
    "v_mov_b32 v130, 0x20400\n"
    "global_load_dwordx2 v[132:133], v130, %1 sc1\n"
    "v_mbcnt_lo_u32_b32 v131, -1, 0\n"
    "v_min_u32 v131, 7, v131\n"
    "v_lshlrev_b32 v131, 7, v131\n"
    "v_add_u32 v131, 0x20000, v131\n"
    "global_load_dwordx2 v[134:135], v131, %1 sc1\n"
    "s_waitcnt vmcnt(0) lgkmcnt(0)\n"
    "s_barrier\n"
    "v_mov_b32 v1, 0x4444\n"
    "v_mov_b32 v118, 0xeeee\n"
    "v_and_b32 v4, 2, v113\n"
    "v_cmp_eq_u32 vcc, 0, v4\n"
    "s_nop 1\n"
    "v_cndmask_b32 v1, v118, v1, vcc\n"
    "v_xor_b32 v118, 7, v111\n"
    "v_cndmask_b32 v4, v118, v111, vcc\n"
    "s_lshl_b32 s73, %2, 10\n"
    "v_add_u32 v5, s73, v0\n"
    "v_and_b32 v119, 7, v113\n"
    "v_lshrrev_b32 v120, 3, v113\n"
    "v_mul_u32_u24 v3, 544, v114\n"
    "v_lshl_add_u32 v3, v113, 2, v3\n"
    "s_lshl_b32 s73, %2, 6\n"
    "s_add_u32 s73, s73, 20480\n"
    "v_add_u32 v3, s73, v3\n"
    "v_mul_u32_u24 v4, 272, v4\n"
    "v_lshl_add_u32 v4, v114, 4, v4\n"
    "v_and_b32 v6, 13, v113\n"
    "v_lshl_add_u32 v6, v116, 4, v6\n"
    "v_lshl_add_u32 v6, v115, 1, v6\n"
    "s_lshl_b32 s73, %2, 9\n"
    "v_lshl_add_u32 v6, v6, 4, s73\n"
    "s_waitcnt vmcnt(0)\n"
    "v_add_u32 v132, 1, v132\n"
    "v_add_u32 v133, 1, v133\n"
    "s_mov_b32 s56, 1048576\n"
    ".Lcgo_a%=:\n"
    "v_cmp_ne_u64 vcc, v[132:133], v[134:135]\n"
    "s_cbranch_vccz .Lcgo_x%=\n"
    "global_load_dwordx2 v[134:135], v131, %1 sc1\n"
    "s_sub_u32 s56, s56, 1\n"
    "s_waitcnt vmcnt(0)\n"
    "s_cbranch_scc0 .Lcgo_a%=\n"
    ".Lcgo_x%=:\n"
    "s_mov_b32 s56, 0\n"
    "v_mov_b32 v134, 64\n"
    ".Lpoll1_%=:\n"
    "global_load_dword v135, v134, %1 sc1\n"
    "s_waitcnt vmcnt(0)\n"
    "v_readfirstlane_b32 s73, v135\n"
    "s_cmp_ge_u32 s73, 4\n"
    "s_cbranch_scc1 .Lpoll1_x%=\n"
    "s_add_u32 s56, s56, 1\n"
    "s_cmp_lt_u32 s56, 3\n"
    "s_cbranch_scc1 .Lpoll1_%=\n"
    "s_sleep 8\n"
    "s_cmp_lt_u32 s56, s57\n"
    "s_cbranch_scc1 .Lpoll1_%=\n"
    "s_mov_b32 s57, 0\n"
    "v_mov_b32 v135, 1\n"
    "global_store_dword v11, v135, %1 sc1\n"
    ".Lpoll1_x%=:\n"
    "s_xor_b32 s69, s73, %7\n"
    "s_lshr_b32 s51, s73, 1\n"
    "s_xor_b32 s51, s51, %7\n"
    "s_and_b32 s51, s51, 1\n"
    "s_lshl_b32 s51, s51, 13\n"
    "s_add_u32 s68, s51, 0x200000\n"
    "s_and_b32 s60, s69, 1\n"
    "s_lshl_b32 s60, s60, 13\n"
    "v_lshl_add_u32 v8, v116, 2, v115\n"
    "v_lshlrev_b32 v8, 12, v8\n"
    "v_lshl_add_u32 v8, v119, 4, v8\n"
    "v_lshl_add_u32 v7, v120, 15, v8\n"
    "v_lshl_add_u32 v8, v120, 30, v8\n"
    "s_mul_i32 s73, %5, 294912\n"
    "s_lshl_b32 s51, %4, 7\n"
    "s_add_u32 s73, s73, s51\n"
    "s_add_u32 s73, s73, s60\n"
    "s_lshl_b32 s51, %2, 16\n"
    "s_add_u32 s51, s51, s73\n"
    "v_add_u32 v7, s51, v7\n"
    "s_lshl3_add_u32 s51, %2, s73\n"
    "s_add_u32 s51, s51, 262128\n"
    "s_cmp_lt_u32 %2, 2\n"
    "s_cselect_b32 s51, 0x80000000, s51\n"
    "v_add_u32 v8, s51, v8\n"
    "s_lshl_b32 s61, %4, 1\n"
    "s_add_u32 s61, s61, %5\n"
    "s_lshl4_add_u32 s44, s61, 1024\n"
    "s_mul_i32 s61, %5, 36\n"
    "s_lshr_b32 s73, %4, 3\n"
    "s_add_u32 s73, s73, s61\n"
    "s_add_u32 s73, s73, 32\n"
    "s_add_u32 s61, s61, %4\n"
    "s_and_b32 s51, %4, 7\n"
    "s_add_u32 s51, s51, %2\n"
    "s_cmp_eq_u32 s51, 1\n"
    "s_cselect_b32 s71, s73, -1\n"
    "s_cmp_eq_u32 %2, 0\n"
    "s_cselect_b32 s71, s61, s71\n"
    "s_max_i32 s61, s71, 0\n"
    "s_lshr_b32 s73, s61, 1\n"
    "s_lshl_b32 s73, s73, 14\n"
    "s_and_b32 s51, s61, 1\n"
    "s_lshl_b32 s51, s51, 12\n"
    "s_add_u32 s65, s73, s51\n"
    "s_add_u32 s65, s65, s60\n"
    "s_lshr_b32 s94, s61, 5\n"
    "s_and_b32 s73, s61, 31\n"
    "s_lshl_b32 s73, s73, 7\n"
    "v_lshl_add_u32 v9, v114, 2, s73\n"
    "v_lshl_add_u32 v9, v119, 4, v9\n"
    "v_lshl_add_u32 v9, v120, 22, v9\n"
    "s_lshl_b32 s73, %2, 11\n"
    "v_add_u32 v10, s73, v0\n"
    "s_cmp_ge_u32 %2, 2\n"
    "s_cbranch_scc0 .Lnw3_%=\n"
    "v_lshl_add_u32 v9, v113, 1, %5\n"
    "v_lshlrev_b32 v118, 3, v114\n"
    "v_sub_u32 v9, v9, v118\n"
    "v_cmp_le_i32 s[76:77], 0, v9\n"
    "v_cmp_le_i32 s[78:79], 2, v9\n"
    "v_cmp_le_i32 s[80:81], 4, v9\n"
    "v_cmp_le_i32 s[82:83], 6, v9\n"
    "v_cmp_le_i32 s[84:85], 1, v9\n"
    "v_cmp_le_i32 s[86:87], 3, v9\n"
    "v_cmp_le_i32 s[88:89], 5, v9\n"
    "v_cmp_le_i32 s[90:91], 7, v9\n"
    ".Lnw3_%=:\n"
    "s_lshr_b32 s73, %4, 2\n"
    "s_lshl1_add_u32 s73, s73, %7\n"
    "s_and_b32 s51, %4, 3\n"
    "s_lshl3_add_u32 s73, s73, s51\n"
    "s_lshl_b32 s73, s73, 8\n"
    "s_add_u32 s92, s73, 86016\n"
    "s_lshr_b32 s51, %4, 3\n"
    "s_lshl2_add_u32 s73, %5, s51\n"
    "s_lshl1_add_u32 s73, s73, %7\n"
    "s_and_b32 s51, %4, 7\n"
    "s_lshl4_add_u32 s73, s73, s51\n"
    "s_lshl_b32 s73, s73, 7\n"
    "s_add_u32 s93, s73, 53248\n"
    "s_lshl_b32 s73, %5, 12\n"
    "s_lshl_b32 s51, %4, 14\n"
    "s_add_u32 s73, s73, s51\n"
    "s_add_u32 s75, s73, 0x9fff000\n"
    "s_brev_b32 s45, 1\n"
    "s_mov_b32 s46, 0x3f803f80\n"
    "s_mov_b32 s47, s46\n"
    "s_mov_b32 s48, 0\n"
    "s_mov_b32 s54, 56\n"
    "s_mov_b32 s55, 80\n"
    "s_mov_b32 s96, 672\n"
    "s_mov_b32 s56, 0\n"
    "v_mov_b32 v134, 128\n"
    ".Lpoll2_%=:\n"
    "global_load_dword v135, v134, %1 sc1\n"
    "s_waitcnt vmcnt(0)\n"
    "v_readfirstlane_b32 s73, v135\n"
    "s_cmp_ge_u32 s73, 64\n"
    "s_cbranch_scc1 .Lpoll2_x%=\n"
    "s_add_u32 s56, s56, 1\n"
    "s_cmp_lt_u32 s56, 3\n"
    "s_cbranch_scc1 .Lpoll2_%=\n"
    "s_sleep 8\n"
    "s_cmp_lt_u32 s56, s57\n"
    "s_cbranch_scc1 .Lpoll2_%=\n"
    "s_mov_b32 s57, 0\n"
    "v_mov_b32 v135, 1\n"
    "global_store_dword v11, v135, %1 sc1\n"
    ".Lpoll2_x%=:\n"
    "ds_read_b128 v[32:35], v2\n"
    "ds_read_b128 v[36:39], v2 offset:1024\n"
    "ds_read_b128 v[40:43], v2 offset:2048\n"
    "ds_read_b128 v[44:47], v2 offset:3072\n"
    "ds_read_b128 v[48:51], v2 offset:4096\n"
    "ds_read_b128 v[52:55], v2 offset:5120\n"
    "ds_read_b128 v[56:59], v2 offset:6144\n"
    "ds_read_b128 v[60:63], v2 offset:7168\n"
    "ds_read_b128 v[64:67], v2 offset:8192\n"
    "ds_read_b128 v[68:71], v2 offset:9216\n"
    "ds_read_b128 v[72:75], v2 offset:10240\n"
    "ds_read_b128 v[76:79], v2 offset:11264\n"
    "ds_read_b128 v[80:83], v2 offset:12288\n"
    "ds_read_b128 v[84:87], v2 offset:13312\n"
    "v_mov_b64 v[124:125], 0\n"
    "v_mov_b64 v[126:127], 0\n"
    "v_mov_b64 v[128:129], 0\n"
    "v_mov_b64 v[130:131], 0\n"
    "s_add_u32 s73, s48, 0\n"
    "s_and_b32 s73, s73, 63\n"
    "s_lshr_b32 s51, s73, 2\n"
    "s_xor_b32 s51, s51, s69\n"
    "s_and_b32 s51, s51, 1\n"
    "s_lshl_b32 s51, s51, 13\n"
    "s_lshl_b32 s73, s73, 5\n"
    "s_add_u32 s73, s73, %4\n"
    "s_lshl_b32 s73, s73, 14\n"
    "s_add_u32 s73, s73, s51\n"
    "s_add_u32 s60, s73, 0x6000000\n"
    "s_add_u32 s61, s73, 0x6001000\n"
    "s_add_u32 s62, s73, 0x8000000\n"
    "s_add_u32 s63, s73, 0x8001000\n"
    "s_lshl_b32 s51, %5, 12\n"
    "s_add_u32 s73, s73, s51\n"
    "s_add_u32 s64, s73, 0x4000000\n"
    "s_mul_i32 s73, s48, 3\n"
    "s_add_u32 s73, s73, 2\n"
    "s_and_b32 s51, s73, 1\n"
    "s_lshl_b32 s51, s51, 12\n"
    "s_lshr_b32 s73, s73, 1\n"
    "s_lshl_b32 s73, s73, 14\n"
    "s_add_u32 s73, s73, s51\n"
    "s_add_u32 s58, s73, s68\n"
    "s_waitcnt lgkmcnt(0)\n"
    "s_mov_b32 s56, 0\n"
    "v_mov_b32 v134, s92\n"
    ".Lpoll3_%=:\n"
    "global_load_dword v132, v134, %1 sc1\n"
    "global_load_dword v133, v134, %1 offset:128 sc1\n"
    "s_waitcnt vmcnt(0)\n"
    "v_and_b32 v133, v132, v133\n"
    "s_nop 0\n"
    "v_readfirstlane_b32 s73, v133\n"
    "s_cmp_ge_u32 s73, 1\n"
    "s_cbranch_scc1 .Lpoll3_x%=\n"
    "s_add_u32 s56, s56, 1\n"
    "s_cmp_lt_u32 s56, 3\n"
    "s_cbranch_scc1 .Lpoll3_%=\n"
    "s_sleep 8\n"
    "s_cmp_lt_u32 s56, s57\n"
    "s_cbranch_scc1 .Lpoll3_%=\n"
    "s_mov_b32 s57, 0\n"
    "v_mov_b32 v135, 1\n"
    "global_store_dword v11, v135, %1 sc1\n"
    ".Lpoll3_x%=:\n"
    "buffer_load_dwordx4 v[12:15], v5, s[40:43], s60 offen sc1\n"
    "buffer_load_dwordx4 v[16:19], v5, s[40:43], s61 offen sc1\n"
    "buffer_load_dwordx4 v[20:23], v5, s[40:43], s62 offen sc1\n"
    "buffer_load_dwordx4 v[24:27], v5, s[40:43], s63 offen sc1\n"
    "buffer_load_dwordx4 v[28:31], v5, s[40:43], s64 offen sc1\n"
    "v_xor_b32 v132, 0x800, v10\n"
    "v_xor_b32 v133, 0x1000, v10\n"
    "v_xor_b32 v134, 0x1800, v10\n"
    "v_lshrrev_b32 v135, 2, v5\n"
    "s_movk_i32 s73, 0x180\n"
    "v_lshlrev_b32 v120, 1, v0\n"
    "v_bfi_b32 v120, s73, v120, v0\n"
    "v_lshrrev_b32 v122, 2, v0\n"
    "v_bfi_b32 v120, 64, v122, v120\n"
    "v_lshlrev_b32 v121, 1, v135\n"
    "v_bfi_b32 v121, s73, v121, v135\n"
    "v_lshrrev_b32 v122, 2, v135\n"
    "v_bfi_b32 v121, 64, v122, v121\n"
    "ds_read_b128 v[88:91], v10 offset:20480\n"
    "ds_read_b128 v[92:95], v10 offset:21504\n"
    "ds_read_b32 v96, v135 offset:28672\n"
    "s_add_u32 s73, s48, 1\n"
    "s_and_b32 s73, s73, 63\n"
    "s_lshr_b32 s51, s73, 2\n"
    "s_xor_b32 s51, s51, s69\n"
    "s_and_b32 s51, s51, 1\n"
    "s_lshl_b32 s51, s51, 13\n"
    "s_lshl_b32 s73, s73, 5\n"
    "s_add_u32 s73, s73, %4\n"
    "s_lshl_b32 s73, s73, 14\n"
    "s_add_u32 s73, s73, s51\n"
    "s_add_u32 s60, s73, 0x6000000\n"
    "s_add_u32 s61, s73, 0x6001000\n"
    "s_add_u32 s62, s73, 0x8000000\n"
    "s_add_u32 s63, s73, 0x8001000\n"
    "s_lshl_b32 s51, %5, 12\n"
    "s_add_u32 s73, s73, s51\n"
    "s_add_u32 s64, s73, 0x4000000\n"
    "s_add_u32 s73, s48, 1\n"
    "s_and_b32 s51, s73, 32\n"
    "s_and_b32 s73, s73, 31\n"
    "s_lshl3_add_u32 s73, s51, s73\n"
    "s_lshl2_add_u32 s73, s73, s92\n"
    "s_waitcnt lgkmcnt(2)\n"
    "v_min_u32 v88, -2, v88\n"
    "v_min_u32 v89, -2, v89\n"
    "v_min_u32 v90, -2, v90\n"
    "v_min_u32 v91, -2, v91\n"
    "s_waitcnt lgkmcnt(1)\n"
    "v_min_u32 v92, -2, v92\n"
    "v_min_u32 v93, -2, v93\n"
    "v_min_u32 v94, -2, v94\n"
    "v_min_u32 v95, -2, v95\n"
    "s_waitcnt lgkmcnt(0)\n"
    "v_min_u32 v96, -2, v96\n"
    "s_waitcnt vmcnt(4)\n"
    "ds_write_b128 v5, v[12:15]\n"
    "s_waitcnt vmcnt(3)\n"
    "ds_write_b128 v5, v[16:19] offset:4096\n"
    "s_waitcnt vmcnt(0)\n"
    "ds_write_b128 v5, v[28:31] offset:16384\n"
    "v_swap_b32 v22, v24\n"
    "v_swap_b32 v23, v25\n"
    "ds_write_b128 v5, v[20:23] offset:8192\n"
    "ds_write_b128 v5, v[24:27] offset:12288\n"
    "v_mov_b32 v14, s73\n"
    "global_load_dword v12, v14, %1 sc1\n"
    "global_load_dword v13, v14, %1 offset:128 sc1\n"
    "global_load_dword v14, v14, %1 sc1\n"
    "v_mov_b64 v[16:17], 0\n"
    "v_mov_b64 v[18:19], 0\n"
    "v_mov_b64 v[20:21], 0\n"
    "v_mov_b64 v[22:23], 0\n"
    "v_mov_b64 v[24:25], 0\n"
    "v_mov_b64 v[26:27], 0\n"
    "v_mov_b64 v[28:29], 0\n"
    "v_mov_b64 v[30:31], 0\n"
    ".Lstep_%=:\n"
    "s_waitcnt vmcnt(5)\n"
    "v_smfmac_f32_16x16x64_bf16 v[16:19], v[88:91], v[32:39], v1\n"
    "ds_write_b128 v10, v[88:91] offset:20480\n"
    "v_smfmac_f32_16x16x64_bf16 v[20:23], v[88:91], v[40:47], v1\n"
    "s_waitcnt vmcnt(4)\n"
    "ds_write_b128 v10, v[92:95] offset:21504\n"
    "v_max3_u32 v100, v88, v89, v90\n"
    "v_smfmac_f32_16x16x64_bf16 v[16:19], v[92:95], v[48:55], v1\n"
    "s_waitcnt vmcnt(3)\n"
    "ds_write_b32 v135, v96 offset:28672\n"
    "v_max3_u32 v100, v100, v91, v92\n"
    "v_smfmac_f32_16x16x64_bf16 v[20:23], v[92:95], v[56:63], v1\n"
    "v_max3_u32 v100, v100, v93, v94\n"
    "v_smfmac_f32_16x16x64_bf16 v[24:27], v[88:91], v[224:231], v1\n"
    "v_max3_u32 v100, v100, v95, v96\n"
    "v_smfmac_f32_16x16x64_bf16 v[28:31], v[88:91], v[232:239], v1\n"
    "v_cmp_eq_u32 vcc, -1, v100\n"
    "v_smfmac_f32_16x16x64_bf16 v[24:27], v[92:95], v[240:247], v1\n"
    "s_cbranch_vccnz .Lgf_%=\n"
    ".Lgx_%=:\n"
    "s_waitcnt lgkmcnt(0)\n"
    "s_barrier\n"
    "s_memrealtime s[52:53]\n"
    "ds_read_b128 v[96:99], v132 offset:20480\n"
    "v_smfmac_f32_16x16x64_bf16 v[28:31], v[92:95], v[248:255], v1\n"
    "ds_read_b128 v[100:103], v132 offset:21504\n"
    "v_smfmac_f32_16x16x64_bf16 v[124:127], v[88:91], a[112:119], v1\n"
    "ds_read_b128 v[104:107], v133 offset:20480\n"
    "v_smfmac_f32_16x16x64_bf16 v[128:131], v[88:91], a[120:127], v1\n"
    "ds_read_b128 v[108:111], v133 offset:21504\n"
    "v_smfmac_f32_16x16x64_bf16 v[124:127], v[92:95], a[128:135], v1\n"
    "ds_read_b128 v[112:115], v134 offset:20480\n"
    "v_smfmac_f32_16x16x64_bf16 v[128:131], v[92:95], a[136:143], v1\n"
    "ds_read_b128 v[116:119], v134 offset:21504\n"
    "s_waitcnt lgkmcnt(5)\n"
    "v_smfmac_f32_16x16x64_bf16 v[16:19], v[96:99], v[64:71], v1\n"
    "ds_read_b128 v[120:123], v0 offset:28672\n"
    "v_smfmac_f32_16x16x64_bf16 v[20:23], v[96:99], v[72:79], v1\n"
    "s_waitcnt lgkmcnt(5)\n"
    "v_smfmac_f32_16x16x64_bf16 v[16:19], v[100:103], v[80:87], v1\n"
    "v_cmp_eq_u32 vcc, s44, v5\n"
    "v_smfmac_f32_16x16x64_bf16 v[20:23], v[100:103], v[136:143], v1\n"
    "v_mov_b32 v79, s45\n"
    "s_waitcnt lgkmcnt(4)\n"
    "v_smfmac_f32_16x16x64_bf16 v[16:19], v[104:107], v[144:151], v1\n"
    "v_cndmask_b32 v79, v79, v0, vcc\n"
    "v_smfmac_f32_16x16x64_bf16 v[20:23], v[104:107], v[152:159], v1\n"
    "s_lshr_b32 s73, s96, 3\n"
    "s_waitcnt lgkmcnt(3)\n"
    "v_smfmac_f32_16x16x64_bf16 v[16:19], v[108:111], v[160:167], v1\n"
    "s_waitcnt lgkmcnt(0)\n"
    "s_add_u32 s73, s73, s52\n"
    "v_and_b32 v78, -2, s73\n"
    "v_smfmac_f32_16x16x64_bf16 v[20:23], v[108:111], v[168:175], v1\n"
    "buffer_store_dword v78, v79, s[40:43], s58 offen offset:3072 sc1\n"
    "s_waitcnt vmcnt(2)\n"
    "v_smfmac_f32_16x16x64_bf16 v[16:19], v[112:115], v[176:183], v1\n"
    "ds_read_b128 v[40:43], v0\n"
    "v_smfmac_f32_16x16x64_bf16 v[20:23], v[112:115], v[184:191], v1\n"
    "ds_read_b128 v[44:47], v0 offset:4096\n"
    "v_and_b32 v12, v12, v13\n"
    "v_smfmac_f32_16x16x64_bf16 v[16:19], v[116:119], v[192:199], v1\n"
    "ds_read_b128 v[48:51], v0 offset:1024\n"
    "v_add_u32 v13, -1, v12\n"
    "v_smfmac_f32_16x16x64_bf16 v[20:23], v[116:119], v[200:207], v1\n"
    "ds_read_b128 v[52:55], v0 offset:5120\n"
    "v_and_or_b32 v15, v13, s45, v5\n"
    "v_smfmac_f32_16x16x64_bf16 v[16:19], v[120:123], v[208:215], v1\n"
    "ds_read_b128 v[56:59], v0 offset:2048\n"
    "v_readfirstlane_b32 s59, v12\n"
    "v_smfmac_f32_16x16x64_bf16 v[20:23], v[120:123], v[216:223], v1\n"
    "ds_read_b128 v[60:63], v0 offset:6144\n"
    "v_smfmac_f32_16x16x64_bf16 v[24:27], v[96:99], a[0:7], v1\n"
    "ds_read_b128 v[64:67], v0 offset:3072\n"
    "v_add_f32 v16, v16, v18\n"
    "v_smfmac_f32_16x16x64_bf16 v[28:31], v[96:99], a[8:15], v1\n"
    "ds_read_b128 v[68:71], v0 offset:7168\n"
    "v_add_f32 v17, v17, v19\n"
    "v_smfmac_f32_16x16x64_bf16 v[24:27], v[100:103], a[16:23], v1\n"
    "ds_read_b128 v[88:91], v2 offset:14336\n"
    "v_add_f32 v20, v20, v22\n"
    "v_smfmac_f32_16x16x64_bf16 v[28:31], v[100:103], a[24:31], v1\n"
    "ds_read_b128 v[92:95], v2 offset:15360\n"
    "v_add_f32 v21, v21, v23\n"
    "v_smfmac_f32_16x16x64_bf16 v[24:27], v[104:107], a[32:39], v1\n"
    "v_cvt_pk_bf16_f32 v76, v16, v20\n"
    "v_smfmac_f32_16x16x64_bf16 v[28:31], v[104:107], a[40:47], v1\n"
    "v_cvt_pk_bf16_f32 v77, v17, v21\n"
    "v_smfmac_f32_16x16x64_bf16 v[24:27], v[108:111], a[48:55], v1\n"
    "ds_write_b32 v3, v76 offset:9216\n"
    "v_smfmac_f32_16x16x64_bf16 v[28:31], v[108:111], a[56:63], v1\n"
    "ds_write_b32 v3, v77 offset:9488\n"
    "v_smfmac_f32_16x16x64_bf16 v[24:27], v[112:115], a[64:71], v1\n"
    "buffer_load_dwordx4 v[16:19], v15, s[40:43], s61 offen sc1\n"
    "v_smfmac_f32_16x16x64_bf16 v[28:31], v[112:115], a[72:79], v1\n"
    "v_smfmac_f32_16x16x64_bf16 v[24:27], v[116:119], a[80:87], v1\n"
    "v_smfmac_f32_16x16x64_bf16 v[28:31], v[116:119], a[88:95], v1\n"
    "s_waitcnt lgkmcnt(0)\n"
    "s_barrier\n"
    "ds_read_b128 v[32:35], v4 offset:29696\n"
    "v_smfmac_f32_16x16x64_bf16 v[24:27], v[120:123], a[96:103], v1\n"
    "ds_read_b128 v[36:39], v4 offset:29760\n"
    "v_smfmac_f32_16x16x64_bf16 v[28:31], v[120:123], a[104:111], v1\n"
    "ds_read_b128 v[132:135], v4 offset:29824\n"
    "v_smfmac_f32_16x16x64_bf16 v[124:127], v[96:99], a[144:151], v1\n"
    "ds_read_b128 v[72:75], v4 offset:29888\n"
    "v_add_f32 v24, v24, v26\n"
    "v_add_f32 v25, v25, v27\n"
    "v_smfmac_f32_16x16x64_bf16 v[128:131], v[96:99], a[152:159], v1\n"
    "buffer_load_dwordx4 v[20:23], v15, s[40:43], s62 offen sc1\n"
    "v_add_f32 v28, v28, v30\n"
    "v_add_f32 v29, v29, v31\n"
    "v_smfmac_f32_16x16x64_bf16 v[124:127], v[100:103], a[160:167], v1\n"
    "v_cvt_pk_bf16_f32 v76, v24, v28\n"
    "v_cvt_pk_bf16_f32 v77, v25, v29\n"
    "s_waitcnt lgkmcnt(3)\n"
    "v_mfma_f32_16x16x32_bf16 v[80:83], v[40:43], v[32:35], 0\n"
    "ds_write2_b32 v3, v76, v77 offset1:68\n"
    "v_mfma_f32_16x16x32_bf16 v[84:87], v[44:47], v[32:35], 0\n"
    "s_add_u32 s73, s48, 1\n"
    "s_and_b32 s51, s73, 32\n"
    "s_and_b32 s73, s73, 31\n"
    "s_waitcnt lgkmcnt(3)\n"
    "v_mfma_f32_16x16x32_bf16 v[80:83], v[48:51], v[36:39], v[80:83]\n"
    "buffer_load_dwordx4 v[24:27], v15, s[40:43], s63 offen sc1\n"
    "v_mfma_f32_16x16x32_bf16 v[84:87], v[52:55], v[36:39], v[84:87]\n"
    "s_lshl3_add_u32 s73, s51, s73\n"
    "s_lshl2_add_u32 s73, s73, s92\n"
    "s_waitcnt lgkmcnt(2)\n"
    "v_mfma_f32_16x16x32_bf16 v[80:83], v[56:59], v[132:135], v[80:83]\n"
    "s_mov_b32 vcc_lo, 0xcccccccc\n"
    "s_mov_b32 vcc_hi, 0xcccccccc\n"
    "v_mfma_f32_16x16x32_bf16 v[84:87], v[60:63], v[132:135], v[84:87]\n"
    "s_waitcnt lgkmcnt(0)\n"
    "s_barrier\n"
    "ds_read_b128 v[40:43], v4 offset:20480\n"
    "v_mfma_f32_16x16x32_bf16 v[80:83], v[64:67], v[72:75], v[80:83]\n"
    "ds_read_b128 v[44:47], v4 offset:20544\n"
    "v_mfma_f32_16x16x32_bf16 v[84:87], v[68:71], v[72:75], v[84:87]\n"
    "ds_read_b128 v[48:51], v4 offset:20608\n"
    "v_smfmac_f32_16x16x64_bf16 v[128:131], v[100:103], a[168:175], v1\n"
    "ds_read_b128 v[52:55], v4 offset:20672\n"
    "v_smfmac_f32_16x16x64_bf16 v[124:127], v[104:107], a[176:183], v1\n"
    "ds_read_b128 v[56:59], v5 offset:8192\n"
    "v_cndmask_b32 v80, v80, v84, vcc\n"
    "v_smfmac_f32_16x16x64_bf16 v[128:131], v[104:107], a[184:191], v1\n"
    "ds_read_b128 v[60:63], v5 offset:12288\n"
    "v_cndmask_b32 v81, v81, v85, vcc\n"
    "s_waitcnt lgkmcnt(5)\n"
    "v_mfma_f32_16x16x32_bf16 v[64:67], v[40:43], v[32:35], 0\n"
    "v_cndmask_b32 v82, v82, v86, vcc\n"
    "v_cndmask_b32 v83, v83, v87, vcc\n"
    "s_waitcnt lgkmcnt(4)\n"
    "v_mfma_f32_16x16x32_bf16 v[64:67], v[44:47], v[36:39], v[64:67]\n"
    "ds_read_b128 v[96:99], v2 offset:16384\n"
    "v_maximum3_f32 v78, v80, v81, v82\n"
    "v_maximum3_f32 v78, v78, v83, v83\n"
    "s_waitcnt lgkmcnt(4)\n"
    "v_mfma_f32_16x16x32_bf16 v[64:67], v[48:51], v[132:135], v[64:67]\n"
    "ds_read_b128 v[100:103], v2 offset:17408\n"
    "v_max_f32_dpp v78, v78, v78 row_mirror row_mask:0xf bank_mask:0xf\n"
    "s_waitcnt lgkmcnt(4)\n"
    "v_mfma_f32_16x16x32_bf16 v[64:67], v[52:55], v[72:75], v[64:67]\n"
    "buffer_load_dwordx4 v[28:31], v15, s[40:43], s64 offen sc1\n"
    "v_mov_b32 v77, v78\n"
    "v_smfmac_f32_16x16x64_bf16 v[124:127], v[108:111], a[192:199], v1\n"
    "ds_read_b128 v[40:43], v2 offset:18432\n"
    "v_permlane32_swap_b32 v77, v78\n"
    "v_smfmac_f32_16x16x64_bf16 v[128:131], v[108:111], a[200:207], v1\n"
    "ds_read_b128 v[44:47], v2 offset:19456\n"
    "v_maximum3_f32 v78, v78, v77, v64\n"
    "v_maximum3_f32 v78, v78, v65, v66\n"
    "v_max_f32 v77, v78, v67\n"
    "v_max_f32 v78, v78, v67\n"
    "v_smfmac_f32_16x16x64_bf16 v[124:127], v[112:115], a[208:215], v1\n"
    "ds_read_b128 v[48:51], v2 offset:22528\n"
    "v_permlane16_swap_b32 v77, v78\n"
    "v_max_f32 v78, v78, v77\n"
    "v_mul_f32 v78, 0xbe0293ee, v78\n"
    "v_smfmac_f32_16x16x64_bf16 v[128:131], v[112:115], a[216:223], v1\n"
    "ds_read_b128 v[52:55], v2 offset:23552\n"
    "v_fmamk_f32 v80, v80, 0x3e0293ee, v78\n"
    "v_fmamk_f32 v81, v81, 0x3e0293ee, v78\n"
    "v_exp_f32 v80, v80\n"
    "v_exp_f32 v81, v81\n"
    "v_smfmac_f32_16x16x64_bf16 v[124:127], v[116:119], a[224:231], v1\n"
    "ds_read_b128 v[32:35], v2 offset:24576\n"
    "v_fmamk_f32 v82, v82, 0x3e0293ee, v78\n"
    "v_fmamk_f32 v83, v83, 0x3e0293ee, v78\n"
    "v_exp_f32 v82, v82\n"
    "v_exp_f32 v83, v83\n"
    "v_cvt_pk_bf16_f32 v80, v80, v81\n"
    "v_smfmac_f32_16x16x64_bf16 v[128:131], v[116:119], a[232:239], v1\n"
    "ds_read_b128 v[36:39], v2 offset:25600\n"
    "v_fmamk_f32 v64, v64, 0x3e0293ee, v78\n"
    "v_fmamk_f32 v65, v65, 0x3e0293ee, v78\n"
    "v_exp_f32 v64, v64\n"
    "v_exp_f32 v65, v65\n"
    "v_cvt_pk_bf16_f32 v81, v82, v83\n"
    "v_smfmac_f32_16x16x64_bf16 v[124:127], v[120:123], a[240:247], v1\n"
    "ds_read_b128 v[104:107], v2 offset:26624\n"
    "v_cvt_pk_bf16_f32 v66, v64, v65\n"
    "v_mov_b32_dpp v82, v80 row_mirror row_mask:0xf bank_mask:0xf\n"
    "v_smfmac_f32_16x16x64_bf16 v[128:131], v[120:123], a[248:255], v1\n"
    "ds_read_b128 v[108:111], v2 offset:27648\n"
    "v_mov_b32_dpp v83, v81 row_mirror row_mask:0xf bank_mask:0xf\n"
    "v_cvt_pk_bf16_f32 v67, v64, v65\n"
    "s_waitcnt lgkmcnt(11)\n"
    "v_mfma_f32_16x16x32_bf16 v[132:135], v[56:59], v[80:83], 0\n"
    "ds_read_b128 v[112:115], v2 offset:20480\n"
    "v_cvt_pk_bf16_f32 v124, v124, v125\n"
    "v_cvt_pk_bf16_f32 v125, v126, v127\n"
    "s_waitcnt lgkmcnt(11)\n"
    "v_mfma_f32_16x16x32_bf16 v[120:123], v[60:63], v[80:83], 0\n"
    "ds_read_b128 v[116:119], v2 offset:21504\n"
    "v_cvt_pk_bf16_f32 v126, v128, v129\n"
    "v_cvt_pk_bf16_f32 v127, v130, v131\n"
    "v_mfma_f32_16x16x16_bf16 v[132:135], v[124:125], v[66:67], v[132:135]\n"
    "buffer_load_dwordx4 v[12:15], v15, s[40:43], s60 offen sc1\n"
    "ds_read_b128 v[68:71], v2 offset:28672\n"
    "s_nop 0\n"
    "v_mfma_f32_16x16x16_bf16 v[120:123], v[126:127], v[66:67], v[120:123]\n"
    "ds_read_b128 v[72:75], v2 offset:29696\n"
    "buffer_load_dword v79, v0, s[40:43], s58 offen offset:3072 sc1\n"
    "v_mov_b64 v[56:57], s[46:47]\n"
    "v_mov_b64 v[58:59], s[46:47]\n"
    "v_xor_b32 v76, 0x200, v6\n"
    "v_cvt_pk_bf16_f32 v84, v132, v133\n"
    "v_cvt_pk_bf16_f32 v85, v134, v135\n"
    "v_xor_b32 v77, 0x400, v6\n"
    "s_not_b64 exec, vcc\n"
    "v_cvt_pk_bf16_f32 v86, v120, v121\n"
    "v_cvt_pk_bf16_f32 v87, v122, v123\n"
    "ds_write_b128 v6, v[84:87] offset:26880\n"
    "s_mov_b64 exec, -1\n"
    "s_cmp_eq_u32 s48, 63\n"
    "v_mfma_f32_16x16x32_bf16 v[128:131], v[80:83], v[56:59], 0\n"
    "v_mov_b32 v67, 0\n"
    "v_mov_b64 v[132:133], 0\n"
    "v_mov_b64 v[134:135], 0\n"
    "v_mov_b32_dpp v84, v84 quad_perm:[0,1,0,1] row_mask:0xf bank_mask:0xf\n"
    "v_mov_b32_dpp v85, v85 quad_perm:[0,1,0,1] row_mask:0xf bank_mask:0xf\n"
    "v_mov_b32_dpp v86, v86 quad_perm:[0,1,0,1] row_mask:0xf bank_mask:0xf\n"
    "v_mov_b32_dpp v87, v87 quad_perm:[0,1,0,1] row_mask:0xf bank_mask:0xf\n"
    "v_mfma_f32_16x16x16_bf16 v[128:131], v[66:67], v[56:57], v[128:131]\n"
    "v_mov_b64 v[80:81], 0\n"
    "v_mov_b64 v[82:83], 0\n"
    "v_smfmac_f32_16x16x64_bf16 v[132:135], v[84:87], v[88:95], v1\n"
    "v_xor_b32 v78, 0x600, v6\n"
    "s_waitcnt lgkmcnt(13)\n"
    "v_smfmac_f32_16x16x64_bf16 v[80:83], v[84:87], v[96:103], v1\n"
    "s_waitcnt lgkmcnt(0)\n"
    "s_barrier\n"
    "s_cbranch_scc1 .Lend_%=\n"
    "ds_read_b128 v[56:59], v76 offset:26880\n"
    "ds_read_b128 v[60:63], v77 offset:26880\n"
    "ds_read_b128 v[120:123], v78 offset:26880\n"
    "ds_read_b128 v[88:91], v2 offset:30720\n"
    "v_rcp_f32 v128, v128\n"
    "v_rcp_f32 v129, v129\n"
    "s_waitcnt lgkmcnt(3)\n"
    "v_smfmac_f32_16x16x64_bf16 v[132:135], v[56:59], v[40:47], v1\n"
    "ds_read_b128 v[40:43], v0 offset:26880\n"
    "v_mov_b32 v78, s73\n"
    "s_waitcnt lgkmcnt(3)\n"
    "v_smfmac_f32_16x16x64_bf16 v[132:135], v[60:63], v[48:55], v1\n"
    "ds_read_b128 v[92:95], v2 offset:31744\n"
    "s_waitcnt lgkmcnt(3)\n"
    "v_smfmac_f32_16x16x64_bf16 v[132:135], v[120:123], v[104:111], v1\n"
    "ds_read_b128 v[96:99], v2 offset:32768\n"
    "v_mov_b64 v[64:65], 0\n"
    "v_smfmac_f32_16x16x64_bf16 v[80:83], v[56:59], v[112:119], v1\n"
    "ds_read_b128 v[100:103], v2 offset:33792\n"
    "v_mov_b64 v[66:67], 0\n"
    "v_smfmac_f32_16x16x64_bf16 v[80:83], v[60:63], v[32:39], v1\n"
    "ds_read_b128 v[44:47], v0 offset:27904\n"
    "v_mul_f32 v132, v132, v128\n"
    "v_mul_f32 v133, v133, v129\n"
    "v_smfmac_f32_16x16x64_bf16 v[80:83], v[120:123], v[68:75], v1\n"
    "global_load_dword v76, v78, %1 sc1\n"
    "v_mul_f32 v134, v134, v128\n"
    "v_mul_f32 v135, v135, v129\n"
    "s_waitcnt lgkmcnt(3)\n"
    "v_smfmac_f32_16x16x64_bf16 v[64:67], v[40:43], v[88:95], v1\n"
    "global_load_dword v77, v78, %1 offset:128 sc1\n"
    "v_cvt_pk_bf16_f32 v132, v132, v133\n"
    "v_cvt_pk_bf16_f32 v133, v134, v135\n"
    "v_bfi_b32 v132, 1, s48, v132\n"
    "s_waitcnt lgkmcnt(0)\n"
    "v_smfmac_f32_16x16x64_bf16 v[64:67], v[44:47], v[96:103], v1\n"
    "v_pk_mul_f32 v[80:81], v[80:81], v[128:129]\n"
    "v_pk_mul_f32 v[82:83], v[82:83], v[128:129]\n"
    "v_cvt_pk_bf16_f32 v134, v80, v81\n"
    "v_cvt_pk_bf16_f32 v135, v82, v83\n"
    "v_bfi_b32 v134, 1, s48, v134\n"
    "buffer_store_dwordx4 v[132:135], v7, s[40:43], 0 offen sc1\n"
    "s_nop 2\n"
    "v_pk_add_f32 v[64:65], v[64:65], v[66:67]\n"
    "v_pk_mul_f32 v[64:65], v[64:65], v[128:129]\n"
    "v_cvt_pk_bf16_f32 v66, v64, v65\n"
    "v_bfi_b32 v64, 1, s48, v66\n"
    "s_nop 0\n"
    "v_mov_b32_dpp v65, v66 row_ror:8 row_mask:0xf bank_mask:0xf\n"
    "buffer_store_dwordx2 v[64:65], v8, s[40:43], 0 offen sc1\n"
    "s_memrealtime s[38:39]\n"
    "ds_write_b128 v5, v[124:127] offset:22784\n"
    "s_cmp_ge_u32 %2, 2\n"
    "s_cbranch_scc1 .Lw3_%=\n"
    "v_and_b32 v104, 0x70, v0\n"
    "ds_read_b128 v[112:115], v104 offset:31936\n"
    "s_movk_i32 s73, 0x60\n"
    "s_movk_i32 s51, 0x80\n"
    "v_lshrrev_b32 v104, 1, v0\n"
    "v_bfi_b32 v104, s73, v104, v0\n"
    "v_lshlrev_b32 v105, 2, v0\n"
    "v_bfi_b32 v104, s51, v105, v104\n"
    "s_waitcnt lgkmcnt(0)\n"
    "s_barrier\n"
    "s_waitcnt vmcnt(4)\n"
    "s_mov_b32 s56, 0\n"
    ".Lr4_a%=:\n"
    "v_subrev_u32 v78, s52, v79\n"
    "v_cmp_eq_u32 vcc, -1, v79\n"
    "s_cbranch_vccnz .Lr4_%=\n"
    ".Lr4_x%=:\n"
    "v_max_i32_dpp v78, v78, v78 row_ror:8 row_mask:0xf bank_mask:0xf\n"
    "s_nop 1\n"
    "v_max_i32_dpp v78, v78, v78 row_ror:4 row_mask:0xf bank_mask:0xf\n"
    "s_nop 1\n"
    "v_max_i32_dpp v78, v78, v78 row_ror:2 row_mask:0xf bank_mask:0xf\n"
    "s_nop 1\n"
    "v_max_i32_dpp v78, v78, v78 row_ror:1 row_mask:0xf bank_mask:0xf\n"
    "s_nop 0\n"
    "v_readlane_b32 s95, v78, 0\n"
    "v_readlane_b32 s73, v78, 16\n"
    "v_readlane_b32 s51, v78, 32\n"
    "v_readlane_b32 s53, v78, 48\n"
    "s_max_i32 s95, s95, s73\n"
    "s_max_i32 s51, s51, s53\n"
    "s_max_i32 s95, s95, s51\n"
    "s_min_i32 s95, s95, 1024\n"
    "s_add_u32 s95, s95, s52\n"
    "s_add_u32 s95, s95, s55\n"
    "s_cmp_lt_i32 s71, 0\n"
    "s_cbranch_scc1 .Lnored_%=\n"
    "s_mov_b32 s56, 0\n"
    "s_sub_u32 s53, s95, s54\n"
    "s_waitcnt lgkmcnt(0)\n"
    ".Lgl5_%=:\n"
    "s_memrealtime s[50:51]\n"
    "s_waitcnt lgkmcnt(0)\n"
    "s_sub_u32 s50, s50, s53\n"
    "s_cmp_lt_i32 s50, 0\n"
    "s_cbranch_scc1 .Lgl5_%=\n"
    "buffer_load_dwordx4 v[88:91], v104, s[40:43], s65 offen sc1\n"
    "buffer_load_dwordx4 v[92:95], v104, s[40:43], s65 offen offset:1024 sc1\n"
    "buffer_load_dwordx4 v[96:99], v104, s[40:43], s65 offen offset:2048 sc1\n"
    "buffer_load_dwordx4 v[100:103], v104, s[40:43], s65 offen offset:3072 sc1\n"
    "ds_read_b128 v[32:35], v2\n"
    "ds_read_b128 v[36:39], v2 offset:1024\n"
    "ds_read_b128 v[40:43], v2 offset:2048\n"
    "ds_read_b128 v[44:47], v2 offset:3072\n"
    "ds_read_b128 v[48:51], v2 offset:4096\n"
    "ds_read_b128 v[52:55], v2 offset:5120\n"
    "ds_read_b128 v[56:59], v2 offset:6144\n"
    "ds_read_b128 v[60:63], v2 offset:7168\n"
    "ds_read_b128 v[64:67], v2 offset:8192\n"
    "ds_read_b128 v[68:71], v2 offset:9216\n"
    "ds_read_b128 v[80:83], v2 offset:12288\n"
    "ds_read_b128 v[84:87], v2 offset:13312\n"
    "s_cmp_eq_u32 %2, 0\n"
    "s_cselect_b64 exec, 1, 0\n"
    "v_and_b32 v106, -2, s95\n"
    "ds_write_b32 v11, v106 offset:31876\n"
    "s_mov_b64 exec, -1\n"
    "v_xor_b32 v132, 0x800, v10\n"
    "v_xor_b32 v133, 0x1000, v10\n"
    "v_xor_b32 v134, 0x1800, v10\n"
    "v_lshrrev_b32 v135, 2, v5\n"
    "s_movk_i32 s73, 0x180\n"
    "v_lshlrev_b32 v120, 1, v0\n"
    "v_bfi_b32 v120, s73, v120, v0\n"
    "v_lshrrev_b32 v122, 2, v0\n"
    "v_bfi_b32 v120, 64, v122, v120\n"
    "v_lshlrev_b32 v121, 1, v135\n"
    "v_bfi_b32 v121, s73, v121, v135\n"
    "v_lshrrev_b32 v122, 2, v135\n"
    "v_bfi_b32 v121, 64, v122, v121\n"
    "s_mul_i32 s73, s48, 3\n"
    "s_add_u32 s73, s73, s94\n"
    "s_and_b32 s51, s73, 1\n"
    "s_lshl_b32 s51, s51, 12\n"
    "s_lshr_b32 s73, s73, 1\n"
    "s_lshl_b32 s73, s73, 14\n"
    "s_add_u32 s73, s73, s51\n"
    "s_add_u32 s66, s73, s68\n"
    "s_lshr_b32 s67, %2, 1\n"
    "s_mul_i32 s73, s48, 3\n"
    "s_add_u32 s73, s73, s67\n"
    "s_and_b32 s51, s73, 1\n"
    "s_lshl_b32 s51, s51, 12\n"
    "s_lshr_b32 s73, s73, 1\n"
    "s_lshl_b32 s73, s73, 14\n"
    "s_add_u32 s73, s73, s51\n"
    "s_add_u32 s67, s73, s68\n"
    "s_and_b32 s73, %2, 1\n"
    "s_lshl_b32 s73, s73, 11\n"
    "s_add_u32 s67, s67, s73\n"
    "s_mov_b32 s72, s58\n"
    "s_mul_i32 s73, s48, 3\n"
    "s_add_u32 s73, s73, 5\n"
    "s_and_b32 s51, s73, 1\n"
    "s_lshl_b32 s51, s51, 12\n"
    "s_lshr_b32 s73, s73, 1\n"
    "s_lshl_b32 s73, s73, 14\n"
    "s_add_u32 s73, s73, s51\n"
    "s_add_u32 s58, s73, s68\n"
    "s_waitcnt vmcnt(6)\n"
    "v_and_b32 v77, v76, v77\n"
    "s_nop 0\n"
    "v_readfirstlane_b32 s70, v77\n"
    "ds_read_b128 v[72:75], v2 offset:10240\n"
    "ds_read_b128 v[76:79], v2 offset:11264\n"
    "v_mov_b64 v[124:125], 0\n"
    "v_mov_b64 v[126:127], 0\n"
    "v_mov_b64 v[128:129], 0\n"
    "v_mov_b64 v[130:131], 0\n"
    "s_sub_u32 s73, s38, s52\n"
    "s_lshl_b32 s73, s73, 3\n"
    "s_sub_u32 s73, s73, s96\n"
    "s_ashr_i32 s73, s73, 2\n"
    "s_max_i32 s73, s73, -16\n"
    "s_min_i32 s73, s73, 16\n"
    "s_add_u32 s96, s96, s73\n"
    "s_waitcnt lgkmcnt(0)\n"
    ".Lred_%=:\n"
    "s_waitcnt vmcnt(3)\n"
    "v_mfma_f32_16x16x32_bf16 v[108:111], v[88:91], v[112:115], 0\n"
    "v_bitop3_b32 v116, v88, v90, s48 bitop3:0x7e\n"
    "s_waitcnt vmcnt(2)\n"
    "v_mfma_f32_16x16x32_bf16 v[108:111], v[92:95], v[112:115], v[108:111]\n"
    "v_bitop3_b32 v117, v92, v94, s48 bitop3:0x7e\n"
    "s_waitcnt vmcnt(1)\n"
    "v_mfma_f32_16x16x32_bf16 v[108:111], v[96:99], v[112:115], v[108:111]\n"
    "v_bitop3_b32 v118, v96, v98, s48 bitop3:0x7e\n"
    "v_or3_b32 v116, v116, v117, v118\n"
    "s_waitcnt vmcnt(0)\n"
    "v_mfma_f32_16x16x32_bf16 v[108:111], v[100:103], v[112:115], v[108:111]\n"
    "v_bitop3_b32 v119, v100, v102, s48 bitop3:0x7e\n"
    "v_or_b32 v116, v116, v119\n"
    "v_and_b32 v116, 1, v116\n"
    "v_cmp_ne_u32 vcc, 0, v116\n"
    "s_cbranch_vccnz .Lrf_%=\n"
    ".Lrx_%=:\n"
    "s_nop 3\n"
    "v_pk_add_f32 v[108:109], v[108:109], v[110:111]\n"
    "v_cvt_pk_bf16_f32 v108, v108, v109\n"
    "v_min_u32 v108, -2, v108\n"
    "buffer_store_dword v108, v9, s[40:43], s66 offen sc1\n"
    ".Lpub_%=:\n"
    "s_cmp_eq_u32 s59, 0\n"
    "s_cbranch_scc1 .Lr6_%=\n"
    ".Lr6_x%=:\n"
    "s_add_u32 s73, s48, 2\n"
    "s_and_b32 s73, s73, 63\n"
    "s_lshr_b32 s51, s73, 2\n"
    "s_xor_b32 s51, s51, s69\n"
    "s_and_b32 s51, s51, 1\n"
    "s_lshl_b32 s51, s51, 13\n"
    "s_lshl_b32 s73, s73, 5\n"
    "s_add_u32 s73, s73, %4\n"
    "s_lshl_b32 s73, s73, 14\n"
    "s_add_u32 s73, s73, s51\n"
    "s_add_u32 s60, s73, 0x6000000\n"
    "s_add_u32 s61, s73, 0x6001000\n"
    "s_add_u32 s62, s73, 0x8000000\n"
    "s_add_u32 s63, s73, 0x8001000\n"
    "s_lshl_b32 s51, %5, 12\n"
    "s_add_u32 s73, s73, s51\n"
    "s_add_u32 s64, s73, 0x4000000\n"
    "s_add_u32 s73, s48, 2\n"
    "s_and_b32 s51, s73, 32\n"
    "s_and_b32 s73, s73, 31\n"
    "s_lshl3_add_u32 s73, s51, s73\n"
    "s_lshl2_add_u32 s73, s73, s92\n"
    "v_mov_b32 v101, s73\n"
    ".Lg_%=:\n"
    "s_waitcnt lgkmcnt(0)\n"
    "s_barrier\n"
    "ds_read_b32 v97, v11 offset:31876\n"
    "s_mov_b32 s56, 0\n"
    "s_waitcnt lgkmcnt(0)\n"
    "v_readfirstlane_b32 s95, v97\n"
    "s_and_b32 s73, s95, 1\n"
    "s_lshl_b32 s73, s73, 22\n"
    "s_or_b32 s67, s67, s73\n"
    "s_or_b32 s72, s72, s73\n"
    "s_sub_u32 s53, s95, 0\n"
    "s_waitcnt lgkmcnt(0)\n"
    ".Lgl7_%=:\n"
    "s_memrealtime s[50:51]\n"
    "s_waitcnt lgkmcnt(0)\n"
    "s_sub_u32 s50, s50, s53\n"
    "s_cmp_lt_i32 s50, 0\n"
    "s_cbranch_scc1 .Lgl7_%=\n"
    "buffer_load_dwordx4 v[88:91], v120, s[40:43], s67 offen sc1\n"
    "buffer_load_dwordx4 v[92:95], v120, s[40:43], s67 offen offset:1024 sc1\n"
    "buffer_load_dword v96, v121, s[40:43], s72 offen sc1\n"
    "ds_write_b128 v5, v[12:15]\n"
    "ds_write_b128 v5, v[16:19] offset:4096\n"
    "ds_write_b128 v5, v[28:31] offset:16384\n"
    "v_swap_b32 v22, v24\n"
    "v_swap_b32 v23, v25\n"
    "ds_write_b128 v5, v[20:23] offset:8192\n"
    "ds_write_b128 v5, v[24:27] offset:12288\n"
    "v_mov_b64 v[16:17], 0\n"
    "v_mov_b64 v[18:19], 0\n"
    "v_mov_b64 v[20:21], 0\n"
    "v_mov_b64 v[22:23], 0\n"
    "v_mov_b64 v[24:25], 0\n"
    "v_mov_b64 v[26:27], 0\n"
    "v_mov_b64 v[28:29], 0\n"
    "v_mov_b64 v[30:31], 0\n"
    "v_mov_b32 v14, v101\n"
    "global_load_dword v12, v14, %1 sc1\n"
    "global_load_dword v13, v14, %1 offset:128 sc1\n"
    "s_cmp_ge_u32 %2, 2\n"
    "s_cbranch_scc1 .Lcob_%=\n"
    ".Lcod_%=:\n"
    "global_load_dword v14, v14, %1 sc1\n"
    ".Lcox_%=:\n"
    "s_add_u32 s48, s48, 1\n"
    "s_branch .Lstep_%=\n"
    ".Lcob_%=:\n"
    "s_add_u32 s73, s48, 0\n"
    "s_and_b32 s51, s73, 32\n"
    "s_and_b32 s73, s73, 31\n"
    "s_lshl3_add_u32 s73, s51, s73\n"
    "s_lshl2_add_u32 s73, s73, s93\n"
    "v_mov_b32 v100, s73\n"
    "v_mov_b32 v101, 1\n"
    "s_waitcnt vmcnt(5)\n"
    "s_mov_b64 exec, 1\n"
    "ds_add_rtn_u32 v101, v11, v101 offset:31872\n"
    "s_mov_b64 exec, -1\n"
    "s_waitcnt lgkmcnt(0)\n"
    "v_readfirstlane_b32 s73, v101\n"
    "v_mov_b32 v101, 1\n"
    "s_bitcmp1_b32 s73, 0\n"
    "s_cbranch_scc0 .Lcod_%=\n"
    "s_mov_b64 exec, 1\n"
    "global_store_dword v100, v101, %1 sc1\n"
    "s_mov_b64 exec, -1\n"
    "s_branch .Lcox_%=\n"
    ".Lgf_%=:\n"
    "s_bitset1_b32 s67, 22\n"
    "s_bitset1_b32 s72, 22\n"
    "s_waitcnt lgkmcnt(0)\n"
    "buffer_load_dwordx4 v[88:91], v120, s[40:43], s67 offen sc1\n"
    "buffer_load_dwordx4 v[92:95], v120, s[40:43], s67 offen offset:1024 sc1\n"
    "buffer_load_dword v96, v121, s[40:43], s72 offen sc1\n"
    "s_waitcnt vmcnt(0)\n"
    "v_mov_b64 v[16:17], 0\n"
    "v_mov_b64 v[18:19], 0\n"
    "v_mov_b64 v[20:21], 0\n"
    "v_mov_b64 v[22:23], 0\n"
    "v_mov_b64 v[24:25], 0\n"
    "v_mov_b64 v[26:27], 0\n"
    "v_mov_b64 v[28:29], 0\n"
    "v_mov_b64 v[30:31], 0\n"
    "v_mov_b64 v[124:125], 0\n"
    "v_mov_b64 v[126:127], 0\n"
    "v_mov_b64 v[128:129], 0\n"
    "v_mov_b64 v[130:131], 0\n"
    "s_add_u32 s56, s56, 1\n"
    "s_cmp_lt_u32 s56, s57\n"
    "s_cbranch_scc1 .Lstep_%=\n"
    "s_mov_b32 s57, 0\n"
    "v_mov_b32 v135, 1\n"
    "global_store_dword v11, v135, %1 sc1\n"
    "s_branch .Lgx_%=\n"
    ".Lrf_%=:\n"
    "s_memrealtime s[50:51]\n"
    "s_waitcnt lgkmcnt(0)\n"
    "s_add_u32 s95, s50, 62\n"
    "v_or_b32 v106, 1, s95\n"
    "ds_write_b32 v11, v106 offset:31876\n"
    "buffer_load_dwordx4 v[88:91], v104, s[40:43], s65 offen sc1\n"
    "buffer_load_dwordx4 v[92:95], v104, s[40:43], s65 offen offset:1024 sc1\n"
    "buffer_load_dwordx4 v[96:99], v104, s[40:43], s65 offen offset:2048 sc1\n"
    "buffer_load_dwordx4 v[100:103], v104, s[40:43], s65 offen offset:3072 sc1\n"
    "s_add_u32 s56, s56, 1\n"
    "s_cmp_lt_u32 s56, s57\n"
    "s_cbranch_scc1 .Lred_%=\n"
    "s_mov_b32 s57, 0\n"
    "v_mov_b32 v135, 1\n"
    "global_store_dword v11, v135, %1 sc1\n"
    "s_branch .Lrx_%=\n"
    ".Lnored_%=:\n"
    "s_waitcnt vmcnt(0)\n"
    "ds_read_b128 v[32:35], v2\n"
    "ds_read_b128 v[36:39], v2 offset:1024\n"
    "ds_read_b128 v[40:43], v2 offset:2048\n"
    "ds_read_b128 v[44:47], v2 offset:3072\n"
    "ds_read_b128 v[48:51], v2 offset:4096\n"
    "ds_read_b128 v[52:55], v2 offset:5120\n"
    "ds_read_b128 v[56:59], v2 offset:6144\n"
    "ds_read_b128 v[60:63], v2 offset:7168\n"
    "ds_read_b128 v[64:67], v2 offset:8192\n"
    "ds_read_b128 v[68:71], v2 offset:9216\n"
    "ds_read_b128 v[80:83], v2 offset:12288\n"
    "ds_read_b128 v[84:87], v2 offset:13312\n"
    "s_cmp_eq_u32 %2, 0\n"
    "s_cselect_b64 exec, 1, 0\n"
    "v_and_b32 v106, -2, s95\n"
    "ds_write_b32 v11, v106 offset:31876\n"
    "s_mov_b64 exec, -1\n"
    "v_xor_b32 v132, 0x800, v10\n"
    "v_xor_b32 v133, 0x1000, v10\n"
    "v_xor_b32 v134, 0x1800, v10\n"
    "v_lshrrev_b32 v135, 2, v5\n"
    "s_movk_i32 s73, 0x180\n"
    "v_lshlrev_b32 v120, 1, v0\n"
    "v_bfi_b32 v120, s73, v120, v0\n"
    "v_lshrrev_b32 v122, 2, v0\n"
    "v_bfi_b32 v120, 64, v122, v120\n"
    "v_lshlrev_b32 v121, 1, v135\n"
    "v_bfi_b32 v121, s73, v121, v135\n"
    "v_lshrrev_b32 v122, 2, v135\n"
    "v_bfi_b32 v121, 64, v122, v121\n"
    "s_mul_i32 s73, s48, 3\n"
    "s_add_u32 s73, s73, s94\n"
    "s_and_b32 s51, s73, 1\n"
    "s_lshl_b32 s51, s51, 12\n"
    "s_lshr_b32 s73, s73, 1\n"
    "s_lshl_b32 s73, s73, 14\n"
    "s_add_u32 s73, s73, s51\n"
    "s_add_u32 s66, s73, s68\n"
    "s_lshr_b32 s67, %2, 1\n"
    "s_mul_i32 s73, s48, 3\n"
    "s_add_u32 s73, s73, s67\n"
    "s_and_b32 s51, s73, 1\n"
    "s_lshl_b32 s51, s51, 12\n"
    "s_lshr_b32 s73, s73, 1\n"
    "s_lshl_b32 s73, s73, 14\n"
    "s_add_u32 s73, s73, s51\n"
    "s_add_u32 s67, s73, s68\n"
    "s_and_b32 s73, %2, 1\n"
    "s_lshl_b32 s73, s73, 11\n"
    "s_add_u32 s67, s67, s73\n"
    "s_mov_b32 s72, s58\n"
    "s_mul_i32 s73, s48, 3\n"
    "s_add_u32 s73, s73, 5\n"
    "s_and_b32 s51, s73, 1\n"
    "s_lshl_b32 s51, s51, 12\n"
    "s_lshr_b32 s73, s73, 1\n"
    "s_lshl_b32 s73, s73, 14\n"
    "s_add_u32 s73, s73, s51\n"
    "s_add_u32 s58, s73, s68\n"
    "s_waitcnt vmcnt(6)\n"
    "v_and_b32 v77, v76, v77\n"
    "s_nop 0\n"
    "v_readfirstlane_b32 s70, v77\n"
    "ds_read_b128 v[72:75], v2 offset:10240\n"
    "ds_read_b128 v[76:79], v2 offset:11264\n"
    "v_mov_b64 v[124:125], 0\n"
    "v_mov_b64 v[126:127], 0\n"
    "v_mov_b64 v[128:129], 0\n"
    "v_mov_b64 v[130:131], 0\n"
    "s_sub_u32 s73, s38, s52\n"
    "s_lshl_b32 s73, s73, 3\n"
    "s_sub_u32 s73, s73, s96\n"
    "s_ashr_i32 s73, s73, 2\n"
    "s_max_i32 s73, s73, -16\n"
    "s_min_i32 s73, s73, 16\n"
    "s_add_u32 s96, s96, s73\n"
    "s_waitcnt lgkmcnt(0)\n"
    "s_branch .Lpub_%=\n"
    ".Lw3_%=:\n"
    "ds_read_b128 v[32:35], v0 offset:16384\n"
    "ds_read_b128 v[48:51], v0\n"
    "ds_read_b128 v[88:91], v0 offset:4096\n"
    "ds_read_b128 v[104:107], v4 offset:20480\n"
    "ds_read_b128 v[36:39], v0 offset:17408\n"
    "ds_read_b128 v[52:55], v0 offset:1024\n"
    "ds_read_b128 v[92:95], v0 offset:5120\n"
    "ds_read_b128 v[108:111], v4 offset:20544\n"
    "ds_read_b128 v[40:43], v0 offset:18432\n"
    "ds_read_b128 v[56:59], v0 offset:2048\n"
    "ds_read_b128 v[96:99], v0 offset:6144\n"
    "ds_read_b128 v[112:115], v4 offset:20608\n"
    "s_waitcnt lgkmcnt(10)\n"
    "v_mfma_f32_16x16x32_bf16 v[120:123], v[48:51], v[32:35], 0\n"
    "s_barrier\n"
    "ds_read_b128 v[44:47], v0 offset:19456\n"
    "ds_read_b128 v[60:63], v0 offset:3072\n"
    "ds_read_b128 v[100:103], v0 offset:7168\n"
    "ds_read_b128 v[116:119], v4 offset:20672\n"
    "s_waitcnt lgkmcnt(13)\n"
    "v_mfma_f32_16x16x32_bf16 v[124:127], v[88:91], v[32:35], 0\n"
    "s_waitcnt lgkmcnt(12)\n"
    "v_mfma_f32_16x16x32_bf16 v[128:131], v[104:107], v[32:35], 0\n"
    "s_waitcnt lgkmcnt(10)\n"
    "v_mfma_f32_16x16x32_bf16 v[120:123], v[52:55], v[36:39], v[120:123]\n"
    "s_waitcnt lgkmcnt(9)\n"
    "v_mfma_f32_16x16x32_bf16 v[124:127], v[92:95], v[36:39], v[124:127]\n"
    "ds_read_b128 v[64:67], v10 offset:4096\n"
    "s_waitcnt lgkmcnt(9)\n"
    "v_mfma_f32_16x16x32_bf16 v[128:131], v[108:111], v[36:39], v[128:131]\n"
    "ds_read_b128 v[68:71], v10 offset:8192\n"
    "s_waitcnt lgkmcnt(8)\n"
    "v_mfma_f32_16x16x32_bf16 v[120:123], v[56:59], v[40:43], v[120:123]\n"
    "ds_read_b128 v[72:75], v10 offset:5120\n"
    "s_waitcnt lgkmcnt(8)\n"
    "v_mfma_f32_16x16x32_bf16 v[124:127], v[96:99], v[40:43], v[124:127]\n"
    "ds_read_b128 v[80:83], v10 offset:9216\n"
    "s_waitcnt lgkmcnt(8)\n"
    "v_mfma_f32_16x16x32_bf16 v[128:131], v[112:115], v[40:43], v[128:131]\n"
    "ds_read_b128 v[84:87], v11 offset:31888\n"
    "s_waitcnt lgkmcnt(7)\n"
    "v_mfma_f32_16x16x32_bf16 v[120:123], v[60:63], v[44:47], v[120:123]\n"
    "ds_read_b64 v[134:135], v11 offset:31904\n"
    "s_waitcnt lgkmcnt(7)\n"
    "v_mfma_f32_16x16x32_bf16 v[124:127], v[100:103], v[44:47], v[124:127]\n"
    "ds_read_b128 v[88:91], v10 offset:18688\n"
    "s_waitcnt lgkmcnt(7)\n"
    "v_mfma_f32_16x16x32_bf16 v[128:131], v[116:119], v[44:47], v[128:131]\n"
    "ds_read_b128 v[92:95], v10 offset:19712\n"
    "v_mov_b32 v132, 0xd01502f9\n"
    "s_cmp_eq_u32 s59, 0\n"
    "s_cbranch_scc1 .Lr8_%=\n"
    ".Lr8_x%=:\n"
    "ds_read_b128 v[32:35], v2\n"
    "ds_read_b128 v[36:39], v2 offset:1024\n"
    "ds_read_b128 v[40:43], v2 offset:2048\n"
    "ds_read_b128 v[44:47], v2 offset:3072\n"
    "v_cndmask_b32 v120, v132, v120, s[76:77]\n"
    "v_cndmask_b32 v121, v132, v121, s[78:79]\n"
    "v_cndmask_b32 v122, v132, v122, s[80:81]\n"
    "v_cndmask_b32 v123, v132, v123, s[82:83]\n"
    "v_cndmask_b32 v124, v132, v124, s[84:85]\n"
    "v_cndmask_b32 v125, v132, v125, s[86:87]\n"
    "v_cndmask_b32 v126, v132, v126, s[88:89]\n"
    "v_cndmask_b32 v127, v132, v127, s[90:91]\n"
    "v_maximum3_f32 v133, v128, v129, v120\n"
    "v_maximum3_f32 v133, v133, v121, v122\n"
    "v_maximum3_f32 v133, v133, v123, v124\n"
    "v_maximum3_f32 v133, v133, v125, v126\n"
    "v_maximum3_f32 v133, v133, v127, v127\n"
    "v_mov_b32 v132, v133\n"
    "ds_read_b128 v[48:51], v2 offset:4096\n"
    "ds_read_b128 v[52:55], v2 offset:5120\n"
    "v_permlane16_swap_b32 v132, v133\n"
    "v_maximum3_f32 v133, v133, v132, v132\n"
    "v_mov_b32 v132, v133\n"
    "ds_read_b128 v[56:59], v2 offset:6144\n"
    "ds_read_b128 v[60:63], v2 offset:7168\n"
    "v_permlane32_swap_b32 v132, v133\n"
    "v_maximum3_f32 v133, v133, v132, v132\n"
    "v_mul_f32 v133, 0xbe0293ee, v133\n"
    "v_fmamk_f32 v120, v120, 0x3e0293ee, v133\n"
    "v_fmamk_f32 v121, v121, 0x3e0293ee, v133\n"
    "v_fmamk_f32 v122, v122, 0x3e0293ee, v133\n"
    "v_fmamk_f32 v123, v123, 0x3e0293ee, v133\n"
    "v_fmamk_f32 v124, v124, 0x3e0293ee, v133\n"
    "v_fmamk_f32 v125, v125, 0x3e0293ee, v133\n"
    "v_fmamk_f32 v126, v126, 0x3e0293ee, v133\n"
    "v_fmamk_f32 v127, v127, 0x3e0293ee, v133\n"
    "v_fmamk_f32 v128, v128, 0x3e0293ee, v133\n"
    "v_fmamk_f32 v129, v129, 0x3e0293ee, v133\n"
    "v_exp_f32 v120, v120\n"
    "v_exp_f32 v121, v121\n"
    "v_exp_f32 v122, v122\n"
    "v_exp_f32 v123, v123\n"
    "v_exp_f32 v124, v124\n"
    "v_exp_f32 v125, v125\n"
    "v_exp_f32 v126, v126\n"
    "v_exp_f32 v127, v127\n"
    "v_exp_f32 v128, v128\n"
    "v_exp_f32 v129, v129\n"
    "v_cvt_pk_bf16_f32 v120, v120, v121\n"
    "v_cvt_pk_bf16_f32 v121, v122, v123\n"
    "v_cvt_pk_bf16_f32 v122, v124, v125\n"
    "v_cvt_pk_bf16_f32 v123, v126, v127\n"
    "v_cvt_pk_bf16_f32 v124, v128, v129\n"
    "v_cvt_pk_bf16_f32 v125, v128, v129\n"
    "s_waitcnt lgkmcnt(11)\n"
    "v_mfma_f32_16x16x32_bf16 v[100:103], v[84:87], v[120:123], 0\n"
    "v_mfma_f32_16x16x32_bf16 v[104:107], v[64:67], v[120:123], 0\n"
    "v_mfma_f32_16x16x32_bf16 v[108:111], v[68:71], v[120:123], 0\n"
    "s_waitcnt lgkmcnt(10)\n"
    "v_mfma_f32_16x16x16_bf16 v[100:103], v[134:135], v[124:125], v[100:103]\n"
    "v_mfma_f32_16x16x32_bf16 v[112:115], v[72:75], v[120:123], 0\n"
    "v_mfma_f32_16x16x32_bf16 v[116:119], v[80:83], v[120:123], 0\n"
    "s_waitcnt lgkmcnt(9)\n"
    "v_mfma_f32_16x16x16_bf16 v[104:107], v[88:89], v[124:125], v[104:107]\n"
    "ds_read_b128 v[64:67], v2 offset:8192\n"
    "v_mfma_f32_16x16x16_bf16 v[108:111], v[90:91], v[124:125], v[108:111]\n"
    "ds_read_b128 v[68:71], v2 offset:9216\n"
    "v_rcp_f32 v132, v100\n"
    "s_waitcnt lgkmcnt(10)\n"
    "v_mfma_f32_16x16x16_bf16 v[112:115], v[92:93], v[124:125], v[112:115]\n"
    "ds_read_b128 v[80:83], v2 offset:12288\n"
    "v_mfma_f32_16x16x16_bf16 v[116:119], v[94:95], v[124:125], v[116:119]\n"
    "ds_read_b128 v[84:87], v2 offset:13312\n"
    "v_pk_mul_f32 v[104:105], v[104:105], v[132:133] op_sel_hi:[1,0]\n"
    "v_pk_mul_f32 v[106:107], v[106:107], v[132:133] op_sel_hi:[1,0]\n"
    "v_pk_mul_f32 v[108:109], v[108:109], v[132:133] op_sel_hi:[1,0]\n"
    "v_pk_mul_f32 v[110:111], v[110:111], v[132:133] op_sel_hi:[1,0]\n"
    "v_cvt_pk_bf16_f32 v104, v104, v108\n"
    "v_cvt_pk_bf16_f32 v105, v105, v109\n"
    "v_cvt_pk_bf16_f32 v106, v106, v110\n"
    "v_cvt_pk_bf16_f32 v107, v107, v111\n"
    "v_pk_mul_f32 v[112:113], v[112:113], v[132:133] op_sel_hi:[1,0]\n"
    "v_pk_mul_f32 v[114:115], v[114:115], v[132:133] op_sel_hi:[1,0]\n"
    "v_pk_mul_f32 v[116:117], v[116:117], v[132:133] op_sel_hi:[1,0]\n"
    "v_pk_mul_f32 v[118:119], v[118:119], v[132:133] op_sel_hi:[1,0]\n"
    "v_cvt_pk_bf16_f32 v112, v112, v116\n"
    "v_cvt_pk_bf16_f32 v113, v113, v117\n"
    "v_cvt_pk_bf16_f32 v114, v114, v118\n"
    "v_cvt_pk_bf16_f32 v115, v115, v119\n"
    "s_lshr_b32 s51, s48, 2\n"
    "s_xor_b32 s51, s51, s69\n"
    "s_and_b32 s51, s51, 1\n"
    "s_lshl_b32 s51, s51, 13\n"
    "s_lshl_b32 s73, s48, 19\n"
    "s_add_u32 s73, s73, s75\n"
    "s_add_u32 s73, s73, s51\n"
    "buffer_store_dwordx4 v[104:107], v10, s[40:43], s73 offen sc1\n"
    "buffer_store_dwordx4 v[112:115], v10, s[40:43], s73 offen offset:1024 sc1\n"
    "s_waitcnt vmcnt(2)\n"
    "ds_read_b128 v[72:75], v2 offset:10240\n"
    "ds_read_b128 v[76:79], v2 offset:11264\n"
    "v_mov_b64 v[124:125], 0\n"
    "v_mov_b64 v[126:127], 0\n"
    "v_mov_b64 v[128:129], 0\n"
    "v_mov_b64 v[130:131], 0\n"
    "v_xor_b32 v132, 0x800, v10\n"
    "v_xor_b32 v133, 0x1000, v10\n"
    "v_xor_b32 v134, 0x1800, v10\n"
    "v_lshrrev_b32 v135, 2, v5\n"
    "s_movk_i32 s73, 0x180\n"
    "v_lshlrev_b32 v120, 1, v0\n"
    "v_bfi_b32 v120, s73, v120, v0\n"
    "v_lshrrev_b32 v122, 2, v0\n"
    "v_bfi_b32 v120, 64, v122, v120\n"
    "v_lshlrev_b32 v121, 1, v135\n"
    "v_bfi_b32 v121, s73, v121, v135\n"
    "v_lshrrev_b32 v122, 2, v135\n"
    "v_bfi_b32 v121, 64, v122, v121\n"
    "s_lshr_b32 s67, %2, 1\n"
    "s_mul_i32 s73, s48, 3\n"
    "s_add_u32 s73, s73, s67\n"
    "s_and_b32 s51, s73, 1\n"
    "s_lshl_b32 s51, s51, 12\n"
    "s_lshr_b32 s73, s73, 1\n"
    "s_lshl_b32 s73, s73, 14\n"
    "s_add_u32 s73, s73, s51\n"
    "s_add_u32 s67, s73, s68\n"
    "s_and_b32 s73, %2, 1\n"
    "s_lshl_b32 s73, s73, 11\n"
    "s_add_u32 s67, s67, s73\n"
    "s_mov_b32 s72, s58\n"
    "s_mul_i32 s73, s48, 3\n"
    "s_add_u32 s73, s73, 5\n"
    "s_and_b32 s51, s73, 1\n"
    "s_lshl_b32 s51, s51, 12\n"
    "s_lshr_b32 s73, s73, 1\n"
    "s_lshl_b32 s73, s73, 14\n"
    "s_add_u32 s73, s73, s51\n"
    "s_add_u32 s58, s73, s68\n"
    "s_cmp_eq_u32 s59, 0\n"
    "s_cbranch_scc1 .Lr9_%=\n"
    ".Lr9_x%=:\n"
    "s_add_u32 s73, s48, 2\n"
    "s_and_b32 s73, s73, 63\n"
    "s_lshr_b32 s51, s73, 2\n"
    "s_xor_b32 s51, s51, s69\n"
    "s_and_b32 s51, s51, 1\n"
    "s_lshl_b32 s51, s51, 13\n"
    "s_lshl_b32 s73, s73, 5\n"
    "s_add_u32 s73, s73, %4\n"
    "s_lshl_b32 s73, s73, 14\n"
    "s_add_u32 s73, s73, s51\n"
    "s_add_u32 s60, s73, 0x6000000\n"
    "s_add_u32 s61, s73, 0x6001000\n"
    "s_add_u32 s62, s73, 0x8000000\n"
    "s_add_u32 s63, s73, 0x8001000\n"
    "s_lshl_b32 s51, %5, 12\n"
    "s_add_u32 s73, s73, s51\n"
    "s_add_u32 s64, s73, 0x4000000\n"
    "s_add_u32 s73, s48, 2\n"
    "s_and_b32 s51, s73, 32\n"
    "s_and_b32 s73, s73, 31\n"
    "s_lshl3_add_u32 s73, s51, s73\n"
    "s_lshl2_add_u32 s73, s73, s92\n"
    "v_mov_b32 v101, s73\n"
    "s_branch .Lg_%=\n"
    ".Lr4_%=:\n"
    "s_sleep 2\n"
    "buffer_load_dword v79, v0, s[40:43], s58 offen offset:3072 sc1\n"
    "s_waitcnt vmcnt(0)\n"
    "s_add_u32 s56, s56, 1\n"
    "s_cmp_lt_u32 s56, s57\n"
    "s_cbranch_scc1 .Lr4_a%=\n"
    "s_mov_b32 s57, 0\n"
    "v_mov_b32 v135, 1\n"
    "global_store_dword v11, v135, %1 sc1\n"
    "s_branch .Lr4_x%=\n"
    ".Lr6_%=:\n"
    "s_cmp_lg_u32 s70, 0\n"
    "s_cbranch_scc1 .Lr6_y%=\n"
    "s_waitcnt lgkmcnt(0)\n"
    "s_add_u32 s73, s48, 1\n"
    "s_and_b32 s51, s73, 32\n"
    "s_and_b32 s73, s73, 31\n"
    "s_lshl3_add_u32 s73, s51, s73\n"
    "s_lshl2_add_u32 s73, s73, s92\n"
    "s_mov_b32 s53, s73\n"
    "s_mov_b32 s56, 0\n"
    "v_mov_b32 v134, s53\n"
    ".Lpoll10_%=:\n"
    "global_load_dword v132, v134, %1 sc1\n"
    "global_load_dword v133, v134, %1 offset:128 sc1\n"
    "s_waitcnt vmcnt(0)\n"
    "v_and_b32 v133, v132, v133\n"
    "s_nop 0\n"
    "v_readfirstlane_b32 s73, v133\n"
    "s_cmp_ge_u32 s73, 1\n"
    "s_cbranch_scc1 .Lpoll10_x%=\n"
    "s_add_u32 s56, s56, 1\n"
    "s_cmp_lt_u32 s56, 3\n"
    "s_cbranch_scc1 .Lpoll10_%=\n"
    "s_sleep 8\n"
    "s_cmp_lt_u32 s56, s57\n"
    "s_cbranch_scc1 .Lpoll10_%=\n"
    "s_mov_b32 s57, 0\n"
    "v_mov_b32 v135, 1\n"
    "global_store_dword v11, v135, %1 sc1\n"
    ".Lpoll10_x%=:\n"
    ".Lr6_y%=:\n"
    "buffer_load_dwordx4 v[12:15], v5, s[40:43], s60 offen sc1\n"
    "buffer_load_dwordx4 v[16:19], v5, s[40:43], s61 offen sc1\n"
    "buffer_load_dwordx4 v[20:23], v5, s[40:43], s62 offen sc1\n"
    "buffer_load_dwordx4 v[24:27], v5, s[40:43], s63 offen sc1\n"
    "buffer_load_dwordx4 v[28:31], v5, s[40:43], s64 offen sc1\n"
    "s_waitcnt vmcnt(0)\n"
    "v_xor_b32 v132, 0x800, v10\n"
    "v_xor_b32 v133, 0x1000, v10\n"
    "v_xor_b32 v134, 0x1800, v10\n"
    "v_lshrrev_b32 v135, 2, v5\n"
    "s_movk_i32 s73, 0x180\n"
    "v_lshlrev_b32 v120, 1, v0\n"
    "v_bfi_b32 v120, s73, v120, v0\n"
    "v_lshrrev_b32 v122, 2, v0\n"
    "v_bfi_b32 v120, 64, v122, v120\n"
    "v_lshlrev_b32 v121, 1, v135\n"
    "v_bfi_b32 v121, s73, v121, v135\n"
    "v_lshrrev_b32 v122, 2, v135\n"
    "v_bfi_b32 v121, 64, v122, v121\n"
    "s_branch .Lr6_x%=\n"
    ".Lr8_%=:\n"
    "s_waitcnt vmcnt(2)\n"
    "v_and_b32 v77, v76, v77\n"
    "s_nop 0\n"
    "v_readfirstlane_b32 s59, v77\n"
    "s_cmp_eq_u32 s59, 0\n"
    "s_cbranch_scc1 .Lr8_x%=\n"
    "buffer_load_dwordx4 v[12:15], v5, s[40:43], s60 offen sc1\n"
    "buffer_load_dwordx4 v[16:19], v5, s[40:43], s61 offen sc1\n"
    "buffer_load_dwordx4 v[20:23], v5, s[40:43], s62 offen sc1\n"
    "buffer_load_dwordx4 v[24:27], v5, s[40:43], s63 offen sc1\n"
    "buffer_load_dwordx4 v[28:31], v5, s[40:43], s64 offen sc1\n"
    "s_branch .Lr8_x%=\n"
    ".Lr9_%=:\n"
    "s_waitcnt lgkmcnt(0)\n"
    "s_add_u32 s73, s48, 1\n"
    "s_and_b32 s51, s73, 32\n"
    "s_and_b32 s73, s73, 31\n"
    "s_lshl3_add_u32 s73, s51, s73\n"
    "s_lshl2_add_u32 s73, s73, s92\n"
    "s_mov_b32 s53, s73\n"
    "s_mov_b32 s56, 0\n"
    "v_mov_b32 v134, s53\n"
    ".Lpoll11_%=:\n"
    "global_load_dword v132, v134, %1 sc1\n"
    "global_load_dword v133, v134, %1 offset:128 sc1\n"
    "s_waitcnt vmcnt(0)\n"
    "v_and_b32 v133, v132, v133\n"
    "s_nop 0\n"
    "v_readfirstlane_b32 s73, v133\n"
    "s_cmp_ge_u32 s73, 1\n"
    "s_cbranch_scc1 .Lpoll11_x%=\n"
    "s_add_u32 s56, s56, 1\n"
    "s_cmp_lt_u32 s56, 3\n"
    "s_cbranch_scc1 .Lpoll11_%=\n"
    "s_sleep 8\n"
    "s_cmp_lt_u32 s56, s57\n"
    "s_cbranch_scc1 .Lpoll11_%=\n"
    "s_mov_b32 s57, 0\n"
    "v_mov_b32 v135, 1\n"
    "global_store_dword v11, v135, %1 sc1\n"
    ".Lpoll11_x%=:\n"
    ".Lr9_y%=:\n"
    "buffer_load_dwordx4 v[12:15], v5, s[40:43], s60 offen sc1\n"
    "buffer_load_dwordx4 v[16:19], v5, s[40:43], s61 offen sc1\n"
    "buffer_load_dwordx4 v[20:23], v5, s[40:43], s62 offen sc1\n"
    "buffer_load_dwordx4 v[24:27], v5, s[40:43], s63 offen sc1\n"
    "buffer_load_dwordx4 v[28:31], v5, s[40:43], s64 offen sc1\n"
    "s_waitcnt vmcnt(0)\n"
    "v_xor_b32 v132, 0x800, v10\n"
    "v_xor_b32 v133, 0x1000, v10\n"
    "v_xor_b32 v134, 0x1800, v10\n"
    "v_lshrrev_b32 v135, 2, v5\n"
    "s_movk_i32 s73, 0x180\n"
    "v_lshlrev_b32 v120, 1, v0\n"
    "v_bfi_b32 v120, s73, v120, v0\n"
    "v_lshrrev_b32 v122, 2, v0\n"
    "v_bfi_b32 v120, 64, v122, v120\n"
    "v_lshlrev_b32 v121, 1, v135\n"
    "v_bfi_b32 v121, s73, v121, v135\n"
    "v_lshrrev_b32 v122, 2, v135\n"
    "v_bfi_b32 v121, 64, v122, v121\n"
    "s_branch .Lr9_x%=\n"
    ".Lend_%=:\n"
    "ds_write_b128 v5, v[124:127] offset:22784\n"
    "s_waitcnt vmcnt(0)\n"
    "s_cmp_ge_u32 %2, 2\n"
    "s_cbranch_scc1 .Lla_%=\n"
    "s_waitcnt lgkmcnt(0)\n"
    "s_barrier\n"
    "s_branch .Lout_%=\n"
    ".Lla_%=:\n"
    "ds_read_b128 v[32:35], v0 offset:16384\n"
    "ds_read_b128 v[48:51], v0\n"
    "ds_read_b128 v[88:91], v0 offset:4096\n"
    "ds_read_b128 v[104:107], v4 offset:20480\n"
    "ds_read_b128 v[36:39], v0 offset:17408\n"
    "ds_read_b128 v[52:55], v0 offset:1024\n"
    "ds_read_b128 v[92:95], v0 offset:5120\n"
    "ds_read_b128 v[108:111], v4 offset:20544\n"
    "ds_read_b128 v[40:43], v0 offset:18432\n"
    "ds_read_b128 v[56:59], v0 offset:2048\n"
    "ds_read_b128 v[96:99], v0 offset:6144\n"
    "ds_read_b128 v[112:115], v4 offset:20608\n"
    "s_waitcnt lgkmcnt(10)\n"
    "v_mfma_f32_16x16x32_bf16 v[120:123], v[48:51], v[32:35], 0\n"
    "s_barrier\n"
    "ds_read_b128 v[44:47], v0 offset:19456\n"
    "ds_read_b128 v[60:63], v0 offset:3072\n"
    "ds_read_b128 v[100:103], v0 offset:7168\n"
    "ds_read_b128 v[116:119], v4 offset:20672\n"
    "s_waitcnt lgkmcnt(13)\n"
    "v_mfma_f32_16x16x32_bf16 v[124:127], v[88:91], v[32:35], 0\n"
    "s_waitcnt lgkmcnt(12)\n"
    "v_mfma_f32_16x16x32_bf16 v[128:131], v[104:107], v[32:35], 0\n"
    "s_waitcnt lgkmcnt(10)\n"
    "v_mfma_f32_16x16x32_bf16 v[120:123], v[52:55], v[36:39], v[120:123]\n"
    "s_waitcnt lgkmcnt(9)\n"
    "v_mfma_f32_16x16x32_bf16 v[124:127], v[92:95], v[36:39], v[124:127]\n"
    "ds_read_b128 v[64:67], v10 offset:4096\n"
    "s_waitcnt lgkmcnt(9)\n"
    "v_mfma_f32_16x16x32_bf16 v[128:131], v[108:111], v[36:39], v[128:131]\n"
    "ds_read_b128 v[68:71], v10 offset:8192\n"
    "s_waitcnt lgkmcnt(8)\n"
    "v_mfma_f32_16x16x32_bf16 v[120:123], v[56:59], v[40:43], v[120:123]\n"
    "ds_read_b128 v[72:75], v10 offset:5120\n"
    "s_waitcnt lgkmcnt(8)\n"
    "v_mfma_f32_16x16x32_bf16 v[124:127], v[96:99], v[40:43], v[124:127]\n"
    "ds_read_b128 v[80:83], v10 offset:9216\n"
    "s_waitcnt lgkmcnt(8)\n"
    "v_mfma_f32_16x16x32_bf16 v[128:131], v[112:115], v[40:43], v[128:131]\n"
    "ds_read_b128 v[84:87], v11 offset:31888\n"
    "s_waitcnt lgkmcnt(7)\n"
    "v_mfma_f32_16x16x32_bf16 v[120:123], v[60:63], v[44:47], v[120:123]\n"
    "ds_read_b64 v[134:135], v11 offset:31904\n"
    "s_waitcnt lgkmcnt(7)\n"
    "v_mfma_f32_16x16x32_bf16 v[124:127], v[100:103], v[44:47], v[124:127]\n"
    "ds_read_b128 v[88:91], v10 offset:18688\n"
    "s_waitcnt lgkmcnt(7)\n"
    "v_mfma_f32_16x16x32_bf16 v[128:131], v[116:119], v[44:47], v[128:131]\n"
    "ds_read_b128 v[92:95], v10 offset:19712\n"
    "v_mov_b32 v132, 0xd01502f9\n"
    "v_cndmask_b32 v120, v132, v120, s[76:77]\n"
    "v_cndmask_b32 v121, v132, v121, s[78:79]\n"
    "v_cndmask_b32 v122, v132, v122, s[80:81]\n"
    "v_cndmask_b32 v123, v132, v123, s[82:83]\n"
    "v_cndmask_b32 v124, v132, v124, s[84:85]\n"
    "v_cndmask_b32 v125, v132, v125, s[86:87]\n"
    "v_cndmask_b32 v126, v132, v126, s[88:89]\n"
    "v_cndmask_b32 v127, v132, v127, s[90:91]\n"
    "v_maximum3_f32 v133, v128, v129, v120\n"
    "v_maximum3_f32 v133, v133, v121, v122\n"
    "v_maximum3_f32 v133, v133, v123, v124\n"
    "v_maximum3_f32 v133, v133, v125, v126\n"
    "v_maximum3_f32 v133, v133, v127, v127\n"
    "v_mov_b32 v132, v133\n"
    "s_nop 1\n"
    "v_permlane16_swap_b32 v132, v133\n"
    "v_maximum3_f32 v133, v133, v132, v132\n"
    "v_mov_b32 v132, v133\n"
    "s_nop 1\n"
    "v_permlane32_swap_b32 v132, v133\n"
    "v_maximum3_f32 v133, v133, v132, v132\n"
    "v_mul_f32 v133, 0xbe0293ee, v133\n"
    "v_fmamk_f32 v120, v120, 0x3e0293ee, v133\n"
    "v_fmamk_f32 v121, v121, 0x3e0293ee, v133\n"
    "v_fmamk_f32 v122, v122, 0x3e0293ee, v133\n"
    "v_fmamk_f32 v123, v123, 0x3e0293ee, v133\n"
    "v_fmamk_f32 v124, v124, 0x3e0293ee, v133\n"
    "v_fmamk_f32 v125, v125, 0x3e0293ee, v133\n"
    "v_fmamk_f32 v126, v126, 0x3e0293ee, v133\n"
    "v_fmamk_f32 v127, v127, 0x3e0293ee, v133\n"
    "v_fmamk_f32 v128, v128, 0x3e0293ee, v133\n"
    "v_fmamk_f32 v129, v129, 0x3e0293ee, v133\n"
    "v_exp_f32 v120, v120\n"
    "v_exp_f32 v121, v121\n"
    "v_exp_f32 v122, v122\n"
    "v_exp_f32 v123, v123\n"
    "v_exp_f32 v124, v124\n"
    "v_exp_f32 v125, v125\n"
    "v_exp_f32 v126, v126\n"
    "v_exp_f32 v127, v127\n"
    "v_exp_f32 v128, v128\n"
    "v_exp_f32 v129, v129\n"
    "v_cvt_pk_bf16_f32 v120, v120, v121\n"
    "v_cvt_pk_bf16_f32 v121, v122, v123\n"
    "v_cvt_pk_bf16_f32 v122, v124, v125\n"
    "v_cvt_pk_bf16_f32 v123, v126, v127\n"
    "v_cvt_pk_bf16_f32 v124, v128, v129\n"
    "v_cvt_pk_bf16_f32 v125, v128, v129\n"
    "s_waitcnt lgkmcnt(3)\n"
    "v_mfma_f32_16x16x32_bf16 v[100:103], v[84:87], v[120:123], 0\n"
    "v_mfma_f32_16x16x32_bf16 v[104:107], v[64:67], v[120:123], 0\n"
    "v_mfma_f32_16x16x32_bf16 v[108:111], v[68:71], v[120:123], 0\n"
    "s_waitcnt lgkmcnt(2)\n"
    "v_mfma_f32_16x16x16_bf16 v[100:103], v[134:135], v[124:125], v[100:103]\n"
    "v_mfma_f32_16x16x32_bf16 v[112:115], v[72:75], v[120:123], 0\n"
    "v_mfma_f32_16x16x32_bf16 v[116:119], v[80:83], v[120:123], 0\n"
    "s_waitcnt lgkmcnt(1)\n"
    "v_mfma_f32_16x16x16_bf16 v[104:107], v[88:89], v[124:125], v[104:107]\n"
    "v_mfma_f32_16x16x16_bf16 v[108:111], v[90:91], v[124:125], v[108:111]\n"
    "v_rcp_f32 v132, v100\n"
    "s_waitcnt lgkmcnt(0)\n"
    "v_mfma_f32_16x16x16_bf16 v[112:115], v[92:93], v[124:125], v[112:115]\n"
    "v_mfma_f32_16x16x16_bf16 v[116:119], v[94:95], v[124:125], v[116:119]\n"
    "s_nop 1\n"
    "v_pk_mul_f32 v[104:105], v[104:105], v[132:133] op_sel_hi:[1,0]\n"
    "v_pk_mul_f32 v[106:107], v[106:107], v[132:133] op_sel_hi:[1,0]\n"
    "v_pk_mul_f32 v[108:109], v[108:109], v[132:133] op_sel_hi:[1,0]\n"
    "v_pk_mul_f32 v[110:111], v[110:111], v[132:133] op_sel_hi:[1,0]\n"
    "v_cvt_pk_bf16_f32 v104, v104, v108\n"
    "v_cvt_pk_bf16_f32 v105, v105, v109\n"
    "v_cvt_pk_bf16_f32 v106, v106, v110\n"
    "v_cvt_pk_bf16_f32 v107, v107, v111\n"
    "v_pk_mul_f32 v[112:113], v[112:113], v[132:133] op_sel_hi:[1,0]\n"
    "v_pk_mul_f32 v[114:115], v[114:115], v[132:133] op_sel_hi:[1,0]\n"
    "v_pk_mul_f32 v[116:117], v[116:117], v[132:133] op_sel_hi:[1,0]\n"
    "v_pk_mul_f32 v[118:119], v[118:119], v[132:133] op_sel_hi:[1,0]\n"
    "v_cvt_pk_bf16_f32 v112, v112, v116\n"
    "v_cvt_pk_bf16_f32 v113, v113, v117\n"
    "v_cvt_pk_bf16_f32 v114, v114, v118\n"
    "v_cvt_pk_bf16_f32 v115, v115, v119\n"
    "s_lshr_b32 s51, s48, 2\n"
    "s_xor_b32 s51, s51, s69\n"
    "s_and_b32 s51, s51, 1\n"
    "s_lshl_b32 s51, s51, 13\n"
    "s_lshl_b32 s73, s48, 19\n"
    "s_add_u32 s73, s73, s75\n"
    "s_add_u32 s73, s73, s51\n"
    "buffer_store_dwordx4 v[104:107], v10, s[40:43], s73 offen sc1\n"
    "buffer_store_dwordx4 v[112:115], v10, s[40:43], s73 offen offset:1024 sc1\n"
    "s_add_u32 s73, s48, 0\n"
    "s_and_b32 s51, s73, 32\n"
    "s_and_b32 s73, s73, 31\n"
    "s_lshl3_add_u32 s73, s51, s73\n"
    "s_lshl2_add_u32 s73, s73, s93\n"
    "v_mov_b32 v100, s73\n"
    "v_mov_b32 v101, 1\n"
    "s_waitcnt vmcnt(0)\n"
    "s_mov_b64 exec, 1\n"
    "ds_add_rtn_u32 v101, v11, v101 offset:31872\n"
    "s_mov_b64 exec, -1\n"
    "s_waitcnt lgkmcnt(0)\n"
    "v_readfirstlane_b32 s73, v101\n"
    "v_mov_b32 v101, 1\n"
    "s_bitcmp1_b32 s73, 0\n"
    "s_cbranch_scc0 .Lout_%=\n"
    "s_mov_b64 exec, 1\n"
    "global_store_dword v100, v101, %1 sc1\n"
    "s_mov_b64 exec, -1\n"
    "s_cmp_eq_u32 %3, 128\n"
    "s_cbranch_scc0 .Lout_%=\n"
    "v_mov_b32 v130, 0x20400\n"
    "global_load_dwordx2 v[132:133], v130, %1 sc1\n"
    "v_mbcnt_lo_u32_b32 v131, -1, 0\n"
    "v_min_u32 v131, 7, v131\n"
    "v_lshlrev_b32 v131, 7, v131\n"
    "v_add_u32 v131, 0x20000, v131\n"
    "global_load_dwordx2 v[134:135], v131, %1 sc1\n"
    "s_waitcnt vmcnt(0)\n"
    "v_add_u32 v132, 1, v132\n"
    "v_add_u32 v133, 1, v133\n"
    "global_store_dwordx2 v130, v[132:133], %1 sc1\n"
    ".Lout_%=:\n"
    "s_waitcnt vmcnt(0)\n"
    "s_endpgm\n"
    ".p2align 6\n"
    ".Lrole1_%=:\n"
    "s_mov_b64 s[40:41], %0\n"
    "s_and_b32 s41, s41, 0xffff\n"
    "s_mov_b32 s42, 0xc000000\n"
    "s_mov_b32 s43, 0x20000\n"
    "s_mov_b64 s[92:93], %0\n"
    "s_and_b32 s93, s93, 0xffff\n"
    "s_mov_b32 s94, 0xc000000\n"
    "s_mov_b32 s95, 0x20000\n"
    "s_mov_b32 s53, 131072\n"
    "s_mov_b64 s[56:57], 0\n"
    "v_mbcnt_lo_u32_b32 v7, -1, 0\n"
    "v_mbcnt_hi_u32_b32 v7, -1, v7\n"
    "s_lshl_b32 s50, %2, 8\n"
    "s_add_u32 s50, s50, 147456\n"
    "v_lshl_add_u32 v7, v7, 2, s50\n"
    "ds_read_b32 v1, v7\n"
    "ds_read_b32 v2, v7 offset:1024\n"
    "ds_read_b32 v3, v7 offset:2048\n"
    "ds_read_b32 v5, v7 offset:3072\n"
    "ds_read_b32 v48, v7 offset:4096\n"
    "ds_read_b32 v49, v7 offset:5120\n"
    "ds_read_b32 v50, v7 offset:6144\n"
    "s_waitcnt lgkmcnt(0)\n"
    "s_barrier\n"
    "s_movk_i32 s86, 255\n"
    "s_lshl_b32 s54, %2, 10\n"
    "s_lshl_b32 s58, %2, 13\n"
    "s_add_u32 s58, s58, 65536\n"
    "s_add_u32 s88, s40, 32768\n"
    "s_mov_b32 s89, s41\n"
    "s_cmp_lt_u32 %2, 2\n"
    "s_cselect_b32 s96, s42, 0\n"
    "s_mov_b32 s91, s43\n"
    "s_mul_i32 s59, %2, 576\n"
    "s_add_u32 s59, s59, 68608\n"
    "s_bfm_b64 s[60:61], 36, 0\n"
    "v_mov_b32 v72, 0x3f803f80\n"
    "v_mov_b32 v73, 0x3f803f80\n"
    "v_mov_b32 v74, 0x3f803f80\n"
    "v_mov_b32 v75, 0x3f803f80\n"
    "s_mov_b64 s[44:45], %8\n"
    "s_and_b32 s45, s45, 0xffff\n"
    "s_mov_b32 s46, 0x480000\n"
    "s_mov_b32 s47, 0x20000\n"
    "s_add_u32 m0, s54, 0\n"
    "s_add_u32 s55, %6, 0\n"
    "buffer_load_dwordx4 v48, s[44:47], s55 offen lds\n"
    "s_add_u32 m0, s54, 4096\n"
    "s_add_u32 s55, %6, 1048576\n"
    "buffer_load_dwordx4 v48, s[44:47], s55 offen lds\n"
    "s_add_u32 m0, s54, 8192\n"
    "s_add_u32 s55, %6, 2097152\n"
    "buffer_load_dwordx4 v48, s[44:47], s55 offen lds\n"
    "s_add_u32 m0, s54, 12288\n"
    "s_add_u32 s55, %6, 3145728\n"
    "buffer_load_dwordx4 v48, s[44:47], s55 offen lds\n"
    "s_add_u32 m0, s54, 16384\n"
    "s_add_u32 s55, %6, 32768\n"
    "buffer_load_dwordx4 v48, s[44:47], s55 offen lds\n"
    "s_add_u32 m0, s54, 20480\n"
    "s_add_u32 s55, %6, 1081344\n"
    "buffer_load_dwordx4 v48, s[44:47], s55 offen lds\n"
    "s_add_u32 m0, s54, 24576\n"
    "s_add_u32 s55, %6, 2129920\n"
    "buffer_load_dwordx4 v48, s[44:47], s55 offen lds\n"
    "s_add_u32 m0, s54, 28672\n"
    "s_add_u32 s55, %6, 3178496\n"
    "buffer_load_dwordx4 v48, s[44:47], s55 offen lds\n"
    "s_add_u32 m0, s54, 32768\n"
    "s_add_u32 s55, %6, 262144\n"
    "buffer_load_dwordx4 v48, s[44:47], s55 offen lds\n"
    "s_add_u32 m0, s54, 36864\n"
    "s_add_u32 s55, %6, 1310720\n"
    "buffer_load_dwordx4 v48, s[44:47], s55 offen lds\n"
    "s_add_u32 m0, s54, 40960\n"
    "s_add_u32 s55, %6, 2359296\n"
    "buffer_load_dwordx4 v48, s[44:47], s55 offen lds\n"
    "s_add_u32 m0, s54, 45056\n"
    "s_add_u32 s55, %6, 3407872\n"
    "buffer_load_dwordx4 v48, s[44:47], s55 offen lds\n"
    "s_add_u32 m0, s54, 49152\n"
    "s_add_u32 s55, %6, 294912\n"
    "buffer_load_dwordx4 v48, s[44:47], s55 offen lds\n"
    "s_add_u32 m0, s54, 53248\n"
    "s_add_u32 s55, %6, 1343488\n"
    "buffer_load_dwordx4 v48, s[44:47], s55 offen lds\n"
    "s_add_u32 m0, s54, 57344\n"
    "s_add_u32 s55, %6, 2392064\n"
    "buffer_load_dwordx4 v48, s[44:47], s55 offen lds\n"
    "s_add_u32 m0, s54, 61440\n"
    "s_add_u32 s55, %6, 3440640\n"
    "buffer_load_dwordx4 v48, s[44:47], s55 offen lds\n"
    "s_add_u32 m0, s54, 65536\n"
    "s_add_u32 s55, %6, 524288\n"
    "buffer_load_dwordx4 v48, s[44:47], s55 offen lds\n"
    "s_add_u32 m0, s54, 69632\n"
    "s_add_u32 s55, %6, 1572864\n"
    "buffer_load_dwordx4 v48, s[44:47], s55 offen lds\n"
    "s_add_u32 m0, s54, 73728\n"
    "s_add_u32 s55, %6, 2621440\n"
    "buffer_load_dwordx4 v48, s[44:47], s55 offen lds\n"
    "s_add_u32 m0, s54, 77824\n"
    "s_add_u32 s55, %6, 3670016\n"
    "buffer_load_dwordx4 v48, s[44:47], s55 offen lds\n"
    "s_add_u32 m0, s54, 81920\n"
    "s_add_u32 s55, %6, 557056\n"
    "buffer_load_dwordx4 v48, s[44:47], s55 offen lds\n"
    "s_add_u32 m0, s54, 86016\n"
    "s_add_u32 s55, %6, 1605632\n"
    "buffer_load_dwordx4 v48, s[44:47], s55 offen lds\n"
    "s_add_u32 m0, s54, 90112\n"
    "s_add_u32 s55, %6, 2654208\n"
    "buffer_load_dwordx4 v48, s[44:47], s55 offen lds\n"
    "s_add_u32 m0, s54, 94208\n"
    "s_add_u32 s55, %6, 3702784\n"
    "buffer_load_dwordx4 v48, s[44:47], s55 offen lds\n"
    "s_add_u32 m0, s54, 98304\n"
    "s_add_u32 s55, %6, 786432\n"
    "buffer_load_dwordx4 v48, s[44:47], s55 offen lds\n"
    "s_add_u32 m0, s54, 102400\n"
    "s_add_u32 s55, %6, 1835008\n"
    "buffer_load_dwordx4 v48, s[44:47], s55 offen lds\n"
    "s_add_u32 m0, s54, 106496\n"
    "s_add_u32 s55, %6, 2883584\n"
    "buffer_load_dwordx4 v48, s[44:47], s55 offen lds\n"
    "s_add_u32 m0, s54, 110592\n"
    "s_add_u32 s55, %6, 3932160\n"
    "buffer_load_dwordx4 v48, s[44:47], s55 offen lds\n"
    "s_add_u32 m0, s54, 114688\n"
    "s_add_u32 s55, %6, 819200\n"
    "buffer_load_dwordx4 v48, s[44:47], s55 offen lds\n"
    "s_add_u32 m0, s54, 118784\n"
    "s_add_u32 s55, %6, 1867776\n"
    "buffer_load_dwordx4 v48, s[44:47], s55 offen lds\n"
    "s_add_u32 m0, s54, 122880\n"
    "s_add_u32 s55, %6, 2916352\n"
    "buffer_load_dwordx4 v48, s[44:47], s55 offen lds\n"
    "s_add_u32 m0, s54, 126976\n"
    "s_add_u32 s55, %6, 3964928\n"
    "buffer_load_dwordx4 v48, s[44:47], s55 offen lds\n"
    "s_add_u32 m0, s54, 131072\n"
    "s_add_u32 s55, %6, 4194304\n"
    "buffer_load_dwordx4 v48, s[44:47], s55 offen lds\n"
    "s_add_u32 m0, s54, 135168\n"
    "s_add_u32 s55, %6, 4227072\n"
    "buffer_load_dwordx4 v48, s[44:47], s55 offen lds\n"
    "s_add_u32 m0, s54, 139264\n"
    "s_add_u32 s55, %6, 4456448\n"
    "buffer_load_dwordx4 v48, s[44:47], s55 offen lds\n"
    "s_add_u32 m0, s54, 143360\n"
    "s_add_u32 s55, %6, 4489216\n"
    "buffer_load_dwordx4 v48, s[44:47], s55 offen lds\n"
    "s_xor_b32 s50, %2, 0\n"
    "s_lshl_b32 s50, s50, 12\n"
    "v_add_u32 v32, s50, v49\n"
    "v_add_u32 v33, s50, v50\n"
    "s_xor_b32 s50, %2, 1\n"
    "s_lshl_b32 s50, s50, 12\n"
    "v_add_u32 v34, s50, v49\n"
    "v_add_u32 v35, s50, v50\n"
    "s_xor_b32 s50, %2, 2\n"
    "s_lshl_b32 s50, s50, 12\n"
    "v_add_u32 v36, s50, v49\n"
    "v_add_u32 v37, s50, v50\n"
    "s_xor_b32 s50, %2, 3\n"
    "s_lshl_b32 s50, s50, 12\n"
    "v_add_u32 v38, s50, v49\n"
    "v_add_u32 v39, s50, v50\n"
    "s_waitcnt vmcnt(32) lgkmcnt(0)\n"
    "s_barrier\n"
    "ds_read_b64_tr_b16 a[0:1], v32\n"
    "ds_read_b64_tr_b16 a[4:5], v33\n"
    "ds_read_b64_tr_b16 a[32:33], v34\n"
    "ds_read_b64_tr_b16 a[36:37], v35\n"
    "ds_read_b64_tr_b16 a[64:65], v36\n"
    "ds_read_b64_tr_b16 a[68:69], v37\n"
    "ds_read_b64_tr_b16 a[96:97], v38\n"
    "ds_read_b64_tr_b16 a[100:101], v39\n"
    "s_waitcnt vmcnt(28) lgkmcnt(0)\n"
    "s_barrier\n"
    "s_mov_b64 s[44:45], %9\n"
    "s_and_b32 s45, s45, 0xffff\n"
    "s_mov_b32 s46, 0x480000\n"
    "s_mov_b32 s47, 0x20000\n"
    "s_add_u32 m0, s54, 0\n"
    "s_add_u32 s55, %6, 0\n"
    "buffer_load_dwordx4 v48, s[44:47], s55 offen lds\n"
    "s_add_u32 m0, s54, 4096\n"
    "s_add_u32 s55, %6, 1048576\n"
    "buffer_load_dwordx4 v48, s[44:47], s55 offen lds\n"
    "s_add_u32 m0, s54, 8192\n"
    "s_add_u32 s55, %6, 2097152\n"
    "buffer_load_dwordx4 v48, s[44:47], s55 offen lds\n"
    "s_add_u32 m0, s54, 12288\n"
    "s_add_u32 s55, %6, 3145728\n"
    "buffer_load_dwordx4 v48, s[44:47], s55 offen lds\n"
    "ds_read_b64_tr_b16 a[2:3], v32 offset:16384\n"
    "ds_read_b64_tr_b16 a[6:7], v33 offset:16384\n"
    "ds_read_b64_tr_b16 a[34:35], v34 offset:16384\n"
    "ds_read_b64_tr_b16 a[38:39], v35 offset:16384\n"
    "ds_read_b64_tr_b16 a[66:67], v36 offset:16384\n"
    "ds_read_b64_tr_b16 a[70:71], v37 offset:16384\n"
    "ds_read_b64_tr_b16 a[98:99], v38 offset:16384\n"
    "ds_read_b64_tr_b16 a[102:103], v39 offset:16384\n"
    "s_waitcnt vmcnt(28) lgkmcnt(0)\n"
    "s_barrier\n"
    "s_add_u32 m0, s54, 16384\n"
    "s_add_u32 s55, %6, 32768\n"
    "buffer_load_dwordx4 v48, s[44:47], s55 offen lds\n"
    "s_add_u32 m0, s54, 20480\n"
    "s_add_u32 s55, %6, 1081344\n"
    "buffer_load_dwordx4 v48, s[44:47], s55 offen lds\n"
    "s_add_u32 m0, s54, 24576\n"
    "s_add_u32 s55, %6, 2129920\n"
    "buffer_load_dwordx4 v48, s[44:47], s55 offen lds\n"
    "s_add_u32 m0, s54, 28672\n"
    "s_add_u32 s55, %6, 3178496\n"
    "buffer_load_dwordx4 v48, s[44:47], s55 offen lds\n"
    "v_mfma_f32_16x16x32_bf16 v[8:11], a[0:3], v[72:75], 0\n"
    "v_mfma_f32_16x16x32_bf16 v[12:15], a[4:7], v[72:75], 0\n"
    "v_mfma_f32_16x16x32_bf16 v[8:11], a[32:35], v[72:75], v[8:11]\n"
    "v_mfma_f32_16x16x32_bf16 v[12:15], a[36:39], v[72:75], v[12:15]\n"
    "v_mfma_f32_16x16x32_bf16 v[8:11], a[64:67], v[72:75], v[8:11]\n"
    "v_mfma_f32_16x16x32_bf16 v[12:15], a[68:71], v[72:75], v[12:15]\n"
    "v_mfma_f32_16x16x32_bf16 v[8:11], a[96:99], v[72:75], v[8:11]\n"
    "v_mfma_f32_16x16x32_bf16 v[12:15], a[100:103], v[72:75], v[12:15]\n"
    "ds_read_b64_tr_b16 a[8:9], v32 offset:32768\n"
    "ds_read_b64_tr_b16 a[12:13], v33 offset:32768\n"
    "ds_read_b64_tr_b16 a[40:41], v34 offset:32768\n"
    "ds_read_b64_tr_b16 a[44:45], v35 offset:32768\n"
    "ds_read_b64_tr_b16 a[72:73], v36 offset:32768\n"
    "ds_read_b64_tr_b16 a[76:77], v37 offset:32768\n"
    "ds_read_b64_tr_b16 a[104:105], v38 offset:32768\n"
    "ds_read_b64_tr_b16 a[108:109], v39 offset:32768\n"
    "s_waitcnt vmcnt(28) lgkmcnt(0)\n"
    "s_barrier\n"
    "s_add_u32 m0, s54, 32768\n"
    "s_add_u32 s55, %6, 262144\n"
    "buffer_load_dwordx4 v48, s[44:47], s55 offen lds\n"
    "s_add_u32 m0, s54, 36864\n"
    "s_add_u32 s55, %6, 1310720\n"
    "buffer_load_dwordx4 v48, s[44:47], s55 offen lds\n"
    "s_add_u32 m0, s54, 40960\n"
    "s_add_u32 s55, %6, 2359296\n"
    "buffer_load_dwordx4 v48, s[44:47], s55 offen lds\n"
    "s_add_u32 m0, s54, 45056\n"
    "s_add_u32 s55, %6, 3407872\n"
    "buffer_load_dwordx4 v48, s[44:47], s55 offen lds\n"
    "ds_read_b64_tr_b16 a[10:11], v32 offset:49152\n"
    "ds_read_b64_tr_b16 a[14:15], v33 offset:49152\n"
    "ds_read_b64_tr_b16 a[42:43], v34 offset:49152\n"
    "ds_read_b64_tr_b16 a[46:47], v35 offset:49152\n"
    "ds_read_b64_tr_b16 a[74:75], v36 offset:49152\n"
    "ds_read_b64_tr_b16 a[78:79], v37 offset:49152\n"
    "ds_read_b64_tr_b16 a[106:107], v38 offset:49152\n"
    "ds_read_b64_tr_b16 a[110:111], v39 offset:49152\n"
    "s_waitcnt vmcnt(28) lgkmcnt(0)\n"
    "s_barrier\n"
    "s_add_u32 m0, s54, 49152\n"
    "s_add_u32 s55, %6, 294912\n"
    "buffer_load_dwordx4 v48, s[44:47], s55 offen lds\n"
    "s_add_u32 m0, s54, 53248\n"
    "s_add_u32 s55, %6, 1343488\n"
    "buffer_load_dwordx4 v48, s[44:47], s55 offen lds\n"
    "s_add_u32 m0, s54, 57344\n"
    "s_add_u32 s55, %6, 2392064\n"
    "buffer_load_dwordx4 v48, s[44:47], s55 offen lds\n"
    "s_add_u32 m0, s54, 61440\n"
    "s_add_u32 s55, %6, 3440640\n"
    "buffer_load_dwordx4 v48, s[44:47], s55 offen lds\n"
    "v_mfma_f32_16x16x32_bf16 v[8:11], a[8:11], v[72:75], v[8:11]\n"
    "v_mfma_f32_16x16x32_bf16 v[12:15], a[12:15], v[72:75], v[12:15]\n"
    "v_mfma_f32_16x16x32_bf16 v[8:11], a[40:43], v[72:75], v[8:11]\n"
    "v_mfma_f32_16x16x32_bf16 v[12:15], a[44:47], v[72:75], v[12:15]\n"
    "v_mfma_f32_16x16x32_bf16 v[8:11], a[72:75], v[72:75], v[8:11]\n"
    "v_mfma_f32_16x16x32_bf16 v[12:15], a[76:79], v[72:75], v[12:15]\n"
    "v_mfma_f32_16x16x32_bf16 v[8:11], a[104:107], v[72:75], v[8:11]\n"
    "v_mfma_f32_16x16x32_bf16 v[12:15], a[108:111], v[72:75], v[12:15]\n"
    "v_add_u32 v71, 0x10000, v32\n"
    "ds_read_b64_tr_b16 a[16:17], v71\n"
    "v_add_u32 v70, 0x10000, v33\n"
    "ds_read_b64_tr_b16 a[20:21], v70\n"
    "v_add_u32 v69, 0x10000, v34\n"
    "ds_read_b64_tr_b16 a[48:49], v69\n"
    "v_add_u32 v68, 0x10000, v35\n"
    "ds_read_b64_tr_b16 a[52:53], v68\n"
    "v_add_u32 v67, 0x10000, v36\n"
    "ds_read_b64_tr_b16 a[80:81], v67\n"
    "v_add_u32 v66, 0x10000, v37\n"
    "ds_read_b64_tr_b16 a[84:85], v66\n"
    "v_add_u32 v65, 0x10000, v38\n"
    "ds_read_b64_tr_b16 a[112:113], v65\n"
    "v_add_u32 v64, 0x10000, v39\n"
    "ds_read_b64_tr_b16 a[116:117], v64\n"
    "s_waitcnt vmcnt(28) lgkmcnt(0)\n"
    "s_barrier\n"
    "s_add_u32 m0, s54, 65536\n"
    "s_add_u32 s55, %6, 524288\n"
    "buffer_load_dwordx4 v48, s[44:47], s55 offen lds\n"
    "s_add_u32 m0, s54, 69632\n"
    "s_add_u32 s55, %6, 1572864\n"
    "buffer_load_dwordx4 v48, s[44:47], s55 offen lds\n"
    "s_add_u32 m0, s54, 73728\n"
    "s_add_u32 s55, %6, 2621440\n"
    "buffer_load_dwordx4 v48, s[44:47], s55 offen lds\n"
    "s_add_u32 m0, s54, 77824\n"
    "s_add_u32 s55, %6, 3670016\n"
    "buffer_load_dwordx4 v48, s[44:47], s55 offen lds\n"
    "ds_read_b64_tr_b16 a[18:19], v71 offset:16384\n"
    "ds_read_b64_tr_b16 a[22:23], v70 offset:16384\n"
    "ds_read_b64_tr_b16 a[50:51], v69 offset:16384\n"
    "ds_read_b64_tr_b16 a[54:55], v68 offset:16384\n"
    "ds_read_b64_tr_b16 a[82:83], v67 offset:16384\n"
    "ds_read_b64_tr_b16 a[86:87], v66 offset:16384\n"
    "ds_read_b64_tr_b16 a[114:115], v65 offset:16384\n"
    "ds_read_b64_tr_b16 a[118:119], v64 offset:16384\n"
    "s_waitcnt vmcnt(28) lgkmcnt(0)\n"
    "s_barrier\n"
    "s_add_u32 m0, s54, 81920\n"
    "s_add_u32 s55, %6, 557056\n"
    "buffer_load_dwordx4 v48, s[44:47], s55 offen lds\n"
    "s_add_u32 m0, s54, 86016\n"
    "s_add_u32 s55, %6, 1605632\n"
    "buffer_load_dwordx4 v48, s[44:47], s55 offen lds\n"
    "s_add_u32 m0, s54, 90112\n"
    "s_add_u32 s55, %6, 2654208\n"
    "buffer_load_dwordx4 v48, s[44:47], s55 offen lds\n"
    "s_add_u32 m0, s54, 94208\n"
    "s_add_u32 s55, %6, 3702784\n"
    "buffer_load_dwordx4 v48, s[44:47], s55 offen lds\n"
    "v_mfma_f32_16x16x32_bf16 v[8:11], a[16:19], v[72:75], v[8:11]\n"
    "v_mfma_f32_16x16x32_bf16 v[12:15], a[20:23], v[72:75], v[12:15]\n"
    "v_mfma_f32_16x16x32_bf16 v[8:11], a[48:51], v[72:75], v[8:11]\n"
    "v_mfma_f32_16x16x32_bf16 v[12:15], a[52:55], v[72:75], v[12:15]\n"
    "v_mfma_f32_16x16x32_bf16 v[8:11], a[80:83], v[72:75], v[8:11]\n"
    "v_mfma_f32_16x16x32_bf16 v[12:15], a[84:87], v[72:75], v[12:15]\n"
    "v_mfma_f32_16x16x32_bf16 v[8:11], a[112:115], v[72:75], v[8:11]\n"
    "v_mfma_f32_16x16x32_bf16 v[12:15], a[116:119], v[72:75], v[12:15]\n"
    "ds_read_b64_tr_b16 a[24:25], v71 offset:32768\n"
    "ds_read_b64_tr_b16 a[28:29], v70 offset:32768\n"
    "ds_read_b64_tr_b16 a[56:57], v69 offset:32768\n"
    "ds_read_b64_tr_b16 a[60:61], v68 offset:32768\n"
    "ds_read_b64_tr_b16 a[88:89], v67 offset:32768\n"
    "ds_read_b64_tr_b16 a[92:93], v66 offset:32768\n"
    "ds_read_b64_tr_b16 a[120:121], v65 offset:32768\n"
    "ds_read_b64_tr_b16 a[124:125], v64 offset:32768\n"
    "s_waitcnt vmcnt(28) lgkmcnt(0)\n"
    "s_barrier\n"
    "s_add_u32 m0, s54, 98304\n"
    "s_add_u32 s55, %6, 786432\n"
    "buffer_load_dwordx4 v48, s[44:47], s55 offen lds\n"
    "s_add_u32 m0, s54, 102400\n"
    "s_add_u32 s55, %6, 1835008\n"
    "buffer_load_dwordx4 v48, s[44:47], s55 offen lds\n"
    "s_add_u32 m0, s54, 106496\n"
    "s_add_u32 s55, %6, 2883584\n"
    "buffer_load_dwordx4 v48, s[44:47], s55 offen lds\n"
    "s_add_u32 m0, s54, 110592\n"
    "s_add_u32 s55, %6, 3932160\n"
    "buffer_load_dwordx4 v48, s[44:47], s55 offen lds\n"
    "ds_read_b64_tr_b16 a[26:27], v71 offset:49152\n"
    "ds_read_b64_tr_b16 a[30:31], v70 offset:49152\n"
    "ds_read_b64_tr_b16 a[58:59], v69 offset:49152\n"
    "ds_read_b64_tr_b16 a[62:63], v68 offset:49152\n"
    "ds_read_b64_tr_b16 a[90:91], v67 offset:49152\n"
    "ds_read_b64_tr_b16 a[94:95], v66 offset:49152\n"
    "ds_read_b64_tr_b16 a[122:123], v65 offset:49152\n"
    "ds_read_b64_tr_b16 a[126:127], v64 offset:49152\n"
    "s_waitcnt vmcnt(28) lgkmcnt(0)\n"
    "s_barrier\n"
    "s_add_u32 m0, s54, 114688\n"
    "s_add_u32 s55, %6, 819200\n"
    "buffer_load_dwordx4 v48, s[44:47], s55 offen lds\n"
    "s_add_u32 m0, s54, 118784\n"
    "s_add_u32 s55, %6, 1867776\n"
    "buffer_load_dwordx4 v48, s[44:47], s55 offen lds\n"
    "s_add_u32 m0, s54, 122880\n"
    "s_add_u32 s55, %6, 2916352\n"
    "buffer_load_dwordx4 v48, s[44:47], s55 offen lds\n"
    "s_add_u32 m0, s54, 126976\n"
    "s_add_u32 s55, %6, 3964928\n"
    "buffer_load_dwordx4 v48, s[44:47], s55 offen lds\n"
    "v_mfma_f32_16x16x32_bf16 v[8:11], a[24:27], v[72:75], v[8:11]\n"
    "v_mfma_f32_16x16x32_bf16 v[12:15], a[28:31], v[72:75], v[12:15]\n"
    "v_mfma_f32_16x16x32_bf16 v[8:11], a[56:59], v[72:75], v[8:11]\n"
    "v_mfma_f32_16x16x32_bf16 v[12:15], a[60:63], v[72:75], v[12:15]\n"
    "v_mfma_f32_16x16x32_bf16 v[8:11], a[88:91], v[72:75], v[8:11]\n"
    "v_mfma_f32_16x16x32_bf16 v[12:15], a[92:95], v[72:75], v[12:15]\n"
    "v_mfma_f32_16x16x32_bf16 v[8:11], a[120:123], v[72:75], v[8:11]\n"
    "v_mfma_f32_16x16x32_bf16 v[12:15], a[124:127], v[72:75], v[12:15]\n"
    "v_add_u32 v63, 0x20000, v49\n"
    "ds_read_b64_tr_b16 a[128:129], v63\n"
    "v_add_u32 v62, 0x20000, v50\n"
    "ds_read_b64_tr_b16 a[132:133], v62\n"
    "ds_read_b64_tr_b16 a[130:131], v63 offset:4096\n"
    "ds_read_b64_tr_b16 a[134:135], v62 offset:4096\n"
    "ds_read_b64_tr_b16 a[136:137], v63 offset:8192\n"
    "ds_read_b64_tr_b16 a[140:141], v62 offset:8192\n"
    "ds_read_b64_tr_b16 a[138:139], v63 offset:12288\n"
    "ds_read_b64_tr_b16 a[142:143], v62 offset:12288\n"
    "s_waitcnt vmcnt(28) lgkmcnt(0)\n"
    "s_barrier\n"
    "s_add_u32 m0, s54, 131072\n"
    "s_add_u32 s55, %6, 4194304\n"
    "buffer_load_dwordx4 v48, s[44:47], s55 offen lds\n"
    "s_add_u32 m0, s54, 135168\n"
    "s_add_u32 s55, %6, 4227072\n"
    "buffer_load_dwordx4 v48, s[44:47], s55 offen lds\n"
    "s_add_u32 m0, s54, 139264\n"
    "s_add_u32 s55, %6, 4456448\n"
    "buffer_load_dwordx4 v48, s[44:47], s55 offen lds\n"
    "s_add_u32 m0, s54, 143360\n"
    "s_add_u32 s55, %6, 4489216\n"
    "buffer_load_dwordx4 v48, s[44:47], s55 offen lds\n"
    "v_mfma_f32_16x16x32_bf16 v[8:11], a[128:131], v[72:75], v[8:11]\n"
    "v_mfma_f32_16x16x32_bf16 v[12:15], a[132:135], v[72:75], v[12:15]\n"
    "v_mfma_f32_16x16x32_bf16 v[8:11], a[136:139], v[72:75], v[8:11]\n"
    "v_mfma_f32_16x16x32_bf16 v[12:15], a[140:143], v[72:75], v[12:15]\n"
    "ds_read_b64_tr_b16 a[144:145], v32\n"
    "ds_read_b64_tr_b16 a[148:149], v33\n"
    "ds_read_b64_tr_b16 a[176:177], v34\n"
    "ds_read_b64_tr_b16 a[180:181], v35\n"
    "ds_read_b64_tr_b16 a[208:209], v36\n"
    "ds_read_b64_tr_b16 a[212:213], v37\n"
    "ds_read_b64_tr_b16 a[240:241], v38\n"
    "ds_read_b64_tr_b16 a[244:245], v39\n"
    "s_waitcnt vmcnt(28) lgkmcnt(0)\n"
    "s_barrier\n"
    "s_mov_b64 s[44:45], %10\n"
    "s_and_b32 s45, s45, 0xffff\n"
    "s_mov_b32 s46, 0x480000\n"
    "s_mov_b32 s47, 0x20000\n"
    "s_add_u32 m0, s54, 0\n"
    "s_add_u32 s55, %6, 0\n"
    "buffer_load_dwordx4 v48, s[44:47], s55 offen lds\n"
    "s_add_u32 m0, s54, 4096\n"
    "s_add_u32 s55, %6, 1048576\n"
    "buffer_load_dwordx4 v48, s[44:47], s55 offen lds\n"
    "s_add_u32 m0, s54, 8192\n"
    "s_add_u32 s55, %6, 2097152\n"
    "buffer_load_dwordx4 v48, s[44:47], s55 offen lds\n"
    "s_add_u32 m0, s54, 12288\n"
    "s_add_u32 s55, %6, 3145728\n"
    "buffer_load_dwordx4 v48, s[44:47], s55 offen lds\n"
    "ds_read_b64_tr_b16 a[146:147], v32 offset:16384\n"
    "ds_read_b64_tr_b16 a[150:151], v33 offset:16384\n"
    "ds_read_b64_tr_b16 a[178:179], v34 offset:16384\n"
    "ds_read_b64_tr_b16 a[182:183], v35 offset:16384\n"
    "ds_read_b64_tr_b16 a[210:211], v36 offset:16384\n"
    "ds_read_b64_tr_b16 a[214:215], v37 offset:16384\n"
    "ds_read_b64_tr_b16 a[242:243], v38 offset:16384\n"
    "ds_read_b64_tr_b16 a[246:247], v39 offset:16384\n"
    "s_waitcnt vmcnt(28) lgkmcnt(0)\n"
    "s_barrier\n"
    "s_add_u32 m0, s54, 16384\n"
    "s_add_u32 s55, %6, 32768\n"
    "buffer_load_dwordx4 v48, s[44:47], s55 offen lds\n"
    "s_add_u32 m0, s54, 20480\n"
    "s_add_u32 s55, %6, 1081344\n"
    "buffer_load_dwordx4 v48, s[44:47], s55 offen lds\n"
    "s_add_u32 m0, s54, 24576\n"
    "s_add_u32 s55, %6, 2129920\n"
    "buffer_load_dwordx4 v48, s[44:47], s55 offen lds\n"
    "s_add_u32 m0, s54, 28672\n"
    "s_add_u32 s55, %6, 3178496\n"
    "buffer_load_dwordx4 v48, s[44:47], s55 offen lds\n"
    "v_mfma_f32_16x16x32_bf16 v[16:19], a[144:147], v[72:75], 0\n"
    "v_mfma_f32_16x16x32_bf16 v[20:23], a[148:151], v[72:75], 0\n"
    "v_mfma_f32_16x16x32_bf16 v[16:19], a[176:179], v[72:75], v[16:19]\n"
    "v_mfma_f32_16x16x32_bf16 v[20:23], a[180:183], v[72:75], v[20:23]\n"
    "v_mfma_f32_16x16x32_bf16 v[16:19], a[208:211], v[72:75], v[16:19]\n"
    "v_mfma_f32_16x16x32_bf16 v[20:23], a[212:215], v[72:75], v[20:23]\n"
    "v_mfma_f32_16x16x32_bf16 v[16:19], a[240:243], v[72:75], v[16:19]\n"
    "v_mfma_f32_16x16x32_bf16 v[20:23], a[244:247], v[72:75], v[20:23]\n"
    "ds_read_b64_tr_b16 a[152:153], v32 offset:32768\n"
    "ds_read_b64_tr_b16 a[156:157], v33 offset:32768\n"
    "ds_read_b64_tr_b16 a[184:185], v34 offset:32768\n"
    "ds_read_b64_tr_b16 a[188:189], v35 offset:32768\n"
    "ds_read_b64_tr_b16 a[216:217], v36 offset:32768\n"
    "ds_read_b64_tr_b16 a[220:221], v37 offset:32768\n"
    "ds_read_b64_tr_b16 a[248:249], v38 offset:32768\n"
    "ds_read_b64_tr_b16 a[252:253], v39 offset:32768\n"
    "s_waitcnt vmcnt(28) lgkmcnt(0)\n"
    "s_barrier\n"
    "s_add_u32 m0, s54, 32768\n"
    "s_add_u32 s55, %6, 262144\n"
    "buffer_load_dwordx4 v48, s[44:47], s55 offen lds\n"
    "s_add_u32 m0, s54, 36864\n"
    "s_add_u32 s55, %6, 1310720\n"
    "buffer_load_dwordx4 v48, s[44:47], s55 offen lds\n"
    "s_add_u32 m0, s54, 40960\n"
    "s_add_u32 s55, %6, 2359296\n"
    "buffer_load_dwordx4 v48, s[44:47], s55 offen lds\n"
    "s_add_u32 m0, s54, 45056\n"
    "s_add_u32 s55, %6, 3407872\n"
    "buffer_load_dwordx4 v48, s[44:47], s55 offen lds\n"
    "ds_read_b64_tr_b16 a[154:155], v32 offset:49152\n"
    "ds_read_b64_tr_b16 a[158:159], v33 offset:49152\n"
    "ds_read_b64_tr_b16 a[186:187], v34 offset:49152\n"
    "ds_read_b64_tr_b16 a[190:191], v35 offset:49152\n"
    "ds_read_b64_tr_b16 a[218:219], v36 offset:49152\n"
    "ds_read_b64_tr_b16 a[222:223], v37 offset:49152\n"
    "ds_read_b64_tr_b16 a[250:251], v38 offset:49152\n"
    "ds_read_b64_tr_b16 a[254:255], v39 offset:49152\n"
    "s_waitcnt vmcnt(28) lgkmcnt(0)\n"
    "s_barrier\n"
    "s_add_u32 m0, s54, 49152\n"
    "s_add_u32 s55, %6, 294912\n"
    "buffer_load_dwordx4 v48, s[44:47], s55 offen lds\n"
    "s_add_u32 m0, s54, 53248\n"
    "s_add_u32 s55, %6, 1343488\n"
    "buffer_load_dwordx4 v48, s[44:47], s55 offen lds\n"
    "s_add_u32 m0, s54, 57344\n"
    "s_add_u32 s55, %6, 2392064\n"
    "buffer_load_dwordx4 v48, s[44:47], s55 offen lds\n"
    "s_add_u32 m0, s54, 61440\n"
    "s_add_u32 s55, %6, 3440640\n"
    "buffer_load_dwordx4 v48, s[44:47], s55 offen lds\n"
    "v_mfma_f32_16x16x32_bf16 v[16:19], a[152:155], v[72:75], v[16:19]\n"
    "v_mfma_f32_16x16x32_bf16 v[20:23], a[156:159], v[72:75], v[20:23]\n"
    "v_mfma_f32_16x16x32_bf16 v[16:19], a[184:187], v[72:75], v[16:19]\n"
    "v_mfma_f32_16x16x32_bf16 v[20:23], a[188:191], v[72:75], v[20:23]\n"
    "v_mfma_f32_16x16x32_bf16 v[16:19], a[216:219], v[72:75], v[16:19]\n"
    "v_mfma_f32_16x16x32_bf16 v[20:23], a[220:223], v[72:75], v[20:23]\n"
    "v_mfma_f32_16x16x32_bf16 v[16:19], a[248:251], v[72:75], v[16:19]\n"
    "v_mfma_f32_16x16x32_bf16 v[20:23], a[252:255], v[72:75], v[20:23]\n"
    "ds_read_b64_tr_b16 a[160:161], v71\n"
    "ds_read_b64_tr_b16 a[164:165], v70\n"
    "ds_read_b64_tr_b16 a[192:193], v69\n"
    "ds_read_b64_tr_b16 a[196:197], v68\n"
    "ds_read_b64_tr_b16 a[224:225], v67\n"
    "ds_read_b64_tr_b16 a[228:229], v66\n"
    "ds_read_b64_tr_b16 v[84:85], v65\n"
    "ds_read_b64_tr_b16 v[88:89], v64\n"
    "s_waitcnt vmcnt(28) lgkmcnt(0)\n"
    "s_barrier\n"
    "s_add_u32 m0, s54, 65536\n"
    "s_add_u32 s55, %6, 524288\n"
    "buffer_load_dwordx4 v48, s[44:47], s55 offen lds\n"
    "s_add_u32 m0, s54, 69632\n"
    "s_add_u32 s55, %6, 1572864\n"
    "buffer_load_dwordx4 v48, s[44:47], s55 offen lds\n"
    "s_add_u32 m0, s54, 73728\n"
    "s_add_u32 s55, %6, 2621440\n"
    "buffer_load_dwordx4 v48, s[44:47], s55 offen lds\n"
    "s_add_u32 m0, s54, 77824\n"
    "s_add_u32 s55, %6, 3670016\n"
    "buffer_load_dwordx4 v48, s[44:47], s55 offen lds\n"
    "ds_read_b64_tr_b16 a[162:163], v71 offset:16384\n"
    "ds_read_b64_tr_b16 a[166:167], v70 offset:16384\n"
    "ds_read_b64_tr_b16 a[194:195], v69 offset:16384\n"
    "ds_read_b64_tr_b16 a[198:199], v68 offset:16384\n"
    "ds_read_b64_tr_b16 a[226:227], v67 offset:16384\n"
    "ds_read_b64_tr_b16 a[230:231], v66 offset:16384\n"
    "ds_read_b64_tr_b16 v[86:87], v65 offset:16384\n"
    "ds_read_b64_tr_b16 v[90:91], v64 offset:16384\n"
    "s_waitcnt vmcnt(28) lgkmcnt(0)\n"
    "s_barrier\n"
    "s_add_u32 m0, s54, 81920\n"
    "s_add_u32 s55, %6, 557056\n"
    "buffer_load_dwordx4 v48, s[44:47], s55 offen lds\n"
    "s_add_u32 m0, s54, 86016\n"
    "s_add_u32 s55, %6, 1605632\n"
    "buffer_load_dwordx4 v48, s[44:47], s55 offen lds\n"
    "s_add_u32 m0, s54, 90112\n"
    "s_add_u32 s55, %6, 2654208\n"
    "buffer_load_dwordx4 v48, s[44:47], s55 offen lds\n"
    "s_add_u32 m0, s54, 94208\n"
    "s_add_u32 s55, %6, 3702784\n"
    "buffer_load_dwordx4 v48, s[44:47], s55 offen lds\n"
    "v_mfma_f32_16x16x32_bf16 v[16:19], a[160:163], v[72:75], v[16:19]\n"
    "v_mfma_f32_16x16x32_bf16 v[20:23], a[164:167], v[72:75], v[20:23]\n"
    "v_mfma_f32_16x16x32_bf16 v[16:19], a[192:195], v[72:75], v[16:19]\n"
    "v_mfma_f32_16x16x32_bf16 v[20:23], a[196:199], v[72:75], v[20:23]\n"
    "v_mfma_f32_16x16x32_bf16 v[16:19], a[224:227], v[72:75], v[16:19]\n"
    "v_mfma_f32_16x16x32_bf16 v[20:23], a[228:231], v[72:75], v[20:23]\n"
    "v_mfma_f32_16x16x32_bf16 v[16:19], v[84:87], v[72:75], v[16:19]\n"
    "v_mfma_f32_16x16x32_bf16 v[20:23], v[88:91], v[72:75], v[20:23]\n"
    "ds_read_b64_tr_b16 a[168:169], v71 offset:32768\n"
    "ds_read_b64_tr_b16 a[172:173], v70 offset:32768\n"
    "ds_read_b64_tr_b16 a[200:201], v69 offset:32768\n"
    "ds_read_b64_tr_b16 a[204:205], v68 offset:32768\n"
    "ds_read_b64_tr_b16 a[232:233], v67 offset:32768\n"
    "ds_read_b64_tr_b16 a[236:237], v66 offset:32768\n"
    "ds_read_b64_tr_b16 v[92:93], v65 offset:32768\n"
    "ds_read_b64_tr_b16 v[96:97], v64 offset:32768\n"
    "s_waitcnt vmcnt(28) lgkmcnt(0)\n"
    "s_barrier\n"
    "s_add_u32 m0, s54, 98304\n"
    "s_add_u32 s55, %6, 786432\n"
    "buffer_load_dwordx4 v48, s[44:47], s55 offen lds\n"
    "s_add_u32 m0, s54, 102400\n"
    "s_add_u32 s55, %6, 1835008\n"
    "buffer_load_dwordx4 v48, s[44:47], s55 offen lds\n"
    "s_add_u32 m0, s54, 106496\n"
    "s_add_u32 s55, %6, 2883584\n"
    "buffer_load_dwordx4 v48, s[44:47], s55 offen lds\n"
    "s_add_u32 m0, s54, 110592\n"
    "s_add_u32 s55, %6, 3932160\n"
    "buffer_load_dwordx4 v48, s[44:47], s55 offen lds\n"
    "ds_read_b64_tr_b16 a[170:171], v71 offset:49152\n"
    "ds_read_b64_tr_b16 a[174:175], v70 offset:49152\n"
    "ds_read_b64_tr_b16 a[202:203], v69 offset:49152\n"
    "ds_read_b64_tr_b16 a[206:207], v68 offset:49152\n"
    "ds_read_b64_tr_b16 a[234:235], v67 offset:49152\n"
    "ds_read_b64_tr_b16 a[238:239], v66 offset:49152\n"
    "ds_read_b64_tr_b16 v[94:95], v65 offset:49152\n"
    "ds_read_b64_tr_b16 v[98:99], v64 offset:49152\n"
    "s_waitcnt vmcnt(28) lgkmcnt(0)\n"
    "s_barrier\n"
    "s_add_u32 m0, s54, 114688\n"
    "s_add_u32 s55, %6, 819200\n"
    "buffer_load_dwordx4 v48, s[44:47], s55 offen lds\n"
    "s_add_u32 m0, s54, 118784\n"
    "s_add_u32 s55, %6, 1867776\n"
    "buffer_load_dwordx4 v48, s[44:47], s55 offen lds\n"
    "s_add_u32 m0, s54, 122880\n"
    "s_add_u32 s55, %6, 2916352\n"
    "buffer_load_dwordx4 v48, s[44:47], s55 offen lds\n"
    "s_add_u32 m0, s54, 126976\n"
    "s_add_u32 s55, %6, 3964928\n"
    "buffer_load_dwordx4 v48, s[44:47], s55 offen lds\n"
    "v_mfma_f32_16x16x32_bf16 v[16:19], a[168:171], v[72:75], v[16:19]\n"
    "v_mfma_f32_16x16x32_bf16 v[20:23], a[172:175], v[72:75], v[20:23]\n"
    "v_mfma_f32_16x16x32_bf16 v[16:19], a[200:203], v[72:75], v[16:19]\n"
    "v_mfma_f32_16x16x32_bf16 v[20:23], a[204:207], v[72:75], v[20:23]\n"
    "v_mfma_f32_16x16x32_bf16 v[16:19], a[232:235], v[72:75], v[16:19]\n"
    "v_mfma_f32_16x16x32_bf16 v[20:23], a[236:239], v[72:75], v[20:23]\n"
    "v_mfma_f32_16x16x32_bf16 v[16:19], v[92:95], v[72:75], v[16:19]\n"
    "v_mfma_f32_16x16x32_bf16 v[20:23], v[96:99], v[72:75], v[20:23]\n"
    "ds_read_b64_tr_b16 v[100:101], v63\n"
    "ds_read_b64_tr_b16 v[104:105], v62\n"
    "ds_read_b64_tr_b16 v[102:103], v63 offset:4096\n"
    "ds_read_b64_tr_b16 v[106:107], v62 offset:4096\n"
    "ds_read_b64_tr_b16 v[108:109], v63 offset:8192\n"
    "ds_read_b64_tr_b16 v[112:113], v62 offset:8192\n"
    "ds_read_b64_tr_b16 v[110:111], v63 offset:12288\n"
    "ds_read_b64_tr_b16 v[114:115], v62 offset:12288\n"
    "s_waitcnt vmcnt(28) lgkmcnt(0)\n"
    "s_barrier\n"
    "s_add_u32 m0, s54, 131072\n"
    "s_add_u32 s55, %6, 4194304\n"
    "buffer_load_dwordx4 v48, s[44:47], s55 offen lds\n"
    "s_add_u32 m0, s54, 135168\n"
    "s_add_u32 s55, %6, 4227072\n"
    "buffer_load_dwordx4 v48, s[44:47], s55 offen lds\n"
    "s_add_u32 m0, s54, 139264\n"
    "s_add_u32 s55, %6, 4456448\n"
    "buffer_load_dwordx4 v48, s[44:47], s55 offen lds\n"
    "s_add_u32 m0, s54, 143360\n"
    "s_add_u32 s55, %6, 4489216\n"
    "buffer_load_dwordx4 v48, s[44:47], s55 offen lds\n"
    "v_mov_b32 v7, 0x20400\n"
    "global_load_dwordx2 v[80:81], v7, %1 sc1\n"
    "v_mbcnt_lo_u32_b32 v4, -1, 0\n"
    "v_min_u32 v4, 7, v4\n"
    "v_lshlrev_b32 v4, 7, v4\n"
    "v_add_u32 v4, 0x20000, v4\n"
    "global_load_dwordx2 v[82:83], v4, %1 sc1\n"
    "v_mfma_f32_16x16x32_bf16 v[16:19], v[100:103], v[72:75], v[16:19]\n"
    "v_mfma_f32_16x16x32_bf16 v[20:23], v[104:107], v[72:75], v[20:23]\n"
    "v_mfma_f32_16x16x32_bf16 v[16:19], v[108:111], v[72:75], v[16:19]\n"
    "v_mfma_f32_16x16x32_bf16 v[20:23], v[112:115], v[72:75], v[20:23]\n"
    "ds_read_b64_tr_b16 v[116:117], v32\n"
    "ds_read_b64_tr_b16 v[120:121], v33\n"
    "ds_read_b64_tr_b16 v[148:149], v34\n"
    "ds_read_b64_tr_b16 v[152:153], v35\n"
    "ds_read_b64_tr_b16 v[180:181], v36\n"
    "ds_read_b64_tr_b16 v[184:185], v37\n"
    "ds_read_b64_tr_b16 v[212:213], v38\n"
    "ds_read_b64_tr_b16 v[216:217], v39\n"
    "s_waitcnt vmcnt(30) lgkmcnt(0)\n"
    "s_barrier\n"
    "ds_read_b64_tr_b16 v[118:119], v32 offset:16384\n"
    "ds_read_b64_tr_b16 v[122:123], v33 offset:16384\n"
    "ds_read_b64_tr_b16 v[150:151], v34 offset:16384\n"
    "ds_read_b64_tr_b16 v[154:155], v35 offset:16384\n"
    "ds_read_b64_tr_b16 v[182:183], v36 offset:16384\n"
    "ds_read_b64_tr_b16 v[186:187], v37 offset:16384\n"
    "ds_read_b64_tr_b16 v[214:215], v38 offset:16384\n"
    "ds_read_b64_tr_b16 v[218:219], v39 offset:16384\n"
    "s_waitcnt vmcnt(26) lgkmcnt(0)\n"
    "s_barrier\n"
    "v_mfma_f32_16x16x32_bf16 v[24:27], v[72:75], v[116:119], 0\n"
    "v_mfma_f32_16x16x32_bf16 v[28:31], v[72:75], v[120:123], 0\n"
    "v_mfma_f32_16x16x32_bf16 v[24:27], v[72:75], v[148:151], v[24:27]\n"
    "v_mfma_f32_16x16x32_bf16 v[28:31], v[72:75], v[152:155], v[28:31]\n"
    "v_mfma_f32_16x16x32_bf16 v[24:27], v[72:75], v[180:183], v[24:27]\n"
    "v_mfma_f32_16x16x32_bf16 v[28:31], v[72:75], v[184:187], v[28:31]\n"
    "v_mfma_f32_16x16x32_bf16 v[24:27], v[72:75], v[212:215], v[24:27]\n"
    "v_mfma_f32_16x16x32_bf16 v[28:31], v[72:75], v[216:219], v[28:31]\n"
    "ds_read_b64_tr_b16 v[124:125], v32 offset:32768\n"
    "ds_read_b64_tr_b16 v[128:129], v33 offset:32768\n"
    "ds_read_b64_tr_b16 v[156:157], v34 offset:32768\n"
    "ds_read_b64_tr_b16 v[160:161], v35 offset:32768\n"
    "ds_read_b64_tr_b16 v[188:189], v36 offset:32768\n"
    "ds_read_b64_tr_b16 v[192:193], v37 offset:32768\n"
    "ds_read_b64_tr_b16 v[220:221], v38 offset:32768\n"
    "ds_read_b64_tr_b16 v[224:225], v39 offset:32768\n"
    "s_waitcnt vmcnt(22) lgkmcnt(0)\n"
    "s_barrier\n"
    "ds_read_b64_tr_b16 v[126:127], v32 offset:49152\n"
    "ds_read_b64_tr_b16 v[130:131], v33 offset:49152\n"
    "ds_read_b64_tr_b16 v[158:159], v34 offset:49152\n"
    "ds_read_b64_tr_b16 v[162:163], v35 offset:49152\n"
    "ds_read_b64_tr_b16 v[190:191], v36 offset:49152\n"
    "ds_read_b64_tr_b16 v[194:195], v37 offset:49152\n"
    "ds_read_b64_tr_b16 v[222:223], v38 offset:49152\n"
    "ds_read_b64_tr_b16 v[226:227], v39 offset:49152\n"
    "s_waitcnt vmcnt(18) lgkmcnt(0)\n"
    "s_barrier\n"
    "v_mfma_f32_16x16x32_bf16 v[24:27], v[72:75], v[124:127], v[24:27]\n"
    "v_mfma_f32_16x16x32_bf16 v[28:31], v[72:75], v[128:131], v[28:31]\n"
    "v_mfma_f32_16x16x32_bf16 v[24:27], v[72:75], v[156:159], v[24:27]\n"
    "v_mfma_f32_16x16x32_bf16 v[28:31], v[72:75], v[160:163], v[28:31]\n"
    "v_mfma_f32_16x16x32_bf16 v[24:27], v[72:75], v[188:191], v[24:27]\n"
    "v_mfma_f32_16x16x32_bf16 v[28:31], v[72:75], v[192:195], v[28:31]\n"
    "v_mfma_f32_16x16x32_bf16 v[24:27], v[72:75], v[220:223], v[24:27]\n"
    "v_mfma_f32_16x16x32_bf16 v[28:31], v[72:75], v[224:227], v[28:31]\n"
    "ds_read_b64_tr_b16 v[132:133], v71\n"
    "ds_read_b64_tr_b16 v[136:137], v70\n"
    "ds_read_b64_tr_b16 v[164:165], v69\n"
    "ds_read_b64_tr_b16 v[168:169], v68\n"
    "ds_read_b64_tr_b16 v[196:197], v67\n"
    "ds_read_b64_tr_b16 v[200:201], v66\n"
    "ds_read_b64_tr_b16 v[228:229], v65\n"
    "ds_read_b64_tr_b16 v[232:233], v64\n"
    "s_waitcnt vmcnt(14) lgkmcnt(0)\n"
    "s_barrier\n"
    "ds_read_b64_tr_b16 v[134:135], v71 offset:16384\n"
    "ds_read_b64_tr_b16 v[138:139], v70 offset:16384\n"
    "ds_read_b64_tr_b16 v[166:167], v69 offset:16384\n"
    "ds_read_b64_tr_b16 v[170:171], v68 offset:16384\n"
    "ds_read_b64_tr_b16 v[198:199], v67 offset:16384\n"
    "ds_read_b64_tr_b16 v[202:203], v66 offset:16384\n"
    "ds_read_b64_tr_b16 v[230:231], v65 offset:16384\n"
    "ds_read_b64_tr_b16 v[234:235], v64 offset:16384\n"
    "s_waitcnt vmcnt(10) lgkmcnt(0)\n"
    "s_barrier\n"
    "v_mfma_f32_16x16x32_bf16 v[24:27], v[72:75], v[132:135], v[24:27]\n"
    "v_mfma_f32_16x16x32_bf16 v[28:31], v[72:75], v[136:139], v[28:31]\n"
    "v_mfma_f32_16x16x32_bf16 v[24:27], v[72:75], v[164:167], v[24:27]\n"
    "v_mfma_f32_16x16x32_bf16 v[28:31], v[72:75], v[168:171], v[28:31]\n"
    "v_mfma_f32_16x16x32_bf16 v[24:27], v[72:75], v[196:199], v[24:27]\n"
    "v_mfma_f32_16x16x32_bf16 v[28:31], v[72:75], v[200:203], v[28:31]\n"
    "v_mfma_f32_16x16x32_bf16 v[24:27], v[72:75], v[228:231], v[24:27]\n"
    "v_mfma_f32_16x16x32_bf16 v[28:31], v[72:75], v[232:235], v[28:31]\n"
    "ds_read_b64_tr_b16 v[140:141], v71 offset:32768\n"
    "ds_read_b64_tr_b16 v[144:145], v70 offset:32768\n"
    "ds_read_b64_tr_b16 v[172:173], v69 offset:32768\n"
    "ds_read_b64_tr_b16 v[176:177], v68 offset:32768\n"
    "ds_read_b64_tr_b16 v[204:205], v67 offset:32768\n"
    "ds_read_b64_tr_b16 v[208:209], v66 offset:32768\n"
    "ds_read_b64_tr_b16 v[236:237], v65 offset:32768\n"
    "ds_read_b64_tr_b16 v[240:241], v64 offset:32768\n"
    "s_waitcnt vmcnt(6) lgkmcnt(0)\n"
    "s_barrier\n"
    "ds_read_b64_tr_b16 v[142:143], v71 offset:49152\n"
    "ds_read_b64_tr_b16 v[146:147], v70 offset:49152\n"
    "ds_read_b64_tr_b16 v[174:175], v69 offset:49152\n"
    "ds_read_b64_tr_b16 v[178:179], v68 offset:49152\n"
    "ds_read_b64_tr_b16 v[206:207], v67 offset:49152\n"
    "ds_read_b64_tr_b16 v[210:211], v66 offset:49152\n"
    "ds_read_b64_tr_b16 v[238:239], v65 offset:49152\n"
    "ds_read_b64_tr_b16 v[242:243], v64 offset:49152\n"
    "s_waitcnt vmcnt(2) lgkmcnt(0)\n"
    "s_barrier\n"
    "v_mfma_f32_16x16x32_bf16 v[24:27], v[72:75], v[140:143], v[24:27]\n"
    "v_mfma_f32_16x16x32_bf16 v[28:31], v[72:75], v[144:147], v[28:31]\n"
    "v_mfma_f32_16x16x32_bf16 v[24:27], v[72:75], v[172:175], v[24:27]\n"
    "v_mfma_f32_16x16x32_bf16 v[28:31], v[72:75], v[176:179], v[28:31]\n"
    "v_mfma_f32_16x16x32_bf16 v[24:27], v[72:75], v[204:207], v[24:27]\n"
    "v_mfma_f32_16x16x32_bf16 v[28:31], v[72:75], v[208:211], v[28:31]\n"
    "v_mfma_f32_16x16x32_bf16 v[24:27], v[72:75], v[236:239], v[24:27]\n"
    "v_mfma_f32_16x16x32_bf16 v[28:31], v[72:75], v[240:243], v[28:31]\n"
    "ds_read_b64_tr_b16 v[244:245], v63\n"
    "ds_read_b64_tr_b16 v[248:249], v62\n"
    "ds_read_b64_tr_b16 v[246:247], v63 offset:4096\n"
    "ds_read_b64_tr_b16 v[250:251], v62 offset:4096\n"
    "ds_read_b64_tr_b16 v[252:253], v63 offset:8192\n"
    "ds_read_b64_tr_b16 v[76:77], v62 offset:8192\n"
    "ds_read_b64_tr_b16 v[254:255], v63 offset:12288\n"
    "ds_read_b64_tr_b16 v[78:79], v62 offset:12288\n"
    "s_waitcnt lgkmcnt(0)\n"
    "s_barrier\n"
    "v_mfma_f32_16x16x32_bf16 v[24:27], v[72:75], v[244:247], v[24:27]\n"
    "v_mfma_f32_16x16x32_bf16 v[28:31], v[72:75], v[248:251], v[28:31]\n"
    "v_mfma_f32_16x16x32_bf16 v[24:27], v[72:75], v[252:255], v[24:27]\n"
    "v_mfma_f32_16x16x32_bf16 v[28:31], v[72:75], v[76:79], v[28:31]\n"
    "ds_write_b128 v2, v[8:11] offset:8192\n"
    "ds_write_b128 v2, v[12:15] offset:12288\n"
    "ds_write_b128 v2, v[16:19] offset:16384\n"
    "ds_write_b128 v2, v[20:23] offset:20480\n"
    "s_nop 0\n"
    "v_mov_b32 v74, v24\n"
    "s_nop 2\n"
    "v_mov_b32 v75, v28\n"
    "s_mov_b64 s[44:45], %1\n"
    "s_and_b32 s45, s45, 0xffff\n"
    "s_mov_b32 s46, 0x0\n"
    "s_mov_b32 s47, 0x20000\n"
    "v_mov_b32 v6, 0\n"
    "s_waitcnt vmcnt(0)\n"
    "v_add_u32 v80, 1, v80\n"
    "v_add_u32 v81, 1, v81\n"
    "s_mov_b32 s52, 1048576\n"
    ".Lhgo_a%=:\n"
    "v_cmp_ne_u64 vcc, v[80:81], v[82:83]\n"
    "s_cbranch_vccz .Lhgo_x%=\n"
    "global_load_dwordx2 v[82:83], v4, %1 sc1\n"
    "s_sub_u32 s52, s52, 1\n"
    "s_waitcnt vmcnt(0)\n"
    "s_cbranch_scc0 .Lhgo_a%=\n"
    ".Lhgo_x%=:\n"
    "v_mbcnt_lo_u32_b32 v80, -1, 0\n"
    "v_mbcnt_hi_u32_b32 v80, -1, v80\n"
    "v_min_u32 v4, 5, v80\n"
    "v_lshlrev_b32 v4, 7, v4\n"
    "v_mov_b32 v7, 0x26c00\n"
    "ds_read_b128 v[80:83], v7\n"
    "s_waitcnt lgkmcnt(0)\n"
    "v_readfirstlane_b32 s49, v81\n"
    "v_add_u32 v80, v80, v4\n"
    "s_branch .Lhchk_%=\n"
    ".Lhgo_%=:\n"
    "s_waitcnt lgkmcnt(1)\n"
    "v_mfma_f32_16x16x32_bf16 v[8:11], a[0:3], v[48:51], 0\n"
    "s_waitcnt vmcnt(5)\n"
    "ds_read_b128 v[56:59], v1 offset:2048\n"
    "v_add_u32 v7, 32, v7\n"
    "v_mfma_f32_16x16x32_bf16 v[12:15], a[4:7], v[48:51], 0\n"
    "ds_read_b128 v[80:83], v7\n"
    "v_mfma_f32_16x16x32_bf16 v[16:19], a[144:147], v[48:51], 0\n"
    "ds_read_b128 v[60:63], v1 offset:3072\n"
    "v_mul_f32 v24, v24, v68\n"
    "v_mfma_f32_16x16x32_bf16 v[20:23], a[148:151], v[48:51], 0\n"
    "v_mul_f32 v25, v25, v69\n"
    "v_cvt_pk_bf16_f32 v24, v24, v25\n"
    "s_waitcnt lgkmcnt(3)\n"
    "v_mfma_f32_16x16x32_bf16 v[8:11], a[8:11], v[52:55], v[8:11]\n"
    "v_mul_f32 v26, v26, v70\n"
    "v_mul_f32 v27, v27, v71\n"
    "v_mfma_f32_16x16x32_bf16 v[12:15], a[12:15], v[52:55], v[12:15]\n"
    "v_cvt_pk_bf16_f32 v25, v26, v27\n"
    "v_mul_f32 v28, v28, v68\n"
    "v_mfma_f32_16x16x32_bf16 v[16:19], a[152:155], v[52:55], v[16:19]\n"
    "v_mul_f32 v29, v29, v69\n"
    "v_cvt_pk_bf16_f32 v26, v28, v29\n"
    "v_mfma_f32_16x16x32_bf16 v[20:23], a[156:159], v[52:55], v[20:23]\n"
    "v_mul_f32 v30, v30, v70\n"
    "v_mul_f32 v31, v31, v71\n"
    "s_waitcnt lgkmcnt(2)\n"
    "v_mfma_f32_16x16x32_bf16 v[8:11], a[16:19], v[56:59], v[8:11]\n"
    "v_cvt_pk_bf16_f32 v27, v30, v31\n"
    "v_mfma_f32_16x16x32_bf16 v[12:15], a[20:23], v[56:59], v[12:15]\n"
    "buffer_store_dwordx4 v[24:27], v2, s[40:43], s64 offen sc1\n"
    "s_waitcnt lgkmcnt(1)\n"
    "v_readfirstlane_b32 s49, v81\n"
    "v_mfma_f32_16x16x32_bf16 v[16:19], a[160:163], v[56:59], v[16:19]\n"
    "v_readfirstlane_b32 s62, v82\n"
    "v_add_u32 v80, v80, v4\n"
    ".Lhtop_%=:\n"
    "v_mfma_f32_16x16x32_bf16 v[20:23], a[164:167], v[56:59], v[20:23]\n"
    "v_xor_b32 v0, 0x2000, v1\n"
    "v_mfma_f32_16x16x32_bf16 v[24:27], v[48:51], v[116:119], 0\n"
    "s_waitcnt vmcnt(4)\n"
    "s_barrier\n"
    "ds_read_b128 v[32:35], v0\n"
    "v_mfma_f32_16x16x32_bf16 v[28:31], v[48:51], v[120:123], 0\n"
    "ds_read2st64_b32 v[64:65], v5 offset1:1\n"
    "s_xor_b32 s58, s58, 0x8000\n"
    "v_mfma_f32_16x16x32_bf16 v[24:27], v[52:55], v[124:127], v[24:27]\n"
    "ds_read2st64_b32 v[66:67], v5 offset0:2 offset1:3\n"
    "s_xor_b32 s59, s59, 0x8000\n"
    "v_mfma_f32_16x16x32_bf16 v[28:31], v[52:55], v[128:131], v[28:31]\n"
    "ds_read2st64_b32 v[68:69], v5 offset0:4 offset1:5\n"
    "s_add_u32 s63, s62, 0x2000000\n"
    "v_mfma_f32_16x16x32_bf16 v[24:27], v[56:59], v[132:135], v[24:27]\n"
    "ds_read2st64_b32 v[70:71], v5 offset0:6 offset1:7\n"
    "s_add_u32 s64, s62, 0x4000000\n"
    "v_mfma_f32_16x16x32_bf16 v[28:31], v[56:59], v[136:139], v[28:31]\n"
    "ds_read_b32 v72, v5 offset:2048\n"
    "s_waitcnt lgkmcnt(6)\n"
    "v_mfma_f32_16x16x32_bf16 v[8:11], a[24:27], v[60:63], v[8:11]\n"
    "ds_read_b128 v[36:39], v0 offset:1024\n"
    "v_mfma_f32_16x16x32_bf16 v[12:15], a[28:31], v[60:63], v[12:15]\n"
    "ds_read_b128 v[40:43], v0 offset:2048\n"
    "v_mfma_f32_16x16x32_bf16 v[16:19], a[168:171], v[60:63], v[16:19]\n"
    "ds_read_b128 v[44:47], v0 offset:3072\n"
    "v_mfma_f32_16x16x32_bf16 v[20:23], a[172:175], v[60:63], v[20:23]\n"
    "v_mfma_f32_16x16x32_bf16 v[24:27], v[60:63], v[140:143], v[24:27]\n"
    "s_waitcnt lgkmcnt(7)\n"
    "v_add_f32 v64, v64, v65\n"
    "s_waitcnt lgkmcnt(6)\n"
    "v_add_f32 v66, v66, v67\n"
    "v_mfma_f32_16x16x32_bf16 v[28:31], v[60:63], v[144:147], v[28:31]\n"
    "s_waitcnt lgkmcnt(5)\n"
    "v_add_f32 v68, v68, v69\n"
    "s_waitcnt lgkmcnt(4)\n"
    "v_add_f32 v70, v70, v71\n"
    "v_mfma_f32_16x16x32_bf16 v[8:11], a[32:35], v[32:35], v[8:11]\n"
    "s_waitcnt vmcnt(3)\n"
    "v_cmp_eq_u32 vcc, 0, v6\n"
    "s_cmp_eq_u64 vcc, 0\n"
    "v_add_f32 v64, v64, v66\n"
    "v_mfma_f32_16x16x32_bf16 v[12:15], a[36:39], v[32:35], v[12:15]\n"
    "s_cselect_b32 s94, s42, 0\n"
    "s_cselect_b32 s90, s96, 0\n"
    "s_cselect_b32 s46, 0, 0x20000\n"
    "v_mfma_f32_16x16x32_bf16 v[16:19], a[176:179], v[32:35], v[16:19]\n"
    "buffer_load_dword v81, v80, s[44:47], 0 offen sc1\n"
    "v_add_f32 v68, v68, v70\n"
    "v_mfma_f32_16x16x32_bf16 v[20:23], a[180:183], v[32:35], v[20:23]\n"
    "v_xor_b32 v0, 0x4000, v1\n"
    "s_mov_b32 m0, s58\n"
    "v_mfma_f32_16x16x32_bf16 v[24:27], v[32:35], v[148:151], v[24:27]\n"
    "buffer_load_dwordx4 v3, s[92:95], s49 offen sc1 lds\n"
    "s_waitcnt lgkmcnt(3)\n"
    "v_add_f32 v64, v64, v72\n"
    "v_mfma_f32_16x16x32_bf16 v[28:31], v[32:35], v[152:155], v[28:31]\n"
    "ds_read_b128 v[32:35], v0\n"
    "v_add_f32 v72, v64, v68\n"
    "s_waitcnt lgkmcnt(3)\n"
    "v_mfma_f32_16x16x32_bf16 v[8:11], a[40:43], v[36:39], v[8:11]\n"
    "ds_read_b32 v6, v7 offset:32\n"
    "v_add_f32 v64, v64, v68\n"
    "v_mfma_f32_16x16x32_bf16 v[12:15], a[44:47], v[36:39], v[12:15]\n"
    "s_nop 0\n"
    "v_permlane32_swap_b32 v64, v72\n"
    "v_mfma_f32_16x16x32_bf16 v[16:19], a[184:187], v[36:39], v[16:19]\n"
    "v_add_f32 v65, v64, v72\n"
    "v_add_f32 v72, v72, v64\n"
    "v_mfma_f32_16x16x32_bf16 v[20:23], a[188:191], v[36:39], v[20:23]\n"
    "s_nop 0\n"
    "v_permlane16_swap_b32 v65, v72\n"
    "v_mfma_f32_16x16x32_bf16 v[24:27], v[36:39], v[156:159], v[24:27]\n"
    "v_mul_f32 v64, 0xbae38e39, v65\n"
    "v_mul_f32 v72, 0x3ae38e39, v72\n"
    "v_mfma_f32_16x16x32_bf16 v[28:31], v[36:39], v[160:163], v[28:31]\n"
    "ds_read_b128 v[36:39], v0 offset:1024\n"
    "s_waitcnt lgkmcnt(1)\n"
    "v_add_u32 v6, v6, v4\n"
    "v_mfma_f32_16x16x32_bf16 v[8:11], a[48:51], v[40:43], v[8:11]\n"
    "buffer_load_dwordx4 v3, s[92:95], s49 offen offset:1024 sc1 lds\n"
    "v_cvt_pk_bf16_f32 v73, 0, v64\n"
    "v_mfma_f32_16x16x32_bf16 v[12:15], a[52:55], v[40:43], v[12:15]\n"
    "v_fma_f32 v72, -v64, v64, v72\n"
    "v_max_f32 v72, 0, v72\n"
    "v_mfma_f32_16x16x32_bf16 v[16:19], a[192:195], v[40:43], v[16:19]\n"
    "v_cvt_pk_bf16_f32 v72, 0, v72\n"
    "v_add_f32 v72, 0x3727c5ac, v72\n"
    "v_mfma_f32_16x16x32_bf16 v[20:23], a[196:199], v[40:43], v[20:23]\n"
    "v_cvt_pk_bf16_f32 v72, 0, v72\n"
    "v_sqrt_f32 v72, v72\n"
    "v_mfma_f32_16x16x32_bf16 v[24:27], v[40:43], v[164:167], v[24:27]\n"
    "v_lshrrev_b32 v68, 2, v5\n"
    "v_cvt_pk_bf16_f32 v72, 0, v72\n"
    "v_mfma_f32_16x16x32_bf16 v[28:31], v[40:43], v[168:171], v[28:31]\n"
    "ds_read_b128 v[40:43], v0 offset:2048\n"
    "v_rcp_f32 v72, v72\n"
    "v_mfma_f32_16x16x32_bf16 v[8:11], a[56:59], v[44:47], v[8:11]\n"
    "ds_read_b128 v[48:51], v2 offset:8192\n"
    "v_and_b32 v68, 48, v68\n"
    "v_mfma_f32_16x16x32_bf16 v[12:15], a[60:63], v[44:47], v[12:15]\n"
    "buffer_load_dwordx4 v3, s[92:95], s49 offen offset:2048 sc1 lds\n"
    "v_mfma_f32_16x16x32_bf16 v[16:19], a[200:203], v[44:47], v[16:19]\n"
    "ds_write2st64_b32 v5, v73, v72 offset0:9 offset1:10\n"
    "v_bfi_b32 v68, s86, v68, v5\n"
    "v_mfma_f32_16x16x32_bf16 v[20:23], a[204:207], v[44:47], v[20:23]\n"
    "v_mfma_f32_16x16x32_bf16 v[24:27], v[44:47], v[172:175], v[24:27]\n"
    "ds_read_b128 v[64:67], v68 offset:2304\n"
    "v_mov_b32 v82, 1\n"
    "v_mfma_f32_16x16x32_bf16 v[28:31], v[44:47], v[176:179], v[28:31]\n"
    "ds_read_b128 v[44:47], v0 offset:3072\n"
    "v_mfma_f32_16x16x32_bf16 v[8:11], a[64:67], v[32:35], v[8:11]\n"
    "s_waitcnt vmcnt(4)\n"
    "s_mov_b64 exec, s[56:57]\n"
    "ds_add_rtn_u32 v82, v7, v82 offset:16\n"
    "s_mov_b64 exec, -1\n"
    "v_mfma_f32_16x16x32_bf16 v[12:15], a[68:71], v[32:35], v[12:15]\n"
    "ds_read_b128 v[68:71], v68 offset:2560\n"
    "v_mfma_f32_16x16x32_bf16 v[16:19], a[208:211], v[32:35], v[16:19]\n"
    "buffer_load_dwordx4 v3, s[92:95], s49 offen offset:3072 sc1 lds\n"
    "v_mfma_f32_16x16x32_bf16 v[20:23], a[212:215], v[32:35], v[20:23]\n"
    "ds_read_b128 v[52:55], v2 offset:12288\n"
    "v_xor_b32 v0, 0x6000, v1\n"
    "v_mfma_f32_16x16x32_bf16 v[24:27], v[32:35], v[180:183], v[24:27]\n"
    "v_mfma_f32_16x16x32_bf16 v[28:31], v[32:35], v[184:187], v[28:31]\n"
    "ds_read_b128 v[32:35], v0\n"
    "s_waitcnt lgkmcnt(3)\n"
    "v_readfirstlane_b32 s50, v82\n"
    "v_mfma_f32_16x16x32_bf16 v[8:11], a[72:75], v[36:39], v[8:11]\n"
    "s_cmp_eq_u32 s50, 3\n"
    "s_cselect_b32 s46, 0x20000, 0\n"
    "v_mfma_f32_16x16x32_bf16 v[12:15], a[76:79], v[36:39], v[12:15]\n"
    "buffer_store_dword v82, v83, s[44:47], 0 offen sc1\n"
    "v_mfma_f32_16x16x32_bf16 v[16:19], a[216:219], v[36:39], v[16:19]\n"
    "ds_read_b128 v[56:59], v2 offset:16384\n"
    "s_bitset1_b32 m0, 12\n"
    "v_mfma_f32_16x16x32_bf16 v[20:23], a[220:223], v[36:39], v[20:23]\n"
    "buffer_load_dwordx4 v2, s[88:91], s49 offen sc1 lds\n"
    "v_mfma_f32_16x16x32_bf16 v[24:27], v[36:39], v[188:191], v[24:27]\n"
    "v_mfma_f32_16x16x32_bf16 v[28:31], v[36:39], v[192:195], v[28:31]\n"
    "ds_read_b128 v[36:39], v0 offset:1024\n"
    "v_mfma_f32_16x16x32_bf16 v[8:11], a[80:83], v[40:43], v[8:11]\n"
    "ds_read_b128 v[60:63], v2 offset:20480\n"
    "v_mfma_f32_16x16x32_bf16 v[12:15], a[84:87], v[40:43], v[12:15]\n"
    "v_mfma_f32_16x16x32_bf16 v[16:19], a[224:227], v[40:43], v[16:19]\n"
    "v_mfma_f32_16x16x32_bf16 v[20:23], a[228:231], v[40:43], v[20:23]\n"
    "s_mov_b32 m0, s59\n"
    "s_mov_b32 s55, s49\n"
    "s_bitset1_b32 s55, 15\n"
    "v_mfma_f32_16x16x32_bf16 v[24:27], v[40:43], v[196:199], v[24:27]\n"
    "s_mov_b64 exec, s[60:61]\n"
    "buffer_load_dwordx4 v2, s[92:95], s55 offen offset:2048 sc1 lds\n"
    "s_mov_b64 exec, -1\n"
    "v_fmac_f32 v8, v48, v73\n"
    "v_mfma_f32_16x16x32_bf16 v[28:31], v[40:43], v[200:203], v[28:31]\n"
    "ds_read_b128 v[40:43], v0 offset:2048\n"
    "s_waitcnt lgkmcnt(5)\n"
    "v_fmac_f32 v12, v52, v73\n"
    "v_mfma_f32_16x16x32_bf16 v[8:11], a[88:91], v[44:47], v[8:11]\n"
    "s_waitcnt lgkmcnt(3)\n"
    "v_fmac_f32 v16, v56, v73\n"
    "v_fmac_f32 v17, v57, v73\n"
    "v_mfma_f32_16x16x32_bf16 v[12:15], a[92:95], v[44:47], v[12:15]\n"
    "s_waitcnt lgkmcnt(1)\n"
    "v_fmac_f32 v20, v60, v73\n"
    "v_fmac_f32 v21, v61, v73\n"
    "v_mfma_f32_16x16x32_bf16 v[16:19], a[232:235], v[44:47], v[16:19]\n"
    "s_cmp_eq_u32 s94, 0\n"
    "s_cbranch_scc1 .Lhk1_%=\n"
    ".Lhk1_x%=:\n"
    "v_mfma_f32_16x16x32_bf16 v[20:23], a[236:239], v[44:47], v[20:23]\n"
    "v_fmac_f32 v9, v49, v73\n"
    "v_fmac_f32 v10, v50, v73\n"
    "v_mfma_f32_16x16x32_bf16 v[24:27], v[44:47], v[204:207], v[24:27]\n"
    "v_fmac_f32 v11, v51, v73\n"
    "v_fmac_f32 v13, v53, v73\n"
    "v_mfma_f32_16x16x32_bf16 v[28:31], v[44:47], v[208:211], v[28:31]\n"
    "ds_read_b128 v[44:47], v0 offset:3072\n"
    "v_fmac_f32 v14, v54, v73\n"
    "v_mfma_f32_16x16x32_bf16 v[8:11], a[96:99], v[32:35], v[8:11]\n"
    "v_fmac_f32 v18, v58, v73\n"
    "v_fmac_f32 v19, v59, v73\n"
    "v_mfma_f32_16x16x32_bf16 v[12:15], a[100:103], v[32:35], v[12:15]\n"
    "v_fmac_f32 v22, v62, v73\n"
    "v_fmac_f32 v23, v63, v73\n"
    "v_mfma_f32_16x16x32_bf16 v[16:19], a[240:243], v[32:35], v[16:19]\n"
    "v_fmac_f32 v24, v64, v74\n"
    "v_fmac_f32 v25, v65, v74\n"
    "v_mfma_f32_16x16x32_bf16 v[20:23], a[244:247], v[32:35], v[20:23]\n"
    "v_and_b32 v0, 0xffff9fff, v1\n"
    "v_fmac_f32 v28, v64, v75\n"
    "v_mfma_f32_16x16x32_bf16 v[24:27], v[32:35], v[212:215], v[24:27]\n"
    "v_fmac_f32 v15, v55, v73\n"
    "v_mfma_f32_16x16x32_bf16 v[28:31], v[32:35], v[216:219], v[28:31]\n"
    "ds_read_b128 v[32:35], v0 offset:4096\n"
    "v_mfma_f32_16x16x32_bf16 v[8:11], a[104:107], v[36:39], v[8:11]\n"
    "v_mfma_f32_16x16x32_bf16 v[12:15], a[108:111], v[36:39], v[12:15]\n"
    "v_fmac_f32 v26, v66, v74\n"
    "v_fmac_f32 v27, v67, v74\n"
    "v_mfma_f32_16x16x32_bf16 v[16:19], a[248:251], v[36:39], v[16:19]\n"
    "v_fmac_f32 v29, v65, v75\n"
    "v_fmac_f32 v30, v66, v75\n"
    "v_mfma_f32_16x16x32_bf16 v[20:23], a[252:255], v[36:39], v[20:23]\n"
    "v_fmac_f32 v31, v67, v75\n"
    "v_mfma_f32_16x16x32_bf16 v[24:27], v[36:39], v[220:223], v[24:27]\n"
    "s_nop 0\n"
    "v_mfma_f32_16x16x32_bf16 v[28:31], v[36:39], v[224:227], v[28:31]\n"
    "ds_read_b128 v[36:39], v0 offset:12288\n"
    "s_waitcnt lgkmcnt(3)\n"
    "v_mfma_f32_16x16x32_bf16 v[8:11], a[112:115], v[40:43], v[8:11]\n"
    "v_mfma_f32_16x16x32_bf16 v[12:15], a[116:119], v[40:43], v[12:15]\n"
    "v_xor_b32 v1, 0x8000, v1\n"
    "v_xor_b32 v5, 0x8000, v5\n"
    "s_waitcnt lgkmcnt(2)\n"
    "v_mfma_f32_16x16x32_bf16 v[8:11], a[120:123], v[44:47], v[8:11]\n"
    "v_mfma_f32_16x16x32_bf16 v[12:15], a[124:127], v[44:47], v[12:15]\n"
    "s_waitcnt lgkmcnt(1)\n"
    "v_mfma_f32_16x16x32_bf16 v[8:11], a[128:131], v[32:35], v[8:11]\n"
    "s_cmp_eq_u32 s94, 0\n"
    "s_cbranch_scc1 .Lhk2_%=\n"
    ".Lhk2_x%=:\n"
    "v_mfma_f32_16x16x32_bf16 v[12:15], a[132:135], v[32:35], v[12:15]\n"
    "s_waitcnt lgkmcnt(0)\n"
    "v_mfma_f32_16x16x32_bf16 v[8:11], a[136:139], v[36:39], v[8:11]\n"
    "v_mfma_f32_16x16x32_bf16 v[12:15], a[140:143], v[36:39], v[12:15]\n"
    "global_load_dword v6, v6, %1 sc1\n"
    "v_mfma_f32_16x16x32_bf16 v[16:19], v[84:87], v[40:43], v[16:19]\n"
    "s_waitcnt vmcnt(5)\n"
    "ds_read_b128 v[48:51], v1\n"
    "v_mfma_f32_16x16x32_bf16 v[20:23], v[88:91], v[40:43], v[20:23]\n"
    "v_mul_f32 v8, v8, v72\n"
    "v_mul_f32 v12, v12, v72\n"
    "v_mfma_f32_16x16x32_bf16 v[16:19], v[92:95], v[44:47], v[16:19]\n"
    "v_cvt_pk_bf16_f32 v8, v8, v12\n"
    "v_mul_f32 v9, v9, v72\n"
    "v_mfma_f32_16x16x32_bf16 v[20:23], v[96:99], v[44:47], v[20:23]\n"
    "v_mul_f32 v13, v13, v72\n"
    "v_cvt_pk_bf16_f32 v9, v9, v13\n"
    "v_mfma_f32_16x16x32_bf16 v[16:19], v[100:103], v[32:35], v[16:19]\n"
    "v_mul_f32 v10, v10, v72\n"
    "v_mul_f32 v14, v14, v72\n"
    "v_mfma_f32_16x16x32_bf16 v[20:23], v[104:107], v[32:35], v[20:23]\n"
    "v_cvt_pk_bf16_f32 v10, v10, v14\n"
    "v_mul_f32 v11, v11, v72\n"
    "v_mfma_f32_16x16x32_bf16 v[16:19], v[108:111], v[36:39], v[16:19]\n"
    "v_mul_f32 v15, v15, v72\n"
    "v_cvt_pk_bf16_f32 v11, v11, v15\n"
    "v_mfma_f32_16x16x32_bf16 v[20:23], v[112:115], v[36:39], v[20:23]\n"
    "buffer_store_dwordx4 v[8:11], v2, s[40:43], s62 offen sc1\n"
    "v_mfma_f32_16x16x32_bf16 v[24:27], v[40:43], v[228:231], v[24:27]\n"
    "ds_read_b128 v[52:55], v1 offset:1024\n"
    "v_mfma_f32_16x16x32_bf16 v[28:31], v[40:43], v[232:235], v[28:31]\n"
    "v_mul_f32 v16, v16, v72\n"
    "v_mul_f32 v20, v20, v72\n"
    "v_mfma_f32_16x16x32_bf16 v[24:27], v[44:47], v[236:239], v[24:27]\n"
    "v_cvt_pk_bf16_f32 v16, v16, v20\n"
    "v_mul_f32 v17, v17, v72\n"
    "v_mfma_f32_16x16x32_bf16 v[28:31], v[44:47], v[240:243], v[28:31]\n"
    "v_mul_f32 v21, v21, v72\n"
    "v_cvt_pk_bf16_f32 v17, v17, v21\n"
    "v_mfma_f32_16x16x32_bf16 v[24:27], v[32:35], v[244:247], v[24:27]\n"
    "v_mul_f32 v18, v18, v72\n"
    "v_mul_f32 v22, v22, v72\n"
    "v_mfma_f32_16x16x32_bf16 v[28:31], v[32:35], v[248:251], v[28:31]\n"
    "v_cvt_pk_bf16_f32 v18, v18, v22\n"
    "v_mul_f32 v19, v19, v72\n"
    "v_mfma_f32_16x16x32_bf16 v[24:27], v[36:39], v[252:255], v[24:27]\n"
    "v_mul_f32 v23, v23, v72\n"
    "v_cvt_pk_bf16_f32 v19, v19, v23\n"
    "v_mfma_f32_16x16x32_bf16 v[28:31], v[36:39], v[76:79], v[28:31]\n"
    "buffer_store_dwordx4 v[16:19], v2, s[40:43], s63 offen sc1\n"
    "s_mov_b32 s56, 1\n"
    "s_cmp_lg_u32 s94, 0\n"
    "s_cbranch_scc1 .Lhgo_%=\n"
    "ds_read_b32 v83, v7 offset:44\n"
    "v_mul_f32 v24, v24, v68\n"
    "v_mul_f32 v25, v25, v69\n"
    "v_cvt_pk_bf16_f32 v24, v24, v25\n"
    "v_mul_f32 v26, v26, v70\n"
    "v_mul_f32 v27, v27, v71\n"
    "v_cvt_pk_bf16_f32 v25, v26, v27\n"
    "v_mul_f32 v28, v28, v68\n"
    "v_mul_f32 v29, v29, v69\n"
    "v_cvt_pk_bf16_f32 v26, v28, v29\n"
    "v_mul_f32 v30, v30, v70\n"
    "v_mul_f32 v31, v31, v71\n"
    "v_cvt_pk_bf16_f32 v27, v30, v31\n"
    "buffer_store_dwordx4 v[24:27], v2, s[40:43], s64 offen sc1\n"
    "s_waitcnt vmcnt(9) lgkmcnt(0)\n"
    "v_cmp_eq_u32 vcc, 0, v81\n"
    "s_cmp_eq_u64 vcc, 0\n"
    "s_cbranch_scc1 .Lhld_%=\n"
    "global_load_dword v81, v80, %1 sc1\n"
    "s_waitcnt vmcnt(0)\n"
    "v_mov_b32 v82, 1\n"
    "s_mov_b64 exec, s[56:57]\n"
    "ds_add_rtn_u32 v82, v7, v82 offset:48\n"
    "s_mov_b64 exec, -1\n"
    "s_waitcnt lgkmcnt(0)\n"
    "v_readfirstlane_b32 s50, v82\n"
    "s_cmp_eq_u32 s50, 3\n"
    "s_cselect_b32 s46, 0x20000, 0\n"
    "buffer_store_dword v82, v83, s[44:47], 0 offen sc1\n"
    "s_mov_b32 s56, 0\n"
    "v_cmp_eq_u32 vcc, 0, v81\n"
    "s_cmp_eq_u64 vcc, 0\n"
    "s_cbranch_scc1 .Lhld_%=\n"
    ".Lhchk_%=:\n"
    "v_readfirstlane_b32 s50, v80\n"
    "s_cmp_lt_u32 s50, 200\n"
    "s_cbranch_scc1 .Lhend_%=\n"
    ".Lhwait_%=:\n"
    "global_load_dword v81, v80, %1 sc1\n"
    "s_mov_b32 s52, 0\n"
    ".Lhp3_%=:\n"
    "s_waitcnt vmcnt(0)\n"
    "v_cmp_eq_u32 vcc, 0, v81\n"
    "s_cmp_eq_u64 vcc, 0\n"
    "s_cbranch_scc1 .Lhp3_x%=\n"
    "s_add_u32 s52, s52, 1\n"
    "s_sleep 2\n"
    "global_load_dword v81, v80, %1 sc1\n"
    "s_cmp_lt_u32 s52, s53\n"
    "s_cbranch_scc1 .Lhp3_%=\n"
    "s_mov_b32 s53, 0\n"
    "v_mov_b32 v82, 0\n"
    "v_mov_b32 v83, 1\n"
    "global_store_dword v82, v83, %1 sc1\n"
    "s_waitcnt vmcnt(0)\n"
    ".Lhp3_x%=:\n"
    ".Lhld_%=:\n"
    "s_mov_b32 s94, s42\n"
    "s_mov_b32 s90, s96\n"
    "s_mov_b32 m0, s58\n"
    "s_nop 0\n"
    "buffer_load_dwordx4 v3, s[92:95], s49 offen sc1 lds\n"
    "buffer_load_dwordx4 v3, s[92:95], s49 offen offset:1024 sc1 lds\n"
    "buffer_load_dwordx4 v3, s[92:95], s49 offen offset:2048 sc1 lds\n"
    "buffer_load_dwordx4 v3, s[92:95], s49 offen offset:3072 sc1 lds\n"
    "s_bitset1_b32 m0, 12\n"
    "s_nop 0\n"
    "buffer_load_dwordx4 v2, s[88:91], s49 offen sc1 lds\n"
    "s_mov_b32 m0, s59\n"
    "s_mov_b32 s55, s49\n"
    "s_bitset1_b32 s55, 15\n"
    "s_mov_b64 exec, s[60:61]\n"
    "buffer_load_dwordx4 v2, s[92:95], s55 offen offset:2048 sc1 lds\n"
    "s_mov_b64 exec, -1\n"
    "s_waitcnt vmcnt(6)\n"
    "v_mov_b32 v82, 1\n"
    "s_mov_b64 exec, s[56:57]\n"
    "ds_add_rtn_u32 v82, v7, v82 offset:48\n"
    "s_mov_b64 exec, -1\n"
    "s_waitcnt lgkmcnt(0)\n"
    "v_readfirstlane_b32 s50, v82\n"
    "s_cmp_eq_u32 s50, 3\n"
    "s_cselect_b32 s46, 0x20000, 0\n"
    "buffer_store_dword v82, v83, s[44:47], 0 offen sc1\n"
    "s_mov_b32 s56, 0\n"
    "s_waitcnt vmcnt(5)\n"
    "ds_read_b128 v[48:51], v1\n"
    "ds_read_b128 v[52:55], v1 offset:1024\n"
    "s_waitcnt lgkmcnt(1)\n"
    "v_mfma_f32_16x16x32_bf16 v[8:11], a[0:3], v[48:51], 0\n"
    "s_waitcnt vmcnt(1)\n"
    "ds_read_b128 v[56:59], v1 offset:2048\n"
    "v_add_u32 v7, 32, v7\n"
    "v_mfma_f32_16x16x32_bf16 v[12:15], a[4:7], v[48:51], 0\n"
    "ds_read_b128 v[80:83], v7\n"
    "v_mfma_f32_16x16x32_bf16 v[16:19], a[144:147], v[48:51], 0\n"
    "ds_read_b128 v[60:63], v1 offset:3072\n"
    "v_mfma_f32_16x16x32_bf16 v[20:23], a[148:151], v[48:51], 0\n"
    "s_waitcnt lgkmcnt(3)\n"
    "v_mfma_f32_16x16x32_bf16 v[8:11], a[8:11], v[52:55], v[8:11]\n"
    "v_mfma_f32_16x16x32_bf16 v[12:15], a[12:15], v[52:55], v[12:15]\n"
    "v_mfma_f32_16x16x32_bf16 v[16:19], a[152:155], v[52:55], v[16:19]\n"
    "v_mfma_f32_16x16x32_bf16 v[20:23], a[156:159], v[52:55], v[20:23]\n"
    "s_waitcnt lgkmcnt(2)\n"
    "v_mfma_f32_16x16x32_bf16 v[8:11], a[16:19], v[56:59], v[8:11]\n"
    "v_mfma_f32_16x16x32_bf16 v[12:15], a[20:23], v[56:59], v[12:15]\n"
    "s_waitcnt lgkmcnt(1)\n"
    "v_readfirstlane_b32 s49, v81\n"
    "v_readfirstlane_b32 s62, v82\n"
    "v_mfma_f32_16x16x32_bf16 v[16:19], a[160:163], v[56:59], v[16:19]\n"
    "v_add_u32 v80, v80, v4\n"
    "s_branch .Lhtop_%=\n"
    ".Lhk1_%=:\n"
    "s_waitcnt vmcnt(6)\n"
    "v_cmp_eq_u32 vcc, 0, v81\n"
    "s_cmp_eq_u64 vcc, 0\n"
    "s_cselect_b32 s94, s42, 0\n"
    "s_cselect_b32 s90, s96, 0\n"
    "s_cselect_b32 s46, 0, 0x20000\n"
    "buffer_load_dword v81, v80, s[44:47], 0 offen sc1\n"
    "s_mov_b32 m0, s58\n"
    "s_nop 0\n"
    "buffer_load_dwordx4 v3, s[92:95], s49 offen sc1 lds\n"
    "buffer_load_dwordx4 v3, s[92:95], s49 offen offset:1024 sc1 lds\n"
    "buffer_load_dwordx4 v3, s[92:95], s49 offen offset:2048 sc1 lds\n"
    "buffer_load_dwordx4 v3, s[92:95], s49 offen offset:3072 sc1 lds\n"
    "s_bitset1_b32 m0, 12\n"
    "s_nop 0\n"
    "buffer_load_dwordx4 v2, s[88:91], s49 offen sc1 lds\n"
    "s_mov_b32 m0, s59\n"
    "s_mov_b32 s55, s49\n"
    "s_bitset1_b32 s55, 15\n"
    "s_mov_b64 exec, s[60:61]\n"
    "buffer_load_dwordx4 v2, s[92:95], s55 offen offset:2048 sc1 lds\n"
    "s_mov_b64 exec, -1\n"
    "s_branch .Lhk1_x%=\n"
    ".Lhk2_%=:\n"
    "s_waitcnt vmcnt(6)\n"
    "v_cmp_eq_u32 vcc, 0, v81\n"
    "s_cmp_eq_u64 vcc, 0\n"
    "s_cselect_b32 s94, s42, 0\n"
    "s_cselect_b32 s90, s96, 0\n"
    "s_cselect_b32 s46, 0, 0x20000\n"
    "buffer_load_dword v81, v80, s[44:47], 0 offen sc1\n"
    "s_mov_b32 m0, s58\n"
    "s_nop 0\n"
    "buffer_load_dwordx4 v3, s[92:95], s49 offen sc1 lds\n"
    "buffer_load_dwordx4 v3, s[92:95], s49 offen offset:1024 sc1 lds\n"
    "buffer_load_dwordx4 v3, s[92:95], s49 offen offset:2048 sc1 lds\n"
    "buffer_load_dwordx4 v3, s[92:95], s49 offen offset:3072 sc1 lds\n"
    "s_bitset1_b32 m0, 12\n"
    "s_nop 0\n"
    "buffer_load_dwordx4 v2, s[88:91], s49 offen sc1 lds\n"
    "s_mov_b32 m0, s59\n"
    "s_mov_b32 s55, s49\n"
    "s_bitset1_b32 s55, 15\n"
    "s_mov_b64 exec, s[60:61]\n"
    "buffer_load_dwordx4 v2, s[92:95], s55 offen offset:2048 sc1 lds\n"
    "s_mov_b64 exec, -1\n"
    "s_branch .Lhk2_x%=\n"
    ".Lhpol_%=:\n"
    "v_mov_b32 v80, 64\n"
    "global_load_dword v81, v80, %1 sc1\n"
    "s_mov_b32 s52, 0\n"
    ".Lhp4_%=:\n"
    "s_waitcnt vmcnt(0)\n"
    "v_cmp_eq_u32 vcc, 0, v81\n"
    "s_cmp_eq_u64 vcc, 0\n"
    "s_cbranch_scc1 .Lhp4_x%=\n"
    "s_add_u32 s52, s52, 1\n"
    "s_sleep 2\n"
    "global_load_dword v81, v80, %1 sc1\n"
    "s_cmp_lt_u32 s52, s53\n"
    "s_cbranch_scc1 .Lhp4_%=\n"
    "s_mov_b32 s53, 0\n"
    "v_mov_b32 v82, 0\n"
    "v_mov_b32 v83, 1\n"
    "global_store_dword v82, v83, %1 sc1\n"
    "s_waitcnt vmcnt(0)\n"
    ".Lhp4_x%=:\n"
    "v_readfirstlane_b32 s50, v81\n"
    "v_mbcnt_lo_u32_b32 v80, -1, 0\n"
    "v_mbcnt_hi_u32_b32 v80, -1, v80\n"
    "s_lshl_b32 s51, %2, 6\n"
    "v_add_u32 v80, s51, v80\n"
    "v_min_u32 v80, 129, v80\n"
    "v_lshlrev_b32 v80, 5, v80\n"
    "v_add_u32 v80, 0x26c00, v80\n"
    "ds_read2_b32 v[82:83], v80 offset0:1 offset1:2\n"
    "s_and_b32 s50, s50, 1\n"
    "s_waitcnt lgkmcnt(0)\n"
    "v_and_b32 v81, s50, v82\n"
    "v_lshlrev_b32 v81, 13, v81\n"
    "v_and_b32 v82, -2, v82\n"
    "v_xor_b32 v82, v81, v82\n"
    "v_and_b32 v81, s50, v83\n"
    "v_lshlrev_b32 v81, 13, v81\n"
    "v_and_b32 v83, -2, v83\n"
    "v_xor_b32 v83, v81, v83\n"
    "ds_write2_b32 v80, v82, v83 offset0:1 offset1:2\n"
    "s_waitcnt lgkmcnt(0)\n"
    "s_barrier\n"
    "ds_read_b32 v81, v7 offset:4\n"
    "v_add_u32 v80, %4, v4\n"
    "s_waitcnt lgkmcnt(0)\n"
    "v_readfirstlane_b32 s49, v81\n"
    "s_branch .Lhwait_%=\n"
    ".Lhend_%=:\n"
    "s_cmp_eq_u32 s50, 192\n"
    "s_cbranch_scc0 .Lhpol_%=\n"
    "s_waitcnt vmcnt(0) lgkmcnt(0)\n"
    "s_endpgm\n"
    ".p2align 6\n"
    ".Lrole2_%=:\n"
    "s_mov_b64 s[40:41], %0\n"
    "s_and_b32 s41, s41, 0xffff\n"
    "s_mov_b32 s42, 0xc000000\n"
    "s_mov_b32 s43, 0x20000\n"
    "s_mov_b64 s[92:93], %0\n"
    "s_and_b32 s93, s93, 0xffff\n"
    "s_mov_b32 s94, 0xc000000\n"
    "s_mov_b32 s95, 0x20000\n"
    "s_mov_b32 s53, 131072\n"
    "s_mov_b64 s[56:57], 0\n"
    "v_mbcnt_lo_u32_b32 v7, -1, 0\n"
    "v_mbcnt_hi_u32_b32 v7, -1, v7\n"
    "s_lshl_b32 s50, %2, 8\n"
    "s_add_u32 s50, s50, 147456\n"
    "v_lshl_add_u32 v7, v7, 2, s50\n"
    "ds_read_b32 v1, v7\n"
    "ds_read_b32 v4, v7 offset:1024\n"
    "ds_read_b32 v2, v7 offset:2048\n"
    "ds_read_b32 v8, v7 offset:3072\n"
    "ds_read_b32 v9, v7 offset:4096\n"
    "ds_read_b32 v10, v7 offset:5120\n"
    "ds_read_b32 v11, v7 offset:6144\n"
    "ds_read_b32 v3, v7 offset:7168\n"
    "s_waitcnt lgkmcnt(0)\n"
    "s_barrier\n"
    "s_mul_i32 s54, %2, 6144\n"
    "v_add_u32 v5, 16384, v4\n"
    "s_lshl_b32 s58, %2, 13\n"
    "s_add_u32 s58, s58, 0\n"
    "s_xor_b32 s50, %2, 0\n"
    "s_mul_i32 s50, s50, 6144\n"
    "v_add_u32 v32, s50, v9\n"
    "v_add_u32 v33, s50, v10\n"
    "v_add_u32 v34, s50, v11\n"
    "s_xor_b32 s50, %2, 1\n"
    "s_mul_i32 s50, s50, 6144\n"
    "v_add_u32 v35, s50, v9\n"
    "v_add_u32 v36, s50, v10\n"
    "v_add_u32 v37, s50, v11\n"
    "s_xor_b32 s50, %2, 2\n"
    "s_mul_i32 s50, s50, 6144\n"
    "v_add_u32 v38, s50, v9\n"
    "v_add_u32 v39, s50, v10\n"
    "v_add_u32 v40, s50, v11\n"
    "s_xor_b32 s50, %2, 3\n"
    "s_mul_i32 s50, s50, 6144\n"
    "v_add_u32 v41, s50, v9\n"
    "v_add_u32 v42, s50, v10\n"
    "v_add_u32 v43, s50, v11\n"
    "s_mov_b64 s[44:45], %8\n"
    "s_and_b32 s45, s45, 0xffff\n"
    "s_mov_b32 s46, 0x480000\n"
    "s_mov_b32 s47, 0x20000\n"
    "s_add_u32 m0, s54, 0\n"
    "s_add_u32 s55, %6, 0\n"
    "buffer_load_dwordx4 v8, s[44:47], s55 offen lds\n"
    "s_add_u32 m0, s54, 1024\n"
    "s_add_u32 s55, %6, 18432\n"
    "buffer_load_dwordx4 v8, s[44:47], s55 offen lds\n"
    "s_add_u32 m0, s54, 2048\n"
    "s_add_u32 s55, %6, 128\n"
    "buffer_load_dwordx4 v8, s[44:47], s55 offen lds\n"
    "s_add_u32 m0, s54, 3072\n"
    "s_add_u32 s55, %6, 18560\n"
    "buffer_load_dwordx4 v8, s[44:47], s55 offen lds\n"
    "s_add_u32 m0, s54, 4096\n"
    "s_add_u32 s55, %6, 256\n"
    "buffer_load_dwordx4 v8, s[44:47], s55 offen lds\n"
    "s_add_u32 m0, s54, 5120\n"
    "s_add_u32 s55, %6, 18688\n"
    "buffer_load_dwordx4 v8, s[44:47], s55 offen lds\n"
    "s_add_u32 m0, s54, 24576\n"
    "s_add_u32 s55, %6, 2304\n"
    "buffer_load_dwordx4 v8, s[44:47], s55 offen lds\n"
    "s_add_u32 m0, s54, 25600\n"
    "s_add_u32 s55, %6, 20736\n"
    "buffer_load_dwordx4 v8, s[44:47], s55 offen lds\n"
    "s_add_u32 m0, s54, 26624\n"
    "s_add_u32 s55, %6, 2432\n"
    "buffer_load_dwordx4 v8, s[44:47], s55 offen lds\n"
    "s_add_u32 m0, s54, 27648\n"
    "s_add_u32 s55, %6, 20864\n"
    "buffer_load_dwordx4 v8, s[44:47], s55 offen lds\n"
    "s_add_u32 m0, s54, 28672\n"
    "s_add_u32 s55, %6, 2560\n"
    "buffer_load_dwordx4 v8, s[44:47], s55 offen lds\n"
    "s_add_u32 m0, s54, 29696\n"
    "s_add_u32 s55, %6, 20992\n"
    "buffer_load_dwordx4 v8, s[44:47], s55 offen lds\n"
    "s_add_u32 m0, s54, 49152\n"
    "s_add_u32 s55, %6, 36864\n"
    "buffer_load_dwordx4 v8, s[44:47], s55 offen lds\n"
    "s_add_u32 m0, s54, 50176\n"
    "s_add_u32 s55, %6, 55296\n"
    "buffer_load_dwordx4 v8, s[44:47], s55 offen lds\n"
    "s_add_u32 m0, s54, 51200\n"
    "s_add_u32 s55, %6, 36992\n"
    "buffer_load_dwordx4 v8, s[44:47], s55 offen lds\n"
    "s_add_u32 m0, s54, 52224\n"
    "s_add_u32 s55, %6, 55424\n"
    "buffer_load_dwordx4 v8, s[44:47], s55 offen lds\n"
    "s_add_u32 m0, s54, 53248\n"
    "s_add_u32 s55, %6, 37120\n"
    "buffer_load_dwordx4 v8, s[44:47], s55 offen lds\n"
    "s_add_u32 m0, s54, 54272\n"
    "s_add_u32 s55, %6, 55552\n"
    "buffer_load_dwordx4 v8, s[44:47], s55 offen lds\n"
    "s_add_u32 m0, s54, 73728\n"
    "s_add_u32 s55, %6, 39168\n"
    "buffer_load_dwordx4 v8, s[44:47], s55 offen lds\n"
    "s_add_u32 m0, s54, 74752\n"
    "s_add_u32 s55, %6, 57600\n"
    "buffer_load_dwordx4 v8, s[44:47], s55 offen lds\n"
    "s_add_u32 m0, s54, 75776\n"
    "s_add_u32 s55, %6, 39296\n"
    "buffer_load_dwordx4 v8, s[44:47], s55 offen lds\n"
    "s_add_u32 m0, s54, 76800\n"
    "s_add_u32 s55, %6, 57728\n"
    "buffer_load_dwordx4 v8, s[44:47], s55 offen lds\n"
    "s_add_u32 m0, s54, 77824\n"
    "s_add_u32 s55, %6, 39424\n"
    "buffer_load_dwordx4 v8, s[44:47], s55 offen lds\n"
    "s_add_u32 m0, s54, 78848\n"
    "s_add_u32 s55, %6, 57856\n"
    "buffer_load_dwordx4 v8, s[44:47], s55 offen lds\n"
    "s_add_u32 m0, s54, 98304\n"
    "s_add_u32 s55, %6, 73728\n"
    "buffer_load_dwordx4 v8, s[44:47], s55 offen lds\n"
    "s_add_u32 m0, s54, 99328\n"
    "s_add_u32 s55, %6, 92160\n"
    "buffer_load_dwordx4 v8, s[44:47], s55 offen lds\n"
    "s_add_u32 m0, s54, 100352\n"
    "s_add_u32 s55, %6, 73856\n"
    "buffer_load_dwordx4 v8, s[44:47], s55 offen lds\n"
    "s_add_u32 m0, s54, 101376\n"
    "s_add_u32 s55, %6, 92288\n"
    "buffer_load_dwordx4 v8, s[44:47], s55 offen lds\n"
    "s_add_u32 m0, s54, 102400\n"
    "s_add_u32 s55, %6, 73984\n"
    "buffer_load_dwordx4 v8, s[44:47], s55 offen lds\n"
    "s_add_u32 m0, s54, 103424\n"
    "s_add_u32 s55, %6, 92416\n"
    "buffer_load_dwordx4 v8, s[44:47], s55 offen lds\n"
    "s_add_u32 m0, s54, 122880\n"
    "s_add_u32 s55, %6, 76032\n"
    "buffer_load_dwordx4 v8, s[44:47], s55 offen lds\n"
    "s_add_u32 m0, s54, 123904\n"
    "s_add_u32 s55, %6, 94464\n"
    "buffer_load_dwordx4 v8, s[44:47], s55 offen lds\n"
    "s_add_u32 m0, s54, 124928\n"
    "s_add_u32 s55, %6, 76160\n"
    "buffer_load_dwordx4 v8, s[44:47], s55 offen lds\n"
    "s_add_u32 m0, s54, 125952\n"
    "s_add_u32 s55, %6, 94592\n"
    "buffer_load_dwordx4 v8, s[44:47], s55 offen lds\n"
    "s_add_u32 m0, s54, 126976\n"
    "s_add_u32 s55, %6, 76288\n"
    "buffer_load_dwordx4 v8, s[44:47], s55 offen lds\n"
    "s_add_u32 m0, s54, 128000\n"
    "s_add_u32 s55, %6, 94720\n"
    "buffer_load_dwordx4 v8, s[44:47], s55 offen lds\n"
    "s_waitcnt vmcnt(30) lgkmcnt(0)\n"
    "s_barrier\n"
    "ds_read_b64_tr_b16 a[0:1], v32\n"
    "ds_read_b64_tr_b16 a[4:5], v33\n"
    "ds_read_b64_tr_b16 a[8:9], v34\n"
    "ds_read_b64_tr_b16 a[96:97], v35\n"
    "ds_read_b64_tr_b16 a[100:101], v36\n"
    "ds_read_b64_tr_b16 a[104:105], v37\n"
    "ds_read_b64_tr_b16 a[192:193], v38\n"
    "ds_read_b64_tr_b16 a[196:197], v39\n"
    "ds_read_b64_tr_b16 a[200:201], v40\n"
    "ds_read_b64_tr_b16 v[160:161], v41\n"
    "ds_read_b64_tr_b16 v[164:165], v42\n"
    "ds_read_b64_tr_b16 v[168:169], v43\n"
    "s_waitcnt vmcnt(24) lgkmcnt(0)\n"
    "s_barrier\n"
    "s_add_u32 m0, s54, 0\n"
    "s_add_u32 s55, %6, 110592\n"
    "buffer_load_dwordx4 v8, s[44:47], s55 offen lds\n"
    "s_add_u32 m0, s54, 1024\n"
    "s_add_u32 s55, %6, 129024\n"
    "buffer_load_dwordx4 v8, s[44:47], s55 offen lds\n"
    "s_add_u32 m0, s54, 2048\n"
    "s_add_u32 s55, %6, 110720\n"
    "buffer_load_dwordx4 v8, s[44:47], s55 offen lds\n"
    "s_add_u32 m0, s54, 3072\n"
    "s_add_u32 s55, %6, 129152\n"
    "buffer_load_dwordx4 v8, s[44:47], s55 offen lds\n"
    "s_add_u32 m0, s54, 4096\n"
    "s_add_u32 s55, %6, 110848\n"
    "buffer_load_dwordx4 v8, s[44:47], s55 offen lds\n"
    "s_add_u32 m0, s54, 5120\n"
    "s_add_u32 s55, %6, 129280\n"
    "buffer_load_dwordx4 v8, s[44:47], s55 offen lds\n"
    "ds_read_b64_tr_b16 a[2:3], v32 offset:24576\n"
    "ds_read_b64_tr_b16 a[6:7], v33 offset:24576\n"
    "ds_read_b64_tr_b16 a[10:11], v34 offset:24576\n"
    "ds_read_b64_tr_b16 a[98:99], v35 offset:24576\n"
    "ds_read_b64_tr_b16 a[102:103], v36 offset:24576\n"
    "ds_read_b64_tr_b16 a[106:107], v37 offset:24576\n"
    "ds_read_b64_tr_b16 a[194:195], v38 offset:24576\n"
    "ds_read_b64_tr_b16 a[198:199], v39 offset:24576\n"
    "ds_read_b64_tr_b16 a[202:203], v40 offset:24576\n"
    "ds_read_b64_tr_b16 v[162:163], v41 offset:24576\n"
    "ds_read_b64_tr_b16 v[166:167], v42 offset:24576\n"
    "ds_read_b64_tr_b16 v[170:171], v43 offset:24576\n"
    "s_waitcnt vmcnt(24) lgkmcnt(0)\n"
    "s_barrier\n"
    "s_add_u32 m0, s54, 24576\n"
    "s_add_u32 s55, %6, 112896\n"
    "buffer_load_dwordx4 v8, s[44:47], s55 offen lds\n"
    "s_add_u32 m0, s54, 25600\n"
    "s_add_u32 s55, %6, 131328\n"
    "buffer_load_dwordx4 v8, s[44:47], s55 offen lds\n"
    "s_add_u32 m0, s54, 26624\n"
    "s_add_u32 s55, %6, 113024\n"
    "buffer_load_dwordx4 v8, s[44:47], s55 offen lds\n"
    "s_add_u32 m0, s54, 27648\n"
    "s_add_u32 s55, %6, 131456\n"
    "buffer_load_dwordx4 v8, s[44:47], s55 offen lds\n"
    "s_add_u32 m0, s54, 28672\n"
    "s_add_u32 s55, %6, 113152\n"
    "buffer_load_dwordx4 v8, s[44:47], s55 offen lds\n"
    "s_add_u32 m0, s54, 29696\n"
    "s_add_u32 s55, %6, 131584\n"
    "buffer_load_dwordx4 v8, s[44:47], s55 offen lds\n"
    "ds_read_b64_tr_b16 a[12:13], v32 offset:49152\n"
    "ds_read_b64_tr_b16 a[16:17], v33 offset:49152\n"
    "ds_read_b64_tr_b16 a[20:21], v34 offset:49152\n"
    "ds_read_b64_tr_b16 a[108:109], v35 offset:49152\n"
    "ds_read_b64_tr_b16 a[112:113], v36 offset:49152\n"
    "ds_read_b64_tr_b16 a[116:117], v37 offset:49152\n"
    "ds_read_b64_tr_b16 a[204:205], v38 offset:49152\n"
    "ds_read_b64_tr_b16 a[208:209], v39 offset:49152\n"
    "ds_read_b64_tr_b16 a[212:213], v40 offset:49152\n"
    "ds_read_b64_tr_b16 v[172:173], v41 offset:49152\n"
    "ds_read_b64_tr_b16 v[176:177], v42 offset:49152\n"
    "ds_read_b64_tr_b16 v[180:181], v43 offset:49152\n"
    "s_waitcnt vmcnt(24) lgkmcnt(0)\n"
    "s_barrier\n"
    "s_add_u32 m0, s54, 49152\n"
    "s_add_u32 s55, %6, 147456\n"
    "buffer_load_dwordx4 v8, s[44:47], s55 offen lds\n"
    "s_add_u32 m0, s54, 50176\n"
    "s_add_u32 s55, %6, 165888\n"
    "buffer_load_dwordx4 v8, s[44:47], s55 offen lds\n"
    "s_add_u32 m0, s54, 51200\n"
    "s_add_u32 s55, %6, 147584\n"
    "buffer_load_dwordx4 v8, s[44:47], s55 offen lds\n"
    "s_add_u32 m0, s54, 52224\n"
    "s_add_u32 s55, %6, 166016\n"
    "buffer_load_dwordx4 v8, s[44:47], s55 offen lds\n"
    "s_add_u32 m0, s54, 53248\n"
    "s_add_u32 s55, %6, 147712\n"
    "buffer_load_dwordx4 v8, s[44:47], s55 offen lds\n"
    "s_add_u32 m0, s54, 54272\n"
    "s_add_u32 s55, %6, 166144\n"
    "buffer_load_dwordx4 v8, s[44:47], s55 offen lds\n"
    "v_add_u32 v71, 0x10000, v32\n"
    "ds_read_b64_tr_b16 a[14:15], v71 offset:8192\n"
    "v_add_u32 v70, 0x10000, v33\n"
    "ds_read_b64_tr_b16 a[18:19], v70 offset:8192\n"
    "v_add_u32 v69, 0x10000, v34\n"
    "ds_read_b64_tr_b16 a[22:23], v69 offset:8192\n"
    "v_add_u32 v68, 0x10000, v35\n"
    "ds_read_b64_tr_b16 a[110:111], v68 offset:8192\n"
    "v_add_u32 v67, 0x10000, v36\n"
    "ds_read_b64_tr_b16 a[114:115], v67 offset:8192\n"
    "v_add_u32 v66, 0x10000, v37\n"
    "ds_read_b64_tr_b16 a[118:119], v66 offset:8192\n"
    "v_add_u32 v65, 0x10000, v38\n"
    "ds_read_b64_tr_b16 a[206:207], v65 offset:8192\n"
    "v_add_u32 v64, 0x10000, v39\n"
    "ds_read_b64_tr_b16 a[210:211], v64 offset:8192\n"
    "v_add_u32 v63, 0x10000, v40\n"
    "ds_read_b64_tr_b16 a[214:215], v63 offset:8192\n"
    "v_add_u32 v62, 0x10000, v41\n"
    "ds_read_b64_tr_b16 v[174:175], v62 offset:8192\n"
    "v_add_u32 v61, 0x10000, v42\n"
    "ds_read_b64_tr_b16 v[178:179], v61 offset:8192\n"
    "v_add_u32 v60, 0x10000, v43\n"
    "ds_read_b64_tr_b16 v[182:183], v60 offset:8192\n"
    "s_waitcnt vmcnt(24) lgkmcnt(0)\n"
    "s_barrier\n"
    "s_add_u32 m0, s54, 73728\n"
    "s_add_u32 s55, %6, 149760\n"
    "buffer_load_dwordx4 v8, s[44:47], s55 offen lds\n"
    "s_add_u32 m0, s54, 74752\n"
    "s_add_u32 s55, %6, 168192\n"
    "buffer_load_dwordx4 v8, s[44:47], s55 offen lds\n"
    "s_add_u32 m0, s54, 75776\n"
    "s_add_u32 s55, %6, 149888\n"
    "buffer_load_dwordx4 v8, s[44:47], s55 offen lds\n"
    "s_add_u32 m0, s54, 76800\n"
    "s_add_u32 s55, %6, 168320\n"
    "buffer_load_dwordx4 v8, s[44:47], s55 offen lds\n"
    "s_add_u32 m0, s54, 77824\n"
    "s_add_u32 s55, %6, 150016\n"
    "buffer_load_dwordx4 v8, s[44:47], s55 offen lds\n"
    "s_add_u32 m0, s54, 78848\n"
    "s_add_u32 s55, %6, 168448\n"
    "buffer_load_dwordx4 v8, s[44:47], s55 offen lds\n"
    "ds_read_b64_tr_b16 a[24:25], v71 offset:32768\n"
    "ds_read_b64_tr_b16 a[28:29], v70 offset:32768\n"
    "ds_read_b64_tr_b16 a[32:33], v69 offset:32768\n"
    "ds_read_b64_tr_b16 a[120:121], v68 offset:32768\n"
    "ds_read_b64_tr_b16 a[124:125], v67 offset:32768\n"
    "ds_read_b64_tr_b16 a[128:129], v66 offset:32768\n"
    "ds_read_b64_tr_b16 a[216:217], v65 offset:32768\n"
    "ds_read_b64_tr_b16 a[220:221], v64 offset:32768\n"
    "ds_read_b64_tr_b16 a[224:225], v63 offset:32768\n"
    "ds_read_b64_tr_b16 v[184:185], v62 offset:32768\n"
    "ds_read_b64_tr_b16 v[188:189], v61 offset:32768\n"
    "ds_read_b64_tr_b16 v[192:193], v60 offset:32768\n"
    "s_waitcnt vmcnt(24) lgkmcnt(0)\n"
    "s_barrier\n"
    "s_add_u32 m0, s54, 98304\n"
    "s_add_u32 s55, %6, 184320\n"
    "buffer_load_dwordx4 v8, s[44:47], s55 offen lds\n"
    "s_add_u32 m0, s54, 99328\n"
    "s_add_u32 s55, %6, 202752\n"
    "buffer_load_dwordx4 v8, s[44:47], s55 offen lds\n"
    "s_add_u32 m0, s54, 100352\n"
    "s_add_u32 s55, %6, 184448\n"
    "buffer_load_dwordx4 v8, s[44:47], s55 offen lds\n"
    "s_add_u32 m0, s54, 101376\n"
    "s_add_u32 s55, %6, 202880\n"
    "buffer_load_dwordx4 v8, s[44:47], s55 offen lds\n"
    "s_add_u32 m0, s54, 102400\n"
    "s_add_u32 s55, %6, 184576\n"
    "buffer_load_dwordx4 v8, s[44:47], s55 offen lds\n"
    "s_add_u32 m0, s54, 103424\n"
    "s_add_u32 s55, %6, 203008\n"
    "buffer_load_dwordx4 v8, s[44:47], s55 offen lds\n"
    "ds_read_b64_tr_b16 a[26:27], v71 offset:57344\n"
    "ds_read_b64_tr_b16 a[30:31], v70 offset:57344\n"
    "ds_read_b64_tr_b16 a[34:35], v69 offset:57344\n"
    "ds_read_b64_tr_b16 a[122:123], v68 offset:57344\n"
    "ds_read_b64_tr_b16 a[126:127], v67 offset:57344\n"
    "ds_read_b64_tr_b16 a[130:131], v66 offset:57344\n"
    "ds_read_b64_tr_b16 a[218:219], v65 offset:57344\n"
    "ds_read_b64_tr_b16 a[222:223], v64 offset:57344\n"
    "ds_read_b64_tr_b16 a[226:227], v63 offset:57344\n"
    "ds_read_b64_tr_b16 v[186:187], v62 offset:57344\n"
    "ds_read_b64_tr_b16 v[190:191], v61 offset:57344\n"
    "ds_read_b64_tr_b16 v[194:195], v60 offset:57344\n"
    "s_waitcnt vmcnt(24) lgkmcnt(0)\n"
    "s_barrier\n"
    "s_add_u32 m0, s54, 122880\n"
    "s_add_u32 s55, %6, 186624\n"
    "buffer_load_dwordx4 v8, s[44:47], s55 offen lds\n"
    "s_add_u32 m0, s54, 123904\n"
    "s_add_u32 s55, %6, 205056\n"
    "buffer_load_dwordx4 v8, s[44:47], s55 offen lds\n"
    "s_add_u32 m0, s54, 124928\n"
    "s_add_u32 s55, %6, 186752\n"
    "buffer_load_dwordx4 v8, s[44:47], s55 offen lds\n"
    "s_add_u32 m0, s54, 125952\n"
    "s_add_u32 s55, %6, 205184\n"
    "buffer_load_dwordx4 v8, s[44:47], s55 offen lds\n"
    "s_add_u32 m0, s54, 126976\n"
    "s_add_u32 s55, %6, 186880\n"
    "buffer_load_dwordx4 v8, s[44:47], s55 offen lds\n"
    "s_add_u32 m0, s54, 128000\n"
    "s_add_u32 s55, %6, 205312\n"
    "buffer_load_dwordx4 v8, s[44:47], s55 offen lds\n"
    "ds_read_b64_tr_b16 a[36:37], v32\n"
    "ds_read_b64_tr_b16 a[40:41], v33\n"
    "ds_read_b64_tr_b16 a[44:45], v34\n"
    "ds_read_b64_tr_b16 a[132:133], v35\n"
    "ds_read_b64_tr_b16 a[136:137], v36\n"
    "ds_read_b64_tr_b16 a[140:141], v37\n"
    "ds_read_b64_tr_b16 a[228:229], v38\n"
    "ds_read_b64_tr_b16 a[232:233], v39\n"
    "ds_read_b64_tr_b16 a[236:237], v40\n"
    "ds_read_b64_tr_b16 v[196:197], v41\n"
    "ds_read_b64_tr_b16 v[200:201], v42\n"
    "ds_read_b64_tr_b16 v[204:205], v43\n"
    "s_waitcnt vmcnt(24) lgkmcnt(0)\n"
    "s_barrier\n"
    "s_add_u32 m0, s54, 0\n"
    "s_add_u32 s55, %6, 221184\n"
    "buffer_load_dwordx4 v8, s[44:47], s55 offen lds\n"
    "s_add_u32 m0, s54, 1024\n"
    "s_add_u32 s55, %6, 239616\n"
    "buffer_load_dwordx4 v8, s[44:47], s55 offen lds\n"
    "s_add_u32 m0, s54, 2048\n"
    "s_add_u32 s55, %6, 221312\n"
    "buffer_load_dwordx4 v8, s[44:47], s55 offen lds\n"
    "s_add_u32 m0, s54, 3072\n"
    "s_add_u32 s55, %6, 239744\n"
    "buffer_load_dwordx4 v8, s[44:47], s55 offen lds\n"
    "s_add_u32 m0, s54, 4096\n"
    "s_add_u32 s55, %6, 221440\n"
    "buffer_load_dwordx4 v8, s[44:47], s55 offen lds\n"
    "s_add_u32 m0, s54, 5120\n"
    "s_add_u32 s55, %6, 239872\n"
    "buffer_load_dwordx4 v8, s[44:47], s55 offen lds\n"
    "ds_read_b64_tr_b16 a[38:39], v32 offset:24576\n"
    "ds_read_b64_tr_b16 a[42:43], v33 offset:24576\n"
    "ds_read_b64_tr_b16 a[46:47], v34 offset:24576\n"
    "ds_read_b64_tr_b16 a[134:135], v35 offset:24576\n"
    "ds_read_b64_tr_b16 a[138:139], v36 offset:24576\n"
    "ds_read_b64_tr_b16 a[142:143], v37 offset:24576\n"
    "ds_read_b64_tr_b16 a[230:231], v38 offset:24576\n"
    "ds_read_b64_tr_b16 a[234:235], v39 offset:24576\n"
    "ds_read_b64_tr_b16 a[238:239], v40 offset:24576\n"
    "ds_read_b64_tr_b16 v[198:199], v41 offset:24576\n"
    "ds_read_b64_tr_b16 v[202:203], v42 offset:24576\n"
    "ds_read_b64_tr_b16 v[206:207], v43 offset:24576\n"
    "s_waitcnt vmcnt(24) lgkmcnt(0)\n"
    "s_barrier\n"
    "s_add_u32 m0, s54, 24576\n"
    "s_add_u32 s55, %6, 223488\n"
    "buffer_load_dwordx4 v8, s[44:47], s55 offen lds\n"
    "s_add_u32 m0, s54, 25600\n"
    "s_add_u32 s55, %6, 241920\n"
    "buffer_load_dwordx4 v8, s[44:47], s55 offen lds\n"
    "s_add_u32 m0, s54, 26624\n"
    "s_add_u32 s55, %6, 223616\n"
    "buffer_load_dwordx4 v8, s[44:47], s55 offen lds\n"
    "s_add_u32 m0, s54, 27648\n"
    "s_add_u32 s55, %6, 242048\n"
    "buffer_load_dwordx4 v8, s[44:47], s55 offen lds\n"
    "s_add_u32 m0, s54, 28672\n"
    "s_add_u32 s55, %6, 223744\n"
    "buffer_load_dwordx4 v8, s[44:47], s55 offen lds\n"
    "s_add_u32 m0, s54, 29696\n"
    "s_add_u32 s55, %6, 242176\n"
    "buffer_load_dwordx4 v8, s[44:47], s55 offen lds\n"
    "ds_read_b64_tr_b16 a[48:49], v32 offset:49152\n"
    "ds_read_b64_tr_b16 a[52:53], v33 offset:49152\n"
    "ds_read_b64_tr_b16 a[56:57], v34 offset:49152\n"
    "ds_read_b64_tr_b16 a[144:145], v35 offset:49152\n"
    "ds_read_b64_tr_b16 a[148:149], v36 offset:49152\n"
    "ds_read_b64_tr_b16 a[152:153], v37 offset:49152\n"
    "ds_read_b64_tr_b16 a[240:241], v38 offset:49152\n"
    "ds_read_b64_tr_b16 a[244:245], v39 offset:49152\n"
    "ds_read_b64_tr_b16 a[248:249], v40 offset:49152\n"
    "ds_read_b64_tr_b16 v[208:209], v41 offset:49152\n"
    "ds_read_b64_tr_b16 v[212:213], v42 offset:49152\n"
    "ds_read_b64_tr_b16 v[216:217], v43 offset:49152\n"
    "s_waitcnt vmcnt(24) lgkmcnt(0)\n"
    "s_barrier\n"
    "s_add_u32 m0, s54, 49152\n"
    "s_add_u32 s55, %6, 258048\n"
    "buffer_load_dwordx4 v8, s[44:47], s55 offen lds\n"
    "s_add_u32 m0, s54, 50176\n"
    "s_add_u32 s55, %6, 276480\n"
    "buffer_load_dwordx4 v8, s[44:47], s55 offen lds\n"
    "s_add_u32 m0, s54, 51200\n"
    "s_add_u32 s55, %6, 258176\n"
    "buffer_load_dwordx4 v8, s[44:47], s55 offen lds\n"
    "s_add_u32 m0, s54, 52224\n"
    "s_add_u32 s55, %6, 276608\n"
    "buffer_load_dwordx4 v8, s[44:47], s55 offen lds\n"
    "s_add_u32 m0, s54, 53248\n"
    "s_add_u32 s55, %6, 258304\n"
    "buffer_load_dwordx4 v8, s[44:47], s55 offen lds\n"
    "s_add_u32 m0, s54, 54272\n"
    "s_add_u32 s55, %6, 276736\n"
    "buffer_load_dwordx4 v8, s[44:47], s55 offen lds\n"
    "ds_read_b64_tr_b16 a[50:51], v71 offset:8192\n"
    "ds_read_b64_tr_b16 a[54:55], v70 offset:8192\n"
    "ds_read_b64_tr_b16 a[58:59], v69 offset:8192\n"
    "ds_read_b64_tr_b16 a[146:147], v68 offset:8192\n"
    "ds_read_b64_tr_b16 a[150:151], v67 offset:8192\n"
    "ds_read_b64_tr_b16 a[154:155], v66 offset:8192\n"
    "ds_read_b64_tr_b16 a[242:243], v65 offset:8192\n"
    "ds_read_b64_tr_b16 a[246:247], v64 offset:8192\n"
    "ds_read_b64_tr_b16 a[250:251], v63 offset:8192\n"
    "ds_read_b64_tr_b16 v[210:211], v62 offset:8192\n"
    "ds_read_b64_tr_b16 v[214:215], v61 offset:8192\n"
    "ds_read_b64_tr_b16 v[218:219], v60 offset:8192\n"
    "s_waitcnt vmcnt(24) lgkmcnt(0)\n"
    "s_barrier\n"
    "s_add_u32 m0, s54, 73728\n"
    "s_add_u32 s55, %6, 260352\n"
    "buffer_load_dwordx4 v8, s[44:47], s55 offen lds\n"
    "s_add_u32 m0, s54, 74752\n"
    "s_add_u32 s55, %6, 278784\n"
    "buffer_load_dwordx4 v8, s[44:47], s55 offen lds\n"
    "s_add_u32 m0, s54, 75776\n"
    "s_add_u32 s55, %6, 260480\n"
    "buffer_load_dwordx4 v8, s[44:47], s55 offen lds\n"
    "s_add_u32 m0, s54, 76800\n"
    "s_add_u32 s55, %6, 278912\n"
    "buffer_load_dwordx4 v8, s[44:47], s55 offen lds\n"
    "s_add_u32 m0, s54, 77824\n"
    "s_add_u32 s55, %6, 260608\n"
    "buffer_load_dwordx4 v8, s[44:47], s55 offen lds\n"
    "s_add_u32 m0, s54, 78848\n"
    "s_add_u32 s55, %6, 279040\n"
    "buffer_load_dwordx4 v8, s[44:47], s55 offen lds\n"
    "v_mov_b32 v7, 0x20400\n"
    "global_load_dwordx2 v[20:21], v7, %1 sc1\n"
    "v_mbcnt_lo_u32_b32 v6, -1, 0\n"
    "v_min_u32 v6, 7, v6\n"
    "v_lshlrev_b32 v6, 7, v6\n"
    "v_add_u32 v6, 0x20000, v6\n"
    "global_load_dwordx2 v[22:23], v6, %1 sc1\n"
    "ds_read_b64_tr_b16 a[60:61], v71 offset:32768\n"
    "ds_read_b64_tr_b16 a[64:65], v70 offset:32768\n"
    "ds_read_b64_tr_b16 a[68:69], v69 offset:32768\n"
    "ds_read_b64_tr_b16 a[156:157], v68 offset:32768\n"
    "ds_read_b64_tr_b16 a[160:161], v67 offset:32768\n"
    "ds_read_b64_tr_b16 a[164:165], v66 offset:32768\n"
    "ds_read_b64_tr_b16 a[252:253], v65 offset:32768\n"
    "ds_read_b64_tr_b16 v[128:129], v64 offset:32768\n"
    "ds_read_b64_tr_b16 v[132:133], v63 offset:32768\n"
    "ds_read_b64_tr_b16 v[220:221], v62 offset:32768\n"
    "ds_read_b64_tr_b16 v[224:225], v61 offset:32768\n"
    "ds_read_b64_tr_b16 v[228:229], v60 offset:32768\n"
    "s_waitcnt vmcnt(26) lgkmcnt(0)\n"
    "s_barrier\n"
    "ds_read_b64_tr_b16 a[62:63], v71 offset:57344\n"
    "ds_read_b64_tr_b16 a[66:67], v70 offset:57344\n"
    "ds_read_b64_tr_b16 a[70:71], v69 offset:57344\n"
    "ds_read_b64_tr_b16 a[158:159], v68 offset:57344\n"
    "ds_read_b64_tr_b16 a[162:163], v67 offset:57344\n"
    "ds_read_b64_tr_b16 a[166:167], v66 offset:57344\n"
    "ds_read_b64_tr_b16 a[254:255], v65 offset:57344\n"
    "ds_read_b64_tr_b16 v[130:131], v64 offset:57344\n"
    "ds_read_b64_tr_b16 v[134:135], v63 offset:57344\n"
    "ds_read_b64_tr_b16 v[222:223], v62 offset:57344\n"
    "ds_read_b64_tr_b16 v[226:227], v61 offset:57344\n"
    "ds_read_b64_tr_b16 v[230:231], v60 offset:57344\n"
    "s_waitcnt vmcnt(20) lgkmcnt(0)\n"
    "s_barrier\n"
    "ds_read_b64_tr_b16 a[72:73], v32\n"
    "ds_read_b64_tr_b16 a[76:77], v33\n"
    "ds_read_b64_tr_b16 a[80:81], v34\n"
    "ds_read_b64_tr_b16 a[168:169], v35\n"
    "ds_read_b64_tr_b16 a[172:173], v36\n"
    "ds_read_b64_tr_b16 a[176:177], v37\n"
    "ds_read_b64_tr_b16 v[136:137], v38\n"
    "ds_read_b64_tr_b16 v[140:141], v39\n"
    "ds_read_b64_tr_b16 v[144:145], v40\n"
    "ds_read_b64_tr_b16 v[232:233], v41\n"
    "ds_read_b64_tr_b16 v[236:237], v42\n"
    "ds_read_b64_tr_b16 v[240:241], v43\n"
    "s_waitcnt vmcnt(14) lgkmcnt(0)\n"
    "s_barrier\n"
    "ds_read_b64_tr_b16 a[74:75], v32 offset:24576\n"
    "ds_read_b64_tr_b16 a[78:79], v33 offset:24576\n"
    "ds_read_b64_tr_b16 a[82:83], v34 offset:24576\n"
    "ds_read_b64_tr_b16 a[170:171], v35 offset:24576\n"
    "ds_read_b64_tr_b16 a[174:175], v36 offset:24576\n"
    "ds_read_b64_tr_b16 a[178:179], v37 offset:24576\n"
    "ds_read_b64_tr_b16 v[138:139], v38 offset:24576\n"
    "ds_read_b64_tr_b16 v[142:143], v39 offset:24576\n"
    "ds_read_b64_tr_b16 v[146:147], v40 offset:24576\n"
    "ds_read_b64_tr_b16 v[234:235], v41 offset:24576\n"
    "ds_read_b64_tr_b16 v[238:239], v42 offset:24576\n"
    "ds_read_b64_tr_b16 v[242:243], v43 offset:24576\n"
    "s_waitcnt vmcnt(8) lgkmcnt(0)\n"
    "s_barrier\n"
    "ds_read_b64_tr_b16 a[84:85], v32 offset:49152\n"
    "ds_read_b64_tr_b16 a[88:89], v33 offset:49152\n"
    "ds_read_b64_tr_b16 a[92:93], v34 offset:49152\n"
    "ds_read_b64_tr_b16 a[180:181], v35 offset:49152\n"
    "ds_read_b64_tr_b16 a[184:185], v36 offset:49152\n"
    "ds_read_b64_tr_b16 a[188:189], v37 offset:49152\n"
    "ds_read_b64_tr_b16 v[148:149], v38 offset:49152\n"
    "ds_read_b64_tr_b16 v[152:153], v39 offset:49152\n"
    "ds_read_b64_tr_b16 v[156:157], v40 offset:49152\n"
    "ds_read_b64_tr_b16 v[244:245], v41 offset:49152\n"
    "ds_read_b64_tr_b16 v[248:249], v42 offset:49152\n"
    "ds_read_b64_tr_b16 v[252:253], v43 offset:49152\n"
    "s_waitcnt vmcnt(2) lgkmcnt(0)\n"
    "s_barrier\n"
    "ds_read_b64_tr_b16 a[86:87], v71 offset:8192\n"
    "ds_read_b64_tr_b16 a[90:91], v70 offset:8192\n"
    "ds_read_b64_tr_b16 a[94:95], v69 offset:8192\n"
    "ds_read_b64_tr_b16 a[182:183], v68 offset:8192\n"
    "ds_read_b64_tr_b16 a[186:187], v67 offset:8192\n"
    "ds_read_b64_tr_b16 a[190:191], v66 offset:8192\n"
    "ds_read_b64_tr_b16 v[150:151], v65 offset:8192\n"
    "ds_read_b64_tr_b16 v[154:155], v64 offset:8192\n"
    "ds_read_b64_tr_b16 v[158:159], v63 offset:8192\n"
    "ds_read_b64_tr_b16 v[246:247], v62 offset:8192\n"
    "ds_read_b64_tr_b16 v[250:251], v61 offset:8192\n"
    "ds_read_b64_tr_b16 v[254:255], v60 offset:8192\n"
    "s_waitcnt lgkmcnt(0)\n"
    "s_barrier\n"
    "s_mov_b64 s[44:45], %1\n"
    "s_and_b32 s45, s45, 0xffff\n"
    "s_mov_b32 s46, 0x0\n"
    "s_mov_b32 s47, 0x20000\n"
    "v_mov_b32 v97, 0\n"
    "s_waitcnt vmcnt(0)\n"
    "v_add_u32 v20, 1, v20\n"
    "v_add_u32 v21, 1, v21\n"
    "s_mov_b32 s52, 1048576\n"
    ".Lwgo_a%=:\n"
    "v_cmp_ne_u64 vcc, v[20:21], v[22:23]\n"
    "s_cbranch_vccz .Lwgo_x%=\n"
    "global_load_dwordx2 v[22:23], v6, %1 sc1\n"
    "s_sub_u32 s52, s52, 1\n"
    "s_waitcnt vmcnt(0)\n"
    "s_cbranch_scc0 .Lwgo_a%=\n"
    ".Lwgo_x%=:\n"
    "v_mbcnt_lo_u32_b32 v20, -1, 0\n"
    "v_mbcnt_hi_u32_b32 v20, -1, v20\n"
    "v_min_u32 v6, 7, v20\n"
    "v_lshlrev_b32 v6, 7, v6\n"
    "v_mov_b32 v7, 0x26c00\n"
    "ds_read_b128 v[20:23], v7\n"
    "s_waitcnt lgkmcnt(0)\n"
    "v_readfirstlane_b32 s49, v21\n"
    "v_add_u32 v20, v20, v6\n"
    "s_branch .Lwchk_%=\n"
    ".Lwgo_%=:\n"
    "s_waitcnt lgkmcnt(3)\n"
    "v_mfma_f32_16x16x32_bf16 v[8:11], a[0:3], v[64:67], 0\n"
    "v_add_u32 v7, 32, v7\n"
    "v_mfma_f32_16x16x32_bf16 v[12:15], a[4:7], v[64:67], 0\n"
    "ds_read_b128 v[20:23], v7\n"
    "s_waitcnt lgkmcnt(3)\n"
    "v_mfma_f32_16x16x32_bf16 v[8:11], a[12:15], v[68:71], v[8:11]\n"
    "v_cvt_pk_bf16_f32 v28, v16, v17\n"
    "v_cvt_pk_bf16_f32 v30, v16, v17\n"
    "v_mfma_f32_16x16x32_bf16 v[12:15], a[16:19], v[68:71], v[12:15]\n"
    "v_cvt_pk_bf16_f32 v29, v18, v19\n"
    "v_cvt_pk_bf16_f32 v31, v18, v19\n"
    "s_waitcnt lgkmcnt(2)\n"
    "v_mfma_f32_16x16x32_bf16 v[8:11], a[24:27], v[72:75], v[8:11]\n"
    "s_waitcnt vmcnt(4)\n"
    "ds_read_b128 v[80:83], v1 offset:4096\n"
    "v_permlane16_swap_b32 v28, v30\n"
    "v_mfma_f32_16x16x32_bf16 v[12:15], a[28:31], v[72:75], v[12:15]\n"
    "ds_read_b128 v[84:87], v1 offset:5120\n"
    "v_permlane16_swap_b32 v29, v31\n"
    "s_waitcnt lgkmcnt(3)\n"
    "v_mfma_f32_16x16x32_bf16 v[8:11], a[36:39], v[76:79], v[8:11]\n"
    "buffer_store_dwordx4 v[28:31], v3, s[40:43], s62 offen sc1\n"
    "v_mfma_f32_16x16x32_bf16 v[12:15], a[40:43], v[76:79], v[12:15]\n"
    "s_waitcnt vmcnt(3)\n"
    "ds_read_b128 v[88:91], v1 offset:6144\n"
    "s_waitcnt lgkmcnt(3)\n"
    "v_readfirstlane_b32 s49, v21\n"
    "v_mfma_f32_16x16x32_bf16 v[16:19], a[8:11], v[64:67], 0\n"
    "ds_read_b128 v[92:95], v1 offset:7168\n"
    "v_readfirstlane_b32 s62, v22\n"
    "v_mfma_f32_16x16x32_bf16 v[16:19], a[20:23], v[68:71], v[16:19]\n"
    "v_add_u32 v20, v20, v6\n"
    ".Lwtop_%=:\n"
    "v_mfma_f32_16x16x32_bf16 v[16:19], a[32:35], v[72:75], v[16:19]\n"
    "v_xor_b32 v0, 0x2000, v1\n"
    "s_xor_b32 s58, s58, 0x8000\n"
    "v_mfma_f32_16x16x32_bf16 v[16:19], a[44:47], v[76:79], v[16:19]\n"
    "s_waitcnt lgkmcnt(3)\n"
    "v_mfma_f32_16x16x32_bf16 v[8:11], a[48:51], v[80:83], v[8:11]\n"
    "s_barrier\n"
    "ds_read_b128 v[32:35], v0\n"
    "v_mfma_f32_16x16x32_bf16 v[12:15], a[52:55], v[80:83], v[12:15]\n"
    "ds_read_b128 v[36:39], v0 offset:1024\n"
    "v_mfma_f32_16x16x32_bf16 v[16:19], a[56:59], v[80:83], v[16:19]\n"
    "ds_read_b128 v[40:43], v0 offset:2048\n"
    "s_waitcnt lgkmcnt(5)\n"
    "v_mfma_f32_16x16x32_bf16 v[8:11], a[60:63], v[84:87], v[8:11]\n"
    "ds_read_b128 v[44:47], v0 offset:3072\n"
    "v_mfma_f32_16x16x32_bf16 v[12:15], a[64:67], v[84:87], v[12:15]\n"
    "ds_read_b128 v[48:51], v0 offset:4096\n"
    "v_mfma_f32_16x16x32_bf16 v[16:19], a[68:71], v[84:87], v[16:19]\n"
    "ds_read_b128 v[52:55], v0 offset:5120\n"
    "s_waitcnt lgkmcnt(7)\n"
    "v_mfma_f32_16x16x32_bf16 v[8:11], a[72:75], v[88:91], v[8:11]\n"
    "ds_read_b128 v[56:59], v0 offset:6144\n"
    "s_waitcnt vmcnt(2)\n"
    "v_cmp_eq_u32 vcc, 0, v97\n"
    "s_cmp_eq_u64 vcc, 0\n"
    "v_mfma_f32_16x16x32_bf16 v[12:15], a[76:79], v[88:91], v[12:15]\n"
    "ds_read_b128 v[60:63], v0 offset:7168\n"
    "v_mfma_f32_16x16x32_bf16 v[16:19], a[80:83], v[88:91], v[16:19]\n"
    "s_cselect_b32 s94, s42, 0\n"
    "s_cselect_b32 s46, 0, 0x20000\n"
    "s_waitcnt lgkmcnt(8)\n"
    "v_mfma_f32_16x16x32_bf16 v[8:11], a[84:87], v[92:95], v[8:11]\n"
    "buffer_load_dword v96, v20, s[44:47], 0 offen sc1\n"
    "v_mfma_f32_16x16x32_bf16 v[12:15], a[88:91], v[92:95], v[12:15]\n"
    "s_mov_b32 m0, s58\n"
    "v_mfma_f32_16x16x32_bf16 v[16:19], a[92:95], v[92:95], v[16:19]\n"
    "buffer_load_dwordx4 v4, s[92:95], s49 offen sc1 lds\n"
    "s_waitcnt lgkmcnt(7)\n"
    "v_mfma_f32_16x16x32_bf16 v[8:11], a[96:99], v[32:35], v[8:11]\n"
    "ds_read_b32 v97, v7 offset:32\n"
    "v_mfma_f32_16x16x32_bf16 v[12:15], a[100:103], v[32:35], v[12:15]\n"
    "v_xor_b32 v0, 0x4000, v1\n"
    "v_mfma_f32_16x16x32_bf16 v[16:19], a[104:107], v[32:35], v[16:19]\n"
    "ds_read_b128 v[32:35], v0\n"
    "s_waitcnt lgkmcnt(8)\n"
    "v_mfma_f32_16x16x32_bf16 v[8:11], a[108:111], v[36:39], v[8:11]\n"
    "buffer_load_dwordx4 v4, s[92:95], s49 offen offset:1024 sc1 lds\n"
    "v_mfma_f32_16x16x32_bf16 v[12:15], a[112:115], v[36:39], v[12:15]\n"
    "v_mfma_f32_16x16x32_bf16 v[16:19], a[116:119], v[36:39], v[16:19]\n"
    "ds_read_b128 v[36:39], v0 offset:1024\n"
    "s_waitcnt lgkmcnt(2)\n"
    "v_add_u32 v97, v97, v6\n"
    "v_mfma_f32_16x16x32_bf16 v[8:11], a[120:123], v[40:43], v[8:11]\n"
    "v_mfma_f32_16x16x32_bf16 v[12:15], a[124:127], v[40:43], v[12:15]\n"
    "buffer_load_dwordx4 v4, s[92:95], s49 offen offset:2048 sc1 lds\n"
    "v_mfma_f32_16x16x32_bf16 v[16:19], a[128:131], v[40:43], v[16:19]\n"
    "ds_read_b128 v[40:43], v0 offset:2048\n"
    "v_mfma_f32_16x16x32_bf16 v[8:11], a[132:135], v[44:47], v[8:11]\n"
    "v_mfma_f32_16x16x32_bf16 v[12:15], a[136:139], v[44:47], v[12:15]\n"
    "v_mfma_f32_16x16x32_bf16 v[16:19], a[140:143], v[44:47], v[16:19]\n"
    "ds_read_b128 v[44:47], v0 offset:3072\n"
    "v_mfma_f32_16x16x32_bf16 v[8:11], a[144:147], v[48:51], v[8:11]\n"
    "buffer_load_dwordx4 v4, s[92:95], s49 offen offset:3072 sc1 lds\n"
    "v_mfma_f32_16x16x32_bf16 v[12:15], a[148:151], v[48:51], v[12:15]\n"
    "v_mfma_f32_16x16x32_bf16 v[16:19], a[152:155], v[48:51], v[16:19]\n"
    "ds_read_b128 v[48:51], v0 offset:4096\n"
    "v_mfma_f32_16x16x32_bf16 v[8:11], a[156:159], v[52:55], v[8:11]\n"
    "s_bitset1_b32 m0, 12\n"
    "v_mfma_f32_16x16x32_bf16 v[12:15], a[160:163], v[52:55], v[12:15]\n"
    "buffer_load_dwordx4 v5, s[92:95], s49 offen sc1 lds\n"
    "v_mov_b32 v22, 1\n"
    "v_mfma_f32_16x16x32_bf16 v[16:19], a[164:167], v[52:55], v[16:19]\n"
    "ds_read_b128 v[52:55], v0 offset:5120\n"
    "v_mfma_f32_16x16x32_bf16 v[8:11], a[168:171], v[56:59], v[8:11]\n"
    "s_waitcnt vmcnt(6)\n"
    "s_mov_b64 exec, s[56:57]\n"
    "ds_add_rtn_u32 v22, v7, v22 offset:16\n"
    "s_mov_b64 exec, -1\n"
    "v_mfma_f32_16x16x32_bf16 v[12:15], a[172:175], v[56:59], v[12:15]\n"
    "v_mfma_f32_16x16x32_bf16 v[16:19], a[176:179], v[56:59], v[16:19]\n"
    "ds_read_b128 v[56:59], v0 offset:6144\n"
    "v_mfma_f32_16x16x32_bf16 v[8:11], a[180:183], v[60:63], v[8:11]\n"
    "buffer_load_dwordx4 v5, s[92:95], s49 offen offset:1024 sc1 lds\n"
    "v_mfma_f32_16x16x32_bf16 v[12:15], a[184:187], v[60:63], v[12:15]\n"
    "v_mfma_f32_16x16x32_bf16 v[16:19], a[188:191], v[60:63], v[16:19]\n"
    "ds_read_b128 v[60:63], v0 offset:7168\n"
    "s_waitcnt lgkmcnt(2)\n"
    "v_readfirstlane_b32 s50, v22\n"
    "v_mfma_f32_16x16x32_bf16 v[8:11], a[192:195], v[32:35], v[8:11]\n"
    "s_cmp_eq_u32 s50, 3\n"
    "s_cselect_b32 s46, 0x20000, 0\n"
    "v_mfma_f32_16x16x32_bf16 v[12:15], a[196:199], v[32:35], v[12:15]\n"
    "buffer_store_dword v22, v23, s[44:47], 0 offen sc1\n"
    "v_xor_b32 v0, 0x6000, v1\n"
    "v_mfma_f32_16x16x32_bf16 v[16:19], a[200:203], v[32:35], v[16:19]\n"
    "ds_read_b128 v[32:35], v0\n"
    "v_mfma_f32_16x16x32_bf16 v[8:11], a[204:207], v[36:39], v[8:11]\n"
    "buffer_load_dwordx4 v5, s[92:95], s49 offen offset:2048 sc1 lds\n"
    "v_mfma_f32_16x16x32_bf16 v[12:15], a[208:211], v[36:39], v[12:15]\n"
    "v_mfma_f32_16x16x32_bf16 v[16:19], a[212:215], v[36:39], v[16:19]\n"
    "ds_read_b128 v[36:39], v0 offset:1024\n"
    "v_mfma_f32_16x16x32_bf16 v[8:11], a[216:219], v[40:43], v[8:11]\n"
    "v_mfma_f32_16x16x32_bf16 v[12:15], a[220:223], v[40:43], v[12:15]\n"
    "buffer_load_dwordx4 v5, s[92:95], s49 offen offset:3072 sc1 lds\n"
    "v_mfma_f32_16x16x32_bf16 v[16:19], a[224:227], v[40:43], v[16:19]\n"
    "ds_read_b128 v[40:43], v0 offset:2048\n"
    "v_mfma_f32_16x16x32_bf16 v[8:11], a[228:231], v[44:47], v[8:11]\n"
    "s_cmp_eq_u32 s94, 0\n"
    "s_cbranch_scc1 .Lwk1_%=\n"
    ".Lwk1_x%=:\n"
    "v_mfma_f32_16x16x32_bf16 v[12:15], a[232:235], v[44:47], v[12:15]\n"
    "v_mfma_f32_16x16x32_bf16 v[16:19], a[236:239], v[44:47], v[16:19]\n"
    "ds_read_b128 v[44:47], v0 offset:3072\n"
    "v_mfma_f32_16x16x32_bf16 v[8:11], a[240:243], v[48:51], v[8:11]\n"
    "v_mfma_f32_16x16x32_bf16 v[12:15], a[244:247], v[48:51], v[12:15]\n"
    "v_mfma_f32_16x16x32_bf16 v[16:19], a[248:251], v[48:51], v[16:19]\n"
    "ds_read_b128 v[48:51], v0 offset:4096\n"
    "v_mfma_f32_16x16x32_bf16 v[8:11], a[252:255], v[52:55], v[8:11]\n"
    "v_mfma_f32_16x16x32_bf16 v[12:15], v[128:131], v[52:55], v[12:15]\n"
    "v_mfma_f32_16x16x32_bf16 v[16:19], v[132:135], v[52:55], v[16:19]\n"
    "ds_read_b128 v[52:55], v0 offset:5120\n"
    "s_waitcnt lgkmcnt(7)\n"
    "v_mfma_f32_16x16x32_bf16 v[8:11], v[136:139], v[56:59], v[8:11]\n"
    "v_mfma_f32_16x16x32_bf16 v[12:15], v[140:143], v[56:59], v[12:15]\n"
    "v_mfma_f32_16x16x32_bf16 v[16:19], v[144:147], v[56:59], v[16:19]\n"
    "ds_read_b128 v[56:59], v0 offset:6144\n"
    "s_waitcnt lgkmcnt(7)\n"
    "v_mfma_f32_16x16x32_bf16 v[8:11], v[148:151], v[60:63], v[8:11]\n"
    "v_mfma_f32_16x16x32_bf16 v[12:15], v[152:155], v[60:63], v[12:15]\n"
    "v_mfma_f32_16x16x32_bf16 v[16:19], v[156:159], v[60:63], v[16:19]\n"
    "ds_read_b128 v[60:63], v0 offset:7168\n"
    "s_waitcnt lgkmcnt(7)\n"
    "v_mfma_f32_16x16x32_bf16 v[8:11], v[160:163], v[32:35], v[8:11]\n"
    "v_mfma_f32_16x16x32_bf16 v[12:15], v[164:167], v[32:35], v[12:15]\n"
    "v_mfma_f32_16x16x32_bf16 v[16:19], v[168:171], v[32:35], v[16:19]\n"
    "s_waitcnt lgkmcnt(6)\n"
    "v_mfma_f32_16x16x32_bf16 v[8:11], v[172:175], v[36:39], v[8:11]\n"
    "v_mfma_f32_16x16x32_bf16 v[12:15], v[176:179], v[36:39], v[12:15]\n"
    "v_mfma_f32_16x16x32_bf16 v[16:19], v[180:183], v[36:39], v[16:19]\n"
    "s_waitcnt lgkmcnt(5)\n"
    "v_mfma_f32_16x16x32_bf16 v[8:11], v[184:187], v[40:43], v[8:11]\n"
    "s_cmp_eq_u32 s94, 0\n"
    "s_cbranch_scc1 .Lwk2_%=\n"
    ".Lwk2_x%=:\n"
    "v_mfma_f32_16x16x32_bf16 v[12:15], v[188:191], v[40:43], v[12:15]\n"
    "v_mfma_f32_16x16x32_bf16 v[16:19], v[192:195], v[40:43], v[16:19]\n"
    "s_waitcnt lgkmcnt(4)\n"
    "v_mfma_f32_16x16x32_bf16 v[8:11], v[196:199], v[44:47], v[8:11]\n"
    "v_xor_b32 v1, 0x8000, v1\n"
    "v_mfma_f32_16x16x32_bf16 v[12:15], v[200:203], v[44:47], v[12:15]\n"
    "global_load_dword v97, v97, %1 sc1\n"
    "s_waitcnt lgkmcnt(3)\n"
    "v_mfma_f32_16x16x32_bf16 v[8:11], v[208:211], v[48:51], v[8:11]\n"
    "v_mfma_f32_16x16x32_bf16 v[12:15], v[212:215], v[48:51], v[12:15]\n"
    "s_waitcnt lgkmcnt(2)\n"
    "v_mfma_f32_16x16x32_bf16 v[8:11], v[220:223], v[52:55], v[8:11]\n"
    "v_mfma_f32_16x16x32_bf16 v[12:15], v[224:227], v[52:55], v[12:15]\n"
    "s_waitcnt lgkmcnt(1)\n"
    "v_mfma_f32_16x16x32_bf16 v[8:11], v[232:235], v[56:59], v[8:11]\n"
    "s_waitcnt vmcnt(5)\n"
    "ds_read_b128 v[64:67], v1\n"
    "v_mfma_f32_16x16x32_bf16 v[12:15], v[236:239], v[56:59], v[12:15]\n"
    "ds_read_b128 v[68:71], v1 offset:1024\n"
    "s_waitcnt lgkmcnt(2)\n"
    "v_mfma_f32_16x16x32_bf16 v[8:11], v[244:247], v[60:63], v[8:11]\n"
    "ds_read_b128 v[72:75], v1 offset:2048\n"
    "v_mfma_f32_16x16x32_bf16 v[12:15], v[248:251], v[60:63], v[12:15]\n"
    "ds_read_b128 v[76:79], v1 offset:3072\n"
    "v_mfma_f32_16x16x32_bf16 v[16:19], v[204:207], v[44:47], v[16:19]\n"
    "v_mfma_f32_16x16x32_bf16 v[16:19], v[216:219], v[48:51], v[16:19]\n"
    "v_cvt_pk_bf16_f32 v24, v8, v9\n"
    "v_cvt_pk_bf16_f32 v25, v10, v11\n"
    "v_mfma_f32_16x16x32_bf16 v[16:19], v[228:231], v[52:55], v[16:19]\n"
    "v_cvt_pk_bf16_f32 v26, v12, v13\n"
    "v_cvt_pk_bf16_f32 v27, v14, v15\n"
    "v_mfma_f32_16x16x32_bf16 v[16:19], v[240:243], v[56:59], v[16:19]\n"
    "buffer_store_dwordx4 v[24:27], v2, s[40:43], s62 offen sc1\n"
    "v_mfma_f32_16x16x32_bf16 v[16:19], v[252:255], v[60:63], v[16:19]\n"
    "s_mov_b32 s56, 1\n"
    "s_cmp_lg_u32 s94, 0\n"
    "s_cbranch_scc1 .Lwgo_%=\n"
    "ds_read_b32 v23, v7 offset:44\n"
    "s_nop 4\n"
    "v_cvt_pk_bf16_f32 v28, v16, v17\n"
    "v_cvt_pk_bf16_f32 v30, v16, v17\n"
    "v_cvt_pk_bf16_f32 v29, v18, v19\n"
    "v_cvt_pk_bf16_f32 v31, v18, v19\n"
    "v_permlane16_swap_b32 v28, v30\n"
    "s_nop 0\n"
    "v_permlane16_swap_b32 v29, v31\n"
    "buffer_store_dwordx4 v[28:31], v3, s[40:43], s62 offen sc1\n"
    "s_waitcnt vmcnt(10) lgkmcnt(0)\n"
    "v_cmp_eq_u32 vcc, 0, v96\n"
    "s_cmp_eq_u64 vcc, 0\n"
    "s_cbranch_scc1 .Lwld_%=\n"
    "global_load_dword v96, v20, %1 sc1\n"
    "s_waitcnt vmcnt(0)\n"
    "v_mov_b32 v22, 1\n"
    "s_mov_b64 exec, s[56:57]\n"
    "ds_add_rtn_u32 v22, v7, v22 offset:48\n"
    "s_mov_b64 exec, -1\n"
    "s_waitcnt lgkmcnt(0)\n"
    "v_readfirstlane_b32 s50, v22\n"
    "s_cmp_eq_u32 s50, 3\n"
    "s_cselect_b32 s46, 0x20000, 0\n"
    "buffer_store_dword v22, v23, s[44:47], 0 offen sc1\n"
    "s_mov_b32 s56, 0\n"
    "v_cmp_eq_u32 vcc, 0, v96\n"
    "s_cmp_eq_u64 vcc, 0\n"
    "s_cbranch_scc1 .Lwld_%=\n"
    ".Lwchk_%=:\n"
    "v_readfirstlane_b32 s50, v20\n"
    "s_cmp_lt_u32 s50, 200\n"
    "s_cbranch_scc1 .Lwend_%=\n"
    ".Lwwait_%=:\n"
    "global_load_dword v96, v20, %1 sc1\n"
    "s_mov_b32 s52, 0\n"
    ".Lwp3_%=:\n"
    "s_waitcnt vmcnt(0)\n"
    "v_cmp_eq_u32 vcc, 0, v96\n"
    "s_cmp_eq_u64 vcc, 0\n"
    "s_cbranch_scc1 .Lwp3_x%=\n"
    "s_add_u32 s52, s52, 1\n"
    "s_sleep 2\n"
    "global_load_dword v96, v20, %1 sc1\n"
    "s_cmp_lt_u32 s52, s53\n"
    "s_cbranch_scc1 .Lwp3_%=\n"
    "s_mov_b32 s53, 0\n"
    "v_mov_b32 v22, 0\n"
    "v_mov_b32 v23, 1\n"
    "global_store_dword v22, v23, %1 sc1\n"
    "s_waitcnt vmcnt(0)\n"
    ".Lwp3_x%=:\n"
    ".Lwld_%=:\n"
    "s_mov_b32 s94, s42\n"
    "s_mov_b32 m0, s58\n"
    "s_nop 0\n"
    "buffer_load_dwordx4 v4, s[92:95], s49 offen sc1 lds\n"
    "buffer_load_dwordx4 v4, s[92:95], s49 offen offset:1024 sc1 lds\n"
    "buffer_load_dwordx4 v4, s[92:95], s49 offen offset:2048 sc1 lds\n"
    "buffer_load_dwordx4 v4, s[92:95], s49 offen offset:3072 sc1 lds\n"
    "s_bitset1_b32 m0, 12\n"
    "s_nop 0\n"
    "buffer_load_dwordx4 v5, s[92:95], s49 offen sc1 lds\n"
    "buffer_load_dwordx4 v5, s[92:95], s49 offen offset:1024 sc1 lds\n"
    "buffer_load_dwordx4 v5, s[92:95], s49 offen offset:2048 sc1 lds\n"
    "buffer_load_dwordx4 v5, s[92:95], s49 offen offset:3072 sc1 lds\n"
    "s_waitcnt vmcnt(8)\n"
    "v_mov_b32 v22, 1\n"
    "s_mov_b64 exec, s[56:57]\n"
    "ds_add_rtn_u32 v22, v7, v22 offset:48\n"
    "s_mov_b64 exec, -1\n"
    "s_waitcnt lgkmcnt(0)\n"
    "v_readfirstlane_b32 s50, v22\n"
    "s_cmp_eq_u32 s50, 3\n"
    "s_cselect_b32 s46, 0x20000, 0\n"
    "buffer_store_dword v22, v23, s[44:47], 0 offen sc1\n"
    "s_mov_b32 s56, 0\n"
    "s_waitcnt vmcnt(5)\n"
    "ds_read_b128 v[64:67], v1\n"
    "ds_read_b128 v[68:71], v1 offset:1024\n"
    "ds_read_b128 v[72:75], v1 offset:2048\n"
    "ds_read_b128 v[76:79], v1 offset:3072\n"
    "s_waitcnt lgkmcnt(3)\n"
    "v_mfma_f32_16x16x32_bf16 v[8:11], a[0:3], v[64:67], 0\n"
    "v_add_u32 v7, 32, v7\n"
    "v_mfma_f32_16x16x32_bf16 v[12:15], a[4:7], v[64:67], 0\n"
    "ds_read_b128 v[20:23], v7\n"
    "s_waitcnt lgkmcnt(3)\n"
    "v_mfma_f32_16x16x32_bf16 v[8:11], a[12:15], v[68:71], v[8:11]\n"
    "v_mfma_f32_16x16x32_bf16 v[12:15], a[16:19], v[68:71], v[12:15]\n"
    "s_waitcnt lgkmcnt(2)\n"
    "v_mfma_f32_16x16x32_bf16 v[8:11], a[24:27], v[72:75], v[8:11]\n"
    "s_waitcnt vmcnt(1)\n"
    "ds_read_b128 v[80:83], v1 offset:4096\n"
    "v_mfma_f32_16x16x32_bf16 v[12:15], a[28:31], v[72:75], v[12:15]\n"
    "ds_read_b128 v[84:87], v1 offset:5120\n"
    "s_waitcnt lgkmcnt(3)\n"
    "v_mfma_f32_16x16x32_bf16 v[8:11], a[36:39], v[76:79], v[8:11]\n"
    "v_mfma_f32_16x16x32_bf16 v[12:15], a[40:43], v[76:79], v[12:15]\n"
    "ds_read_b128 v[88:91], v1 offset:6144\n"
    "s_waitcnt lgkmcnt(3)\n"
    "v_readfirstlane_b32 s49, v21\n"
    "v_mfma_f32_16x16x32_bf16 v[16:19], a[8:11], v[64:67], 0\n"
    "ds_read_b128 v[92:95], v1 offset:7168\n"
    "v_readfirstlane_b32 s62, v22\n"
    "v_mfma_f32_16x16x32_bf16 v[16:19], a[20:23], v[68:71], v[16:19]\n"
    "v_add_u32 v20, v20, v6\n"
    "s_branch .Lwtop_%=\n"
    ".Lwk1_%=:\n"
    "s_waitcnt vmcnt(8)\n"
    "v_cmp_eq_u32 vcc, 0, v96\n"
    "s_cmp_eq_u64 vcc, 0\n"
    "s_cselect_b32 s94, s42, 0\n"
    "s_cselect_b32 s46, 0, 0x20000\n"
    "buffer_load_dword v96, v20, s[44:47], 0 offen sc1\n"
    "s_mov_b32 m0, s58\n"
    "s_nop 0\n"
    "buffer_load_dwordx4 v4, s[92:95], s49 offen sc1 lds\n"
    "buffer_load_dwordx4 v4, s[92:95], s49 offen offset:1024 sc1 lds\n"
    "buffer_load_dwordx4 v4, s[92:95], s49 offen offset:2048 sc1 lds\n"
    "buffer_load_dwordx4 v4, s[92:95], s49 offen offset:3072 sc1 lds\n"
    "s_bitset1_b32 m0, 12\n"
    "s_nop 0\n"
    "buffer_load_dwordx4 v5, s[92:95], s49 offen sc1 lds\n"
    "buffer_load_dwordx4 v5, s[92:95], s49 offen offset:1024 sc1 lds\n"
    "buffer_load_dwordx4 v5, s[92:95], s49 offen offset:2048 sc1 lds\n"
    "buffer_load_dwordx4 v5, s[92:95], s49 offen offset:3072 sc1 lds\n"
    "s_branch .Lwk1_x%=\n"
    ".Lwk2_%=:\n"
    "s_waitcnt vmcnt(8)\n"
    "v_cmp_eq_u32 vcc, 0, v96\n"
    "s_cmp_eq_u64 vcc, 0\n"
    "s_cselect_b32 s94, s42, 0\n"
    "s_cselect_b32 s46, 0, 0x20000\n"
    "buffer_load_dword v96, v20, s[44:47], 0 offen sc1\n"
    "s_mov_b32 m0, s58\n"
    "s_nop 0\n"
    "buffer_load_dwordx4 v4, s[92:95], s49 offen sc1 lds\n"
    "buffer_load_dwordx4 v4, s[92:95], s49 offen offset:1024 sc1 lds\n"
    "buffer_load_dwordx4 v4, s[92:95], s49 offen offset:2048 sc1 lds\n"
    "buffer_load_dwordx4 v4, s[92:95], s49 offen offset:3072 sc1 lds\n"
    "s_bitset1_b32 m0, 12\n"
    "s_nop 0\n"
    "buffer_load_dwordx4 v5, s[92:95], s49 offen sc1 lds\n"
    "buffer_load_dwordx4 v5, s[92:95], s49 offen offset:1024 sc1 lds\n"
    "buffer_load_dwordx4 v5, s[92:95], s49 offen offset:2048 sc1 lds\n"
    "buffer_load_dwordx4 v5, s[92:95], s49 offen offset:3072 sc1 lds\n"
    "s_branch .Lwk2_x%=\n"
    ".Lwpol_%=:\n"
    "v_mov_b32 v20, 64\n"
    "global_load_dword v96, v20, %1 sc1\n"
    "s_mov_b32 s52, 0\n"
    ".Lwp4_%=:\n"
    "s_waitcnt vmcnt(0)\n"
    "v_cmp_eq_u32 vcc, 0, v96\n"
    "s_cmp_eq_u64 vcc, 0\n"
    "s_cbranch_scc1 .Lwp4_x%=\n"
    "s_add_u32 s52, s52, 1\n"
    "s_sleep 2\n"
    "global_load_dword v96, v20, %1 sc1\n"
    "s_cmp_lt_u32 s52, s53\n"
    "s_cbranch_scc1 .Lwp4_%=\n"
    "s_mov_b32 s53, 0\n"
    "v_mov_b32 v22, 0\n"
    "v_mov_b32 v23, 1\n"
    "global_store_dword v22, v23, %1 sc1\n"
    "s_waitcnt vmcnt(0)\n"
    ".Lwp4_x%=:\n"
    "v_readfirstlane_b32 s50, v96\n"
    "v_mbcnt_lo_u32_b32 v20, -1, 0\n"
    "v_mbcnt_hi_u32_b32 v20, -1, v20\n"
    "s_lshl_b32 s51, %2, 6\n"
    "v_add_u32 v20, s51, v20\n"
    "v_min_u32 v20, 129, v20\n"
    "v_lshlrev_b32 v20, 5, v20\n"
    "v_add_u32 v20, 0x26c00, v20\n"
    "ds_read2_b32 v[22:23], v20 offset0:1 offset1:2\n"
    "s_and_b32 s50, s50, 1\n"
    "s_waitcnt lgkmcnt(0)\n"
    "v_and_b32 v21, s50, v22\n"
    "v_lshlrev_b32 v21, 13, v21\n"
    "v_and_b32 v22, -2, v22\n"
    "v_xor_b32 v22, v21, v22\n"
    "v_and_b32 v21, s50, v23\n"
    "v_lshlrev_b32 v21, 13, v21\n"
    "v_and_b32 v23, -2, v23\n"
    "v_xor_b32 v23, v21, v23\n"
    "ds_write2_b32 v20, v22, v23 offset0:1 offset1:2\n"
    "s_waitcnt lgkmcnt(0)\n"
    "s_barrier\n"
    "ds_read_b32 v21, v7 offset:4\n"
    "v_add_u32 v20, %4, v6\n"
    "s_waitcnt lgkmcnt(0)\n"
    "v_readfirstlane_b32 s49, v21\n"
    "s_branch .Lwwait_%=\n"
    ".Lwend_%=:\n"
    "s_cmp_eq_u32 s50, 192\n"
    "s_cbranch_scc0 .Lwpol_%=\n"
    "s_waitcnt vmcnt(0) lgkmcnt(0)\n"
    "s_endpgm\n"
    ".p2align 6\n"
    ".Lrole3_%=:\n"
    "s_mov_b64 s[40:41], %0\n"
    "s_and_b32 s41, s41, 0xffff\n"
    "s_mov_b32 s42, 0xc000000\n"
    "s_mov_b32 s43, 0x20000\n"
    "s_mov_b64 s[92:93], %0\n"
    "s_and_b32 s93, s93, 0xffff\n"
    "s_mov_b32 s94, 0xc000000\n"
    "s_mov_b32 s95, 0x20000\n"
    "s_mov_b32 s53, 131072\n"
    "s_mov_b64 s[56:57], 0\n"
    "v_mbcnt_lo_u32_b32 v100, -1, 0\n"
    "v_mbcnt_hi_u32_b32 v100, -1, v100\n"
    "s_lshl_b32 s50, %2, 8\n"
    "s_add_u32 s50, s50, 144384\n"
    "v_lshl_add_u32 v100, v100, 2, s50\n"
    "ds_read_b32 v0, v100\n"
    "ds_read_b32 v1, v100 offset:1024\n"
    "ds_read_b32 v2, v100 offset:2048\n"
    "ds_read_b32 v3, v100 offset:3072\n"
    "ds_read_b32 v4, v100 offset:4096\n"
    "ds_read_b32 v5, v100 offset:5120\n"
    "ds_read_b32 v6, v100 offset:6144\n"
    "ds_read_b32 v7, v100 offset:7168\n"
    "ds_read_b32 v8, v100 offset:8192\n"
    "ds_read_b32 v152, v100 offset:9216\n"
    "ds_read_b32 v98, v100 offset:10240\n"
    "ds_read_b32 v99, v100 offset:11264\n"
    "s_waitcnt lgkmcnt(0)\n"
    "s_barrier\n"
    "v_mbcnt_lo_u32_b32 v100, -1, 0\n"
    "v_mbcnt_hi_u32_b32 v100, -1, v100\n"
    "v_add_u32 v101, 0, v100\n"
    "v_mul_u32_u24 v102, 7282, v101\n"
    "v_lshrrev_b32 v102, 16, v102\n"
    "v_and_b32 v103, 12, v102\n"
    "v_and_b32 v104, 48, v102\n"
    "v_mul_i32_i24 v104, 24192, v104\n"
    "v_mul_u32_u24 v103, 3456, v103\n"
    "v_mul_u32_u24 v102, 3312, v102\n"
    "v_add3_u32 v102, v102, v103, v104\n"
    "v_lshl_add_u32 v80, v101, 4, v102\n"
    "v_add_u32 v101, 64, v100\n"
    "v_mul_u32_u24 v102, 7282, v101\n"
    "v_lshrrev_b32 v102, 16, v102\n"
    "v_and_b32 v103, 12, v102\n"
    "v_and_b32 v104, 48, v102\n"
    "v_mul_i32_i24 v104, 24192, v104\n"
    "v_mul_u32_u24 v103, 3456, v103\n"
    "v_mul_u32_u24 v102, 3312, v102\n"
    "v_add3_u32 v102, v102, v103, v104\n"
    "v_lshl_add_u32 v81, v101, 4, v102\n"
    "v_add_u32 v101, 128, v100\n"
    "v_mul_u32_u24 v102, 7282, v101\n"
    "v_lshrrev_b32 v102, 16, v102\n"
    "v_and_b32 v103, 12, v102\n"
    "v_and_b32 v104, 48, v102\n"
    "v_mul_i32_i24 v104, 24192, v104\n"
    "v_mul_u32_u24 v103, 3456, v103\n"
    "v_mul_u32_u24 v102, 3312, v102\n"
    "v_add3_u32 v102, v102, v103, v104\n"
    "v_lshl_add_u32 v82, v101, 4, v102\n"
    "v_add_u32 v101, 192, v100\n"
    "v_mul_u32_u24 v102, 7282, v101\n"
    "v_lshrrev_b32 v102, 16, v102\n"
    "v_and_b32 v103, 12, v102\n"
    "v_and_b32 v104, 48, v102\n"
    "v_mul_i32_i24 v104, 24192, v104\n"
    "v_mul_u32_u24 v103, 3456, v103\n"
    "v_mul_u32_u24 v102, 3312, v102\n"
    "v_add3_u32 v102, v102, v103, v104\n"
    "v_lshl_add_u32 v83, v101, 4, v102\n"
    "v_add_u32 v101, 256, v100\n"
    "v_mul_u32_u24 v102, 7282, v101\n"
    "v_lshrrev_b32 v102, 16, v102\n"
    "v_and_b32 v103, 12, v102\n"
    "v_and_b32 v104, 48, v102\n"
    "v_mul_i32_i24 v104, 24192, v104\n"
    "v_mul_u32_u24 v103, 3456, v103\n"
    "v_mul_u32_u24 v102, 3312, v102\n"
    "v_add3_u32 v102, v102, v103, v104\n"
    "v_lshl_add_u32 v84, v101, 4, v102\n"
    "v_add_u32 v101, 320, v100\n"
    "v_mul_u32_u24 v102, 7282, v101\n"
    "v_lshrrev_b32 v102, 16, v102\n"
    "v_and_b32 v103, 12, v102\n"
    "v_and_b32 v104, 48, v102\n"
    "v_mul_i32_i24 v104, 24192, v104\n"
    "v_mul_u32_u24 v103, 3456, v103\n"
    "v_mul_u32_u24 v102, 3312, v102\n"
    "v_add3_u32 v102, v102, v103, v104\n"
    "v_lshl_add_u32 v85, v101, 4, v102\n"
    "v_add_u32 v101, 384, v100\n"
    "v_mul_u32_u24 v102, 7282, v101\n"
    "v_lshrrev_b32 v102, 16, v102\n"
    "v_and_b32 v103, 12, v102\n"
    "v_and_b32 v104, 48, v102\n"
    "v_mul_i32_i24 v104, 24192, v104\n"
    "v_mul_u32_u24 v103, 3456, v103\n"
    "v_mul_u32_u24 v102, 3312, v102\n"
    "v_add3_u32 v102, v102, v103, v104\n"
    "v_lshl_add_u32 v86, v101, 4, v102\n"
    "v_add_u32 v101, 448, v100\n"
    "v_mul_u32_u24 v102, 7282, v101\n"
    "v_lshrrev_b32 v102, 16, v102\n"
    "v_and_b32 v103, 12, v102\n"
    "v_and_b32 v104, 48, v102\n"
    "v_mul_i32_i24 v104, 24192, v104\n"
    "v_mul_u32_u24 v103, 3456, v103\n"
    "v_mul_u32_u24 v102, 3312, v102\n"
    "v_add3_u32 v102, v102, v103, v104\n"
    "v_lshl_add_u32 v87, v101, 4, v102\n"
    "v_add_u32 v101, 512, v100\n"
    "v_mul_u32_u24 v102, 7282, v101\n"
    "v_lshrrev_b32 v102, 16, v102\n"
    "v_and_b32 v103, 12, v102\n"
    "v_and_b32 v104, 48, v102\n"
    "v_mul_i32_i24 v104, 24192, v104\n"
    "v_mul_u32_u24 v103, 3456, v103\n"
    "v_mul_u32_u24 v102, 3312, v102\n"
    "v_add3_u32 v102, v102, v103, v104\n"
    "v_lshl_add_u32 v88, v101, 4, v102\n"
    "s_min_u32 s50, %2, 2\n"
    "s_mul_i32 s54, s50, 9216\n"
    "s_mul_i32 s50, s50, 1152\n"
    "s_add_u32 s51, s50, %6\n"
    "s_mov_b64 s[44:45], %8\n"
    "s_and_b32 s45, s45, 0xffff\n"
    "s_mov_b32 s46, 0x1e6000\n"
    "s_mov_b32 s47, 0x20000\n"
    "v_add_u32 v101, 0, v100\n"
    "v_mul_u32_u24 v102, 7282, v101\n"
    "v_lshrrev_b32 v102, 16, v102\n"
    "v_and_b32 v103, 12, v102\n"
    "v_and_b32 v104, 16, v102\n"
    "v_mul_i32_i24 v104, -2592, v104\n"
    "v_mul_u32_u24 v103, 3456, v103\n"
    "v_mul_u32_u24 v102, 3312, v102\n"
    "v_add3_u32 v102, v102, v103, v104\n"
    "v_lshl_add_u32 v89, v101, 4, v102\n"
    "v_add_u32 v101, 64, v100\n"
    "v_mul_u32_u24 v102, 7282, v101\n"
    "v_lshrrev_b32 v102, 16, v102\n"
    "v_and_b32 v103, 12, v102\n"
    "v_and_b32 v104, 16, v102\n"
    "v_mul_i32_i24 v104, -2592, v104\n"
    "v_mul_u32_u24 v103, 3456, v103\n"
    "v_mul_u32_u24 v102, 3312, v102\n"
    "v_add3_u32 v102, v102, v103, v104\n"
    "v_lshl_add_u32 v90, v101, 4, v102\n"
    "v_add_u32 v101, 128, v100\n"
    "v_mul_u32_u24 v102, 7282, v101\n"
    "v_lshrrev_b32 v102, 16, v102\n"
    "v_and_b32 v103, 12, v102\n"
    "v_and_b32 v104, 16, v102\n"
    "v_mul_i32_i24 v104, -2592, v104\n"
    "v_mul_u32_u24 v103, 3456, v103\n"
    "v_mul_u32_u24 v102, 3312, v102\n"
    "v_add3_u32 v102, v102, v103, v104\n"
    "v_lshl_add_u32 v91, v101, 4, v102\n"
    "v_add_u32 v101, 192, v100\n"
    "v_mul_u32_u24 v102, 7282, v101\n"
    "v_lshrrev_b32 v102, 16, v102\n"
    "v_and_b32 v103, 12, v102\n"
    "v_and_b32 v104, 16, v102\n"
    "v_mul_i32_i24 v104, -2592, v104\n"
    "v_mul_u32_u24 v103, 3456, v103\n"
    "v_mul_u32_u24 v102, 3312, v102\n"
    "v_add3_u32 v102, v102, v103, v104\n"
    "v_lshl_add_u32 v92, v101, 4, v102\n"
    "v_add_u32 v101, 256, v100\n"
    "v_mul_u32_u24 v102, 7282, v101\n"
    "v_lshrrev_b32 v102, 16, v102\n"
    "v_and_b32 v103, 12, v102\n"
    "v_and_b32 v104, 16, v102\n"
    "v_mul_i32_i24 v104, -2592, v104\n"
    "v_mul_u32_u24 v103, 3456, v103\n"
    "v_mul_u32_u24 v102, 3312, v102\n"
    "v_add3_u32 v102, v102, v103, v104\n"
    "v_lshl_add_u32 v93, v101, 4, v102\n"
    "v_add_u32 v101, 320, v100\n"
    "v_mul_u32_u24 v102, 7282, v101\n"
    "v_lshrrev_b32 v102, 16, v102\n"
    "v_and_b32 v103, 12, v102\n"
    "v_and_b32 v104, 16, v102\n"
    "v_mul_i32_i24 v104, -2592, v104\n"
    "v_mul_u32_u24 v103, 3456, v103\n"
    "v_mul_u32_u24 v102, 3312, v102\n"
    "v_add3_u32 v102, v102, v103, v104\n"
    "v_lshl_add_u32 v94, v101, 4, v102\n"
    "v_add_u32 v101, 384, v100\n"
    "v_mul_u32_u24 v102, 7282, v101\n"
    "v_lshrrev_b32 v102, 16, v102\n"
    "v_and_b32 v103, 12, v102\n"
    "v_and_b32 v104, 16, v102\n"
    "v_mul_i32_i24 v104, -2592, v104\n"
    "v_mul_u32_u24 v103, 3456, v103\n"
    "v_mul_u32_u24 v102, 3312, v102\n"
    "v_add3_u32 v102, v102, v103, v104\n"
    "v_lshl_add_u32 v95, v101, 4, v102\n"
    "v_add_u32 v101, 448, v100\n"
    "v_mul_u32_u24 v102, 7282, v101\n"
    "v_lshrrev_b32 v102, 16, v102\n"
    "v_and_b32 v103, 12, v102\n"
    "v_and_b32 v104, 16, v102\n"
    "v_mul_i32_i24 v104, -2592, v104\n"
    "v_mul_u32_u24 v103, 3456, v103\n"
    "v_mul_u32_u24 v102, 3312, v102\n"
    "v_add3_u32 v102, v102, v103, v104\n"
    "v_lshl_add_u32 v96, v101, 4, v102\n"
    "v_add_u32 v101, 512, v100\n"
    "v_mul_u32_u24 v102, 7282, v101\n"
    "v_lshrrev_b32 v102, 16, v102\n"
    "v_and_b32 v103, 12, v102\n"
    "v_and_b32 v104, 16, v102\n"
    "v_mul_i32_i24 v104, -2592, v104\n"
    "v_mul_u32_u24 v103, 3456, v103\n"
    "v_mul_u32_u24 v102, 3312, v102\n"
    "v_add3_u32 v102, v102, v103, v104\n"
    "v_lshl_add_u32 v97, v101, 4, v102\n"
    "v_add_u32 v9, 0x21c00, v3\n"
    "v_add_u32 v10, 1024, v6\n"
    "s_lshl_b32 s58, %2, 13\n"
    "s_add_u32 s58, s58, 0\n"
    "s_add_u32 s88, s40, 32768\n"
    "s_mov_b32 s89, s41\n"
    "s_cmp_lt_u32 %2, 2\n"
    "s_cselect_b32 s96, s42, 0\n"
    "s_mov_b32 s91, s43\n"
    "s_mul_i32 s59, %2, 576\n"
    "s_add_u32 s59, s59, 3072\n"
    "s_bfm_b64 s[60:61], 36, 0\n"
    "s_xor_b32 s50, %2, 0\n"
    "s_mul_i32 s50, s50, 2304\n"
    "v_add_u32 v106, s50, v98\n"
    "v_add_u32 v107, s50, v99\n"
    "s_xor_b32 s50, %2, 1\n"
    "s_mul_i32 s50, s50, 2304\n"
    "v_add_u32 v108, s50, v98\n"
    "v_add_u32 v109, s50, v99\n"
    "s_xor_b32 s50, %2, 2\n"
    "s_mul_i32 s50, s50, 2304\n"
    "v_add_u32 v110, s50, v98\n"
    "v_add_u32 v111, s50, v99\n"
    "s_xor_b32 s50, %2, 3\n"
    "s_mul_i32 s50, s50, 2304\n"
    "v_add_u32 v112, s50, v98\n"
    "v_add_u32 v113, s50, v99\n"
    "s_xor_b32 s50, %2, 4\n"
    "s_and_b32 s50, s50, 1\n"
    "s_lshl_b32 s50, s50, 1\n"
    "s_mul_i32 s50, s50, 2304\n"
    "v_add_u32 v114, s50, v98\n"
    "v_add_u32 v115, s50, v99\n"
    "s_xor_b32 s50, %2, 5\n"
    "s_and_b32 s50, s50, 1\n"
    "s_lshl_b32 s50, s50, 1\n"
    "s_mul_i32 s50, s50, 2304\n"
    "v_add_u32 v116, s50, v98\n"
    "v_add_u32 v117, s50, v99\n"
    "v_mov_b32 v56, 0x3f803f80\n"
    "v_mov_b32 v57, 0x3f803f80\n"
    "v_mov_b32 v58, 0x3f803f80\n"
    "v_mov_b32 v59, 0x3f803f80\n"
    "s_cmp_eq_u32 %2, 3\n"
    "s_cbranch_scc1 .Lac1_%=\n"
    "s_add_u32 s55, s51, 0\n"
    "s_add_u32 m0, s54, 0\n"
    "s_nop 0\n"
    "buffer_load_dwordx4 v80, s[44:47], s55 offen lds\n"
    "s_add_u32 m0, s54, 1024\n"
    "s_nop 0\n"
    "buffer_load_dwordx4 v81, s[44:47], s55 offen lds\n"
    "s_add_u32 m0, s54, 2048\n"
    "s_nop 0\n"
    "buffer_load_dwordx4 v82, s[44:47], s55 offen lds\n"
    "s_add_u32 m0, s54, 3072\n"
    "s_nop 0\n"
    "buffer_load_dwordx4 v83, s[44:47], s55 offen lds\n"
    "s_add_u32 m0, s54, 4096\n"
    "s_nop 0\n"
    "buffer_load_dwordx4 v84, s[44:47], s55 offen lds\n"
    "s_add_u32 m0, s54, 5120\n"
    "s_nop 0\n"
    "buffer_load_dwordx4 v85, s[44:47], s55 offen lds\n"
    "s_add_u32 m0, s54, 6144\n"
    "s_nop 0\n"
    "buffer_load_dwordx4 v86, s[44:47], s55 offen lds\n"
    "s_add_u32 m0, s54, 7168\n"
    "s_nop 0\n"
    "buffer_load_dwordx4 v87, s[44:47], s55 offen lds\n"
    "s_add_u32 m0, s54, 8192\n"
    "s_nop 0\n"
    "buffer_load_dwordx4 v88, s[44:47], s55 offen lds\n"
    ".Lac1_%=:\n"
    "s_cmp_eq_u32 %2, 3\n"
    "s_cbranch_scc1 .Lac2_%=\n"
    "s_add_u32 s55, s51, 13824\n"
    "s_add_u32 m0, s54, 27648\n"
    "s_nop 0\n"
    "buffer_load_dwordx4 v80, s[44:47], s55 offen lds\n"
    "s_add_u32 m0, s54, 28672\n"
    "s_nop 0\n"
    "buffer_load_dwordx4 v81, s[44:47], s55 offen lds\n"
    "s_add_u32 m0, s54, 29696\n"
    "s_nop 0\n"
    "buffer_load_dwordx4 v82, s[44:47], s55 offen lds\n"
    "s_add_u32 m0, s54, 30720\n"
    "s_nop 0\n"
    "buffer_load_dwordx4 v83, s[44:47], s55 offen lds\n"
    "s_add_u32 m0, s54, 31744\n"
    "s_nop 0\n"
    "buffer_load_dwordx4 v84, s[44:47], s55 offen lds\n"
    "s_add_u32 m0, s54, 32768\n"
    "s_nop 0\n"
    "buffer_load_dwordx4 v85, s[44:47], s55 offen lds\n"
    "s_add_u32 m0, s54, 33792\n"
    "s_nop 0\n"
    "buffer_load_dwordx4 v86, s[44:47], s55 offen lds\n"
    "s_add_u32 m0, s54, 34816\n"
    "s_nop 0\n"
    "buffer_load_dwordx4 v87, s[44:47], s55 offen lds\n"
    "s_add_u32 m0, s54, 35840\n"
    "s_nop 0\n"
    "buffer_load_dwordx4 v88, s[44:47], s55 offen lds\n"
    ".Lac2_%=:\n"
    "s_cmp_eq_u32 %2, 3\n"
    "s_cbranch_scc1 .Lac3_%=\n"
    "s_add_u32 s55, s51, 110592\n"
    "s_add_u32 m0, s54, 55296\n"
    "s_nop 0\n"
    "buffer_load_dwordx4 v80, s[44:47], s55 offen lds\n"
    "s_add_u32 m0, s54, 56320\n"
    "s_nop 0\n"
    "buffer_load_dwordx4 v81, s[44:47], s55 offen lds\n"
    "s_add_u32 m0, s54, 57344\n"
    "s_nop 0\n"
    "buffer_load_dwordx4 v82, s[44:47], s55 offen lds\n"
    "s_add_u32 m0, s54, 58368\n"
    "s_nop 0\n"
    "buffer_load_dwordx4 v83, s[44:47], s55 offen lds\n"
    "s_add_u32 m0, s54, 59392\n"
    "s_nop 0\n"
    "buffer_load_dwordx4 v84, s[44:47], s55 offen lds\n"
    "s_add_u32 m0, s54, 60416\n"
    "s_nop 0\n"
    "buffer_load_dwordx4 v85, s[44:47], s55 offen lds\n"
    "s_add_u32 m0, s54, 61440\n"
    "s_nop 0\n"
    "buffer_load_dwordx4 v86, s[44:47], s55 offen lds\n"
    "s_add_u32 m0, s54, 62464\n"
    "s_nop 0\n"
    "buffer_load_dwordx4 v87, s[44:47], s55 offen lds\n"
    "s_add_u32 m0, s54, 63488\n"
    "s_nop 0\n"
    "buffer_load_dwordx4 v88, s[44:47], s55 offen lds\n"
    ".Lac3_%=:\n"
    "s_cmp_eq_u32 %2, 3\n"
    "s_cbranch_scc1 .Lac4_%=\n"
    "s_add_u32 s55, s51, 124416\n"
    "s_add_u32 m0, s54, 82944\n"
    "s_nop 0\n"
    "buffer_load_dwordx4 v80, s[44:47], s55 offen lds\n"
    "s_add_u32 m0, s54, 83968\n"
    "s_nop 0\n"
    "buffer_load_dwordx4 v81, s[44:47], s55 offen lds\n"
    "s_add_u32 m0, s54, 84992\n"
    "s_nop 0\n"
    "buffer_load_dwordx4 v82, s[44:47], s55 offen lds\n"
    "s_add_u32 m0, s54, 86016\n"
    "s_nop 0\n"
    "buffer_load_dwordx4 v83, s[44:47], s55 offen lds\n"
    "s_add_u32 m0, s54, 87040\n"
    "s_nop 0\n"
    "buffer_load_dwordx4 v84, s[44:47], s55 offen lds\n"
    "s_add_u32 m0, s54, 88064\n"
    "s_nop 0\n"
    "buffer_load_dwordx4 v85, s[44:47], s55 offen lds\n"
    "s_add_u32 m0, s54, 89088\n"
    "s_nop 0\n"
    "buffer_load_dwordx4 v86, s[44:47], s55 offen lds\n"
    "s_add_u32 m0, s54, 90112\n"
    "s_nop 0\n"
    "buffer_load_dwordx4 v87, s[44:47], s55 offen lds\n"
    "s_add_u32 m0, s54, 91136\n"
    "s_nop 0\n"
    "buffer_load_dwordx4 v88, s[44:47], s55 offen lds\n"
    ".Lac4_%=:\n"
    "s_cmp_eq_u32 %2, 3\n"
    "s_cbranch_scc1 .Lac5_%=\n"
    "s_add_u32 s55, s51, 221184\n"
    "s_add_u32 m0, s54, 110592\n"
    "s_nop 0\n"
    "buffer_load_dwordx4 v80, s[44:47], s55 offen lds\n"
    "s_add_u32 m0, s54, 111616\n"
    "s_nop 0\n"
    "buffer_load_dwordx4 v81, s[44:47], s55 offen lds\n"
    "s_add_u32 m0, s54, 112640\n"
    "s_nop 0\n"
    "buffer_load_dwordx4 v82, s[44:47], s55 offen lds\n"
    "s_add_u32 m0, s54, 113664\n"
    "s_nop 0\n"
    "buffer_load_dwordx4 v83, s[44:47], s55 offen lds\n"
    "s_add_u32 m0, s54, 114688\n"
    "s_nop 0\n"
    "buffer_load_dwordx4 v84, s[44:47], s55 offen lds\n"
    "s_add_u32 m0, s54, 115712\n"
    "s_nop 0\n"
    "buffer_load_dwordx4 v85, s[44:47], s55 offen lds\n"
    "s_add_u32 m0, s54, 116736\n"
    "s_nop 0\n"
    "buffer_load_dwordx4 v86, s[44:47], s55 offen lds\n"
    "s_add_u32 m0, s54, 117760\n"
    "s_nop 0\n"
    "buffer_load_dwordx4 v87, s[44:47], s55 offen lds\n"
    "s_add_u32 m0, s54, 118784\n"
    "s_nop 0\n"
    "buffer_load_dwordx4 v88, s[44:47], s55 offen lds\n"
    ".Lac5_%=:\n"
    "s_waitcnt vmcnt(36) lgkmcnt(0)\n"
    "s_barrier\n"
    "ds_read_b64_tr_b16 a[0:1], v106\n"
    "ds_read_b64_tr_b16 a[72:73], v106 offset:9216\n"
    "ds_read_b64_tr_b16 a[144:145], v106 offset:18432\n"
    "ds_read_b64_tr_b16 a[216:217], v107\n"
    "ds_read_b64_tr_b16 a[16:17], v108\n"
    "ds_read_b64_tr_b16 a[88:89], v108 offset:9216\n"
    "ds_read_b64_tr_b16 a[160:161], v108 offset:18432\n"
    "ds_read_b64_tr_b16 a[32:33], v110\n"
    "ds_read_b64_tr_b16 a[104:105], v110 offset:9216\n"
    "ds_read_b64_tr_b16 a[176:177], v110 offset:18432\n"
    "ds_read_b64_tr_b16 a[232:233], v111\n"
    "ds_read_b64_tr_b16 a[48:49], v112\n"
    "ds_read_b64_tr_b16 a[120:121], v112 offset:9216\n"
    "ds_read_b64_tr_b16 a[192:193], v112 offset:18432\n"
    "s_waitcnt vmcnt(27) lgkmcnt(0)\n"
    "s_barrier\n"
    "s_cmp_eq_u32 %2, 3\n"
    "s_cbranch_scc1 .Lac6_%=\n"
    "s_add_u32 s55, s51, 235008\n"
    "s_add_u32 m0, s54, 0\n"
    "s_nop 0\n"
    "buffer_load_dwordx4 v80, s[44:47], s55 offen lds\n"
    "s_add_u32 m0, s54, 1024\n"
    "s_nop 0\n"
    "buffer_load_dwordx4 v81, s[44:47], s55 offen lds\n"
    "s_add_u32 m0, s54, 2048\n"
    "s_nop 0\n"
    "buffer_load_dwordx4 v82, s[44:47], s55 offen lds\n"
    "s_add_u32 m0, s54, 3072\n"
    "s_nop 0\n"
    "buffer_load_dwordx4 v83, s[44:47], s55 offen lds\n"
    "s_add_u32 m0, s54, 4096\n"
    "s_nop 0\n"
    "buffer_load_dwordx4 v84, s[44:47], s55 offen lds\n"
    "s_add_u32 m0, s54, 5120\n"
    "s_nop 0\n"
    "buffer_load_dwordx4 v85, s[44:47], s55 offen lds\n"
    "s_add_u32 m0, s54, 6144\n"
    "s_nop 0\n"
    "buffer_load_dwordx4 v86, s[44:47], s55 offen lds\n"
    "s_add_u32 m0, s54, 7168\n"
    "s_nop 0\n"
    "buffer_load_dwordx4 v87, s[44:47], s55 offen lds\n"
    "s_add_u32 m0, s54, 8192\n"
    "s_nop 0\n"
    "buffer_load_dwordx4 v88, s[44:47], s55 offen lds\n"
    ".Lac6_%=:\n"
    "ds_read_b64_tr_b16 a[2:3], v106 offset:27648\n"
    "ds_read_b64_tr_b16 a[74:75], v106 offset:36864\n"
    "ds_read_b64_tr_b16 a[146:147], v106 offset:46080\n"
    "ds_read_b64_tr_b16 a[218:219], v107 offset:27648\n"
    "ds_read_b64_tr_b16 a[18:19], v108 offset:27648\n"
    "ds_read_b64_tr_b16 a[90:91], v108 offset:36864\n"
    "ds_read_b64_tr_b16 a[162:163], v108 offset:46080\n"
    "ds_read_b64_tr_b16 a[34:35], v110 offset:27648\n"
    "ds_read_b64_tr_b16 a[106:107], v110 offset:36864\n"
    "ds_read_b64_tr_b16 a[178:179], v110 offset:46080\n"
    "ds_read_b64_tr_b16 a[234:235], v111 offset:27648\n"
    "ds_read_b64_tr_b16 a[50:51], v112 offset:27648\n"
    "ds_read_b64_tr_b16 a[122:123], v112 offset:36864\n"
    "ds_read_b64_tr_b16 a[194:195], v112 offset:46080\n"
    "s_waitcnt vmcnt(27) lgkmcnt(0)\n"
    "s_barrier\n"
    "s_cmp_eq_u32 %2, 3\n"
    "s_cbranch_scc1 .Lac7_%=\n"
    "s_add_u32 s55, s51, 331776\n"
    "s_add_u32 m0, s54, 27648\n"
    "s_nop 0\n"
    "buffer_load_dwordx4 v80, s[44:47], s55 offen lds\n"
    "s_add_u32 m0, s54, 28672\n"
    "s_nop 0\n"
    "buffer_load_dwordx4 v81, s[44:47], s55 offen lds\n"
    "s_add_u32 m0, s54, 29696\n"
    "s_nop 0\n"
    "buffer_load_dwordx4 v82, s[44:47], s55 offen lds\n"
    "s_add_u32 m0, s54, 30720\n"
    "s_nop 0\n"
    "buffer_load_dwordx4 v83, s[44:47], s55 offen lds\n"
    "s_add_u32 m0, s54, 31744\n"
    "s_nop 0\n"
    "buffer_load_dwordx4 v84, s[44:47], s55 offen lds\n"
    "s_add_u32 m0, s54, 32768\n"
    "s_nop 0\n"
    "buffer_load_dwordx4 v85, s[44:47], s55 offen lds\n"
    "s_add_u32 m0, s54, 33792\n"
    "s_nop 0\n"
    "buffer_load_dwordx4 v86, s[44:47], s55 offen lds\n"
    "s_add_u32 m0, s54, 34816\n"
    "s_nop 0\n"
    "buffer_load_dwordx4 v87, s[44:47], s55 offen lds\n"
    "s_add_u32 m0, s54, 35840\n"
    "s_nop 0\n"
    "buffer_load_dwordx4 v88, s[44:47], s55 offen lds\n"
    ".Lac7_%=:\n"
    "v_mfma_f32_16x16x32_bf16 v[12:15], v[56:59], a[0:3], 0\n"
    "v_mfma_f32_16x16x32_bf16 v[16:19], v[56:59], a[72:75], 0\n"
    "v_mfma_f32_16x16x32_bf16 v[20:23], v[56:59], a[144:147], 0\n"
    "v_mfma_f32_16x16x32_bf16 v[24:27], v[56:59], a[216:219], 0\n"
    "v_mfma_f32_16x16x32_bf16 v[12:15], v[56:59], a[16:19], v[12:15]\n"
    "v_mfma_f32_16x16x32_bf16 v[16:19], v[56:59], a[88:91], v[16:19]\n"
    "v_mfma_f32_16x16x32_bf16 v[20:23], v[56:59], a[160:163], v[20:23]\n"
    "v_mfma_f32_16x16x32_bf16 v[12:15], v[56:59], a[32:35], v[12:15]\n"
    "v_mfma_f32_16x16x32_bf16 v[16:19], v[56:59], a[104:107], v[16:19]\n"
    "v_mfma_f32_16x16x32_bf16 v[20:23], v[56:59], a[176:179], v[20:23]\n"
    "v_mfma_f32_16x16x32_bf16 v[24:27], v[56:59], a[232:235], v[24:27]\n"
    "v_mfma_f32_16x16x32_bf16 v[12:15], v[56:59], a[48:51], v[12:15]\n"
    "v_mfma_f32_16x16x32_bf16 v[16:19], v[56:59], a[120:123], v[16:19]\n"
    "v_mfma_f32_16x16x32_bf16 v[20:23], v[56:59], a[192:195], v[20:23]\n"
    "ds_read_b64_tr_b16 a[4:5], v106 offset:55296\n"
    "ds_read_b64_tr_b16 a[76:77], v106 offset:64512\n"
    "v_add_u32 v149, 0x10000, v106\n"
    "ds_read_b64_tr_b16 a[148:149], v149 offset:8192\n"
    "ds_read_b64_tr_b16 a[220:221], v107 offset:55296\n"
    "ds_read_b64_tr_b16 a[20:21], v108 offset:55296\n"
    "ds_read_b64_tr_b16 a[92:93], v108 offset:64512\n"
    "v_add_u32 v148, 0x10000, v108\n"
    "ds_read_b64_tr_b16 a[164:165], v148 offset:8192\n"
    "ds_read_b64_tr_b16 a[36:37], v110 offset:55296\n"
    "ds_read_b64_tr_b16 a[108:109], v110 offset:64512\n"
    "v_add_u32 v147, 0x10000, v110\n"
    "ds_read_b64_tr_b16 a[180:181], v147 offset:8192\n"
    "ds_read_b64_tr_b16 a[236:237], v111 offset:55296\n"
    "ds_read_b64_tr_b16 a[52:53], v112 offset:55296\n"
    "ds_read_b64_tr_b16 a[124:125], v112 offset:64512\n"
    "v_add_u32 v146, 0x10000, v112\n"
    "ds_read_b64_tr_b16 a[196:197], v146 offset:8192\n"
    "s_waitcnt vmcnt(27) lgkmcnt(0)\n"
    "s_barrier\n"
    "s_cmp_eq_u32 %2, 3\n"
    "s_cbranch_scc1 .Lac8_%=\n"
    "s_add_u32 s55, s51, 345600\n"
    "s_add_u32 m0, s54, 55296\n"
    "s_nop 0\n"
    "buffer_load_dwordx4 v80, s[44:47], s55 offen lds\n"
    "s_add_u32 m0, s54, 56320\n"
    "s_nop 0\n"
    "buffer_load_dwordx4 v81, s[44:47], s55 offen lds\n"
    "s_add_u32 m0, s54, 57344\n"
    "s_nop 0\n"
    "buffer_load_dwordx4 v82, s[44:47], s55 offen lds\n"
    "s_add_u32 m0, s54, 58368\n"
    "s_nop 0\n"
    "buffer_load_dwordx4 v83, s[44:47], s55 offen lds\n"
    "s_add_u32 m0, s54, 59392\n"
    "s_nop 0\n"
    "buffer_load_dwordx4 v84, s[44:47], s55 offen lds\n"
    "s_add_u32 m0, s54, 60416\n"
    "s_nop 0\n"
    "buffer_load_dwordx4 v85, s[44:47], s55 offen lds\n"
    "s_add_u32 m0, s54, 61440\n"
    "s_nop 0\n"
    "buffer_load_dwordx4 v86, s[44:47], s55 offen lds\n"
    "s_add_u32 m0, s54, 62464\n"
    "s_nop 0\n"
    "buffer_load_dwordx4 v87, s[44:47], s55 offen lds\n"
    "s_add_u32 m0, s54, 63488\n"
    "s_nop 0\n"
    "buffer_load_dwordx4 v88, s[44:47], s55 offen lds\n"
    ".Lac8_%=:\n"
    "ds_read_b64_tr_b16 a[6:7], v149 offset:17408\n"
    "ds_read_b64_tr_b16 a[78:79], v149 offset:26624\n"
    "ds_read_b64_tr_b16 a[150:151], v149 offset:35840\n"
    "v_add_u32 v145, 0x10000, v107\n"
    "ds_read_b64_tr_b16 a[222:223], v145 offset:17408\n"
    "ds_read_b64_tr_b16 a[22:23], v148 offset:17408\n"
    "ds_read_b64_tr_b16 a[94:95], v148 offset:26624\n"
    "ds_read_b64_tr_b16 a[166:167], v148 offset:35840\n"
    "ds_read_b64_tr_b16 a[38:39], v147 offset:17408\n"
    "ds_read_b64_tr_b16 a[110:111], v147 offset:26624\n"
    "ds_read_b64_tr_b16 a[182:183], v147 offset:35840\n"
    "v_add_u32 v144, 0x10000, v111\n"
    "ds_read_b64_tr_b16 a[238:239], v144 offset:17408\n"
    "ds_read_b64_tr_b16 a[54:55], v146 offset:17408\n"
    "ds_read_b64_tr_b16 a[126:127], v146 offset:26624\n"
    "ds_read_b64_tr_b16 a[198:199], v146 offset:35840\n"
    "s_waitcnt vmcnt(27) lgkmcnt(0)\n"
    "s_barrier\n"
    "s_cmp_eq_u32 %2, 3\n"
    "s_cbranch_scc1 .Lac9_%=\n"
    "s_add_u32 s55, s51, 1769472\n"
    "s_add_u32 m0, s54, 82944\n"
    "s_nop 0\n"
    "buffer_load_dwordx4 v89, s[44:47], s55 offen lds\n"
    "s_add_u32 m0, s54, 83968\n"
    "s_nop 0\n"
    "buffer_load_dwordx4 v90, s[44:47], s55 offen lds\n"
    "s_add_u32 m0, s54, 84992\n"
    "s_nop 0\n"
    "buffer_load_dwordx4 v91, s[44:47], s55 offen lds\n"
    "s_add_u32 m0, s54, 86016\n"
    "s_nop 0\n"
    "buffer_load_dwordx4 v92, s[44:47], s55 offen lds\n"
    "s_add_u32 m0, s54, 87040\n"
    "s_nop 0\n"
    "buffer_load_dwordx4 v93, s[44:47], s55 offen lds\n"
    "s_add_u32 m0, s54, 88064\n"
    "s_nop 0\n"
    "buffer_load_dwordx4 v94, s[44:47], s55 offen lds\n"
    "s_add_u32 m0, s54, 89088\n"
    "s_nop 0\n"
    "buffer_load_dwordx4 v95, s[44:47], s55 offen lds\n"
    "s_add_u32 m0, s54, 90112\n"
    "s_nop 0\n"
    "buffer_load_dwordx4 v96, s[44:47], s55 offen lds\n"
    "s_add_u32 m0, s54, 91136\n"
    "s_nop 0\n"
    "buffer_load_dwordx4 v97, s[44:47], s55 offen lds\n"
    ".Lac9_%=:\n"
    "v_mov_b32 v11, 0x20400\n"
    "global_load_dwordx2 v[76:77], v11, %1 sc1\n"
    "v_mbcnt_lo_u32_b32 v72, -1, 0\n"
    "v_min_u32 v72, 7, v72\n"
    "v_lshlrev_b32 v72, 7, v72\n"
    "v_add_u32 v72, 0x20000, v72\n"
    "global_load_dwordx2 v[78:79], v72, %1 sc1\n"
    "v_mfma_f32_16x16x32_bf16 v[12:15], v[56:59], a[4:7], v[12:15]\n"
    "v_mfma_f32_16x16x32_bf16 v[16:19], v[56:59], a[76:79], v[16:19]\n"
    "v_mfma_f32_16x16x32_bf16 v[20:23], v[56:59], a[148:151], v[20:23]\n"
    "v_mfma_f32_16x16x32_bf16 v[24:27], v[56:59], a[220:223], v[24:27]\n"
    "v_mfma_f32_16x16x32_bf16 v[12:15], v[56:59], a[20:23], v[12:15]\n"
    "v_mfma_f32_16x16x32_bf16 v[16:19], v[56:59], a[92:95], v[16:19]\n"
    "v_mfma_f32_16x16x32_bf16 v[20:23], v[56:59], a[164:167], v[20:23]\n"
    "v_mfma_f32_16x16x32_bf16 v[12:15], v[56:59], a[36:39], v[12:15]\n"
    "v_mfma_f32_16x16x32_bf16 v[16:19], v[56:59], a[108:111], v[16:19]\n"
    "v_mfma_f32_16x16x32_bf16 v[20:23], v[56:59], a[180:183], v[20:23]\n"
    "v_mfma_f32_16x16x32_bf16 v[24:27], v[56:59], a[236:239], v[24:27]\n"
    "v_mfma_f32_16x16x32_bf16 v[12:15], v[56:59], a[52:55], v[12:15]\n"
    "v_mfma_f32_16x16x32_bf16 v[16:19], v[56:59], a[124:127], v[16:19]\n"
    "v_mfma_f32_16x16x32_bf16 v[20:23], v[56:59], a[196:199], v[20:23]\n"
    "ds_read_b64_tr_b16 a[8:9], v149 offset:45056\n"
    "ds_read_b64_tr_b16 a[80:81], v149 offset:54272\n"
    "ds_read_b64_tr_b16 a[152:153], v149 offset:63488\n"
    "ds_read_b64_tr_b16 a[224:225], v145 offset:45056\n"
    "ds_read_b64_tr_b16 a[24:25], v148 offset:45056\n"
    "ds_read_b64_tr_b16 a[96:97], v148 offset:54272\n"
    "ds_read_b64_tr_b16 a[168:169], v148 offset:63488\n"
    "ds_read_b64_tr_b16 a[40:41], v147 offset:45056\n"
    "ds_read_b64_tr_b16 a[112:113], v147 offset:54272\n"
    "ds_read_b64_tr_b16 a[184:185], v147 offset:63488\n"
    "ds_read_b64_tr_b16 a[240:241], v144 offset:45056\n"
    "ds_read_b64_tr_b16 a[56:57], v146 offset:45056\n"
    "ds_read_b64_tr_b16 a[128:129], v146 offset:54272\n"
    "ds_read_b64_tr_b16 a[200:201], v146 offset:63488\n"
    "s_waitcnt vmcnt(29) lgkmcnt(0)\n"
    "s_barrier\n"
    "ds_read_b64_tr_b16 a[10:11], v106\n"
    "ds_read_b64_tr_b16 a[82:83], v106 offset:9216\n"
    "ds_read_b64_tr_b16 a[154:155], v106 offset:18432\n"
    "ds_read_b64_tr_b16 a[226:227], v107\n"
    "ds_read_b64_tr_b16 a[26:27], v108\n"
    "ds_read_b64_tr_b16 a[98:99], v108 offset:9216\n"
    "ds_read_b64_tr_b16 a[170:171], v108 offset:18432\n"
    "ds_read_b64_tr_b16 a[42:43], v110\n"
    "ds_read_b64_tr_b16 a[114:115], v110 offset:9216\n"
    "ds_read_b64_tr_b16 a[186:187], v110 offset:18432\n"
    "ds_read_b64_tr_b16 a[242:243], v111\n"
    "ds_read_b64_tr_b16 a[58:59], v112\n"
    "ds_read_b64_tr_b16 a[130:131], v112 offset:9216\n"
    "ds_read_b64_tr_b16 a[202:203], v112 offset:18432\n"
    "s_waitcnt vmcnt(20) lgkmcnt(0)\n"
    "s_barrier\n"
    "v_mfma_f32_16x16x32_bf16 v[12:15], v[56:59], a[8:11], v[12:15]\n"
    "v_mfma_f32_16x16x32_bf16 v[16:19], v[56:59], a[80:83], v[16:19]\n"
    "v_mfma_f32_16x16x32_bf16 v[20:23], v[56:59], a[152:155], v[20:23]\n"
    "v_mfma_f32_16x16x32_bf16 v[24:27], v[56:59], a[224:227], v[24:27]\n"
    "v_mfma_f32_16x16x32_bf16 v[12:15], v[56:59], a[24:27], v[12:15]\n"
    "v_mfma_f32_16x16x32_bf16 v[16:19], v[56:59], a[96:99], v[16:19]\n"
    "v_mfma_f32_16x16x32_bf16 v[20:23], v[56:59], a[168:171], v[20:23]\n"
    "v_mfma_f32_16x16x32_bf16 v[12:15], v[56:59], a[40:43], v[12:15]\n"
    "v_mfma_f32_16x16x32_bf16 v[16:19], v[56:59], a[112:115], v[16:19]\n"
    "v_mfma_f32_16x16x32_bf16 v[20:23], v[56:59], a[184:187], v[20:23]\n"
    "v_mfma_f32_16x16x32_bf16 v[24:27], v[56:59], a[240:243], v[24:27]\n"
    "v_mfma_f32_16x16x32_bf16 v[12:15], v[56:59], a[56:59], v[12:15]\n"
    "v_mfma_f32_16x16x32_bf16 v[16:19], v[56:59], a[128:131], v[16:19]\n"
    "v_mfma_f32_16x16x32_bf16 v[20:23], v[56:59], a[200:203], v[20:23]\n"
    "ds_read_b64_tr_b16 a[12:13], v106 offset:27648\n"
    "ds_read_b64_tr_b16 a[84:85], v106 offset:36864\n"
    "ds_read_b64_tr_b16 a[156:157], v106 offset:46080\n"
    "ds_read_b64_tr_b16 a[228:229], v107 offset:27648\n"
    "ds_read_b64_tr_b16 a[28:29], v108 offset:27648\n"
    "ds_read_b64_tr_b16 a[100:101], v108 offset:36864\n"
    "ds_read_b64_tr_b16 a[172:173], v108 offset:46080\n"
    "ds_read_b64_tr_b16 a[44:45], v110 offset:27648\n"
    "ds_read_b64_tr_b16 a[116:117], v110 offset:36864\n"
    "ds_read_b64_tr_b16 a[188:189], v110 offset:46080\n"
    "ds_read_b64_tr_b16 a[244:245], v111 offset:27648\n"
    "ds_read_b64_tr_b16 a[60:61], v112 offset:27648\n"
    "ds_read_b64_tr_b16 a[132:133], v112 offset:36864\n"
    "ds_read_b64_tr_b16 a[204:205], v112 offset:46080\n"
    "s_waitcnt vmcnt(11) lgkmcnt(0)\n"
    "s_barrier\n"
    "ds_read_b64_tr_b16 a[14:15], v106 offset:55296\n"
    "ds_read_b64_tr_b16 a[86:87], v106 offset:64512\n"
    "ds_read_b64_tr_b16 a[158:159], v149 offset:8192\n"
    "ds_read_b64_tr_b16 a[230:231], v107 offset:55296\n"
    "ds_read_b64_tr_b16 a[30:31], v108 offset:55296\n"
    "ds_read_b64_tr_b16 a[102:103], v108 offset:64512\n"
    "ds_read_b64_tr_b16 a[174:175], v148 offset:8192\n"
    "ds_read_b64_tr_b16 a[46:47], v110 offset:55296\n"
    "ds_read_b64_tr_b16 a[118:119], v110 offset:64512\n"
    "ds_read_b64_tr_b16 a[190:191], v147 offset:8192\n"
    "ds_read_b64_tr_b16 a[246:247], v111 offset:55296\n"
    "ds_read_b64_tr_b16 a[62:63], v112 offset:55296\n"
    "ds_read_b64_tr_b16 a[134:135], v112 offset:64512\n"
    "ds_read_b64_tr_b16 a[206:207], v146 offset:8192\n"
    "s_waitcnt vmcnt(2) lgkmcnt(0)\n"
    "s_barrier\n"
    "v_mfma_f32_16x16x32_bf16 v[12:15], v[56:59], a[12:15], v[12:15]\n"
    "v_mfma_f32_16x16x32_bf16 v[16:19], v[56:59], a[84:87], v[16:19]\n"
    "v_mfma_f32_16x16x32_bf16 v[20:23], v[56:59], a[156:159], v[20:23]\n"
    "v_mfma_f32_16x16x32_bf16 v[24:27], v[56:59], a[228:231], v[24:27]\n"
    "v_mfma_f32_16x16x32_bf16 v[12:15], v[56:59], a[28:31], v[12:15]\n"
    "v_mfma_f32_16x16x32_bf16 v[16:19], v[56:59], a[100:103], v[16:19]\n"
    "v_mfma_f32_16x16x32_bf16 v[20:23], v[56:59], a[172:175], v[20:23]\n"
    "v_mfma_f32_16x16x32_bf16 v[12:15], v[56:59], a[44:47], v[12:15]\n"
    "v_mfma_f32_16x16x32_bf16 v[16:19], v[56:59], a[116:119], v[16:19]\n"
    "v_mfma_f32_16x16x32_bf16 v[20:23], v[56:59], a[188:191], v[20:23]\n"
    "v_mfma_f32_16x16x32_bf16 v[24:27], v[56:59], a[244:247], v[24:27]\n"
    "v_mfma_f32_16x16x32_bf16 v[12:15], v[56:59], a[60:63], v[12:15]\n"
    "v_mfma_f32_16x16x32_bf16 v[16:19], v[56:59], a[132:135], v[16:19]\n"
    "v_mfma_f32_16x16x32_bf16 v[20:23], v[56:59], a[204:207], v[20:23]\n"
    "v_add_u32 v143, 0x10000, v114\n"
    "ds_read_b64_tr_b16 a[64:65], v143 offset:17408\n"
    "ds_read_b64_tr_b16 a[136:137], v143 offset:26624\n"
    "ds_read_b64_tr_b16 a[208:209], v143 offset:35840\n"
    "v_add_u32 v142, 0x10000, v115\n"
    "ds_read_b64_tr_b16 a[248:249], v142 offset:17408\n"
    "ds_read_b64_tr_b16 a[66:67], v143 offset:19712\n"
    "ds_read_b64_tr_b16 a[138:139], v143 offset:28928\n"
    "ds_read_b64_tr_b16 a[210:211], v143 offset:38144\n"
    "ds_read_b64_tr_b16 a[250:251], v142 offset:19712\n"
    "v_add_u32 v141, 0x10000, v116\n"
    "ds_read_b64_tr_b16 a[68:69], v141 offset:17408\n"
    "ds_read_b64_tr_b16 a[140:141], v141 offset:26624\n"
    "ds_read_b64_tr_b16 a[212:213], v141 offset:35840\n"
    "ds_read_b64_tr_b16 a[70:71], v141 offset:19712\n"
    "ds_read_b64_tr_b16 a[142:143], v141 offset:28928\n"
    "ds_read_b64_tr_b16 a[214:215], v141 offset:38144\n"
    "s_waitcnt lgkmcnt(0)\n"
    "s_barrier\n"
    "v_mfma_f32_16x16x32_bf16 v[12:15], v[56:59], a[64:67], v[12:15]\n"
    "v_mfma_f32_16x16x32_bf16 v[16:19], v[56:59], a[136:139], v[16:19]\n"
    "v_mfma_f32_16x16x32_bf16 v[20:23], v[56:59], a[208:211], v[20:23]\n"
    "v_mfma_f32_16x16x32_bf16 v[24:27], v[56:59], a[248:251], v[24:27]\n"
    "v_mfma_f32_16x16x32_bf16 v[12:15], v[56:59], a[68:71], v[12:15]\n"
    "v_mfma_f32_16x16x32_bf16 v[16:19], v[56:59], a[140:143], v[16:19]\n"
    "v_mfma_f32_16x16x32_bf16 v[20:23], v[56:59], a[212:215], v[20:23]\n"
    "v_mov_b32 v28, 0\n"
    "v_mov_b32 v29, 0\n"
    "v_mov_b32 v30, 0\n"
    "v_mov_b32 v31, 0\n"
    "v_mov_b32 v199, 0\n"
    "v_mov_b32 v215, 0\n"
    "v_mov_b32 v218, 0\n"
    "v_mov_b32 v219, 0\n"
    "v_add_u32 v73, 0x14000, v3\n"
    "ds_write_b128 v73, v[28:31]\n"
    "ds_write_b128 v73, v[28:31] offset:4096\n"
    "ds_write_b128 v73, v[28:31] offset:8192\n"
    "ds_write_b128 v73, v[28:31] offset:16384\n"
    "ds_write_b128 v73, v[28:31] offset:20480\n"
    "ds_write_b128 v73, v[28:31] offset:24576\n"
    "v_mov_b32 v153, 0xf149f2ca\n"
    "v_add_u32 v103, 7, v152\n"
    "v_cmp_gt_u32 vcc, 8, v103\n"
    "s_nop 1\n"
    "v_cndmask_b32 v44, v153, v28, vcc\n"
    "v_cmp_le_i32 vcc, 9, v152\n"
    "s_nop 1\n"
    "v_cndmask_b32 v68, v153, v28, vcc\n"
    "v_add_u32 v103, 8, v152\n"
    "v_cmp_gt_u32 vcc, 8, v103\n"
    "s_nop 1\n"
    "v_cndmask_b32 v45, v153, v28, vcc\n"
    "v_cmp_le_i32 vcc, 8, v152\n"
    "s_nop 1\n"
    "v_cndmask_b32 v69, v153, v28, vcc\n"
    "v_add_u32 v103, 9, v152\n"
    "v_cmp_gt_u32 vcc, 8, v103\n"
    "s_nop 1\n"
    "v_cndmask_b32 v46, v153, v28, vcc\n"
    "v_cmp_le_i32 vcc, 7, v152\n"
    "s_nop 1\n"
    "v_cndmask_b32 v70, v153, v28, vcc\n"
    "v_add_u32 v103, 10, v152\n"
    "v_cmp_gt_u32 vcc, 8, v103\n"
    "s_nop 1\n"
    "v_cndmask_b32 v47, v153, v28, vcc\n"
    "v_cmp_le_i32 vcc, 6, v152\n"
    "s_nop 1\n"
    "v_cndmask_b32 v71, v153, v28, vcc\n"
    "s_mov_b32 s86, 0xcccccccc\n"
    "s_mov_b32 s87, 0xcccccccc\n"
    "v_cvt_pk_bf16_f32 v13, 0, v12\n"
    "v_sub_f32 v12, v12, v13\n"
    "v_cvt_pk_bf16_f32 v14, 0, v12\n"
    "v_sub_f32 v12, v12, v14\n"
    "v_cvt_pk_bf16_f32 v13, v13, v14\n"
    "v_cvt_pk_bf16_f32 v14, v12, 0\n"
    "s_mov_b64 vcc, 0xffff\n"
    "v_cndmask_b32 v60, 0, v13, vcc\n"
    "v_cndmask_b32 v61, 0, v14, vcc\n"
    "v_cvt_pk_bf16_f32 v17, 0, v16\n"
    "v_sub_f32 v16, v16, v17\n"
    "v_cvt_pk_bf16_f32 v18, 0, v16\n"
    "v_sub_f32 v16, v16, v18\n"
    "v_cvt_pk_bf16_f32 v17, v17, v18\n"
    "v_cvt_pk_bf16_f32 v18, v16, 0\n"
    "s_mov_b64 vcc, 0xffff\n"
    "v_cndmask_b32 v62, 0, v17, vcc\n"
    "v_cndmask_b32 v63, 0, v18, vcc\n"
    "v_cvt_pk_bf16_f32 v21, 0, v20\n"
    "v_sub_f32 v20, v20, v21\n"
    "v_cvt_pk_bf16_f32 v22, 0, v20\n"
    "v_sub_f32 v20, v20, v22\n"
    "v_cvt_pk_bf16_f32 v21, v21, v22\n"
    "v_cvt_pk_bf16_f32 v22, v20, 0\n"
    "s_mov_b64 vcc, 0xffff\n"
    "v_cndmask_b32 v64, 0, v21, vcc\n"
    "v_cndmask_b32 v65, 0, v22, vcc\n"
    "v_cvt_pk_bf16_f32 v25, 0, v24\n"
    "v_sub_f32 v24, v24, v25\n"
    "v_cvt_pk_bf16_f32 v26, 0, v24\n"
    "v_sub_f32 v24, v24, v26\n"
    "v_cvt_pk_bf16_f32 v25, v25, v26\n"
    "v_cvt_pk_bf16_f32 v26, v24, 0\n"
    "s_mov_b64 vcc, 0xffff\n"
    "v_cndmask_b32 v66, 0, v25, vcc\n"
    "v_cndmask_b32 v67, 0, v26, vcc\n"
    "s_mov_b64 s[44:45], %1\n"
    "s_and_b32 s45, s45, 0xffff\n"
    "s_mov_b32 s46, 0x0\n"
    "s_mov_b32 s47, 0x20000\n"
    "v_mov_b32 v158, 0\n"
    "s_waitcnt vmcnt(0)\n"
    "v_add_u32 v76, 1, v76\n"
    "v_add_u32 v77, 1, v77\n"
    "s_mov_b32 s52, 1048576\n"
    ".Lago_a%=:\n"
    "v_cmp_ne_u64 vcc, v[76:77], v[78:79]\n"
    "s_cbranch_vccz .Lago_x%=\n"
    "global_load_dwordx2 v[78:79], v72, %1 sc1\n"
    "s_sub_u32 s52, s52, 1\n"
    "s_waitcnt vmcnt(0)\n"
    "s_cbranch_scc0 .Lago_a%=\n"
    ".Lago_x%=:\n"
    "v_mbcnt_lo_u32_b32 v76, -1, 0\n"
    "v_mbcnt_hi_u32_b32 v76, -1, v76\n"
    "v_min_u32 v72, 5, v76\n"
    "v_lshlrev_b32 v72, 7, v72\n"
    "v_mov_b32 v11, 0x26c00\n"
    "ds_read_b128 v[76:79], v11\n"
    "s_waitcnt lgkmcnt(0)\n"
    "v_readfirstlane_b32 s49, v77\n"
    "v_add_u32 v76, v76, v72\n"
    "s_branch .Lachk_%=\n"
    ".Lago_%=:\n"
    "v_add_u32 v11, 32, v11\n"
    "ds_read_b128 v[76:79], v11\n"
    "ds_read2_b32 v[74:75], v11 offset0:5 offset1:6\n"
    ".Latop_%=:\n"
    "s_waitcnt lgkmcnt(5)\n"
    "v_mfma_f32_16x16x32_bf16 v[24:27], a[216:219], v[80:83], 0\n"
    "v_xor_b32 v50, 0x4000, v0\n"
    "v_xor_b32 v49, 0x2000, v0\n"
    "s_waitcnt lgkmcnt(4)\n"
    "v_mfma_f32_16x16x32_bf16 v[24:27], a[220:223], v[84:87], v[24:27]\n"
    "v_xor_b32 v51, 0x6000, v0\n"
    "v_and_b32 v52, 0xffffbfff, v0\n"
    "s_waitcnt lgkmcnt(3)\n"
    "v_mfma_f32_16x16x32_bf16 v[24:27], a[224:227], v[88:91], v[24:27]\n"
    "v_xor_b32 v53, 0x2000, v52\n"
    "s_waitcnt lgkmcnt(2)\n"
    "v_mfma_f32_16x16x32_bf16 v[24:27], a[228:231], v[92:95], v[24:27]\n"
    "s_waitcnt vmcnt(2)\n"
    "s_barrier\n"
    "ds_read_b128 v[112:115], v50\n"
    "v_mfma_f32_16x16x32_bf16 v[12:15], a[0:3], v[80:83], 0\n"
    "ds_read2st64_b32 v[160:161], v7 offset1:1\n"
    "s_waitcnt lgkmcnt(3)\n"
    "v_add_u32 v76, v76, v72\n"
    "v_mfma_f32_16x16x32_bf16 v[16:19], a[72:75], v[80:83], 0\n"
    "ds_read2st64_b32 v[162:163], v7 offset0:2 offset1:3\n"
    "v_readfirstlane_b32 s62, v78\n"
    "v_mfma_f32_16x16x32_bf16 v[12:15], a[4:7], v[84:87], v[12:15]\n"
    "ds_read2st64_b32 v[164:165], v7 offset0:4 offset1:5\n"
    "v_readfirstlane_b32 s49, v77\n"
    "v_mfma_f32_16x16x32_bf16 v[16:19], a[76:79], v[84:87], v[16:19]\n"
    "ds_read2st64_b32 v[166:167], v7 offset0:6 offset1:7\n"
    "s_xor_b32 s58, s58, 0x8000\n"
    "v_mfma_f32_16x16x32_bf16 v[12:15], a[8:11], v[88:91], v[12:15]\n"
    "ds_read_b32 v168, v7 offset:2048\n"
    "s_xor_b32 s59, s59, 0x8000\n"
    "v_mfma_f32_16x16x32_bf16 v[16:19], a[80:83], v[88:91], v[16:19]\n"
    "ds_read_b128 v[116:119], v50 offset:1024\n"
    "s_waitcnt lgkmcnt(7)\n"
    "v_add_u32 v73, v75, v3\n"
    "v_mfma_f32_16x16x32_bf16 v[12:15], a[12:15], v[92:95], v[12:15]\n"
    "ds_read_b128 v[120:123], v50 offset:2048\n"
    "v_mfma_f32_16x16x32_bf16 v[16:19], a[84:87], v[92:95], v[16:19]\n"
    "ds_read_b128 v[124:127], v50 offset:3072\n"
    "s_waitcnt lgkmcnt(8)\n"
    "v_mfma_f32_16x16x32_bf16 v[24:27], a[232:235], v[112:115], v[24:27]\n"
    "ds_read_b128 v[144:147], v52 offset:4096\n"
    "s_waitcnt lgkmcnt(8)\n"
    "v_add_f32 v160, v160, v161\n"
    "s_waitcnt lgkmcnt(7)\n"
    "v_add_f32 v162, v162, v163\n"
    "v_mfma_f32_16x16x32_bf16 v[12:15], a[32:35], v[112:115], v[12:15]\n"
    "ds_read_b128 v[96:99], v49\n"
    "s_waitcnt lgkmcnt(7)\n"
    "v_add_f32 v164, v164, v165\n"
    "s_waitcnt lgkmcnt(6)\n"
    "v_add_f32 v166, v166, v167\n"
    "v_mfma_f32_16x16x32_bf16 v[16:19], a[104:107], v[112:115], v[16:19]\n"
    "ds_read_b128 v[100:103], v49 offset:1024\n"
    "s_waitcnt vmcnt(1)\n"
    "v_cmp_eq_u32 vcc, 0, v158\n"
    "s_cmp_eq_u64 vcc, 0\n"
    "v_add_f32 v160, v160, v162\n"
    "s_waitcnt lgkmcnt(5)\n"
    "v_mfma_f32_16x16x32_bf16 v[24:27], a[236:239], v[116:119], v[24:27]\n"
    "s_cselect_b32 s94, s42, 0\n"
    "s_cselect_b32 s90, s96, 0\n"
    "s_cselect_b32 s46, 0, 0x20000\n"
    "v_add_f32 v164, v164, v166\n"
    "v_mfma_f32_16x16x32_bf16 v[12:15], a[36:39], v[116:119], v[12:15]\n"
    "buffer_load_dword v55, v76, s[44:47], 0 offen sc1\n"
    "v_add_f32 v160, v160, v168\n"
    "v_add_f32 v168, v160, v164\n"
    "v_mfma_f32_16x16x32_bf16 v[16:19], a[108:111], v[116:119], v[16:19]\n"
    "ds_read_b128 v[104:107], v49 offset:2048\n"
    "s_mov_b32 m0, s58\n"
    "v_add_f32 v160, v160, v164\n"
    "s_waitcnt lgkmcnt(5)\n"
    "v_mfma_f32_16x16x32_bf16 v[24:27], a[240:243], v[120:123], v[24:27]\n"
    "buffer_load_dwordx4 v2, s[92:95], s49 offen sc1 lds\n"
    "v_permlane32_swap_b32 v160, v168\n"
    "v_mfma_f32_16x16x32_bf16 v[12:15], a[40:43], v[120:123], v[12:15]\n"
    "ds_read_b32 v158, v11 offset:32\n"
    "v_add_f32 v161, v160, v168\n"
    "v_add_f32 v168, v168, v160\n"
    "v_mfma_f32_16x16x32_bf16 v[16:19], a[112:115], v[120:123], v[16:19]\n"
    "ds_read_b128 v[108:111], v49 offset:3072\n"
    "v_permlane16_swap_b32 v161, v168\n"
    "s_waitcnt lgkmcnt(6)\n"
    "v_mfma_f32_16x16x32_bf16 v[24:27], a[244:247], v[124:127], v[24:27]\n"
    "ds_read_b128 v[128:131], v51\n"
    "v_mul_f32 v160, 0xbae38e39, v161\n"
    "v_mul_f32 v168, 0x3ae38e39, v168\n"
    "v_mfma_f32_16x16x32_bf16 v[12:15], a[44:47], v[124:127], v[12:15]\n"
    "ds_read_b128 v[132:135], v51 offset:1024\n"
    "v_mov_b32 v54, 1\n"
    "v_cvt_pk_bf16_f32 v216, v160, v160\n"
    "v_mfma_f32_16x16x32_bf16 v[16:19], a[116:119], v[124:127], v[16:19]\n"
    "buffer_load_dwordx4 v2, s[92:95], s49 offen offset:1024 sc1 lds\n"
    "v_cvt_pk_bf16_f32 v217, v160, v160\n"
    "v_fma_f32 v168, -v160, v160, v168\n"
    "s_waitcnt lgkmcnt(7)\n"
    "v_mfma_f32_16x16x32_bf16 v[24:27], a[248:251], v[144:147], v[24:27]\n"
    "s_waitcnt vmcnt(3)\n"
    "s_mov_b64 exec, s[56:57]\n"
    "ds_add_rtn_u32 v54, v11, v54 offset:16\n"
    "s_mov_b64 exec, -1\n"
    "v_max_f32 v168, 0, v168\n"
    "v_mfma_f32_16x16x32_bf16 v[12:15], a[64:67], v[144:147], v[12:15]\n"
    "ds_read_b128 v[136:139], v51 offset:2048\n"
    "s_waitcnt lgkmcnt(4)\n"
    "v_add_u32 v158, v158, v72\n"
    "v_mfma_f32_16x16x32_bf16 v[12:15], v[60:63], v[216:219], v[12:15]\n"
    "v_mfma_f32_16x16x32_bf16 v[16:19], a[136:139], v[144:147], v[16:19]\n"
    "ds_read_b128 v[140:143], v51 offset:3072\n"
    "v_mfma_f32_16x16x32_bf16 v[16:19], v[62:65], v[216:219], v[16:19]\n"
    "v_mfma_f32_16x16x32_bf16 v[24:27], v[66:69], v[216:219], v[24:27]\n"
    "v_mfma_f32_16x16x32_bf16 v[12:15], a[16:19], v[96:99], v[12:15]\n"
    "ds_read_b128 v[148:151], v53 offset:4096\n"
    "v_cvt_pk_bf16_f32 v168, 0, v168\n"
    "v_add_f32 v168, 0x3727c5ac, v168\n"
    "v_mfma_f32_16x16x32_bf16 v[16:19], a[88:91], v[96:99], v[16:19]\n"
    "buffer_load_dwordx4 v2, s[92:95], s49 offen offset:2048 sc1 lds\n"
    "v_cvt_pk_bf16_f32 v168, 0, v168\n"
    "v_sqrt_f32 v168, v168\n"
    "v_mfma_f32_16x16x32_bf16 v[12:15], a[20:23], v[100:103], v[12:15]\n"
    "ds_read_b128 v[28:31], v73\n"
    "s_waitcnt lgkmcnt(4)\n"
    "v_readfirstlane_b32 s50, v54\n"
    "v_mfma_f32_16x16x32_bf16 v[16:19], a[92:95], v[100:103], v[16:19]\n"
    "s_cmp_eq_u32 s50, 3\n"
    "s_cselect_b32 s46, 0x20000, 0\n"
    "v_cvt_pk_bf16_f32 v168, 0, v168\n"
    "v_mfma_f32_16x16x32_bf16 v[12:15], a[24:27], v[104:107], v[12:15]\n"
    "buffer_store_dword v54, v79, s[44:47], 0 offen sc1\n"
    "v_rcp_f32 v168, v168\n"
    "v_mfma_f32_16x16x32_bf16 v[16:19], a[96:99], v[104:107], v[16:19]\n"
    "ds_read_b64 v[32:33], v73 offset:4096\n"
    "v_mul_f32 v24, v24, v168\n"
    "v_mul_f32 v25, v25, v168\n"
    "v_mfma_f32_16x16x32_bf16 v[12:15], a[28:31], v[108:111], v[12:15]\n"
    "buffer_load_dwordx4 v2, s[92:95], s49 offen offset:3072 sc1 lds\n"
    "v_mul_f32 v26, v26, v168\n"
    "v_mul_f32 v27, v27, v168\n"
    "v_mfma_f32_16x16x32_bf16 v[16:19], a[100:103], v[108:111], v[16:19]\n"
    "ds_read_b64 v[36:37], v73 offset:8192\n"
    "v_mfma_f32_16x16x32_bf16 v[12:15], a[48:51], v[128:131], v[12:15]\n"
    "ds_write_b128 v9, v[24:27]\n"
    "v_mfma_f32_16x16x32_bf16 v[16:19], a[120:123], v[128:131], v[16:19]\n"
    "ds_write_b32 v7, v168 offset:2304\n"
    "v_mfma_f32_16x16x32_bf16 v[12:15], a[52:55], v[132:135], v[12:15]\n"
    "ds_read_b128 v[200:203], v8\n"
    "s_bitset1_b32 m0, 12\n"
    "v_mfma_f32_16x16x32_bf16 v[16:19], a[124:127], v[132:135], v[16:19]\n"
    "buffer_load_dwordx4 v3, s[88:91], s49 offen sc1 lds\n"
    "v_min_f32 v40, v74, v68\n"
    "s_waitcnt lgkmcnt(8)\n"
    "v_mfma_f32_16x16x32_bf16 v[12:15], a[56:59], v[136:139], v[12:15]\n"
    "v_min_f32 v41, v74, v69\n"
    "v_min_f32 v42, v74, v70\n"
    "v_mfma_f32_16x16x32_bf16 v[16:19], a[128:131], v[136:139], v[16:19]\n"
    "s_waitcnt lgkmcnt(0)\n"
    "s_barrier\n"
    "ds_read2st64_b64 v[180:183], v4 offset1:2\n"
    "v_min_f32 v43, v74, v71\n"
    "v_mfma_f32_16x16x32_bf16 v[12:15], a[60:63], v[140:143], v[12:15]\n"
    "ds_read2st64_b64 v[184:187], v5 offset1:2\n"
    "v_xor_b32 v0, 0x8000, v0\n"
    "v_mfma_f32_16x16x32_bf16 v[16:19], a[132:135], v[140:143], v[16:19]\n"
    "ds_read2_b32 v[188:189], v6 offset1:4\n"
    "s_mov_b32 m0, s59\n"
    "s_mov_b32 s55, s49\n"
    "s_bitset1_b32 s55, 15\n"
    "v_mfma_f32_16x16x32_bf16 v[12:15], a[68:71], v[148:151], v[12:15]\n"
    "s_mov_b64 exec, s[60:61]\n"
    "buffer_load_dwordx4 v3, s[92:95], s55 offen offset:2048 sc1 lds\n"
    "s_mov_b64 exec, -1\n"
    "v_xor_b32 v7, 0x8000, v7\n"
    "v_mfma_f32_16x16x32_bf16 v[16:19], a[140:143], v[148:151], v[16:19]\n"
    "ds_read2_b32 v[190:191], v6 offset0:8 offset1:12\n"
    "v_xor_b32 v8, 0x8000, v8\n"
    "v_mfma_f32_16x16x32_bf16 v[20:23], v[216:219], v[64:67], 0\n"
    "ds_read2_b32 v[192:193], v10 offset1:4\n"
    "v_mfma_f32_16x16x32_bf16 v[20:23], v[80:83], a[144:147], v[20:23]\n"
    "ds_read2_b32 v[194:195], v10 offset0:8 offset1:12\n"
    "v_mfma_f32_16x16x32_bf16 v[20:23], v[84:87], a[148:151], v[20:23]\n"
    "v_mfma_f32_16x16x32_bf16 v[20:23], v[88:91], a[152:155], v[20:23]\n"
    "s_cmp_eq_u32 s94, 0\n"
    "s_cbranch_scc1 .Lak10_%=\n"
    ".Lak10_x%=:\n"
    "v_mfma_f32_16x16x32_bf16 v[20:23], v[92:95], a[156:159], v[20:23]\n"
    "s_waitcnt lgkmcnt(5)\n"
    "v_add_f32 v180, v180, v182\n"
    "v_add_f32 v181, v181, v183\n"
    "v_mfma_f32_16x16x32_bf16 v[20:23], v[96:99], a[160:163], v[20:23]\n"
    "v_cvt_pk_bf16_f32 v198, v180, v181\n"
    "s_waitcnt lgkmcnt(4)\n"
    "v_add_f32 v184, v184, v186\n"
    "v_mfma_f32_16x16x32_bf16 v[20:23], v[100:103], a[164:167], v[20:23]\n"
    "v_add_f32 v185, v185, v187\n"
    "v_cvt_pk_bf16_f32 v214, v184, v185\n"
    "v_mfma_f32_16x16x32_bf16 v[20:23], v[104:107], a[168:171], v[20:23]\n"
    "v_mul_f32 v12, v12, v168\n"
    "v_mul_f32 v13, v13, v168\n"
    "v_mul_f32 v14, v14, v168\n"
    "v_mul_f32 v15, v15, v168\n"
    "v_mfma_f32_16x16x32_bf16 v[20:23], v[108:111], a[172:175], v[20:23]\n"
    "v_cvt_pk_bf16_f32 v196, v12, v13\n"
    "v_cvt_pk_bf16_f32 v197, v14, v15\n"
    "v_mul_f32 v16, v16, v168\n"
    "v_mul_f32 v17, v17, v168\n"
    "v_mfma_f32_16x16x32_bf16 v[20:23], v[112:115], a[176:179], v[20:23]\n"
    "v_mfma_f32_16x16x32_bf16 v[204:207], v[28:31], v[196:199], v[40:43]\n"
    "v_mul_f32 v18, v18, v168\n"
    "v_mul_f32 v19, v19, v168\n"
    "v_cvt_pk_bf16_f32 v212, v16, v17\n"
    "v_mfma_f32_16x16x32_bf16 v[20:23], v[116:119], a[180:183], v[20:23]\n"
    "v_cvt_pk_bf16_f32 v213, v18, v19\n"
    "s_waitcnt lgkmcnt(1)\n"
    "v_add_f32 v188, v188, v192\n"
    "v_add_f32 v189, v189, v193\n"
    "v_mfma_f32_16x16x32_bf16 v[208:211], v[212:215], v[196:199], v[44:47]\n"
    "v_mfma_f32_16x16x32_bf16 v[20:23], v[120:123], a[184:187], v[20:23]\n"
    "ds_write_b128 v73, v[212:215]\n"
    "s_waitcnt lgkmcnt(1)\n"
    "v_add_f32 v190, v190, v194\n"
    "v_add_f32 v191, v191, v195\n"
    "v_maximum3_f32 v40, v204, v205, v206\n"
    "v_mfma_f32_16x16x32_bf16 v[20:23], v[124:127], a[188:191], v[20:23]\n"
    "v_cvt_pk_bf16_f32 v38, v188, v189\n"
    "v_cvt_pk_bf16_f32 v39, v190, v191\n"
    "v_cndmask_b32 v38, v38, v56, s[86:87]\n"
    "v_cndmask_b32 v39, v39, v56, s[86:87]\n"
    "v_mfma_f32_16x16x32_bf16 v[20:23], v[128:131], a[192:195], v[20:23]\n"
    "v_maximum3_f32 v188, v40, v207, v208\n"
    "v_maximum3_f32 v188, v188, v209, v210\n"
    "v_maximum3_f32 v188, v188, v211, v211\n"
    "v_mov_b32 v189, v188\n"
    "v_mfma_f32_16x16x32_bf16 v[20:23], v[132:135], a[196:199], v[20:23]\n"
    "s_nop 0\n"
    "v_permlane16_swap_b32 v189, v188\n"
    "v_maximum3_f32 v188, v188, v189, v189\n"
    "v_mov_b32 v189, v188\n"
    "v_mfma_f32_16x16x32_bf16 v[20:23], v[136:139], a[200:203], v[20:23]\n"
    "s_nop 0\n"
    "v_permlane32_swap_b32 v189, v188\n"
    "v_maximum3_f32 v188, v188, v189, v189\n"
    "v_mul_f32 v188, 0xbeae1a93, v188\n"
    "v_fmamk_f32 v204, v204, 0x3eae1a93, v188\n"
    "v_mfma_f32_16x16x32_bf16 v[20:23], v[140:143], a[204:207], v[20:23]\n"
    "v_fmamk_f32 v205, v205, 0x3eae1a93, v188\n"
    "v_fmamk_f32 v206, v206, 0x3eae1a93, v188\n"
    "v_fmamk_f32 v207, v207, 0x3eae1a93, v188\n"
    "v_fmamk_f32 v208, v208, 0x3eae1a93, v188\n"
    "v_mfma_f32_16x16x32_bf16 v[20:23], v[144:147], a[208:211], v[20:23]\n"
    "global_load_dword v158, v158, %1 sc1\n"
    "v_fmamk_f32 v209, v209, 0x3eae1a93, v188\n"
    "v_fmamk_f32 v210, v210, 0x3eae1a93, v188\n"
    "v_fmamk_f32 v211, v211, 0x3eae1a93, v188\n"
    "v_mfma_f32_16x16x32_bf16 v[20:23], v[148:151], a[212:215], v[20:23]\n"
    "v_exp_f32 v204, v204\n"
    "v_exp_f32 v205, v205\n"
    "v_exp_f32 v206, v206\n"
    "v_exp_f32 v207, v207\n"
    "v_exp_f32 v208, v208\n"
    "v_exp_f32 v209, v209\n"
    "v_exp_f32 v210, v210\n"
    "v_exp_f32 v211, v211\n"
    "v_cvt_pk_bf16_f32 v204, v204, v205\n"
    "v_cvt_pk_bf16_f32 v205, v206, v207\n"
    "v_cvt_pk_bf16_f32 v206, v208, v209\n"
    "v_cvt_pk_bf16_f32 v207, v210, v211\n"
    "s_nop 1\n"
    "v_mfma_f32_16x16x32_bf16 v[184:187], v[36:39], v[204:207], 0\n"
    "v_mul_f32 v20, v20, v200\n"
    "v_mul_f32 v21, v21, v201\n"
    "v_mul_f32 v22, v22, v202\n"
    "v_mul_f32 v23, v23, v203\n"
    "v_cvt_pk_bf16_f32 v34, v20, v21\n"
    "v_cvt_pk_bf16_f32 v35, v22, v23\n"
    "s_nop 1\n"
    "v_mfma_f32_16x16x32_bf16 v[180:183], v[32:35], v[204:207], 0\n"
    "ds_write2st64_b64 v73, v[34:35], v[38:39] offset0:8 offset1:16\n"
    "v_rcp_f32 v188, v186\n"
    "s_waitcnt vmcnt(3)\n"
    "ds_read_b128 v[80:83], v0\n"
    "ds_read_b128 v[84:87], v0 offset:1024\n"
    "ds_read_b128 v[88:91], v0 offset:2048\n"
    "ds_read_b128 v[92:95], v0 offset:3072\n"
    "v_mul_f32 v184, v184, v188\n"
    "v_mul_f32 v185, v185, v188\n"
    "v_cvt_pk_bf16_f32 v184, v184, v185\n"
    "v_pk_mul_f32 v[180:181], v[180:181], v[188:189] op_sel_hi:[1,0]\n"
    "v_pk_mul_f32 v[182:183], v[182:183], v[188:189] op_sel_hi:[1,0]\n"
    "v_cvt_pk_bf16_f32 v204, v180, v181\n"
    "v_cvt_pk_bf16_f32 v205, v182, v183\n"
    "v_cvt_pk_bf16_f32 v206, v180, v181\n"
    "v_cvt_pk_bf16_f32 v207, v182, v183\n"
    "s_nop 0\n"
    "v_permlane16_swap_b32 v204, v206\n"
    "v_permlane16_swap_b32 v205, v207\n"
    "s_nop 0\n"
    "v_mov_b32_dpp v204, v184 quad_perm:[0,0,0,0] row_mask:0x2 bank_mask:0xf\n"
    "v_mov_b32_dpp v205, v184 quad_perm:[1,1,1,1] row_mask:0x2 bank_mask:0xf\n"
    "v_mov_b32_dpp v206, v184 quad_perm:[2,2,2,2] row_mask:0x2 bank_mask:0xf\n"
    "v_mov_b32_dpp v207, v184 quad_perm:[3,3,3,3] row_mask:0x2 bank_mask:0xf\n"
    "buffer_store_dwordx4 v[204:207], v1, s[40:43], s62 offen sc1\n"
    "s_mov_b32 s56, 1\n"
    "s_cmp_lg_u32 s94, 0\n"
    "s_cbranch_scc1 .Lago_%=\n"
    "ds_read_b32 v79, v11 offset:44\n"
    "s_waitcnt vmcnt(7) lgkmcnt(0)\n"
    "v_cmp_eq_u32 vcc, 0, v55\n"
    "s_cmp_eq_u64 vcc, 0\n"
    "s_cbranch_scc1 .Lald_%=\n"
    "global_load_dword v55, v76, %1 sc1\n"
    "s_waitcnt vmcnt(0)\n"
    "v_mov_b32 v54, 1\n"
    "s_mov_b64 exec, s[56:57]\n"
    "ds_add_rtn_u32 v54, v11, v54 offset:48\n"
    "s_mov_b64 exec, -1\n"
    "s_waitcnt lgkmcnt(0)\n"
    "v_readfirstlane_b32 s50, v54\n"
    "s_cmp_eq_u32 s50, 3\n"
    "s_cselect_b32 s46, 0x20000, 0\n"
    "buffer_store_dword v54, v79, s[44:47], 0 offen sc1\n"
    "s_mov_b32 s56, 0\n"
    "v_cmp_eq_u32 vcc, 0, v55\n"
    "s_cmp_eq_u64 vcc, 0\n"
    "s_cbranch_scc1 .Lald_%=\n"
    ".Lachk_%=:\n"
    "v_readfirstlane_b32 s50, v76\n"
    "s_cmp_lt_u32 s50, 200\n"
    "s_cbranch_scc1 .Laend_%=\n"
    ".Lawait_%=:\n"
    "global_load_dword v55, v76, %1 sc1\n"
    "s_mov_b32 s52, 0\n"
    ".Lap11_%=:\n"
    "s_waitcnt vmcnt(0)\n"
    "v_cmp_eq_u32 vcc, 0, v55\n"
    "s_cmp_eq_u64 vcc, 0\n"
    "s_cbranch_scc1 .Lap11_x%=\n"
    "s_add_u32 s52, s52, 1\n"
    "s_sleep 2\n"
    "global_load_dword v55, v76, %1 sc1\n"
    "s_cmp_lt_u32 s52, s53\n"
    "s_cbranch_scc1 .Lap11_%=\n"
    "s_mov_b32 s53, 0\n"
    "v_mov_b32 v78, 0\n"
    "v_mov_b32 v79, 1\n"
    "global_store_dword v78, v79, %1 sc1\n"
    "s_waitcnt vmcnt(0)\n"
    ".Lap11_x%=:\n"
    ".Lald_%=:\n"
    "s_mov_b32 s94, s42\n"
    "s_mov_b32 s90, s96\n"
    "s_mov_b32 m0, s58\n"
    "s_nop 0\n"
    "buffer_load_dwordx4 v2, s[92:95], s49 offen sc1 lds\n"
    "buffer_load_dwordx4 v2, s[92:95], s49 offen offset:1024 sc1 lds\n"
    "buffer_load_dwordx4 v2, s[92:95], s49 offen offset:2048 sc1 lds\n"
    "buffer_load_dwordx4 v2, s[92:95], s49 offen offset:3072 sc1 lds\n"
    "s_bitset1_b32 m0, 12\n"
    "s_nop 0\n"
    "buffer_load_dwordx4 v3, s[88:91], s49 offen sc1 lds\n"
    "s_mov_b32 m0, s59\n"
    "s_mov_b32 s55, s49\n"
    "s_bitset1_b32 s55, 15\n"
    "s_mov_b64 exec, s[60:61]\n"
    "buffer_load_dwordx4 v3, s[92:95], s55 offen offset:2048 sc1 lds\n"
    "s_mov_b64 exec, -1\n"
    "s_waitcnt vmcnt(6)\n"
    "v_mov_b32 v54, 1\n"
    "s_mov_b64 exec, s[56:57]\n"
    "ds_add_rtn_u32 v54, v11, v54 offset:48\n"
    "s_mov_b64 exec, -1\n"
    "s_waitcnt lgkmcnt(0)\n"
    "v_readfirstlane_b32 s50, v54\n"
    "s_cmp_eq_u32 s50, 3\n"
    "s_cselect_b32 s46, 0x20000, 0\n"
    "buffer_store_dword v54, v79, s[44:47], 0 offen sc1\n"
    "s_mov_b32 s56, 0\n"
    "s_waitcnt vmcnt(3)\n"
    "ds_read_b128 v[80:83], v0\n"
    "ds_read_b128 v[84:87], v0 offset:1024\n"
    "ds_read_b128 v[88:91], v0 offset:2048\n"
    "ds_read_b128 v[92:95], v0 offset:3072\n"
    "s_waitcnt vmcnt(1)\n"
    "v_add_u32 v11, 32, v11\n"
    "ds_read_b128 v[76:79], v11\n"
    "ds_read2_b32 v[74:75], v11 offset0:5 offset1:6\n"
    "s_waitcnt lgkmcnt(0)\n"
    "s_branch .Latop_%=\n"
    ".Lak10_%=:\n"
    "s_waitcnt vmcnt(6)\n"
    "v_cmp_eq_u32 vcc, 0, v55\n"
    "s_cmp_eq_u64 vcc, 0\n"
    "s_cselect_b32 s94, s42, 0\n"
    "s_cselect_b32 s90, s96, 0\n"
    "s_cselect_b32 s46, 0, 0x20000\n"
    "buffer_load_dword v55, v76, s[44:47], 0 offen sc1\n"
    "s_mov_b32 m0, s58\n"
    "s_nop 0\n"
    "buffer_load_dwordx4 v2, s[92:95], s49 offen sc1 lds\n"
    "buffer_load_dwordx4 v2, s[92:95], s49 offen offset:1024 sc1 lds\n"
    "buffer_load_dwordx4 v2, s[92:95], s49 offen offset:2048 sc1 lds\n"
    "buffer_load_dwordx4 v2, s[92:95], s49 offen offset:3072 sc1 lds\n"
    "s_bitset1_b32 m0, 12\n"
    "s_nop 0\n"
    "buffer_load_dwordx4 v3, s[88:91], s49 offen sc1 lds\n"
    "s_mov_b32 m0, s59\n"
    "s_mov_b32 s55, s49\n"
    "s_bitset1_b32 s55, 15\n"
    "s_mov_b64 exec, s[60:61]\n"
    "buffer_load_dwordx4 v3, s[92:95], s55 offen offset:2048 sc1 lds\n"
    "s_mov_b64 exec, -1\n"
    "s_branch .Lak10_x%=\n"
    ".Lapol_%=:\n"
    "v_mov_b32 v76, 64\n"
    "global_load_dword v55, v76, %1 sc1\n"
    "s_mov_b32 s52, 0\n"
    ".Lap12_%=:\n"
    "s_waitcnt vmcnt(0)\n"
    "v_cmp_eq_u32 vcc, 0, v55\n"
    "s_cmp_eq_u64 vcc, 0\n"
    "s_cbranch_scc1 .Lap12_x%=\n"
    "s_add_u32 s52, s52, 1\n"
    "s_sleep 2\n"
    "global_load_dword v55, v76, %1 sc1\n"
    "s_cmp_lt_u32 s52, s53\n"
    "s_cbranch_scc1 .Lap12_%=\n"
    "s_mov_b32 s53, 0\n"
    "v_mov_b32 v78, 0\n"
    "v_mov_b32 v79, 1\n"
    "global_store_dword v78, v79, %1 sc1\n"
    "s_waitcnt vmcnt(0)\n"
    ".Lap12_x%=:\n"
    "v_readfirstlane_b32 s50, v55\n"
    "v_mbcnt_lo_u32_b32 v76, -1, 0\n"
    "v_mbcnt_hi_u32_b32 v76, -1, v76\n"
    "s_lshl_b32 s51, %2, 6\n"
    "v_add_u32 v76, s51, v76\n"
    "v_min_u32 v76, 129, v76\n"
    "v_lshlrev_b32 v76, 5, v76\n"
    "v_add_u32 v76, 0x26c00, v76\n"
    "ds_read2_b32 v[78:79], v76 offset0:1 offset1:2\n"
    "s_and_b32 s50, s50, 1\n"
    "s_waitcnt lgkmcnt(0)\n"
    "v_and_b32 v77, s50, v78\n"
    "v_lshlrev_b32 v77, 13, v77\n"
    "v_and_b32 v78, -2, v78\n"
    "v_xor_b32 v78, v77, v78\n"
    "v_and_b32 v77, s50, v79\n"
    "v_lshlrev_b32 v77, 13, v77\n"
    "v_and_b32 v79, -2, v79\n"
    "v_xor_b32 v79, v77, v79\n"
    "ds_write2_b32 v76, v78, v79 offset0:1 offset1:2\n"
    "s_waitcnt lgkmcnt(0)\n"
    "s_barrier\n"
    "ds_read_b32 v77, v11 offset:4\n"
    "v_add_u32 v76, %4, v72\n"
    "s_waitcnt lgkmcnt(0)\n"
    "v_readfirstlane_b32 s49, v77\n"
    "s_branch .Lawait_%=\n"
    ".Laend_%=:\n"
    "s_cmp_eq_u32 s50, 192\n"
    "s_cbranch_scc0 .Lapol_%=\n"
    "s_waitcnt vmcnt(0) lgkmcnt(0)\n"
    "s_endpgm\n"
    ".p2align 6\n"
    ".Lrole4_%=:\n"
    "s_mov_b64 s[40:41], %0\n"
    "s_and_b32 s41, s41, 0xffff\n"
    "s_mov_b32 s42, 0xc000000\n"
    "s_mov_b32 s43, 0x20000\n"
    "s_mov_b64 s[92:93], %0\n"
    "s_and_b32 s93, s93, 0xffff\n"
    "s_mov_b32 s94, 0xc000000\n"
    "s_mov_b32 s95, 0x20000\n"
    "s_mov_b32 s53, 131072\n"
    "s_mov_b64 s[56:57], 0\n"
    "v_mbcnt_lo_u32_b32 v20, -1, 0\n"
    "v_mbcnt_hi_u32_b32 v20, -1, v20\n"
    "s_lshl_b32 s50, %2, 8\n"
    "s_add_u32 s50, s50, 147456\n"
    "v_lshl_add_u32 v20, v20, 2, s50\n"
    "ds_read_b32 v0, v20\n"
    "ds_read_b32 v1, v20 offset:1024\n"
    "ds_read_b32 v2, v20 offset:2048\n"
    "ds_read_b32 v3, v20 offset:3072\n"
    "ds_read_b32 v4, v20 offset:4096\n"
    "ds_read_b32 v5, v20 offset:5120\n"
    "ds_read_b32 v8, v20 offset:6144\n"
    "ds_read_b32 v9, v20 offset:7168\n"
    "ds_read_b32 v10, v20 offset:8192\n"
    "s_waitcnt lgkmcnt(0)\n"
    "s_barrier\n"
    "s_lshl_b32 s54, %2, 10\n"
    "s_lshl_b32 s58, %2, 13\n"
    "s_add_u32 s58, s58, 0\n"
    "s_add_u32 s88, s40, 32768\n"
    "s_mov_b32 s89, s41\n"
    "s_cmp_lt_u32 %2, 2\n"
    "s_cselect_b32 s96, s42, 0\n"
    "s_mov_b32 s91, s43\n"
    "s_mul_i32 s59, %2, 576\n"
    "s_add_u32 s59, s59, 3072\n"
    "s_bfm_b64 s[60:61], 36, 0\n"
    "s_mov_b32 s86, 0x3f803f80\n"
    "s_mov_b64 s[44:45], %8\n"
    "s_and_b32 s45, s45, 0xffff\n"
    "s_mov_b32 s46, 0xa2000\n"
    "s_mov_b32 s47, 0x20000\n"
    "s_add_u32 m0, s54, 0\n"
    "s_add_u32 s55, %6, 0\n"
    "buffer_load_dwordx4 v8, s[44:47], s55 offen lds\n"
    "s_add_u32 m0, s54, 36864\n"
    "s_add_u32 s55, %6, 64\n"
    "buffer_load_dwordx4 v8, s[44:47], s55 offen lds\n"
    "s_add_u32 m0, s54, 73728\n"
    "s_add_u32 s55, %6, 128\n"
    "buffer_load_dwordx4 v8, s[44:47], s55 offen lds\n"
    "s_add_u32 m0, s54, 4096\n"
    "s_add_u32 s55, %6, 73728\n"
    "buffer_load_dwordx4 v8, s[44:47], s55 offen lds\n"
    "s_add_u32 m0, s54, 40960\n"
    "s_add_u32 s55, %6, 73792\n"
    "buffer_load_dwordx4 v8, s[44:47], s55 offen lds\n"
    "s_add_u32 m0, s54, 77824\n"
    "s_add_u32 s55, %6, 73856\n"
    "buffer_load_dwordx4 v8, s[44:47], s55 offen lds\n"
    "s_add_u32 m0, s54, 8192\n"
    "s_add_u32 s55, %6, 147456\n"
    "buffer_load_dwordx4 v8, s[44:47], s55 offen lds\n"
    "s_add_u32 m0, s54, 45056\n"
    "s_add_u32 s55, %6, 147520\n"
    "buffer_load_dwordx4 v8, s[44:47], s55 offen lds\n"
    "s_add_u32 m0, s54, 81920\n"
    "s_add_u32 s55, %6, 147584\n"
    "buffer_load_dwordx4 v8, s[44:47], s55 offen lds\n"
    "s_add_u32 m0, s54, 12288\n"
    "s_add_u32 s55, %6, 221184\n"
    "buffer_load_dwordx4 v8, s[44:47], s55 offen lds\n"
    "s_add_u32 m0, s54, 49152\n"
    "s_add_u32 s55, %6, 221248\n"
    "buffer_load_dwordx4 v8, s[44:47], s55 offen lds\n"
    "s_add_u32 m0, s54, 86016\n"
    "s_add_u32 s55, %6, 221312\n"
    "buffer_load_dwordx4 v8, s[44:47], s55 offen lds\n"
    "s_add_u32 m0, s54, 16384\n"
    "s_add_u32 s55, %6, 294912\n"
    "buffer_load_dwordx4 v8, s[44:47], s55 offen lds\n"
    "s_add_u32 m0, s54, 53248\n"
    "s_add_u32 s55, %6, 294976\n"
    "buffer_load_dwordx4 v8, s[44:47], s55 offen lds\n"
    "s_add_u32 m0, s54, 90112\n"
    "s_add_u32 s55, %6, 295040\n"
    "buffer_load_dwordx4 v8, s[44:47], s55 offen lds\n"
    "s_add_u32 m0, s54, 20480\n"
    "s_add_u32 s55, %6, 368640\n"
    "buffer_load_dwordx4 v8, s[44:47], s55 offen lds\n"
    "s_add_u32 m0, s54, 57344\n"
    "s_add_u32 s55, %6, 368704\n"
    "buffer_load_dwordx4 v8, s[44:47], s55 offen lds\n"
    "s_add_u32 m0, s54, 94208\n"
    "s_add_u32 s55, %6, 368768\n"
    "buffer_load_dwordx4 v8, s[44:47], s55 offen lds\n"
    "s_add_u32 m0, s54, 24576\n"
    "s_add_u32 s55, %6, 442368\n"
    "buffer_load_dwordx4 v8, s[44:47], s55 offen lds\n"
    "s_add_u32 m0, s54, 61440\n"
    "s_add_u32 s55, %6, 442432\n"
    "buffer_load_dwordx4 v8, s[44:47], s55 offen lds\n"
    "s_add_u32 m0, s54, 98304\n"
    "s_add_u32 s55, %6, 442496\n"
    "buffer_load_dwordx4 v8, s[44:47], s55 offen lds\n"
    "s_add_u32 m0, s54, 28672\n"
    "s_add_u32 s55, %6, 516096\n"
    "buffer_load_dwordx4 v8, s[44:47], s55 offen lds\n"
    "s_add_u32 m0, s54, 65536\n"
    "s_add_u32 s55, %6, 516160\n"
    "buffer_load_dwordx4 v8, s[44:47], s55 offen lds\n"
    "s_add_u32 m0, s54, 102400\n"
    "s_add_u32 s55, %6, 516224\n"
    "buffer_load_dwordx4 v8, s[44:47], s55 offen lds\n"
    "s_add_u32 m0, s54, 32768\n"
    "s_add_u32 s55, %6, 589824\n"
    "buffer_load_dwordx4 v8, s[44:47], s55 offen lds\n"
    "s_add_u32 m0, s54, 69632\n"
    "s_add_u32 s55, %6, 589888\n"
    "buffer_load_dwordx4 v8, s[44:47], s55 offen lds\n"
    "s_add_u32 m0, s54, 106496\n"
    "s_add_u32 s55, %6, 589952\n"
    "buffer_load_dwordx4 v8, s[44:47], s55 offen lds\n"
    "v_mov_b32 v7, 0x20400\n"
    "global_load_dwordx2 v[20:21], v7, %1 sc1\n"
    "v_mbcnt_lo_u32_b32 v6, -1, 0\n"
    "v_min_u32 v6, 7, v6\n"
    "v_lshlrev_b32 v6, 7, v6\n"
    "v_add_u32 v6, 0x20000, v6\n"
    "global_load_dwordx2 v[22:23], v6, %1 sc1\n"
    "s_waitcnt vmcnt(2) lgkmcnt(0)\n"
    "s_barrier\n"
    "ds_read_b64_tr_b16 a[0:1], v9\n"
    "ds_read_b64_tr_b16 a[2:3], v9 offset:256\n"
    "ds_read_b64_tr_b16 a[4:5], v9 offset:8\n"
    "ds_read_b64_tr_b16 a[6:7], v9 offset:264\n"
    "ds_read_b64_tr_b16 a[8:9], v9 offset:2304\n"
    "ds_read_b64_tr_b16 a[10:11], v9 offset:2560\n"
    "ds_read_b64_tr_b16 a[12:13], v9 offset:2312\n"
    "ds_read_b64_tr_b16 a[14:15], v9 offset:2568\n"
    "ds_read_b64_tr_b16 a[16:17], v9 offset:4608\n"
    "ds_read_b64_tr_b16 a[18:19], v9 offset:4864\n"
    "ds_read_b64_tr_b16 a[20:21], v9 offset:4616\n"
    "ds_read_b64_tr_b16 a[22:23], v9 offset:4872\n"
    "ds_read_b64_tr_b16 a[24:25], v9 offset:6912\n"
    "ds_read_b64_tr_b16 a[26:27], v9 offset:7168\n"
    "ds_read_b64_tr_b16 a[28:29], v9 offset:6920\n"
    "ds_read_b64_tr_b16 a[30:31], v9 offset:7176\n"
    "ds_read_b64_tr_b16 a[32:33], v9 offset:9216\n"
    "ds_read_b64_tr_b16 a[34:35], v9 offset:9472\n"
    "ds_read_b64_tr_b16 a[36:37], v9 offset:9224\n"
    "ds_read_b64_tr_b16 a[38:39], v9 offset:9480\n"
    "ds_read_b64_tr_b16 a[40:41], v9 offset:11520\n"
    "ds_read_b64_tr_b16 a[42:43], v9 offset:11776\n"
    "ds_read_b64_tr_b16 a[44:45], v9 offset:11528\n"
    "ds_read_b64_tr_b16 a[46:47], v9 offset:11784\n"
    "ds_read_b64_tr_b16 a[48:49], v9 offset:13824\n"
    "ds_read_b64_tr_b16 a[50:51], v9 offset:14080\n"
    "ds_read_b64_tr_b16 a[52:53], v9 offset:13832\n"
    "ds_read_b64_tr_b16 a[54:55], v9 offset:14088\n"
    "ds_read_b64_tr_b16 a[56:57], v9 offset:16128\n"
    "ds_read_b64_tr_b16 a[58:59], v9 offset:16384\n"
    "ds_read_b64_tr_b16 a[60:61], v9 offset:16136\n"
    "ds_read_b64_tr_b16 a[62:63], v9 offset:16392\n"
    "ds_read_b64_tr_b16 a[64:65], v9 offset:18432\n"
    "ds_read_b64_tr_b16 a[66:67], v9 offset:18688\n"
    "ds_read_b64_tr_b16 a[68:69], v9 offset:18440\n"
    "ds_read_b64_tr_b16 a[70:71], v9 offset:18696\n"
    "ds_read_b64_tr_b16 a[72:73], v9 offset:20736\n"
    "ds_read_b64_tr_b16 a[74:75], v9 offset:20992\n"
    "ds_read_b64_tr_b16 a[76:77], v9 offset:20744\n"
    "ds_read_b64_tr_b16 a[78:79], v9 offset:21000\n"
    "ds_read_b64_tr_b16 a[80:81], v9 offset:23040\n"
    "ds_read_b64_tr_b16 a[82:83], v9 offset:23296\n"
    "ds_read_b64_tr_b16 a[84:85], v9 offset:23048\n"
    "ds_read_b64_tr_b16 a[86:87], v9 offset:23304\n"
    "ds_read_b64_tr_b16 a[88:89], v9 offset:25344\n"
    "ds_read_b64_tr_b16 a[90:91], v9 offset:25600\n"
    "ds_read_b64_tr_b16 a[92:93], v9 offset:25352\n"
    "ds_read_b64_tr_b16 a[94:95], v9 offset:25608\n"
    "ds_read_b64_tr_b16 a[96:97], v9 offset:27648\n"
    "ds_read_b64_tr_b16 a[98:99], v9 offset:27904\n"
    "ds_read_b64_tr_b16 a[100:101], v9 offset:27656\n"
    "ds_read_b64_tr_b16 a[102:103], v9 offset:27912\n"
    "ds_read_b64_tr_b16 a[104:105], v9 offset:29952\n"
    "ds_read_b64_tr_b16 a[106:107], v9 offset:30208\n"
    "ds_read_b64_tr_b16 a[108:109], v9 offset:29960\n"
    "ds_read_b64_tr_b16 a[110:111], v9 offset:30216\n"
    "ds_read_b64_tr_b16 a[112:113], v9 offset:32256\n"
    "ds_read_b64_tr_b16 a[114:115], v9 offset:32512\n"
    "ds_read_b64_tr_b16 a[116:117], v9 offset:32264\n"
    "ds_read_b64_tr_b16 a[118:119], v9 offset:32520\n"
    "ds_read_b64_tr_b16 a[120:121], v9 offset:34560\n"
    "ds_read_b64_tr_b16 a[122:123], v9 offset:34816\n"
    "ds_read_b64_tr_b16 a[124:125], v9 offset:34568\n"
    "ds_read_b64_tr_b16 a[126:127], v9 offset:34824\n"
    "ds_read_b64_tr_b16 a[128:129], v10\n"
    "ds_read_b64_tr_b16 a[130:131], v10 offset:2304\n"
    "ds_read_b64_tr_b16 a[132:133], v10 offset:8\n"
    "ds_read_b64_tr_b16 a[134:135], v10 offset:2312\n"
    "ds_read_b64_tr_b16 a[136:137], v10 offset:18432\n"
    "ds_read_b64_tr_b16 a[138:139], v10 offset:20736\n"
    "ds_read_b64_tr_b16 a[140:141], v10 offset:18440\n"
    "ds_read_b64_tr_b16 a[142:143], v10 offset:20744\n"
    "s_waitcnt lgkmcnt(0)\n"
    "s_barrier\n"
    "s_mov_b64 s[44:45], %1\n"
    "s_and_b32 s45, s45, 0xffff\n"
    "s_mov_b32 s46, 0x0\n"
    "s_mov_b32 s47, 0x20000\n"
    "s_waitcnt vmcnt(0)\n"
    "v_add_u32 v20, 1, v20\n"
    "v_add_u32 v21, 1, v21\n"
    "s_mov_b32 s52, 1048576\n"
    ".Lbgo_a%=:\n"
    "v_cmp_ne_u64 vcc, v[20:21], v[22:23]\n"
    "s_cbranch_vccz .Lbgo_x%=\n"
    "global_load_dwordx2 v[22:23], v6, %1 sc1\n"
    "s_sub_u32 s52, s52, 1\n"
    "s_waitcnt vmcnt(0)\n"
    "s_cbranch_scc0 .Lbgo_a%=\n"
    ".Lbgo_x%=:\n"
    "v_mbcnt_lo_u32_b32 v20, -1, 0\n"
    "v_mbcnt_hi_u32_b32 v20, -1, v20\n"
    "v_min_u32 v6, 7, v20\n"
    "v_lshlrev_b32 v6, 7, v6\n"
    "v_mov_b32 v7, 0x26c00\n"
    "ds_read_b128 v[20:23], v7\n"
    "s_waitcnt lgkmcnt(0)\n"
    "v_readfirstlane_b32 s49, v21\n"
    "v_add_u32 v20, v20, v6\n"
    "s_branch .Lbchk_%=\n"
    ".Lbgo_%=:\n"
    "v_add_u32 v7, 32, v7\n"
    "ds_read_b128 v[20:23], v7\n"
    "v_xor_b32 v0, 0x8000, v0\n"
    "v_xor_b32 v4, 0x8000, v4\n"
    "v_add_u32 v104, 8192, v4\n"
    "s_waitcnt lgkmcnt(0)\n"
    "v_add_u32 v20, v20, v6\n"
    "v_readfirstlane_b32 s62, v22\n"
    "v_readfirstlane_b32 s49, v21\n"
    "s_xor_b32 s58, s58, 0x8000\n"
    "global_load_dword v105, v20, %1 sc1\n"
    "s_waitcnt vmcnt(3)\n"
    ".Lbtop_%=:\n"
    "s_barrier\n"
    "ds_read_b128 v[24:27], v0\n"
    "ds_read_b128 v[28:31], v0 offset:1024\n"
    "ds_read_b128 v[32:35], v0 offset:2048\n"
    "ds_read_b128 v[36:39], v0 offset:3072\n"
    "ds_read_b128 v[40:43], v0 offset:8192\n"
    "ds_read_b128 v[44:47], v0 offset:9216\n"
    "v_lshlrev_b32 v8, 16, v100\n"
    "v_and_b32 v9, 0xffff0000, v100\n"
    "v_lshlrev_b32 v10, 16, v101\n"
    "v_and_b32 v11, 0xffff0000, v101\n"
    "v_lshlrev_b32 v12, 16, v102\n"
    "v_and_b32 v13, 0xffff0000, v102\n"
    "v_lshlrev_b32 v14, 16, v103\n"
    "v_and_b32 v15, 0xffff0000, v103\n"
    "s_waitcnt lgkmcnt(5)\n"
    "v_mfma_f32_16x16x32_bf16 v[8:11], a[0:3], v[24:27], v[8:11]\n"
    "ds_read_b128 v[48:51], v0 offset:10240\n"
    "v_mfma_f32_16x16x32_bf16 v[12:15], a[4:7], v[24:27], v[12:15]\n"
    "ds_read_b128 v[52:55], v0 offset:11264\n"
    "s_waitcnt lgkmcnt(6)\n"
    "v_mfma_f32_16x16x32_bf16 v[8:11], a[8:11], v[28:31], v[8:11]\n"
    "ds_read_b128 v[56:59], v0 offset:16384\n"
    "v_mfma_f32_16x16x32_bf16 v[12:15], a[12:15], v[28:31], v[12:15]\n"
    "ds_read_b128 v[60:63], v0 offset:17408\n"
    "s_waitcnt lgkmcnt(7)\n"
    "v_mfma_f32_16x16x32_bf16 v[8:11], a[16:19], v[32:35], v[8:11]\n"
    "ds_read_b128 v[64:67], v0 offset:18432\n"
    "v_mfma_f32_16x16x32_bf16 v[12:15], a[20:23], v[32:35], v[12:15]\n"
    "ds_read_b128 v[68:71], v0 offset:19456\n"
    "s_waitcnt lgkmcnt(8)\n"
    "v_mfma_f32_16x16x32_bf16 v[8:11], a[24:27], v[36:39], v[8:11]\n"
    "ds_read_b128 v[72:75], v0 offset:24576\n"
    "v_mfma_f32_16x16x32_bf16 v[12:15], a[28:31], v[36:39], v[12:15]\n"
    "ds_read_b128 v[76:79], v0 offset:25600\n"
    "s_waitcnt lgkmcnt(9)\n"
    "v_mfma_f32_16x16x32_bf16 v[8:11], a[32:35], v[40:43], v[8:11]\n"
    "ds_read_b128 v[80:83], v0 offset:26624\n"
    "v_mfma_f32_16x16x32_bf16 v[12:15], a[36:39], v[40:43], v[12:15]\n"
    "ds_read_b128 v[84:87], v0 offset:27648\n"
    "s_waitcnt lgkmcnt(10)\n"
    "v_mfma_f32_16x16x32_bf16 v[8:11], a[40:43], v[44:47], v[8:11]\n"
    "ds_read2_b32 v[88:89], v4 offset1:16\n"
    "v_mov_b32 v22, 1\n"
    "v_mfma_f32_16x16x32_bf16 v[12:15], a[44:47], v[44:47], v[12:15]\n"
    "ds_read2_b32 v[90:91], v4 offset0:32 offset1:48\n"
    "s_waitcnt lgkmcnt(11)\n"
    "v_mfma_f32_16x16x32_bf16 v[8:11], a[48:51], v[48:51], v[8:11]\n"
    "s_waitcnt vmcnt(1)\n"
    "s_mov_b64 exec, s[56:57]\n"
    "ds_add_rtn_u32 v22, v7, v22 offset:16\n"
    "s_mov_b64 exec, -1\n"
    "v_mfma_f32_16x16x32_bf16 v[12:15], a[52:55], v[48:51], v[12:15]\n"
    "ds_read2_b32 v[92:93], v104 offset1:16\n"
    "s_waitcnt vmcnt(0)\n"
    "v_cmp_eq_u32 vcc, 0, v105\n"
    "s_cmp_eq_u64 vcc, 0\n"
    "s_waitcnt lgkmcnt(11)\n"
    "v_mfma_f32_16x16x32_bf16 v[8:11], a[56:59], v[52:55], v[8:11]\n"
    "s_cselect_b32 s94, s42, 0\n"
    "s_cselect_b32 s90, s96, 0\n"
    "s_cselect_b32 s46, 0, 0x20000\n"
    "v_mfma_f32_16x16x32_bf16 v[12:15], a[60:63], v[52:55], v[12:15]\n"
    "buffer_load_dword v105, v20, s[44:47], 0 offen sc1\n"
    "s_waitcnt lgkmcnt(10)\n"
    "v_mfma_f32_16x16x32_bf16 v[8:11], a[64:67], v[56:59], v[8:11]\n"
    "ds_read2_b32 v[94:95], v104 offset0:32 offset1:48\n"
    "s_mov_b32 m0, s58\n"
    "v_mfma_f32_16x16x32_bf16 v[12:15], a[68:71], v[56:59], v[12:15]\n"
    "buffer_load_dwordx4 v2, s[92:95], s49 offen sc1 lds\n"
    "s_waitcnt lgkmcnt(2)\n"
    "v_readfirstlane_b32 s50, v22\n"
    "v_mfma_f32_16x16x32_bf16 v[8:11], a[72:75], v[60:63], v[8:11]\n"
    "s_cmp_eq_u32 s50, 3\n"
    "s_cselect_b32 s46, 0x20000, 0\n"
    "v_mfma_f32_16x16x32_bf16 v[12:15], a[76:79], v[60:63], v[12:15]\n"
    "buffer_store_dword v22, v23, s[44:47], 0 offen sc1\n"
    "v_mfma_f32_16x16x32_bf16 v[8:11], a[80:83], v[64:67], v[8:11]\n"
    "buffer_load_dwordx4 v2, s[92:95], s49 offen offset:1024 sc1 lds\n"
    "v_mfma_f32_16x16x32_bf16 v[12:15], a[84:87], v[64:67], v[12:15]\n"
    "v_mfma_f32_16x16x32_bf16 v[8:11], a[88:91], v[68:71], v[8:11]\n"
    "v_mfma_f32_16x16x32_bf16 v[12:15], a[92:95], v[68:71], v[12:15]\n"
    "buffer_load_dwordx4 v2, s[92:95], s49 offen offset:2048 sc1 lds\n"
    "v_mfma_f32_16x16x32_bf16 v[8:11], a[96:99], v[72:75], v[8:11]\n"
    "v_mfma_f32_16x16x32_bf16 v[12:15], a[100:103], v[72:75], v[12:15]\n"
    "v_mfma_f32_16x16x32_bf16 v[8:11], a[104:107], v[76:79], v[8:11]\n"
    "buffer_load_dwordx4 v2, s[92:95], s49 offen offset:3072 sc1 lds\n"
    "v_mfma_f32_16x16x32_bf16 v[12:15], a[108:111], v[76:79], v[12:15]\n"
    "v_mfma_f32_16x16x32_bf16 v[8:11], a[112:115], v[80:83], v[8:11]\n"
    "s_bitset1_b32 m0, 12\n"
    "v_mfma_f32_16x16x32_bf16 v[12:15], a[116:119], v[80:83], v[12:15]\n"
    "buffer_load_dwordx4 v1, s[88:91], s49 offen sc1 lds\n"
    "v_mfma_f32_16x16x32_bf16 v[8:11], a[120:123], v[84:87], v[8:11]\n"
    "v_mfma_f32_16x16x32_bf16 v[12:15], a[124:127], v[84:87], v[12:15]\n"
    "s_sub_u32 s55, s49, 0x1000000\n"
    "v_mfma_f32_16x16x32_bf16 v[8:11], a[128:131], v[88:91], v[8:11]\n"
    "buffer_load_dwordx4 v[100:103], v3, s[92:95], s55 offen sc1\n"
    "v_mfma_f32_16x16x32_bf16 v[12:15], a[132:135], v[88:91], v[12:15]\n"
    "s_waitcnt lgkmcnt(0)\n"
    "v_mfma_f32_16x16x32_bf16 v[8:11], a[136:139], v[92:95], v[8:11]\n"
    "v_mfma_f32_16x16x32_bf16 v[12:15], a[140:143], v[92:95], v[12:15]\n"
    "s_nop 4\n"
    "v_cvt_pk_bf16_f32 v16, v8, v9\n"
    "v_cvt_pk_bf16_f32 v17, v10, v11\n"
    "s_nop 1\n"
    "v_cvt_pk_bf16_f32 v18, v12, v13\n"
    "v_cvt_pk_bf16_f32 v19, v14, v15\n"
    "buffer_store_dwordx4 v[16:19], v3, s[40:43], s62 offen sc1\n"
    "v_dot2_f32_bf16 v8, v16, s86, 0\n"
    "v_dot2_f32_bf16 v9, v16, v16, 0\n"
    "v_dot2_f32_bf16 v8, v17, s86, v8\n"
    "v_dot2_f32_bf16 v9, v17, v17, v9\n"
    "v_dot2_f32_bf16 v8, v18, s86, v8\n"
    "v_dot2_f32_bf16 v9, v18, v18, v9\n"
    "v_dot2_f32_bf16 v8, v19, s86, v8\n"
    "v_dot2_f32_bf16 v9, v19, v19, v9\n"
    "s_nop 2\n"
    "v_permlane32_swap_b32 v8, v9\n"
    "v_add_f32 v10, v8, v9\n"
    "v_add_f32 v8, v8, v9\n"
    "s_nop 1\n"
    "v_permlane16_swap_b32 v10, v8\n"
    "v_add_f32 v8, v8, v10\n"
    "buffer_store_dword v8, v5, s[40:43], s62 offen sc1\n"
    "s_mov_b32 s56, 1\n"
    "s_cmp_lg_u32 s94, 0\n"
    "s_cbranch_scc1 .Lbgo_%=\n"
    "ds_read_b32 v23, v7 offset:44\n"
    "s_waitcnt vmcnt(8) lgkmcnt(0)\n"
    "v_cmp_eq_u32 vcc, 0, v105\n"
    "s_cmp_eq_u64 vcc, 0\n"
    "s_cbranch_scc1 .Lbld_%=\n"
    "global_load_dword v105, v20, %1 sc1\n"
    "s_waitcnt vmcnt(0)\n"
    "v_mov_b32 v22, 1\n"
    "s_mov_b64 exec, s[56:57]\n"
    "ds_add_rtn_u32 v22, v7, v22 offset:48\n"
    "s_mov_b64 exec, -1\n"
    "s_waitcnt lgkmcnt(0)\n"
    "v_readfirstlane_b32 s50, v22\n"
    "s_cmp_eq_u32 s50, 3\n"
    "s_cselect_b32 s46, 0x20000, 0\n"
    "buffer_store_dword v22, v23, s[44:47], 0 offen sc1\n"
    "s_mov_b32 s56, 0\n"
    "v_cmp_eq_u32 vcc, 0, v105\n"
    "s_cmp_eq_u64 vcc, 0\n"
    "s_cbranch_scc1 .Lbld_%=\n"
    ".Lbchk_%=:\n"
    "v_readfirstlane_b32 s50, v20\n"
    "s_cmp_lt_u32 s50, 200\n"
    "s_cbranch_scc1 .Lbend_%=\n"
    ".Lbwait_%=:\n"
    "global_load_dword v105, v20, %1 sc1\n"
    "s_mov_b32 s52, 0\n"
    ".Lbp1_%=:\n"
    "s_waitcnt vmcnt(0)\n"
    "v_cmp_eq_u32 vcc, 0, v105\n"
    "s_cmp_eq_u64 vcc, 0\n"
    "s_cbranch_scc1 .Lbp1_x%=\n"
    "s_add_u32 s52, s52, 1\n"
    "s_sleep 2\n"
    "global_load_dword v105, v20, %1 sc1\n"
    "s_cmp_lt_u32 s52, s53\n"
    "s_cbranch_scc1 .Lbp1_%=\n"
    "s_mov_b32 s53, 0\n"
    "v_mov_b32 v22, 0\n"
    "v_mov_b32 v23, 1\n"
    "global_store_dword v22, v23, %1 sc1\n"
    "s_waitcnt vmcnt(0)\n"
    ".Lbp1_x%=:\n"
    ".Lbld_%=:\n"
    "s_mov_b32 s94, s42\n"
    "s_mov_b32 s90, s96\n"
    "s_mov_b32 m0, s58\n"
    "s_nop 0\n"
    "buffer_load_dwordx4 v2, s[92:95], s49 offen sc1 lds\n"
    "buffer_load_dwordx4 v2, s[92:95], s49 offen offset:1024 sc1 lds\n"
    "buffer_load_dwordx4 v2, s[92:95], s49 offen offset:2048 sc1 lds\n"
    "buffer_load_dwordx4 v2, s[92:95], s49 offen offset:3072 sc1 lds\n"
    "s_bitset1_b32 m0, 12\n"
    "s_nop 0\n"
    "buffer_load_dwordx4 v1, s[88:91], s49 offen sc1 lds\n"
    "s_sub_u32 s55, s49, 0x1000000\n"
    "buffer_load_dwordx4 v[100:103], v3, s[92:95], s55 offen sc1\n"
    "s_waitcnt vmcnt(6)\n"
    "v_mov_b32 v22, 1\n"
    "s_mov_b64 exec, s[56:57]\n"
    "ds_add_rtn_u32 v22, v7, v22 offset:48\n"
    "s_mov_b64 exec, -1\n"
    "s_waitcnt lgkmcnt(0)\n"
    "v_readfirstlane_b32 s50, v22\n"
    "s_cmp_eq_u32 s50, 3\n"
    "s_cselect_b32 s46, 0x20000, 0\n"
    "buffer_store_dword v22, v23, s[44:47], 0 offen sc1\n"
    "s_mov_b32 s56, 0\n"
    "v_add_u32 v7, 32, v7\n"
    "ds_read_b128 v[20:23], v7\n"
    "v_xor_b32 v0, 0x8000, v0\n"
    "v_xor_b32 v4, 0x8000, v4\n"
    "v_add_u32 v104, 8192, v4\n"
    "s_waitcnt lgkmcnt(0)\n"
    "v_add_u32 v20, v20, v6\n"
    "v_readfirstlane_b32 s62, v22\n"
    "v_readfirstlane_b32 s49, v21\n"
    "s_xor_b32 s58, s58, 0x8000\n"
    "s_waitcnt vmcnt(1)\n"
    "global_load_dword v105, v20, %1 sc1\n"
    "s_branch .Lbtop_%=\n"
    ".Lbpol_%=:\n"
    "v_mov_b32 v20, 64\n"
    "global_load_dword v105, v20, %1 sc1\n"
    "s_mov_b32 s52, 0\n"
    ".Lbp2_%=:\n"
    "s_waitcnt vmcnt(0)\n"
    "v_cmp_eq_u32 vcc, 0, v105\n"
    "s_cmp_eq_u64 vcc, 0\n"
    "s_cbranch_scc1 .Lbp2_x%=\n"
    "s_add_u32 s52, s52, 1\n"
    "s_sleep 2\n"
    "global_load_dword v105, v20, %1 sc1\n"
    "s_cmp_lt_u32 s52, s53\n"
    "s_cbranch_scc1 .Lbp2_%=\n"
    "s_mov_b32 s53, 0\n"
    "v_mov_b32 v22, 0\n"
    "v_mov_b32 v23, 1\n"
    "global_store_dword v22, v23, %1 sc1\n"
    "s_waitcnt vmcnt(0)\n"
    ".Lbp2_x%=:\n"
    "v_readfirstlane_b32 s50, v105\n"
    "v_mbcnt_lo_u32_b32 v20, -1, 0\n"
    "v_mbcnt_hi_u32_b32 v20, -1, v20\n"
    "s_lshl_b32 s51, %2, 6\n"
    "v_add_u32 v20, s51, v20\n"
    "v_min_u32 v20, 129, v20\n"
    "v_lshlrev_b32 v20, 5, v20\n"
    "v_add_u32 v20, 0x26c00, v20\n"
    "ds_read2_b32 v[22:23], v20 offset0:1 offset1:2\n"
    "s_and_b32 s50, s50, 1\n"
    "s_waitcnt lgkmcnt(0)\n"
    "v_and_b32 v21, s50, v22\n"
    "v_lshlrev_b32 v21, 13, v21\n"
    "v_and_b32 v22, -2, v22\n"
    "v_xor_b32 v22, v21, v22\n"
    "v_and_b32 v21, s50, v23\n"
    "v_lshlrev_b32 v21, 13, v21\n"
    "v_and_b32 v23, -2, v23\n"
    "v_xor_b32 v23, v21, v23\n"
    "ds_write2_b32 v20, v22, v23 offset0:1 offset1:2\n"
    "s_waitcnt lgkmcnt(0)\n"
    "s_barrier\n"
    "ds_read_b32 v21, v7 offset:4\n"
    "v_add_u32 v20, %4, v6\n"
    "s_waitcnt lgkmcnt(0)\n"
    "v_readfirstlane_b32 s49, v21\n"
    "s_branch .Lbwait_%=\n"
    ".Lbend_%=:\n"
    "s_cmp_eq_u32 s50, 192\n"
    "s_cbranch_scc0 .Lbpol_%=\n"
    "s_waitcnt vmcnt(0) lgkmcnt(0)\n"
    ".Lrole5_%=:\n"
    ".Lfin:\n"
        :: "s"(wd), "s"(ws), "s"(wave), "s"(rid), "s"(P.h), "s"(P.p), "s"(P.woff), "s"(layer), "s"(w0), "s"(Wk), "s"(Wv), "s"(Wo),
           "s"(mem0), "s"(P.role) : CLOBBERS);
}

extern "C" void kernel_launch(void* const* d_in, const int* in_sizes, int n_in, void* d_out, int out_size, void* d_ws,
                              size_t ws_size, hipStream_t stream) {
    mega_kernel<<<256, 256, 0, stream>>>((const u16*)d_in[0], (const u16*)d_in[1], (const u16*)d_in[2], (const u16*)d_in[4],
                                         (const u16*)d_in[5], (const u16*)d_in[6], (const u16*)d_in[7], (const u16*)d_in[8],
                                         (u16*)d_out, (char*)d_ws);
}
```

```cpp
#include <hip/hip_runtime.h>
#include <hip/hip_bf16.h>
#include <stdint.h>

typedef unsigned short u16;
typedef unsigned u32;
typedef unsigned long long u64;
using bf16x8 = __attribute__((ext_vector_type(8))) short;
using bf16x4 = __attribute__((ext_vector_type(4))) short;
using f32x4 = __attribute__((ext_vector_type(4))) float;
using f32x2 = __attribute__((ext_vector_type(2))) float;
using v4u = __attribute__((ext_vector_type(4))) unsigned;
using v2u = __attribute__((ext_vector_type(2))) unsigned;
typedef __bf16 bfv2 __attribute__((ext_vector_type(2)));
#define GAS __attribute__((address_space(1)))
typedef GAS unsigned gu32;
typedef GAS u64 gu64;
#define RLX_AGENT __ATOMIC_RELAXED, __HIP_MEMORY_SCOPE_AGENT
typedef __amdgpu_buffer_rsrc_t rsrc_t;

constexpr int S_ = 2048, D_ = 576, NSEG = 64, SEG = 32, NB = 4096, NQA = 1728;

constexpr u32 OFF_ABORT = 0;
constexpr u32 OFF_POL = 64;
constexpr u32 OFF_RDY = 128;
constexpr u32 OFF_NEVER = 192;
constexpr u32 OFF_FA = 4096;
constexpr u32 OFF_FO = OFF_FA + 8192;
constexpr u32 OFF_FB = OFF_FO + 8192;
constexpr u32 OFF_FP = OFF_FB + 8192;
constexpr u32 OFF_COB = OFF_FP + 24576;
constexpr u32 OFF_FKV = OFF_COB + 32768;
constexpr u32 OFF_GO = 131072;
constexpr u32 OFF_EP = OFF_GO + 1024;
constexpr u32 SYNC_BYTES = OFF_EP + 128;
constexpr u32 MB2 = 2u << 20;
constexpr u32 OFF_XA = 0;
constexpr u32 OFF_XB = MB2;
constexpr u32 XB = S_ * D_ * 2;
constexpr u32 OFF_TA = 4 * MB2;
constexpr u32 OFF_TB = 8 * MB2;
constexpr u32 OFF_OA = 12 * MB2;
constexpr u32 OFF_PB = 16 * MB2;
constexpr u32 HB_ = 2 * 64 * 32 * 8192;
constexpr u32 OFF_QX = 32 * MB2;
constexpr u32 OFF_KX = OFF_QX + HB_;
constexpr u32 OFF_VX = OFF_KX + HB_;
constexpr u32 OFF_OX = OFF_VX + HB_;
constexpr u32 WS_BYTES = OFF_OX + HB_;
constexpr u32 L_TAB = 147456, L_IT = L_TAB + 11264, L_EX = 138240, L_W = 32064;
constexpr u32 O_SUM = 32768 + 2048;

#define STAMP(i, k) do {} while (0)
#define PH(k) do {} while (0)
__device__ __forceinline__ float bf2f(u16 v) { return __uint_as_float(((u32)v) << 16); }
__device__ __forceinline__ float bflo(u32 v) { return __uint_as_float(v << 16); }
__device__ __forceinline__ float bfhi(u32 v) { return __uint_as_float(v & 0xffff0000u); }
__device__ __forceinline__ u32 pack2(float a, float b) { bfv2 v = {(__bf16)a, (__bf16)b}; return __builtin_bit_cast(u32, v); }
__device__ __forceinline__ float rbf(float f) { return (float)(__bf16)f; }
__device__ __forceinline__ v4u ld16(rsrc_t r, u32 off) { return __builtin_amdgcn_raw_buffer_load_b128(r, off, 0, 16); }
__device__ __forceinline__ v2u ld8(rsrc_t r, u32 off) { return __builtin_amdgcn_raw_buffer_load_b64(r, off, 0, 16); }
__device__ __forceinline__ void st16(rsrc_t r, u32 off, v4u v) { __builtin_amdgcn_raw_buffer_store_b128(v, r, off, 0, 16); }
__device__ __forceinline__ void st8(rsrc_t r, u32 off, v2u v) { __builtin_amdgcn_raw_buffer_store_b64(v, r, off, 0, 16); }
__device__ __forceinline__ void st4(rsrc_t r, u32 off, u32 v) { __builtin_amdgcn_raw_buffer_store_b32(v, r, off, 0, 16); }
#define DRAIN() asm volatile("s_waitcnt vmcnt(0)" ::: "memory")
#define TOUCH(p) asm volatile("global_load_dword v255, %0, off" :: "v"(p) : "v255")
#define CBAR() asm volatile("" ::: "memory")
__device__ __forceinline__ u32 ldw(char* ws, u32 off) { return __hip_atomic_load((gu32*)(ws + off), RLX_AGENT); }
__device__ __forceinline__ void setw(char* ws, u32 off) { __hip_atomic_store((gu32*)(ws + off), 1u, RLX_AGENT); }
__device__ __forceinline__ u32 fl(u32 i) { return (i >> 5) * 1024 + (i & 31) * 4; }
constexpr u32 SPIN_CAP = 1u << 20;
#define SLP_W 2
#define SLP_C 8
__device__ __forceinline__ void until(u64 t) { while (__builtin_amdgcn_s_memrealtime() < t) __builtin_amdgcn_s_sleep(8); }
#define TDEL 0
__device__ __forceinline__ u64 succ(char* ws) {
    const u64 e = __hip_atomic_load((gu64*)(ws + OFF_EP), RLX_AGENT);
    return (u64)((u32)(e >> 32) + 1) << 32 | ((u32)e + 1);
}
__device__ __forceinline__ void wait_go(char* ws) {
    const u64 e = succ(ws);
    for (u32 n = 0; n < SPIN_CAP && __builtin_amdgcn_ballot_w64(__hip_atomic_load((gu64*)(ws + OFF_GO + (threadIdx.x & 7) * 128), RLX_AGENT) != e); ++n) __builtin_amdgcn_s_sleep(2);
    CBAR();
}
__device__ __forceinline__ bool spin_fail(char* ws, u32& spins) {
    ++spins;
    if ((spins & 63u) == 0u) {
        if (ldw(ws, OFF_ABORT)) return true;
        if (spins > SPIN_CAP) { __hip_atomic_store((gu32*)(ws + OFF_ABORT), 1u, RLX_AGENT); return true; }
    }
    return false;
}
__device__ __forceinline__ void wait_set(char* ws, u32 off) {
    u32 spins = 0;
    while (__builtin_amdgcn_ballot_w64(ldw(ws, off) == 0)) {
        if (spin_fail(ws, spins)) break;
        __builtin_amdgcn_s_sleep(SLP_W);
    }
    CBAR();
}
constexpr u32 OOB = 0x80000000u;
__device__ __forceinline__ u32 cgo(u32 cg) { return (cg >> 5) * 16384 + (cg & 31) * 256; }
__device__ __forceinline__ u32 sumo(u32 l) {
    const u32 lane = threadIdx.x & 63, b = l * 128 + (lane >> 5) * 64 + (lane & 15) * 4;
    return lane & 16 ? OOB : O_SUM + b / 576 * 1024 + b % 576;
}
__device__ __forceinline__ u32 sums(v4u x) {
    float s = 0.f, q = 0.f;
    __builtin_amdgcn_sched_barrier(0);
#pragma unroll
    for (int e = 0; e < 4; ++e) {
        const u32 z = x[e];
        const bfv2 y = __builtin_bit_cast(bfv2, z);
        s = __builtin_amdgcn_fdot2_f32_bf16(y, __builtin_bit_cast(bfv2, 0x3f803f80u), s, false);
        q = __builtin_amdgcn_fdot2_f32_bf16(y, y, q, false);
    }
    auto a = __builtin_amdgcn_permlane32_swap(__float_as_uint(s), __float_as_uint(q), false, false);
    const u32 r = __float_as_uint(__uint_as_float(a[0]) + __uint_as_float(a[1]));
    a = __builtin_amdgcn_permlane16_swap(r, r, false, false);
    return __float_as_uint(__uint_as_float(a[0]) + __uint_as_float(a[1]));
}

template <int N, bool FIN>
__device__ __forceinline__ void t_item(int rid, u32 i, u32 j, u32 m, u32 pol, u32 w, rsrc_t ro, char* ws, rsrc_t rs) {
    const u32 lane = threadIdx.x & 63, s = i & 63, side = (((s >> 5) ^ pol ^ m) & 1) << 13;
    v4u x[N], p[N][4];
    u32 v[N];
    wait_set(ws, OFF_FB + m * 4096 + j * 128 + fl(i));
#pragma unroll
    for (int u = 0; u < N; ++u) { v[u] = cgo(12 * j + 4 * (w + u) + (lane >> 4)) + (lane & 15) * 16; x[u] = ld16(rs, OFF_TB + (i << 16) + (FIN ? side : m << 13) + v[u]); }
    wait_set(ws, OFF_FP + (m * 3 + (j >> 1)) * 4096 + (lane & 3) * 128 + fl(i));
    STAMP(i, 0);
#pragma unroll
    for (int u = 0; u < N; ++u)
#pragma unroll
        for (int k = 0; k < 4; ++k) p[u][k] = __builtin_amdgcn_raw_buffer_load_b128(rs, v[u], OFF_PB + (i << 18) + (k << 16) + ((((i >> 3) ^ pol ^ m) & 1) << 13), 16);
#pragma unroll
    for (int u = 0; u < N; ++u) {
#pragma unroll
        for (int e = 0; e < 4; ++e) {
            f32x2 a = {bflo(x[u][e]), bfhi(x[u][e])};
#pragma unroll
            for (int k = 0; k < 4; ++k) a += f32x2{bflo(p[u][k][e]), bfhi(p[u][k][e])};
            x[u][e] = pack2(a[0], a[1]);
        }
        if (FIN) __builtin_amdgcn_raw_buffer_store_b128(x[u], ro, (s * 32 + 2 * (lane & 15) + m) * 1152 + (12 * j + 4 * (w + u) + (lane >> 4)) * 16, 0, 0);
        else {
            st16(rs, OFF_TA + ((64 + s) << 16) + side + v[u], x[u]);
            __builtin_amdgcn_raw_buffer_store_b32(sums(x[u]), rs, sumo(3 * j + w), OFF_TA + ((64 + s) << 16) + side, 16);
        }
    }
    STAMP(i, 1);
}
__device__ __forceinline__ bool t_last(u32* cnt) {
    DRAIN();
    u32 o = 0;
    if ((threadIdx.x & 63) == 0) o = __hip_atomic_fetch_add(cnt, 1u, __ATOMIC_RELAXED, __HIP_MEMORY_SCOPE_WORKGROUP);
    return __builtin_amdgcn_readfirstlane(o) == 2;
}
__device__ __forceinline__ void role_t(u64 t_in, int rid, u32 j, u32 m, const u16* x0, u16* out, char* ws, rsrc_t rs, char* smem) {
    const u32 lane = threadIdx.x & 63, w = __builtin_amdgcn_readfirstlane(threadIdx.x >> 6);
    u32* cnt = (u32*)smem + 64;
    cnt[threadIdx.x] = 0;
    __syncthreads();
    const rsrc_t ro = __builtin_amdgcn_make_buffer_rsrc(out, 0, XB, 0x00020000);
    if (w < 3) {
        const rsrc_t rx = __builtin_amdgcn_make_buffer_rsrc((void*)x0, 0, XB, 0x00020000);
        const u32 cg = 12 * j + 4 * w + (lane >> 4), vx = (2 * (lane & 15) + m) * 1152 + cg * 16, v = OFF_TA + (m << 13) + cgo(cg) + (lane & 15) * 16;
#pragma unroll 1
        for (u32 s0 = 0; s0 < 64; s0 += s0 < 4 ? 2 : 10) {
            const u32 n = s0 < 4 ? 2 : 10;
            if (s0 == 14) until(t_in + TDEL);
            v4u x[10];
#pragma unroll
            for (u32 u = 0; u < 10; ++u) x[u] = __builtin_amdgcn_raw_buffer_load_b128(rx, u < n ? vx + (s0 + u) * 36864 : OOB, 0, 0);
#pragma unroll
            for (u32 u = 0; u < 10; ++u) if (u < n) {
                st16(rs, v + ((s0 + u) << 16), x[u]);
                __builtin_amdgcn_raw_buffer_store_b32(sums(x[u]), rs, sumo(3 * j + w), OFF_TA + (m << 13) + ((s0 + u) << 16), 16);
            }
            if (s0 == 0) { wait_go(ws); __builtin_amdgcn_s_barrier(); }
            if (t_last(cnt + s0) && lane < n) setw(ws, OFF_FA + m * 4096 + j * 128 + fl(s0 + lane));
        }
    }
    else __builtin_amdgcn_s_barrier();
    CBAR();
    wait_set(ws, OFF_POL);
    const u32 pol = ldw(ws, OFF_POL);
    if (w == 3) {
        for (u32 i = 64; i < 127; ++i) t_item<3, true>(rid, i, j, m, pol, 0, ro, ws, rs);
        return;
    }
    for (u32 i = 0; i < 64; ++i) {
        t_item<1, false>(rid, i, j, m, pol, w, ro, ws, rs);
        if (t_last(cnt + 64 + i) && lane == 0) setw(ws, OFF_FA + m * 4096 + j * 128 + fl(64 + i));
    }
    t_item<1, true>(rid, 127, j, m, pol, w, ro, ws, rs);
}

#define CLOBBERS "memory", "vcc", "scc", \
    "v0", "v1", "v2", "v3", "v4", "v5", "v6", "v7", "v8", "v9", "v10", "v11", "v12", "v13", "v14", "v15", "v16", "v17", "v18", "v19", "v20", "v21", "v22", "v23", \
    "v24", "v25", "v26", "v27", "v28", "v29", "v30", "v31", "v32", "v33", "v34", "v35", "v36", "v37", "v38", "v39", "v40", "v41", "v42", "v43", "v44", "v45", "v46", "v47", \
    "v48", "v49", "v50", "v51", "v52", "v53", "v54", "v55", "v56", "v57", "v58", "v59", "v60", "v61", "v62", "v63", "v64", "v65", "v66", "v67", "v68", "v69", "v70", "v71", \
    "v72", "v73", "v74", "v75", "v76", "v77", "v78", "v79", "v80", "v81", "v82", "v83", "v84", "v85", "v86", "v87", "v88", "v89", "v90", "v91", "v92", "v93", "v94", "v95", \
    "v96", "v97", "v98", "v99", "v100", "v101", "v102", "v103", "v104", "v105", "v106", "v107", "v108", "v109", "v110", "v111", "v112", "v113", "v114", "v115", "v116", "v117", "v118", "v119", \
    "v120", "v121", "v122", "v123", "v124", "v125", "v126", "v127", "v128", "v129", "v130", "v131", "v132", "v133", "v134", "v135", "v136", "v137", "v138", "v139", "v140", "v141", "v142", "v143", \
    "v144", "v145", "v146", "v147", "v148", "v149", "v150", "v151", "v152", "v153", "v154", "v155", "v156", "v157", "v158", "v159", "v160", "v161", "v162", "v163", "v164", "v165", "v166", "v167", \
    "v168", "v169", "v170", "v171", "v172", "v173", "v174", "v175", "v176", "v177", "v178", "v179", "v180", "v181", "v182", "v183", "v184", "v185", "v186", "v187", "v188", "v189", "v190", "v191", \
    "v192", "v193", "v194", "v195", "v196", "v197", "v198", "v199", "v200", "v201", "v202", "v203", "v204", "v205", "v206", "v207", "v208", "v209", "v210", "v211", "v212", "v213", "v214", "v215", \
    "v216", "v217", "v218", "v219", "v220", "v221", "v222", "v223", "v224", "v225", "v226", "v227", "v228", "v229", "v230", "v231", "v232", "v233", "v234", "v235", "v236", "v237", "v238", "v239", \
    "v240", "v241", "v242", "v243", "v244", "v245", "v246", "v247", "v248", "v249", "v250", "v251", "v252", "v253", "v254", "v255", "a0", "a1", "a2", "a3", "a4", "a5", "a6", "a7", \
    "a8", "a9", "a10", "a11", "a12", "a13", "a14", "a15", "a16", "a17", "a18", "a19", "a20", "a21", "a22", "a23", "a24", "a25", "a26", "a27", "a28", "a29", "a30", "a31", \
    "a32", "a33", "a34", "a35", "a36", "a37", "a38", "a39", "a40", "a41", "a42", "a43", "a44", "a45", "a46", "a47", "a48", "a49", "a50", "a51", "a52", "a53", "a54", "a55", \
    "a56", "a57", "a58", "a59", "a60", "a61", "a62", "a63", "a64", "a65", "a66", "a67", "a68", "a69", "a70", "a71", "a72", "a73", "a74", "a75", "a76", "a77", "a78", "a79", \
    "a80", "a81", "a82", "a83", "a84", "a85", "a86", "a87", "a88", "a89", "a90", "a91", "a92", "a93", "a94", "a95", "a96", "a97", "a98", "a99", "a100", "a101", "a102", "a103", \
    "a104", "a105", "a106", "a107", "a108", "a109", "a110", "a111", "a112", "a113", "a114", "a115", "a116", "a117", "a118", "a119", "a120", "a121", "a122", "a123", "a124", "a125", "a126", "a127", \
    "a128", "a129", "a130", "a131", "a132", "a133", "a134", "a135", "a136", "a137", "a138", "a139", "a140", "a141", "a142", "a143", "a144", "a145", "a146", "a147", "a148", "a149", "a150", "a151", \
    "a152", "a153", "a154", "a155", "a156", "a157", "a158", "a159", "a160", "a161", "a162", "a163", "a164", "a165", "a166", "a167", "a168", "a169", "a170", "a171", "a172", "a173", "a174", "a175", \
    "a176", "a177", "a178", "a179", "a180", "a181", "a182", "a183", "a184", "a185", "a186", "a187", "a188", "a189", "a190", "a191", "a192", "a193", "a194", "a195", "a196", "a197", "a198", "a199", \
    "a200", "a201", "a202", "a203", "a204", "a205", "a206", "a207", "a208", "a209", "a210", "a211", "a212", "a213", "a214", "a215", "a216", "a217", "a218", "a219", "a220", "a221", "a222", "a223", \
    "a224", "a225", "a226", "a227", "a228", "a229", "a230", "a231", "a232", "a233", "a234", "a235", "a236", "a237", "a238", "a239", "a240", "a241", "a242", "a243", "a244", "a245", "a246", "a247", \
    "a248", "a249", "a250", "a251", "a252", "a253", "a254", "a255", "s38", "s39", "s40", "s41", "s42", "s43", "s44", "s45", "s46", "s47", "s48", "s49", "s50", "s51", "s52", "s53", \
    "s54", "s55", "s56", "s57", "s58", "s59", "s60", "s61", "s62", "s63", "s64", "s65", "s66", "s67", "s68", "s69", "s70", "s71", "s72", "s73", "s74", "s75", "s76", "s77", \
    "s78", "s79", "s80", "s81", "s82", "s83", "s84", "s85", "s86", "s87", "s88", "s89", "s90", "s91", "s92", "s93", "s94", "s95", "s96", "s97", "s98", "s99", "s100", "s101"
#define LANES() const int tid = threadIdx.x, lane = tid & 63, c = lane & 15, g = lane >> 4, q = c >> 2, p4 = c & 3; \
    const int wave = __builtin_amdgcn_readfirstlane(tid >> 6); u32* tab = (u32*)(smem + L_TAB) + tid; (void)q; (void)p4; (void)g
struct Par { u32 role, h, p, woff; };
__device__ __forceinline__ u32 rows(u32 i, u32 m) { return (i << 16) + (i < 64 ? m << 13 : (((i >> 5 ^ m) & 1) << 13) + 1); }
__device__ __forceinline__ u32 slot(u32 i) { return ((i & 63) << 19) + (((i >> 2 ^ i >> 6) & 1) << 13) + 1; }
__device__ __forceinline__ u32 part(u32 i, u32 m) { return (i << 18) + (((i >> 3 ^ m) & 1) << 13) + 1; }
template <class FS, class FD>
__device__ __forceinline__ u32 items(u32 K, u32 mk, u32 fin, u32 fout, FS src, FD dst, char* smem) {
    const u32 n = threadIdx.x - 1;
    auto idx = [&](u32 k) { const u32 t = k - K; return t < 2 * (64 - K) ? ((t & 1) ? K : 64) + (t >> 1) : k & 127; };
    if (threadIdx.x < 130) {
        v4u* row = (v4u*)(smem + L_IT) + 2 * threadIdx.x;
        row[0] = v4u{n + 1 == mk ? OFF_NEVER + 4 : n + 1 < 128 ? fin + fl(idx(n + 1)) : OFF_NEVER, src(idx(n + 1)), n + 1 ? dst(idx(n)) : dst(3) & ~1u, n ? fout + fl(idx(n - 1)) : 2048u};
        row[1] = v4u{0u, 0u, 0x14000 + (idx(n) >> 6) * 16384, idx(n)};
    }
    return fin + fl(mk ? 64 : 0);
}
#define KA 15
#define KB 14
#define KH 13
#define KW 12

__device__ __forceinline__ Par role_h(u32 m, u32 h, char* smem) {
    LANES();
    tab[0] = 0x10000 + wave * 8192 + lane * 16;
    tab[256] = tid * 16;
    tab[2 * 256] = cgo(16 * wave) + lane * 16;
    tab[3 * 256] = 0x10000 + 5120 + lane * 4;
    tab[4 * 256] = (8 * wave + g) * 8192 + (c ^ ((g | (wave & 1) << 2) << 1)) * 16;
    tab[5 * 256] = (4 * g + q) * 256 + ((2 * wave) ^ (q | (g & 1) << 2)) * 32 + p4 * 8;
    tab[6 * 256] = tab[5 * 256] ^ 32;
    const u32 f = items(KH, 0, OFF_FB + m * 4096, OFF_FKV + (h >> 2) * 4096 + (h & 3) * 256 + m * 128, [&](u32 i) { return OFF_TB + rows(i, m); }, [&](u32 i) { return OFF_QX + h * 16384 + m * 4096 + slot(i); }, smem);
    return Par{1, f, 0, h * 256};
}

__device__ __forceinline__ Par role_w(u32 m, u32 kb, u32 nb, char* smem) {
    LANES();
    tab[0] = wave * 8192 + lane * 16;
    tab[256] = wave * 32768 + lane * 16;
    tab[512] = cgo(24 * nb + 6 * wave + g) + c * 16;
    tab[768] = (256 * wave + 4 * g + (c >> 3)) * 1152 + ((lane & 7) ^ ((g & 1) << 2)) * 16;
#pragma unroll
    for (int t = 0; t < 3; ++t) {
        const int col = wave * 96 + (t < 2 ? p4 * 16 + t * 8 : 64 + p4 * 8);
        tab[(4 + t) * 256] = (col >> 7) * 2048 + (q & 1) * 1024 + (2 * g + (q >> 1)) * 128 + ((col & 127) ^ ((g & 1) << 6));
    }
    tab[7 * 256] = (g & 1) ? OOB : cgo(24 * nb + 6 * wave + 4 + (g >> 1)) + c * 16;
    const u32 f = items(KW, 0, OFF_COB + (m * 4 + kb) * 4096, OFF_FP + (m * 3 + nb) * 4096 + kb * 128, [&](u32 i) { return OFF_OX + kb * 8 * 16384 + m * 4096 + slot(i); },
          [&](u32 i) { return OFF_PB + kb * 65536 + part(i, m); }, smem);
    return Par{2, f, 0, kb * 1024 * 1152 + nb * 384};
}

__device__ __forceinline__ Par role_a2(u32 m, u32 j, char* smem) {
    LANES();
    const int w = wave < 3 ? wave : 2;
    tab[0] = 0x8000 + lane * 16;
    tab[256] = tid * 16;
    tab[512] = cgo(16 * wave) + lane * 16;
    tab[768] = wave < 3 ? cgo(12 * j + 4 * wave + g) + c * 16 : OOB;
    tab[4 * 256] = 0x9000 + g * 256 + c * 4;
    tab[5 * 256] = wave < 3 ? sumo(3 * j + wave) : OOB;
    tab[6 * 256] = (16 * wave + (lane >> 2)) * 1152 + (lane & 3) * 16;
    tab[7 * 256] = w * 36864 + p4 * 16 + (20 * (g >> 1) + 8 * (g & 1) + q) * 64;
    tab[8 * 256] = w * 36864 + p4 * 16 + (72 * g + 16 + q) * 64;
    const u32 f = items(KB, KB, OFF_FO + m * 4096, OFF_FB + m * 4096 + j * 128, [&](u32 i) { return OFF_OA + rows(i, m); }, [&](u32 i) { return OFF_TB + rows(i, m); }, smem);
    return Par{4, f, 0, j * 192};
}

__device__ __forceinline__ Par role_a1(u32 m, u32 hg, char* smem) {
    LANES();
    const int hd = 4 * hg + wave;
    tab -= 768;
    tab[0] = wave * 8192 + lane * 16;
    tab[256] = !(g & 1) ? cgo(2 * hd + (g >> 1)) + c * 16 : g == 1 && !p4 ? 32768 + hd * 64 + c * 4 : OOB;
    tab[2 * 256] = cgo(16 * wave) + lane * 16;
    tab[3 * 256] = tid * 16;
    tab[4 * 256] = L_EX + ((wave >> 1) * 16 + c) * 16 + (wave & 1) * 8;
    tab[5 * 256] = g ? L_EX + 4096 : tab[4 * 256] + 512;
    tab[6 * 256] = L_EX + 2048 + ((wave >> 1) * 16 + 4 * g) * 16 + (2 * (wave & 1) + (c & 1)) * 4;
    tab[7 * 256] = 5120 + lane * 4;
    tab[8 * 256] = 7424 + g * 16;
    tab[9 * 256] = 4 * g - c;
    tab[10 * 256] = (4 * g + q) * 144 + 36 * wave + 4 * (wave & 1) + p4 * 8;
    tab[11 * 256] = (wave < 2 ? p4 >> 1 : 2) * 9216 + (4 * g + q) * 144 + 72 * (p4 & 1) + 32;
    if (tid < 2) *(v4u*)(smem + L_EX + 4096 + tid * 1024) = v4u{0u, 0u, 0u, 0u};
    const u32 f = items(KA, KA, OFF_FA + m * 4096, OFF_FO + m * 4096 + hg * 128, [&](u32 i) { return OFF_TA + rows(i, m); }, [&](u32 i) { return OFF_OA + rows(i, m); }, smem);
    return Par{3, f, 0, hg * 144};
}

#define SB() __builtin_amdgcn_sched_barrier(0)
__device__ __forceinline__ u32 now32() { return (u32)__builtin_amdgcn_s_memtime(); }
__device__ __forceinline__ Par role_chain(u32 h, u32 p, char* smem) {
    LANES();
    const int ro = (q & 1) + 4 * (q >> 1) + 8 * (g >> 1) + 16 * (g & 1), rr = (q & 1) | (g & 1) << 1 | (q >> 1) << 2;
    tab[0] = lane * 16;
    tab[256] = L_W + (wave * 15 + (wave == 3 ? 2 : 0)) * 2048 + lane * 16;
    tab[2 * 256] = ((g & 1) + 4 * (g >> 1) + 8 * wave) * 8192 + (c ^ ((g | (wave & 1) << 2) << 1)) * 16;
    tab[3 * 256] = (rr + 8 * (g >> 1)) * 256 + ((2 * wave) ^ rr) * 32 + p4 * 8;
    tab[4 * 256] = tab[3 * 256] ^ 32;
    tab[5 * 256] = (2 * wave + (lane >> 5)) * 1152 + ((lane & 31) ^ ((lane >> 5 | (wave & 2)) << 2)) * 16;
    tab[6 * 256] = ((q & 1) + 4 * (g >> 1) + 16 * (g & 1)) * 512 + ((2 * wave + (p4 >> 1)) ^ ((q & 1) | (g & 2))) * 64 + (q >> 1) * 16 + (p4 & 1) * 8;
    tab[7 * 256] = (32 * wave + (lane >> 2)) * 1152 + (lane & 3) * 16;
    tab[8 * 256] = ro * 64 + (wave & 1) * 32 + p4 * 8;
    return Par{0, h, p, 0};
}

#define MAP 4
#define DED 3
#define YX 0
constexpr int RW = MAP == 4 ? (YX & 2 ? 94 : 93) : 90, RA1 = 24, RA2 = MAP >= 2 ? 184 : 56, RTT = 58;
#define PFO 3
#define WDEL1 700
#define PDEL 1000
#define ODEL 600
#define NSW 0
#define CDEL 1200
#define WDEL 700
#define NPOL 6
static_assert(OFF_GO == 131072u);
static_assert(OFF_EP == 132096u);
static_assert(OFF_POL == 64u);
static_assert(OFF_RDY == 128u);
static_assert(OFF_COB == 53248u);
static_assert(OFF_FKV == 86016u);
static_assert(OFF_XA == 0u);
static_assert(OFF_XB == 2097152u);
static_assert(OFF_TA == 8388608u);
static_assert(OFF_OA == 25165824u);
static_assert(OFF_NEVER == 192u);
static_assert(L_IT == 158720u);
static_assert(OFF_QX == 67108864u);
static_assert(OFF_KX == 100663296u);
static_assert(OFF_VX == 134217728u);
static_assert(OFF_OX == 167772160u);
static_assert(WS_BYTES == 201326592u);
static_assert(L_TAB == 147456u);
static_assert(L_EX == 138240u);
static_assert(O_SUM == 34816u);
static_assert(L_W == 32064);
__device__ __forceinline__ void measure_pol(char* ws, rsrc_t rs, char* smem) {
    const int wave = __builtin_amdgcn_readfirstlane(threadIdx.x >> 6);
    u32 best[2] = {~0u, ~0u};
    for (int n = 0; n < NPOL; ++n)
#pragma unroll
        for (int i = 0; i < 2; ++i) {
            SB();
            const u32 t0 = now32();
            SB();
            st4(rs, (wave & 1) * MB2 + (508 + 2 * i + (wave >> 1)) * 4096 + n * 128, t0);
            DRAIN();
            const u32 t = now32() - t0;
            best[i] = t < best[i] ? t : best[i];
        }
    int* d = (int*)smem;
    if ((threadIdx.x & 63) == 0) d[wave] = (int)(best[0] - best[1]);
    wait_go(ws);
    __syncthreads();
    if (threadIdx.x == 0) __hip_atomic_store((gu32*)(ws + OFF_POL), 4u | (d[0] + d[2] >= 0) | (d[1] + d[3] >= 0) << 1, RLX_AGENT);
}

__global__ __launch_bounds__(256, 1) void mega_kernel(const u16* __restrict__ x0, const u16* __restrict__ wqkv, const u16* __restrict__ woA,
                                                     const u16* __restrict__ mem0, const u16* __restrict__ Wq, const u16* __restrict__ Wk,
                                                     const u16* __restrict__ Wv, const u16* __restrict__ Wo, u16* out, char* ws) {
    __shared__ __attribute__((aligned(16))) char smem[163840];
    char* wd = (char*)(((uintptr_t)ws + SYNC_BYTES + MB2 - 1) & ~(uintptr_t)(MB2 - 1));
    const rsrc_t rs = __builtin_amdgcn_make_buffer_rsrc(wd, 0, WS_BYTES, 0x00020000);
    const u64 t_in = __builtin_amdgcn_s_memrealtime();
    {
        const char* pc; u32 n;
        asm volatile("s_getpc_b64 %0\n.Lbeg:\ns_mov_b32 %1, .Lfin-.Lbeg" : "=s"(pc), "=s"(n));
        const u32 o = ((blockIdx.x >> 3 & 3) * 256 + threadIdx.x) * 128;
        if (o < n) TOUCH(pc + o);
    }
    u32 rid = (blockIdx.x & 7) * 32 + (blockIdx.x >> 3);
    rid = (rid & 143) | (rid & 48) << 1 | (rid & 64) >> 2 ^ (DED & 16);
    if (rid & 16) {
        const u32 y = (rid >> 6) ^ YX, sl = blockIdx.x >> 3, m = sl & 1, e = sl >> 1;
        auto rw = [](u32 i) { const u32 v = 12 + (i + 3) % 12; return v / 6 * 32 + v % 6 + 26; };
        auto rh = [](u32 h) { return (h >> 3) * 32 + 16 + (h & 7); };
        if (y < 2) rid = e < 10 ? m * 128 + rh(y * 10 + e) : e < 12 ? m * 128 + rw((2 * y + e - 10) * 3) : y * 128 + (e < 13 ? 62 + m : (e - 12) * 32 + 24 + m);
        else rid = m * 128 + (y == 2 ? (e < 8 ? 24 + e : e < 16 - NSW ? rw((e & 3) * 3 + (e >> 2) - 1) : rh(16 + e)) : e < 12 - NSW ? rh(20 + e) : e < 12 ? rw((e & 3) * 3 + 2) : 46 + e);
    }
    asm volatile("" : "+s"(rid));
    STAMP(159, 0);
    const u32 xq = (rid >> 5) & 3, t = rid & 31, layer = rid >> 7;
    const int u = t - 24;
    const int role = t < 16 ? 0 : t < 24 ? 1 : xq == 0 ? 3 : u < 2 ? 4 : xq == 1 ? 5 : 2;
    const int ix = role == 3 ? u : role == 4 ? 2 * xq - 2 + u : role == 5 ? u - 2 : (xq * 6 + u - 5) % 12;
    if (role == 5) { role_t(t_in, rid, ix, layer, x0, out, ws, rs, smem); asm volatile("s_endpgm" ::: "memory"); }
    if (role == 3) for (u32 k = (layer * 8 + ix) * 256 + threadIdx.x; k < 15552; k += 4096) TOUCH((const char*)wqkv + k * 128);
    if (role == 0 && layer) {
        const u32 i = t * 256 + threadIdx.x;
        if ((DED & 2) && i < 3888) TOUCH((const char*)wqkv + (xq * 3888 + i) * 128);
        if (xq && i < 1728) TOUCH((const char*)woA + (i / 3) * 1152 + (xq - 1) * 384 + (i % 3) * 128);
        for (const u16* w : {Wq, Wk, Wv}) if ((PFO & 2) || w != Wq)
            for (u32 k = i; k < 9216; k += 4096) TOUCH((const char*)w + (k >> 4) * 8192 + xq * 2048 + (k & 15) * 128);
        until(t_in + ODEL);
        if (PFO & 1) for (u32 k = i; k < 9216; k += 4096) TOUCH((const char*)Wo + (xq * 9216 + k) * 128);
        until(t_in + PDEL);
        for (u32 k = 0; k < 18; ++k) st16(rs, (k % 3 * 3 >> 1) * MB2 + k / 3 * 262144 + (xq * 4096 + i) * 16, v4u{~0u, ~0u, ~0u, ~0u});
        DRAIN();
        wait_go(ws);
        __syncthreads();
        if (threadIdx.x == 0) __hip_atomic_fetch_add((gu32*)(ws + OFF_RDY), 1u, RLX_AGENT);
    }
    if (role == 2 && layer && ix < 8) {
        const u64 e = succ(ws);
        const rsrc_t rc = __builtin_amdgcn_make_buffer_rsrc(ws, 0, OFF_GO, 0x00020000);
        for (u32 k = 0; k < 4; ++k) st16(rc, ix * 16384 + k * 4096 + threadIdx.x * 16, v4u{0u, 0u, 0u, 0u});
        DRAIN();
        __syncthreads();
        if (threadIdx.x == 0) __hip_atomic_store((gu64*)(ws + OFF_GO + ix * 128), e, RLX_AGENT);
    }
    if (rid == RW) measure_pol(ws, rs, smem);
    until(t_in + (role == 2 ? (layer ? WDEL1 : WDEL) : role == 0 && layer ? CDEL : 0));
    Par P{5, 0, 0, 0};
    const u16* w0 = role == 2 ? Wo : role == 3 ? wqkv : role == 4 ? woA : Wq;
    if (role == 0) P = role_chain(xq * 8 + (t >> 1), t & 1, smem);
    else if (role == 1) P = role_h(layer, xq * 8 + t - 16, smem);
    else if (role == 2) P = role_w(layer, ix / 3, ix % 3, smem);
    else if (role == 3) P = role_a1(layer, ix, smem);
    else if (role == 4) P = role_a2(layer ^ (MAP >= 2), ix, smem);
    if (role == 0) P.woff = (u32)t_in;
    const int wave = __builtin_amdgcn_readfirstlane(threadIdx.x >> 6);
    asm volatile(
    "s_cmp_eq_u32 %13, 1\n"
    "s_cbranch_scc1 .Lrole1_%=\n"
    "s_cmp_eq_u32 %13, 2\n"
    "s_cbranch_scc1 .Lrole2_%=\n"
    "s_cmp_eq_u32 %13, 3\n"
    "s_cbranch_scc1 .Lrole3_%=\n"
    "s_cmp_eq_u32 %13, 4\n"
    "s_cbranch_scc1 .Lrole4_%=\n"
    "s_cmp_eq_u32 %13, 5\n"
    "s_cbranch_scc1 .Lrole5_%=\n"
    "s_mov_b64 s[40:41], %0\n"
    "s_and_b32 s41, s41, 0xffff\n"
    "s_mov_b32 s42, 0xc000000\n"
    "s_mov_b32 s43, 0x20000\n"
    "s_mov_b32 s57, 131072\n"
    "v_mbcnt_lo_u32_b32 v10, -1, 0\n"
    "v_mbcnt_hi_u32_b32 v10, -1, v10\n"
    "s_lshl_b32 s73, %2, 8\n"
    "s_add_u32 s73, s73, 147456\n"
    "v_lshl_add_u32 v10, v10, 2, s73\n"
    "ds_read_b32 v0, v10\n"
    "ds_read_b32 v2, v10 offset:1024\n"
    "ds_read_b32 v1, v10 offset:2048\n"
    "ds_read_b32 v3, v10 offset:3072\n"
    "ds_read_b32 v4, v10 offset:4096\n"
    "ds_read_b32 v5, v10 offset:5120\n"
    "ds_read_b32 v6, v10 offset:6144\n"
    "ds_read_b32 v8, v10 offset:7168\n"
    "ds_read_b32 v9, v10 offset:8192\n"
    "s_waitcnt lgkmcnt(0)\n"
    "s_barrier\n"
    "s_xor_b32 s73, %2, 0\n"
    "s_lshl_b32 s73, s73, 12\n"
    "v_add_u32 v96, s73, v3\n"
    "v_add_u32 v97, s73, v4\n"
    "s_xor_b32 s73, %2, 1\n"
    "s_lshl_b32 s73, s73, 12\n"
    "v_add_u32 v98, s73, v3\n"
    "v_add_u32 v99, s73, v4\n"
    "s_xor_b32 s73, %2, 2\n"
    "s_lshl_b32 s73, s73, 12\n"
    "v_add_u32 v100, s73, v3\n"
    "v_add_u32 v101, s73, v4\n"
    "s_xor_b32 s73, %2, 3\n"
    "s_lshl_b32 s73, s73, 12\n"
    "v_add_u32 v102, s73, v3\n"
    "v_add_u32 v103, s73, v4\n"
    "s_lshl_b32 s60, %4, 8\n"
    "s_lshl_b32 s61, %2, 10\n"
    "s_mul_i32 s63, %4, 147456\n"
    "s_mul_i32 s62, %5, 576\n"
    "s_add_u32 s63, s63, s62\n"
    "s_mov_b64 s[44:45], %11\n"
    "s_and_b32 s45, s45, 0xffff\n"
    "s_mov_b32 s46, 0x480000\n"
    "s_mov_b32 s47, 0x20000\n"
    "s_lshl_b32 s73, s61, 1\n"
    "s_add_u32 m0, s73, 0\n"
    "s_add_u32 s62, s63, 512\n"
    "buffer_load_dwordx4 v8, s[44:47], s62 offen lds\n"
    "s_lshl_b32 s73, s61, 1\n"
    "s_add_u32 m0, s73, 1024\n"
    "s_add_u32 s62, s63, 18944\n"
    "buffer_load_dwordx4 v8, s[44:47], s62 offen lds\n"
    "s_mov_b64 vcc, 0\n"
    "s_mov_b32 vcc_lo, %7\n"
    "s_cbranch_vccz .Lsl0_%=\n"
    "s_sleep 6\n"
    ".Lsl0_%=:\n"
    "s_mov_b64 s[44:45], %8\n"
    "s_and_b32 s45, s45, 0xffff\n"
    "s_mov_b32 s46, 0x480000\n"
    "s_mov_b32 s47, 0x20000\n"
    "s_add_u32 m0, s61, 16384\n"
    "s_add_u32 s62, s60, 262144\n"
    "buffer_load_dwordx4 v1, s[44:47], s62 offen lds\n"
    "s_add_u32 m0, s61, 20480\n"
    "s_add_u32 s62, s60, 1310720\n"
    "buffer_load_dwordx4 v1, s[44:47], s62 offen lds\n"
    "s_add_u32 m0, s61, 24576\n"
    "s_add_u32 s62, s60, 2359296\n"
    "buffer_load_dwordx4 v1, s[44:47], s62 offen lds\n"
    "s_add_u32 m0, s61, 28672\n"
    "s_add_u32 s62, s60, 3407872\n"
    "buffer_load_dwordx4 v1, s[44:47], s62 offen lds\n"
    "s_cbranch_vccz .Lsl1_%=\n"
    "s_sleep 6\n"
    ".Lsl1_%=:\n"
    "s_add_u32 m0, s61, 32768\n"
    "s_add_u32 s62, s60, 786432\n"
    "buffer_load_dwordx4 v1, s[44:47], s62 offen lds\n"
    "s_add_u32 m0, s61, 36864\n"
    "s_add_u32 s62, s60, 1835008\n"
    "buffer_load_dwordx4 v1, s[44:47], s62 offen lds\n"
    "s_add_u32 m0, s61, 40960\n"
    "s_add_u32 s62, s60, 2883584\n"
    "buffer_load_dwordx4 v1, s[44:47], s62 offen lds\n"
    "s_add_u32 m0, s61, 45056\n"
    "s_add_u32 s62, s60, 3932160\n"
    "buffer_load_dwordx4 v1, s[44:47], s62 offen lds\n"
    "s_cbranch_vccz .Lsl2_%=\n"
    "s_sleep 6\n"
    ".Lsl2_%=:\n"
    "s_add_u32 m0, s61, 49152\n"
    "s_add_u32 s62, s60, 278528\n"
    "buffer_load_dwordx4 v1, s[44:47], s62 offen lds\n"
    "s_add_u32 m0, s61, 53248\n"
    "s_add_u32 s62, s60, 1327104\n"
    "buffer_load_dwordx4 v1, s[44:47], s62 offen lds\n"
    "s_add_u32 m0, s61, 57344\n"
    "s_add_u32 s62, s60, 2375680\n"
    "buffer_load_dwordx4 v1, s[44:47], s62 offen lds\n"
    "s_add_u32 m0, s61, 61440\n"
    "s_add_u32 s62, s60, 3424256\n"
    "buffer_load_dwordx4 v1, s[44:47], s62 offen lds\n"
    "s_cbranch_vccz .Lsl3_%=\n"
    "s_sleep 6\n"
    ".Lsl3_%=:\n"
    "s_add_u32 m0, s61, 65536\n"
    "s_add_u32 s62, s60, 802816\n"
    "buffer_load_dwordx4 v1, s[44:47], s62 offen lds\n"
    "s_add_u32 m0, s61, 69632\n"
    "s_add_u32 s62, s60, 1851392\n"
    "buffer_load_dwordx4 v1, s[44:47], s62 offen lds\n"
    "s_add_u32 m0, s61, 73728\n"
    "s_add_u32 s62, s60, 2899968\n"
    "buffer_load_dwordx4 v1, s[44:47], s62 offen lds\n"
    "s_add_u32 m0, s61, 77824\n"
    "s_add_u32 s62, s60, 3948544\n"
    "buffer_load_dwordx4 v1, s[44:47], s62 offen lds\n"
    "s_cbranch_vccz .Lsl4_%=\n"
    "s_sleep 6\n"
    ".Lsl4_%=:\n"
    "s_add_u32 m0, s61, 81920\n"
    "s_add_u32 s62, s60, 4194304\n"
    "buffer_load_dwordx4 v1, s[44:47], s62 offen lds\n"
    "s_add_u32 m0, s61, 86016\n"
    "s_add_u32 s62, s60, 4210688\n"
    "buffer_load_dwordx4 v1, s[44:47], s62 offen lds\n"
    "s_add_u32 m0, s61, 90112\n"
    "s_add_u32 s62, s60, 4456448\n"
    "buffer_load_dwordx4 v1, s[44:47], s62 offen lds\n"
    "s_add_u32 m0, s61, 94208\n"
    "s_add_u32 s62, s60, 4472832\n"
    "buffer_load_dwordx4 v1, s[44:47], s62 offen lds\n"
    "s_cbranch_vccz .Lsl5_%=\n"
    "s_sleep 6\n"
    ".Lsl5_%=:\n"
    "s_mov_b64 s[44:45], %9\n"
    "s_and_b32 s45, s45, 0xffff\n"
    "s_mov_b32 s46, 0x480000\n"
    "s_mov_b32 s47, 0x20000\n"
    "s_add_u32 m0, s61, 98304\n"
    "s_add_u32 s62, s60, 0\n"
    "buffer_load_dwordx4 v1, s[44:47], s62 offen lds\n"
    "s_add_u32 m0, s61, 102400\n"
    "s_add_u32 s62, s60, 1048576\n"
    "buffer_load_dwordx4 v1, s[44:47], s62 offen lds\n"
    "s_add_u32 m0, s61, 106496\n"
    "s_add_u32 s62, s60, 2097152\n"
    "buffer_load_dwordx4 v1, s[44:47], s62 offen lds\n"
    "s_add_u32 m0, s61, 110592\n"
    "s_add_u32 s62, s60, 3145728\n"
    "buffer_load_dwordx4 v1, s[44:47], s62 offen lds\n"
    "s_cbranch_vccz .Lsl6_%=\n"
    "s_sleep 6\n"
    ".Lsl6_%=:\n"
    "s_add_u32 m0, s61, 114688\n"
    "s_add_u32 s62, s60, 524288\n"
    "buffer_load_dwordx4 v1, s[44:47], s62 offen lds\n"
    "s_add_u32 m0, s61, 118784\n"
    "s_add_u32 s62, s60, 1572864\n"
    "buffer_load_dwordx4 v1, s[44:47], s62 offen lds\n"
    "s_add_u32 m0, s61, 122880\n"
    "s_add_u32 s62, s60, 2621440\n"
    "buffer_load_dwordx4 v1, s[44:47], s62 offen lds\n"
    "s_add_u32 m0, s61, 126976\n"
    "s_add_u32 s62, s60, 3670016\n"
    "buffer_load_dwordx4 v1, s[44:47], s62 offen lds\n"
    "s_cbranch_vccz .Lsl7_%=\n"
    "s_sleep 6\n"
    ".Lsl7_%=:\n"
    "s_add_u32 m0, s61, 131072\n"
    "s_add_u32 s62, s60, 16384\n"
    "buffer_load_dwordx4 v1, s[44:47], s62 offen lds\n"
    "s_add_u32 m0, s61, 135168\n"
    "s_add_u32 s62, s60, 1064960\n"
    "buffer_load_dwordx4 v1, s[44:47], s62 offen lds\n"
    "s_add_u32 m0, s61, 139264\n"
    "s_add_u32 s62, s60, 2113536\n"
    "buffer_load_dwordx4 v1, s[44:47], s62 offen lds\n"
    "s_add_u32 m0, s61, 143360\n"
    "s_add_u32 s62, s60, 3162112\n"
    "buffer_load_dwordx4 v1, s[44:47], s62 offen lds\n"
    "s_cbranch_vccz .Lsl8_%=\n"
    "s_sleep 6\n"
    ".Lsl8_%=:\n"
    "s_add_u32 m0, s61, 147456\n"
    "s_add_u32 s62, s60, 540672\n"
    "buffer_load_dwordx4 v1, s[44:47], s62 offen lds\n"
    "s_add_u32 m0, s61, 151552\n"
    "s_add_u32 s62, s60, 1589248\n"
    "buffer_load_dwordx4 v1, s[44:47], s62 offen lds\n"
    "s_add_u32 m0, s61, 155648\n"
    "s_add_u32 s62, s60, 2637824\n"
    "buffer_load_dwordx4 v1, s[44:47], s62 offen lds\n"
    "s_add_u32 m0, s61, 159744\n"
    "s_add_u32 s62, s60, 3686400\n"
    "buffer_load_dwordx4 v1, s[44:47], s62 offen lds\n"
    "s_waitcnt vmcnt(32) lgkmcnt(0)\n"
    "s_barrier\n"
    "ds_read_b64_tr_b16 v[36:37], v96 offset:16384\n"
    "ds_read_b64_tr_b16 v[68:69], v98 offset:16384\n"
    "ds_read_b64_tr_b16 v[148:149], v100 offset:16384\n"
    "ds_read_b64_tr_b16 v[180:181], v102 offset:16384\n"
    "ds_read_b64_tr_b16 v[44:45], v97 offset:16384\n"
    "ds_read_b64_tr_b16 v[76:77], v99 offset:16384\n"
    "ds_read_b64_tr_b16 v[156:157], v101 offset:16384\n"
    "ds_read_b64_tr_b16 v[188:189], v103 offset:16384\n"
    "s_waitcnt vmcnt(28) lgkmcnt(0)\n"
    "s_barrier\n"
    "s_cbranch_vccz .Lsl9_%=\n"
    "s_sleep 6\n"
    ".Lsl9_%=:\n"
    "s_add_u32 m0, s61, 16384\n"
    "s_add_u32 s62, s60, 262144\n"
    "buffer_load_dwordx4 v1, s[44:47], s62 offen lds\n"
    "s_add_u32 m0, s61, 20480\n"
    "s_add_u32 s62, s60, 1310720\n"
    "buffer_load_dwordx4 v1, s[44:47], s62 offen lds\n"
    "s_add_u32 m0, s61, 24576\n"
    "s_add_u32 s62, s60, 2359296\n"
    "buffer_load_dwordx4 v1, s[44:47], s62 offen lds\n"
    "s_add_u32 m0, s61, 28672\n"
    "s_add_u32 s62, s60, 3407872\n"
    "buffer_load_dwordx4 v1, s[44:47], s62 offen lds\n"
    "ds_read_b64_tr_b16 v[52:53], v96 offset:32768\n"
    "ds_read_b64_tr_b16 v[84:85], v98 offset:32768\n"
    "ds_read_b64_tr_b16 v[164:165], v100 offset:32768\n"
    "ds_read_b64_tr_b16 v[196:197], v102 offset:32768\n"
    "ds_read_b64_tr_b16 v[60:61], v97 offset:32768\n"
    "ds_read_b64_tr_b16 v[140:141], v99 offset:32768\n"
    "ds_read_b64_tr_b16 v[172:173], v101 offset:32768\n"
    "ds_read_b64_tr_b16 v[204:205], v103 offset:32768\n"
    "s_waitcnt vmcnt(28) lgkmcnt(0)\n"
    "s_barrier\n"
    "s_cbranch_vccz .Lsl10_%=\n"
    "s_sleep 6\n"
    ".Lsl10_%=:\n"
    "s_add_u32 m0, s61, 32768\n"
    "s_add_u32 s62, s60, 786432\n"
    "buffer_load_dwordx4 v1, s[44:47], s62 offen lds\n"
    "s_add_u32 m0, s61, 36864\n"
    "s_add_u32 s62, s60, 1835008\n"
    "buffer_load_dwordx4 v1, s[44:47], s62 offen lds\n"
    "s_add_u32 m0, s61, 40960\n"
    "s_add_u32 s62, s60, 2883584\n"
    "buffer_load_dwordx4 v1, s[44:47], s62 offen lds\n"
    "s_add_u32 m0, s61, 45056\n"
    "s_add_u32 s62, s60, 3932160\n"
    "buffer_load_dwordx4 v1, s[44:47], s62 offen lds\n"
    "ds_read_b64_tr_b16 v[38:39], v96 offset:49152\n"
    "ds_read_b64_tr_b16 v[70:71], v98 offset:49152\n"
    "ds_read_b64_tr_b16 v[150:151], v100 offset:49152\n"
    "ds_read_b64_tr_b16 v[182:183], v102 offset:49152\n"
    "ds_read_b64_tr_b16 v[46:47], v97 offset:49152\n"
    "ds_read_b64_tr_b16 v[78:79], v99 offset:49152\n"
    "ds_read_b64_tr_b16 v[158:159], v101 offset:49152\n"
    "ds_read_b64_tr_b16 v[190:191], v103 offset:49152\n"
    "s_waitcnt vmcnt(28) lgkmcnt(0)\n"
    "s_barrier\n"
    "s_cbranch_vccz .Lsl11_%=\n"
    "s_sleep 6\n"
    ".Lsl11_%=:\n"
    "s_add_u32 m0, s61, 49152\n"
    "s_add_u32 s62, s60, 278528\n"
    "buffer_load_dwordx4 v1, s[44:47], s62 offen lds\n"
    "s_add_u32 m0, s61, 53248\n"
    "s_add_u32 s62, s60, 1327104\n"
    "buffer_load_dwordx4 v1, s[44:47], s62 offen lds\n"
    "s_add_u32 m0, s61, 57344\n"
    "s_add_u32 s62, s60, 2375680\n"
    "buffer_load_dwordx4 v1, s[44:47], s62 offen lds\n"
    "s_add_u32 m0, s61, 61440\n"
    "s_add_u32 s62, s60, 3424256\n"
    "buffer_load_dwordx4 v1, s[44:47], s62 offen lds\n"
    "v_add_u32 v127, 0x10000, v96\n"
    "ds_read_b64_tr_b16 v[54:55], v127\n"
    "v_add_u32 v126, 0x10000, v98\n"
    "ds_read_b64_tr_b16 v[86:87], v126\n"
    "v_add_u32 v125, 0x10000, v100\n"
    "ds_read_b64_tr_b16 v[166:167], v125\n"
    "v_add_u32 v124, 0x10000, v102\n"
    "ds_read_b64_tr_b16 v[198:199], v124\n"
    "v_add_u32 v123, 0x10000, v97\n"
    "ds_read_b64_tr_b16 v[62:63], v123\n"
    "v_add_u32 v122, 0x10000, v99\n"
    "ds_read_b64_tr_b16 v[142:143], v122\n"
    "v_add_u32 v121, 0x10000, v101\n"
    "ds_read_b64_tr_b16 v[174:175], v121\n"
    "v_add_u32 v120, 0x10000, v103\n"
    "ds_read_b64_tr_b16 v[206:207], v120\n"
    "s_waitcnt vmcnt(28) lgkmcnt(0)\n"
    "s_barrier\n"
    "s_cbranch_vccz .Lsl12_%=\n"
    "s_sleep 6\n"
    ".Lsl12_%=:\n"
    "s_add_u32 m0, s61, 65536\n"
    "s_add_u32 s62, s60, 802816\n"
    "buffer_load_dwordx4 v1, s[44:47], s62 offen lds\n"
    "s_add_u32 m0, s61, 69632\n"
    "s_add_u32 s62, s60, 1851392\n"
    "buffer_load_dwordx4 v1, s[44:47], s62 offen lds\n"
    "s_add_u32 m0, s61, 73728\n"
    "s_add_u32 s62, s60, 2899968\n"
    "buffer_load_dwordx4 v1, s[44:47], s62 offen lds\n"
    "s_add_u32 m0, s61, 77824\n"
    "s_add_u32 s62, s60, 3948544\n"
    "buffer_load_dwordx4 v1, s[44:47], s62 offen lds\n"
    "v_add_u32 v119, 0x10000, v3\n"
    "ds_read_b64_tr_b16 v[208:209], v119 offset:16384\n"
    "ds_read_b64_tr_b16 v[210:211], v119 offset:20480\n"
    "ds_read_b64_tr_b16 v[212:213], v119 offset:24576\n"
    "ds_read_b64_tr_b16 v[214:215], v119 offset:28672\n"
    "v_add_u32 v118, 0x10000, v4\n"
    "ds_read_b64_tr_b16 v[216:217], v118 offset:16384\n"
    "ds_read_b64_tr_b16 v[218:219], v118 offset:20480\n"
    "ds_read_b64_tr_b16 v[220:221], v118 offset:24576\n"
    "ds_read_b64_tr_b16 v[222:223], v118 offset:28672\n"
    "s_waitcnt vmcnt(28) lgkmcnt(0)\n"
    "s_barrier\n"
    "s_cbranch_vccz .Lsl13_%=\n"
    "s_sleep 6\n"
    ".Lsl13_%=:\n"
    "s_add_u32 m0, s61, 81920\n"
    "s_add_u32 s62, s60, 4194304\n"
    "buffer_load_dwordx4 v1, s[44:47], s62 offen lds\n"
    "s_add_u32 m0, s61, 86016\n"
    "s_add_u32 s62, s60, 4210688\n"
    "buffer_load_dwordx4 v1, s[44:47], s62 offen lds\n"
    "s_add_u32 m0, s61, 90112\n"
    "s_add_u32 s62, s60, 4456448\n"
    "buffer_load_dwordx4 v1, s[44:47], s62 offen lds\n"
    "s_add_u32 m0, s61, 94208\n"
    "s_add_u32 s62, s60, 4472832\n"
    "buffer_load_dwordx4 v1, s[44:47], s62 offen lds\n"
    "ds_read_b64_tr_b16 v[224:225], v127 offset:32768\n"
    "ds_read_b64_tr_b16 a[0:1], v126 offset:32768\n"
    "ds_read_b64_tr_b16 a[32:33], v125 offset:32768\n"
    "ds_read_b64_tr_b16 a[64:65], v124 offset:32768\n"
    "ds_read_b64_tr_b16 v[232:233], v123 offset:32768\n"
    "ds_read_b64_tr_b16 a[8:9], v122 offset:32768\n"
    "ds_read_b64_tr_b16 a[40:41], v121 offset:32768\n"
    "ds_read_b64_tr_b16 a[72:73], v120 offset:32768\n"
    "s_waitcnt vmcnt(28) lgkmcnt(0)\n"
    "s_barrier\n"
    "s_cbranch_vccz .Lsl14_%=\n"
    "s_sleep 6\n"
    ".Lsl14_%=:\n"
    "s_mov_b64 s[44:45], %10\n"
    "s_and_b32 s45, s45, 0xffff\n"
    "s_mov_b32 s46, 0x480000\n"
    "s_mov_b32 s47, 0x20000\n"
    "s_add_u32 m0, s61, 98304\n"
    "s_add_u32 s62, s60, 0\n"
    "buffer_load_dwordx4 v1, s[44:47], s62 offen lds\n"
    "s_add_u32 m0, s61, 102400\n"
    "s_add_u32 s62, s60, 1048576\n"
    "buffer_load_dwordx4 v1, s[44:47], s62 offen lds\n"
    "s_add_u32 m0, s61, 106496\n"
    "s_add_u32 s62, s60, 2097152\n"
    "buffer_load_dwordx4 v1, s[44:47], s62 offen lds\n"
    "s_add_u32 m0, s61, 110592\n"
    "s_add_u32 s62, s60, 3145728\n"
    "buffer_load_dwordx4 v1, s[44:47], s62 offen lds\n"
    "ds_read_b64_tr_b16 v[240:241], v127 offset:49152\n"
    "ds_read_b64_tr_b16 a[16:17], v126 offset:49152\n"
    "ds_read_b64_tr_b16 a[48:49], v125 offset:49152\n"
    "ds_read_b64_tr_b16 a[80:81], v124 offset:49152\n"
    "ds_read_b64_tr_b16 v[248:249], v123 offset:49152\n"
    "ds_read_b64_tr_b16 a[24:25], v122 offset:49152\n"
    "ds_read_b64_tr_b16 a[56:57], v121 offset:49152\n"
    "ds_read_b64_tr_b16 a[88:89], v120 offset:49152\n"
    "s_waitcnt vmcnt(28) lgkmcnt(0)\n"
    "s_barrier\n"
    "s_cbranch_vccz .Lsl15_%=\n"
    "s_sleep 6\n"
    ".Lsl15_%=:\n"
    "s_add_u32 m0, s61, 114688\n"
    "s_add_u32 s62, s60, 524288\n"
    "buffer_load_dwordx4 v1, s[44:47], s62 offen lds\n"
    "s_add_u32 m0, s61, 118784\n"
    "s_add_u32 s62, s60, 1572864\n"
    "buffer_load_dwordx4 v1, s[44:47], s62 offen lds\n"
    "s_add_u32 m0, s61, 122880\n"
    "s_add_u32 s62, s60, 2621440\n"
    "buffer_load_dwordx4 v1, s[44:47], s62 offen lds\n"
    "s_add_u32 m0, s61, 126976\n"
    "s_add_u32 s62, s60, 3670016\n"
    "buffer_load_dwordx4 v1, s[44:47], s62 offen lds\n"
    "v_add_u32 v117, 0x20000, v96\n"
    "ds_read_b64_tr_b16 v[226:227], v117\n"
    "v_add_u32 v116, 0x20000, v98\n"
    "ds_read_b64_tr_b16 a[2:3], v116\n"
    "v_add_u32 v115, 0x20000, v100\n"
    "ds_read_b64_tr_b16 a[34:35], v115\n"
    "v_add_u32 v114, 0x20000, v102\n"
    "ds_read_b64_tr_b16 a[66:67], v114\n"
    "v_add_u32 v113, 0x20000, v97\n"
    "ds_read_b64_tr_b16 v[234:235], v113\n"
    "v_add_u32 v112, 0x20000, v99\n"
    "ds_read_b64_tr_b16 a[10:11], v112\n"
    "v_add_u32 v111, 0x20000, v101\n"
    "ds_read_b64_tr_b16 a[42:43], v111\n"
    "v_add_u32 v110, 0x20000, v103\n"
    "ds_read_b64_tr_b16 a[74:75], v110\n"
    "s_waitcnt vmcnt(28) lgkmcnt(0)\n"
    "s_barrier\n"
    "s_cbranch_vccz .Lsl16_%=\n"
    "s_sleep 6\n"
    ".Lsl16_%=:\n"
    "s_add_u32 m0, s61, 131072\n"
    "s_add_u32 s62, s60, 16384\n"
    "buffer_load_dwordx4 v1, s[44:47], s62 offen lds\n"
    "s_add_u32 m0, s61, 135168\n"
    "s_add_u32 s62, s60, 1064960\n"
    "buffer_load_dwordx4 v1, s[44:47], s62 offen lds\n"
    "s_add_u32 m0, s61, 139264\n"
    "s_add_u32 s62, s60, 2113536\n"
    "buffer_load_dwordx4 v1, s[44:47], s62 offen lds\n"
    "s_add_u32 m0, s61, 143360\n"
    "s_add_u32 s62, s60, 3162112\n"
    "buffer_load_dwordx4 v1, s[44:47], s62 offen lds\n"
    "ds_read_b64_tr_b16 v[242:243], v117 offset:16384\n"
    "ds_read_b64_tr_b16 a[18:19], v116 offset:16384\n"
    "ds_read_b64_tr_b16 a[50:51], v115 offset:16384\n"
    "ds_read_b64_tr_b16 a[82:83], v114 offset:16384\n"
    "ds_read_b64_tr_b16 v[250:251], v113 offset:16384\n"
    "ds_read_b64_tr_b16 a[26:27], v112 offset:16384\n"
    "ds_read_b64_tr_b16 a[58:59], v111 offset:16384\n"
    "ds_read_b64_tr_b16 a[90:91], v110 offset:16384\n"
    "s_waitcnt vmcnt(28) lgkmcnt(0)\n"
    "s_barrier\n"
    "s_cbranch_vccz .Lsl17_%=\n"
    "s_sleep 6\n"
    ".Lsl17_%=:\n"
    "s_add_u32 m0, s61, 147456\n"
    "s_add_u32 s62, s60, 540672\n"
    "buffer_load_dwordx4 v1, s[44:47], s62 offen lds\n"
    "s_add_u32 m0, s61, 151552\n"
    "s_add_u32 s62, s60, 1589248\n"
    "buffer_load_dwordx4 v1, s[44:47], s62 offen lds\n"
    "s_add_u32 m0, s61, 155648\n"
    "s_add_u32 s62, s60, 2637824\n"
    "buffer_load_dwordx4 v1, s[44:47], s62 offen lds\n"
    "s_add_u32 m0, s61, 159744\n"
    "s_add_u32 s62, s60, 3686400\n"
    "buffer_load_dwordx4 v1, s[44:47], s62 offen lds\n"
    "ds_read_b64_tr_b16 v[228:229], v96 offset:16384\n"
    "ds_read_b64_tr_b16 a[4:5], v98 offset:16384\n"
    "ds_read_b64_tr_b16 a[36:37], v100 offset:16384\n"
    "ds_read_b64_tr_b16 a[68:69], v102 offset:16384\n"
    "ds_read_b64_tr_b16 v[236:237], v97 offset:16384\n"
    "ds_read_b64_tr_b16 a[12:13], v99 offset:16384\n"
    "ds_read_b64_tr_b16 a[44:45], v101 offset:16384\n"
    "ds_read_b64_tr_b16 a[76:77], v103 offset:16384\n"
    "s_waitcnt vmcnt(28) lgkmcnt(0)\n"
    "s_barrier\n"
    "s_cbranch_vccz .Lsl18_%=\n"
    "s_sleep 6\n"
    ".Lsl18_%=:\n"
    "s_add_u32 m0, s61, 16384\n"
    "s_add_u32 s62, s60, 262144\n"
    "buffer_load_dwordx4 v1, s[44:47], s62 offen lds\n"
    "s_add_u32 m0, s61, 20480\n"
    "s_add_u32 s62, s60, 1310720\n"
    "buffer_load_dwordx4 v1, s[44:47], s62 offen lds\n"
    "s_add_u32 m0, s61, 24576\n"
    "s_add_u32 s62, s60, 2359296\n"
    "buffer_load_dwordx4 v1, s[44:47], s62 offen lds\n"
    "s_add_u32 m0, s61, 28672\n"
    "s_add_u32 s62, s60, 3407872\n"
    "buffer_load_dwordx4 v1, s[44:47], s62 offen lds\n"
    "ds_read_b64_tr_b16 v[244:245], v96 offset:32768\n"
    "ds_read_b64_tr_b16 a[20:21], v98 offset:32768\n"
    "ds_read_b64_tr_b16 a[52:53], v100 offset:32768\n"
    "ds_read_b64_tr_b16 a[84:85], v102 offset:32768\n"
    "ds_read_b64_tr_b16 v[252:253], v97 offset:32768\n"
    "ds_read_b64_tr_b16 a[28:29], v99 offset:32768\n"
    "ds_read_b64_tr_b16 a[60:61], v101 offset:32768\n"
    "ds_read_b64_tr_b16 a[92:93], v103 offset:32768\n"
    "s_waitcnt vmcnt(28) lgkmcnt(0)\n"
    "s_barrier\n"
    "s_cbranch_vccz .Lsl19_%=\n"
    "s_sleep 6\n"
    ".Lsl19_%=:\n"
    "s_add_u32 m0, s61, 32768\n"
    "s_add_u32 s62, s60, 786432\n"
    "buffer_load_dwordx4 v1, s[44:47], s62 offen lds\n"
    "s_add_u32 m0, s61, 36864\n"
    "s_add_u32 s62, s60, 1835008\n"
    "buffer_load_dwordx4 v1, s[44:47], s62 offen lds\n"
    "s_add_u32 m0, s61, 40960\n"
    "s_add_u32 s62, s60, 2883584\n"
    "buffer_load_dwordx4 v1, s[44:47], s62 offen lds\n"
    "s_add_u32 m0, s61, 45056\n"
    "s_add_u32 s62, s60, 3932160\n"
    "buffer_load_dwordx4 v1, s[44:47], s62 offen lds\n"
    "ds_read_b64_tr_b16 v[230:231], v96 offset:49152\n"
    "ds_read_b64_tr_b16 a[6:7], v98 offset:49152\n"
    "ds_read_b64_tr_b16 a[38:39], v100 offset:49152\n"
    "ds_read_b64_tr_b16 a[70:71], v102 offset:49152\n"
    "ds_read_b64_tr_b16 v[238:239], v97 offset:49152\n"
    "ds_read_b64_tr_b16 a[14:15], v99 offset:49152\n"
    "ds_read_b64_tr_b16 a[46:47], v101 offset:49152\n"
    "ds_read_b64_tr_b16 a[78:79], v103 offset:49152\n"
    "s_waitcnt vmcnt(28) lgkmcnt(0)\n"
    "s_barrier\n"
    "s_cbranch_vccz .Lsl20_%=\n"
    "s_sleep 6\n"
    ".Lsl20_%=:\n"
    "s_add_u32 m0, s61, 49152\n"
    "s_add_u32 s62, s60, 278528\n"
    "buffer_load_dwordx4 v1, s[44:47], s62 offen lds\n"
    "s_add_u32 m0, s61, 53248\n"
    "s_add_u32 s62, s60, 1327104\n"
    "buffer_load_dwordx4 v1, s[44:47], s62 offen lds\n"
    "s_add_u32 m0, s61, 57344\n"
    "s_add_u32 s62, s60, 2375680\n"
    "buffer_load_dwordx4 v1, s[44:47], s62 offen lds\n"
    "s_add_u32 m0, s61, 61440\n"
    "s_add_u32 s62, s60, 3424256\n"
    "buffer_load_dwordx4 v1, s[44:47], s62 offen lds\n"
    "ds_read_b64_tr_b16 v[246:247], v127\n"
    "ds_read_b64_tr_b16 a[22:23], v126\n"
    "ds_read_b64_tr_b16 a[54:55], v125\n"
    "ds_read_b64_tr_b16 a[86:87], v124\n"
    "ds_read_b64_tr_b16 v[254:255], v123\n"
    "ds_read_b64_tr_b16 a[30:31], v122\n"
    "ds_read_b64_tr_b16 a[62:63], v121\n"
    "ds_read_b64_tr_b16 a[94:95], v120\n"
    "s_waitcnt vmcnt(28) lgkmcnt(0)\n"
    "s_barrier\n"
    "s_cbranch_vccz .Lsl21_%=\n"
    "s_sleep 6\n"
    ".Lsl21_%=:\n"
    "s_add_u32 m0, s61, 65536\n"
    "s_add_u32 s62, s60, 802816\n"
    "buffer_load_dwordx4 v1, s[44:47], s62 offen lds\n"
    "s_add_u32 m0, s61, 69632\n"
    "s_add_u32 s62, s60, 1851392\n"
    "buffer_load_dwordx4 v1, s[44:47], s62 offen lds\n"
    "s_add_u32 m0, s61, 73728\n"
    "s_add_u32 s62, s60, 2899968\n"
    "buffer_load_dwordx4 v1, s[44:47], s62 offen lds\n"
    "s_add_u32 m0, s61, 77824\n"
    "s_add_u32 s62, s60, 3948544\n"
    "buffer_load_dwordx4 v1, s[44:47], s62 offen lds\n"
    "ds_read_b64_tr_b16 a[96:97], v119 offset:16384\n"
    "ds_read_b64_tr_b16 a[98:99], v119 offset:20480\n"
    "ds_read_b64_tr_b16 a[100:101], v119 offset:24576\n"
    "ds_read_b64_tr_b16 a[102:103], v119 offset:28672\n"
    "ds_read_b64_tr_b16 a[104:105], v118 offset:16384\n"
    "ds_read_b64_tr_b16 a[106:107], v118 offset:20480\n"
    "ds_read_b64_tr_b16 a[108:109], v118 offset:24576\n"
    "ds_read_b64_tr_b16 a[110:111], v118 offset:28672\n"
    "s_waitcnt vmcnt(28) lgkmcnt(0)\n"
    "s_barrier\n"
    "s_cbranch_vccz .Lsl22_%=\n"
    "s_sleep 6\n"
    ".Lsl22_%=:\n"
    "s_add_u32 m0, s61, 81920\n"
    "s_add_u32 s62, s60, 4194304\n"
    "buffer_load_dwordx4 v1, s[44:47], s62 offen lds\n"
    "s_add_u32 m0, s61, 86016\n"
    "s_add_u32 s62, s60, 4210688\n"
    "buffer_load_dwordx4 v1, s[44:47], s62 offen lds\n"
    "s_add_u32 m0, s61, 90112\n"
    "s_add_u32 s62, s60, 4456448\n"
    "buffer_load_dwordx4 v1, s[44:47], s62 offen lds\n"
    "s_add_u32 m0, s61, 94208\n"
    "s_add_u32 s62, s60, 4472832\n"
    "buffer_load_dwordx4 v1, s[44:47], s62 offen lds\n"
    "ds_read_b64_tr_b16 a[112:113], v127 offset:32768\n"
    "ds_read_b64_tr_b16 a[144:145], v126 offset:32768\n"
    "ds_read_b64_tr_b16 a[176:177], v125 offset:32768\n"
    "ds_read_b64_tr_b16 a[208:209], v124 offset:32768\n"
    "ds_read_b64_tr_b16 a[120:121], v123 offset:32768\n"
    "ds_read_b64_tr_b16 a[152:153], v122 offset:32768\n"
    "ds_read_b64_tr_b16 a[184:185], v121 offset:32768\n"
    "ds_read_b64_tr_b16 a[216:217], v120 offset:32768\n"
    "s_waitcnt vmcnt(28) lgkmcnt(0)\n"
    "s_barrier\n"
    "s_cbranch_vccz .Lsl23_%=\n"
    "s_sleep 6\n"
    ".Lsl23_%=:\n"
    "s_mov_b64 s[44:45], %8\n"
    "s_and_b32 s45, s45, 0xffff\n"
    "s_mov_b32 s46, 0x480000\n"
    "s_mov_b32 s47, 0x20000\n"
    "s_add_u32 m0, s61, 98304\n"
    "s_add_u32 s62, s60, 0\n"
    "buffer_load_dwordx4 v1, s[44:47], s62 offen lds\n"
    "s_add_u32 m0, s61, 102400\n"
    "s_add_u32 s62, s60, 1048576\n"
    "buffer_load_dwordx4 v1, s[44:47], s62 offen lds\n"
    "s_add_u32 m0, s61, 106496\n"
    "s_add_u32 s62, s60, 2097152\n"
    "buffer_load_dwordx4 v1, s[44:47], s62 offen lds\n"
    "s_add_u32 m0, s61, 110592\n"
    "s_add_u32 s62, s60, 3145728\n"
    "buffer_load_dwordx4 v1, s[44:47], s62 offen lds\n"
    "ds_read_b64_tr_b16 a[128:129], v127 offset:49152\n"
    "ds_read_b64_tr_b16 a[160:161], v126 offset:49152\n"
    "ds_read_b64_tr_b16 a[192:193], v125 offset:49152\n"
    "ds_read_b64_tr_b16 a[224:225], v124 offset:49152\n"
    "ds_read_b64_tr_b16 a[136:137], v123 offset:49152\n"
    "ds_read_b64_tr_b16 a[168:169], v122 offset:49152\n"
    "ds_read_b64_tr_b16 a[200:201], v121 offset:49152\n"
    "ds_read_b64_tr_b16 a[232:233], v120 offset:49152\n"
    "s_waitcnt vmcnt(28) lgkmcnt(0)\n"
    "s_barrier\n"
    "s_cbranch_vccz .Lsl24_%=\n"
    "s_sleep 6\n"
    ".Lsl24_%=:\n"
    "s_add_u32 m0, s61, 114688\n"
    "s_add_u32 s62, s60, 524288\n"
    "buffer_load_dwordx4 v1, s[44:47], s62 offen lds\n"
    "s_add_u32 m0, s61, 118784\n"
    "s_add_u32 s62, s60, 1572864\n"
    "buffer_load_dwordx4 v1, s[44:47], s62 offen lds\n"
    "s_add_u32 m0, s61, 122880\n"
    "s_add_u32 s62, s60, 2621440\n"
    "buffer_load_dwordx4 v1, s[44:47], s62 offen lds\n"
    "s_add_u32 m0, s61, 126976\n"
    "s_add_u32 s62, s60, 3670016\n"
    "buffer_load_dwordx4 v1, s[44:47], s62 offen lds\n"
    "ds_read_b64_tr_b16 a[114:115], v117\n"
    "ds_read_b64_tr_b16 a[146:147], v116\n"
    "ds_read_b64_tr_b16 a[178:179], v115\n"
    "ds_read_b64_tr_b16 a[210:211], v114\n"
    "ds_read_b64_tr_b16 a[122:123], v113\n"
    "ds_read_b64_tr_b16 a[154:155], v112\n"
    "ds_read_b64_tr_b16 a[186:187], v111\n"
    "ds_read_b64_tr_b16 a[218:219], v110\n"
    "s_waitcnt vmcnt(28) lgkmcnt(0)\n"
    "s_barrier\n"
    "s_cbranch_vccz .Lsl25_%=\n"
    "s_sleep 6\n"
    ".Lsl25_%=:\n"
    "s_add_u32 m0, s61, 131072\n"
    "s_add_u32 s62, s60, 16384\n"
    "buffer_load_dwordx4 v1, s[44:47], s62 offen lds\n"
    "s_add_u32 m0, s61, 135168\n"
    "s_add_u32 s62, s60, 1064960\n"
    "buffer_load_dwordx4 v1, s[44:47], s62 offen lds\n"
    "s_add_u32 m0, s61, 139264\n"
    "s_add_u32 s62, s60, 2113536\n"
    "buffer_load_dwordx4 v1, s[44:47], s62 offen lds\n"
    "s_add_u32 m0, s61, 143360\n"
    "s_add_u32 s62, s60, 3162112\n"
    "buffer_load_dwordx4 v1, s[44:47], s62 offen lds\n"
    "ds_read_b64_tr_b16 a[130:131], v117 offset:16384\n"
    "ds_read_b64_tr_b16 a[162:163], v116 offset:16384\n"
    "ds_read_b64_tr_b16 a[194:195], v115 offset:16384\n"
    "ds_read_b64_tr_b16 a[226:227], v114 offset:16384\n"
    "ds_read_b64_tr_b16 a[138:139], v113 offset:16384\n"
    "ds_read_b64_tr_b16 a[170:171], v112 offset:16384\n"
    "ds_read_b64_tr_b16 a[202:203], v111 offset:16384\n"
    "ds_read_b64_tr_b16 a[234:235], v110 offset:16384\n"
    "s_waitcnt vmcnt(28) lgkmcnt(0)\n"
    "s_barrier\n"
    "s_cbranch_vccz .Lsl26_%=\n"
    "s_sleep 6\n"
    ".Lsl26_%=:\n"
    "s_add_u32 m0, s61, 147456\n"
    "s_add_u32 s62, s60, 540672\n"
    "buffer_load_dwordx4 v1, s[44:47], s62 offen lds\n"
    "s_add_u32 m0, s61, 151552\n"
    "s_add_u32 s62, s60, 1589248\n"
    "buffer_load_dwordx4 v1, s[44:47], s62 offen lds\n"
    "s_add_u32 m0, s61, 155648\n"
    "s_add_u32 s62, s60, 2637824\n"
    "buffer_load_dwordx4 v1, s[44:47], s62 offen lds\n"
    "s_add_u32 m0, s61, 159744\n"
    "s_add_u32 s62, s60, 3686400\n"
    "buffer_load_dwordx4 v1, s[44:47], s62 offen lds\n"
    "ds_read_b64_tr_b16 a[116:117], v96 offset:16384\n"
    "ds_read_b64_tr_b16 a[148:149], v98 offset:16384\n"
    "ds_read_b64_tr_b16 a[180:181], v100 offset:16384\n"
    "ds_read_b64_tr_b16 a[212:213], v102 offset:16384\n"
    "ds_read_b64_tr_b16 a[124:125], v97 offset:16384\n"
    "ds_read_b64_tr_b16 a[156:157], v99 offset:16384\n"
    "ds_read_b64_tr_b16 a[188:189], v101 offset:16384\n"
    "ds_read_b64_tr_b16 a[220:221], v103 offset:16384\n"
    "s_waitcnt vmcnt(28) lgkmcnt(0)\n"
    "s_barrier\n"
    "s_cbranch_vccz .Lsl27_%=\n"
    "s_sleep 6\n"
    ".Lsl27_%=:\n"
    "s_mov_b64 s[44:45], %11\n"
    "s_and_b32 s45, s45, 0xffff\n"
    "s_mov_b32 s46, 0x480000\n"
    "s_mov_b32 s47, 0x20000\n"
    "s_add_u32 m0, s61, 16384\n"
    "s_add_u32 s62, s63, 0\n"
    "buffer_load_dwordx4 v5, s[44:47], s62 offen lds\n"
    "s_add_u32 m0, s61, 20480\n"
    "s_add_u32 s62, s63, 9216\n"
    "buffer_load_dwordx4 v5, s[44:47], s62 offen lds\n"
    "s_add_u32 m0, s61, 24576\n"
    "s_add_u32 s62, s63, 18432\n"
    "buffer_load_dwordx4 v5, s[44:47], s62 offen lds\n"
    "s_add_u32 m0, s61, 28672\n"
    "s_add_u32 s62, s63, 27648\n"
    "buffer_load_dwordx4 v5, s[44:47], s62 offen lds\n"
    "ds_read_b64_tr_b16 a[132:133], v96 offset:32768\n"
    "ds_read_b64_tr_b16 a[164:165], v98 offset:32768\n"
    "ds_read_b64_tr_b16 a[196:197], v100 offset:32768\n"
    "ds_read_b64_tr_b16 a[228:229], v102 offset:32768\n"
    "ds_read_b64_tr_b16 a[140:141], v97 offset:32768\n"
    "ds_read_b64_tr_b16 a[172:173], v99 offset:32768\n"
    "ds_read_b64_tr_b16 a[204:205], v101 offset:32768\n"
    "ds_read_b64_tr_b16 a[236:237], v103 offset:32768\n"
    "s_waitcnt vmcnt(28) lgkmcnt(0)\n"
    "s_barrier\n"
    "s_cbranch_vccz .Lsl28_%=\n"
    "s_sleep 6\n"
    ".Lsl28_%=:\n"
    "s_add_u32 m0, s61, 32768\n"
    "s_add_u32 s62, s63, 36864\n"
    "buffer_load_dwordx4 v5, s[44:47], s62 offen lds\n"
    "s_add_u32 m0, s61, 36864\n"
    "s_add_u32 s62, s63, 46080\n"
    "buffer_load_dwordx4 v5, s[44:47], s62 offen lds\n"
    "s_add_u32 m0, s61, 40960\n"
    "s_add_u32 s62, s63, 55296\n"
    "buffer_load_dwordx4 v5, s[44:47], s62 offen lds\n"
    "s_add_u32 m0, s61, 45056\n"
    "s_add_u32 s62, s63, 64512\n"
    "buffer_load_dwordx4 v5, s[44:47], s62 offen lds\n"
    "ds_read_b64_tr_b16 a[118:119], v96 offset:49152\n"
    "ds_read_b64_tr_b16 a[150:151], v98 offset:49152\n"
    "ds_read_b64_tr_b16 a[182:183], v100 offset:49152\n"
    "ds_read_b64_tr_b16 a[214:215], v102 offset:49152\n"
    "ds_read_b64_tr_b16 a[126:127], v97 offset:49152\n"
    "ds_read_b64_tr_b16 a[158:159], v99 offset:49152\n"
    "ds_read_b64_tr_b16 a[190:191], v101 offset:49152\n"
    "ds_read_b64_tr_b16 a[222:223], v103 offset:49152\n"
    "s_waitcnt vmcnt(28) lgkmcnt(0)\n"
    "s_barrier\n"
    "s_cbranch_vccz .Lsl29_%=\n"
    "s_sleep 6\n"
    ".Lsl29_%=:\n"
    "s_add_u32 m0, s61, 49152\n"
    "s_add_u32 s62, s63, 73728\n"
    "buffer_load_dwordx4 v5, s[44:47], s62 offen lds\n"
    "s_add_u32 m0, s61, 53248\n"
    "s_add_u32 s62, s63, 82944\n"
    "buffer_load_dwordx4 v5, s[44:47], s62 offen lds\n"
    "s_add_u32 m0, s61, 57344\n"
    "s_add_u32 s62, s63, 92160\n"
    "buffer_load_dwordx4 v5, s[44:47], s62 offen lds\n"
    "s_add_u32 m0, s61, 61440\n"
    "s_add_u32 s62, s63, 101376\n"
    "buffer_load_dwordx4 v5, s[44:47], s62 offen lds\n"
    "ds_read_b64_tr_b16 a[134:135], v127\n"
    "ds_read_b64_tr_b16 a[166:167], v126\n"
    "ds_read_b64_tr_b16 a[198:199], v125\n"
    "ds_read_b64_tr_b16 a[230:231], v124\n"
    "ds_read_b64_tr_b16 a[142:143], v123\n"
    "ds_read_b64_tr_b16 a[174:175], v122\n"
    "ds_read_b64_tr_b16 a[206:207], v121\n"
    "ds_read_b64_tr_b16 a[238:239], v120\n"
    "s_waitcnt vmcnt(28) lgkmcnt(0)\n"
    "s_barrier\n"
    "s_cbranch_vccz .Lsl30_%=\n"
    "s_sleep 6\n"
    ".Lsl30_%=:\n"
    "s_add_u32 m0, s61, 65536\n"
    "s_add_u32 s62, s63, 110592\n"
    "buffer_load_dwordx4 v5, s[44:47], s62 offen lds\n"
    "s_add_u32 m0, s61, 69632\n"
    "s_add_u32 s62, s63, 119808\n"
    "buffer_load_dwordx4 v5, s[44:47], s62 offen lds\n"
    "s_add_u32 m0, s61, 73728\n"
    "s_add_u32 s62, s63, 129024\n"
    "buffer_load_dwordx4 v5, s[44:47], s62 offen lds\n"
    "s_add_u32 m0, s61, 77824\n"
    "s_add_u32 s62, s63, 138240\n"
    "buffer_load_dwordx4 v5, s[44:47], s62 offen lds\n"
    "ds_read_b64_tr_b16 a[240:241], v119 offset:16384\n"
    "ds_read_b64_tr_b16 a[242:243], v119 offset:20480\n"
    "ds_read_b64_tr_b16 a[244:245], v119 offset:24576\n"
    "ds_read_b64_tr_b16 a[246:247], v119 offset:28672\n"
    "ds_read_b64_tr_b16 a[248:249], v118 offset:16384\n"
    "ds_read_b64_tr_b16 a[250:251], v118 offset:20480\n"
    "ds_read_b64_tr_b16 a[252:253], v118 offset:24576\n"
    "ds_read_b64_tr_b16 a[254:255], v118 offset:28672\n"
    "s_waitcnt vmcnt(28) lgkmcnt(0)\n"
    "s_barrier\n"
    "ds_read_b64_tr_b16 v[32:33], v127 offset:32768\n"
    "ds_read_b64_tr_b16 v[64:65], v126 offset:32768\n"
    "ds_read_b64_tr_b16 v[144:145], v125 offset:32768\n"
    "ds_read_b64_tr_b16 v[176:177], v124 offset:32768\n"
    "ds_read_b64_tr_b16 v[40:41], v123 offset:32768\n"
    "ds_read_b64_tr_b16 v[72:73], v122 offset:32768\n"
    "ds_read_b64_tr_b16 v[152:153], v121 offset:32768\n"
    "ds_read_b64_tr_b16 v[184:185], v120 offset:32768\n"
    "s_waitcnt vmcnt(24) lgkmcnt(0)\n"
    "s_barrier\n"
    "ds_read_b64_tr_b16 v[48:49], v127 offset:49152\n"
    "ds_read_b64_tr_b16 v[80:81], v126 offset:49152\n"
    "ds_read_b64_tr_b16 v[160:161], v125 offset:49152\n"
    "ds_read_b64_tr_b16 v[192:193], v124 offset:49152\n"
    "ds_read_b64_tr_b16 v[56:57], v123 offset:49152\n"
    "ds_read_b64_tr_b16 v[136:137], v122 offset:49152\n"
    "ds_read_b64_tr_b16 v[168:169], v121 offset:49152\n"
    "ds_read_b64_tr_b16 v[200:201], v120 offset:49152\n"
    "s_waitcnt vmcnt(20) lgkmcnt(0)\n"
    "s_barrier\n"
    "ds_read_b64_tr_b16 v[34:35], v117\n"
    "ds_read_b64_tr_b16 v[66:67], v116\n"
    "ds_read_b64_tr_b16 v[146:147], v115\n"
    "ds_read_b64_tr_b16 v[178:179], v114\n"
    "ds_read_b64_tr_b16 v[42:43], v113\n"
    "ds_read_b64_tr_b16 v[74:75], v112\n"
    "ds_read_b64_tr_b16 v[154:155], v111\n"
    "ds_read_b64_tr_b16 v[186:187], v110\n"
    "s_waitcnt vmcnt(16) lgkmcnt(0)\n"
    "s_barrier\n"
    "ds_read_b64_tr_b16 v[50:51], v117 offset:16384\n"
    "ds_read_b64_tr_b16 v[82:83], v116 offset:16384\n"
    "ds_read_b64_tr_b16 v[162:163], v115 offset:16384\n"
    "ds_read_b64_tr_b16 v[194:195], v114 offset:16384\n"
    "ds_read_b64_tr_b16 v[58:59], v113 offset:16384\n"
    "ds_read_b64_tr_b16 v[138:139], v112 offset:16384\n"
    "ds_read_b64_tr_b16 v[170:171], v111 offset:16384\n"
    "ds_read_b64_tr_b16 v[202:203], v110 offset:16384\n"
    "s_waitcnt vmcnt(12) lgkmcnt(0)\n"
    "s_barrier\n"
    "ds_read_b64_tr_b16 v[88:89], v6 offset:16384\n"
    "ds_read_b64_tr_b16 v[90:91], v6 offset:17408\n"
    "ds_read_b64_tr_b16 v[92:93], v6 offset:20480\n"
    "ds_read_b64_tr_b16 v[94:95], v6 offset:21504\n"
    "ds_read_b64_tr_b16 v[96:97], v6 offset:16416\n"
    "ds_read_b64_tr_b16 v[98:99], v6 offset:17440\n"
    "ds_read_b64_tr_b16 v[100:101], v6 offset:20512\n"
    "ds_read_b64_tr_b16 v[102:103], v6 offset:21536\n"
    "s_waitcnt vmcnt(8) lgkmcnt(0)\n"
    "s_barrier\n"
    "ds_read_b64_tr_b16 v[104:105], v6 offset:32768\n"
    "ds_read_b64_tr_b16 v[106:107], v6 offset:33792\n"
    "ds_read_b64_tr_b16 v[108:109], v6 offset:36864\n"
    "ds_read_b64_tr_b16 v[110:111], v6 offset:37888\n"
    "ds_read_b64_tr_b16 v[112:113], v6 offset:32800\n"
    "ds_read_b64_tr_b16 v[114:115], v6 offset:33824\n"
    "ds_read_b64_tr_b16 v[116:117], v6 offset:36896\n"
    "ds_read_b64_tr_b16 v[118:119], v6 offset:37920\n"
    "s_waitcnt vmcnt(4) lgkmcnt(0)\n"
    "s_barrier\n"
    "ds_read_b64_tr_b16 v[120:121], v6 offset:49152\n"
    "ds_read_b64_tr_b16 v[122:123], v6 offset:50176\n"
    "ds_read_b64_tr_b16 v[124:125], v6 offset:53248\n"
    "ds_read_b64_tr_b16 v[126:127], v6 offset:54272\n"
    "ds_read_b64_tr_b16 v[128:129], v6 offset:49184\n"
    "ds_read_b64_tr_b16 v[130:131], v6 offset:50208\n"
    "ds_read_b64_tr_b16 v[132:133], v6 offset:53280\n"
    "ds_read_b64_tr_b16 v[134:135], v6 offset:54304\n"
    "s_waitcnt vmcnt(0) lgkmcnt(0)\n"
    "s_barrier\n"
    "v_add_u32 v31, 0x10000, v6\n"
    "ds_read_b64_tr_b16 v[12:13], v31\n"
    "ds_read_b64_tr_b16 v[14:15], v31 offset:1024\n"
    "ds_read_b64_tr_b16 v[16:17], v31 offset:4096\n"
    "ds_read_b64_tr_b16 v[18:19], v31 offset:5120\n"
    "ds_read_b64_tr_b16 v[20:21], v31 offset:32\n"
    "ds_read_b64_tr_b16 v[22:23], v31 offset:1056\n"
    "ds_read_b64_tr_b16 v[24:25], v31 offset:4128\n"
    "ds_read_b64_tr_b16 v[26:27], v31 offset:5152\n"
    "s_waitcnt lgkmcnt(0)\n"
    "s_barrier\n"
    "s_xor_b32 s73, %2, 0\n"
    "s_lshl_b32 s73, s73, 12\n"
    "v_add_u32 v6, s73, v2\n"
    "ds_write_b128 v6, v[88:91] offset:14336\n"
    "ds_write_b128 v6, v[92:95] offset:15360\n"
    "ds_write_b128 v6, v[96:99] offset:16384\n"
    "ds_write_b128 v6, v[100:103] offset:17408\n"
    "s_xor_b32 s73, %2, 1\n"
    "s_lshl_b32 s73, s73, 12\n"
    "v_add_u32 v6, s73, v2\n"
    "ds_write_b128 v6, v[104:107] offset:14336\n"
    "ds_write_b128 v6, v[108:111] offset:15360\n"
    "ds_write_b128 v6, v[112:115] offset:16384\n"
    "ds_write_b128 v6, v[116:119] offset:17408\n"
    "s_xor_b32 s73, %2, 2\n"
    "s_lshl_b32 s73, s73, 12\n"
    "v_add_u32 v6, s73, v2\n"
    "ds_write_b128 v6, v[120:123] offset:14336\n"
    "ds_write_b128 v6, v[124:127] offset:15360\n"
    "ds_write_b128 v6, v[128:131] offset:16384\n"
    "ds_write_b128 v6, v[132:135] offset:17408\n"
    "s_xor_b32 s73, %2, 3\n"
    "s_lshl_b32 s73, s73, 12\n"
    "v_add_u32 v6, s73, v2\n"
    "ds_write_b128 v6, v[12:15] offset:14336\n"
    "ds_write_b128 v6, v[16:19] offset:15360\n"
    "ds_write_b128 v6, v[20:23] offset:16384\n"
    "ds_write_b128 v6, v[24:27] offset:17408\n"
    "ds_read_b64_tr_b16 v[88:89], v9\n"
    "ds_read_b64_tr_b16 v[90:91], v9 offset:128\n"
    "ds_read_b64_tr_b16 v[92:93], v9 offset:2048\n"
    "ds_read_b64_tr_b16 v[94:95], v9 offset:2176\n"
    "ds_read_b64_tr_b16 v[96:97], v9 offset:4096\n"
    "ds_read_b64_tr_b16 v[98:99], v9 offset:4224\n"
    "ds_read_b64_tr_b16 v[100:101], v9 offset:6144\n"
    "ds_read_b64_tr_b16 v[102:103], v9 offset:6272\n"
    "ds_write_b128 v2, v[32:35]\n"
    "ds_write_b128 v2, v[36:39] offset:1024\n"
    "ds_write_b128 v2, v[40:43] offset:2048\n"
    "ds_write_b128 v2, v[44:47] offset:3072\n"
    "ds_write_b128 v2, v[48:51] offset:4096\n"
    "ds_write_b128 v2, v[52:55] offset:5120\n"
    "ds_write_b128 v2, v[56:59] offset:6144\n"
    "ds_write_b128 v2, v[60:63] offset:7168\n"
    "ds_write_b128 v2, v[64:67] offset:8192\n"
    "ds_write_b128 v2, v[68:71] offset:9216\n"
    "ds_write_b128 v2, v[72:75] offset:10240\n"
    "ds_write_b128 v2, v[76:79] offset:11264\n"
    "ds_write_b128 v2, v[80:83] offset:12288\n"
    "ds_write_b128 v2, v[84:87] offset:13312\n"
    "v_lshrrev_b32 v112, 4, v0\n"
    "v_and_b32 v113, 15, v112\n"
    "v_lshrrev_b32 v114, 4, v112\n"
    "v_mov_b32 v11, 0\n"
    "v_lshrrev_b32 v115, 1, v113\n"
    "v_bfi_b32 v111, 1, v113, v115\n"
    "v_and_b32 v115, 1, v115\n"
    "v_lshl_add_u32 v115, v114, 1, v115\n"
    "v_mul_u32_u24 v117, 1152, v111\n"
    "v_lshl_add_u32 v117, v115, 4, v117\n"
    "v_and_b32 v115, 1, v114\n"
    "v_lshrrev_b32 v116, 1, v114\n"
    "v_mov_b32 v120, 0\n"
    "v_mov_b32 v121, 0\n"
    "v_mov_b32 v122, 0\n"
    "v_mov_b32 v123, 0\n"
    "ds_write_b128 v11, v[120:123] offset:31872\n"
    "v_mov_b32 v124, 0x3f803f80\n"
    "v_mov_b32 v125, 0x3f803f80\n"
    "v_mov_b32 v126, 0x3f803f80\n"
    "v_mov_b32 v127, 0x3f803f80\n"
    "ds_write_b128 v11, v[124:127] offset:31888\n"
    "v_mov_b32 v128, 0x3f003f00\n"
    "v_mov_b32 v129, 0x3f003f00\n"
    "v_mov_b32 v130, 0x3f003f00\n"
    "v_mov_b32 v131, 0x3f003f00\n"
    "ds_write_b128 v11, v[128:131] offset:31904\n"
    "v_mul_u32_u24 v118, 57, v112\n"
    "v_lshrrev_b32 v118, 9, v118\n"
    "v_mul_u32_u24 v118, 9, v118\n"
    "v_cmp_eq_u32 vcc, v118, v112\n"
    "v_lshlrev_b32 v118, 1, v112\n"
    "s_nop 0\n"
    "v_cndmask_b32 v120, v11, v124, vcc\n"
    "ds_write_b16 v118, v120 offset:31936\n"
    "s_waitcnt lgkmcnt(0)\n"
    "s_cmp_lt_u32 %2, 2\n"
    "s_cbranch_scc1 .Lsk_%=\n"
    "ds_write_b128 v2, v[88:91] offset:30720\n"
    "ds_write_b128 v2, v[92:95] offset:31744\n"
    "ds_write_b128 v2, v[96:99] offset:32768\n"
    "ds_write_b128 v2, v[100:103] offset:33792\n"
    ".Lsk_%=:\n"
    "s_cmp_eq_u32 %2, 3\n"
    "s_cbranch_scc1 .Lsm_%=\n"
    "s_mov_b64 s[44:45], %12\n"
    "s_and_b32 s45, s45, 0xffff\n"
    "s_mov_b32 s46, 0x2400\n"
    "s_mov_b32 s47, 0x20000\n"
    "s_mul_i32 s62, %2, 384\n"
    "s_mul_i32 s61, %2, 3072\n"
    "s_add_u32 m0, s61, 20480\n"
    "s_add_u32 s73, s62, 0\n"
    "buffer_load_dwordx4 v117, s[44:47], s73 offen lds\n"
    "s_add_u32 m0, s61, 21504\n"
    "s_add_u32 s73, s62, 128\n"
    "buffer_load_dwordx4 v117, s[44:47], s73 offen lds\n"
    "s_add_u32 m0, s61, 22528\n"
    "s_add_u32 s73, s62, 256\n"
    "buffer_load_dwordx4 v117, s[44:47], s73 offen lds\n"
    ".Lsm_%=:\n"
    "v_mov_b32 v130, 0x20400\n"
    "global_load_dwordx2 v[132:133], v130, %1 sc1\n"
    "v_mbcnt_lo_u32_b32 v131, -1, 0\n"
    "v_min_u32 v131, 7, v131\n"
    "v_lshlrev_b32 v131, 7, v131\n"
    "v_add_u32 v131, 0x20000, v131\n"
    "global_load_dwordx2 v[134:135], v131, %1 sc1\n"
    "s_waitcnt vmcnt(0) lgkmcnt(0)\n"
    "s_barrier\n"
    "v_mov_b32 v1, 0x4444\n"
    "v_mov_b32 v118, 0xeeee\n"
    "v_and_b32 v4, 2, v113\n"
    "v_cmp_eq_u32 vcc, 0, v4\n"
    "s_nop 1\n"
    "v_cndmask_b32 v1, v118, v1, vcc\n"
    "v_xor_b32 v118, 7, v111\n"
    "v_cndmask_b32 v4, v118, v111, vcc\n"
    "s_lshl_b32 s73, %2, 10\n"
    "v_add_u32 v5, s73, v0\n"
    "v_and_b32 v119, 7, v113\n"
    "v_lshrrev_b32 v120, 3, v113\n"
    "v_mul_u32_u24 v3, 544, v114\n"
    "v_lshl_add_u32 v3, v113, 2, v3\n"
    "s_lshl_b32 s73, %2, 6\n"
    "s_add_u32 s73, s73, 20480\n"
    "v_add_u32 v3, s73, v3\n"
    "v_mul_u32_u24 v4, 272, v4\n"
    "v_lshl_add_u32 v4, v114, 4, v4\n"
    "v_and_b32 v6, 13, v113\n"
    "v_lshl_add_u32 v6, v116, 4, v6\n"
    "v_lshl_add_u32 v6, v115, 1, v6\n"
    "s_lshl_b32 s73, %2, 9\n"
    "v_lshl_add_u32 v6, v6, 4, s73\n"
    "s_waitcnt vmcnt(0)\n"
    "v_add_u32 v132, 1, v132\n"
    "v_add_u32 v133, 1, v133\n"
    "s_mov_b32 s56, 1048576\n"
    ".Lcgo_a%=:\n"
    "v_cmp_ne_u64 vcc, v[132:133], v[134:135]\n"
    "s_cbranch_vccz .Lcgo_x%=\n"
    "global_load_dwordx2 v[134:135], v131, %1 sc1\n"
    "s_sub_u32 s56, s56, 1\n"
    "s_waitcnt vmcnt(0)\n"
    "s_cbranch_scc0 .Lcgo_a%=\n"
    ".Lcgo_x%=:\n"
    "s_mov_b32 s56, 0\n"
    "v_mov_b32 v134, 64\n"
    ".Lpoll1_%=:\n"
    "global_load_dword v135, v134, %1 sc1\n"
    "s_waitcnt vmcnt(0)\n"
    "v_readfirstlane_b32 s73, v135\n"
    "s_cmp_ge_u32 s73, 4\n"
    "s_cbranch_scc1 .Lpoll1_x%=\n"
    "s_add_u32 s56, s56, 1\n"
    "s_cmp_lt_u32 s56, 3\n"
    "s_cbranch_scc1 .Lpoll1_%=\n"
    "s_sleep 8\n"
    "s_cmp_lt_u32 s56, s57\n"
    "s_cbranch_scc1 .Lpoll1_%=\n"
    "s_mov_b32 s57, 0\n"
    "v_mov_b32 v135, 1\n"
    "global_store_dword v11, v135, %1 sc1\n"
    ".Lpoll1_x%=:\n"
    "s_xor_b32 s69, s73, %7\n"
    "s_lshr_b32 s51, s73, 1\n"
    "s_xor_b32 s51, s51, %7\n"
    "s_and_b32 s51, s51, 1\n"
    "s_lshl_b32 s51, s51, 13\n"
    "s_add_u32 s68, s51, 0x200000\n"
    "s_and_b32 s60, s69, 1\n"
    "s_lshl_b32 s60, s60, 13\n"
    "v_lshl_add_u32 v8, v116, 2, v115\n"
    "v_lshlrev_b32 v8, 12, v8\n"
    "v_lshl_add_u32 v8, v119, 4, v8\n"
    "v_lshl_add_u32 v7, v120, 15, v8\n"
    "v_lshl_add_u32 v8, v120, 30, v8\n"
    "s_mul_i32 s73, %5, 294912\n"
    "s_lshl_b32 s51, %4, 7\n"
    "s_add_u32 s73, s73, s51\n"
    "s_add_u32 s73, s73, s60\n"
    "s_lshl_b32 s51, %2, 16\n"
    "s_add_u32 s51, s51, s73\n"
    "v_add_u32 v7, s51, v7\n"
    "s_lshl3_add_u32 s51, %2, s73\n"
    "s_add_u32 s51, s51, 262128\n"
    "s_cmp_lt_u32 %2, 2\n"
    "s_cselect_b32 s51, 0x80000000, s51\n"
    "v_add_u32 v8, s51, v8\n"
    "s_lshl_b32 s61, %4, 1\n"
    "s_add_u32 s61, s61, %5\n"
    "s_lshl4_add_u32 s44, s61, 1024\n"
    "s_mul_i32 s61, %5, 36\n"
    "s_lshr_b32 s73, %4, 3\n"
    "s_add_u32 s73, s73, s61\n"
    "s_add_u32 s73, s73, 32\n"
    "s_add_u32 s61, s61, %4\n"
    "s_and_b32 s51, %4, 7\n"
    "s_add_u32 s51, s51, %2\n"
    "s_cmp_eq_u32 s51, 1\n"
    "s_cselect_b32 s71, s73, -1\n"
    "s_cmp_eq_u32 %2, 0\n"
    "s_cselect_b32 s71, s61, s71\n"
    "s_max_i32 s61, s71, 0\n"
    "s_lshr_b32 s73, s61, 1\n"
    "s_lshl_b32 s73, s73, 14\n"
    "s_and_b32 s51, s61, 1\n"
    "s_lshl_b32 s51, s51, 12\n"
    "s_add_u32 s65, s73, s51\n"
    "s_add_u32 s65, s65, s60\n"
    "s_lshr_b32 s94, s61, 5\n"
    "s_and_b32 s73, s61, 31\n"
    "s_lshl_b32 s73, s73, 7\n"
    "v_lshl_add_u32 v9, v114, 2, s73\n"
    "v_lshl_add_u32 v9, v119, 4, v9\n"
    "v_lshl_add_u32 v9, v120, 22, v9\n"
    "s_lshl_b32 s73, %2, 11\n"
    "v_add_u32 v10, s73, v0\n"
    "s_cmp_ge_u32 %2, 2\n"
    "s_cbranch_scc0 .Lnw3_%=\n"
    "v_lshl_add_u32 v9, v113, 1, %5\n"
    "v_lshlrev_b32 v118, 3, v114\n"
    "v_sub_u32 v9, v9, v118\n"
    "v_cmp_le_i32 s[76:77], 0, v9\n"
    "v_cmp_le_i32 s[78:79], 2, v9\n"
    "v_cmp_le_i32 s[80:81], 4, v9\n"
    "v_cmp_le_i32 s[82:83], 6, v9\n"
    "v_cmp_le_i32 s[84:85], 1, v9\n"
    "v_cmp_le_i32 s[86:87], 3, v9\n"
    "v_cmp_le_i32 s[88:89], 5, v9\n"
    "v_cmp_le_i32 s[90:91], 7, v9\n"
    ".Lnw3_%=:\n"
    "s_lshr_b32 s73, %4, 2\n"
    "s_lshl1_add_u32 s73, s73, %7\n"
    "s_and_b32 s51, %4, 3\n"
    "s_lshl3_add_u32 s73, s73, s51\n"
    "s_lshl_b32 s73, s73, 8\n"
    "s_add_u32 s92, s73, 86016\n"
    "s_lshr_b32 s51, %4, 3\n"
    "s_lshl2_add_u32 s73, %5, s51\n"
    "s_lshl1_add_u32 s73, s73, %7\n"
    "s_and_b32 s51, %4, 7\n"
    "s_lshl4_add_u32 s73, s73, s51\n"
    "s_lshl_b32 s73, s73, 7\n"
    "s_add_u32 s93, s73, 53248\n"
    "s_lshl_b32 s73, %5, 12\n"
    "s_lshl_b32 s51, %4, 14\n"
    "s_add_u32 s73, s73, s51\n"
    "s_add_u32 s75, s73, 0x9fff000\n"
    "s_brev_b32 s45, 1\n"
    "s_mov_b32 s46, 0x3f803f80\n"
    "s_mov_b32 s47, s46\n"
    "s_mov_b32 s48, 0\n"
    "s_mov_b32 s54, 56\n"
    "s_mov_b32 s55, 80\n"
    "s_mov_b32 s96, 736\n"
    "s_mov_b32 s56, 0\n"
    "v_mov_b32 v134, 128\n"
    ".Lpoll2_%=:\n"
    "global_load_dword v135, v134, %1 sc1\n"
    "s_waitcnt vmcnt(0)\n"
    "v_readfirstlane_b32 s73, v135\n"
    "s_cmp_ge_u32 s73, 64\n"
    "s_cbranch_scc1 .Lpoll2_x%=\n"
    "s_add_u32 s56, s56, 1\n"
    "s_cmp_lt_u32 s56, 3\n"
    "s_cbranch_scc1 .Lpoll2_%=\n"
    "s_sleep 8\n"
    "s_cmp_lt_u32 s56, s57\n"
    "s_cbranch_scc1 .Lpoll2_%=\n"
    "s_mov_b32 s57, 0\n"
    "v_mov_b32 v135, 1\n"
    "global_store_dword v11, v135, %1 sc1\n"
    ".Lpoll2_x%=:\n"
    "ds_read_b128 v[32:35], v2\n"
    "ds_read_b128 v[36:39], v2 offset:1024\n"
    "ds_read_b128 v[40:43], v2 offset:2048\n"
    "ds_read_b128 v[44:47], v2 offset:3072\n"
    "ds_read_b128 v[48:51], v2 offset:4096\n"
    "ds_read_b128 v[52:55], v2 offset:5120\n"
    "ds_read_b128 v[56:59], v2 offset:6144\n"
    "ds_read_b128 v[60:63], v2 offset:7168\n"
    "ds_read_b128 v[64:67], v2 offset:8192\n"
    "ds_read_b128 v[68:71], v2 offset:9216\n"
    "ds_read_b128 v[72:75], v2 offset:10240\n"
    "ds_read_b128 v[76:79], v2 offset:11264\n"
    "ds_read_b128 v[80:83], v2 offset:12288\n"
    "ds_read_b128 v[84:87], v2 offset:13312\n"
    "v_mov_b64 v[124:125], 0\n"
    "v_mov_b64 v[126:127], 0\n"
    "v_mov_b64 v[128:129], 0\n"
    "v_mov_b64 v[130:131], 0\n"
    "s_add_u32 s73, s48, 0\n"
    "s_and_b32 s73, s73, 63\n"
    "s_lshr_b32 s51, s73, 2\n"
    "s_xor_b32 s51, s51, s69\n"
    "s_and_b32 s51, s51, 1\n"
    "s_lshl_b32 s51, s51, 13\n"
    "s_lshl_b32 s73, s73, 5\n"
    "s_add_u32 s73, s73, %4\n"
    "s_lshl_b32 s73, s73, 14\n"
    "s_add_u32 s73, s73, s51\n"
    "s_add_u32 s60, s73, 0x6000000\n"
    "s_add_u32 s61, s73, 0x6001000\n"
    "s_add_u32 s62, s73, 0x8000000\n"
    "s_add_u32 s63, s73, 0x8001000\n"
    "s_lshl_b32 s51, %5, 12\n"
    "s_add_u32 s73, s73, s51\n"
    "s_add_u32 s64, s73, 0x4000000\n"
    "s_mul_i32 s73, s48, 3\n"
    "s_add_u32 s73, s73, 2\n"
    "s_and_b32 s51, s73, 1\n"
    "s_lshl_b32 s51, s51, 12\n"
    "s_lshr_b32 s73, s73, 1\n"
    "s_lshl_b32 s73, s73, 14\n"
    "s_add_u32 s73, s73, s51\n"
    "s_add_u32 s58, s73, s68\n"
    "s_waitcnt lgkmcnt(0)\n"
    "s_mov_b32 s56, 0\n"
    "v_mov_b32 v134, s92\n"
    ".Lpoll3_%=:\n"
    "global_load_dword v132, v134, %1 sc1\n"
    "global_load_dword v133, v134, %1 offset:128 sc1\n"
    "s_waitcnt vmcnt(0)\n"
    "v_and_b32 v133, v132, v133\n"
    "s_nop 0\n"
    "v_readfirstlane_b32 s73, v133\n"
    "s_cmp_ge_u32 s73, 1\n"
    "s_cbranch_scc1 .Lpoll3_x%=\n"
    "s_add_u32 s56, s56, 1\n"
    "s_cmp_lt_u32 s56, 3\n"
    "s_cbranch_scc1 .Lpoll3_%=\n"
    "s_sleep 8\n"
    "s_cmp_lt_u32 s56, s57\n"
    "s_cbranch_scc1 .Lpoll3_%=\n"
    "s_mov_b32 s57, 0\n"
    "v_mov_b32 v135, 1\n"
    "global_store_dword v11, v135, %1 sc1\n"
    ".Lpoll3_x%=:\n"
    "buffer_load_dwordx4 v[12:15], v5, s[40:43], s60 offen sc1\n"
    "buffer_load_dwordx4 v[16:19], v5, s[40:43], s61 offen sc1\n"
    "buffer_load_dwordx4 v[20:23], v5, s[40:43], s62 offen sc1\n"
    "buffer_load_dwordx4 v[24:27], v5, s[40:43], s63 offen sc1\n"
    "buffer_load_dwordx4 v[28:31], v5, s[40:43], s64 offen sc1\n"
    "v_xor_b32 v132, 0x800, v10\n"
    "v_xor_b32 v133, 0x1000, v10\n"
    "v_xor_b32 v134, 0x1800, v10\n"
    "v_lshrrev_b32 v135, 2, v5\n"
    "s_movk_i32 s73, 0x180\n"
    "v_lshlrev_b32 v120, 1, v0\n"
    "v_bfi_b32 v120, s73, v120, v0\n"
    "v_lshrrev_b32 v122, 2, v0\n"
    "v_bfi_b32 v120, 64, v122, v120\n"
    "v_lshlrev_b32 v121, 1, v135\n"
    "v_bfi_b32 v121, s73, v121, v135\n"
    "v_lshrrev_b32 v122, 2, v135\n"
    "v_bfi_b32 v121, 64, v122, v121\n"
    "ds_read_b128 v[88:91], v10 offset:20480\n"
    "ds_read_b128 v[92:95], v10 offset:21504\n"
    "ds_read_b32 v96, v135 offset:28672\n"
    "s_add_u32 s73, s48, 1\n"
    "s_and_b32 s73, s73, 63\n"
    "s_lshr_b32 s51, s73, 2\n"
    "s_xor_b32 s51, s51, s69\n"
    "s_and_b32 s51, s51, 1\n"
    "s_lshl_b32 s51, s51, 13\n"
    "s_lshl_b32 s73, s73, 5\n"
    "s_add_u32 s73, s73, %4\n"
    "s_lshl_b32 s73, s73, 14\n"
    "s_add_u32 s73, s73, s51\n"
    "s_add_u32 s60, s73, 0x6000000\n"
    "s_add_u32 s61, s73, 0x6001000\n"
    "s_add_u32 s62, s73, 0x8000000\n"
    "s_add_u32 s63, s73, 0x8001000\n"
    "s_lshl_b32 s51, %5, 12\n"
    "s_add_u32 s73, s73, s51\n"
    "s_add_u32 s64, s73, 0x4000000\n"
    "s_add_u32 s73, s48, 1\n"
    "s_and_b32 s51, s73, 32\n"
    "s_and_b32 s73, s73, 31\n"
    "s_lshl3_add_u32 s73, s51, s73\n"
    "s_lshl2_add_u32 s73, s73, s92\n"
    "s_waitcnt lgkmcnt(2)\n"
    "v_min_u32 v88, -2, v88\n"
    "v_min_u32 v89, -2, v89\n"
    "v_min_u32 v90, -2, v90\n"
    "v_min_u32 v91, -2, v91\n"
    "s_waitcnt lgkmcnt(1)\n"
    "v_min_u32 v92, -2, v92\n"
    "v_min_u32 v93, -2, v93\n"
    "v_min_u32 v94, -2, v94\n"
    "v_min_u32 v95, -2, v95\n"
    "s_waitcnt lgkmcnt(0)\n"
    "v_min_u32 v96, -2, v96\n"
    "s_waitcnt vmcnt(4)\n"
    "ds_write_b128 v5, v[12:15]\n"
    "s_waitcnt vmcnt(3)\n"
    "ds_write_b128 v5, v[16:19] offset:4096\n"
    "s_waitcnt vmcnt(0)\n"
    "ds_write_b128 v5, v[28:31] offset:16384\n"
    "v_swap_b32 v22, v24\n"
    "v_swap_b32 v23, v25\n"
    "ds_write_b128 v5, v[20:23] offset:8192\n"
    "ds_write_b128 v5, v[24:27] offset:12288\n"
    "v_mov_b32 v14, s73\n"
    "global_load_dword v12, v14, %1 sc1\n"
    "global_load_dword v13, v14, %1 offset:128 sc1\n"
    "global_load_dword v14, v14, %1 sc1\n"
    "v_mov_b64 v[16:17], 0\n"
    "v_mov_b64 v[18:19], 0\n"
    "v_mov_b64 v[20:21], 0\n"
    "v_mov_b64 v[22:23], 0\n"
    "v_mov_b64 v[24:25], 0\n"
    "v_mov_b64 v[26:27], 0\n"
    "v_mov_b64 v[28:29], 0\n"
    "v_mov_b64 v[30:31], 0\n"
    ".Lstep_%=:\n"
    "s_waitcnt vmcnt(5)\n"
    "v_smfmac_f32_16x16x64_bf16 v[16:19], v[88:91], v[32:39], v1\n"
    "ds_write_b128 v10, v[88:91] offset:20480\n"
    "v_smfmac_f32_16x16x64_bf16 v[20:23], v[88:91], v[40:47], v1\n"
    "s_waitcnt vmcnt(4)\n"
    "ds_write_b128 v10, v[92:95] offset:21504\n"
    "v_max3_u32 v100, v88, v89, v90\n"
    "v_smfmac_f32_16x16x64_bf16 v[16:19], v[92:95], v[48:55], v1\n"
    "s_waitcnt vmcnt(3)\n"
    "ds_write_b32 v135, v96 offset:28672\n"
    "v_max3_u32 v100, v100, v91, v92\n"
    "v_smfmac_f32_16x16x64_bf16 v[20:23], v[92:95], v[56:63], v1\n"
    "v_max3_u32 v100, v100, v93, v94\n"
    "v_smfmac_f32_16x16x64_bf16 v[24:27], v[88:91], v[224:231], v1\n"
    "v_max3_u32 v100, v100, v95, v96\n"
    "v_smfmac_f32_16x16x64_bf16 v[28:31], v[88:91], v[232:239], v1\n"
    "v_cmp_eq_u32 vcc, -1, v100\n"
    "v_smfmac_f32_16x16x64_bf16 v[24:27], v[92:95], v[240:247], v1\n"
    "s_cbranch_vccnz .Lgf_%=\n"
    ".Lgx_%=:\n"
    "s_waitcnt lgkmcnt(0)\n"
    "s_barrier\n"
    "s_memrealtime s[52:53]\n"
    "ds_read_b128 v[96:99], v132 offset:20480\n"
    "v_smfmac_f32_16x16x64_bf16 v[28:31], v[92:95], v[248:255], v1\n"
    "ds_read_b128 v[100:103], v132 offset:21504\n"
    "v_smfmac_f32_16x16x64_bf16 v[124:127], v[88:91], a[112:119], v1\n"
    "ds_read_b128 v[104:107], v133 offset:20480\n"
    "v_smfmac_f32_16x16x64_bf16 v[128:131], v[88:91], a[120:127], v1\n"
    "ds_read_b128 v[108:111], v133 offset:21504\n"
    "v_smfmac_f32_16x16x64_bf16 v[124:127], v[92:95], a[128:135], v1\n"
    "ds_read_b128 v[112:115], v134 offset:20480\n"
    "v_smfmac_f32_16x16x64_bf16 v[128:131], v[92:95], a[136:143], v1\n"
    "ds_read_b128 v[116:119], v134 offset:21504\n"
    "s_waitcnt lgkmcnt(5)\n"
    "v_smfmac_f32_16x16x64_bf16 v[16:19], v[96:99], v[64:71], v1\n"
    "ds_read_b128 v[120:123], v0 offset:28672\n"
    "v_smfmac_f32_16x16x64_bf16 v[20:23], v[96:99], v[72:79], v1\n"
    "s_waitcnt lgkmcnt(5)\n"
    "v_smfmac_f32_16x16x64_bf16 v[16:19], v[100:103], v[80:87], v1\n"
    "v_cmp_eq_u32 vcc, s44, v5\n"
    "v_smfmac_f32_16x16x64_bf16 v[20:23], v[100:103], v[136:143], v1\n"
    "v_mov_b32 v79, s45\n"
    "s_waitcnt lgkmcnt(4)\n"
    "v_smfmac_f32_16x16x64_bf16 v[16:19], v[104:107], v[144:151], v1\n"
    "v_cndmask_b32 v79, v79, v0, vcc\n"
    "v_smfmac_f32_16x16x64_bf16 v[20:23], v[104:107], v[152:159], v1\n"
    "s_lshr_b32 s73, s96, 3\n"
    "s_waitcnt lgkmcnt(3)\n"
    "v_smfmac_f32_16x16x64_bf16 v[16:19], v[108:111], v[160:167], v1\n"
    "s_waitcnt lgkmcnt(0)\n"
    "s_add_u32 s73, s73, s52\n"
    "v_and_b32 v78, -2, s73\n"
    "v_smfmac_f32_16x16x64_bf16 v[20:23], v[108:111], v[168:175], v1\n"
    "buffer_store_dword v78, v79, s[40:43], s58 offen offset:3072 sc1\n"
    "s_waitcnt vmcnt(2)\n"
    "v_smfmac_f32_16x16x64_bf16 v[16:19], v[112:115], v[176:183], v1\n"
    "ds_read_b128 v[40:43], v0\n"
    "v_smfmac_f32_16x16x64_bf16 v[20:23], v[112:115], v[184:191], v1\n"
    "ds_read_b128 v[44:47], v0 offset:4096\n"
    "v_and_b32 v12, v12, v13\n"
    "v_smfmac_f32_16x16x64_bf16 v[16:19], v[116:119], v[192:199], v1\n"
    "ds_read_b128 v[48:51], v0 offset:1024\n"
    "v_add_u32 v13, -1, v12\n"
    "v_smfmac_f32_16x16x64_bf16 v[20:23], v[116:119], v[200:207], v1\n"
    "ds_read_b128 v[52:55], v0 offset:5120\n"
    "v_and_or_b32 v15, v13, s45, v5\n"
    "v_smfmac_f32_16x16x64_bf16 v[16:19], v[120:123], v[208:215], v1\n"
    "ds_read_b128 v[56:59], v0 offset:2048\n"
    "v_readfirstlane_b32 s59, v12\n"
    "v_smfmac_f32_16x16x64_bf16 v[20:23], v[120:123], v[216:223], v1\n"
    "ds_read_b128 v[60:63], v0 offset:6144\n"
    "v_smfmac_f32_16x16x64_bf16 v[24:27], v[96:99], a[0:7], v1\n"
    "ds_read_b128 v[64:67], v0 offset:3072\n"
    "v_add_f32 v16, v16, v18\n"
    "v_smfmac_f32_16x16x64_bf16 v[28:31], v[96:99], a[8:15], v1\n"
    "ds_read_b128 v[68:71], v0 offset:7168\n"
    "v_add_f32 v17, v17, v19\n"
    "v_smfmac_f32_16x16x64_bf16 v[24:27], v[100:103], a[16:23], v1\n"
    "ds_read_b128 v[88:91], v2 offset:14336\n"
    "v_add_f32 v20, v20, v22\n"
    "v_smfmac_f32_16x16x64_bf16 v[28:31], v[100:103], a[24:31], v1\n"
    "ds_read_b128 v[92:95], v2 offset:15360\n"
    "v_add_f32 v21, v21, v23\n"
    "v_smfmac_f32_16x16x64_bf16 v[24:27], v[104:107], a[32:39], v1\n"
    "v_cvt_pk_bf16_f32 v76, v16, v20\n"
    "v_smfmac_f32_16x16x64_bf16 v[28:31], v[104:107], a[40:47], v1\n"
    "v_cvt_pk_bf16_f32 v77, v17, v21\n"
    "v_smfmac_f32_16x16x64_bf16 v[24:27], v[108:111], a[48:55], v1\n"
    "ds_write_b32 v3, v76 offset:9216\n"
    "v_smfmac_f32_16x16x64_bf16 v[28:31], v[108:111], a[56:63], v1\n"
    "ds_write_b32 v3, v77 offset:9488\n"
    "v_smfmac_f32_16x16x64_bf16 v[24:27], v[112:115], a[64:71], v1\n"
    "buffer_load_dwordx4 v[16:19], v15, s[40:43], s61 offen sc1\n"
    "v_smfmac_f32_16x16x64_bf16 v[28:31], v[112:115], a[72:79], v1\n"
    "v_smfmac_f32_16x16x64_bf16 v[24:27], v[116:119], a[80:87], v1\n"
    "v_smfmac_f32_16x16x64_bf16 v[28:31], v[116:119], a[88:95], v1\n"
    "s_waitcnt lgkmcnt(0)\n"
    "s_barrier\n"
    "ds_read_b128 v[32:35], v4 offset:29696\n"
    "v_smfmac_f32_16x16x64_bf16 v[24:27], v[120:123], a[96:103], v1\n"
    "ds_read_b128 v[36:39], v4 offset:29760\n"
    "v_smfmac_f32_16x16x64_bf16 v[28:31], v[120:123], a[104:111], v1\n"
    "ds_read_b128 v[132:135], v4 offset:29824\n"
    "v_smfmac_f32_16x16x64_bf16 v[124:127], v[96:99], a[144:151], v1\n"
    "ds_read_b128 v[72:75], v4 offset:29888\n"
    "v_add_f32 v24, v24, v26\n"
    "v_add_f32 v25, v25, v27\n"
    "v_smfmac_f32_16x16x64_bf16 v[128:131], v[96:99], a[152:159], v1\n"
    "buffer_load_dwordx4 v[20:23], v15, s[40:43], s62 offen sc1\n"
    "v_add_f32 v28, v28, v30\n"
    "v_add_f32 v29, v29, v31\n"
    "v_smfmac_f32_16x16x64_bf16 v[124:127], v[100:103], a[160:167], v1\n"
    "v_cvt_pk_bf16_f32 v76, v24, v28\n"
    "v_cvt_pk_bf16_f32 v77, v25, v29\n"
    "s_waitcnt lgkmcnt(3)\n"
    "v_mfma_f32_16x16x32_bf16 v[80:83], v[40:43], v[32:35], 0\n"
    "ds_write2_b32 v3, v76, v77 offset1:68\n"
    "v_mfma_f32_16x16x32_bf16 v[84:87], v[44:47], v[32:35], 0\n"
    "s_add_u32 s73, s48, 1\n"
    "s_and_b32 s51, s73, 32\n"
    "s_and_b32 s73, s73, 31\n"
    "s_waitcnt lgkmcnt(3)\n"
    "v_mfma_f32_16x16x32_bf16 v[80:83], v[48:51], v[36:39], v[80:83]\n"
    "buffer_load_dwordx4 v[24:27], v15, s[40:43], s63 offen sc1\n"
    "v_mfma_f32_16x16x32_bf16 v[84:87], v[52:55], v[36:39], v[84:87]\n"
    "s_lshl3_add_u32 s73, s51, s73\n"
    "s_lshl2_add_u32 s73, s73, s92\n"
    "s_waitcnt lgkmcnt(2)\n"
    "v_mfma_f32_16x16x32_bf16 v[80:83], v[56:59], v[132:135], v[80:83]\n"
    "s_mov_b32 vcc_lo, 0xcccccccc\n"
    "s_mov_b32 vcc_hi, 0xcccccccc\n"
    "v_mfma_f32_16x16x32_bf16 v[84:87], v[60:63], v[132:135], v[84:87]\n"
    "s_waitcnt lgkmcnt(0)\n"
    "s_barrier\n"
    "ds_read_b128 v[40:43], v4 offset:20480\n"
    "v_mfma_f32_16x16x32_bf16 v[80:83], v[64:67], v[72:75], v[80:83]\n"
    "ds_read_b128 v[44:47], v4 offset:20544\n"
    "v_mfma_f32_16x16x32_bf16 v[84:87], v[68:71], v[72:75], v[84:87]\n"
    "ds_read_b128 v[48:51], v4 offset:20608\n"
    "v_smfmac_f32_16x16x64_bf16 v[128:131], v[100:103], a[168:175], v1\n"
    "ds_read_b128 v[52:55], v4 offset:20672\n"
    "v_smfmac_f32_16x16x64_bf16 v[124:127], v[104:107], a[176:183], v1\n"
    "ds_read_b128 v[56:59], v5 offset:8192\n"
    "v_cndmask_b32 v80, v80, v84, vcc\n"
    "v_smfmac_f32_16x16x64_bf16 v[128:131], v[104:107], a[184:191], v1\n"
    "ds_read_b128 v[60:63], v5 offset:12288\n"
    "v_cndmask_b32 v81, v81, v85, vcc\n"
    "s_waitcnt lgkmcnt(5)\n"
    "v_mfma_f32_16x16x32_bf16 v[64:67], v[40:43], v[32:35], 0\n"
    "v_cndmask_b32 v82, v82, v86, vcc\n"
    "v_cndmask_b32 v83, v83, v87, vcc\n"
    "s_waitcnt lgkmcnt(4)\n"
    "v_mfma_f32_16x16x32_bf16 v[64:67], v[44:47], v[36:39], v[64:67]\n"
    "ds_read_b128 v[96:99], v2 offset:16384\n"
    "v_maximum3_f32 v78, v80, v81, v82\n"
    "v_maximum3_f32 v78, v78, v83, v83\n"
    "s_waitcnt lgkmcnt(4)\n"
    "v_mfma_f32_16x16x32_bf16 v[64:67], v[48:51], v[132:135], v[64:67]\n"
    "ds_read_b128 v[100:103], v2 offset:17408\n"
    "v_max_f32_dpp v78, v78, v78 row_mirror row_mask:0xf bank_mask:0xf\n"
    "s_waitcnt lgkmcnt(4)\n"
    "v_mfma_f32_16x16x32_bf16 v[64:67], v[52:55], v[72:75], v[64:67]\n"
    "buffer_load_dwordx4 v[28:31], v15, s[40:43], s64 offen sc1\n"
    "v_mov_b32 v77, v78\n"
    "v_smfmac_f32_16x16x64_bf16 v[124:127], v[108:111], a[192:199], v1\n"
    "ds_read_b128 v[40:43], v2 offset:18432\n"
    "v_permlane32_swap_b32 v77, v78\n"
    "v_smfmac_f32_16x16x64_bf16 v[128:131], v[108:111], a[200:207], v1\n"
    "ds_read_b128 v[44:47], v2 offset:19456\n"
    "v_maximum3_f32 v78, v78, v77, v64\n"
    "v_maximum3_f32 v78, v78, v65, v66\n"
    "v_max_f32 v77, v78, v67\n"
    "v_max_f32 v78, v78, v67\n"
    "v_smfmac_f32_16x16x64_bf16 v[124:127], v[112:115], a[208:215], v1\n"
    "ds_read_b128 v[48:51], v2 offset:22528\n"
    "v_permlane16_swap_b32 v77, v78\n"
    "v_max_f32 v78, v78, v77\n"
    "v_mul_f32 v78, 0xbe0293ee, v78\n"
    "v_smfmac_f32_16x16x64_bf16 v[128:131], v[112:115], a[216:223], v1\n"
    "ds_read_b128 v[52:55], v2 offset:23552\n"
    "v_fmamk_f32 v80, v80, 0x3e0293ee, v78\n"
    "v_fmamk_f32 v81, v81, 0x3e0293ee, v78\n"
    "v_exp_f32 v80, v80\n"
    "v_exp_f32 v81, v81\n"
    "v_smfmac_f32_16x16x64_bf16 v[124:127], v[116:119], a[224:231], v1\n"
    "ds_read_b128 v[32:35], v2 offset:24576\n"
    "v_fmamk_f32 v82, v82, 0x3e0293ee, v78\n"
    "v_fmamk_f32 v83, v83, 0x3e0293ee, v78\n"
    "v_exp_f32 v82, v82\n"
    "v_exp_f32 v83, v83\n"
    "v_cvt_pk_bf16_f32 v80, v80, v81\n"
    "v_smfmac_f32_16x16x64_bf16 v[128:131], v[116:119], a[232:239], v1\n"
    "ds_read_b128 v[36:39], v2 offset:25600\n"
    "v_fmamk_f32 v64, v64, 0x3e0293ee, v78\n"
    "v_fmamk_f32 v65, v65, 0x3e0293ee, v78\n"
    "v_exp_f32 v64, v64\n"
    "v_exp_f32 v65, v65\n"
    "v_cvt_pk_bf16_f32 v81, v82, v83\n"
    "v_smfmac_f32_16x16x64_bf16 v[124:127], v[120:123], a[240:247], v1\n"
    "ds_read_b128 v[104:107], v2 offset:26624\n"
    "v_cvt_pk_bf16_f32 v66, v64, v65\n"
    "v_mov_b32_dpp v82, v80 row_mirror row_mask:0xf bank_mask:0xf\n"
    "v_smfmac_f32_16x16x64_bf16 v[128:131], v[120:123], a[248:255], v1\n"
    "ds_read_b128 v[108:111], v2 offset:27648\n"
    "v_mov_b32_dpp v83, v81 row_mirror row_mask:0xf bank_mask:0xf\n"
    "v_cvt_pk_bf16_f32 v67, v64, v65\n"
    "s_waitcnt lgkmcnt(11)\n"
    "v_mfma_f32_16x16x32_bf16 v[132:135], v[56:59], v[80:83], 0\n"
    "ds_read_b128 v[112:115], v2 offset:20480\n"
    "v_cvt_pk_bf16_f32 v124, v124, v125\n"
    "v_cvt_pk_bf16_f32 v125, v126, v127\n"
    "s_waitcnt lgkmcnt(11)\n"
    "v_mfma_f32_16x16x32_bf16 v[120:123], v[60:63], v[80:83], 0\n"
    "ds_read_b128 v[116:119], v2 offset:21504\n"
    "v_cvt_pk_bf16_f32 v126, v128, v129\n"
    "v_cvt_pk_bf16_f32 v127, v130, v131\n"
    "v_mfma_f32_16x16x16_bf16 v[132:135], v[124:125], v[66:67], v[132:135]\n"
    "buffer_load_dwordx4 v[12:15], v15, s[40:43], s60 offen sc1\n"
    "ds_read_b128 v[68:71], v2 offset:28672\n"
    "s_nop 0\n"
    "v_mfma_f32_16x16x16_bf16 v[120:123], v[126:127], v[66:67], v[120:123]\n"
    "ds_read_b128 v[72:75], v2 offset:29696\n"
    "buffer_load_dword v79, v0, s[40:43], s58 offen offset:3072 sc1\n"
    "v_mov_b64 v[56:57], s[46:47]\n"
    "v_mov_b64 v[58:59], s[46:47]\n"
    "v_xor_b32 v76, 0x200, v6\n"
    "v_cvt_pk_bf16_f32 v84, v132, v133\n"
    "v_cvt_pk_bf16_f32 v85, v134, v135\n"
    "v_xor_b32 v77, 0x400, v6\n"
    "s_not_b64 exec, vcc\n"
    "v_cvt_pk_bf16_f32 v86, v120, v121\n"
    "v_cvt_pk_bf16_f32 v87, v122, v123\n"
    "ds_write_b128 v6, v[84:87] offset:26880\n"
    "s_mov_b64 exec, -1\n"
    "s_cmp_eq_u32 s48, 63\n"
    "v_mfma_f32_16x16x32_bf16 v[128:131], v[80:83], v[56:59], 0\n"
    "v_mov_b32 v67, 0\n"
    "v_mov_b64 v[132:133], 0\n"
    "v_mov_b64 v[134:135], 0\n"
    "v_mov_b32_dpp v84, v84 quad_perm:[0,1,0,1] row_mask:0xf bank_mask:0xf\n"
    "v_mov_b32_dpp v85, v85 quad_perm:[0,1,0,1] row_mask:0xf bank_mask:0xf\n"
    "v_mov_b32_dpp v86, v86 quad_perm:[0,1,0,1] row_mask:0xf bank_mask:0xf\n"
    "v_mov_b32_dpp v87, v87 quad_perm:[0,1,0,1] row_mask:0xf bank_mask:0xf\n"
    "v_mfma_f32_16x16x16_bf16 v[128:131], v[66:67], v[56:57], v[128:131]\n"
    "v_mov_b64 v[80:81], 0\n"
    "v_mov_b64 v[82:83], 0\n"
    "v_smfmac_f32_16x16x64_bf16 v[132:135], v[84:87], v[88:95], v1\n"
    "v_xor_b32 v78, 0x600, v6\n"
    "s_waitcnt lgkmcnt(13)\n"
    "v_smfmac_f32_16x16x64_bf16 v[80:83], v[84:87], v[96:103], v1\n"
    "s_waitcnt lgkmcnt(0)\n"
    "s_barrier\n"
    "s_cbranch_scc1 .Lend_%=\n"
    "ds_read_b128 v[56:59], v76 offset:26880\n"
    "ds_read_b128 v[60:63], v77 offset:26880\n"
    "ds_read_b128 v[120:123], v78 offset:26880\n"
    "ds_read_b128 v[88:91], v2 offset:30720\n"
    "v_rcp_f32 v128, v128\n"
    "v_rcp_f32 v129, v129\n"
    "s_waitcnt lgkmcnt(3)\n"
    "v_smfmac_f32_16x16x64_bf16 v[132:135], v[56:59], v[40:47], v1\n"
    "ds_read_b128 v[40:43], v0 offset:26880\n"
    "v_mov_b32 v78, s73\n"
    "s_waitcnt lgkmcnt(3)\n"
    "v_smfmac_f32_16x16x64_bf16 v[132:135], v[60:63], v[48:55], v1\n"
    "ds_read_b128 v[92:95], v2 offset:31744\n"
    "s_waitcnt lgkmcnt(3)\n"
    "v_smfmac_f32_16x16x64_bf16 v[132:135], v[120:123], v[104:111], v1\n"
    "ds_read_b128 v[96:99], v2 offset:32768\n"
    "v_mov_b64 v[64:65], 0\n"
    "v_smfmac_f32_16x16x64_bf16 v[80:83], v[56:59], v[112:119], v1\n"
    "ds_read_b128 v[100:103], v2 offset:33792\n"
    "v_mov_b64 v[66:67], 0\n"
    "v_smfmac_f32_16x16x64_bf16 v[80:83], v[60:63], v[32:39], v1\n"
    "ds_read_b128 v[44:47], v0 offset:27904\n"
    "v_mul_f32 v132, v132, v128\n"
    "v_mul_f32 v133, v133, v129\n"
    "v_smfmac_f32_16x16x64_bf16 v[80:83], v[120:123], v[68:75], v1\n"
    "global_load_dword v76, v78, %1 sc1\n"
    "v_mul_f32 v134, v134, v128\n"
    "v_mul_f32 v135, v135, v129\n"
    "s_waitcnt lgkmcnt(3)\n"
    "v_smfmac_f32_16x16x64_bf16 v[64:67], v[40:43], v[88:95], v1\n"
    "global_load_dword v77, v78, %1 offset:128 sc1\n"
    "v_cvt_pk_bf16_f32 v132, v132, v133\n"
    "v_cvt_pk_bf16_f32 v133, v134, v135\n"
    "v_bfi_b32 v132, 1, s48, v132\n"
    "s_waitcnt lgkmcnt(0)\n"
    "v_smfmac_f32_16x16x64_bf16 v[64:67], v[44:47], v[96:103], v1\n"
    "v_pk_mul_f32 v[80:81], v[80:81], v[128:129]\n"
    "v_pk_mul_f32 v[82:83], v[82:83], v[128:129]\n"
    "v_cvt_pk_bf16_f32 v134, v80, v81\n"
    "v_cvt_pk_bf16_f32 v135, v82, v83\n"
    "v_bfi_b32 v134, 1, s48, v134\n"
    "buffer_store_dwordx4 v[132:135], v7, s[40:43], 0 offen sc1\n"
    "s_nop 2\n"
    "v_pk_add_f32 v[64:65], v[64:65], v[66:67]\n"
    "v_pk_mul_f32 v[64:65], v[64:65], v[128:129]\n"
    "v_cvt_pk_bf16_f32 v66, v64, v65\n"
    "v_bfi_b32 v64, 1, s48, v66\n"
    "s_nop 0\n"
    "v_mov_b32_dpp v65, v66 row_ror:8 row_mask:0xf bank_mask:0xf\n"
    "buffer_store_dwordx2 v[64:65], v8, s[40:43], 0 offen sc1\n"
    "s_memrealtime s[38:39]\n"
    "ds_write_b128 v5, v[124:127] offset:22784\n"
    "s_cmp_ge_u32 %2, 2\n"
    "s_cbranch_scc1 .Lw3_%=\n"
    "v_and_b32 v104, 0x70, v0\n"
    "ds_read_b128 v[112:115], v104 offset:31936\n"
    "s_movk_i32 s73, 0x60\n"
    "s_movk_i32 s51, 0x80\n"
    "v_lshrrev_b32 v104, 1, v0\n"
    "v_bfi_b32 v104, s73, v104, v0\n"
    "v_lshlrev_b32 v105, 2, v0\n"
    "v_bfi_b32 v104, s51, v105, v104\n"
    "s_waitcnt lgkmcnt(0)\n"
    "s_barrier\n"
    "s_waitcnt vmcnt(4)\n"
    "s_mov_b32 s56, 0\n"
    ".Lr4_a%=:\n"
    "v_subrev_u32 v78, s52, v79\n"
    "v_cmp_eq_u32 vcc, -1, v79\n"
    "s_cbranch_vccnz .Lr4_%=\n"
    ".Lr4_x%=:\n"
    "v_max_i32_dpp v78, v78, v78 row_ror:8 row_mask:0xf bank_mask:0xf\n"
    "s_nop 1\n"
    "v_max_i32_dpp v78, v78, v78 row_ror:4 row_mask:0xf bank_mask:0xf\n"
    "s_nop 1\n"
    "v_max_i32_dpp v78, v78, v78 row_ror:2 row_mask:0xf bank_mask:0xf\n"
    "s_nop 1\n"
    "v_max_i32_dpp v78, v78, v78 row_ror:1 row_mask:0xf bank_mask:0xf\n"
    "s_nop 0\n"
    "v_readlane_b32 s95, v78, 0\n"
    "v_readlane_b32 s73, v78, 16\n"
    "v_readlane_b32 s51, v78, 32\n"
    "v_readlane_b32 s53, v78, 48\n"
    "s_max_i32 s95, s95, s73\n"
    "s_max_i32 s51, s51, s53\n"
    "s_max_i32 s95, s95, s51\n"
    "s_min_i32 s95, s95, 1024\n"
    "s_add_u32 s95, s95, s52\n"
    "s_add_u32 s95, s95, s55\n"
    "s_cmp_lt_i32 s71, 0\n"
    "s_cbranch_scc1 .Lnored_%=\n"
    "s_mov_b32 s56, 0\n"
    "s_sub_u32 s53, s95, s54\n"
    "s_waitcnt lgkmcnt(0)\n"
    ".Lgl5_%=:\n"
    "s_memrealtime s[50:51]\n"
    "s_waitcnt lgkmcnt(0)\n"
    "s_sub_u32 s50, s50, s53\n"
    "s_cmp_lt_i32 s50, 0\n"
    "s_cbranch_scc1 .Lgl5_%=\n"
    "buffer_load_dwordx4 v[88:91], v104, s[40:43], s65 offen sc1\n"
    "buffer_load_dwordx4 v[92:95], v104, s[40:43], s65 offen offset:1024 sc1\n"
    "buffer_load_dwordx4 v[96:99], v104, s[40:43], s65 offen offset:2048 sc1\n"
    "buffer_load_dwordx4 v[100:103], v104, s[40:43], s65 offen offset:3072 sc1\n"
    "ds_read_b128 v[32:35], v2\n"
    "ds_read_b128 v[36:39], v2 offset:1024\n"
    "ds_read_b128 v[40:43], v2 offset:2048\n"
    "ds_read_b128 v[44:47], v2 offset:3072\n"
    "ds_read_b128 v[48:51], v2 offset:4096\n"
    "ds_read_b128 v[52:55], v2 offset:5120\n"
    "ds_read_b128 v[56:59], v2 offset:6144\n"
    "ds_read_b128 v[60:63], v2 offset:7168\n"
    "ds_read_b128 v[64:67], v2 offset:8192\n"
    "ds_read_b128 v[68:71], v2 offset:9216\n"
    "ds_read_b128 v[80:83], v2 offset:12288\n"
    "ds_read_b128 v[84:87], v2 offset:13312\n"
    "s_cmp_eq_u32 %2, 0\n"
    "s_cselect_b64 exec, 1, 0\n"
    "v_and_b32 v106, -2, s95\n"
    "ds_write_b32 v11, v106 offset:31876\n"
    "s_mov_b64 exec, -1\n"
    "v_xor_b32 v132, 0x800, v10\n"
    "v_xor_b32 v133, 0x1000, v10\n"
    "v_xor_b32 v134, 0x1800, v10\n"
    "v_lshrrev_b32 v135, 2, v5\n"
    "s_movk_i32 s73, 0x180\n"
    "v_lshlrev_b32 v120, 1, v0\n"
    "v_bfi_b32 v120, s73, v120, v0\n"
    "v_lshrrev_b32 v122, 2, v0\n"
    "v_bfi_b32 v120, 64, v122, v120\n"
    "v_lshlrev_b32 v121, 1, v135\n"
    "v_bfi_b32 v121, s73, v121, v135\n"
    "v_lshrrev_b32 v122, 2, v135\n"
    "v_bfi_b32 v121, 64, v122, v121\n"
    "s_mul_i32 s73, s48, 3\n"
    "s_add_u32 s73, s73, s94\n"
    "s_and_b32 s51, s73, 1\n"
    "s_lshl_b32 s51, s51, 12\n"
    "s_lshr_b32 s73, s73, 1\n"
    "s_lshl_b32 s73, s73, 14\n"
    "s_add_u32 s73, s73, s51\n"
    "s_add_u32 s66, s73, s68\n"
    "s_lshr_b32 s67, %2, 1\n"
    "s_mul_i32 s73, s48, 3\n"
    "s_add_u32 s73, s73, s67\n"
    "s_and_b32 s51, s73, 1\n"
    "s_lshl_b32 s51, s51, 12\n"
    "s_lshr_b32 s73, s73, 1\n"
    "s_lshl_b32 s73, s73, 14\n"
    "s_add_u32 s73, s73, s51\n"
    "s_add_u32 s67, s73, s68\n"
    "s_and_b32 s73, %2, 1\n"
    "s_lshl_b32 s73, s73, 11\n"
    "s_add_u32 s67, s67, s73\n"
    "s_mov_b32 s72, s58\n"
    "s_mul_i32 s73, s48, 3\n"
    "s_add_u32 s73, s73, 5\n"
    "s_and_b32 s51, s73, 1\n"
    "s_lshl_b32 s51, s51, 12\n"
    "s_lshr_b32 s73, s73, 1\n"
    "s_lshl_b32 s73, s73, 14\n"
    "s_add_u32 s73, s73, s51\n"
    "s_add_u32 s58, s73, s68\n"
    "s_waitcnt vmcnt(6)\n"
    "v_and_b32 v77, v76, v77\n"
    "s_nop 0\n"
    "v_readfirstlane_b32 s70, v77\n"
    "ds_read_b128 v[72:75], v2 offset:10240\n"
    "ds_read_b128 v[76:79], v2 offset:11264\n"
    "v_mov_b64 v[124:125], 0\n"
    "v_mov_b64 v[126:127], 0\n"
    "v_mov_b64 v[128:129], 0\n"
    "v_mov_b64 v[130:131], 0\n"
    "s_sub_u32 s73, s38, s52\n"
    "s_lshl_b32 s73, s73, 3\n"
    "s_sub_u32 s73, s73, s96\n"
    "s_ashr_i32 s73, s73, 2\n"
    "s_max_i32 s73, s73, -16\n"
    "s_min_i32 s73, s73, 16\n"
    "s_add_u32 s96, s96, s73\n"
    "s_waitcnt lgkmcnt(0)\n"
    ".Lred_%=:\n"
    "s_waitcnt vmcnt(3)\n"
    "v_mfma_f32_16x16x32_bf16 v[108:111], v[88:91], v[112:115], 0\n"
    "v_bitop3_b32 v116, v88, v90, s48 bitop3:0x7e\n"
    "s_waitcnt vmcnt(2)\n"
    "v_mfma_f32_16x16x32_bf16 v[108:111], v[92:95], v[112:115], v[108:111]\n"
    "v_bitop3_b32 v117, v92, v94, s48 bitop3:0x7e\n"
    "s_waitcnt vmcnt(1)\n"
    "v_mfma_f32_16x16x32_bf16 v[108:111], v[96:99], v[112:115], v[108:111]\n"
    "v_bitop3_b32 v118, v96, v98, s48 bitop3:0x7e\n"
    "v_or3_b32 v116, v116, v117, v118\n"
    "s_waitcnt vmcnt(0)\n"
    "v_mfma_f32_16x16x32_bf16 v[108:111], v[100:103], v[112:115], v[108:111]\n"
    "v_bitop3_b32 v119, v100, v102, s48 bitop3:0x7e\n"
    "v_or_b32 v116, v116, v119\n"
    "v_and_b32 v116, 1, v116\n"
    "v_cmp_ne_u32 vcc, 0, v116\n"
    "s_cbranch_vccnz .Lrf_%=\n"
    ".Lrx_%=:\n"
    "s_nop 3\n"
    "v_pk_add_f32 v[108:109], v[108:109], v[110:111]\n"
    "v_cvt_pk_bf16_f32 v108, v108, v109\n"
    "v_min_u32 v108, -2, v108\n"
    "buffer_store_dword v108, v9, s[40:43], s66 offen sc1\n"
    ".Lpub_%=:\n"
    "s_cmp_eq_u32 s59, 0\n"
    "s_cbranch_scc1 .Lr6_%=\n"
    ".Lr6_x%=:\n"
    "s_add_u32 s73, s48, 2\n"
    "s_and_b32 s73, s73, 63\n"
    "s_lshr_b32 s51, s73, 2\n"
    "s_xor_b32 s51, s51, s69\n"
    "s_and_b32 s51, s51, 1\n"
    "s_lshl_b32 s51, s51, 13\n"
    "s_lshl_b32 s73, s73, 5\n"
    "s_add_u32 s73, s73, %4\n"
    "s_lshl_b32 s73, s73, 14\n"
    "s_add_u32 s73, s73, s51\n"
    "s_add_u32 s60, s73, 0x6000000\n"
    "s_add_u32 s61, s73, 0x6001000\n"
    "s_add_u32 s62, s73, 0x8000000\n"
    "s_add_u32 s63, s73, 0x8001000\n"
    "s_lshl_b32 s51, %5, 12\n"
    "s_add_u32 s73, s73, s51\n"
    "s_add_u32 s64, s73, 0x4000000\n"
    "s_add_u32 s73, s48, 2\n"
    "s_and_b32 s51, s73, 32\n"
    "s_and_b32 s73, s73, 31\n"
    "s_lshl3_add_u32 s73, s51, s73\n"
    "s_lshl2_add_u32 s73, s73, s92\n"
    "v_mov_b32 v101, s73\n"
    ".Lg_%=:\n"
    "s_waitcnt lgkmcnt(0)\n"
    "s_barrier\n"
    "ds_read_b32 v97, v11 offset:31876\n"
    "s_mov_b32 s56, 0\n"
    "s_waitcnt lgkmcnt(0)\n"
    "v_readfirstlane_b32 s95, v97\n"
    "s_and_b32 s73, s95, 1\n"
    "s_lshl_b32 s73, s73, 22\n"
    "s_or_b32 s67, s67, s73\n"
    "s_or_b32 s72, s72, s73\n"
    "s_sub_u32 s53, s95, 0\n"
    "s_waitcnt lgkmcnt(0)\n"
    ".Lgl7_%=:\n"
    "s_memrealtime s[50:51]\n"
    "s_waitcnt lgkmcnt(0)\n"
    "s_sub_u32 s50, s50, s53\n"
    "s_cmp_lt_i32 s50, 0\n"
    "s_cbranch_scc1 .Lgl7_%=\n"
    "buffer_load_dwordx4 v[88:91], v120, s[40:43], s67 offen sc1\n"
    "buffer_load_dwordx4 v[92:95], v120, s[40:43], s67 offen offset:1024 sc1\n"
    "buffer_load_dword v96, v121, s[40:43], s72 offen sc1\n"
    "s_waitcnt vmcnt(5)\n"
    "ds_write_b128 v5, v[12:15]\n"
    "ds_write_b128 v5, v[16:19] offset:4096\n"
    "ds_write_b128 v5, v[28:31] offset:16384\n"
    "v_swap_b32 v22, v24\n"
    "v_swap_b32 v23, v25\n"
    "ds_write_b128 v5, v[20:23] offset:8192\n"
    "ds_write_b128 v5, v[24:27] offset:12288\n"
    "v_mov_b64 v[16:17], 0\n"
    "v_mov_b64 v[18:19], 0\n"
    "v_mov_b64 v[20:21], 0\n"
    "v_mov_b64 v[22:23], 0\n"
    "v_mov_b64 v[24:25], 0\n"
    "v_mov_b64 v[26:27], 0\n"
    "v_mov_b64 v[28:29], 0\n"
    "v_mov_b64 v[30:31], 0\n"
    "v_mov_b32 v14, v101\n"
    "global_load_dword v12, v14, %1 sc1\n"
    "global_load_dword v13, v14, %1 offset:128 sc1\n"
    "s_cmp_ge_u32 %2, 2\n"
    "s_cbranch_scc1 .Lcob_%=\n"
    ".Lcod_%=:\n"
    "global_load_dword v14, v14, %1 sc1\n"
    ".Lcox_%=:\n"
    "s_add_u32 s48, s48, 1\n"
    "s_branch .Lstep_%=\n"
    ".Lcob_%=:\n"
    "s_add_u32 s73, s48, 0\n"
    "s_and_b32 s51, s73, 32\n"
    "s_and_b32 s73, s73, 31\n"
    "s_lshl3_add_u32 s73, s51, s73\n"
    "s_lshl2_add_u32 s73, s73, s93\n"
    "v_mov_b32 v100, s73\n"
    "v_mov_b32 v101, 1\n"
    "s_waitcnt vmcnt(5)\n"
    "s_mov_b64 exec, 1\n"
    "ds_add_rtn_u32 v101, v11, v101 offset:31872\n"
    "s_mov_b64 exec, -1\n"
    "s_waitcnt lgkmcnt(0)\n"
    "v_readfirstlane_b32 s73, v101\n"
    "v_mov_b32 v101, 1\n"
    "s_bitcmp1_b32 s73, 0\n"
    "s_cbranch_scc0 .Lcod_%=\n"
    "s_mov_b64 exec, 1\n"
    "global_store_dword v100, v101, %1 sc1\n"
    "s_mov_b64 exec, -1\n"
    "s_branch .Lcox_%=\n"
    ".Lgf_%=:\n"
    "s_bitset1_b32 s67, 22\n"
    "s_bitset1_b32 s72, 22\n"
    "s_waitcnt lgkmcnt(0)\n"
    "buffer_load_dwordx4 v[88:91], v120, s[40:43], s67 offen sc1\n"
    "buffer_load_dwordx4 v[92:95], v120, s[40:43], s67 offen offset:1024 sc1\n"
    "buffer_load_dword v96, v121, s[40:43], s72 offen sc1\n"
    "s_waitcnt vmcnt(0)\n"
    "v_mov_b64 v[16:17], 0\n"
    "v_mov_b64 v[18:19], 0\n"
    "v_mov_b64 v[20:21], 0\n"
    "v_mov_b64 v[22:23], 0\n"
    "v_mov_b64 v[24:25], 0\n"
    "v_mov_b64 v[26:27], 0\n"
    "v_mov_b64 v[28:29], 0\n"
    "v_mov_b64 v[30:31], 0\n"
    "v_mov_b64 v[124:125], 0\n"
    "v_mov_b64 v[126:127], 0\n"
    "v_mov_b64 v[128:129], 0\n"
    "v_mov_b64 v[130:131], 0\n"
    "s_add_u32 s56, s56, 1\n"
    "s_cmp_lt_u32 s56, s57\n"
    "s_cbranch_scc1 .Lstep_%=\n"
    "s_mov_b32 s57, 0\n"
    "v_mov_b32 v135, 1\n"
    "global_store_dword v11, v135, %1 sc1\n"
    "s_branch .Lgx_%=\n"
    ".Lrf_%=:\n"
    "s_memrealtime s[50:51]\n"
    "s_waitcnt lgkmcnt(0)\n"
    "s_add_u32 s95, s50, 62\n"
    "v_or_b32 v106, 1, s95\n"
    "ds_write_b32 v11, v106 offset:31876\n"
    "buffer_load_dwordx4 v[88:91], v104, s[40:43], s65 offen sc1\n"
    "buffer_load_dwordx4 v[92:95], v104, s[40:43], s65 offen offset:1024 sc1\n"
    "buffer_load_dwordx4 v[96:99], v104, s[40:43], s65 offen offset:2048 sc1\n"
    "buffer_load_dwordx4 v[100:103], v104, s[40:43], s65 offen offset:3072 sc1\n"
    "s_add_u32 s56, s56, 1\n"
    "s_cmp_lt_u32 s56, s57\n"
    "s_cbranch_scc1 .Lred_%=\n"
    "s_mov_b32 s57, 0\n"
    "v_mov_b32 v135, 1\n"
    "global_store_dword v11, v135, %1 sc1\n"
    "s_branch .Lrx_%=\n"
    ".Lnored_%=:\n"
    "s_waitcnt vmcnt(0)\n"
    "ds_read_b128 v[32:35], v2\n"
    "ds_read_b128 v[36:39], v2 offset:1024\n"
    "ds_read_b128 v[40:43], v2 offset:2048\n"
    "ds_read_b128 v[44:47], v2 offset:3072\n"
    "ds_read_b128 v[48:51], v2 offset:4096\n"
    "ds_read_b128 v[52:55], v2 offset:5120\n"
    "ds_read_b128 v[56:59], v2 offset:6144\n"
    "ds_read_b128 v[60:63], v2 offset:7168\n"
    "ds_read_b128 v[64:67], v2 offset:8192\n"
    "ds_read_b128 v[68:71], v2 offset:9216\n"
    "ds_read_b128 v[80:83], v2 offset:12288\n"
    "ds_read_b128 v[84:87], v2 offset:13312\n"
    "s_cmp_eq_u32 %2, 0\n"
    "s_cselect_b64 exec, 1, 0\n"
    "v_and_b32 v106, -2, s95\n"
    "ds_write_b32 v11, v106 offset:31876\n"
    "s_mov_b64 exec, -1\n"
    "v_xor_b32 v132, 0x800, v10\n"
    "v_xor_b32 v133, 0x1000, v10\n"
    "v_xor_b32 v134, 0x1800, v10\n"
    "v_lshrrev_b32 v135, 2, v5\n"
    "s_movk_i32 s73, 0x180\n"
    "v_lshlrev_b32 v120, 1, v0\n"
    "v_bfi_b32 v120, s73, v120, v0\n"
    "v_lshrrev_b32 v122, 2, v0\n"
    "v_bfi_b32 v120, 64, v122, v120\n"
    "v_lshlrev_b32 v121, 1, v135\n"
    "v_bfi_b32 v121, s73, v121, v135\n"
    "v_lshrrev_b32 v122, 2, v135\n"
    "v_bfi_b32 v121, 64, v122, v121\n"
    "s_mul_i32 s73, s48, 3\n"
    "s_add_u32 s73, s73, s94\n"
    "s_and_b32 s51, s73, 1\n"
    "s_lshl_b32 s51, s51, 12\n"
    "s_lshr_b32 s73, s73, 1\n"
    "s_lshl_b32 s73, s73, 14\n"
    "s_add_u32 s73, s73, s51\n"
    "s_add_u32 s66, s73, s68\n"
    "s_lshr_b32 s67, %2, 1\n"
    "s_mul_i32 s73, s48, 3\n"
    "s_add_u32 s73, s73, s67\n"
    "s_and_b32 s51, s73, 1\n"
    "s_lshl_b32 s51, s51, 12\n"
    "s_lshr_b32 s73, s73, 1\n"
    "s_lshl_b32 s73, s73, 14\n"
    "s_add_u32 s73, s73, s51\n"
    "s_add_u32 s67, s73, s68\n"
    "s_and_b32 s73, %2, 1\n"
    "s_lshl_b32 s73, s73, 11\n"
    "s_add_u32 s67, s67, s73\n"
    "s_mov_b32 s72, s58\n"
    "s_mul_i32 s73, s48, 3\n"
    "s_add_u32 s73, s73, 5\n"
    "s_and_b32 s51, s73, 1\n"
    "s_lshl_b32 s51, s51, 12\n"
    "s_lshr_b32 s73, s73, 1\n"
    "s_lshl_b32 s73, s73, 14\n"
    "s_add_u32 s73, s73, s51\n"
    "s_add_u32 s58, s73, s68\n"
    "s_waitcnt vmcnt(6)\n"
    "v_and_b32 v77, v76, v77\n"
    "s_nop 0\n"
    "v_readfirstlane_b32 s70, v77\n"
    "ds_read_b128 v[72:75], v2 offset:10240\n"
    "ds_read_b128 v[76:79], v2 offset:11264\n"
    "v_mov_b64 v[124:125], 0\n"
    "v_mov_b64 v[126:127], 0\n"
    "v_mov_b64 v[128:129], 0\n"
    "v_mov_b64 v[130:131], 0\n"
    "s_sub_u32 s73, s38, s52\n"
    "s_lshl_b32 s73, s73, 3\n"
    "s_sub_u32 s73, s73, s96\n"
    "s_ashr_i32 s73, s73, 2\n"
    "s_max_i32 s73, s73, -16\n"
    "s_min_i32 s73, s73, 16\n"
    "s_add_u32 s96, s96, s73\n"
    "s_waitcnt lgkmcnt(0)\n"
    "s_branch .Lpub_%=\n"
    ".Lw3_%=:\n"
    "ds_read_b128 v[32:35], v0 offset:16384\n"
    "ds_read_b128 v[48:51], v0\n"
    "ds_read_b128 v[88:91], v0 offset:4096\n"
    "ds_read_b128 v[104:107], v4 offset:20480\n"
    "ds_read_b128 v[36:39], v0 offset:17408\n"
    "ds_read_b128 v[52:55], v0 offset:1024\n"
    "ds_read_b128 v[92:95], v0 offset:5120\n"
    "ds_read_b128 v[108:111], v4 offset:20544\n"
    "ds_read_b128 v[40:43], v0 offset:18432\n"
    "ds_read_b128 v[56:59], v0 offset:2048\n"
    "ds_read_b128 v[96:99], v0 offset:6144\n"
    "ds_read_b128 v[112:115], v4 offset:20608\n"
    "s_waitcnt lgkmcnt(10)\n"
    "v_mfma_f32_16x16x32_bf16 v[120:123], v[48:51], v[32:35], 0\n"
    "s_barrier\n"
    "ds_read_b128 v[44:47], v0 offset:19456\n"
    "ds_read_b128 v[60:63], v0 offset:3072\n"
    "ds_read_b128 v[100:103], v0 offset:7168\n"
    "ds_read_b128 v[116:119], v4 offset:20672\n"
    "s_waitcnt lgkmcnt(13)\n"
    "v_mfma_f32_16x16x32_bf16 v[124:127], v[88:91], v[32:35], 0\n"
    "s_waitcnt lgkmcnt(12)\n"
    "v_mfma_f32_16x16x32_bf16 v[128:131], v[104:107], v[32:35], 0\n"
    "s_waitcnt lgkmcnt(10)\n"
    "v_mfma_f32_16x16x32_bf16 v[120:123], v[52:55], v[36:39], v[120:123]\n"
    "s_waitcnt lgkmcnt(9)\n"
    "v_mfma_f32_16x16x32_bf16 v[124:127], v[92:95], v[36:39], v[124:127]\n"
    "ds_read_b128 v[64:67], v10 offset:4096\n"
    "s_waitcnt lgkmcnt(9)\n"
    "v_mfma_f32_16x16x32_bf16 v[128:131], v[108:111], v[36:39], v[128:131]\n"
    "ds_read_b128 v[68:71], v10 offset:8192\n"
    "s_waitcnt lgkmcnt(8)\n"
    "v_mfma_f32_16x16x32_bf16 v[120:123], v[56:59], v[40:43], v[120:123]\n"
    "ds_read_b128 v[72:75], v10 offset:5120\n"
    "s_waitcnt lgkmcnt(8)\n"
    "v_mfma_f32_16x16x32_bf16 v[124:127], v[96:99], v[40:43], v[124:127]\n"
    "ds_read_b128 v[80:83], v10 offset:9216\n"
    "s_waitcnt lgkmcnt(8)\n"
    "v_mfma_f32_16x16x32_bf16 v[128:131], v[112:115], v[40:43], v[128:131]\n"
    "ds_read_b128 v[84:87], v11 offset:31888\n"
    "s_waitcnt lgkmcnt(7)\n"
    "v_mfma_f32_16x16x32_bf16 v[120:123], v[60:63], v[44:47], v[120:123]\n"
    "ds_read_b64 v[134:135], v11 offset:31904\n"
    "s_waitcnt lgkmcnt(7)\n"
    "v_mfma_f32_16x16x32_bf16 v[124:127], v[100:103], v[44:47], v[124:127]\n"
    "ds_read_b128 v[88:91], v10 offset:18688\n"
    "s_waitcnt lgkmcnt(7)\n"
    "v_mfma_f32_16x16x32_bf16 v[128:131], v[116:119], v[44:47], v[128:131]\n"
    "ds_read_b128 v[92:95], v10 offset:19712\n"
    "v_mov_b32 v132, 0xd01502f9\n"
    "ds_read_b128 v[32:35], v2\n"
    "ds_read_b128 v[36:39], v2 offset:1024\n"
    "ds_read_b128 v[40:43], v2 offset:2048\n"
    "ds_read_b128 v[44:47], v2 offset:3072\n"
    "v_cndmask_b32 v120, v132, v120, s[76:77]\n"
    "v_cndmask_b32 v121, v132, v121, s[78:79]\n"
    "v_cndmask_b32 v122, v132, v122, s[80:81]\n"
    "v_cndmask_b32 v123, v132, v123, s[82:83]\n"
    "v_cndmask_b32 v124, v132, v124, s[84:85]\n"
    "v_cndmask_b32 v125, v132, v125, s[86:87]\n"
    "v_cndmask_b32 v126, v132, v126, s[88:89]\n"
    "v_cndmask_b32 v127, v132, v127, s[90:91]\n"
    "v_maximum3_f32 v133, v128, v129, v120\n"
    "v_maximum3_f32 v133, v133, v121, v122\n"
    "v_maximum3_f32 v133, v133, v123, v124\n"
    "v_maximum3_f32 v133, v133, v125, v126\n"
    "v_maximum3_f32 v133, v133, v127, v127\n"
    "v_mov_b32 v132, v133\n"
    "ds_read_b128 v[48:51], v2 offset:4096\n"
    "ds_read_b128 v[52:55], v2 offset:5120\n"
    "v_permlane16_swap_b32 v132, v133\n"
    "v_maximum3_f32 v133, v133, v132, v132\n"
    "v_mov_b32 v132, v133\n"
    "ds_read_b128 v[56:59], v2 offset:6144\n"
    "ds_read_b128 v[60:63], v2 offset:7168\n"
    "v_permlane32_swap_b32 v132, v133\n"
    "v_maximum3_f32 v133, v133, v132, v132\n"
    "v_mul_f32 v133, 0xbe0293ee, v133\n"
    "v_fmamk_f32 v120, v120, 0x3e0293ee, v133\n"
    "v_fmamk_f32 v121, v121, 0x3e0293ee, v133\n"
    "v_fmamk_f32 v122, v122, 0x3e0293ee, v133\n"
    "v_fmamk_f32 v123, v123, 0x3e0293ee, v133\n"
    "v_fmamk_f32 v124, v124, 0x3e0293ee, v133\n"
    "v_fmamk_f32 v125, v125, 0x3e0293ee, v133\n"
    "v_fmamk_f32 v126, v126, 0x3e0293ee, v133\n"
    "v_fmamk_f32 v127, v127, 0x3e0293ee, v133\n"
    "v_fmamk_f32 v128, v128, 0x3e0293ee, v133\n"
    "v_fmamk_f32 v129, v129, 0x3e0293ee, v133\n"
    "v_exp_f32 v120, v120\n"
    "v_exp_f32 v121, v121\n"
    "v_exp_f32 v122, v122\n"
    "v_exp_f32 v123, v123\n"
    "v_exp_f32 v124, v124\n"
    "v_exp_f32 v125, v125\n"
    "v_exp_f32 v126, v126\n"
    "v_exp_f32 v127, v127\n"
    "v_exp_f32 v128, v128\n"
    "v_exp_f32 v129, v129\n"
    "v_cvt_pk_bf16_f32 v120, v120, v121\n"
    "v_cvt_pk_bf16_f32 v121, v122, v123\n"
    "v_cvt_pk_bf16_f32 v122, v124, v125\n"
    "v_cvt_pk_bf16_f32 v123, v126, v127\n"
    "v_cvt_pk_bf16_f32 v124, v128, v129\n"
    "v_cvt_pk_bf16_f32 v125, v128, v129\n"
    "s_waitcnt lgkmcnt(11)\n"
    "v_mfma_f32_16x16x32_bf16 v[100:103], v[84:87], v[120:123], 0\n"
    "v_mfma_f32_16x16x32_bf16 v[104:107], v[64:67], v[120:123], 0\n"
    "v_mfma_f32_16x16x32_bf16 v[108:111], v[68:71], v[120:123], 0\n"
    "s_waitcnt lgkmcnt(10)\n"
    "v_mfma_f32_16x16x16_bf16 v[100:103], v[134:135], v[124:125], v[100:103]\n"
    "v_mfma_f32_16x16x32_bf16 v[112:115], v[72:75], v[120:123], 0\n"
    "v_mfma_f32_16x16x32_bf16 v[116:119], v[80:83], v[120:123], 0\n"
    "s_waitcnt lgkmcnt(9)\n"
    "v_mfma_f32_16x16x16_bf16 v[104:107], v[88:89], v[124:125], v[104:107]\n"
    "ds_read_b128 v[64:67], v2 offset:8192\n"
    "v_mfma_f32_16x16x16_bf16 v[108:111], v[90:91], v[124:125], v[108:111]\n"
    "ds_read_b128 v[68:71], v2 offset:9216\n"
    "v_rcp_f32 v132, v100\n"
    "s_waitcnt lgkmcnt(10)\n"
    "v_mfma_f32_16x16x16_bf16 v[112:115], v[92:93], v[124:125], v[112:115]\n"
    "ds_read_b128 v[80:83], v2 offset:12288\n"
    "v_mfma_f32_16x16x16_bf16 v[116:119], v[94:95], v[124:125], v[116:119]\n"
    "ds_read_b128 v[84:87], v2 offset:13312\n"
    "v_pk_mul_f32 v[104:105], v[104:105], v[132:133] op_sel_hi:[1,0]\n"
    "v_pk_mul_f32 v[106:107], v[106:107], v[132:133] op_sel_hi:[1,0]\n"
    "v_pk_mul_f32 v[108:109], v[108:109], v[132:133] op_sel_hi:[1,0]\n"
    "v_pk_mul_f32 v[110:111], v[110:111], v[132:133] op_sel_hi:[1,0]\n"
    "v_cvt_pk_bf16_f32 v104, v104, v108\n"
    "v_cvt_pk_bf16_f32 v105, v105, v109\n"
    "v_cvt_pk_bf16_f32 v106, v106, v110\n"
    "v_cvt_pk_bf16_f32 v107, v107, v111\n"
    "v_pk_mul_f32 v[112:113], v[112:113], v[132:133] op_sel_hi:[1,0]\n"
    "v_pk_mul_f32 v[114:115], v[114:115], v[132:133] op_sel_hi:[1,0]\n"
    "v_pk_mul_f32 v[116:117], v[116:117], v[132:133] op_sel_hi:[1,0]\n"
    "v_pk_mul_f32 v[118:119], v[118:119], v[132:133] op_sel_hi:[1,0]\n"
    "v_cvt_pk_bf16_f32 v112, v112, v116\n"
    "v_cvt_pk_bf16_f32 v113, v113, v117\n"
    "v_cvt_pk_bf16_f32 v114, v114, v118\n"
    "v_cvt_pk_bf16_f32 v115, v115, v119\n"
    "s_waitcnt vmcnt(2)\n"
    "s_cmp_eq_u32 s59, 0\n"
    "s_cbranch_scc1 .Lr8_%=\n"
    ".Lr8_x%=:\n"
    "s_lshr_b32 s51, s48, 2\n"
    "s_xor_b32 s51, s51, s69\n"
    "s_and_b32 s51, s51, 1\n"
    "s_lshl_b32 s51, s51, 13\n"
    "s_lshl_b32 s73, s48, 19\n"
    "s_add_u32 s73, s73, s75\n"
    "s_add_u32 s73, s73, s51\n"
    "buffer_store_dwordx4 v[104:107], v10, s[40:43], s73 offen sc1\n"
    "buffer_store_dwordx4 v[112:115], v10, s[40:43], s73 offen offset:1024 sc1\n"
    "ds_read_b128 v[72:75], v2 offset:10240\n"
    "ds_read_b128 v[76:79], v2 offset:11264\n"
    "v_mov_b64 v[124:125], 0\n"
    "v_mov_b64 v[126:127], 0\n"
    "v_mov_b64 v[128:129], 0\n"
    "v_mov_b64 v[130:131], 0\n"
    "v_xor_b32 v132, 0x800, v10\n"
    "v_xor_b32 v133, 0x1000, v10\n"
    "v_xor_b32 v134, 0x1800, v10\n"
    "v_lshrrev_b32 v135, 2, v5\n"
    "s_movk_i32 s73, 0x180\n"
    "v_lshlrev_b32 v120, 1, v0\n"
    "v_bfi_b32 v120, s73, v120, v0\n"
    "v_lshrrev_b32 v122, 2, v0\n"
    "v_bfi_b32 v120, 64, v122, v120\n"
    "v_lshlrev_b32 v121, 1, v135\n"
    "v_bfi_b32 v121, s73, v121, v135\n"
    "v_lshrrev_b32 v122, 2, v135\n"
    "v_bfi_b32 v121, 64, v122, v121\n"
    "s_lshr_b32 s67, %2, 1\n"
    "s_mul_i32 s73, s48, 3\n"
    "s_add_u32 s73, s73, s67\n"
    "s_and_b32 s51, s73, 1\n"
    "s_lshl_b32 s51, s51, 12\n"
    "s_lshr_b32 s73, s73, 1\n"
    "s_lshl_b32 s73, s73, 14\n"
    "s_add_u32 s73, s73, s51\n"
    "s_add_u32 s67, s73, s68\n"
    "s_and_b32 s73, %2, 1\n"
    "s_lshl_b32 s73, s73, 11\n"
    "s_add_u32 s67, s67, s73\n"
    "s_mov_b32 s72, s58\n"
    "s_mul_i32 s73, s48, 3\n"
    "s_add_u32 s73, s73, 5\n"
    "s_and_b32 s51, s73, 1\n"
    "s_lshl_b32 s51, s51, 12\n"
    "s_lshr_b32 s73, s73, 1\n"
    "s_lshl_b32 s73, s73, 14\n"
    "s_add_u32 s73, s73, s51\n"
    "s_add_u32 s58, s73, s68\n"
    "s_cmp_eq_u32 s59, 0\n"
    "s_cbranch_scc1 .Lr9_%=\n"
    ".Lr9_x%=:\n"
    "s_add_u32 s73, s48, 2\n"
    "s_and_b32 s73, s73, 63\n"
    "s_lshr_b32 s51, s73, 2\n"
    "s_xor_b32 s51, s51, s69\n"
    "s_and_b32 s51, s51, 1\n"
    "s_lshl_b32 s51, s51, 13\n"
    "s_lshl_b32 s73, s73, 5\n"
    "s_add_u32 s73, s73, %4\n"
    "s_lshl_b32 s73, s73, 14\n"
    "s_add_u32 s73, s73, s51\n"
    "s_add_u32 s60, s73, 0x6000000\n"
    "s_add_u32 s61, s73, 0x6001000\n"
    "s_add_u32 s62, s73, 0x8000000\n"
    "s_add_u32 s63, s73, 0x8001000\n"
    "s_lshl_b32 s51, %5, 12\n"
    "s_add_u32 s73, s73, s51\n"
    "s_add_u32 s64, s73, 0x4000000\n"
    "s_add_u32 s73, s48, 2\n"
    "s_and_b32 s51, s73, 32\n"
    "s_and_b32 s73, s73, 31\n"
    "s_lshl3_add_u32 s73, s51, s73\n"
    "s_lshl2_add_u32 s73, s73, s92\n"
    "v_mov_b32 v101, s73\n"
    "s_branch .Lg_%=\n"
    ".Lr4_%=:\n"
    "s_sleep 2\n"
    "buffer_load_dword v79, v0, s[40:43], s58 offen offset:3072 sc1\n"
    "s_waitcnt vmcnt(0)\n"
    "s_add_u32 s56, s56, 1\n"
    "s_cmp_lt_u32 s56, s57\n"
    "s_cbranch_scc1 .Lr4_a%=\n"
    "s_mov_b32 s57, 0\n"
    "v_mov_b32 v135, 1\n"
    "global_store_dword v11, v135, %1 sc1\n"
    "s_branch .Lr4_x%=\n"
    ".Lr6_%=:\n"
    "s_cmp_lg_u32 s70, 0\n"
    "s_cbranch_scc1 .Lr6_y%=\n"
    "s_waitcnt lgkmcnt(0)\n"
    "s_add_u32 s73, s48, 1\n"
    "s_and_b32 s51, s73, 32\n"
    "s_and_b32 s73, s73, 31\n"
    "s_lshl3_add_u32 s73, s51, s73\n"
    "s_lshl2_add_u32 s73, s73, s92\n"
    "s_mov_b32 s53, s73\n"
    "s_mov_b32 s56, 0\n"
    "v_mov_b32 v134, s53\n"
    ".Lpoll10_%=:\n"
    "global_load_dword v132, v134, %1 sc1\n"
    "global_load_dword v133, v134, %1 offset:128 sc1\n"
    "s_waitcnt vmcnt(0)\n"
    "v_and_b32 v133, v132, v133\n"
    "s_nop 0\n"
    "v_readfirstlane_b32 s73, v133\n"
    "s_cmp_ge_u32 s73, 1\n"
    "s_cbranch_scc1 .Lpoll10_x%=\n"
    "s_add_u32 s56, s56, 1\n"
    "s_cmp_lt_u32 s56, 3\n"
    "s_cbranch_scc1 .Lpoll10_%=\n"
    "s_sleep 8\n"
    "s_cmp_lt_u32 s56, s57\n"
    "s_cbranch_scc1 .Lpoll10_%=\n"
    "s_mov_b32 s57, 0\n"
    "v_mov_b32 v135, 1\n"
    "global_store_dword v11, v135, %1 sc1\n"
    ".Lpoll10_x%=:\n"
    "v_xor_b32 v132, 0x800, v10\n"
    "v_xor_b32 v133, 0x1000, v10\n"
    "v_xor_b32 v134, 0x1800, v10\n"
    "v_lshrrev_b32 v135, 2, v5\n"
    "s_movk_i32 s73, 0x180\n"
    "v_lshlrev_b32 v120, 1, v0\n"
    "v_bfi_b32 v120, s73, v120, v0\n"
    "v_lshrrev_b32 v122, 2, v0\n"
    "v_bfi_b32 v120, 64, v122, v120\n"
    "v_lshlrev_b32 v121, 1, v135\n"
    "v_bfi_b32 v121, s73, v121, v135\n"
    "v_lshrrev_b32 v122, 2, v135\n"
    "v_bfi_b32 v121, 64, v122, v121\n"
    ".Lr6_y%=:\n"
    "buffer_load_dwordx4 v[12:15], v5, s[40:43], s60 offen sc1\n"
    "buffer_load_dwordx4 v[16:19], v5, s[40:43], s61 offen sc1\n"
    "buffer_load_dwordx4 v[20:23], v5, s[40:43], s62 offen sc1\n"
    "buffer_load_dwordx4 v[24:27], v5, s[40:43], s63 offen sc1\n"
    "buffer_load_dwordx4 v[28:31], v5, s[40:43], s64 offen sc1\n"
    "global_load_dword v116, v11, %1 sc1\n"
    "global_load_dword v117, v11, %1 sc1\n"
    "s_branch .Lr6_x%=\n"
    ".Lr8_%=:\n"
    "v_and_b32 v77, v76, v77\n"
    "s_nop 0\n"
    "v_readfirstlane_b32 s59, v77\n"
    "s_cmp_eq_u32 s59, 0\n"
    "s_cbranch_scc1 .Lr8_x%=\n"
    "buffer_load_dwordx4 v[12:15], v5, s[40:43], s60 offen sc1\n"
    "buffer_load_dwordx4 v[16:19], v5, s[40:43], s61 offen sc1\n"
    "buffer_load_dwordx4 v[20:23], v5, s[40:43], s62 offen sc1\n"
    "buffer_load_dwordx4 v[24:27], v5, s[40:43], s63 offen sc1\n"
    "buffer_load_dwordx4 v[28:31], v5, s[40:43], s64 offen sc1\n"
    "s_branch .Lr8_x%=\n"
    ".Lr9_%=:\n"
    "s_waitcnt lgkmcnt(0)\n"
    "s_add_u32 s73, s48, 1\n"
    "s_and_b32 s51, s73, 32\n"
    "s_and_b32 s73, s73, 31\n"
    "s_lshl3_add_u32 s73, s51, s73\n"
    "s_lshl2_add_u32 s73, s73, s92\n"
    "s_mov_b32 s53, s73\n"
    "s_mov_b32 s56, 0\n"
    "v_mov_b32 v134, s53\n"
    ".Lpoll11_%=:\n"
    "global_load_dword v132, v134, %1 sc1\n"
    "global_load_dword v133, v134, %1 offset:128 sc1\n"
    "s_waitcnt vmcnt(0)\n"
    "v_and_b32 v133, v132, v133\n"
    "s_nop 0\n"
    "v_readfirstlane_b32 s73, v133\n"
    "s_cmp_ge_u32 s73, 1\n"
    "s_cbranch_scc1 .Lpoll11_x%=\n"
    "s_add_u32 s56, s56, 1\n"
    "s_cmp_lt_u32 s56, 3\n"
    "s_cbranch_scc1 .Lpoll11_%=\n"
    "s_sleep 8\n"
    "s_cmp_lt_u32 s56, s57\n"
    "s_cbranch_scc1 .Lpoll11_%=\n"
    "s_mov_b32 s57, 0\n"
    "v_mov_b32 v135, 1\n"
    "global_store_dword v11, v135, %1 sc1\n"
    ".Lpoll11_x%=:\n"
    "v_xor_b32 v132, 0x800, v10\n"
    "v_xor_b32 v133, 0x1000, v10\n"
    "v_xor_b32 v134, 0x1800, v10\n"
    "v_lshrrev_b32 v135, 2, v5\n"
    "s_movk_i32 s73, 0x180\n"
    "v_lshlrev_b32 v120, 1, v0\n"
    "v_bfi_b32 v120, s73, v120, v0\n"
    "v_lshrrev_b32 v122, 2, v0\n"
    "v_bfi_b32 v120, 64, v122, v120\n"
    "v_lshlrev_b32 v121, 1, v135\n"
    "v_bfi_b32 v121, s73, v121, v135\n"
    "v_lshrrev_b32 v122, 2, v135\n"
    "v_bfi_b32 v121, 64, v122, v121\n"
    ".Lr9_y%=:\n"
    "buffer_load_dwordx4 v[12:15], v5, s[40:43], s60 offen sc1\n"
    "buffer_load_dwordx4 v[16:19], v5, s[40:43], s61 offen sc1\n"
    "buffer_load_dwordx4 v[20:23], v5, s[40:43], s62 offen sc1\n"
    "buffer_load_dwordx4 v[24:27], v5, s[40:43], s63 offen sc1\n"
    "buffer_load_dwordx4 v[28:31], v5, s[40:43], s64 offen sc1\n"
    "global_load_dword v116, v11, %1 sc1\n"
    "global_load_dword v117, v11, %1 sc1\n"
    "s_branch .Lr9_x%=\n"
    ".Lend_%=:\n"
    "ds_write_b128 v5, v[124:127] offset:22784\n"
    "s_waitcnt vmcnt(0)\n"
    "s_cmp_ge_u32 %2, 2\n"
    "s_cbranch_scc1 .Lla_%=\n"
    "s_waitcnt lgkmcnt(0)\n"
    "s_barrier\n"
    "s_branch .Lout_%=\n"
    ".Lla_%=:\n"
    "ds_read_b128 v[32:35], v0 offset:16384\n"
    "ds_read_b128 v[48:51], v0\n"
    "ds_read_b128 v[88:91], v0 offset:4096\n"
    "ds_read_b128 v[104:107], v4 offset:20480\n"
    "ds_read_b128 v[36:39], v0 offset:17408\n"
    "ds_read_b128 v[52:55], v0 offset:1024\n"
    "ds_read_b128 v[92:95], v0 offset:5120\n"
    "ds_read_b128 v[108:111], v4 offset:20544\n"
    "ds_read_b128 v[40:43], v0 offset:18432\n"
    "ds_read_b128 v[56:59], v0 offset:2048\n"
    "ds_read_b128 v[96:99], v0 offset:6144\n"
    "ds_read_b128 v[112:115], v4 offset:20608\n"
    "s_waitcnt lgkmcnt(10)\n"
    "v_mfma_f32_16x16x32_bf16 v[120:123], v[48:51], v[32:35], 0\n"
    "s_barrier\n"
    "ds_read_b128 v[44:47], v0 offset:19456\n"
    "ds_read_b128 v[60:63], v0 offset:3072\n"
    "ds_read_b128 v[100:103], v0 offset:7168\n"
    "ds_read_b128 v[116:119], v4 offset:20672\n"
    "s_waitcnt lgkmcnt(13)\n"
    "v_mfma_f32_16x16x32_bf16 v[124:127], v[88:91], v[32:35], 0\n"
    "s_waitcnt lgkmcnt(12)\n"
    "v_mfma_f32_16x16x32_bf16 v[128:131], v[104:107], v[32:35], 0\n"
    "s_waitcnt lgkmcnt(10)\n"
    "v_mfma_f32_16x16x32_bf16 v[120:123], v[52:55], v[36:39], v[120:123]\n"
    "s_waitcnt lgkmcnt(9)\n"
    "v_mfma_f32_16x16x32_bf16 v[124:127], v[92:95], v[36:39], v[124:127]\n"
    "ds_read_b128 v[64:67], v10 offset:4096\n"
    "s_waitcnt lgkmcnt(9)\n"
    "v_mfma_f32_16x16x32_bf16 v[128:131], v[108:111], v[36:39], v[128:131]\n"
    "ds_read_b128 v[68:71], v10 offset:8192\n"
    "s_waitcnt lgkmcnt(8)\n"
    "v_mfma_f32_16x16x32_bf16 v[120:123], v[56:59], v[40:43], v[120:123]\n"
    "ds_read_b128 v[72:75], v10 offset:5120\n"
    "s_waitcnt lgkmcnt(8)\n"
    "v_mfma_f32_16x16x32_bf16 v[124:127], v[96:99], v[40:43], v[124:127]\n"
    "ds_read_b128 v[80:83], v10 offset:9216\n"
    "s_waitcnt lgkmcnt(8)\n"
    "v_mfma_f32_16x16x32_bf16 v[128:131], v[112:115], v[40:43], v[128:131]\n"
    "ds_read_b128 v[84:87], v11 offset:31888\n"
    "s_waitcnt lgkmcnt(7)\n"
    "v_mfma_f32_16x16x32_bf16 v[120:123], v[60:63], v[44:47], v[120:123]\n"
    "ds_read_b64 v[134:135], v11 offset:31904\n"
    "s_waitcnt lgkmcnt(7)\n"
    "v_mfma_f32_16x16x32_bf16 v[124:127], v[100:103], v[44:47], v[124:127]\n"
    "ds_read_b128 v[88:91], v10 offset:18688\n"
    "s_waitcnt lgkmcnt(7)\n"
    "v_mfma_f32_16x16x32_bf16 v[128:131], v[116:119], v[44:47], v[128:131]\n"
    "ds_read_b128 v[92:95], v10 offset:19712\n"
    "v_mov_b32 v132, 0xd01502f9\n"
    "v_cndmask_b32 v120, v132, v120, s[76:77]\n"
    "v_cndmask_b32 v121, v132, v121, s[78:79]\n"
    "v_cndmask_b32 v122, v132, v122, s[80:81]\n"
    "v_cndmask_b32 v123, v132, v123, s[82:83]\n"
    "v_cndmask_b32 v124, v132, v124, s[84:85]\n"
    "v_cndmask_b32 v125, v132, v125, s[86:87]\n"
    "v_cndmask_b32 v126, v132, v126, s[88:89]\n"
    "v_cndmask_b32 v127, v132, v127, s[90:91]\n"
    "v_maximum3_f32 v133, v128, v129, v120\n"
    "v_maximum3_f32 v133, v133, v121, v122\n"
    "v_maximum3_f32 v133, v133, v123, v124\n"
    "v_maximum3_f32 v133, v133, v125, v126\n"
    "v_maximum3_f32 v133, v133, v127, v127\n"
    "v_mov_b32 v132, v133\n"
    "s_nop 1\n"
    "v_permlane16_swap_b32 v132, v133\n"
    "v_maximum3_f32 v133, v133, v132, v132\n"
    "v_mov_b32 v132, v133\n"
    "s_nop 1\n"
    "v_permlane32_swap_b32 v132, v133\n"
    "v_maximum3_f32 v133, v133, v132, v132\n"
    "v_mul_f32 v133, 0xbe0293ee, v133\n"
    "v_fmamk_f32 v120, v120, 0x3e0293ee, v133\n"
    "v_fmamk_f32 v121, v121, 0x3e0293ee, v133\n"
    "v_fmamk_f32 v122, v122, 0x3e0293ee, v133\n"
    "v_fmamk_f32 v123, v123, 0x3e0293ee, v133\n"
    "v_fmamk_f32 v124, v124, 0x3e0293ee, v133\n"
    "v_fmamk_f32 v125, v125, 0x3e0293ee, v133\n"
    "v_fmamk_f32 v126, v126, 0x3e0293ee, v133\n"
    "v_fmamk_f32 v127, v127, 0x3e0293ee, v133\n"
    "v_fmamk_f32 v128, v128, 0x3e0293ee, v133\n"
    "v_fmamk_f32 v129, v129, 0x3e0293ee, v133\n"
    "v_exp_f32 v120, v120\n"
    "v_exp_f32 v121, v121\n"
    "v_exp_f32 v122, v122\n"
    "v_exp_f32 v123, v123\n"
    "v_exp_f32 v124, v124\n"
    "v_exp_f32 v125, v125\n"
    "v_exp_f32 v126, v126\n"
    "v_exp_f32 v127, v127\n"
    "v_exp_f32 v128, v128\n"
    "v_exp_f32 v129, v129\n"
    "v_cvt_pk_bf16_f32 v120, v120, v121\n"
    "v_cvt_pk_bf16_f32 v121, v122, v123\n"
    "v_cvt_pk_bf16_f32 v122, v124, v125\n"
    "v_cvt_pk_bf16_f32 v123, v126, v127\n"
    "v_cvt_pk_bf16_f32 v124, v128, v129\n"
    "v_cvt_pk_bf16_f32 v125, v128, v129\n"
    "s_waitcnt lgkmcnt(3)\n"
    "v_mfma_f32_16x16x32_bf16 v[100:103], v[84:87], v[120:123], 0\n"
    "v_mfma_f32_16x16x32_bf16 v[104:107], v[64:67], v[120:123], 0\n"
    "v_mfma_f32_16x16x32_bf16 v[108:111], v[68:71], v[120:123], 0\n"
    "s_waitcnt lgkmcnt(2)\n"
    "v_mfma_f32_16x16x16_bf16 v[100:103], v[134:135], v[124:125], v[100:103]\n"
    "v_mfma_f32_16x16x32_bf16 v[112:115], v[72:75], v[120:123], 0\n"
    "v_mfma_f32_16x16x32_bf16 v[116:119], v[80:83], v[120:123], 0\n"
    "s_waitcnt lgkmcnt(1)\n"
    "v_mfma_f32_16x16x16_bf16 v[104:107], v[88:89], v[124:125], v[104:107]\n"
    "v_mfma_f32_16x16x16_bf16 v[108:111], v[90:91], v[124:125], v[108:111]\n"
    "v_rcp_f32 v132, v100\n"
    "s_waitcnt lgkmcnt(0)\n"
    "v_mfma_f32_16x16x16_bf16 v[112:115], v[92:93], v[124:125], v[112:115]\n"
    "v_mfma_f32_16x16x16_bf16 v[116:119], v[94:95], v[124:125], v[116:119]\n"
    "s_nop 1\n"
    "v_pk_mul_f32 v[104:105], v[104:105], v[132:133] op_sel_hi:[1,0]\n"
    "v_pk_mul_f32 v[106:107], v[106:107], v[132:133] op_sel_hi:[1,0]\n"
    "v_pk_mul_f32 v[108:109], v[108:109], v[132:133] op_sel_hi:[1,0]\n"
    "v_pk_mul_f32 v[110:111], v[110:111], v[132:133] op_sel_hi:[1,0]\n"
    "v_cvt_pk_bf16_f32 v104, v104, v108\n"
    "v_cvt_pk_bf16_f32 v105, v105, v109\n"
    "v_cvt_pk_bf16_f32 v106, v106, v110\n"
    "v_cvt_pk_bf16_f32 v107, v107, v111\n"
    "v_pk_mul_f32 v[112:113], v[112:113], v[132:133] op_sel_hi:[1,0]\n"
    "v_pk_mul_f32 v[114:115], v[114:115], v[132:133] op_sel_hi:[1,0]\n"
    "v_pk_mul_f32 v[116:117], v[116:117], v[132:133] op_sel_hi:[1,0]\n"
    "v_pk_mul_f32 v[118:119], v[118:119], v[132:133] op_sel_hi:[1,0]\n"
    "v_cvt_pk_bf16_f32 v112, v112, v116\n"
    "v_cvt_pk_bf16_f32 v113, v113, v117\n"
    "v_cvt_pk_bf16_f32 v114, v114, v118\n"
    "v_cvt_pk_bf16_f32 v115, v115, v119\n"
    "s_lshr_b32 s51, s48, 2\n"
    "s_xor_b32 s51, s51, s69\n"
    "s_and_b32 s51, s51, 1\n"
    "s_lshl_b32 s51, s51, 13\n"
    "s_lshl_b32 s73, s48, 19\n"
    "s_add_u32 s73, s73, s75\n"
    "s_add_u32 s73, s73, s51\n"
    "buffer_store_dwordx4 v[104:107], v10, s[40:43], s73 offen sc1\n"
    "buffer_store_dwordx4 v[112:115], v10, s[40:43], s73 offen offset:1024 sc1\n"
    "s_add_u32 s73, s48, 0\n"
    "s_and_b32 s51, s73, 32\n"
    "s_and_b32 s73, s73, 31\n"
    "s_lshl3_add_u32 s73, s51, s73\n"
    "s_lshl2_add_u32 s73, s73, s93\n"
    "v_mov_b32 v100, s73\n"
    "v_mov_b32 v101, 1\n"
    "s_waitcnt vmcnt(0)\n"
    "s_mov_b64 exec, 1\n"
    "ds_add_rtn_u32 v101, v11, v101 offset:31872\n"
    "s_mov_b64 exec, -1\n"
    "s_waitcnt lgkmcnt(0)\n"
    "v_readfirstlane_b32 s73, v101\n"
    "v_mov_b32 v101, 1\n"
    "s_bitcmp1_b32 s73, 0\n"
    "s_cbranch_scc0 .Lout_%=\n"
    "s_mov_b64 exec, 1\n"
    "global_store_dword v100, v101, %1 sc1\n"
    "s_mov_b64 exec, -1\n"
    "s_cmp_eq_u32 %3, 128\n"
    "s_cbranch_scc0 .Lout_%=\n"
    "v_mov_b32 v130, 0x20400\n"
    "global_load_dwordx2 v[132:133], v130, %1 sc1\n"
    "v_mbcnt_lo_u32_b32 v131, -1, 0\n"
    "v_min_u32 v131, 7, v131\n"
    "v_lshlrev_b32 v131, 7, v131\n"
    "v_add_u32 v131, 0x20000, v131\n"
    "global_load_dwordx2 v[134:135], v131, %1 sc1\n"
    "s_waitcnt vmcnt(0)\n"
    "v_add_u32 v132, 1, v132\n"
    "v_add_u32 v133, 1, v133\n"
    "global_store_dwordx2 v130, v[132:133], %1 sc1\n"
    ".Lout_%=:\n"
    "s_waitcnt vmcnt(0)\n"
    "s_endpgm\n"
    ".p2align 6\n"
    ".Lrole1_%=:\n"
    "s_mov_b64 s[40:41], %0\n"
    "s_and_b32 s41, s41, 0xffff\n"
    "s_mov_b32 s42, 0xc000000\n"
    "s_mov_b32 s43, 0x20000\n"
    "s_mov_b64 s[92:93], %0\n"
    "s_and_b32 s93, s93, 0xffff\n"
    "s_mov_b32 s94, 0xc000000\n"
    "s_mov_b32 s95, 0x20000\n"
    "s_mov_b32 s53, 131072\n"
    "s_mov_b64 s[56:57], 0\n"
    "v_mbcnt_lo_u32_b32 v7, -1, 0\n"
    "v_mbcnt_hi_u32_b32 v7, -1, v7\n"
    "s_lshl_b32 s50, %2, 8\n"
    "s_add_u32 s50, s50, 147456\n"
    "v_lshl_add_u32 v7, v7, 2, s50\n"
    "ds_read_b32 v1, v7\n"
    "ds_read_b32 v2, v7 offset:1024\n"
    "ds_read_b32 v3, v7 offset:2048\n"
    "ds_read_b32 v5, v7 offset:3072\n"
    "ds_read_b32 v48, v7 offset:4096\n"
    "ds_read_b32 v49, v7 offset:5120\n"
    "ds_read_b32 v50, v7 offset:6144\n"
    "s_waitcnt lgkmcnt(0)\n"
    "s_barrier\n"
    "s_movk_i32 s86, 255\n"
    "s_lshl_b32 s54, %2, 10\n"
    "s_lshl_b32 s58, %2, 13\n"
    "s_add_u32 s58, s58, 65536\n"
    "s_add_u32 s88, s40, 32768\n"
    "s_mov_b32 s89, s41\n"
    "s_cmp_lt_u32 %2, 2\n"
    "s_cselect_b32 s96, s42, 0\n"
    "s_mov_b32 s91, s43\n"
    "s_mul_i32 s59, %2, 576\n"
    "s_add_u32 s59, s59, 68608\n"
    "s_bfm_b64 s[60:61], 36, 0\n"
    "v_mov_b32 v72, 0x3f803f80\n"
    "v_mov_b32 v73, 0x3f803f80\n"
    "v_mov_b32 v74, 0x3f803f80\n"
    "v_mov_b32 v75, 0x3f803f80\n"
    "s_mov_b64 s[44:45], %8\n"
    "s_and_b32 s45, s45, 0xffff\n"
    "s_mov_b32 s46, 0x480000\n"
    "s_mov_b32 s47, 0x20000\n"
    "s_add_u32 m0, s54, 0\n"
    "s_add_u32 s55, %6, 0\n"
    "buffer_load_dwordx4 v48, s[44:47], s55 offen lds\n"
    "s_add_u32 m0, s54, 4096\n"
    "s_add_u32 s55, %6, 1048576\n"
    "buffer_load_dwordx4 v48, s[44:47], s55 offen lds\n"
    "s_add_u32 m0, s54, 8192\n"
    "s_add_u32 s55, %6, 2097152\n"
    "buffer_load_dwordx4 v48, s[44:47], s55 offen lds\n"
    "s_add_u32 m0, s54, 12288\n"
    "s_add_u32 s55, %6, 3145728\n"
    "buffer_load_dwordx4 v48, s[44:47], s55 offen lds\n"
    "s_add_u32 m0, s54, 16384\n"
    "s_add_u32 s55, %6, 32768\n"
    "buffer_load_dwordx4 v48, s[44:47], s55 offen lds\n"
    "s_add_u32 m0, s54, 20480\n"
    "s_add_u32 s55, %6, 1081344\n"
    "buffer_load_dwordx4 v48, s[44:47], s55 offen lds\n"
    "s_add_u32 m0, s54, 24576\n"
    "s_add_u32 s55, %6, 2129920\n"
    "buffer_load_dwordx4 v48, s[44:47], s55 offen lds\n"
    "s_add_u32 m0, s54, 28672\n"
    "s_add_u32 s55, %6, 3178496\n"
    "buffer_load_dwordx4 v48, s[44:47], s55 offen lds\n"
    "s_add_u32 m0, s54, 32768\n"
    "s_add_u32 s55, %6, 262144\n"
    "buffer_load_dwordx4 v48, s[44:47], s55 offen lds\n"
    "s_add_u32 m0, s54, 36864\n"
    "s_add_u32 s55, %6, 1310720\n"
    "buffer_load_dwordx4 v48, s[44:47], s55 offen lds\n"
    "s_add_u32 m0, s54, 40960\n"
    "s_add_u32 s55, %6, 2359296\n"
    "buffer_load_dwordx4 v48, s[44:47], s55 offen lds\n"
    "s_add_u32 m0, s54, 45056\n"
    "s_add_u32 s55, %6, 3407872\n"
    "buffer_load_dwordx4 v48, s[44:47], s55 offen lds\n"
    "s_add_u32 m0, s54, 49152\n"
    "s_add_u32 s55, %6, 294912\n"
    "buffer_load_dwordx4 v48, s[44:47], s55 offen lds\n"
    "s_add_u32 m0, s54, 53248\n"
    "s_add_u32 s55, %6, 1343488\n"
    "buffer_load_dwordx4 v48, s[44:47], s55 offen lds\n"
    "s_add_u32 m0, s54, 57344\n"
    "s_add_u32 s55, %6, 2392064\n"
    "buffer_load_dwordx4 v48, s[44:47], s55 offen lds\n"
    "s_add_u32 m0, s54, 61440\n"
    "s_add_u32 s55, %6, 3440640\n"
    "buffer_load_dwordx4 v48, s[44:47], s55 offen lds\n"
    "s_add_u32 m0, s54, 65536\n"
    "s_add_u32 s55, %6, 524288\n"
    "buffer_load_dwordx4 v48, s[44:47], s55 offen lds\n"
    "s_add_u32 m0, s54, 69632\n"
    "s_add_u32 s55, %6, 1572864\n"
    "buffer_load_dwordx4 v48, s[44:47], s55 offen lds\n"
    "s_add_u32 m0, s54, 73728\n"
    "s_add_u32 s55, %6, 2621440\n"
    "buffer_load_dwordx4 v48, s[44:47], s55 offen lds\n"
    "s_add_u32 m0, s54, 77824\n"
    "s_add_u32 s55, %6, 3670016\n"
    "buffer_load_dwordx4 v48, s[44:47], s55 offen lds\n"
    "s_add_u32 m0, s54, 81920\n"
    "s_add_u32 s55, %6, 557056\n"
    "buffer_load_dwordx4 v48, s[44:47], s55 offen lds\n"
    "s_add_u32 m0, s54, 86016\n"
    "s_add_u32 s55, %6, 1605632\n"
    "buffer_load_dwordx4 v48, s[44:47], s55 offen lds\n"
    "s_add_u32 m0, s54, 90112\n"
    "s_add_u32 s55, %6, 2654208\n"
    "buffer_load_dwordx4 v48, s[44:47], s55 offen lds\n"
    "s_add_u32 m0, s54, 94208\n"
    "s_add_u32 s55, %6, 3702784\n"
    "buffer_load_dwordx4 v48, s[44:47], s55 offen lds\n"
    "s_add_u32 m0, s54, 98304\n"
    "s_add_u32 s55, %6, 786432\n"
    "buffer_load_dwordx4 v48, s[44:47], s55 offen lds\n"
    "s_add_u32 m0, s54, 102400\n"
    "s_add_u32 s55, %6, 1835008\n"
    "buffer_load_dwordx4 v48, s[44:47], s55 offen lds\n"
    "s_add_u32 m0, s54, 106496\n"
    "s_add_u32 s55, %6, 2883584\n"
    "buffer_load_dwordx4 v48, s[44:47], s55 offen lds\n"
    "s_add_u32 m0, s54, 110592\n"
    "s_add_u32 s55, %6, 3932160\n"
    "buffer_load_dwordx4 v48, s[44:47], s55 offen lds\n"
    "s_add_u32 m0, s54, 114688\n"
    "s_add_u32 s55, %6, 819200\n"
    "buffer_load_dwordx4 v48, s[44:47], s55 offen lds\n"
    "s_add_u32 m0, s54, 118784\n"
    "s_add_u32 s55, %6, 1867776\n"
    "buffer_load_dwordx4 v48, s[44:47], s55 offen lds\n"
    "s_add_u32 m0, s54, 122880\n"
    "s_add_u32 s55, %6, 2916352\n"
    "buffer_load_dwordx4 v48, s[44:47], s55 offen lds\n"
    "s_add_u32 m0, s54, 126976\n"
    "s_add_u32 s55, %6, 3964928\n"
    "buffer_load_dwordx4 v48, s[44:47], s55 offen lds\n"
    "s_add_u32 m0, s54, 131072\n"
    "s_add_u32 s55, %6, 4194304\n"
    "buffer_load_dwordx4 v48, s[44:47], s55 offen lds\n"
    "s_add_u32 m0, s54, 135168\n"
    "s_add_u32 s55, %6, 4227072\n"
    "buffer_load_dwordx4 v48, s[44:47], s55 offen lds\n"
    "s_add_u32 m0, s54, 139264\n"
    "s_add_u32 s55, %6, 4456448\n"
    "buffer_load_dwordx4 v48, s[44:47], s55 offen lds\n"
    "s_add_u32 m0, s54, 143360\n"
    "s_add_u32 s55, %6, 4489216\n"
    "buffer_load_dwordx4 v48, s[44:47], s55 offen lds\n"
    "s_xor_b32 s50, %2, 0\n"
    "s_lshl_b32 s50, s50, 12\n"
    "v_add_u32 v32, s50, v49\n"
    "v_add_u32 v33, s50, v50\n"
    "s_xor_b32 s50, %2, 1\n"
    "s_lshl_b32 s50, s50, 12\n"
    "v_add_u32 v34, s50, v49\n"
    "v_add_u32 v35, s50, v50\n"
    "s_xor_b32 s50, %2, 2\n"
    "s_lshl_b32 s50, s50, 12\n"
    "v_add_u32 v36, s50, v49\n"
    "v_add_u32 v37, s50, v50\n"
    "s_xor_b32 s50, %2, 3\n"
    "s_lshl_b32 s50, s50, 12\n"
    "v_add_u32 v38, s50, v49\n"
    "v_add_u32 v39, s50, v50\n"
    "s_waitcnt vmcnt(32) lgkmcnt(0)\n"
    "s_barrier\n"
    "ds_read_b64_tr_b16 a[0:1], v32\n"
    "ds_read_b64_tr_b16 a[4:5], v33\n"
    "ds_read_b64_tr_b16 a[32:33], v34\n"
    "ds_read_b64_tr_b16 a[36:37], v35\n"
    "ds_read_b64_tr_b16 a[64:65], v36\n"
    "ds_read_b64_tr_b16 a[68:69], v37\n"
    "ds_read_b64_tr_b16 a[96:97], v38\n"
    "ds_read_b64_tr_b16 a[100:101], v39\n"
    "s_waitcnt vmcnt(28) lgkmcnt(0)\n"
    "s_barrier\n"
    "s_mov_b64 s[44:45], %9\n"
    "s_and_b32 s45, s45, 0xffff\n"
    "s_mov_b32 s46, 0x480000\n"
    "s_mov_b32 s47, 0x20000\n"
    "s_add_u32 m0, s54, 0\n"
    "s_add_u32 s55, %6, 0\n"
    "buffer_load_dwordx4 v48, s[44:47], s55 offen lds\n"
    "s_add_u32 m0, s54, 4096\n"
    "s_add_u32 s55, %6, 1048576\n"
    "buffer_load_dwordx4 v48, s[44:47], s55 offen lds\n"
    "s_add_u32 m0, s54, 8192\n"
    "s_add_u32 s55, %6, 2097152\n"
    "buffer_load_dwordx4 v48, s[44:47], s55 offen lds\n"
    "s_add_u32 m0, s54, 12288\n"
    "s_add_u32 s55, %6, 3145728\n"
    "buffer_load_dwordx4 v48, s[44:47], s55 offen lds\n"
    "ds_read_b64_tr_b16 a[2:3], v32 offset:16384\n"
    "ds_read_b64_tr_b16 a[6:7], v33 offset:16384\n"
    "ds_read_b64_tr_b16 a[34:35], v34 offset:16384\n"
    "ds_read_b64_tr_b16 a[38:39], v35 offset:16384\n"
    "ds_read_b64_tr_b16 a[66:67], v36 offset:16384\n"
    "ds_read_b64_tr_b16 a[70:71], v37 offset:16384\n"
    "ds_read_b64_tr_b16 a[98:99], v38 offset:16384\n"
    "ds_read_b64_tr_b16 a[102:103], v39 offset:16384\n"
    "s_waitcnt vmcnt(28) lgkmcnt(0)\n"
    "s_barrier\n"
    "s_add_u32 m0, s54, 16384\n"
    "s_add_u32 s55, %6, 32768\n"
    "buffer_load_dwordx4 v48, s[44:47], s55 offen lds\n"
    "s_add_u32 m0, s54, 20480\n"
    "s_add_u32 s55, %6, 1081344\n"
    "buffer_load_dwordx4 v48, s[44:47], s55 offen lds\n"
    "s_add_u32 m0, s54, 24576\n"
    "s_add_u32 s55, %6, 2129920\n"
    "buffer_load_dwordx4 v48, s[44:47], s55 offen lds\n"
    "s_add_u32 m0, s54, 28672\n"
    "s_add_u32 s55, %6, 3178496\n"
    "buffer_load_dwordx4 v48, s[44:47], s55 offen lds\n"
    "v_mfma_f32_16x16x32_bf16 v[8:11], a[0:3], v[72:75], 0\n"
    "v_mfma_f32_16x16x32_bf16 v[12:15], a[4:7], v[72:75], 0\n"
    "v_mfma_f32_16x16x32_bf16 v[8:11], a[32:35], v[72:75], v[8:11]\n"
    "v_mfma_f32_16x16x32_bf16 v[12:15], a[36:39], v[72:75], v[12:15]\n"
    "v_mfma_f32_16x16x32_bf16 v[8:11], a[64:67], v[72:75], v[8:11]\n"
    "v_mfma_f32_16x16x32_bf16 v[12:15], a[68:71], v[72:75], v[12:15]\n"
    "v_mfma_f32_16x16x32_bf16 v[8:11], a[96:99], v[72:75], v[8:11]\n"
    "v_mfma_f32_16x16x32_bf16 v[12:15], a[100:103], v[72:75], v[12:15]\n"
    "ds_read_b64_tr_b16 a[8:9], v32 offset:32768\n"
    "ds_read_b64_tr_b16 a[12:13], v33 offset:32768\n"
    "ds_read_b64_tr_b16 a[40:41], v34 offset:32768\n"
    "ds_read_b64_tr_b16 a[44:45], v35 offset:32768\n"
    "ds_read_b64_tr_b16 a[72:73], v36 offset:32768\n"
    "ds_read_b64_tr_b16 a[76:77], v37 offset:32768\n"
    "ds_read_b64_tr_b16 a[104:105], v38 offset:32768\n"
    "ds_read_b64_tr_b16 a[108:109], v39 offset:32768\n"
    "s_waitcnt vmcnt(28) lgkmcnt(0)\n"
    "s_barrier\n"
    "s_add_u32 m0, s54, 32768\n"
    "s_add_u32 s55, %6, 262144\n"
    "buffer_load_dwordx4 v48, s[44:47], s55 offen lds\n"
    "s_add_u32 m0, s54, 36864\n"
    "s_add_u32 s55, %6, 1310720\n"
    "buffer_load_dwordx4 v48, s[44:47], s55 offen lds\n"
    "s_add_u32 m0, s54, 40960\n"
    "s_add_u32 s55, %6, 2359296\n"
    "buffer_load_dwordx4 v48, s[44:47], s55 offen lds\n"
    "s_add_u32 m0, s54, 45056\n"
    "s_add_u32 s55, %6, 3407872\n"
    "buffer_load_dwordx4 v48, s[44:47], s55 offen lds\n"
    "ds_read_b64_tr_b16 a[10:11], v32 offset:49152\n"
    "ds_read_b64_tr_b16 a[14:15], v33 offset:49152\n"
    "ds_read_b64_tr_b16 a[42:43], v34 offset:49152\n"
    "ds_read_b64_tr_b16 a[46:47], v35 offset:49152\n"
    "ds_read_b64_tr_b16 a[74:75], v36 offset:49152\n"
    "ds_read_b64_tr_b16 a[78:79], v37 offset:49152\n"
    "ds_read_b64_tr_b16 a[106:107], v38 offset:49152\n"
    "ds_read_b64_tr_b16 a[110:111], v39 offset:49152\n"
    "s_waitcnt vmcnt(28) lgkmcnt(0)\n"
    "s_barrier\n"
    "s_add_u32 m0, s54, 49152\n"
    "s_add_u32 s55, %6, 294912\n"
    "buffer_load_dwordx4 v48, s[44:47], s55 offen lds\n"
    "s_add_u32 m0, s54, 53248\n"
    "s_add_u32 s55, %6, 1343488\n"
    "buffer_load_dwordx4 v48, s[44:47], s55 offen lds\n"
    "s_add_u32 m0, s54, 57344\n"
    "s_add_u32 s55, %6, 2392064\n"
    "buffer_load_dwordx4 v48, s[44:47], s55 offen lds\n"
    "s_add_u32 m0, s54, 61440\n"
    "s_add_u32 s55, %6, 3440640\n"
    "buffer_load_dwordx4 v48, s[44:47], s55 offen lds\n"
    "v_mfma_f32_16x16x32_bf16 v[8:11], a[8:11], v[72:75], v[8:11]\n"
    "v_mfma_f32_16x16x32_bf16 v[12:15], a[12:15], v[72:75], v[12:15]\n"
    "v_mfma_f32_16x16x32_bf16 v[8:11], a[40:43], v[72:75], v[8:11]\n"
    "v_mfma_f32_16x16x32_bf16 v[12:15], a[44:47], v[72:75], v[12:15]\n"
    "v_mfma_f32_16x16x32_bf16 v[8:11], a[72:75], v[72:75], v[8:11]\n"
    "v_mfma_f32_16x16x32_bf16 v[12:15], a[76:79], v[72:75], v[12:15]\n"
    "v_mfma_f32_16x16x32_bf16 v[8:11], a[104:107], v[72:75], v[8:11]\n"
    "v_mfma_f32_16x16x32_bf16 v[12:15], a[108:111], v[72:75], v[12:15]\n"
    "v_add_u32 v71, 0x10000, v32\n"
    "ds_read_b64_tr_b16 a[16:17], v71\n"
    "v_add_u32 v70, 0x10000, v33\n"
    "ds_read_b64_tr_b16 a[20:21], v70\n"
    "v_add_u32 v69, 0x10000, v34\n"
    "ds_read_b64_tr_b16 a[48:49], v69\n"
    "v_add_u32 v68, 0x10000, v35\n"
    "ds_read_b64_tr_b16 a[52:53], v68\n"
    "v_add_u32 v67, 0x10000, v36\n"
    "ds_read_b64_tr_b16 a[80:81], v67\n"
    "v_add_u32 v66, 0x10000, v37\n"
    "ds_read_b64_tr_b16 a[84:85], v66\n"
    "v_add_u32 v65, 0x10000, v38\n"
    "ds_read_b64_tr_b16 a[112:113], v65\n"
    "v_add_u32 v64, 0x10000, v39\n"
    "ds_read_b64_tr_b16 a[116:117], v64\n"
    "s_waitcnt vmcnt(28) lgkmcnt(0)\n"
    "s_barrier\n"
    "s_add_u32 m0, s54, 65536\n"
    "s_add_u32 s55, %6, 524288\n"
    "buffer_load_dwordx4 v48, s[44:47], s55 offen lds\n"
    "s_add_u32 m0, s54, 69632\n"
    "s_add_u32 s55, %6, 1572864\n"
    "buffer_load_dwordx4 v48, s[44:47], s55 offen lds\n"
    "s_add_u32 m0, s54, 73728\n"
    "s_add_u32 s55, %6, 2621440\n"
    "buffer_load_dwordx4 v48, s[44:47], s55 offen lds\n"
    "s_add_u32 m0, s54, 77824\n"
    "s_add_u32 s55, %6, 3670016\n"
    "buffer_load_dwordx4 v48, s[44:47], s55 offen lds\n"
    "ds_read_b64_tr_b16 a[18:19], v71 offset:16384\n"
    "ds_read_b64_tr_b16 a[22:23], v70 offset:16384\n"
    "ds_read_b64_tr_b16 a[50:51], v69 offset:16384\n"
    "ds_read_b64_tr_b16 a[54:55], v68 offset:16384\n"
    "ds_read_b64_tr_b16 a[82:83], v67 offset:16384\n"
    "ds_read_b64_tr_b16 a[86:87], v66 offset:16384\n"
    "ds_read_b64_tr_b16 a[114:115], v65 offset:16384\n"
    "ds_read_b64_tr_b16 a[118:119], v64 offset:16384\n"
    "s_waitcnt vmcnt(28) lgkmcnt(0)\n"
    "s_barrier\n"
    "s_add_u32 m0, s54, 81920\n"
    "s_add_u32 s55, %6, 557056\n"
    "buffer_load_dwordx4 v48, s[44:47], s55 offen lds\n"
    "s_add_u32 m0, s54, 86016\n"
    "s_add_u32 s55, %6, 1605632\n"
    "buffer_load_dwordx4 v48, s[44:47], s55 offen lds\n"
    "s_add_u32 m0, s54, 90112\n"
    "s_add_u32 s55, %6, 2654208\n"
    "buffer_load_dwordx4 v48, s[44:47], s55 offen lds\n"
    "s_add_u32 m0, s54, 94208\n"
    "s_add_u32 s55, %6, 3702784\n"
    "buffer_load_dwordx4 v48, s[44:47], s55 offen lds\n"
    "v_mfma_f32_16x16x32_bf16 v[8:11], a[16:19], v[72:75], v[8:11]\n"
    "v_mfma_f32_16x16x32_bf16 v[12:15], a[20:23], v[72:75], v[12:15]\n"
    "v_mfma_f32_16x16x32_bf16 v[8:11], a[48:51], v[72:75], v[8:11]\n"
    "v_mfma_f32_16x16x32_bf16 v[12:15], a[52:55], v[72:75], v[12:15]\n"
    "v_mfma_f32_16x16x32_bf16 v[8:11], a[80:83], v[72:75], v[8:11]\n"
    "v_mfma_f32_16x16x32_bf16 v[12:15], a[84:87], v[72:75], v[12:15]\n"
    "v_mfma_f32_16x16x32_bf16 v[8:11], a[112:115], v[72:75], v[8:11]\n"
    "v_mfma_f32_16x16x32_bf16 v[12:15], a[116:119], v[72:75], v[12:15]\n"
    "ds_read_b64_tr_b16 a[24:25], v71 offset:32768\n"
    "ds_read_b64_tr_b16 a[28:29], v70 offset:32768\n"
    "ds_read_b64_tr_b16 a[56:57], v69 offset:32768\n"
    "ds_read_b64_tr_b16 a[60:61], v68 offset:32768\n"
    "ds_read_b64_tr_b16 a[88:89], v67 offset:32768\n"
    "ds_read_b64_tr_b16 a[92:93], v66 offset:32768\n"
    "ds_read_b64_tr_b16 a[120:121], v65 offset:32768\n"
    "ds_read_b64_tr_b16 a[124:125], v64 offset:32768\n"
    "s_waitcnt vmcnt(28) lgkmcnt(0)\n"
    "s_barrier\n"
    "s_add_u32 m0, s54, 98304\n"
    "s_add_u32 s55, %6, 786432\n"
    "buffer_load_dwordx4 v48, s[44:47], s55 offen lds\n"
    "s_add_u32 m0, s54, 102400\n"
    "s_add_u32 s55, %6, 1835008\n"
    "buffer_load_dwordx4 v48, s[44:47], s55 offen lds\n"
    "s_add_u32 m0, s54, 106496\n"
    "s_add_u32 s55, %6, 2883584\n"
    "buffer_load_dwordx4 v48, s[44:47], s55 offen lds\n"
    "s_add_u32 m0, s54, 110592\n"
    "s_add_u32 s55, %6, 3932160\n"
    "buffer_load_dwordx4 v48, s[44:47], s55 offen lds\n"
    "ds_read_b64_tr_b16 a[26:27], v71 offset:49152\n"
    "ds_read_b64_tr_b16 a[30:31], v70 offset:49152\n"
    "ds_read_b64_tr_b16 a[58:59], v69 offset:49152\n"
    "ds_read_b64_tr_b16 a[62:63], v68 offset:49152\n"
    "ds_read_b64_tr_b16 a[90:91], v67 offset:49152\n"
    "ds_read_b64_tr_b16 a[94:95], v66 offset:49152\n"
    "ds_read_b64_tr_b16 a[122:123], v65 offset:49152\n"
    "ds_read_b64_tr_b16 a[126:127], v64 offset:49152\n"
    "s_waitcnt vmcnt(28) lgkmcnt(0)\n"
    "s_barrier\n"
    "s_add_u32 m0, s54, 114688\n"
    "s_add_u32 s55, %6, 819200\n"
    "buffer_load_dwordx4 v48, s[44:47], s55 offen lds\n"
    "s_add_u32 m0, s54, 118784\n"
    "s_add_u32 s55, %6, 1867776\n"
    "buffer_load_dwordx4 v48, s[44:47], s55 offen lds\n"
    "s_add_u32 m0, s54, 122880\n"
    "s_add_u32 s55, %6, 2916352\n"
    "buffer_load_dwordx4 v48, s[44:47], s55 offen lds\n"
    "s_add_u32 m0, s54, 126976\n"
    "s_add_u32 s55, %6, 3964928\n"
    "buffer_load_dwordx4 v48, s[44:47], s55 offen lds\n"
    "v_mfma_f32_16x16x32_bf16 v[8:11], a[24:27], v[72:75], v[8:11]\n"
    "v_mfma_f32_16x16x32_bf16 v[12:15], a[28:31], v[72:75], v[12:15]\n"
    "v_mfma_f32_16x16x32_bf16 v[8:11], a[56:59], v[72:75], v[8:11]\n"
    "v_mfma_f32_16x16x32_bf16 v[12:15], a[60:63], v[72:75], v[12:15]\n"
    "v_mfma_f32_16x16x32_bf16 v[8:11], a[88:91], v[72:75], v[8:11]\n"
    "v_mfma_f32_16x16x32_bf16 v[12:15], a[92:95], v[72:75], v[12:15]\n"
    "v_mfma_f32_16x16x32_bf16 v[8:11], a[120:123], v[72:75], v[8:11]\n"
    "v_mfma_f32_16x16x32_bf16 v[12:15], a[124:127], v[72:75], v[12:15]\n"
    "v_add_u32 v63, 0x20000, v49\n"
    "ds_read_b64_tr_b16 a[128:129], v63\n"
    "v_add_u32 v62, 0x20000, v50\n"
    "ds_read_b64_tr_b16 a[132:133], v62\n"
    "ds_read_b64_tr_b16 a[130:131], v63 offset:4096\n"
    "ds_read_b64_tr_b16 a[134:135], v62 offset:4096\n"
    "ds_read_b64_tr_b16 a[136:137], v63 offset:8192\n"
    "ds_read_b64_tr_b16 a[140:141], v62 offset:8192\n"
    "ds_read_b64_tr_b16 a[138:139], v63 offset:12288\n"
    "ds_read_b64_tr_b16 a[142:143], v62 offset:12288\n"
    "s_waitcnt vmcnt(28) lgkmcnt(0)\n"
    "s_barrier\n"
    "s_add_u32 m0, s54, 131072\n"
    "s_add_u32 s55, %6, 4194304\n"
    "buffer_load_dwordx4 v48, s[44:47], s55 offen lds\n"
    "s_add_u32 m0, s54, 135168\n"
    "s_add_u32 s55, %6, 4227072\n"
    "buffer_load_dwordx4 v48, s[44:47], s55 offen lds\n"
    "s_add_u32 m0, s54, 139264\n"
    "s_add_u32 s55, %6, 4456448\n"
    "buffer_load_dwordx4 v48, s[44:47], s55 offen lds\n"
    "s_add_u32 m0, s54, 143360\n"
    "s_add_u32 s55, %6, 4489216\n"
    "buffer_load_dwordx4 v48, s[44:47], s55 offen lds\n"
    "v_mfma_f32_16x16x32_bf16 v[8:11], a[128:131], v[72:75], v[8:11]\n"
    "v_mfma_f32_16x16x32_bf16 v[12:15], a[132:135], v[72:75], v[12:15]\n"
    "v_mfma_f32_16x16x32_bf16 v[8:11], a[136:139], v[72:75], v[8:11]\n"
    "v_mfma_f32_16x16x32_bf16 v[12:15], a[140:143], v[72:75], v[12:15]\n"
    "ds_read_b64_tr_b16 a[144:145], v32\n"
    "ds_read_b64_tr_b16 a[148:149], v33\n"
    "ds_read_b64_tr_b16 a[176:177], v34\n"
    "ds_read_b64_tr_b16 a[180:181], v35\n"
    "ds_read_b64_tr_b16 a[208:209], v36\n"
    "ds_read_b64_tr_b16 a[212:213], v37\n"
    "ds_read_b64_tr_b16 a[240:241], v38\n"
    "ds_read_b64_tr_b16 a[244:245], v39\n"
    "s_waitcnt vmcnt(28) lgkmcnt(0)\n"
    "s_barrier\n"
    "s_mov_b64 s[44:45], %10\n"
    "s_and_b32 s45, s45, 0xffff\n"
    "s_mov_b32 s46, 0x480000\n"
    "s_mov_b32 s47, 0x20000\n"
    "s_add_u32 m0, s54, 0\n"
    "s_add_u32 s55, %6, 0\n"
    "buffer_load_dwordx4 v48, s[44:47], s55 offen lds\n"
    "s_add_u32 m0, s54, 4096\n"
    "s_add_u32 s55, %6, 1048576\n"
    "buffer_load_dwordx4 v48, s[44:47], s55 offen lds\n"
    "s_add_u32 m0, s54, 8192\n"
    "s_add_u32 s55, %6, 2097152\n"
    "buffer_load_dwordx4 v48, s[44:47], s55 offen lds\n"
    "s_add_u32 m0, s54, 12288\n"
    "s_add_u32 s55, %6, 3145728\n"
    "buffer_load_dwordx4 v48, s[44:47], s55 offen lds\n"
    "ds_read_b64_tr_b16 a[146:147], v32 offset:16384\n"
    "ds_read_b64_tr_b16 a[150:151], v33 offset:16384\n"
    "ds_read_b64_tr_b16 a[178:179], v34 offset:16384\n"
    "ds_read_b64_tr_b16 a[182:183], v35 offset:16384\n"
    "ds_read_b64_tr_b16 a[210:211], v36 offset:16384\n"
    "ds_read_b64_tr_b16 a[214:215], v37 offset:16384\n"
    "ds_read_b64_tr_b16 a[242:243], v38 offset:16384\n"
    "ds_read_b64_tr_b16 a[246:247], v39 offset:16384\n"
    "s_waitcnt vmcnt(28) lgkmcnt(0)\n"
    "s_barrier\n"
    "s_add_u32 m0, s54, 16384\n"
    "s_add_u32 s55, %6, 32768\n"
    "buffer_load_dwordx4 v48, s[44:47], s55 offen lds\n"
    "s_add_u32 m0, s54, 20480\n"
    "s_add_u32 s55, %6, 1081344\n"
    "buffer_load_dwordx4 v48, s[44:47], s55 offen lds\n"
    "s_add_u32 m0, s54, 24576\n"
    "s_add_u32 s55, %6, 2129920\n"
    "buffer_load_dwordx4 v48, s[44:47], s55 offen lds\n"
    "s_add_u32 m0, s54, 28672\n"
    "s_add_u32 s55, %6, 3178496\n"
    "buffer_load_dwordx4 v48, s[44:47], s55 offen lds\n"
    "v_mfma_f32_16x16x32_bf16 v[16:19], a[144:147], v[72:75], 0\n"
    "v_mfma_f32_16x16x32_bf16 v[20:23], a[148:151], v[72:75], 0\n"
    "v_mfma_f32_16x16x32_bf16 v[16:19], a[176:179], v[72:75], v[16:19]\n"
    "v_mfma_f32_16x16x32_bf16 v[20:23], a[180:183], v[72:75], v[20:23]\n"
    "v_mfma_f32_16x16x32_bf16 v[16:19], a[208:211], v[72:75], v[16:19]\n"
    "v_mfma_f32_16x16x32_bf16 v[20:23], a[212:215], v[72:75], v[20:23]\n"
    "v_mfma_f32_16x16x32_bf16 v[16:19], a[240:243], v[72:75], v[16:19]\n"
    "v_mfma_f32_16x16x32_bf16 v[20:23], a[244:247], v[72:75], v[20:23]\n"
    "ds_read_b64_tr_b16 a[152:153], v32 offset:32768\n"
    "ds_read_b64_tr_b16 a[156:157], v33 offset:32768\n"
    "ds_read_b64_tr_b16 a[184:185], v34 offset:32768\n"
    "ds_read_b64_tr_b16 a[188:189], v35 offset:32768\n"
    "ds_read_b64_tr_b16 a[216:217], v36 offset:32768\n"
    "ds_read_b64_tr_b16 a[220:221], v37 offset:32768\n"
    "ds_read_b64_tr_b16 a[248:249], v38 offset:32768\n"
    "ds_read_b64_tr_b16 a[252:253], v39 offset:32768\n"
    "s_waitcnt vmcnt(28) lgkmcnt(0)\n"
    "s_barrier\n"
    "s_add_u32 m0, s54, 32768\n"
    "s_add_u32 s55, %6, 262144\n"
    "buffer_load_dwordx4 v48, s[44:47], s55 offen lds\n"
    "s_add_u32 m0, s54, 36864\n"
    "s_add_u32 s55, %6, 1310720\n"
    "buffer_load_dwordx4 v48, s[44:47], s55 offen lds\n"
    "s_add_u32 m0, s54, 40960\n"
    "s_add_u32 s55, %6, 2359296\n"
    "buffer_load_dwordx4 v48, s[44:47], s55 offen lds\n"
    "s_add_u32 m0, s54, 45056\n"
    "s_add_u32 s55, %6, 3407872\n"
    "buffer_load_dwordx4 v48, s[44:47], s55 offen lds\n"
    "ds_read_b64_tr_b16 a[154:155], v32 offset:49152\n"
    "ds_read_b64_tr_b16 a[158:159], v33 offset:49152\n"
    "ds_read_b64_tr_b16 a[186:187], v34 offset:49152\n"
    "ds_read_b64_tr_b16 a[190:191], v35 offset:49152\n"
    "ds_read_b64_tr_b16 a[218:219], v36 offset:49152\n"
    "ds_read_b64_tr_b16 a[222:223], v37 offset:49152\n"
    "ds_read_b64_tr_b16 a[250:251], v38 offset:49152\n"
    "ds_read_b64_tr_b16 a[254:255], v39 offset:49152\n"
    "s_waitcnt vmcnt(28) lgkmcnt(0)\n"
    "s_barrier\n"
    "s_add_u32 m0, s54, 49152\n"
    "s_add_u32 s55, %6, 294912\n"
    "buffer_load_dwordx4 v48, s[44:47], s55 offen lds\n"
    "s_add_u32 m0, s54, 53248\n"
    "s_add_u32 s55, %6, 1343488\n"
    "buffer_load_dwordx4 v48, s[44:47], s55 offen lds\n"
    "s_add_u32 m0, s54, 57344\n"
    "s_add_u32 s55, %6, 2392064\n"
    "buffer_load_dwordx4 v48, s[44:47], s55 offen lds\n"
    "s_add_u32 m0, s54, 61440\n"
    "s_add_u32 s55, %6, 3440640\n"
    "buffer_load_dwordx4 v48, s[44:47], s55 offen lds\n"
    "v_mfma_f32_16x16x32_bf16 v[16:19], a[152:155], v[72:75], v[16:19]\n"
    "v_mfma_f32_16x16x32_bf16 v[20:23], a[156:159], v[72:75], v[20:23]\n"
    "v_mfma_f32_16x16x32_bf16 v[16:19], a[184:187], v[72:75], v[16:19]\n"
    "v_mfma_f32_16x16x32_bf16 v[20:23], a[188:191], v[72:75], v[20:23]\n"
    "v_mfma_f32_16x16x32_bf16 v[16:19], a[216:219], v[72:75], v[16:19]\n"
    "v_mfma_f32_16x16x32_bf16 v[20:23], a[220:223], v[72:75], v[20:23]\n"
    "v_mfma_f32_16x16x32_bf16 v[16:19], a[248:251], v[72:75], v[16:19]\n"
    "v_mfma_f32_16x16x32_bf16 v[20:23], a[252:255], v[72:75], v[20:23]\n"
    "ds_read_b64_tr_b16 a[160:161], v71\n"
    "ds_read_b64_tr_b16 a[164:165], v70\n"
    "ds_read_b64_tr_b16 a[192:193], v69\n"
    "ds_read_b64_tr_b16 a[196:197], v68\n"
    "ds_read_b64_tr_b16 a[224:225], v67\n"
    "ds_read_b64_tr_b16 a[228:229], v66\n"
    "ds_read_b64_tr_b16 v[84:85], v65\n"
    "ds_read_b64_tr_b16 v[88:89], v64\n"
    "s_waitcnt vmcnt(28) lgkmcnt(0)\n"
    "s_barrier\n"
    "s_add_u32 m0, s54, 65536\n"
    "s_add_u32 s55, %6, 524288\n"
    "buffer_load_dwordx4 v48, s[44:47], s55 offen lds\n"
    "s_add_u32 m0, s54, 69632\n"
    "s_add_u32 s55, %6, 1572864\n"
    "buffer_load_dwordx4 v48, s[44:47], s55 offen lds\n"
    "s_add_u32 m0, s54, 73728\n"
    "s_add_u32 s55, %6, 2621440\n"
    "buffer_load_dwordx4 v48, s[44:47], s55 offen lds\n"
    "s_add_u32 m0, s54, 77824\n"
    "s_add_u32 s55, %6, 3670016\n"
    "buffer_load_dwordx4 v48, s[44:47], s55 offen lds\n"
    "ds_read_b64_tr_b16 a[162:163], v71 offset:16384\n"
    "ds_read_b64_tr_b16 a[166:167], v70 offset:16384\n"
    "ds_read_b64_tr_b16 a[194:195], v69 offset:16384\n"
    "ds_read_b64_tr_b16 a[198:199], v68 offset:16384\n"
    "ds_read_b64_tr_b16 a[226:227], v67 offset:16384\n"
    "ds_read_b64_tr_b16 a[230:231], v66 offset:16384\n"
    "ds_read_b64_tr_b16 v[86:87], v65 offset:16384\n"
    "ds_read_b64_tr_b16 v[90:91], v64 offset:16384\n"
    "s_waitcnt vmcnt(28) lgkmcnt(0)\n"
    "s_barrier\n"
    "s_add_u32 m0, s54, 81920\n"
    "s_add_u32 s55, %6, 557056\n"
    "buffer_load_dwordx4 v48, s[44:47], s55 offen lds\n"
    "s_add_u32 m0, s54, 86016\n"
    "s_add_u32 s55, %6, 1605632\n"
    "buffer_load_dwordx4 v48, s[44:47], s55 offen lds\n"
    "s_add_u32 m0, s54, 90112\n"
    "s_add_u32 s55, %6, 2654208\n"
    "buffer_load_dwordx4 v48, s[44:47], s55 offen lds\n"
    "s_add_u32 m0, s54, 94208\n"
    "s_add_u32 s55, %6, 3702784\n"
    "buffer_load_dwordx4 v48, s[44:47], s55 offen lds\n"
    "v_mfma_f32_16x16x32_bf16 v[16:19], a[160:163], v[72:75], v[16:19]\n"
    "v_mfma_f32_16x16x32_bf16 v[20:23], a[164:167], v[72:75], v[20:23]\n"
    "v_mfma_f32_16x16x32_bf16 v[16:19], a[192:195], v[72:75], v[16:19]\n"
    "v_mfma_f32_16x16x32_bf16 v[20:23], a[196:199], v[72:75], v[20:23]\n"
    "v_mfma_f32_16x16x32_bf16 v[16:19], a[224:227], v[72:75], v[16:19]\n"
    "v_mfma_f32_16x16x32_bf16 v[20:23], a[228:231], v[72:75], v[20:23]\n"
    "v_mfma_f32_16x16x32_bf16 v[16:19], v[84:87], v[72:75], v[16:19]\n"
    "v_mfma_f32_16x16x32_bf16 v[20:23], v[88:91], v[72:75], v[20:23]\n"
    "ds_read_b64_tr_b16 a[168:169], v71 offset:32768\n"
    "ds_read_b64_tr_b16 a[172:173], v70 offset:32768\n"
    "ds_read_b64_tr_b16 a[200:201], v69 offset:32768\n"
    "ds_read_b64_tr_b16 a[204:205], v68 offset:32768\n"
    "ds_read_b64_tr_b16 a[232:233], v67 offset:32768\n"
    "ds_read_b64_tr_b16 a[236:237], v66 offset:32768\n"
    "ds_read_b64_tr_b16 v[92:93], v65 offset:32768\n"
    "ds_read_b64_tr_b16 v[96:97], v64 offset:32768\n"
    "s_waitcnt vmcnt(28) lgkmcnt(0)\n"
    "s_barrier\n"
    "s_add_u32 m0, s54, 98304\n"
    "s_add_u32 s55, %6, 786432\n"
    "buffer_load_dwordx4 v48, s[44:47], s55 offen lds\n"
    "s_add_u32 m0, s54, 102400\n"
    "s_add_u32 s55, %6, 1835008\n"
    "buffer_load_dwordx4 v48, s[44:47], s55 offen lds\n"
    "s_add_u32 m0, s54, 106496\n"
    "s_add_u32 s55, %6, 2883584\n"
    "buffer_load_dwordx4 v48, s[44:47], s55 offen lds\n"
    "s_add_u32 m0, s54, 110592\n"
    "s_add_u32 s55, %6, 3932160\n"
    "buffer_load_dwordx4 v48, s[44:47], s55 offen lds\n"
    "ds_read_b64_tr_b16 a[170:171], v71 offset:49152\n"
    "ds_read_b64_tr_b16 a[174:175], v70 offset:49152\n"
    "ds_read_b64_tr_b16 a[202:203], v69 offset:49152\n"
    "ds_read_b64_tr_b16 a[206:207], v68 offset:49152\n"
    "ds_read_b64_tr_b16 a[234:235], v67 offset:49152\n"
    "ds_read_b64_tr_b16 a[238:239], v66 offset:49152\n"
    "ds_read_b64_tr_b16 v[94:95], v65 offset:49152\n"
    "ds_read_b64_tr_b16 v[98:99], v64 offset:49152\n"
    "s_waitcnt vmcnt(28) lgkmcnt(0)\n"
    "s_barrier\n"
    "s_add_u32 m0, s54, 114688\n"
    "s_add_u32 s55, %6, 819200\n"
    "buffer_load_dwordx4 v48, s[44:47], s55 offen lds\n"
    "s_add_u32 m0, s54, 118784\n"
    "s_add_u32 s55, %6, 1867776\n"
    "buffer_load_dwordx4 v48, s[44:47], s55 offen lds\n"
    "s_add_u32 m0, s54, 122880\n"
    "s_add_u32 s55, %6, 2916352\n"
    "buffer_load_dwordx4 v48, s[44:47], s55 offen lds\n"
    "s_add_u32 m0, s54, 126976\n"
    "s_add_u32 s55, %6, 3964928\n"
    "buffer_load_dwordx4 v48, s[44:47], s55 offen lds\n"
    "v_mfma_f32_16x16x32_bf16 v[16:19], a[168:171], v[72:75], v[16:19]\n"
    "v_mfma_f32_16x16x32_bf16 v[20:23], a[172:175], v[72:75], v[20:23]\n"
    "v_mfma_f32_16x16x32_bf16 v[16:19], a[200:203], v[72:75], v[16:19]\n"
    "v_mfma_f32_16x16x32_bf16 v[20:23], a[204:207], v[72:75], v[20:23]\n"
    "v_mfma_f32_16x16x32_bf16 v[16:19], a[232:235], v[72:75], v[16:19]\n"
    "v_mfma_f32_16x16x32_bf16 v[20:23], a[236:239], v[72:75], v[20:23]\n"
    "v_mfma_f32_16x16x32_bf16 v[16:19], v[92:95], v[72:75], v[16:19]\n"
    "v_mfma_f32_16x16x32_bf16 v[20:23], v[96:99], v[72:75], v[20:23]\n"
    "ds_read_b64_tr_b16 v[100:101], v63\n"
    "ds_read_b64_tr_b16 v[104:105], v62\n"
    "ds_read_b64_tr_b16 v[102:103], v63 offset:4096\n"
    "ds_read_b64_tr_b16 v[106:107], v62 offset:4096\n"
    "ds_read_b64_tr_b16 v[108:109], v63 offset:8192\n"
    "ds_read_b64_tr_b16 v[112:113], v62 offset:8192\n"
    "ds_read_b64_tr_b16 v[110:111], v63 offset:12288\n"
    "ds_read_b64_tr_b16 v[114:115], v62 offset:12288\n"
    "s_waitcnt vmcnt(28) lgkmcnt(0)\n"
    "s_barrier\n"
    "s_add_u32 m0, s54, 131072\n"
    "s_add_u32 s55, %6, 4194304\n"
    "buffer_load_dwordx4 v48, s[44:47], s55 offen lds\n"
    "s_add_u32 m0, s54, 135168\n"
    "s_add_u32 s55, %6, 4227072\n"
    "buffer_load_dwordx4 v48, s[44:47], s55 offen lds\n"
    "s_add_u32 m0, s54, 139264\n"
    "s_add_u32 s55, %6, 4456448\n"
    "buffer_load_dwordx4 v48, s[44:47], s55 offen lds\n"
    "s_add_u32 m0, s54, 143360\n"
    "s_add_u32 s55, %6, 4489216\n"
    "buffer_load_dwordx4 v48, s[44:47], s55 offen lds\n"
    "v_mov_b32 v7, 0x20400\n"
    "global_load_dwordx2 v[80:81], v7, %1 sc1\n"
    "v_mbcnt_lo_u32_b32 v4, -1, 0\n"
    "v_min_u32 v4, 7, v4\n"
    "v_lshlrev_b32 v4, 7, v4\n"
    "v_add_u32 v4, 0x20000, v4\n"
    "global_load_dwordx2 v[82:83], v4, %1 sc1\n"
    "v_mfma_f32_16x16x32_bf16 v[16:19], v[100:103], v[72:75], v[16:19]\n"
    "v_mfma_f32_16x16x32_bf16 v[20:23], v[104:107], v[72:75], v[20:23]\n"
    "v_mfma_f32_16x16x32_bf16 v[16:19], v[108:111], v[72:75], v[16:19]\n"
    "v_mfma_f32_16x16x32_bf16 v[20:23], v[112:115], v[72:75], v[20:23]\n"
    "ds_read_b64_tr_b16 v[116:117], v32\n"
    "ds_read_b64_tr_b16 v[120:121], v33\n"
    "ds_read_b64_tr_b16 v[148:149], v34\n"
    "ds_read_b64_tr_b16 v[152:153], v35\n"
    "ds_read_b64_tr_b16 v[180:181], v36\n"
    "ds_read_b64_tr_b16 v[184:185], v37\n"
    "ds_read_b64_tr_b16 v[212:213], v38\n"
    "ds_read_b64_tr_b16 v[216:217], v39\n"
    "s_waitcnt vmcnt(30) lgkmcnt(0)\n"
    "s_barrier\n"
    "ds_read_b64_tr_b16 v[118:119], v32 offset:16384\n"
    "ds_read_b64_tr_b16 v[122:123], v33 offset:16384\n"
    "ds_read_b64_tr_b16 v[150:151], v34 offset:16384\n"
    "ds_read_b64_tr_b16 v[154:155], v35 offset:16384\n"
    "ds_read_b64_tr_b16 v[182:183], v36 offset:16384\n"
    "ds_read_b64_tr_b16 v[186:187], v37 offset:16384\n"
    "ds_read_b64_tr_b16 v[214:215], v38 offset:16384\n"
    "ds_read_b64_tr_b16 v[218:219], v39 offset:16384\n"
    "s_waitcnt vmcnt(26) lgkmcnt(0)\n"
    "s_barrier\n"
    "v_mfma_f32_16x16x32_bf16 v[24:27], v[72:75], v[116:119], 0\n"
    "v_mfma_f32_16x16x32_bf16 v[28:31], v[72:75], v[120:123], 0\n"
    "v_mfma_f32_16x16x32_bf16 v[24:27], v[72:75], v[148:151], v[24:27]\n"
    "v_mfma_f32_16x16x32_bf16 v[28:31], v[72:75], v[152:155], v[28:31]\n"
    "v_mfma_f32_16x16x32_bf16 v[24:27], v[72:75], v[180:183], v[24:27]\n"
    "v_mfma_f32_16x16x32_bf16 v[28:31], v[72:75], v[184:187], v[28:31]\n"
    "v_mfma_f32_16x16x32_bf16 v[24:27], v[72:75], v[212:215], v[24:27]\n"
    "v_mfma_f32_16x16x32_bf16 v[28:31], v[72:75], v[216:219], v[28:31]\n"
    "ds_read_b64_tr_b16 v[124:125], v32 offset:32768\n"
    "ds_read_b64_tr_b16 v[128:129], v33 offset:32768\n"
    "ds_read_b64_tr_b16 v[156:157], v34 offset:32768\n"
    "ds_read_b64_tr_b16 v[160:161], v35 offset:32768\n"
    "ds_read_b64_tr_b16 v[188:189], v36 offset:32768\n"
    "ds_read_b64_tr_b16 v[192:193], v37 offset:32768\n"
    "ds_read_b64_tr_b16 v[220:221], v38 offset:32768\n"
    "ds_read_b64_tr_b16 v[224:225], v39 offset:32768\n"
    "s_waitcnt vmcnt(22) lgkmcnt(0)\n"
    "s_barrier\n"
    "ds_read_b64_tr_b16 v[126:127], v32 offset:49152\n"
    "ds_read_b64_tr_b16 v[130:131], v33 offset:49152\n"
    "ds_read_b64_tr_b16 v[158:159], v34 offset:49152\n"
    "ds_read_b64_tr_b16 v[162:163], v35 offset:49152\n"
    "ds_read_b64_tr_b16 v[190:191], v36 offset:49152\n"
    "ds_read_b64_tr_b16 v[194:195], v37 offset:49152\n"
    "ds_read_b64_tr_b16 v[222:223], v38 offset:49152\n"
    "ds_read_b64_tr_b16 v[226:227], v39 offset:49152\n"
    "s_waitcnt vmcnt(18) lgkmcnt(0)\n"
    "s_barrier\n"
    "v_mfma_f32_16x16x32_bf16 v[24:27], v[72:75], v[124:127], v[24:27]\n"
    "v_mfma_f32_16x16x32_bf16 v[28:31], v[72:75], v[128:131], v[28:31]\n"
    "v_mfma_f32_16x16x32_bf16 v[24:27], v[72:75], v[156:159], v[24:27]\n"
    "v_mfma_f32_16x16x32_bf16 v[28:31], v[72:75], v[160:163], v[28:31]\n"
    "v_mfma_f32_16x16x32_bf16 v[24:27], v[72:75], v[188:191], v[24:27]\n"
    "v_mfma_f32_16x16x32_bf16 v[28:31], v[72:75], v[192:195], v[28:31]\n"
    "v_mfma_f32_16x16x32_bf16 v[24:27], v[72:75], v[220:223], v[24:27]\n"
    "v_mfma_f32_16x16x32_bf16 v[28:31], v[72:75], v[224:227], v[28:31]\n"
    "ds_read_b64_tr_b16 v[132:133], v71\n"
    "ds_read_b64_tr_b16 v[136:137], v70\n"
    "ds_read_b64_tr_b16 v[164:165], v69\n"
    "ds_read_b64_tr_b16 v[168:169], v68\n"
    "ds_read_b64_tr_b16 v[196:197], v67\n"
    "ds_read_b64_tr_b16 v[200:201], v66\n"
    "ds_read_b64_tr_b16 v[228:229], v65\n"
    "ds_read_b64_tr_b16 v[232:233], v64\n"
    "s_waitcnt vmcnt(14) lgkmcnt(0)\n"
    "s_barrier\n"
    "ds_read_b64_tr_b16 v[134:135], v71 offset:16384\n"
    "ds_read_b64_tr_b16 v[138:139], v70 offset:16384\n"
    "ds_read_b64_tr_b16 v[166:167], v69 offset:16384\n"
    "ds_read_b64_tr_b16 v[170:171], v68 offset:16384\n"
    "ds_read_b64_tr_b16 v[198:199], v67 offset:16384\n"
    "ds_read_b64_tr_b16 v[202:203], v66 offset:16384\n"
    "ds_read_b64_tr_b16 v[230:231], v65 offset:16384\n"
    "ds_read_b64_tr_b16 v[234:235], v64 offset:16384\n"
    "s_waitcnt vmcnt(10) lgkmcnt(0)\n"
    "s_barrier\n"
    "v_mfma_f32_16x16x32_bf16 v[24:27], v[72:75], v[132:135], v[24:27]\n"
    "v_mfma_f32_16x16x32_bf16 v[28:31], v[72:75], v[136:139], v[28:31]\n"
    "v_mfma_f32_16x16x32_bf16 v[24:27], v[72:75], v[164:167], v[24:27]\n"
    "v_mfma_f32_16x16x32_bf16 v[28:31], v[72:75], v[168:171], v[28:31]\n"
    "v_mfma_f32_16x16x32_bf16 v[24:27], v[72:75], v[196:199], v[24:27]\n"
    "v_mfma_f32_16x16x32_bf16 v[28:31], v[72:75], v[200:203], v[28:31]\n"
    "v_mfma_f32_16x16x32_bf16 v[24:27], v[72:75], v[228:231], v[24:27]\n"
    "v_mfma_f32_16x16x32_bf16 v[28:31], v[72:75], v[232:235], v[28:31]\n"
    "ds_read_b64_tr_b16 v[140:141], v71 offset:32768\n"
    "ds_read_b64_tr_b16 v[144:145], v70 offset:32768\n"
    "ds_read_b64_tr_b16 v[172:173], v69 offset:32768\n"
    "ds_read_b64_tr_b16 v[176:177], v68 offset:32768\n"
    "ds_read_b64_tr_b16 v[204:205], v67 offset:32768\n"
    "ds_read_b64_tr_b16 v[208:209], v66 offset:32768\n"
    "ds_read_b64_tr_b16 v[236:237], v65 offset:32768\n"
    "ds_read_b64_tr_b16 v[240:241], v64 offset:32768\n"
    "s_waitcnt vmcnt(6) lgkmcnt(0)\n"
    "s_barrier\n"
    "ds_read_b64_tr_b16 v[142:143], v71 offset:49152\n"
    "ds_read_b64_tr_b16 v[146:147], v70 offset:49152\n"
    "ds_read_b64_tr_b16 v[174:175], v69 offset:49152\n"
    "ds_read_b64_tr_b16 v[178:179], v68 offset:49152\n"
    "ds_read_b64_tr_b16 v[206:207], v67 offset:49152\n"
    "ds_read_b64_tr_b16 v[210:211], v66 offset:49152\n"
    "ds_read_b64_tr_b16 v[238:239], v65 offset:49152\n"
    "ds_read_b64_tr_b16 v[242:243], v64 offset:49152\n"
    "s_waitcnt vmcnt(2) lgkmcnt(0)\n"
    "s_barrier\n"
    "v_mfma_f32_16x16x32_bf16 v[24:27], v[72:75], v[140:143], v[24:27]\n"
    "v_mfma_f32_16x16x32_bf16 v[28:31], v[72:75], v[144:147], v[28:31]\n"
    "v_mfma_f32_16x16x32_bf16 v[24:27], v[72:75], v[172:175], v[24:27]\n"
    "v_mfma_f32_16x16x32_bf16 v[28:31], v[72:75], v[176:179], v[28:31]\n"
    "v_mfma_f32_16x16x32_bf16 v[24:27], v[72:75], v[204:207], v[24:27]\n"
    "v_mfma_f32_16x16x32_bf16 v[28:31], v[72:75], v[208:211], v[28:31]\n"
    "v_mfma_f32_16x16x32_bf16 v[24:27], v[72:75], v[236:239], v[24:27]\n"
    "v_mfma_f32_16x16x32_bf16 v[28:31], v[72:75], v[240:243], v[28:31]\n"
    "ds_read_b64_tr_b16 v[244:245], v63\n"
    "ds_read_b64_tr_b16 v[248:249], v62\n"
    "ds_read_b64_tr_b16 v[246:247], v63 offset:4096\n"
    "ds_read_b64_tr_b16 v[250:251], v62 offset:4096\n"
    "ds_read_b64_tr_b16 v[252:253], v63 offset:8192\n"
    "ds_read_b64_tr_b16 v[76:77], v62 offset:8192\n"
    "ds_read_b64_tr_b16 v[254:255], v63 offset:12288\n"
    "ds_read_b64_tr_b16 v[78:79], v62 offset:12288\n"
    "s_waitcnt lgkmcnt(0)\n"
    "s_barrier\n"
    "v_mfma_f32_16x16x32_bf16 v[24:27], v[72:75], v[244:247], v[24:27]\n"
    "v_mfma_f32_16x16x32_bf16 v[28:31], v[72:75], v[248:251], v[28:31]\n"
    "v_mfma_f32_16x16x32_bf16 v[24:27], v[72:75], v[252:255], v[24:27]\n"
    "v_mfma_f32_16x16x32_bf16 v[28:31], v[72:75], v[76:79], v[28:31]\n"
    "ds_write_b128 v2, v[8:11] offset:8192\n"
    "ds_write_b128 v2, v[12:15] offset:12288\n"
    "ds_write_b128 v2, v[16:19] offset:16384\n"
    "ds_write_b128 v2, v[20:23] offset:20480\n"
    "s_nop 0\n"
    "v_mov_b32 v74, v24\n"
    "s_nop 2\n"
    "v_mov_b32 v75, v28\n"
    "s_mov_b64 s[44:45], %1\n"
    "s_and_b32 s45, s45, 0xffff\n"
    "s_mov_b32 s46, 0x0\n"
    "s_mov_b32 s47, 0x20000\n"
    "v_mov_b32 v6, 0\n"
    "s_waitcnt vmcnt(0)\n"
    "v_add_u32 v80, 1, v80\n"
    "v_add_u32 v81, 1, v81\n"
    "s_mov_b32 s52, 1048576\n"
    ".Lhgo_a%=:\n"
    "v_cmp_ne_u64 vcc, v[80:81], v[82:83]\n"
    "s_cbranch_vccz .Lhgo_x%=\n"
    "global_load_dwordx2 v[82:83], v4, %1 sc1\n"
    "s_sub_u32 s52, s52, 1\n"
    "s_waitcnt vmcnt(0)\n"
    "s_cbranch_scc0 .Lhgo_a%=\n"
    ".Lhgo_x%=:\n"
    "v_mbcnt_lo_u32_b32 v80, -1, 0\n"
    "v_mbcnt_hi_u32_b32 v80, -1, v80\n"
    "v_min_u32 v4, 5, v80\n"
    "v_lshlrev_b32 v4, 7, v4\n"
    "v_mov_b32 v7, 0x26c00\n"
    "ds_read_b128 v[80:83], v7\n"
    "s_waitcnt lgkmcnt(0)\n"
    "v_readfirstlane_b32 s49, v81\n"
    "v_add_u32 v80, v80, v4\n"
    "s_branch .Lhchk_%=\n"
    ".Lhgo_%=:\n"
    "s_waitcnt lgkmcnt(1)\n"
    "v_mfma_f32_16x16x32_bf16 v[8:11], a[0:3], v[48:51], 0\n"
    "s_waitcnt vmcnt(5)\n"
    "ds_read_b128 v[56:59], v1 offset:2048\n"
    "v_mfma_f32_16x16x32_bf16 v[12:15], a[4:7], v[48:51], 0\n"
    "ds_read_b128 v[80:83], v7 offset:32\n"
    "v_add_u32 v7, 32, v7\n"
    "v_mfma_f32_16x16x32_bf16 v[16:19], a[144:147], v[48:51], 0\n"
    "ds_read_b128 v[60:63], v1 offset:3072\n"
    "v_mul_f32 v24, v24, v68\n"
    "v_mfma_f32_16x16x32_bf16 v[20:23], a[148:151], v[48:51], 0\n"
    "s_waitcnt lgkmcnt(3)\n"
    "v_mul_f32 v25, v25, v69\n"
    "v_cvt_pk_bf16_f32 v24, v24, v25\n"
    "v_mfma_f32_16x16x32_bf16 v[8:11], a[8:11], v[52:55], v[8:11]\n"
    "v_mul_f32 v26, v26, v70\n"
    "v_mul_f32 v27, v27, v71\n"
    "v_mfma_f32_16x16x32_bf16 v[12:15], a[12:15], v[52:55], v[12:15]\n"
    "v_cvt_pk_bf16_f32 v25, v26, v27\n"
    "v_mul_f32 v28, v28, v68\n"
    "v_mfma_f32_16x16x32_bf16 v[16:19], a[152:155], v[52:55], v[16:19]\n"
    "v_mul_f32 v29, v29, v69\n"
    "v_cvt_pk_bf16_f32 v26, v28, v29\n"
    "v_mfma_f32_16x16x32_bf16 v[20:23], a[156:159], v[52:55], v[20:23]\n"
    "s_waitcnt lgkmcnt(1)\n"
    "v_mul_f32 v30, v30, v70\n"
    "v_mul_f32 v31, v31, v71\n"
    "v_mfma_f32_16x16x32_bf16 v[8:11], a[16:19], v[56:59], v[8:11]\n"
    "v_cvt_pk_bf16_f32 v27, v30, v31\n"
    "v_readfirstlane_b32 s49, v81\n"
    "v_mfma_f32_16x16x32_bf16 v[12:15], a[20:23], v[56:59], v[12:15]\n"
    "buffer_store_dwordx4 v[24:27], v2, s[40:43], s64 offen sc1\n"
    "v_add_u32 v80, v80, v4\n"
    "v_mfma_f32_16x16x32_bf16 v[16:19], a[160:163], v[56:59], v[16:19]\n"
    "v_readfirstlane_b32 s62, v82\n"
    "v_xor_b32 v0, 0x2000, v1\n"
    "v_mfma_f32_16x16x32_bf16 v[20:23], a[164:167], v[56:59], v[20:23]\n"
    "s_waitcnt vmcnt(4)\n"
    ".Lhtop_%=:\n"
    "v_mfma_f32_16x16x32_bf16 v[24:27], v[48:51], v[116:119], 0\n"
    "s_barrier\n"
    "ds_read_b128 v[32:35], v0\n"
    "v_mfma_f32_16x16x32_bf16 v[28:31], v[48:51], v[120:123], 0\n"
    "ds_read2st64_b32 v[64:65], v5 offset1:1\n"
    "s_xor_b32 s58, s58, 0x8000\n"
    "v_mfma_f32_16x16x32_bf16 v[24:27], v[52:55], v[124:127], v[24:27]\n"
    "ds_read2st64_b32 v[66:67], v5 offset0:2 offset1:3\n"
    "s_xor_b32 s59, s59, 0x8000\n"
    "v_mfma_f32_16x16x32_bf16 v[28:31], v[52:55], v[128:131], v[28:31]\n"
    "ds_read2st64_b32 v[68:69], v5 offset0:4 offset1:5\n"
    "s_add_u32 s63, s62, 0x2000000\n"
    "v_mfma_f32_16x16x32_bf16 v[24:27], v[56:59], v[132:135], v[24:27]\n"
    "ds_read2st64_b32 v[70:71], v5 offset0:6 offset1:7\n"
    "s_add_u32 s64, s62, 0x4000000\n"
    "v_mfma_f32_16x16x32_bf16 v[28:31], v[56:59], v[136:139], v[28:31]\n"
    "ds_read_b32 v72, v5 offset:2048\n"
    "s_waitcnt lgkmcnt(6)\n"
    "v_mfma_f32_16x16x32_bf16 v[8:11], a[24:27], v[60:63], v[8:11]\n"
    "ds_read_b128 v[36:39], v0 offset:1024\n"
    "v_lshrrev_b32 v48, 2, v5\n"
    "v_mfma_f32_16x16x32_bf16 v[12:15], a[28:31], v[60:63], v[12:15]\n"
    "ds_read_b128 v[40:43], v0 offset:2048\n"
    "v_and_b32 v48, 48, v48\n"
    "v_mfma_f32_16x16x32_bf16 v[16:19], a[168:171], v[60:63], v[16:19]\n"
    "ds_read_b128 v[44:47], v0 offset:3072\n"
    "v_bfi_b32 v48, s86, v48, v5\n"
    "v_mfma_f32_16x16x32_bf16 v[20:23], a[172:175], v[60:63], v[20:23]\n"
    "s_waitcnt lgkmcnt(3)\n"
    "v_add_f32 v64, v64, v65\n"
    "v_add_f32 v66, v66, v67\n"
    "v_mfma_f32_16x16x32_bf16 v[24:27], v[60:63], v[140:143], v[24:27]\n"
    "v_add_f32 v68, v68, v69\n"
    "v_add_f32 v70, v70, v71\n"
    "v_mfma_f32_16x16x32_bf16 v[28:31], v[60:63], v[144:147], v[28:31]\n"
    "v_add_f32 v64, v64, v66\n"
    "v_add_f32 v68, v68, v70\n"
    "v_mfma_f32_16x16x32_bf16 v[8:11], a[32:35], v[32:35], v[8:11]\n"
    "s_waitcnt vmcnt(3)\n"
    "v_cmp_eq_u32 vcc, 0, v6\n"
    "v_add_f32 v64, v64, v72\n"
    "v_mfma_f32_16x16x32_bf16 v[12:15], a[36:39], v[32:35], v[12:15]\n"
    "s_cmp_eq_u64 vcc, 0\n"
    "s_cselect_b32 s46, 0, 0x20000\n"
    "s_cselect_b32 s94, s42, 0\n"
    "v_mfma_f32_16x16x32_bf16 v[16:19], a[176:179], v[32:35], v[16:19]\n"
    "buffer_load_dword v81, v80, s[44:47], 0 offen sc1\n"
    "s_cselect_b32 s90, s96, 0\n"
    "v_mfma_f32_16x16x32_bf16 v[20:23], a[180:183], v[32:35], v[20:23]\n"
    "s_mov_b32 m0, s58\n"
    "v_xor_b32 v0, 0x4000, v1\n"
    "v_add_f32 v72, v64, v68\n"
    "v_mfma_f32_16x16x32_bf16 v[24:27], v[32:35], v[148:151], v[24:27]\n"
    "buffer_load_dwordx4 v3, s[92:95], s49 offen sc1 lds\n"
    "s_waitcnt lgkmcnt(2)\n"
    "v_mfma_f32_16x16x32_bf16 v[28:31], v[32:35], v[152:155], v[28:31]\n"
    "ds_read_b128 v[32:35], v0\n"
    "v_add_f32 v64, v64, v68\n"
    "v_mfma_f32_16x16x32_bf16 v[8:11], a[40:43], v[36:39], v[8:11]\n"
    "ds_read_b32 v6, v7 offset:32\n"
    "v_mfma_f32_16x16x32_bf16 v[12:15], a[44:47], v[36:39], v[12:15]\n"
    "v_permlane32_swap_b32 v64, v72\n"
    "v_mfma_f32_16x16x32_bf16 v[16:19], a[184:187], v[36:39], v[16:19]\n"
    "v_add_f32 v65, v64, v72\n"
    "v_add_f32 v72, v72, v64\n"
    "v_mfma_f32_16x16x32_bf16 v[20:23], a[188:191], v[36:39], v[20:23]\n"
    "s_nop 0\n"
    "v_permlane16_swap_b32 v65, v72\n"
    "v_mfma_f32_16x16x32_bf16 v[24:27], v[36:39], v[156:159], v[24:27]\n"
    "s_waitcnt lgkmcnt(3)\n"
    "v_mul_f32 v64, 0xbae38e39, v65\n"
    "v_mul_f32 v72, 0x3ae38e39, v72\n"
    "v_mfma_f32_16x16x32_bf16 v[28:31], v[36:39], v[160:163], v[28:31]\n"
    "ds_read_b128 v[36:39], v0 offset:1024\n"
    "v_cvt_pk_bf16_f32 v73, 0, v64\n"
    "v_mfma_f32_16x16x32_bf16 v[8:11], a[48:51], v[40:43], v[8:11]\n"
    "s_waitcnt lgkmcnt(1)\n"
    "v_add_u32 v6, v6, v4\n"
    "v_fma_f32 v72, -v64, v64, v72\n"
    "v_mfma_f32_16x16x32_bf16 v[12:15], a[52:55], v[40:43], v[12:15]\n"
    "buffer_load_dwordx4 v3, s[92:95], s49 offen offset:1024 sc1 lds\n"
    "v_max_f32 v72, 0, v72\n"
    "v_mfma_f32_16x16x32_bf16 v[16:19], a[192:195], v[40:43], v[16:19]\n"
    "v_cvt_pk_bf16_f32 v72, 0, v72\n"
    "v_add_f32 v72, 0x3727c5ac, v72\n"
    "v_mfma_f32_16x16x32_bf16 v[20:23], a[196:199], v[40:43], v[20:23]\n"
    "ds_read_b128 v[52:55], v2 offset:12288\n"
    "v_cvt_pk_bf16_f32 v72, 0, v72\n"
    "v_mfma_f32_16x16x32_bf16 v[24:27], v[40:43], v[164:167], v[24:27]\n"
    "v_sqrt_f32 v72, v72\n"
    "v_mfma_f32_16x16x32_bf16 v[28:31], v[40:43], v[168:171], v[28:31]\n"
    "ds_read_b128 v[40:43], v0 offset:2048\n"
    "v_cvt_pk_bf16_f32 v72, 0, v72\n"
    "v_mfma_f32_16x16x32_bf16 v[8:11], a[56:59], v[44:47], v[8:11]\n"
    "v_rcp_f32 v72, v72\n"
    "v_mfma_f32_16x16x32_bf16 v[12:15], a[60:63], v[44:47], v[12:15]\n"
    "ds_write2st64_b32 v5, v73, v72 offset0:9 offset1:10\n"
    "v_mfma_f32_16x16x32_bf16 v[16:19], a[200:203], v[44:47], v[16:19]\n"
    "buffer_load_dwordx4 v3, s[92:95], s49 offen offset:2048 sc1 lds\n"
    "v_mfma_f32_16x16x32_bf16 v[20:23], a[204:207], v[44:47], v[20:23]\n"
    "ds_read_b128 v[64:67], v48 offset:2304\n"
    "v_mfma_f32_16x16x32_bf16 v[24:27], v[44:47], v[172:175], v[24:27]\n"
    "v_mov_b32 v82, 1\n"
    "v_mfma_f32_16x16x32_bf16 v[28:31], v[44:47], v[176:179], v[28:31]\n"
    "ds_read_b128 v[44:47], v0 offset:3072\n"
    "s_waitcnt vmcnt(4)\n"
    "v_mfma_f32_16x16x32_bf16 v[8:11], a[64:67], v[32:35], v[8:11]\n"
    "s_mov_b64 exec, s[56:57]\n"
    "ds_add_rtn_u32 v82, v7, v82 offset:16\n"
    "s_mov_b64 exec, -1\n"
    "v_mfma_f32_16x16x32_bf16 v[12:15], a[68:71], v[32:35], v[12:15]\n"
    "ds_read_b128 v[68:71], v48 offset:2560\n"
    "v_mfma_f32_16x16x32_bf16 v[16:19], a[208:211], v[32:35], v[16:19]\n"
    "ds_read_b128 v[56:59], v2 offset:16384\n"
    "v_mfma_f32_16x16x32_bf16 v[20:23], a[212:215], v[32:35], v[20:23]\n"
    "buffer_load_dwordx4 v3, s[92:95], s49 offen offset:3072 sc1 lds\n"
    "v_xor_b32 v0, 0x6000, v1\n"
    "v_mfma_f32_16x16x32_bf16 v[24:27], v[32:35], v[180:183], v[24:27]\n"
    "ds_read_b128 v[60:63], v2 offset:20480\n"
    "s_waitcnt lgkmcnt(8)\n"
    "v_mfma_f32_16x16x32_bf16 v[28:31], v[32:35], v[184:187], v[28:31]\n"
    "ds_read_b128 v[32:35], v0\n"
    "v_mfma_f32_16x16x32_bf16 v[8:11], a[72:75], v[36:39], v[8:11]\n"
    "s_waitcnt lgkmcnt(4)\n"
    "v_readfirstlane_b32 s50, v82\n"
    "v_mfma_f32_16x16x32_bf16 v[12:15], a[76:79], v[36:39], v[12:15]\n"
    "s_cmp_eq_u32 s50, 3\n"
    "s_cselect_b32 s46, 0x20000, 0\n"
    "v_mfma_f32_16x16x32_bf16 v[16:19], a[216:219], v[36:39], v[16:19]\n"
    "buffer_store_dword v82, v83, s[44:47], 0 offen sc1\n"
    "v_mfma_f32_16x16x32_bf16 v[20:23], a[220:223], v[36:39], v[20:23]\n"
    "ds_read_b128 v[48:51], v2 offset:8192\n"
    "s_bitset1_b32 m0, 12\n"
    "v_mfma_f32_16x16x32_bf16 v[24:27], v[36:39], v[188:191], v[24:27]\n"
    "buffer_load_dwordx4 v2, s[88:91], s49 offen sc1 lds\n"
    "v_mfma_f32_16x16x32_bf16 v[28:31], v[36:39], v[192:195], v[28:31]\n"
    "ds_read_b128 v[36:39], v0 offset:1024\n"
    "s_mov_b32 m0, s59\n"
    "v_mfma_f32_16x16x32_bf16 v[8:11], a[80:83], v[40:43], v[8:11]\n"
    "s_mov_b32 s55, s49\n"
    "v_mfma_f32_16x16x32_bf16 v[12:15], a[84:87], v[40:43], v[12:15]\n"
    "s_bitset1_b32 s55, 15\n"
    "v_mfma_f32_16x16x32_bf16 v[16:19], a[224:227], v[40:43], v[16:19]\n"
    "s_waitcnt lgkmcnt(1)\n"
    "v_fmac_f32 v24, v64, v74\n"
    "v_mfma_f32_16x16x32_bf16 v[20:23], a[228:231], v[40:43], v[20:23]\n"
    "v_fmac_f32 v28, v64, v75\n"
    "v_mfma_f32_16x16x32_bf16 v[24:27], v[40:43], v[196:199], v[24:27]\n"
    "v_fmac_f32 v8, v48, v73\n"
    "v_mfma_f32_16x16x32_bf16 v[28:31], v[40:43], v[200:203], v[28:31]\n"
    "ds_read_b128 v[40:43], v0 offset:2048\n"
    "v_mfma_f32_16x16x32_bf16 v[8:11], a[88:91], v[44:47], v[8:11]\n"
    "s_mov_b64 exec, s[60:61]\n"
    "buffer_load_dwordx4 v2, s[92:95], s55 offen offset:2048 sc1 lds\n"
    "s_mov_b64 exec, -1\n"
    "v_mfma_f32_16x16x32_bf16 v[12:15], a[92:95], v[44:47], v[12:15]\n"
    "v_fmac_f32 v25, v65, v74\n"
    "v_mfma_f32_16x16x32_bf16 v[16:19], a[232:235], v[44:47], v[16:19]\n"
    "s_cmp_eq_u32 s94, 0\n"
    "s_cbranch_scc1 .Lhk1_%=\n"
    ".Lhk1_x%=:\n"
    "v_mfma_f32_16x16x32_bf16 v[20:23], a[236:239], v[44:47], v[20:23]\n"
    "v_fmac_f32 v29, v65, v75\n"
    "v_fmac_f32 v30, v66, v75\n"
    "v_mfma_f32_16x16x32_bf16 v[24:27], v[44:47], v[204:207], v[24:27]\n"
    "v_fmac_f32 v9, v49, v73\n"
    "v_fmac_f32 v10, v50, v73\n"
    "v_mfma_f32_16x16x32_bf16 v[28:31], v[44:47], v[208:211], v[28:31]\n"
    "ds_read_b128 v[44:47], v0 offset:3072\n"
    "v_fmac_f32 v12, v52, v73\n"
    "v_mfma_f32_16x16x32_bf16 v[8:11], a[96:99], v[32:35], v[8:11]\n"
    "v_fmac_f32 v16, v56, v73\n"
    "v_fmac_f32 v17, v57, v73\n"
    "v_mfma_f32_16x16x32_bf16 v[12:15], a[100:103], v[32:35], v[12:15]\n"
    "v_fmac_f32 v26, v66, v74\n"
    "v_fmac_f32 v27, v67, v74\n"
    "v_mfma_f32_16x16x32_bf16 v[16:19], a[240:243], v[32:35], v[16:19]\n"
    "v_fmac_f32 v31, v67, v75\n"
    "v_mfma_f32_16x16x32_bf16 v[20:23], a[244:247], v[32:35], v[20:23]\n"
    "v_and_b32 v0, 0xffff9fff, v1\n"
    "v_fmac_f32 v11, v51, v73\n"
    "v_mfma_f32_16x16x32_bf16 v[24:27], v[32:35], v[212:215], v[24:27]\n"
    "s_waitcnt lgkmcnt(2)\n"
    "v_fmac_f32 v13, v53, v73\n"
    "v_fmac_f32 v14, v54, v73\n"
    "v_mfma_f32_16x16x32_bf16 v[28:31], v[32:35], v[216:219], v[28:31]\n"
    "ds_read_b128 v[32:35], v0 offset:4096\n"
    "v_fmac_f32 v15, v55, v73\n"
    "v_mfma_f32_16x16x32_bf16 v[8:11], a[104:107], v[36:39], v[8:11]\n"
    "ds_read_b128 v[64:67], v0 offset:12288\n"
    "v_fmac_f32 v18, v58, v73\n"
    "v_mfma_f32_16x16x32_bf16 v[12:15], a[108:111], v[36:39], v[12:15]\n"
    "s_waitcnt lgkmcnt(2)\n"
    "v_fmac_f32 v19, v59, v73\n"
    "v_fmac_f32 v20, v60, v73\n"
    "v_mfma_f32_16x16x32_bf16 v[8:11], a[112:115], v[40:43], v[8:11]\n"
    "v_fmac_f32 v21, v61, v73\n"
    "v_fmac_f32 v22, v62, v73\n"
    "v_mfma_f32_16x16x32_bf16 v[12:15], a[116:119], v[40:43], v[12:15]\n"
    "v_fmac_f32 v23, v63, v73\n"
    "v_xor_b32 v1, 0x8000, v1\n"
    "v_mfma_f32_16x16x32_bf16 v[8:11], a[120:123], v[44:47], v[8:11]\n"
    "v_xor_b32 v5, 0x8000, v5\n"
    "v_mfma_f32_16x16x32_bf16 v[12:15], a[124:127], v[44:47], v[12:15]\n"
    "s_waitcnt lgkmcnt(1)\n"
    "v_mfma_f32_16x16x32_bf16 v[8:11], a[128:131], v[32:35], v[8:11]\n"
    "v_mfma_f32_16x16x32_bf16 v[12:15], a[132:135], v[32:35], v[12:15]\n"
    "s_waitcnt lgkmcnt(0)\n"
    "v_mfma_f32_16x16x32_bf16 v[8:11], a[136:139], v[64:67], v[8:11]\n"
    "s_cmp_eq_u32 s94, 0\n"
    "s_cbranch_scc1 .Lhk2_%=\n"
    ".Lhk2_x%=:\n"
    "v_mfma_f32_16x16x32_bf16 v[12:15], a[140:143], v[64:67], v[12:15]\n"
    "v_mfma_f32_16x16x32_bf16 v[16:19], a[248:251], v[36:39], v[16:19]\n"
    "global_load_dword v6, v6, %1 sc1\n"
    "v_mfma_f32_16x16x32_bf16 v[20:23], a[252:255], v[36:39], v[20:23]\n"
    "v_mul_f32 v8, v8, v72\n"
    "v_mul_f32 v12, v12, v72\n"
    "v_mfma_f32_16x16x32_bf16 v[16:19], v[84:87], v[40:43], v[16:19]\n"
    "v_cvt_pk_bf16_f32 v8, v8, v12\n"
    "v_mul_f32 v9, v9, v72\n"
    "v_mfma_f32_16x16x32_bf16 v[20:23], v[88:91], v[40:43], v[20:23]\n"
    "v_mul_f32 v13, v13, v72\n"
    "v_cvt_pk_bf16_f32 v9, v9, v13\n"
    "v_mfma_f32_16x16x32_bf16 v[16:19], v[92:95], v[44:47], v[16:19]\n"
    "v_mul_f32 v10, v10, v72\n"
    "v_mul_f32 v14, v14, v72\n"
    "v_mfma_f32_16x16x32_bf16 v[20:23], v[96:99], v[44:47], v[20:23]\n"
    "v_cvt_pk_bf16_f32 v10, v10, v14\n"
    "v_mul_f32 v11, v11, v72\n"
    "v_mfma_f32_16x16x32_bf16 v[16:19], v[100:103], v[32:35], v[16:19]\n"
    "v_mul_f32 v15, v15, v72\n"
    "v_cvt_pk_bf16_f32 v11, v11, v15\n"
    "v_mfma_f32_16x16x32_bf16 v[20:23], v[104:107], v[32:35], v[20:23]\n"
    "buffer_store_dwordx4 v[8:11], v2, s[40:43], s62 offen sc1\n"
    "v_mfma_f32_16x16x32_bf16 v[16:19], v[108:111], v[64:67], v[16:19]\n"
    "s_waitcnt vmcnt(5)\n"
    "ds_read_b128 v[48:51], v1\n"
    "v_mfma_f32_16x16x32_bf16 v[20:23], v[112:115], v[64:67], v[20:23]\n"
    "ds_read_b128 v[52:55], v1 offset:1024\n"
    "v_mfma_f32_16x16x32_bf16 v[24:27], v[36:39], v[220:223], v[24:27]\n"
    "v_mfma_f32_16x16x32_bf16 v[28:31], v[36:39], v[224:227], v[28:31]\n"
    "v_mul_f32 v16, v16, v72\n"
    "v_mul_f32 v20, v20, v72\n"
    "v_mfma_f32_16x16x32_bf16 v[24:27], v[40:43], v[228:231], v[24:27]\n"
    "v_cvt_pk_bf16_f32 v16, v16, v20\n"
    "v_mul_f32 v17, v17, v72\n"
    "v_mfma_f32_16x16x32_bf16 v[28:31], v[40:43], v[232:235], v[28:31]\n"
    "v_mul_f32 v21, v21, v72\n"
    "v_cvt_pk_bf16_f32 v17, v17, v21\n"
    "v_mfma_f32_16x16x32_bf16 v[24:27], v[44:47], v[236:239], v[24:27]\n"
    "v_mul_f32 v18, v18, v72\n"
    "v_mul_f32 v22, v22, v72\n"
    "v_mfma_f32_16x16x32_bf16 v[28:31], v[44:47], v[240:243], v[28:31]\n"
    "v_cvt_pk_bf16_f32 v18, v18, v22\n"
    "v_mul_f32 v19, v19, v72\n"
    "v_mfma_f32_16x16x32_bf16 v[24:27], v[32:35], v[244:247], v[24:27]\n"
    "v_mul_f32 v23, v23, v72\n"
    "v_cvt_pk_bf16_f32 v19, v19, v23\n"
    "v_mfma_f32_16x16x32_bf16 v[28:31], v[32:35], v[248:251], v[28:31]\n"
    "buffer_store_dwordx4 v[16:19], v2, s[40:43], s63 offen sc1\n"
    "s_mov_b32 s56, 1\n"
    "v_mfma_f32_16x16x32_bf16 v[24:27], v[64:67], v[252:255], v[24:27]\n"
    "s_cmp_lg_u32 s94, 0\n"
    "v_mfma_f32_16x16x32_bf16 v[28:31], v[64:67], v[76:79], v[28:31]\n"
    "s_cbranch_scc1 .Lhgo_%=\n"
    "ds_read_b32 v83, v7 offset:44\n"
    "s_nop 2\n"
    "v_mul_f32 v24, v24, v68\n"
    "v_mul_f32 v25, v25, v69\n"
    "v_cvt_pk_bf16_f32 v24, v24, v25\n"
    "v_mul_f32 v26, v26, v70\n"
    "v_mul_f32 v27, v27, v71\n"
    "v_cvt_pk_bf16_f32 v25, v26, v27\n"
    "v_mul_f32 v28, v28, v68\n"
    "v_mul_f32 v29, v29, v69\n"
    "v_cvt_pk_bf16_f32 v26, v28, v29\n"
    "v_mul_f32 v30, v30, v70\n"
    "v_mul_f32 v31, v31, v71\n"
    "v_cvt_pk_bf16_f32 v27, v30, v31\n"
    "buffer_store_dwordx4 v[24:27], v2, s[40:43], s64 offen sc1\n"
    "s_waitcnt vmcnt(9) lgkmcnt(0)\n"
    "v_cmp_eq_u32 vcc, 0, v81\n"
    "s_cmp_eq_u64 vcc, 0\n"
    "s_cbranch_scc1 .Lhld_%=\n"
    "global_load_dword v81, v80, %1 sc1\n"
    "s_waitcnt vmcnt(0)\n"
    "v_mov_b32 v82, 1\n"
    "s_mov_b64 exec, s[56:57]\n"
    "ds_add_rtn_u32 v82, v7, v82 offset:48\n"
    "s_mov_b64 exec, -1\n"
    "s_waitcnt lgkmcnt(0)\n"
    "v_readfirstlane_b32 s50, v82\n"
    "s_cmp_eq_u32 s50, 3\n"
    "s_cselect_b32 s46, 0x20000, 0\n"
    "buffer_store_dword v82, v83, s[44:47], 0 offen sc1\n"
    "s_mov_b32 s56, 0\n"
    "v_cmp_eq_u32 vcc, 0, v81\n"
    "s_cmp_eq_u64 vcc, 0\n"
    "s_cbranch_scc1 .Lhld_%=\n"
    ".Lhchk_%=:\n"
    "v_readfirstlane_b32 s50, v80\n"
    "s_cmp_lt_u32 s50, 200\n"
    "s_cbranch_scc1 .Lhend_%=\n"
    ".Lhwait_%=:\n"
    "global_load_dword v81, v80, %1 sc1\n"
    "s_mov_b32 s52, 0\n"
    ".Lhp3_%=:\n"
    "s_waitcnt vmcnt(0)\n"
    "v_cmp_eq_u32 vcc, 0, v81\n"
    "s_cmp_eq_u64 vcc, 0\n"
    "s_cbranch_scc1 .Lhp3_x%=\n"
    "s_add_u32 s52, s52, 1\n"
    "s_sleep 2\n"
    "global_load_dword v81, v80, %1 sc1\n"
    "s_cmp_lt_u32 s52, s53\n"
    "s_cbranch_scc1 .Lhp3_%=\n"
    "s_mov_b32 s53, 0\n"
    "v_mov_b32 v82, 0\n"
    "v_mov_b32 v83, 1\n"
    "global_store_dword v82, v83, %1 sc1\n"
    "s_waitcnt vmcnt(0)\n"
    ".Lhp3_x%=:\n"
    ".Lhld_%=:\n"
    "s_mov_b32 s94, s42\n"
    "s_mov_b32 s90, s96\n"
    "s_mov_b32 m0, s58\n"
    "s_nop 0\n"
    "buffer_load_dwordx4 v3, s[92:95], s49 offen sc1 lds\n"
    "buffer_load_dwordx4 v3, s[92:95], s49 offen offset:1024 sc1 lds\n"
    "buffer_load_dwordx4 v3, s[92:95], s49 offen offset:2048 sc1 lds\n"
    "buffer_load_dwordx4 v3, s[92:95], s49 offen offset:3072 sc1 lds\n"
    "s_bitset1_b32 m0, 12\n"
    "s_nop 0\n"
    "buffer_load_dwordx4 v2, s[88:91], s49 offen sc1 lds\n"
    "s_mov_b32 m0, s59\n"
    "s_mov_b32 s55, s49\n"
    "s_bitset1_b32 s55, 15\n"
    "s_mov_b64 exec, s[60:61]\n"
    "buffer_load_dwordx4 v2, s[92:95], s55 offen offset:2048 sc1 lds\n"
    "s_mov_b64 exec, -1\n"
    "s_waitcnt vmcnt(6)\n"
    "v_mov_b32 v82, 1\n"
    "s_mov_b64 exec, s[56:57]\n"
    "ds_add_rtn_u32 v82, v7, v82 offset:48\n"
    "s_mov_b64 exec, -1\n"
    "s_waitcnt lgkmcnt(0)\n"
    "v_readfirstlane_b32 s50, v82\n"
    "s_cmp_eq_u32 s50, 3\n"
    "s_cselect_b32 s46, 0x20000, 0\n"
    "buffer_store_dword v82, v83, s[44:47], 0 offen sc1\n"
    "s_mov_b32 s56, 0\n"
    "s_waitcnt vmcnt(5)\n"
    "ds_read_b128 v[48:51], v1\n"
    "ds_read_b128 v[52:55], v1 offset:1024\n"
    "s_waitcnt lgkmcnt(1)\n"
    "v_mfma_f32_16x16x32_bf16 v[8:11], a[0:3], v[48:51], 0\n"
    "s_waitcnt vmcnt(1)\n"
    "ds_read_b128 v[56:59], v1 offset:2048\n"
    "v_mfma_f32_16x16x32_bf16 v[12:15], a[4:7], v[48:51], 0\n"
    "ds_read_b128 v[80:83], v7 offset:32\n"
    "v_add_u32 v7, 32, v7\n"
    "v_mfma_f32_16x16x32_bf16 v[16:19], a[144:147], v[48:51], 0\n"
    "ds_read_b128 v[60:63], v1 offset:3072\n"
    "v_mfma_f32_16x16x32_bf16 v[20:23], a[148:151], v[48:51], 0\n"
    "s_waitcnt lgkmcnt(3)\n"
    "v_mfma_f32_16x16x32_bf16 v[8:11], a[8:11], v[52:55], v[8:11]\n"
    "v_mfma_f32_16x16x32_bf16 v[12:15], a[12:15], v[52:55], v[12:15]\n"
    "v_mfma_f32_16x16x32_bf16 v[16:19], a[152:155], v[52:55], v[16:19]\n"
    "v_mfma_f32_16x16x32_bf16 v[20:23], a[156:159], v[52:55], v[20:23]\n"
    "s_waitcnt lgkmcnt(1)\n"
    "v_mfma_f32_16x16x32_bf16 v[8:11], a[16:19], v[56:59], v[8:11]\n"
    "v_readfirstlane_b32 s49, v81\n"
    "v_mfma_f32_16x16x32_bf16 v[12:15], a[20:23], v[56:59], v[12:15]\n"
    "v_add_u32 v80, v80, v4\n"
    "v_mfma_f32_16x16x32_bf16 v[16:19], a[160:163], v[56:59], v[16:19]\n"
    "v_readfirstlane_b32 s62, v82\n"
    "v_xor_b32 v0, 0x2000, v1\n"
    "v_mfma_f32_16x16x32_bf16 v[20:23], a[164:167], v[56:59], v[20:23]\n"
    "s_branch .Lhtop_%=\n"
    ".Lhk1_%=:\n"
    "s_waitcnt vmcnt(6)\n"
    "v_cmp_eq_u32 vcc, 0, v81\n"
    "s_cmp_eq_u64 vcc, 0\n"
    "s_cselect_b32 s46, 0, 0x20000\n"
    "s_cselect_b32 s94, s42, 0\n"
    "s_cselect_b32 s90, s96, 0\n"
    "buffer_load_dword v81, v80, s[44:47], 0 offen sc1\n"
    "s_mov_b32 m0, s58\n"
    "s_nop 0\n"
    "buffer_load_dwordx4 v3, s[92:95], s49 offen sc1 lds\n"
    "buffer_load_dwordx4 v3, s[92:95], s49 offen offset:1024 sc1 lds\n"
    "buffer_load_dwordx4 v3, s[92:95], s49 offen offset:2048 sc1 lds\n"
    "buffer_load_dwordx4 v3, s[92:95], s49 offen offset:3072 sc1 lds\n"
    "s_bitset1_b32 m0, 12\n"
    "s_nop 0\n"
    "buffer_load_dwordx4 v2, s[88:91], s49 offen sc1 lds\n"
    "s_mov_b32 m0, s59\n"
    "s_mov_b32 s55, s49\n"
    "s_bitset1_b32 s55, 15\n"
    "s_mov_b64 exec, s[60:61]\n"
    "buffer_load_dwordx4 v2, s[92:95], s55 offen offset:2048 sc1 lds\n"
    "s_mov_b64 exec, -1\n"
    "s_branch .Lhk1_x%=\n"
    ".Lhk2_%=:\n"
    "s_waitcnt vmcnt(6)\n"
    "v_cmp_eq_u32 vcc, 0, v81\n"
    "s_cmp_eq_u64 vcc, 0\n"
    "s_cselect_b32 s46, 0, 0x20000\n"
    "s_cselect_b32 s94, s42, 0\n"
    "s_cselect_b32 s90, s96, 0\n"
    "buffer_load_dword v81, v80, s[44:47], 0 offen sc1\n"
    "s_mov_b32 m0, s58\n"
    "s_nop 0\n"
    "buffer_load_dwordx4 v3, s[92:95], s49 offen sc1 lds\n"
    "buffer_load_dwordx4 v3, s[92:95], s49 offen offset:1024 sc1 lds\n"
    "buffer_load_dwordx4 v3, s[92:95], s49 offen offset:2048 sc1 lds\n"
    "buffer_load_dwordx4 v3, s[92:95], s49 offen offset:3072 sc1 lds\n"
    "s_bitset1_b32 m0, 12\n"
    "s_nop 0\n"
    "buffer_load_dwordx4 v2, s[88:91], s49 offen sc1 lds\n"
    "s_mov_b32 m0, s59\n"
    "s_mov_b32 s55, s49\n"
    "s_bitset1_b32 s55, 15\n"
    "s_mov_b64 exec, s[60:61]\n"
    "buffer_load_dwordx4 v2, s[92:95], s55 offen offset:2048 sc1 lds\n"
    "s_mov_b64 exec, -1\n"
    "s_branch .Lhk2_x%=\n"
    ".Lhpol_%=:\n"
    "v_mov_b32 v80, 64\n"
    "global_load_dword v81, v80, %1 sc1\n"
    "s_mov_b32 s52, 0\n"
    ".Lhp4_%=:\n"
    "s_waitcnt vmcnt(0)\n"
    "v_cmp_eq_u32 vcc, 0, v81\n"
    "s_cmp_eq_u64 vcc, 0\n"
    "s_cbranch_scc1 .Lhp4_x%=\n"
    "s_add_u32 s52, s52, 1\n"
    "s_sleep 2\n"
    "global_load_dword v81, v80, %1 sc1\n"
    "s_cmp_lt_u32 s52, s53\n"
    "s_cbranch_scc1 .Lhp4_%=\n"
    "s_mov_b32 s53, 0\n"
    "v_mov_b32 v82, 0\n"
    "v_mov_b32 v83, 1\n"
    "global_store_dword v82, v83, %1 sc1\n"
    "s_waitcnt vmcnt(0)\n"
    ".Lhp4_x%=:\n"
    "v_readfirstlane_b32 s50, v81\n"
    "v_mbcnt_lo_u32_b32 v80, -1, 0\n"
    "v_mbcnt_hi_u32_b32 v80, -1, v80\n"
    "s_lshl_b32 s51, %2, 6\n"
    "v_add_u32 v80, s51, v80\n"
    "v_min_u32 v80, 129, v80\n"
    "v_lshlrev_b32 v80, 5, v80\n"
    "v_add_u32 v80, 0x26c00, v80\n"
    "ds_read2_b32 v[82:83], v80 offset0:1 offset1:2\n"
    "s_and_b32 s50, s50, 1\n"
    "s_waitcnt lgkmcnt(0)\n"
    "v_and_b32 v81, s50, v82\n"
    "v_lshlrev_b32 v81, 13, v81\n"
    "v_and_b32 v82, -2, v82\n"
    "v_xor_b32 v82, v81, v82\n"
    "v_and_b32 v81, s50, v83\n"
    "v_lshlrev_b32 v81, 13, v81\n"
    "v_and_b32 v83, -2, v83\n"
    "v_xor_b32 v83, v81, v83\n"
    "ds_write2_b32 v80, v82, v83 offset0:1 offset1:2\n"
    "s_waitcnt lgkmcnt(0)\n"
    "s_barrier\n"
    "ds_read_b32 v81, v7 offset:4\n"
    "v_add_u32 v80, %4, v4\n"
    "s_waitcnt lgkmcnt(0)\n"
    "v_readfirstlane_b32 s49, v81\n"
    "s_branch .Lhwait_%=\n"
    ".Lhend_%=:\n"
    "s_cmp_eq_u32 s50, 192\n"
    "s_cbranch_scc0 .Lhpol_%=\n"
    "s_waitcnt vmcnt(0) lgkmcnt(0)\n"
    "s_endpgm\n"
    ".p2align 6\n"
    ".Lrole2_%=:\n"
    "s_mov_b64 s[40:41], %0\n"
    "s_and_b32 s41, s41, 0xffff\n"
    "s_mov_b32 s42, 0xc000000\n"
    "s_mov_b32 s43, 0x20000\n"
    "s_mov_b64 s[92:93], %0\n"
    "s_and_b32 s93, s93, 0xffff\n"
    "s_mov_b32 s94, 0xc000000\n"
    "s_mov_b32 s95, 0x20000\n"
    "s_mov_b32 s53, 131072\n"
    "s_mov_b64 s[56:57], 0\n"
    "v_mbcnt_lo_u32_b32 v7, -1, 0\n"
    "v_mbcnt_hi_u32_b32 v7, -1, v7\n"
    "s_lshl_b32 s50, %2, 8\n"
    "s_add_u32 s50, s50, 147456\n"
    "v_lshl_add_u32 v7, v7, 2, s50\n"
    "ds_read_b32 v1, v7\n"
    "ds_read_b32 v4, v7 offset:1024\n"
    "ds_read_b32 v2, v7 offset:2048\n"
    "ds_read_b32 v8, v7 offset:3072\n"
    "ds_read_b32 v9, v7 offset:4096\n"
    "ds_read_b32 v10, v7 offset:5120\n"
    "ds_read_b32 v11, v7 offset:6144\n"
    "ds_read_b32 v3, v7 offset:7168\n"
    "s_waitcnt lgkmcnt(0)\n"
    "s_barrier\n"
    "s_mul_i32 s54, %2, 6144\n"
    "v_add_u32 v5, 16384, v4\n"
    "s_lshl_b32 s58, %2, 13\n"
    "s_add_u32 s58, s58, 0\n"
    "s_xor_b32 s50, %2, 0\n"
    "s_mul_i32 s50, s50, 6144\n"
    "v_add_u32 v32, s50, v9\n"
    "v_add_u32 v33, s50, v10\n"
    "v_add_u32 v34, s50, v11\n"
    "s_xor_b32 s50, %2, 1\n"
    "s_mul_i32 s50, s50, 6144\n"
    "v_add_u32 v35, s50, v9\n"
    "v_add_u32 v36, s50, v10\n"
    "v_add_u32 v37, s50, v11\n"
    "s_xor_b32 s50, %2, 2\n"
    "s_mul_i32 s50, s50, 6144\n"
    "v_add_u32 v38, s50, v9\n"
    "v_add_u32 v39, s50, v10\n"
    "v_add_u32 v40, s50, v11\n"
    "s_xor_b32 s50, %2, 3\n"
    "s_mul_i32 s50, s50, 6144\n"
    "v_add_u32 v41, s50, v9\n"
    "v_add_u32 v42, s50, v10\n"
    "v_add_u32 v43, s50, v11\n"
    "s_mov_b64 s[44:45], %8\n"
    "s_and_b32 s45, s45, 0xffff\n"
    "s_mov_b32 s46, 0x480000\n"
    "s_mov_b32 s47, 0x20000\n"
    "s_add_u32 m0, s54, 0\n"
    "s_add_u32 s55, %6, 0\n"
    "buffer_load_dwordx4 v8, s[44:47], s55 offen lds\n"
    "s_add_u32 m0, s54, 1024\n"
    "s_add_u32 s55, %6, 18432\n"
    "buffer_load_dwordx4 v8, s[44:47], s55 offen lds\n"
    "s_add_u32 m0, s54, 2048\n"
    "s_add_u32 s55, %6, 128\n"
    "buffer_load_dwordx4 v8, s[44:47], s55 offen lds\n"
    "s_add_u32 m0, s54, 3072\n"
    "s_add_u32 s55, %6, 18560\n"
    "buffer_load_dwordx4 v8, s[44:47], s55 offen lds\n"
    "s_add_u32 m0, s54, 4096\n"
    "s_add_u32 s55, %6, 256\n"
    "buffer_load_dwordx4 v8, s[44:47], s55 offen lds\n"
    "s_add_u32 m0, s54, 5120\n"
    "s_add_u32 s55, %6, 18688\n"
    "buffer_load_dwordx4 v8, s[44:47], s55 offen lds\n"
    "s_add_u32 m0, s54, 24576\n"
    "s_add_u32 s55, %6, 2304\n"
    "buffer_load_dwordx4 v8, s[44:47], s55 offen lds\n"
    "s_add_u32 m0, s54, 25600\n"
    "s_add_u32 s55, %6, 20736\n"
    "buffer_load_dwordx4 v8, s[44:47], s55 offen lds\n"
    "s_add_u32 m0, s54, 26624\n"
    "s_add_u32 s55, %6, 2432\n"
    "buffer_load_dwordx4 v8, s[44:47], s55 offen lds\n"
    "s_add_u32 m0, s54, 27648\n"
    "s_add_u32 s55, %6, 20864\n"
    "buffer_load_dwordx4 v8, s[44:47], s55 offen lds\n"
    "s_add_u32 m0, s54, 28672\n"
    "s_add_u32 s55, %6, 2560\n"
    "buffer_load_dwordx4 v8, s[44:47], s55 offen lds\n"
    "s_add_u32 m0, s54, 29696\n"
    "s_add_u32 s55, %6, 20992\n"
    "buffer_load_dwordx4 v8, s[44:47], s55 offen lds\n"
    "s_add_u32 m0, s54, 49152\n"
    "s_add_u32 s55, %6, 36864\n"
    "buffer_load_dwordx4 v8, s[44:47], s55 offen lds\n"
    "s_add_u32 m0, s54, 50176\n"
    "s_add_u32 s55, %6, 55296\n"
    "buffer_load_dwordx4 v8, s[44:47], s55 offen lds\n"
    "s_add_u32 m0, s54, 51200\n"
    "s_add_u32 s55, %6, 36992\n"
    "buffer_load_dwordx4 v8, s[44:47], s55 offen lds\n"
    "s_add_u32 m0, s54, 52224\n"
    "s_add_u32 s55, %6, 55424\n"
    "buffer_load_dwordx4 v8, s[44:47], s55 offen lds\n"
    "s_add_u32 m0, s54, 53248\n"
    "s_add_u32 s55, %6, 37120\n"
    "buffer_load_dwordx4 v8, s[44:47], s55 offen lds\n"
    "s_add_u32 m0, s54, 54272\n"
    "s_add_u32 s55, %6, 55552\n"
    "buffer_load_dwordx4 v8, s[44:47], s55 offen lds\n"
    "s_add_u32 m0, s54, 73728\n"
    "s_add_u32 s55, %6, 39168\n"
    "buffer_load_dwordx4 v8, s[44:47], s55 offen lds\n"
    "s_add_u32 m0, s54, 74752\n"
    "s_add_u32 s55, %6, 57600\n"
    "buffer_load_dwordx4 v8, s[44:47], s55 offen lds\n"
    "s_add_u32 m0, s54, 75776\n"
    "s_add_u32 s55, %6, 39296\n"
    "buffer_load_dwordx4 v8, s[44:47], s55 offen lds\n"
    "s_add_u32 m0, s54, 76800\n"
    "s_add_u32 s55, %6, 57728\n"
    "buffer_load_dwordx4 v8, s[44:47], s55 offen lds\n"
    "s_add_u32 m0, s54, 77824\n"
    "s_add_u32 s55, %6, 39424\n"
    "buffer_load_dwordx4 v8, s[44:47], s55 offen lds\n"
    "s_add_u32 m0, s54, 78848\n"
    "s_add_u32 s55, %6, 57856\n"
    "buffer_load_dwordx4 v8, s[44:47], s55 offen lds\n"
    "s_add_u32 m0, s54, 98304\n"
    "s_add_u32 s55, %6, 73728\n"
    "buffer_load_dwordx4 v8, s[44:47], s55 offen lds\n"
    "s_add_u32 m0, s54, 99328\n"
    "s_add_u32 s55, %6, 92160\n"
    "buffer_load_dwordx4 v8, s[44:47], s55 offen lds\n"
    "s_add_u32 m0, s54, 100352\n"
    "s_add_u32 s55, %6, 73856\n"
    "buffer_load_dwordx4 v8, s[44:47], s55 offen lds\n"
    "s_add_u32 m0, s54, 101376\n"
    "s_add_u32 s55, %6, 92288\n"
    "buffer_load_dwordx4 v8, s[44:47], s55 offen lds\n"
    "s_add_u32 m0, s54, 102400\n"
    "s_add_u32 s55, %6, 73984\n"
    "buffer_load_dwordx4 v8, s[44:47], s55 offen lds\n"
    "s_add_u32 m0, s54, 103424\n"
    "s_add_u32 s55, %6, 92416\n"
    "buffer_load_dwordx4 v8, s[44:47], s55 offen lds\n"
    "s_add_u32 m0, s54, 122880\n"
    "s_add_u32 s55, %6, 76032\n"
    "buffer_load_dwordx4 v8, s[44:47], s55 offen lds\n"
    "s_add_u32 m0, s54, 123904\n"
    "s_add_u32 s55, %6, 94464\n"
    "buffer_load_dwordx4 v8, s[44:47], s55 offen lds\n"
    "s_add_u32 m0, s54, 124928\n"
    "s_add_u32 s55, %6, 76160\n"
    "buffer_load_dwordx4 v8, s[44:47], s55 offen lds\n"
    "s_add_u32 m0, s54, 125952\n"
    "s_add_u32 s55, %6, 94592\n"
    "buffer_load_dwordx4 v8, s[44:47], s55 offen lds\n"
    "s_add_u32 m0, s54, 126976\n"
    "s_add_u32 s55, %6, 76288\n"
    "buffer_load_dwordx4 v8, s[44:47], s55 offen lds\n"
    "s_add_u32 m0, s54, 128000\n"
    "s_add_u32 s55, %6, 94720\n"
    "buffer_load_dwordx4 v8, s[44:47], s55 offen lds\n"
    "s_waitcnt vmcnt(30) lgkmcnt(0)\n"
    "s_barrier\n"
    "ds_read_b64_tr_b16 a[0:1], v32\n"
    "ds_read_b64_tr_b16 a[4:5], v33\n"
    "ds_read_b64_tr_b16 a[8:9], v34\n"
    "ds_read_b64_tr_b16 a[96:97], v35\n"
    "ds_read_b64_tr_b16 a[100:101], v36\n"
    "ds_read_b64_tr_b16 a[104:105], v37\n"
    "ds_read_b64_tr_b16 a[192:193], v38\n"
    "ds_read_b64_tr_b16 a[196:197], v39\n"
    "ds_read_b64_tr_b16 a[200:201], v40\n"
    "ds_read_b64_tr_b16 v[160:161], v41\n"
    "ds_read_b64_tr_b16 v[164:165], v42\n"
    "ds_read_b64_tr_b16 v[168:169], v43\n"
    "s_waitcnt vmcnt(24) lgkmcnt(0)\n"
    "s_barrier\n"
    "s_add_u32 m0, s54, 0\n"
    "s_add_u32 s55, %6, 110592\n"
    "buffer_load_dwordx4 v8, s[44:47], s55 offen lds\n"
    "s_add_u32 m0, s54, 1024\n"
    "s_add_u32 s55, %6, 129024\n"
    "buffer_load_dwordx4 v8, s[44:47], s55 offen lds\n"
    "s_add_u32 m0, s54, 2048\n"
    "s_add_u32 s55, %6, 110720\n"
    "buffer_load_dwordx4 v8, s[44:47], s55 offen lds\n"
    "s_add_u32 m0, s54, 3072\n"
    "s_add_u32 s55, %6, 129152\n"
    "buffer_load_dwordx4 v8, s[44:47], s55 offen lds\n"
    "s_add_u32 m0, s54, 4096\n"
    "s_add_u32 s55, %6, 110848\n"
    "buffer_load_dwordx4 v8, s[44:47], s55 offen lds\n"
    "s_add_u32 m0, s54, 5120\n"
    "s_add_u32 s55, %6, 129280\n"
    "buffer_load_dwordx4 v8, s[44:47], s55 offen lds\n"
    "ds_read_b64_tr_b16 a[2:3], v32 offset:24576\n"
    "ds_read_b64_tr_b16 a[6:7], v33 offset:24576\n"
    "ds_read_b64_tr_b16 a[10:11], v34 offset:24576\n"
    "ds_read_b64_tr_b16 a[98:99], v35 offset:24576\n"
    "ds_read_b64_tr_b16 a[102:103], v36 offset:24576\n"
    "ds_read_b64_tr_b16 a[106:107], v37 offset:24576\n"
    "ds_read_b64_tr_b16 a[194:195], v38 offset:24576\n"
    "ds_read_b64_tr_b16 a[198:199], v39 offset:24576\n"
    "ds_read_b64_tr_b16 a[202:203], v40 offset:24576\n"
    "ds_read_b64_tr_b16 v[162:163], v41 offset:24576\n"
    "ds_read_b64_tr_b16 v[166:167], v42 offset:24576\n"
    "ds_read_b64_tr_b16 v[170:171], v43 offset:24576\n"
    "s_waitcnt vmcnt(24) lgkmcnt(0)\n"
    "s_barrier\n"
    "s_add_u32 m0, s54, 24576\n"
    "s_add_u32 s55, %6, 112896\n"
    "buffer_load_dwordx4 v8, s[44:47], s55 offen lds\n"
    "s_add_u32 m0, s54, 25600\n"
    "s_add_u32 s55, %6, 131328\n"
    "buffer_load_dwordx4 v8, s[44:47], s55 offen lds\n"
    "s_add_u32 m0, s54, 26624\n"
    "s_add_u32 s55, %6, 113024\n"
    "buffer_load_dwordx4 v8, s[44:47], s55 offen lds\n"
    "s_add_u32 m0, s54, 27648\n"
    "s_add_u32 s55, %6, 131456\n"
    "buffer_load_dwordx4 v8, s[44:47], s55 offen lds\n"
    "s_add_u32 m0, s54, 28672\n"
    "s_add_u32 s55, %6, 113152\n"
    "buffer_load_dwordx4 v8, s[44:47], s55 offen lds\n"
    "s_add_u32 m0, s54, 29696\n"
    "s_add_u32 s55, %6, 131584\n"
    "buffer_load_dwordx4 v8, s[44:47], s55 offen lds\n"
    "ds_read_b64_tr_b16 a[12:13], v32 offset:49152\n"
    "ds_read_b64_tr_b16 a[16:17], v33 offset:49152\n"
    "ds_read_b64_tr_b16 a[20:21], v34 offset:49152\n"
    "ds_read_b64_tr_b16 a[108:109], v35 offset:49152\n"
    "ds_read_b64_tr_b16 a[112:113], v36 offset:49152\n"
    "ds_read_b64_tr_b16 a[116:117], v37 offset:49152\n"
    "ds_read_b64_tr_b16 a[204:205], v38 offset:49152\n"
    "ds_read_b64_tr_b16 a[208:209], v39 offset:49152\n"
    "ds_read_b64_tr_b16 a[212:213], v40 offset:49152\n"
    "ds_read_b64_tr_b16 v[172:173], v41 offset:49152\n"
    "ds_read_b64_tr_b16 v[176:177], v42 offset:49152\n"
    "ds_read_b64_tr_b16 v[180:181], v43 offset:49152\n"
    "s_waitcnt vmcnt(24) lgkmcnt(0)\n"
    "s_barrier\n"
    "s_add_u32 m0, s54, 49152\n"
    "s_add_u32 s55, %6, 147456\n"
    "buffer_load_dwordx4 v8, s[44:47], s55 offen lds\n"
    "s_add_u32 m0, s54, 50176\n"
    "s_add_u32 s55, %6, 165888\n"
    "buffer_load_dwordx4 v8, s[44:47], s55 offen lds\n"
    "s_add_u32 m0, s54, 51200\n"
    "s_add_u32 s55, %6, 147584\n"
    "buffer_load_dwordx4 v8, s[44:47], s55 offen lds\n"
    "s_add_u32 m0, s54, 52224\n"
    "s_add_u32 s55, %6, 166016\n"
    "buffer_load_dwordx4 v8, s[44:47], s55 offen lds\n"
    "s_add_u32 m0, s54, 53248\n"
    "s_add_u32 s55, %6, 147712\n"
    "buffer_load_dwordx4 v8, s[44:47], s55 offen lds\n"
    "s_add_u32 m0, s54, 54272\n"
    "s_add_u32 s55, %6, 166144\n"
    "buffer_load_dwordx4 v8, s[44:47], s55 offen lds\n"
    "v_add_u32 v71, 0x10000, v32\n"
    "ds_read_b64_tr_b16 a[14:15], v71 offset:8192\n"
    "v_add_u32 v70, 0x10000, v33\n"
    "ds_read_b64_tr_b16 a[18:19], v70 offset:8192\n"
    "v_add_u32 v69, 0x10000, v34\n"
    "ds_read_b64_tr_b16 a[22:23], v69 offset:8192\n"
    "v_add_u32 v68, 0x10000, v35\n"
    "ds_read_b64_tr_b16 a[110:111], v68 offset:8192\n"
    "v_add_u32 v67, 0x10000, v36\n"
    "ds_read_b64_tr_b16 a[114:115], v67 offset:8192\n"
    "v_add_u32 v66, 0x10000, v37\n"
    "ds_read_b64_tr_b16 a[118:119], v66 offset:8192\n"
    "v_add_u32 v65, 0x10000, v38\n"
    "ds_read_b64_tr_b16 a[206:207], v65 offset:8192\n"
    "v_add_u32 v64, 0x10000, v39\n"
    "ds_read_b64_tr_b16 a[210:211], v64 offset:8192\n"
    "v_add_u32 v63, 0x10000, v40\n"
    "ds_read_b64_tr_b16 a[214:215], v63 offset:8192\n"
    "v_add_u32 v62, 0x10000, v41\n"
    "ds_read_b64_tr_b16 v[174:175], v62 offset:8192\n"
    "v_add_u32 v61, 0x10000, v42\n"
    "ds_read_b64_tr_b16 v[178:179], v61 offset:8192\n"
    "v_add_u32 v60, 0x10000, v43\n"
    "ds_read_b64_tr_b16 v[182:183], v60 offset:8192\n"
    "s_waitcnt vmcnt(24) lgkmcnt(0)\n"
    "s_barrier\n"
    "s_add_u32 m0, s54, 73728\n"
    "s_add_u32 s55, %6, 149760\n"
    "buffer_load_dwordx4 v8, s[44:47], s55 offen lds\n"
    "s_add_u32 m0, s54, 74752\n"
    "s_add_u32 s55, %6, 168192\n"
    "buffer_load_dwordx4 v8, s[44:47], s55 offen lds\n"
    "s_add_u32 m0, s54, 75776\n"
    "s_add_u32 s55, %6, 149888\n"
    "buffer_load_dwordx4 v8, s[44:47], s55 offen lds\n"
    "s_add_u32 m0, s54, 76800\n"
    "s_add_u32 s55, %6, 168320\n"
    "buffer_load_dwordx4 v8, s[44:47], s55 offen lds\n"
    "s_add_u32 m0, s54, 77824\n"
    "s_add_u32 s55, %6, 150016\n"
    "buffer_load_dwordx4 v8, s[44:47], s55 offen lds\n"
    "s_add_u32 m0, s54, 78848\n"
    "s_add_u32 s55, %6, 168448\n"
    "buffer_load_dwordx4 v8, s[44:47], s55 offen lds\n"
    "ds_read_b64_tr_b16 a[24:25], v71 offset:32768\n"
    "ds_read_b64_tr_b16 a[28:29], v70 offset:32768\n"
    "ds_read_b64_tr_b16 a[32:33], v69 offset:32768\n"
    "ds_read_b64_tr_b16 a[120:121], v68 offset:32768\n"
    "ds_read_b64_tr_b16 a[124:125], v67 offset:32768\n"
    "ds_read_b64_tr_b16 a[128:129], v66 offset:32768\n"
    "ds_read_b64_tr_b16 a[216:217], v65 offset:32768\n"
    "ds_read_b64_tr_b16 a[220:221], v64 offset:32768\n"
    "ds_read_b64_tr_b16 a[224:225], v63 offset:32768\n"
    "ds_read_b64_tr_b16 v[184:185], v62 offset:32768\n"
    "ds_read_b64_tr_b16 v[188:189], v61 offset:32768\n"
    "ds_read_b64_tr_b16 v[192:193], v60 offset:32768\n"
    "s_waitcnt vmcnt(24) lgkmcnt(0)\n"
    "s_barrier\n"
    "s_add_u32 m0, s54, 98304\n"
    "s_add_u32 s55, %6, 184320\n"
    "buffer_load_dwordx4 v8, s[44:47], s55 offen lds\n"
    "s_add_u32 m0, s54, 99328\n"
    "s_add_u32 s55, %6, 202752\n"
    "buffer_load_dwordx4 v8, s[44:47], s55 offen lds\n"
    "s_add_u32 m0, s54, 100352\n"
    "s_add_u32 s55, %6, 184448\n"
    "buffer_load_dwordx4 v8, s[44:47], s55 offen lds\n"
    "s_add_u32 m0, s54, 101376\n"
    "s_add_u32 s55, %6, 202880\n"
    "buffer_load_dwordx4 v8, s[44:47], s55 offen lds\n"
    "s_add_u32 m0, s54, 102400\n"
    "s_add_u32 s55, %6, 184576\n"
    "buffer_load_dwordx4 v8, s[44:47], s55 offen lds\n"
    "s_add_u32 m0, s54, 103424\n"
    "s_add_u32 s55, %6, 203008\n"
    "buffer_load_dwordx4 v8, s[44:47], s55 offen lds\n"
    "ds_read_b64_tr_b16 a[26:27], v71 offset:57344\n"
    "ds_read_b64_tr_b16 a[30:31], v70 offset:57344\n"
    "ds_read_b64_tr_b16 a[34:35], v69 offset:57344\n"
    "ds_read_b64_tr_b16 a[122:123], v68 offset:57344\n"
    "ds_read_b64_tr_b16 a[126:127], v67 offset:57344\n"
    "ds_read_b64_tr_b16 a[130:131], v66 offset:57344\n"
    "ds_read_b64_tr_b16 a[218:219], v65 offset:57344\n"
    "ds_read_b64_tr_b16 a[222:223], v64 offset:57344\n"
    "ds_read_b64_tr_b16 a[226:227], v63 offset:57344\n"
    "ds_read_b64_tr_b16 v[186:187], v62 offset:57344\n"
    "ds_read_b64_tr_b16 v[190:191], v61 offset:57344\n"
    "ds_read_b64_tr_b16 v[194:195], v60 offset:57344\n"
    "s_waitcnt vmcnt(24) lgkmcnt(0)\n"
    "s_barrier\n"
    "s_add_u32 m0, s54, 122880\n"
    "s_add_u32 s55, %6, 186624\n"
    "buffer_load_dwordx4 v8, s[44:47], s55 offen lds\n"
    "s_add_u32 m0, s54, 123904\n"
    "s_add_u32 s55, %6, 205056\n"
    "buffer_load_dwordx4 v8, s[44:47], s55 offen lds\n"
    "s_add_u32 m0, s54, 124928\n"
    "s_add_u32 s55, %6, 186752\n"
    "buffer_load_dwordx4 v8, s[44:47], s55 offen lds\n"
    "s_add_u32 m0, s54, 125952\n"
    "s_add_u32 s55, %6, 205184\n"
    "buffer_load_dwordx4 v8, s[44:47], s55 offen lds\n"
    "s_add_u32 m0, s54, 126976\n"
    "s_add_u32 s55, %6, 186880\n"
    "buffer_load_dwordx4 v8, s[44:47], s55 offen lds\n"
    "s_add_u32 m0, s54, 128000\n"
    "s_add_u32 s55, %6, 205312\n"
    "buffer_load_dwordx4 v8, s[44:47], s55 offen lds\n"
    "ds_read_b64_tr_b16 a[36:37], v32\n"
    "ds_read_b64_tr_b16 a[40:41], v33\n"
    "ds_read_b64_tr_b16 a[44:45], v34\n"
    "ds_read_b64_tr_b16 a[132:133], v35\n"
    "ds_read_b64_tr_b16 a[136:137], v36\n"
    "ds_read_b64_tr_b16 a[140:141], v37\n"
    "ds_read_b64_tr_b16 a[228:229], v38\n"
    "ds_read_b64_tr_b16 a[232:233], v39\n"
    "ds_read_b64_tr_b16 a[236:237], v40\n"
    "ds_read_b64_tr_b16 v[196:197], v41\n"
    "ds_read_b64_tr_b16 v[200:201], v42\n"
    "ds_read_b64_tr_b16 v[204:205], v43\n"
    "s_waitcnt vmcnt(24) lgkmcnt(0)\n"
    "s_barrier\n"
    "s_add_u32 m0, s54, 0\n"
    "s_add_u32 s55, %6, 221184\n"
    "buffer_load_dwordx4 v8, s[44:47], s55 offen lds\n"
    "s_add_u32 m0, s54, 1024\n"
    "s_add_u32 s55, %6, 239616\n"
    "buffer_load_dwordx4 v8, s[44:47], s55 offen lds\n"
    "s_add_u32 m0, s54, 2048\n"
    "s_add_u32 s55, %6, 221312\n"
    "buffer_load_dwordx4 v8, s[44:47], s55 offen lds\n"
    "s_add_u32 m0, s54, 3072\n"
    "s_add_u32 s55, %6, 239744\n"
    "buffer_load_dwordx4 v8, s[44:47], s55 offen lds\n"
    "s_add_u32 m0, s54, 4096\n"
    "s_add_u32 s55, %6, 221440\n"
    "buffer_load_dwordx4 v8, s[44:47], s55 offen lds\n"
    "s_add_u32 m0, s54, 5120\n"
    "s_add_u32 s55, %6, 239872\n"
    "buffer_load_dwordx4 v8, s[44:47], s55 offen lds\n"
    "ds_read_b64_tr_b16 a[38:39], v32 offset:24576\n"
    "ds_read_b64_tr_b16 a[42:43], v33 offset:24576\n"
    "ds_read_b64_tr_b16 a[46:47], v34 offset:24576\n"
    "ds_read_b64_tr_b16 a[134:135], v35 offset:24576\n"
    "ds_read_b64_tr_b16 a[138:139], v36 offset:24576\n"
    "ds_read_b64_tr_b16 a[142:143], v37 offset:24576\n"
    "ds_read_b64_tr_b16 a[230:231], v38 offset:24576\n"
    "ds_read_b64_tr_b16 a[234:235], v39 offset:24576\n"
    "ds_read_b64_tr_b16 a[238:239], v40 offset:24576\n"
    "ds_read_b64_tr_b16 v[198:199], v41 offset:24576\n"
    "ds_read_b64_tr_b16 v[202:203], v42 offset:24576\n"
    "ds_read_b64_tr_b16 v[206:207], v43 offset:24576\n"
    "s_waitcnt vmcnt(24) lgkmcnt(0)\n"
    "s_barrier\n"
    "s_add_u32 m0, s54, 24576\n"
    "s_add_u32 s55, %6, 223488\n"
    "buffer_load_dwordx4 v8, s[44:47], s55 offen lds\n"
    "s_add_u32 m0, s54, 25600\n"
    "s_add_u32 s55, %6, 241920\n"
    "buffer_load_dwordx4 v8, s[44:47], s55 offen lds\n"
    "s_add_u32 m0, s54, 26624\n"
    "s_add_u32 s55, %6, 223616\n"
    "buffer_load_dwordx4 v8, s[44:47], s55 offen lds\n"
    "s_add_u32 m0, s54, 27648\n"
    "s_add_u32 s55, %6, 242048\n"
    "buffer_load_dwordx4 v8, s[44:47], s55 offen lds\n"
    "s_add_u32 m0, s54, 28672\n"
    "s_add_u32 s55, %6, 223744\n"
    "buffer_load_dwordx4 v8, s[44:47], s55 offen lds\n"
    "s_add_u32 m0, s54, 29696\n"
    "s_add_u32 s55, %6, 242176\n"
    "buffer_load_dwordx4 v8, s[44:47], s55 offen lds\n"
    "ds_read_b64_tr_b16 a[48:49], v32 offset:49152\n"
    "ds_read_b64_tr_b16 a[52:53], v33 offset:49152\n"
    "ds_read_b64_tr_b16 a[56:57], v34 offset:49152\n"
    "ds_read_b64_tr_b16 a[144:145], v35 offset:49152\n"
    "ds_read_b64_tr_b16 a[148:149], v36 offset:49152\n"
    "ds_read_b64_tr_b16 a[152:153], v37 offset:49152\n"
    "ds_read_b64_tr_b16 a[240:241], v38 offset:49152\n"
    "ds_read_b64_tr_b16 a[244:245], v39 offset:49152\n"
    "ds_read_b64_tr_b16 a[248:249], v40 offset:49152\n"
    "ds_read_b64_tr_b16 v[208:209], v41 offset:49152\n"
    "ds_read_b64_tr_b16 v[212:213], v42 offset:49152\n"
    "ds_read_b64_tr_b16 v[216:217], v43 offset:49152\n"
    "s_waitcnt vmcnt(24) lgkmcnt(0)\n"
    "s_barrier\n"
    "s_add_u32 m0, s54, 49152\n"
    "s_add_u32 s55, %6, 258048\n"
    "buffer_load_dwordx4 v8, s[44:47], s55 offen lds\n"
    "s_add_u32 m0, s54, 50176\n"
    "s_add_u32 s55, %6, 276480\n"
    "buffer_load_dwordx4 v8, s[44:47], s55 offen lds\n"
    "s_add_u32 m0, s54, 51200\n"
    "s_add_u32 s55, %6, 258176\n"
    "buffer_load_dwordx4 v8, s[44:47], s55 offen lds\n"
    "s_add_u32 m0, s54, 52224\n"
    "s_add_u32 s55, %6, 276608\n"
    "buffer_load_dwordx4 v8, s[44:47], s55 offen lds\n"
    "s_add_u32 m0, s54, 53248\n"
    "s_add_u32 s55, %6, 258304\n"
    "buffer_load_dwordx4 v8, s[44:47], s55 offen lds\n"
    "s_add_u32 m0, s54, 54272\n"
    "s_add_u32 s55, %6, 276736\n"
    "buffer_load_dwordx4 v8, s[44:47], s55 offen lds\n"
    "ds_read_b64_tr_b16 a[50:51], v71 offset:8192\n"
    "ds_read_b64_tr_b16 a[54:55], v70 offset:8192\n"
    "ds_read_b64_tr_b16 a[58:59], v69 offset:8192\n"
    "ds_read_b64_tr_b16 a[146:147], v68 offset:8192\n"
    "ds_read_b64_tr_b16 a[150:151], v67 offset:8192\n"
    "ds_read_b64_tr_b16 a[154:155], v66 offset:8192\n"
    "ds_read_b64_tr_b16 a[242:243], v65 offset:8192\n"
    "ds_read_b64_tr_b16 a[246:247], v64 offset:8192\n"
    "ds_read_b64_tr_b16 a[250:251], v63 offset:8192\n"
    "ds_read_b64_tr_b16 v[210:211], v62 offset:8192\n"
    "ds_read_b64_tr_b16 v[214:215], v61 offset:8192\n"
    "ds_read_b64_tr_b16 v[218:219], v60 offset:8192\n"
    "s_waitcnt vmcnt(24) lgkmcnt(0)\n"
    "s_barrier\n"
    "s_add_u32 m0, s54, 73728\n"
    "s_add_u32 s55, %6, 260352\n"
    "buffer_load_dwordx4 v8, s[44:47], s55 offen lds\n"
    "s_add_u32 m0, s54, 74752\n"
    "s_add_u32 s55, %6, 278784\n"
    "buffer_load_dwordx4 v8, s[44:47], s55 offen lds\n"
    "s_add_u32 m0, s54, 75776\n"
    "s_add_u32 s55, %6, 260480\n"
    "buffer_load_dwordx4 v8, s[44:47], s55 offen lds\n"
    "s_add_u32 m0, s54, 76800\n"
    "s_add_u32 s55, %6, 278912\n"
    "buffer_load_dwordx4 v8, s[44:47], s55 offen lds\n"
    "s_add_u32 m0, s54, 77824\n"
    "s_add_u32 s55, %6, 260608\n"
    "buffer_load_dwordx4 v8, s[44:47], s55 offen lds\n"
    "s_add_u32 m0, s54, 78848\n"
    "s_add_u32 s55, %6, 279040\n"
    "buffer_load_dwordx4 v8, s[44:47], s55 offen lds\n"
    "v_mov_b32 v7, 0x20400\n"
    "global_load_dwordx2 v[20:21], v7, %1 sc1\n"
    "v_mbcnt_lo_u32_b32 v6, -1, 0\n"
    "v_min_u32 v6, 7, v6\n"
    "v_lshlrev_b32 v6, 7, v6\n"
    "v_add_u32 v6, 0x20000, v6\n"
    "global_load_dwordx2 v[22:23], v6, %1 sc1\n"
    "ds_read_b64_tr_b16 a[60:61], v71 offset:32768\n"
    "ds_read_b64_tr_b16 a[64:65], v70 offset:32768\n"
    "ds_read_b64_tr_b16 a[68:69], v69 offset:32768\n"
    "ds_read_b64_tr_b16 a[156:157], v68 offset:32768\n"
    "ds_read_b64_tr_b16 a[160:161], v67 offset:32768\n"
    "ds_read_b64_tr_b16 a[164:165], v66 offset:32768\n"
    "ds_read_b64_tr_b16 a[252:253], v65 offset:32768\n"
    "ds_read_b64_tr_b16 v[128:129], v64 offset:32768\n"
    "ds_read_b64_tr_b16 v[132:133], v63 offset:32768\n"
    "ds_read_b64_tr_b16 v[220:221], v62 offset:32768\n"
    "ds_read_b64_tr_b16 v[224:225], v61 offset:32768\n"
    "ds_read_b64_tr_b16 v[228:229], v60 offset:32768\n"
    "s_waitcnt vmcnt(26) lgkmcnt(0)\n"
    "s_barrier\n"
    "ds_read_b64_tr_b16 a[62:63], v71 offset:57344\n"
    "ds_read_b64_tr_b16 a[66:67], v70 offset:57344\n"
    "ds_read_b64_tr_b16 a[70:71], v69 offset:57344\n"
    "ds_read_b64_tr_b16 a[158:159], v68 offset:57344\n"
    "ds_read_b64_tr_b16 a[162:163], v67 offset:57344\n"
    "ds_read_b64_tr_b16 a[166:167], v66 offset:57344\n"
    "ds_read_b64_tr_b16 a[254:255], v65 offset:57344\n"
    "ds_read_b64_tr_b16 v[130:131], v64 offset:57344\n"
    "ds_read_b64_tr_b16 v[134:135], v63 offset:57344\n"
    "ds_read_b64_tr_b16 v[222:223], v62 offset:57344\n"
    "ds_read_b64_tr_b16 v[226:227], v61 offset:57344\n"
    "ds_read_b64_tr_b16 v[230:231], v60 offset:57344\n"
    "s_waitcnt vmcnt(20) lgkmcnt(0)\n"
    "s_barrier\n"
    "ds_read_b64_tr_b16 a[72:73], v32\n"
    "ds_read_b64_tr_b16 a[76:77], v33\n"
    "ds_read_b64_tr_b16 a[80:81], v34\n"
    "ds_read_b64_tr_b16 a[168:169], v35\n"
    "ds_read_b64_tr_b16 a[172:173], v36\n"
    "ds_read_b64_tr_b16 a[176:177], v37\n"
    "ds_read_b64_tr_b16 v[136:137], v38\n"
    "ds_read_b64_tr_b16 v[140:141], v39\n"
    "ds_read_b64_tr_b16 v[144:145], v40\n"
    "ds_read_b64_tr_b16 v[232:233], v41\n"
    "ds_read_b64_tr_b16 v[236:237], v42\n"
    "ds_read_b64_tr_b16 v[240:241], v43\n"
    "s_waitcnt vmcnt(14) lgkmcnt(0)\n"
    "s_barrier\n"
    "ds_read_b64_tr_b16 a[74:75], v32 offset:24576\n"
    "ds_read_b64_tr_b16 a[78:79], v33 offset:24576\n"
    "ds_read_b64_tr_b16 a[82:83], v34 offset:24576\n"
    "ds_read_b64_tr_b16 a[170:171], v35 offset:24576\n"
    "ds_read_b64_tr_b16 a[174:175], v36 offset:24576\n"
    "ds_read_b64_tr_b16 a[178:179], v37 offset:24576\n"
    "ds_read_b64_tr_b16 v[138:139], v38 offset:24576\n"
    "ds_read_b64_tr_b16 v[142:143], v39 offset:24576\n"
    "ds_read_b64_tr_b16 v[146:147], v40 offset:24576\n"
    "ds_read_b64_tr_b16 v[234:235], v41 offset:24576\n"
    "ds_read_b64_tr_b16 v[238:239], v42 offset:24576\n"
    "ds_read_b64_tr_b16 v[242:243], v43 offset:24576\n"
    "s_waitcnt vmcnt(8) lgkmcnt(0)\n"
    "s_barrier\n"
    "ds_read_b64_tr_b16 a[84:85], v32 offset:49152\n"
    "ds_read_b64_tr_b16 a[88:89], v33 offset:49152\n"
    "ds_read_b64_tr_b16 a[92:93], v34 offset:49152\n"
    "ds_read_b64_tr_b16 a[180:181], v35 offset:49152\n"
    "ds_read_b64_tr_b16 a[184:185], v36 offset:49152\n"
    "ds_read_b64_tr_b16 a[188:189], v37 offset:49152\n"
    "ds_read_b64_tr_b16 v[148:149], v38 offset:49152\n"
    "ds_read_b64_tr_b16 v[152:153], v39 offset:49152\n"
    "ds_read_b64_tr_b16 v[156:157], v40 offset:49152\n"
    "ds_read_b64_tr_b16 v[244:245], v41 offset:49152\n"
    "ds_read_b64_tr_b16 v[248:249], v42 offset:49152\n"
    "ds_read_b64_tr_b16 v[252:253], v43 offset:49152\n"
    "s_waitcnt vmcnt(2) lgkmcnt(0)\n"
    "s_barrier\n"
    "ds_read_b64_tr_b16 a[86:87], v71 offset:8192\n"
    "ds_read_b64_tr_b16 a[90:91], v70 offset:8192\n"
    "ds_read_b64_tr_b16 a[94:95], v69 offset:8192\n"
    "ds_read_b64_tr_b16 a[182:183], v68 offset:8192\n"
    "ds_read_b64_tr_b16 a[186:187], v67 offset:8192\n"
    "ds_read_b64_tr_b16 a[190:191], v66 offset:8192\n"
    "ds_read_b64_tr_b16 v[150:151], v65 offset:8192\n"
    "ds_read_b64_tr_b16 v[154:155], v64 offset:8192\n"
    "ds_read_b64_tr_b16 v[158:159], v63 offset:8192\n"
    "ds_read_b64_tr_b16 v[246:247], v62 offset:8192\n"
    "ds_read_b64_tr_b16 v[250:251], v61 offset:8192\n"
    "ds_read_b64_tr_b16 v[254:255], v60 offset:8192\n"
    "s_waitcnt lgkmcnt(0)\n"
    "s_barrier\n"
    "s_mov_b64 s[44:45], %1\n"
    "s_and_b32 s45, s45, 0xffff\n"
    "s_mov_b32 s46, 0x0\n"
    "s_mov_b32 s47, 0x20000\n"
    "v_mov_b32 v97, 0\n"
    "s_waitcnt vmcnt(0)\n"
    "v_add_u32 v20, 1, v20\n"
    "v_add_u32 v21, 1, v21\n"
    "s_mov_b32 s52, 1048576\n"
    ".Lwgo_a%=:\n"
    "v_cmp_ne_u64 vcc, v[20:21], v[22:23]\n"
    "s_cbranch_vccz .Lwgo_x%=\n"
    "global_load_dwordx2 v[22:23], v6, %1 sc1\n"
    "s_sub_u32 s52, s52, 1\n"
    "s_waitcnt vmcnt(0)\n"
    "s_cbranch_scc0 .Lwgo_a%=\n"
    ".Lwgo_x%=:\n"
    "v_mbcnt_lo_u32_b32 v20, -1, 0\n"
    "v_mbcnt_hi_u32_b32 v20, -1, v20\n"
    "v_min_u32 v6, 7, v20\n"
    "v_lshlrev_b32 v6, 7, v6\n"
    "v_mov_b32 v7, 0x26c00\n"
    "ds_read_b128 v[20:23], v7\n"
    "s_waitcnt lgkmcnt(0)\n"
    "v_readfirstlane_b32 s49, v21\n"
    "v_add_u32 v20, v20, v6\n"
    "s_branch .Lwchk_%=\n"
    ".Lwgo_%=:\n"
    "s_waitcnt lgkmcnt(3)\n"
    "v_mfma_f32_16x16x32_bf16 v[8:11], a[0:3], v[64:67], 0\n"
    "v_add_u32 v7, 32, v7\n"
    "v_mfma_f32_16x16x32_bf16 v[12:15], a[4:7], v[64:67], 0\n"
    "ds_read_b128 v[20:23], v7\n"
    "s_waitcnt lgkmcnt(3)\n"
    "v_mfma_f32_16x16x32_bf16 v[8:11], a[12:15], v[68:71], v[8:11]\n"
    "v_cvt_pk_bf16_f32 v28, v16, v17\n"
    "v_cvt_pk_bf16_f32 v30, v16, v17\n"
    "v_mfma_f32_16x16x32_bf16 v[12:15], a[16:19], v[68:71], v[12:15]\n"
    "v_cvt_pk_bf16_f32 v29, v18, v19\n"
    "v_cvt_pk_bf16_f32 v31, v18, v19\n"
    "s_waitcnt lgkmcnt(2)\n"
    "v_mfma_f32_16x16x32_bf16 v[8:11], a[24:27], v[72:75], v[8:11]\n"
    "s_waitcnt vmcnt(4)\n"
    "ds_read_b128 v[80:83], v1 offset:4096\n"
    "v_permlane16_swap_b32 v28, v30\n"
    "v_mfma_f32_16x16x32_bf16 v[12:15], a[28:31], v[72:75], v[12:15]\n"
    "ds_read_b128 v[84:87], v1 offset:5120\n"
    "v_permlane16_swap_b32 v29, v31\n"
    "s_waitcnt lgkmcnt(3)\n"
    "v_mfma_f32_16x16x32_bf16 v[8:11], a[36:39], v[76:79], v[8:11]\n"
    "buffer_store_dwordx4 v[28:31], v3, s[40:43], s62 offen sc1\n"
    "v_mfma_f32_16x16x32_bf16 v[12:15], a[40:43], v[76:79], v[12:15]\n"
    "s_waitcnt vmcnt(3)\n"
    "ds_read_b128 v[88:91], v1 offset:6144\n"
    "s_waitcnt lgkmcnt(3)\n"
    "v_readfirstlane_b32 s49, v21\n"
    "v_mfma_f32_16x16x32_bf16 v[16:19], a[8:11], v[64:67], 0\n"
    "ds_read_b128 v[92:95], v1 offset:7168\n"
    "v_readfirstlane_b32 s62, v22\n"
    "v_mfma_f32_16x16x32_bf16 v[16:19], a[20:23], v[68:71], v[16:19]\n"
    "v_add_u32 v20, v20, v6\n"
    ".Lwtop_%=:\n"
    "v_mfma_f32_16x16x32_bf16 v[16:19], a[32:35], v[72:75], v[16:19]\n"
    "v_xor_b32 v0, 0x2000, v1\n"
    "v_mfma_f32_16x16x32_bf16 v[16:19], a[44:47], v[76:79], v[16:19]\n"
    "s_xor_b32 s58, s58, 0x8000\n"
    "s_waitcnt lgkmcnt(3)\n"
    "v_mfma_f32_16x16x32_bf16 v[8:11], a[48:51], v[80:83], v[8:11]\n"
    "s_barrier\n"
    "ds_read_b128 v[32:35], v0\n"
    "v_mfma_f32_16x16x32_bf16 v[12:15], a[52:55], v[80:83], v[12:15]\n"
    "ds_read_b128 v[36:39], v0 offset:1024\n"
    "v_mfma_f32_16x16x32_bf16 v[16:19], a[56:59], v[80:83], v[16:19]\n"
    "ds_read_b128 v[40:43], v0 offset:2048\n"
    "s_waitcnt lgkmcnt(5)\n"
    "v_mfma_f32_16x16x32_bf16 v[8:11], a[60:63], v[84:87], v[8:11]\n"
    "ds_read_b128 v[44:47], v0 offset:3072\n"
    "v_mfma_f32_16x16x32_bf16 v[12:15], a[64:67], v[84:87], v[12:15]\n"
    "ds_read_b128 v[48:51], v0 offset:4096\n"
    "v_mfma_f32_16x16x32_bf16 v[16:19], a[68:71], v[84:87], v[16:19]\n"
    "ds_read_b128 v[52:55], v0 offset:5120\n"
    "s_waitcnt lgkmcnt(7)\n"
    "v_mfma_f32_16x16x32_bf16 v[8:11], a[72:75], v[88:91], v[8:11]\n"
    "ds_read_b128 v[56:59], v0 offset:6144\n"
    "v_mfma_f32_16x16x32_bf16 v[12:15], a[76:79], v[88:91], v[12:15]\n"
    "ds_read_b128 v[60:63], v0 offset:7168\n"
    "v_mfma_f32_16x16x32_bf16 v[16:19], a[80:83], v[88:91], v[16:19]\n"
    "s_waitcnt vmcnt(2)\n"
    "v_cmp_eq_u32 vcc, 0, v97\n"
    "s_waitcnt lgkmcnt(8)\n"
    "v_mfma_f32_16x16x32_bf16 v[8:11], a[84:87], v[92:95], v[8:11]\n"
    "s_cmp_eq_u64 vcc, 0\n"
    "s_cselect_b32 s46, 0, 0x20000\n"
    "s_cselect_b32 s94, s42, 0\n"
    "v_mfma_f32_16x16x32_bf16 v[12:15], a[88:91], v[92:95], v[12:15]\n"
    "buffer_load_dword v96, v20, s[44:47], 0 offen sc1\n"
    "v_mfma_f32_16x16x32_bf16 v[16:19], a[92:95], v[92:95], v[16:19]\n"
    "s_mov_b32 m0, s58\n"
    "s_waitcnt lgkmcnt(7)\n"
    "v_mfma_f32_16x16x32_bf16 v[8:11], a[96:99], v[32:35], v[8:11]\n"
    "buffer_load_dwordx4 v4, s[92:95], s49 offen sc1 lds\n"
    "v_mfma_f32_16x16x32_bf16 v[12:15], a[100:103], v[32:35], v[12:15]\n"
    "ds_read_b32 v97, v7 offset:32\n"
    "v_xor_b32 v0, 0x4000, v1\n"
    "v_mfma_f32_16x16x32_bf16 v[16:19], a[104:107], v[32:35], v[16:19]\n"
    "ds_read_b128 v[32:35], v0\n"
    "s_waitcnt lgkmcnt(8)\n"
    "v_mfma_f32_16x16x32_bf16 v[8:11], a[108:111], v[36:39], v[8:11]\n"
    "v_mfma_f32_16x16x32_bf16 v[12:15], a[112:115], v[36:39], v[12:15]\n"
    "buffer_load_dwordx4 v4, s[92:95], s49 offen offset:1024 sc1 lds\n"
    "v_mfma_f32_16x16x32_bf16 v[16:19], a[116:119], v[36:39], v[16:19]\n"
    "ds_read_b128 v[36:39], v0 offset:1024\n"
    "s_waitcnt lgkmcnt(8)\n"
    "v_mfma_f32_16x16x32_bf16 v[8:11], a[120:123], v[40:43], v[8:11]\n"
    "s_waitcnt lgkmcnt(2)\n"
    "v_add_u32 v97, v97, v6\n"
    "v_mfma_f32_16x16x32_bf16 v[12:15], a[124:127], v[40:43], v[12:15]\n"
    "v_mfma_f32_16x16x32_bf16 v[16:19], a[128:131], v[40:43], v[16:19]\n"
    "ds_read_b128 v[40:43], v0 offset:2048\n"
    "v_mfma_f32_16x16x32_bf16 v[8:11], a[132:135], v[44:47], v[8:11]\n"
    "buffer_load_dwordx4 v4, s[92:95], s49 offen offset:2048 sc1 lds\n"
    "v_mfma_f32_16x16x32_bf16 v[12:15], a[136:139], v[44:47], v[12:15]\n"
    "v_mfma_f32_16x16x32_bf16 v[16:19], a[140:143], v[44:47], v[16:19]\n"
    "ds_read_b128 v[44:47], v0 offset:3072\n"
    "v_mfma_f32_16x16x32_bf16 v[8:11], a[144:147], v[48:51], v[8:11]\n"
    "v_mfma_f32_16x16x32_bf16 v[12:15], a[148:151], v[48:51], v[12:15]\n"
    "buffer_load_dwordx4 v4, s[92:95], s49 offen offset:3072 sc1 lds\n"
    "v_mfma_f32_16x16x32_bf16 v[16:19], a[152:155], v[48:51], v[16:19]\n"
    "ds_read_b128 v[48:51], v0 offset:4096\n"
    "v_mfma_f32_16x16x32_bf16 v[8:11], a[156:159], v[52:55], v[8:11]\n"
    "v_mfma_f32_16x16x32_bf16 v[12:15], a[160:163], v[52:55], v[12:15]\n"
    "v_mov_b32 v22, 1\n"
    "v_mfma_f32_16x16x32_bf16 v[16:19], a[164:167], v[52:55], v[16:19]\n"
    "ds_read_b128 v[52:55], v0 offset:5120\n"
    "s_waitcnt vmcnt(5)\n"
    "v_mfma_f32_16x16x32_bf16 v[8:11], a[168:171], v[56:59], v[8:11]\n"
    "s_mov_b64 exec, s[56:57]\n"
    "ds_add_rtn_u32 v22, v7, v22 offset:16\n"
    "s_mov_b64 exec, -1\n"
    "v_mfma_f32_16x16x32_bf16 v[12:15], a[172:175], v[56:59], v[12:15]\n"
    "v_mfma_f32_16x16x32_bf16 v[16:19], a[176:179], v[56:59], v[16:19]\n"
    "ds_read_b128 v[56:59], v0 offset:6144\n"
    "s_bitset1_b32 m0, 12\n"
    "v_mfma_f32_16x16x32_bf16 v[8:11], a[180:183], v[60:63], v[8:11]\n"
    "buffer_load_dwordx4 v5, s[92:95], s49 offen sc1 lds\n"
    "v_mfma_f32_16x16x32_bf16 v[12:15], a[184:187], v[60:63], v[12:15]\n"
    "v_mfma_f32_16x16x32_bf16 v[16:19], a[188:191], v[60:63], v[16:19]\n"
    "ds_read_b128 v[60:63], v0 offset:7168\n"
    "s_waitcnt lgkmcnt(7)\n"
    "v_mfma_f32_16x16x32_bf16 v[8:11], a[192:195], v[32:35], v[8:11]\n"
    "s_waitcnt lgkmcnt(2)\n"
    "v_readfirstlane_b32 s50, v22\n"
    "v_mfma_f32_16x16x32_bf16 v[12:15], a[196:199], v[32:35], v[12:15]\n"
    "s_cmp_eq_u32 s50, 3\n"
    "s_cselect_b32 s46, 0x20000, 0\n"
    "v_xor_b32 v0, 0x6000, v1\n"
    "v_mfma_f32_16x16x32_bf16 v[16:19], a[200:203], v[32:35], v[16:19]\n"
    "ds_read_b128 v[32:35], v0\n"
    "v_mfma_f32_16x16x32_bf16 v[8:11], a[204:207], v[36:39], v[8:11]\n"
    "buffer_store_dword v22, v23, s[44:47], 0 offen sc1\n"
    "v_mfma_f32_16x16x32_bf16 v[12:15], a[208:211], v[36:39], v[12:15]\n"
    "buffer_load_dwordx4 v5, s[92:95], s49 offen offset:1024 sc1 lds\n"
    "v_mfma_f32_16x16x32_bf16 v[16:19], a[212:215], v[36:39], v[16:19]\n"
    "ds_read_b128 v[36:39], v0 offset:1024\n"
    "v_mfma_f32_16x16x32_bf16 v[8:11], a[216:219], v[40:43], v[8:11]\n"
    "v_mfma_f32_16x16x32_bf16 v[12:15], a[220:223], v[40:43], v[12:15]\n"
    "v_mfma_f32_16x16x32_bf16 v[16:19], a[224:227], v[40:43], v[16:19]\n"
    "ds_read_b128 v[40:43], v0 offset:2048\n"
    "v_mfma_f32_16x16x32_bf16 v[8:11], a[228:231], v[44:47], v[8:11]\n"
    "buffer_load_dwordx4 v5, s[92:95], s49 offen offset:2048 sc1 lds\n"
    "v_mfma_f32_16x16x32_bf16 v[12:15], a[232:235], v[44:47], v[12:15]\n"
    "v_mfma_f32_16x16x32_bf16 v[16:19], a[236:239], v[44:47], v[16:19]\n"
    "ds_read_b128 v[44:47], v0 offset:3072\n"
    "v_mfma_f32_16x16x32_bf16 v[8:11], a[240:243], v[48:51], v[8:11]\n"
    "v_mfma_f32_16x16x32_bf16 v[12:15], a[244:247], v[48:51], v[12:15]\n"
    "buffer_load_dwordx4 v5, s[92:95], s49 offen offset:3072 sc1 lds\n"
    "v_mfma_f32_16x16x32_bf16 v[16:19], a[248:251], v[48:51], v[16:19]\n"
    "ds_read_b128 v[48:51], v0 offset:4096\n"
    "v_mfma_f32_16x16x32_bf16 v[8:11], a[252:255], v[52:55], v[8:11]\n"
    "s_cmp_eq_u32 s94, 0\n"
    "s_cbranch_scc1 .Lwk1_%=\n"
    ".Lwk1_x%=:\n"
    "v_mfma_f32_16x16x32_bf16 v[12:15], v[128:131], v[52:55], v[12:15]\n"
    "v_mfma_f32_16x16x32_bf16 v[16:19], v[132:135], v[52:55], v[16:19]\n"
    "ds_read_b128 v[52:55], v0 offset:5120\n"
    "s_waitcnt lgkmcnt(7)\n"
    "v_mfma_f32_16x16x32_bf16 v[8:11], v[136:139], v[56:59], v[8:11]\n"
    "v_mfma_f32_16x16x32_bf16 v[12:15], v[140:143], v[56:59], v[12:15]\n"
    "v_mfma_f32_16x16x32_bf16 v[16:19], v[144:147], v[56:59], v[16:19]\n"
    "ds_read_b128 v[56:59], v0 offset:6144\n"
    "s_waitcnt lgkmcnt(7)\n"
    "v_mfma_f32_16x16x32_bf16 v[8:11], v[148:151], v[60:63], v[8:11]\n"
    "v_mfma_f32_16x16x32_bf16 v[12:15], v[152:155], v[60:63], v[12:15]\n"
    "v_mfma_f32_16x16x32_bf16 v[16:19], v[156:159], v[60:63], v[16:19]\n"
    "ds_read_b128 v[60:63], v0 offset:7168\n"
    "s_waitcnt lgkmcnt(7)\n"
    "v_mfma_f32_16x16x32_bf16 v[8:11], v[160:163], v[32:35], v[8:11]\n"
    "v_mfma_f32_16x16x32_bf16 v[12:15], v[164:167], v[32:35], v[12:15]\n"
    "v_mfma_f32_16x16x32_bf16 v[16:19], v[168:171], v[32:35], v[16:19]\n"
    "s_waitcnt lgkmcnt(6)\n"
    "v_mfma_f32_16x16x32_bf16 v[8:11], v[172:175], v[36:39], v[8:11]\n"
    "v_mfma_f32_16x16x32_bf16 v[12:15], v[176:179], v[36:39], v[12:15]\n"
    "v_mfma_f32_16x16x32_bf16 v[16:19], v[180:183], v[36:39], v[16:19]\n"
    "s_waitcnt lgkmcnt(5)\n"
    "v_mfma_f32_16x16x32_bf16 v[8:11], v[184:187], v[40:43], v[8:11]\n"
    "s_cmp_eq_u32 s94, 0\n"
    "s_cbranch_scc1 .Lwk2_%=\n"
    ".Lwk2_x%=:\n"
    "v_mfma_f32_16x16x32_bf16 v[12:15], v[188:191], v[40:43], v[12:15]\n"
    "v_mfma_f32_16x16x32_bf16 v[16:19], v[192:195], v[40:43], v[16:19]\n"
    "s_waitcnt lgkmcnt(4)\n"
    "v_mfma_f32_16x16x32_bf16 v[8:11], v[196:199], v[44:47], v[8:11]\n"
    "v_xor_b32 v1, 0x8000, v1\n"
    "v_mfma_f32_16x16x32_bf16 v[12:15], v[200:203], v[44:47], v[12:15]\n"
    "global_load_dword v97, v97, %1 sc1\n"
    "s_waitcnt lgkmcnt(3)\n"
    "v_mfma_f32_16x16x32_bf16 v[8:11], v[208:211], v[48:51], v[8:11]\n"
    "v_mfma_f32_16x16x32_bf16 v[12:15], v[212:215], v[48:51], v[12:15]\n"
    "s_waitcnt lgkmcnt(2)\n"
    "v_mfma_f32_16x16x32_bf16 v[8:11], v[220:223], v[52:55], v[8:11]\n"
    "v_mfma_f32_16x16x32_bf16 v[12:15], v[224:227], v[52:55], v[12:15]\n"
    "s_waitcnt lgkmcnt(1)\n"
    "v_mfma_f32_16x16x32_bf16 v[8:11], v[232:235], v[56:59], v[8:11]\n"
    "s_waitcnt vmcnt(5)\n"
    "ds_read_b128 v[64:67], v1\n"
    "v_mfma_f32_16x16x32_bf16 v[12:15], v[236:239], v[56:59], v[12:15]\n"
    "ds_read_b128 v[68:71], v1 offset:1024\n"
    "s_waitcnt lgkmcnt(2)\n"
    "v_mfma_f32_16x16x32_bf16 v[8:11], v[244:247], v[60:63], v[8:11]\n"
    "ds_read_b128 v[72:75], v1 offset:2048\n"
    "v_mfma_f32_16x16x32_bf16 v[12:15], v[248:251], v[60:63], v[12:15]\n"
    "ds_read_b128 v[76:79], v1 offset:3072\n"
    "v_mfma_f32_16x16x32_bf16 v[16:19], v[204:207], v[44:47], v[16:19]\n"
    "v_mfma_f32_16x16x32_bf16 v[16:19], v[216:219], v[48:51], v[16:19]\n"
    "s_mov_b32 s56, 1\n"
    "v_cvt_pk_bf16_f32 v24, v8, v9\n"
    "v_cvt_pk_bf16_f32 v25, v10, v11\n"
    "v_mfma_f32_16x16x32_bf16 v[16:19], v[228:231], v[52:55], v[16:19]\n"
    "s_cmp_lg_u32 s94, 0\n"
    "v_cvt_pk_bf16_f32 v26, v12, v13\n"
    "v_cvt_pk_bf16_f32 v27, v14, v15\n"
    "v_mfma_f32_16x16x32_bf16 v[16:19], v[240:243], v[56:59], v[16:19]\n"
    "buffer_store_dwordx4 v[24:27], v2, s[40:43], s62 offen sc1\n"
    "v_mfma_f32_16x16x32_bf16 v[16:19], v[252:255], v[60:63], v[16:19]\n"
    "s_cbranch_scc1 .Lwgo_%=\n"
    "ds_read_b32 v23, v7 offset:44\n"
    "s_nop 6\n"
    "v_cvt_pk_bf16_f32 v28, v16, v17\n"
    "v_cvt_pk_bf16_f32 v30, v16, v17\n"
    "v_cvt_pk_bf16_f32 v29, v18, v19\n"
    "v_cvt_pk_bf16_f32 v31, v18, v19\n"
    "v_permlane16_swap_b32 v28, v30\n"
    "s_nop 0\n"
    "v_permlane16_swap_b32 v29, v31\n"
    "buffer_store_dwordx4 v[28:31], v3, s[40:43], s62 offen sc1\n"
    "s_waitcnt vmcnt(10) lgkmcnt(0)\n"
    "v_cmp_eq_u32 vcc, 0, v96\n"
    "s_cmp_eq_u64 vcc, 0\n"
    "s_cbranch_scc1 .Lwld_%=\n"
    "global_load_dword v96, v20, %1 sc1\n"
    "s_waitcnt vmcnt(0)\n"
    "v_mov_b32 v22, 1\n"
    "s_mov_b64 exec, s[56:57]\n"
    "ds_add_rtn_u32 v22, v7, v22 offset:48\n"
    "s_mov_b64 exec, -1\n"
    "s_waitcnt lgkmcnt(0)\n"
    "v_readfirstlane_b32 s50, v22\n"
    "s_cmp_eq_u32 s50, 3\n"
    "s_cselect_b32 s46, 0x20000, 0\n"
    "buffer_store_dword v22, v23, s[44:47], 0 offen sc1\n"
    "s_mov_b32 s56, 0\n"
    "v_cmp_eq_u32 vcc, 0, v96\n"
    "s_cmp_eq_u64 vcc, 0\n"
    "s_cbranch_scc1 .Lwld_%=\n"
    ".Lwchk_%=:\n"
    "v_readfirstlane_b32 s50, v20\n"
    "s_cmp_lt_u32 s50, 200\n"
    "s_cbranch_scc1 .Lwend_%=\n"
    ".Lwwait_%=:\n"
    "global_load_dword v96, v20, %1 sc1\n"
    "s_mov_b32 s52, 0\n"
    ".Lwp3_%=:\n"
    "s_waitcnt vmcnt(0)\n"
    "v_cmp_eq_u32 vcc, 0, v96\n"
    "s_cmp_eq_u64 vcc, 0\n"
    "s_cbranch_scc1 .Lwp3_x%=\n"
    "s_add_u32 s52, s52, 1\n"
    "s_sleep 2\n"
    "global_load_dword v96, v20, %1 sc1\n"
    "s_cmp_lt_u32 s52, s53\n"
    "s_cbranch_scc1 .Lwp3_%=\n"
    "s_mov_b32 s53, 0\n"
    "v_mov_b32 v22, 0\n"
    "v_mov_b32 v23, 1\n"
    "global_store_dword v22, v23, %1 sc1\n"
    "s_waitcnt vmcnt(0)\n"
    ".Lwp3_x%=:\n"
    ".Lwld_%=:\n"
    "s_mov_b32 s94, s42\n"
    "s_mov_b32 m0, s58\n"
    "s_nop 0\n"
    "buffer_load_dwordx4 v4, s[92:95], s49 offen sc1 lds\n"
    "buffer_load_dwordx4 v4, s[92:95], s49 offen offset:1024 sc1 lds\n"
    "buffer_load_dwordx4 v4, s[92:95], s49 offen offset:2048 sc1 lds\n"
    "buffer_load_dwordx4 v4, s[92:95], s49 offen offset:3072 sc1 lds\n"
    "s_bitset1_b32 m0, 12\n"
    "s_nop 0\n"
    "buffer_load_dwordx4 v5, s[92:95], s49 offen sc1 lds\n"
    "buffer_load_dwordx4 v5, s[92:95], s49 offen offset:1024 sc1 lds\n"
    "buffer_load_dwordx4 v5, s[92:95], s49 offen offset:2048 sc1 lds\n"
    "buffer_load_dwordx4 v5, s[92:95], s49 offen offset:3072 sc1 lds\n"
    "s_waitcnt vmcnt(8)\n"
    "v_mov_b32 v22, 1\n"
    "s_mov_b64 exec, s[56:57]\n"
    "ds_add_rtn_u32 v22, v7, v22 offset:48\n"
    "s_mov_b64 exec, -1\n"
    "s_waitcnt lgkmcnt(0)\n"
    "v_readfirstlane_b32 s50, v22\n"
    "s_cmp_eq_u32 s50, 3\n"
    "s_cselect_b32 s46, 0x20000, 0\n"
    "buffer_store_dword v22, v23, s[44:47], 0 offen sc1\n"
    "s_mov_b32 s56, 0\n"
    "s_waitcnt vmcnt(5)\n"
    "ds_read_b128 v[64:67], v1\n"
    "ds_read_b128 v[68:71], v1 offset:1024\n"
    "ds_read_b128 v[72:75], v1 offset:2048\n"
    "ds_read_b128 v[76:79], v1 offset:3072\n"
    "s_waitcnt lgkmcnt(3)\n"
    "v_mfma_f32_16x16x32_bf16 v[8:11], a[0:3], v[64:67], 0\n"
    "v_add_u32 v7, 32, v7\n"
    "v_mfma_f32_16x16x32_bf16 v[12:15], a[4:7], v[64:67], 0\n"
    "ds_read_b128 v[20:23], v7\n"
    "s_waitcnt lgkmcnt(3)\n"
    "v_mfma_f32_16x16x32_bf16 v[8:11], a[12:15], v[68:71], v[8:11]\n"
    "v_mfma_f32_16x16x32_bf16 v[12:15], a[16:19], v[68:71], v[12:15]\n"
    "s_waitcnt lgkmcnt(2)\n"
    "v_mfma_f32_16x16x32_bf16 v[8:11], a[24:27], v[72:75], v[8:11]\n"
    "s_waitcnt vmcnt(1)\n"
    "ds_read_b128 v[80:83], v1 offset:4096\n"
    "v_mfma_f32_16x16x32_bf16 v[12:15], a[28:31], v[72:75], v[12:15]\n"
    "ds_read_b128 v[84:87], v1 offset:5120\n"
    "s_waitcnt lgkmcnt(3)\n"
    "v_mfma_f32_16x16x32_bf16 v[8:11], a[36:39], v[76:79], v[8:11]\n"
    "v_mfma_f32_16x16x32_bf16 v[12:15], a[40:43], v[76:79], v[12:15]\n"
    "ds_read_b128 v[88:91], v1 offset:6144\n"
    "s_waitcnt lgkmcnt(3)\n"
    "v_readfirstlane_b32 s49, v21\n"
    "v_mfma_f32_16x16x32_bf16 v[16:19], a[8:11], v[64:67], 0\n"
    "ds_read_b128 v[92:95], v1 offset:7168\n"
    "v_readfirstlane_b32 s62, v22\n"
    "v_mfma_f32_16x16x32_bf16 v[16:19], a[20:23], v[68:71], v[16:19]\n"
    "v_add_u32 v20, v20, v6\n"
    "s_branch .Lwtop_%=\n"
    ".Lwk1_%=:\n"
    "s_waitcnt vmcnt(8)\n"
    "v_cmp_eq_u32 vcc, 0, v96\n"
    "s_cmp_eq_u64 vcc, 0\n"
    "s_cselect_b32 s46, 0, 0x20000\n"
    "s_cselect_b32 s94, s42, 0\n"
    "buffer_load_dword v96, v20, s[44:47], 0 offen sc1\n"
    "s_mov_b32 m0, s58\n"
    "s_nop 0\n"
    "buffer_load_dwordx4 v4, s[92:95], s49 offen sc1 lds\n"
    "buffer_load_dwordx4 v4, s[92:95], s49 offen offset:1024 sc1 lds\n"
    "buffer_load_dwordx4 v4, s[92:95], s49 offen offset:2048 sc1 lds\n"
    "buffer_load_dwordx4 v4, s[92:95], s49 offen offset:3072 sc1 lds\n"
    "s_bitset1_b32 m0, 12\n"
    "s_nop 0\n"
    "buffer_load_dwordx4 v5, s[92:95], s49 offen sc1 lds\n"
    "buffer_load_dwordx4 v5, s[92:95], s49 offen offset:1024 sc1 lds\n"
    "buffer_load_dwordx4 v5, s[92:95], s49 offen offset:2048 sc1 lds\n"
    "buffer_load_dwordx4 v5, s[92:95], s49 offen offset:3072 sc1 lds\n"
    "s_branch .Lwk1_x%=\n"
    ".Lwk2_%=:\n"
    "s_waitcnt vmcnt(8)\n"
    "v_cmp_eq_u32 vcc, 0, v96\n"
    "s_cmp_eq_u64 vcc, 0\n"
    "s_cselect_b32 s46, 0, 0x20000\n"
    "s_cselect_b32 s94, s42, 0\n"
    "buffer_load_dword v96, v20, s[44:47], 0 offen sc1\n"
    "s_mov_b32 m0, s58\n"
    "s_nop 0\n"
    "buffer_load_dwordx4 v4, s[92:95], s49 offen sc1 lds\n"
    "buffer_load_dwordx4 v4, s[92:95], s49 offen offset:1024 sc1 lds\n"
    "buffer_load_dwordx4 v4, s[92:95], s49 offen offset:2048 sc1 lds\n"
    "buffer_load_dwordx4 v4, s[92:95], s49 offen offset:3072 sc1 lds\n"
    "s_bitset1_b32 m0, 12\n"
    "s_nop 0\n"
    "buffer_load_dwordx4 v5, s[92:95], s49 offen sc1 lds\n"
    "buffer_load_dwordx4 v5, s[92:95], s49 offen offset:1024 sc1 lds\n"
    "buffer_load_dwordx4 v5, s[92:95], s49 offen offset:2048 sc1 lds\n"
    "buffer_load_dwordx4 v5, s[92:95], s49 offen offset:3072 sc1 lds\n"
    "s_branch .Lwk2_x%=\n"
    ".Lwpol_%=:\n"
    "v_mov_b32 v20, 64\n"
    "global_load_dword v96, v20, %1 sc1\n"
    "s_mov_b32 s52, 0\n"
    ".Lwp4_%=:\n"
    "s_waitcnt vmcnt(0)\n"
    "v_cmp_eq_u32 vcc, 0, v96\n"
    "s_cmp_eq_u64 vcc, 0\n"
    "s_cbranch_scc1 .Lwp4_x%=\n"
    "s_add_u32 s52, s52, 1\n"
    "s_sleep 2\n"
    "global_load_dword v96, v20, %1 sc1\n"
    "s_cmp_lt_u32 s52, s53\n"
    "s_cbranch_scc1 .Lwp4_%=\n"
    "s_mov_b32 s53, 0\n"
    "v_mov_b32 v22, 0\n"
    "v_mov_b32 v23, 1\n"
    "global_store_dword v22, v23, %1 sc1\n"
    "s_waitcnt vmcnt(0)\n"
    ".Lwp4_x%=:\n"
    "v_readfirstlane_b32 s50, v96\n"
    "v_mbcnt_lo_u32_b32 v20, -1, 0\n"
    "v_mbcnt_hi_u32_b32 v20, -1, v20\n"
    "s_lshl_b32 s51, %2, 6\n"
    "v_add_u32 v20, s51, v20\n"
    "v_min_u32 v20, 129, v20\n"
    "v_lshlrev_b32 v20, 5, v20\n"
    "v_add_u32 v20, 0x26c00, v20\n"
    "ds_read2_b32 v[22:23], v20 offset0:1 offset1:2\n"
    "s_and_b32 s50, s50, 1\n"
    "s_waitcnt lgkmcnt(0)\n"
    "v_and_b32 v21, s50, v22\n"
    "v_lshlrev_b32 v21, 13, v21\n"
    "v_and_b32 v22, -2, v22\n"
    "v_xor_b32 v22, v21, v22\n"
    "v_and_b32 v21, s50, v23\n"
    "v_lshlrev_b32 v21, 13, v21\n"
    "v_and_b32 v23, -2, v23\n"
    "v_xor_b32 v23, v21, v23\n"
    "ds_write2_b32 v20, v22, v23 offset0:1 offset1:2\n"
    "s_waitcnt lgkmcnt(0)\n"
    "s_barrier\n"
    "ds_read_b32 v21, v7 offset:4\n"
    "v_add_u32 v20, %4, v6\n"
    "s_waitcnt lgkmcnt(0)\n"
    "v_readfirstlane_b32 s49, v21\n"
    "s_branch .Lwwait_%=\n"
    ".Lwend_%=:\n"
    "s_cmp_eq_u32 s50, 192\n"
    "s_cbranch_scc0 .Lwpol_%=\n"
    "s_waitcnt vmcnt(0) lgkmcnt(0)\n"
    "s_endpgm\n"
    ".p2align 6\n"
    ".Lrole3_%=:\n"
    "s_mov_b64 s[40:41], %0\n"
    "s_and_b32 s41, s41, 0xffff\n"
    "s_mov_b32 s42, 0xc000000\n"
    "s_mov_b32 s43, 0x20000\n"
    "s_mov_b64 s[92:93], %0\n"
    "s_and_b32 s93, s93, 0xffff\n"
    "s_mov_b32 s94, 0xc000000\n"
    "s_mov_b32 s95, 0x20000\n"
    "s_mov_b32 s53, 131072\n"
    "s_mov_b64 s[56:57], 0\n"
    "v_mbcnt_lo_u32_b32 v100, -1, 0\n"
    "v_mbcnt_hi_u32_b32 v100, -1, v100\n"
    "s_lshl_b32 s50, %2, 8\n"
    "s_add_u32 s50, s50, 144384\n"
    "v_lshl_add_u32 v100, v100, 2, s50\n"
    "ds_read_b32 v0, v100\n"
    "ds_read_b32 v1, v100 offset:1024\n"
    "ds_read_b32 v2, v100 offset:2048\n"
    "ds_read_b32 v3, v100 offset:3072\n"
    "ds_read_b32 v4, v100 offset:4096\n"
    "ds_read_b32 v5, v100 offset:5120\n"
    "ds_read_b32 v6, v100 offset:6144\n"
    "ds_read_b32 v7, v100 offset:7168\n"
    "ds_read_b32 v8, v100 offset:8192\n"
    "ds_read_b32 v152, v100 offset:9216\n"
    "ds_read_b32 v98, v100 offset:10240\n"
    "ds_read_b32 v99, v100 offset:11264\n"
    "s_waitcnt lgkmcnt(0)\n"
    "s_barrier\n"
    "v_mbcnt_lo_u32_b32 v100, -1, 0\n"
    "v_mbcnt_hi_u32_b32 v100, -1, v100\n"
    "v_add_u32 v101, 0, v100\n"
    "v_mul_u32_u24 v102, 7282, v101\n"
    "v_lshrrev_b32 v102, 16, v102\n"
    "v_and_b32 v103, 12, v102\n"
    "v_and_b32 v104, 48, v102\n"
    "v_mul_i32_i24 v104, 24192, v104\n"
    "v_mul_u32_u24 v103, 3456, v103\n"
    "v_mul_u32_u24 v102, 3312, v102\n"
    "v_add3_u32 v102, v102, v103, v104\n"
    "v_lshl_add_u32 v80, v101, 4, v102\n"
    "v_add_u32 v101, 64, v100\n"
    "v_mul_u32_u24 v102, 7282, v101\n"
    "v_lshrrev_b32 v102, 16, v102\n"
    "v_and_b32 v103, 12, v102\n"
    "v_and_b32 v104, 48, v102\n"
    "v_mul_i32_i24 v104, 24192, v104\n"
    "v_mul_u32_u24 v103, 3456, v103\n"
    "v_mul_u32_u24 v102, 3312, v102\n"
    "v_add3_u32 v102, v102, v103, v104\n"
    "v_lshl_add_u32 v81, v101, 4, v102\n"
    "v_add_u32 v101, 128, v100\n"
    "v_mul_u32_u24 v102, 7282, v101\n"
    "v_lshrrev_b32 v102, 16, v102\n"
    "v_and_b32 v103, 12, v102\n"
    "v_and_b32 v104, 48, v102\n"
    "v_mul_i32_i24 v104, 24192, v104\n"
    "v_mul_u32_u24 v103, 3456, v103\n"
    "v_mul_u32_u24 v102, 3312, v102\n"
    "v_add3_u32 v102, v102, v103, v104\n"
    "v_lshl_add_u32 v82, v101, 4, v102\n"
    "v_add_u32 v101, 192, v100\n"
    "v_mul_u32_u24 v102, 7282, v101\n"
    "v_lshrrev_b32 v102, 16, v102\n"
    "v_and_b32 v103, 12, v102\n"
    "v_and_b32 v104, 48, v102\n"
    "v_mul_i32_i24 v104, 24192, v104\n"
    "v_mul_u32_u24 v103, 3456, v103\n"
    "v_mul_u32_u24 v102, 3312, v102\n"
    "v_add3_u32 v102, v102, v103, v104\n"
    "v_lshl_add_u32 v83, v101, 4, v102\n"
    "v_add_u32 v101, 256, v100\n"
    "v_mul_u32_u24 v102, 7282, v101\n"
    "v_lshrrev_b32 v102, 16, v102\n"
    "v_and_b32 v103, 12, v102\n"
    "v_and_b32 v104, 48, v102\n"
    "v_mul_i32_i24 v104, 24192, v104\n"
    "v_mul_u32_u24 v103, 3456, v103\n"
    "v_mul_u32_u24 v102, 3312, v102\n"
    "v_add3_u32 v102, v102, v103, v104\n"
    "v_lshl_add_u32 v84, v101, 4, v102\n"
    "v_add_u32 v101, 320, v100\n"
    "v_mul_u32_u24 v102, 7282, v101\n"
    "v_lshrrev_b32 v102, 16, v102\n"
    "v_and_b32 v103, 12, v102\n"
    "v_and_b32 v104, 48, v102\n"
    "v_mul_i32_i24 v104, 24192, v104\n"
    "v_mul_u32_u24 v103, 3456, v103\n"
    "v_mul_u32_u24 v102, 3312, v102\n"
    "v_add3_u32 v102, v102, v103, v104\n"
    "v_lshl_add_u32 v85, v101, 4, v102\n"
    "v_add_u32 v101, 384, v100\n"
    "v_mul_u32_u24 v102, 7282, v101\n"
    "v_lshrrev_b32 v102, 16, v102\n"
    "v_and_b32 v103, 12, v102\n"
    "v_and_b32 v104, 48, v102\n"
    "v_mul_i32_i24 v104, 24192, v104\n"
    "v_mul_u32_u24 v103, 3456, v103\n"
    "v_mul_u32_u24 v102, 3312, v102\n"
    "v_add3_u32 v102, v102, v103, v104\n"
    "v_lshl_add_u32 v86, v101, 4, v102\n"
    "v_add_u32 v101, 448, v100\n"
    "v_mul_u32_u24 v102, 7282, v101\n"
    "v_lshrrev_b32 v102, 16, v102\n"
    "v_and_b32 v103, 12, v102\n"
    "v_and_b32 v104, 48, v102\n"
    "v_mul_i32_i24 v104, 24192, v104\n"
    "v_mul_u32_u24 v103, 3456, v103\n"
    "v_mul_u32_u24 v102, 3312, v102\n"
    "v_add3_u32 v102, v102, v103, v104\n"
    "v_lshl_add_u32 v87, v101, 4, v102\n"
    "v_add_u32 v101, 512, v100\n"
    "v_mul_u32_u24 v102, 7282, v101\n"
    "v_lshrrev_b32 v102, 16, v102\n"
    "v_and_b32 v103, 12, v102\n"
    "v_and_b32 v104, 48, v102\n"
    "v_mul_i32_i24 v104, 24192, v104\n"
    "v_mul_u32_u24 v103, 3456, v103\n"
    "v_mul_u32_u24 v102, 3312, v102\n"
    "v_add3_u32 v102, v102, v103, v104\n"
    "v_lshl_add_u32 v88, v101, 4, v102\n"
    "s_min_u32 s50, %2, 2\n"
    "s_mul_i32 s54, s50, 9216\n"
    "s_mul_i32 s50, s50, 1152\n"
    "s_add_u32 s51, s50, %6\n"
    "s_mov_b64 s[44:45], %8\n"
    "s_and_b32 s45, s45, 0xffff\n"
    "s_mov_b32 s46, 0x1e6000\n"
    "s_mov_b32 s47, 0x20000\n"
    "v_add_u32 v101, 0, v100\n"
    "v_mul_u32_u24 v102, 7282, v101\n"
    "v_lshrrev_b32 v102, 16, v102\n"
    "v_and_b32 v103, 12, v102\n"
    "v_and_b32 v104, 16, v102\n"
    "v_mul_i32_i24 v104, -2592, v104\n"
    "v_mul_u32_u24 v103, 3456, v103\n"
    "v_mul_u32_u24 v102, 3312, v102\n"
    "v_add3_u32 v102, v102, v103, v104\n"
    "v_lshl_add_u32 v89, v101, 4, v102\n"
    "v_add_u32 v101, 64, v100\n"
    "v_mul_u32_u24 v102, 7282, v101\n"
    "v_lshrrev_b32 v102, 16, v102\n"
    "v_and_b32 v103, 12, v102\n"
    "v_and_b32 v104, 16, v102\n"
    "v_mul_i32_i24 v104, -2592, v104\n"
    "v_mul_u32_u24 v103, 3456, v103\n"
    "v_mul_u32_u24 v102, 3312, v102\n"
    "v_add3_u32 v102, v102, v103, v104\n"
    "v_lshl_add_u32 v90, v101, 4, v102\n"
    "v_add_u32 v101, 128, v100\n"
    "v_mul_u32_u24 v102, 7282, v101\n"
    "v_lshrrev_b32 v102, 16, v102\n"
    "v_and_b32 v103, 12, v102\n"
    "v_and_b32 v104, 16, v102\n"
    "v_mul_i32_i24 v104, -2592, v104\n"
    "v_mul_u32_u24 v103, 3456, v103\n"
    "v_mul_u32_u24 v102, 3312, v102\n"
    "v_add3_u32 v102, v102, v103, v104\n"
    "v_lshl_add_u32 v91, v101, 4, v102\n"
    "v_add_u32 v101, 192, v100\n"
    "v_mul_u32_u24 v102, 7282, v101\n"
    "v_lshrrev_b32 v102, 16, v102\n"
    "v_and_b32 v103, 12, v102\n"
    "v_and_b32 v104, 16, v102\n"
    "v_mul_i32_i24 v104, -2592, v104\n"
    "v_mul_u32_u24 v103, 3456, v103\n"
    "v_mul_u32_u24 v102, 3312, v102\n"
    "v_add3_u32 v102, v102, v103, v104\n"
    "v_lshl_add_u32 v92, v101, 4, v102\n"
    "v_add_u32 v101, 256, v100\n"
    "v_mul_u32_u24 v102, 7282, v101\n"
    "v_lshrrev_b32 v102, 16, v102\n"
    "v_and_b32 v103, 12, v102\n"
    "v_and_b32 v104, 16, v102\n"
    "v_mul_i32_i24 v104, -2592, v104\n"
    "v_mul_u32_u24 v103, 3456, v103\n"
    "v_mul_u32_u24 v102, 3312, v102\n"
    "v_add3_u32 v102, v102, v103, v104\n"
    "v_lshl_add_u32 v93, v101, 4, v102\n"
    "v_add_u32 v101, 320, v100\n"
    "v_mul_u32_u24 v102, 7282, v101\n"
    "v_lshrrev_b32 v102, 16, v102\n"
    "v_and_b32 v103, 12, v102\n"
    "v_and_b32 v104, 16, v102\n"
    "v_mul_i32_i24 v104, -2592, v104\n"
    "v_mul_u32_u24 v103, 3456, v103\n"
    "v_mul_u32_u24 v102, 3312, v102\n"
    "v_add3_u32 v102, v102, v103, v104\n"
    "v_lshl_add_u32 v94, v101, 4, v102\n"
    "v_add_u32 v101, 384, v100\n"
    "v_mul_u32_u24 v102, 7282, v101\n"
    "v_lshrrev_b32 v102, 16, v102\n"
    "v_and_b32 v103, 12, v102\n"
    "v_and_b32 v104, 16, v102\n"
    "v_mul_i32_i24 v104, -2592, v104\n"
    "v_mul_u32_u24 v103, 3456, v103\n"
    "v_mul_u32_u24 v102, 3312, v102\n"
    "v_add3_u32 v102, v102, v103, v104\n"
    "v_lshl_add_u32 v95, v101, 4, v102\n"
    "v_add_u32 v101, 448, v100\n"
    "v_mul_u32_u24 v102, 7282, v101\n"
    "v_lshrrev_b32 v102, 16, v102\n"
    "v_and_b32 v103, 12, v102\n"
    "v_and_b32 v104, 16, v102\n"
    "v_mul_i32_i24 v104, -2592, v104\n"
    "v_mul_u32_u24 v103, 3456, v103\n"
    "v_mul_u32_u24 v102, 3312, v102\n"
    "v_add3_u32 v102, v102, v103, v104\n"
    "v_lshl_add_u32 v96, v101, 4, v102\n"
    "v_add_u32 v101, 512, v100\n"
    "v_mul_u32_u24 v102, 7282, v101\n"
    "v_lshrrev_b32 v102, 16, v102\n"
    "v_and_b32 v103, 12, v102\n"
    "v_and_b32 v104, 16, v102\n"
    "v_mul_i32_i24 v104, -2592, v104\n"
    "v_mul_u32_u24 v103, 3456, v103\n"
    "v_mul_u32_u24 v102, 3312, v102\n"
    "v_add3_u32 v102, v102, v103, v104\n"
    "v_lshl_add_u32 v97, v101, 4, v102\n"
    "v_add_u32 v9, 0x21c00, v3\n"
    "v_add_u32 v10, 1024, v6\n"
    "s_lshl_b32 s58, %2, 13\n"
    "s_add_u32 s58, s58, 0\n"
    "s_add_u32 s88, s40, 32768\n"
    "s_mov_b32 s89, s41\n"
    "s_cmp_lt_u32 %2, 2\n"
    "s_cselect_b32 s96, s42, 0\n"
    "s_mov_b32 s91, s43\n"
    "s_mul_i32 s59, %2, 576\n"
    "s_add_u32 s59, s59, 3072\n"
    "s_bfm_b64 s[60:61], 36, 0\n"
    "s_xor_b32 s50, %2, 0\n"
    "s_mul_i32 s50, s50, 2304\n"
    "v_add_u32 v106, s50, v98\n"
    "v_add_u32 v107, s50, v99\n"
    "s_xor_b32 s50, %2, 1\n"
    "s_mul_i32 s50, s50, 2304\n"
    "v_add_u32 v108, s50, v98\n"
    "v_add_u32 v109, s50, v99\n"
    "s_xor_b32 s50, %2, 2\n"
    "s_mul_i32 s50, s50, 2304\n"
    "v_add_u32 v110, s50, v98\n"
    "v_add_u32 v111, s50, v99\n"
    "s_xor_b32 s50, %2, 3\n"
    "s_mul_i32 s50, s50, 2304\n"
    "v_add_u32 v112, s50, v98\n"
    "v_add_u32 v113, s50, v99\n"
    "s_xor_b32 s50, %2, 4\n"
    "s_and_b32 s50, s50, 1\n"
    "s_lshl_b32 s50, s50, 1\n"
    "s_mul_i32 s50, s50, 2304\n"
    "v_add_u32 v114, s50, v98\n"
    "v_add_u32 v115, s50, v99\n"
    "s_xor_b32 s50, %2, 5\n"
    "s_and_b32 s50, s50, 1\n"
    "s_lshl_b32 s50, s50, 1\n"
    "s_mul_i32 s50, s50, 2304\n"
    "v_add_u32 v116, s50, v98\n"
    "v_add_u32 v117, s50, v99\n"
    "v_mov_b32 v56, 0x3f803f80\n"
    "v_mov_b32 v57, 0x3f803f80\n"
    "v_mov_b32 v58, 0x3f803f80\n"
    "v_mov_b32 v59, 0x3f803f80\n"
    "s_cmp_eq_u32 %2, 3\n"
    "s_cbranch_scc1 .Lac1_%=\n"
    "s_add_u32 s55, s51, 0\n"
    "s_add_u32 m0, s54, 0\n"
    "s_nop 0\n"
    "buffer_load_dwordx4 v80, s[44:47], s55 offen lds\n"
    "s_add_u32 m0, s54, 1024\n"
    "s_nop 0\n"
    "buffer_load_dwordx4 v81, s[44:47], s55 offen lds\n"
    "s_add_u32 m0, s54, 2048\n"
    "s_nop 0\n"
    "buffer_load_dwordx4 v82, s[44:47], s55 offen lds\n"
    "s_add_u32 m0, s54, 3072\n"
    "s_nop 0\n"
    "buffer_load_dwordx4 v83, s[44:47], s55 offen lds\n"
    "s_add_u32 m0, s54, 4096\n"
    "s_nop 0\n"
    "buffer_load_dwordx4 v84, s[44:47], s55 offen lds\n"
    "s_add_u32 m0, s54, 5120\n"
    "s_nop 0\n"
    "buffer_load_dwordx4 v85, s[44:47], s55 offen lds\n"
    "s_add_u32 m0, s54, 6144\n"
    "s_nop 0\n"
    "buffer_load_dwordx4 v86, s[44:47], s55 offen lds\n"
    "s_add_u32 m0, s54, 7168\n"
    "s_nop 0\n"
    "buffer_load_dwordx4 v87, s[44:47], s55 offen lds\n"
    "s_add_u32 m0, s54, 8192\n"
    "s_nop 0\n"
    "buffer_load_dwordx4 v88, s[44:47], s55 offen lds\n"
    ".Lac1_%=:\n"
    "s_cmp_eq_u32 %2, 3\n"
    "s_cbranch_scc1 .Lac2_%=\n"
    "s_add_u32 s55, s51, 13824\n"
    "s_add_u32 m0, s54, 27648\n"
    "s_nop 0\n"
    "buffer_load_dwordx4 v80, s[44:47], s55 offen lds\n"
    "s_add_u32 m0, s54, 28672\n"
    "s_nop 0\n"
    "buffer_load_dwordx4 v81, s[44:47], s55 offen lds\n"
    "s_add_u32 m0, s54, 29696\n"
    "s_nop 0\n"
    "buffer_load_dwordx4 v82, s[44:47], s55 offen lds\n"
    "s_add_u32 m0, s54, 30720\n"
    "s_nop 0\n"
    "buffer_load_dwordx4 v83, s[44:47], s55 offen lds\n"
    "s_add_u32 m0, s54, 31744\n"
    "s_nop 0\n"
    "buffer_load_dwordx4 v84, s[44:47], s55 offen lds\n"
    "s_add_u32 m0, s54, 32768\n"
    "s_nop 0\n"
    "buffer_load_dwordx4 v85, s[44:47], s55 offen lds\n"
    "s_add_u32 m0, s54, 33792\n"
    "s_nop 0\n"
    "buffer_load_dwordx4 v86, s[44:47], s55 offen lds\n"
    "s_add_u32 m0, s54, 34816\n"
    "s_nop 0\n"
    "buffer_load_dwordx4 v87, s[44:47], s55 offen lds\n"
    "s_add_u32 m0, s54, 35840\n"
    "s_nop 0\n"
    "buffer_load_dwordx4 v88, s[44:47], s55 offen lds\n"
    ".Lac2_%=:\n"
    "s_cmp_eq_u32 %2, 3\n"
    "s_cbranch_scc1 .Lac3_%=\n"
    "s_add_u32 s55, s51, 110592\n"
    "s_add_u32 m0, s54, 55296\n"
    "s_nop 0\n"
    "buffer_load_dwordx4 v80, s[44:47], s55 offen lds\n"
    "s_add_u32 m0, s54, 56320\n"
    "s_nop 0\n"
    "buffer_load_dwordx4 v81, s[44:47], s55 offen lds\n"
    "s_add_u32 m0, s54, 57344\n"
    "s_nop 0\n"
    "buffer_load_dwordx4 v82, s[44:47], s55 offen lds\n"
    "s_add_u32 m0, s54, 58368\n"
    "s_nop 0\n"
    "buffer_load_dwordx4 v83, s[44:47], s55 offen lds\n"
    "s_add_u32 m0, s54, 59392\n"
    "s_nop 0\n"
    "buffer_load_dwordx4 v84, s[44:47], s55 offen lds\n"
    "s_add_u32 m0, s54, 60416\n"
    "s_nop 0\n"
    "buffer_load_dwordx4 v85, s[44:47], s55 offen lds\n"
    "s_add_u32 m0, s54, 61440\n"
    "s_nop 0\n"
    "buffer_load_dwordx4 v86, s[44:47], s55 offen lds\n"
    "s_add_u32 m0, s54, 62464\n"
    "s_nop 0\n"
    "buffer_load_dwordx4 v87, s[44:47], s55 offen lds\n"
    "s_add_u32 m0, s54, 63488\n"
    "s_nop 0\n"
    "buffer_load_dwordx4 v88, s[44:47], s55 offen lds\n"
    ".Lac3_%=:\n"
    "s_cmp_eq_u32 %2, 3\n"
    "s_cbranch_scc1 .Lac4_%=\n"
    "s_add_u32 s55, s51, 124416\n"
    "s_add_u32 m0, s54, 82944\n"
    "s_nop 0\n"
    "buffer_load_dwordx4 v80, s[44:47], s55 offen lds\n"
    "s_add_u32 m0, s54, 83968\n"
    "s_nop 0\n"
    "buffer_load_dwordx4 v81, s[44:47], s55 offen lds\n"
    "s_add_u32 m0, s54, 84992\n"
    "s_nop 0\n"
    "buffer_load_dwordx4 v82, s[44:47], s55 offen lds\n"
    "s_add_u32 m0, s54, 86016\n"
    "s_nop 0\n"
    "buffer_load_dwordx4 v83, s[44:47], s55 offen lds\n"
    "s_add_u32 m0, s54, 87040\n"
    "s_nop 0\n"
    "buffer_load_dwordx4 v84, s[44:47], s55 offen lds\n"
    "s_add_u32 m0, s54, 88064\n"
    "s_nop 0\n"
    "buffer_load_dwordx4 v85, s[44:47], s55 offen lds\n"
    "s_add_u32 m0, s54, 89088\n"
    "s_nop 0\n"
    "buffer_load_dwordx4 v86, s[44:47], s55 offen lds\n"
    "s_add_u32 m0, s54, 90112\n"
    "s_nop 0\n"
    "buffer_load_dwordx4 v87, s[44:47], s55 offen lds\n"
    "s_add_u32 m0, s54, 91136\n"
    "s_nop 0\n"
    "buffer_load_dwordx4 v88, s[44:47], s55 offen lds\n"
    ".Lac4_%=:\n"
    "s_cmp_eq_u32 %2, 3\n"
    "s_cbranch_scc1 .Lac5_%=\n"
    "s_add_u32 s55, s51, 221184\n"
    "s_add_u32 m0, s54, 110592\n"
    "s_nop 0\n"
    "buffer_load_dwordx4 v80, s[44:47], s55 offen lds\n"
    "s_add_u32 m0, s54, 111616\n"
    "s_nop 0\n"
    "buffer_load_dwordx4 v81, s[44:47], s55 offen lds\n"
    "s_add_u32 m0, s54, 112640\n"
    "s_nop 0\n"
    "buffer_load_dwordx4 v82, s[44:47], s55 offen lds\n"
    "s_add_u32 m0, s54, 113664\n"
    "s_nop 0\n"
    "buffer_load_dwordx4 v83, s[44:47], s55 offen lds\n"
    "s_add_u32 m0, s54, 114688\n"
    "s_nop 0\n"
    "buffer_load_dwordx4 v84, s[44:47], s55 offen lds\n"
    "s_add_u32 m0, s54, 115712\n"
    "s_nop 0\n"
    "buffer_load_dwordx4 v85, s[44:47], s55 offen lds\n"
    "s_add_u32 m0, s54, 116736\n"
    "s_nop 0\n"
    "buffer_load_dwordx4 v86, s[44:47], s55 offen lds\n"
    "s_add_u32 m0, s54, 117760\n"
    "s_nop 0\n"
    "buffer_load_dwordx4 v87, s[44:47], s55 offen lds\n"
    "s_add_u32 m0, s54, 118784\n"
    "s_nop 0\n"
    "buffer_load_dwordx4 v88, s[44:47], s55 offen lds\n"
    ".Lac5_%=:\n"
    "s_waitcnt vmcnt(36) lgkmcnt(0)\n"
    "s_barrier\n"
    "ds_read_b64_tr_b16 a[0:1], v106\n"
    "ds_read_b64_tr_b16 a[72:73], v106 offset:9216\n"
    "ds_read_b64_tr_b16 a[144:145], v106 offset:18432\n"
    "ds_read_b64_tr_b16 a[216:217], v107\n"
    "ds_read_b64_tr_b16 a[16:17], v108\n"
    "ds_read_b64_tr_b16 a[88:89], v108 offset:9216\n"
    "ds_read_b64_tr_b16 a[160:161], v108 offset:18432\n"
    "ds_read_b64_tr_b16 a[32:33], v110\n"
    "ds_read_b64_tr_b16 a[104:105], v110 offset:9216\n"
    "ds_read_b64_tr_b16 a[176:177], v110 offset:18432\n"
    "ds_read_b64_tr_b16 a[232:233], v111\n"
    "ds_read_b64_tr_b16 a[48:49], v112\n"
    "ds_read_b64_tr_b16 a[120:121], v112 offset:9216\n"
    "ds_read_b64_tr_b16 a[192:193], v112 offset:18432\n"
    "s_waitcnt vmcnt(27) lgkmcnt(0)\n"
    "s_barrier\n"
    "s_cmp_eq_u32 %2, 3\n"
    "s_cbranch_scc1 .Lac6_%=\n"
    "s_add_u32 s55, s51, 235008\n"
    "s_add_u32 m0, s54, 0\n"
    "s_nop 0\n"
    "buffer_load_dwordx4 v80, s[44:47], s55 offen lds\n"
    "s_add_u32 m0, s54, 1024\n"
    "s_nop 0\n"
    "buffer_load_dwordx4 v81, s[44:47], s55 offen lds\n"
    "s_add_u32 m0, s54, 2048\n"
    "s_nop 0\n"
    "buffer_load_dwordx4 v82, s[44:47], s55 offen lds\n"
    "s_add_u32 m0, s54, 3072\n"
    "s_nop 0\n"
    "buffer_load_dwordx4 v83, s[44:47], s55 offen lds\n"
    "s_add_u32 m0, s54, 4096\n"
    "s_nop 0\n"
    "buffer_load_dwordx4 v84, s[44:47], s55 offen lds\n"
    "s_add_u32 m0, s54, 5120\n"
    "s_nop 0\n"
    "buffer_load_dwordx4 v85, s[44:47], s55 offen lds\n"
    "s_add_u32 m0, s54, 6144\n"
    "s_nop 0\n"
    "buffer_load_dwordx4 v86, s[44:47], s55 offen lds\n"
    "s_add_u32 m0, s54, 7168\n"
    "s_nop 0\n"
    "buffer_load_dwordx4 v87, s[44:47], s55 offen lds\n"
    "s_add_u32 m0, s54, 8192\n"
    "s_nop 0\n"
    "buffer_load_dwordx4 v88, s[44:47], s55 offen lds\n"
    ".Lac6_%=:\n"
    "ds_read_b64_tr_b16 a[2:3], v106 offset:27648\n"
    "ds_read_b64_tr_b16 a[74:75], v106 offset:36864\n"
    "ds_read_b64_tr_b16 a[146:147], v106 offset:46080\n"
    "ds_read_b64_tr_b16 a[218:219], v107 offset:27648\n"
    "ds_read_b64_tr_b16 a[18:19], v108 offset:27648\n"
    "ds_read_b64_tr_b16 a[90:91], v108 offset:36864\n"
    "ds_read_b64_tr_b16 a[162:163], v108 offset:46080\n"
    "ds_read_b64_tr_b16 a[34:35], v110 offset:27648\n"
    "ds_read_b64_tr_b16 a[106:107], v110 offset:36864\n"
    "ds_read_b64_tr_b16 a[178:179], v110 offset:46080\n"
    "ds_read_b64_tr_b16 a[234:235], v111 offset:27648\n"
    "ds_read_b64_tr_b16 a[50:51], v112 offset:27648\n"
    "ds_read_b64_tr_b16 a[122:123], v112 offset:36864\n"
    "ds_read_b64_tr_b16 a[194:195], v112 offset:46080\n"
    "s_waitcnt vmcnt(27) lgkmcnt(0)\n"
    "s_barrier\n"
    "s_cmp_eq_u32 %2, 3\n"
    "s_cbranch_scc1 .Lac7_%=\n"
    "s_add_u32 s55, s51, 331776\n"
    "s_add_u32 m0, s54, 27648\n"
    "s_nop 0\n"
    "buffer_load_dwordx4 v80, s[44:47], s55 offen lds\n"
    "s_add_u32 m0, s54, 28672\n"
    "s_nop 0\n"
    "buffer_load_dwordx4 v81, s[44:47], s55 offen lds\n"
    "s_add_u32 m0, s54, 29696\n"
    "s_nop 0\n"
    "buffer_load_dwordx4 v82, s[44:47], s55 offen lds\n"
    "s_add_u32 m0, s54, 30720\n"
    "s_nop 0\n"
    "buffer_load_dwordx4 v83, s[44:47], s55 offen lds\n"
    "s_add_u32 m0, s54, 31744\n"
    "s_nop 0\n"
    "buffer_load_dwordx4 v84, s[44:47], s55 offen lds\n"
    "s_add_u32 m0, s54, 32768\n"
    "s_nop 0\n"
    "buffer_load_dwordx4 v85, s[44:47], s55 offen lds\n"
    "s_add_u32 m0, s54, 33792\n"
    "s_nop 0\n"
    "buffer_load_dwordx4 v86, s[44:47], s55 offen lds\n"
    "s_add_u32 m0, s54, 34816\n"
    "s_nop 0\n"
    "buffer_load_dwordx4 v87, s[44:47], s55 offen lds\n"
    "s_add_u32 m0, s54, 35840\n"
    "s_nop 0\n"
    "buffer_load_dwordx4 v88, s[44:47], s55 offen lds\n"
    ".Lac7_%=:\n"
    "v_mfma_f32_16x16x32_bf16 v[12:15], v[56:59], a[0:3], 0\n"
    "v_mfma_f32_16x16x32_bf16 v[16:19], v[56:59], a[72:75], 0\n"
    "v_mfma_f32_16x16x32_bf16 v[20:23], v[56:59], a[144:147], 0\n"
    "v_mfma_f32_16x16x32_bf16 v[24:27], v[56:59], a[216:219], 0\n"
    "v_mfma_f32_16x16x32_bf16 v[12:15], v[56:59], a[16:19], v[12:15]\n"
    "v_mfma_f32_16x16x32_bf16 v[16:19], v[56:59], a[88:91], v[16:19]\n"
    "v_mfma_f32_16x16x32_bf16 v[20:23], v[56:59], a[160:163], v[20:23]\n"
    "v_mfma_f32_16x16x32_bf16 v[12:15], v[56:59], a[32:35], v[12:15]\n"
    "v_mfma_f32_16x16x32_bf16 v[16:19], v[56:59], a[104:107], v[16:19]\n"
    "v_mfma_f32_16x16x32_bf16 v[20:23], v[56:59], a[176:179], v[20:23]\n"
    "v_mfma_f32_16x16x32_bf16 v[24:27], v[56:59], a[232:235], v[24:27]\n"
    "v_mfma_f32_16x16x32_bf16 v[12:15], v[56:59], a[48:51], v[12:15]\n"
    "v_mfma_f32_16x16x32_bf16 v[16:19], v[56:59], a[120:123], v[16:19]\n"
    "v_mfma_f32_16x16x32_bf16 v[20:23], v[56:59], a[192:195], v[20:23]\n"
    "ds_read_b64_tr_b16 a[4:5], v106 offset:55296\n"
    "ds_read_b64_tr_b16 a[76:77], v106 offset:64512\n"
    "v_add_u32 v149, 0x10000, v106\n"
    "ds_read_b64_tr_b16 a[148:149], v149 offset:8192\n"
    "ds_read_b64_tr_b16 a[220:221], v107 offset:55296\n"
    "ds_read_b64_tr_b16 a[20:21], v108 offset:55296\n"
    "ds_read_b64_tr_b16 a[92:93], v108 offset:64512\n"
    "v_add_u32 v148, 0x10000, v108\n"
    "ds_read_b64_tr_b16 a[164:165], v148 offset:8192\n"
    "ds_read_b64_tr_b16 a[36:37], v110 offset:55296\n"
    "ds_read_b64_tr_b16 a[108:109], v110 offset:64512\n"
    "v_add_u32 v147, 0x10000, v110\n"
    "ds_read_b64_tr_b16 a[180:181], v147 offset:8192\n"
    "ds_read_b64_tr_b16 a[236:237], v111 offset:55296\n"
    "ds_read_b64_tr_b16 a[52:53], v112 offset:55296\n"
    "ds_read_b64_tr_b16 a[124:125], v112 offset:64512\n"
    "v_add_u32 v146, 0x10000, v112\n"
    "ds_read_b64_tr_b16 a[196:197], v146 offset:8192\n"
    "s_waitcnt vmcnt(27) lgkmcnt(0)\n"
    "s_barrier\n"
    "s_cmp_eq_u32 %2, 3\n"
    "s_cbranch_scc1 .Lac8_%=\n"
    "s_add_u32 s55, s51, 345600\n"
    "s_add_u32 m0, s54, 55296\n"
    "s_nop 0\n"
    "buffer_load_dwordx4 v80, s[44:47], s55 offen lds\n"
    "s_add_u32 m0, s54, 56320\n"
    "s_nop 0\n"
    "buffer_load_dwordx4 v81, s[44:47], s55 offen lds\n"
    "s_add_u32 m0, s54, 57344\n"
    "s_nop 0\n"
    "buffer_load_dwordx4 v82, s[44:47], s55 offen lds\n"
    "s_add_u32 m0, s54, 58368\n"
    "s_nop 0\n"
    "buffer_load_dwordx4 v83, s[44:47], s55 offen lds\n"
    "s_add_u32 m0, s54, 59392\n"
    "s_nop 0\n"
    "buffer_load_dwordx4 v84, s[44:47], s55 offen lds\n"
    "s_add_u32 m0, s54, 60416\n"
    "s_nop 0\n"
    "buffer_load_dwordx4 v85, s[44:47], s55 offen lds\n"
    "s_add_u32 m0, s54, 61440\n"
    "s_nop 0\n"
    "buffer_load_dwordx4 v86, s[44:47], s55 offen lds\n"
    "s_add_u32 m0, s54, 62464\n"
    "s_nop 0\n"
    "buffer_load_dwordx4 v87, s[44:47], s55 offen lds\n"
    "s_add_u32 m0, s54, 63488\n"
    "s_nop 0\n"
    "buffer_load_dwordx4 v88, s[44:47], s55 offen lds\n"
    ".Lac8_%=:\n"
    "ds_read_b64_tr_b16 a[6:7], v149 offset:17408\n"
    "ds_read_b64_tr_b16 a[78:79], v149 offset:26624\n"
    "ds_read_b64_tr_b16 a[150:151], v149 offset:35840\n"
    "v_add_u32 v145, 0x10000, v107\n"
    "ds_read_b64_tr_b16 a[222:223], v145 offset:17408\n"
    "ds_read_b64_tr_b16 a[22:23], v148 offset:17408\n"
    "ds_read_b64_tr_b16 a[94:95], v148 offset:26624\n"
    "ds_read_b64_tr_b16 a[166:167], v148 offset:35840\n"
    "ds_read_b64_tr_b16 a[38:39], v147 offset:17408\n"
    "ds_read_b64_tr_b16 a[110:111], v147 offset:26624\n"
    "ds_read_b64_tr_b16 a[182:183], v147 offset:35840\n"
    "v_add_u32 v144, 0x10000, v111\n"
    "ds_read_b64_tr_b16 a[238:239], v144 offset:17408\n"
    "ds_read_b64_tr_b16 a[54:55], v146 offset:17408\n"
    "ds_read_b64_tr_b16 a[126:127], v146 offset:26624\n"
    "ds_read_b64_tr_b16 a[198:199], v146 offset:35840\n"
    "s_waitcnt vmcnt(27) lgkmcnt(0)\n"
    "s_barrier\n"
    "s_cmp_eq_u32 %2, 3\n"
    "s_cbranch_scc1 .Lac9_%=\n"
    "s_add_u32 s55, s51, 1769472\n"
    "s_add_u32 m0, s54, 82944\n"
    "s_nop 0\n"
    "buffer_load_dwordx4 v89, s[44:47], s55 offen lds\n"
    "s_add_u32 m0, s54, 83968\n"
    "s_nop 0\n"
    "buffer_load_dwordx4 v90, s[44:47], s55 offen lds\n"
    "s_add_u32 m0, s54, 84992\n"
    "s_nop 0\n"
    "buffer_load_dwordx4 v91, s[44:47], s55 offen lds\n"
    "s_add_u32 m0, s54, 86016\n"
    "s_nop 0\n"
    "buffer_load_dwordx4 v92, s[44:47], s55 offen lds\n"
    "s_add_u32 m0, s54, 87040\n"
    "s_nop 0\n"
    "buffer_load_dwordx4 v93, s[44:47], s55 offen lds\n"
    "s_add_u32 m0, s54, 88064\n"
    "s_nop 0\n"
    "buffer_load_dwordx4 v94, s[44:47], s55 offen lds\n"
    "s_add_u32 m0, s54, 89088\n"
    "s_nop 0\n"
    "buffer_load_dwordx4 v95, s[44:47], s55 offen lds\n"
    "s_add_u32 m0, s54, 90112\n"
    "s_nop 0\n"
    "buffer_load_dwordx4 v96, s[44:47], s55 offen lds\n"
    "s_add_u32 m0, s54, 91136\n"
    "s_nop 0\n"
    "buffer_load_dwordx4 v97, s[44:47], s55 offen lds\n"
    ".Lac9_%=:\n"
    "v_mov_b32 v11, 0x20400\n"
    "global_load_dwordx2 v[76:77], v11, %1 sc1\n"
    "v_mbcnt_lo_u32_b32 v72, -1, 0\n"
    "v_min_u32 v72, 7, v72\n"
    "v_lshlrev_b32 v72, 7, v72\n"
    "v_add_u32 v72, 0x20000, v72\n"
    "global_load_dwordx2 v[78:79], v72, %1 sc1\n"
    "v_mfma_f32_16x16x32_bf16 v[12:15], v[56:59], a[4:7], v[12:15]\n"
    "v_mfma_f32_16x16x32_bf16 v[16:19], v[56:59], a[76:79], v[16:19]\n"
    "v_mfma_f32_16x16x32_bf16 v[20:23], v[56:59], a[148:151], v[20:23]\n"
    "v_mfma_f32_16x16x32_bf16 v[24:27], v[56:59], a[220:223], v[24:27]\n"
    "v_mfma_f32_16x16x32_bf16 v[12:15], v[56:59], a[20:23], v[12:15]\n"
    "v_mfma_f32_16x16x32_bf16 v[16:19], v[56:59], a[92:95], v[16:19]\n"
    "v_mfma_f32_16x16x32_bf16 v[20:23], v[56:59], a[164:167], v[20:23]\n"
    "v_mfma_f32_16x16x32_bf16 v[12:15], v[56:59], a[36:39], v[12:15]\n"
    "v_mfma_f32_16x16x32_bf16 v[16:19], v[56:59], a[108:111], v[16:19]\n"
    "v_mfma_f32_16x16x32_bf16 v[20:23], v[56:59], a[180:183], v[20:23]\n"
    "v_mfma_f32_16x16x32_bf16 v[24:27], v[56:59], a[236:239], v[24:27]\n"
    "v_mfma_f32_16x16x32_bf16 v[12:15], v[56:59], a[52:55], v[12:15]\n"
    "v_mfma_f32_16x16x32_bf16 v[16:19], v[56:59], a[124:127], v[16:19]\n"
    "v_mfma_f32_16x16x32_bf16 v[20:23], v[56:59], a[196:199], v[20:23]\n"
    "ds_read_b64_tr_b16 a[8:9], v149 offset:45056\n"
    "ds_read_b64_tr_b16 a[80:81], v149 offset:54272\n"
    "ds_read_b64_tr_b16 a[152:153], v149 offset:63488\n"
    "ds_read_b64_tr_b16 a[224:225], v145 offset:45056\n"
    "ds_read_b64_tr_b16 a[24:25], v148 offset:45056\n"
    "ds_read_b64_tr_b16 a[96:97], v148 offset:54272\n"
    "ds_read_b64_tr_b16 a[168:169], v148 offset:63488\n"
    "ds_read_b64_tr_b16 a[40:41], v147 offset:45056\n"
    "ds_read_b64_tr_b16 a[112:113], v147 offset:54272\n"
    "ds_read_b64_tr_b16 a[184:185], v147 offset:63488\n"
    "ds_read_b64_tr_b16 a[240:241], v144 offset:45056\n"
    "ds_read_b64_tr_b16 a[56:57], v146 offset:45056\n"
    "ds_read_b64_tr_b16 a[128:129], v146 offset:54272\n"
    "ds_read_b64_tr_b16 a[200:201], v146 offset:63488\n"
    "s_waitcnt vmcnt(29) lgkmcnt(0)\n"
    "s_barrier\n"
    "ds_read_b64_tr_b16 a[10:11], v106\n"
    "ds_read_b64_tr_b16 a[82:83], v106 offset:9216\n"
    "ds_read_b64_tr_b16 a[154:155], v106 offset:18432\n"
    "ds_read_b64_tr_b16 a[226:227], v107\n"
    "ds_read_b64_tr_b16 a[26:27], v108\n"
    "ds_read_b64_tr_b16 a[98:99], v108 offset:9216\n"
    "ds_read_b64_tr_b16 a[170:171], v108 offset:18432\n"
    "ds_read_b64_tr_b16 a[42:43], v110\n"
    "ds_read_b64_tr_b16 a[114:115], v110 offset:9216\n"
    "ds_read_b64_tr_b16 a[186:187], v110 offset:18432\n"
    "ds_read_b64_tr_b16 a[242:243], v111\n"
    "ds_read_b64_tr_b16 a[58:59], v112\n"
    "ds_read_b64_tr_b16 a[130:131], v112 offset:9216\n"
    "ds_read_b64_tr_b16 a[202:203], v112 offset:18432\n"
    "s_waitcnt vmcnt(20) lgkmcnt(0)\n"
    "s_barrier\n"
    "v_mfma_f32_16x16x32_bf16 v[12:15], v[56:59], a[8:11], v[12:15]\n"
    "v_mfma_f32_16x16x32_bf16 v[16:19], v[56:59], a[80:83], v[16:19]\n"
    "v_mfma_f32_16x16x32_bf16 v[20:23], v[56:59], a[152:155], v[20:23]\n"
    "v_mfma_f32_16x16x32_bf16 v[24:27], v[56:59], a[224:227], v[24:27]\n"
    "v_mfma_f32_16x16x32_bf16 v[12:15], v[56:59], a[24:27], v[12:15]\n"
    "v_mfma_f32_16x16x32_bf16 v[16:19], v[56:59], a[96:99], v[16:19]\n"
    "v_mfma_f32_16x16x32_bf16 v[20:23], v[56:59], a[168:171], v[20:23]\n"
    "v_mfma_f32_16x16x32_bf16 v[12:15], v[56:59], a[40:43], v[12:15]\n"
    "v_mfma_f32_16x16x32_bf16 v[16:19], v[56:59], a[112:115], v[16:19]\n"
    "v_mfma_f32_16x16x32_bf16 v[20:23], v[56:59], a[184:187], v[20:23]\n"
    "v_mfma_f32_16x16x32_bf16 v[24:27], v[56:59], a[240:243], v[24:27]\n"
    "v_mfma_f32_16x16x32_bf16 v[12:15], v[56:59], a[56:59], v[12:15]\n"
    "v_mfma_f32_16x16x32_bf16 v[16:19], v[56:59], a[128:131], v[16:19]\n"
    "v_mfma_f32_16x16x32_bf16 v[20:23], v[56:59], a[200:203], v[20:23]\n"
    "ds_read_b64_tr_b16 a[12:13], v106 offset:27648\n"
    "ds_read_b64_tr_b16 a[84:85], v106 offset:36864\n"
    "ds_read_b64_tr_b16 a[156:157], v106 offset:46080\n"
    "ds_read_b64_tr_b16 a[228:229], v107 offset:27648\n"
    "ds_read_b64_tr_b16 a[28:29], v108 offset:27648\n"
    "ds_read_b64_tr_b16 a[100:101], v108 offset:36864\n"
    "ds_read_b64_tr_b16 a[172:173], v108 offset:46080\n"
    "ds_read_b64_tr_b16 a[44:45], v110 offset:27648\n"
    "ds_read_b64_tr_b16 a[116:117], v110 offset:36864\n"
    "ds_read_b64_tr_b16 a[188:189], v110 offset:46080\n"
    "ds_read_b64_tr_b16 a[244:245], v111 offset:27648\n"
    "ds_read_b64_tr_b16 a[60:61], v112 offset:27648\n"
    "ds_read_b64_tr_b16 a[132:133], v112 offset:36864\n"
    "ds_read_b64_tr_b16 a[204:205], v112 offset:46080\n"
    "s_waitcnt vmcnt(11) lgkmcnt(0)\n"
    "s_barrier\n"
    "ds_read_b64_tr_b16 a[14:15], v106 offset:55296\n"
    "ds_read_b64_tr_b16 a[86:87], v106 offset:64512\n"
    "ds_read_b64_tr_b16 a[158:159], v149 offset:8192\n"
    "ds_read_b64_tr_b16 a[230:231], v107 offset:55296\n"
    "ds_read_b64_tr_b16 a[30:31], v108 offset:55296\n"
    "ds_read_b64_tr_b16 a[102:103], v108 offset:64512\n"
    "ds_read_b64_tr_b16 a[174:175], v148 offset:8192\n"
    "ds_read_b64_tr_b16 a[46:47], v110 offset:55296\n"
    "ds_read_b64_tr_b16 a[118:119], v110 offset:64512\n"
    "ds_read_b64_tr_b16 a[190:191], v147 offset:8192\n"
    "ds_read_b64_tr_b16 a[246:247], v111 offset:55296\n"
    "ds_read_b64_tr_b16 a[62:63], v112 offset:55296\n"
    "ds_read_b64_tr_b16 a[134:135], v112 offset:64512\n"
    "ds_read_b64_tr_b16 a[206:207], v146 offset:8192\n"
    "s_waitcnt vmcnt(2) lgkmcnt(0)\n"
    "s_barrier\n"
    "v_mfma_f32_16x16x32_bf16 v[12:15], v[56:59], a[12:15], v[12:15]\n"
    "v_mfma_f32_16x16x32_bf16 v[16:19], v[56:59], a[84:87], v[16:19]\n"
    "v_mfma_f32_16x16x32_bf16 v[20:23], v[56:59], a[156:159], v[20:23]\n"
    "v_mfma_f32_16x16x32_bf16 v[24:27], v[56:59], a[228:231], v[24:27]\n"
    "v_mfma_f32_16x16x32_bf16 v[12:15], v[56:59], a[28:31], v[12:15]\n"
    "v_mfma_f32_16x16x32_bf16 v[16:19], v[56:59], a[100:103], v[16:19]\n"
    "v_mfma_f32_16x16x32_bf16 v[20:23], v[56:59], a[172:175], v[20:23]\n"
    "v_mfma_f32_16x16x32_bf16 v[12:15], v[56:59], a[44:47], v[12:15]\n"
    "v_mfma_f32_16x16x32_bf16 v[16:19], v[56:59], a[116:119], v[16:19]\n"
    "v_mfma_f32_16x16x32_bf16 v[20:23], v[56:59], a[188:191], v[20:23]\n"
    "v_mfma_f32_16x16x32_bf16 v[24:27], v[56:59], a[244:247], v[24:27]\n"
    "v_mfma_f32_16x16x32_bf16 v[12:15], v[56:59], a[60:63], v[12:15]\n"
    "v_mfma_f32_16x16x32_bf16 v[16:19], v[56:59], a[132:135], v[16:19]\n"
    "v_mfma_f32_16x16x32_bf16 v[20:23], v[56:59], a[204:207], v[20:23]\n"
    "v_add_u32 v143, 0x10000, v114\n"
    "ds_read_b64_tr_b16 a[64:65], v143 offset:17408\n"
    "ds_read_b64_tr_b16 a[136:137], v143 offset:26624\n"
    "ds_read_b64_tr_b16 a[208:209], v143 offset:35840\n"
    "v_add_u32 v142, 0x10000, v115\n"
    "ds_read_b64_tr_b16 a[248:249], v142 offset:17408\n"
    "ds_read_b64_tr_b16 a[66:67], v143 offset:19712\n"
    "ds_read_b64_tr_b16 a[138:139], v143 offset:28928\n"
    "ds_read_b64_tr_b16 a[210:211], v143 offset:38144\n"
    "ds_read_b64_tr_b16 a[250:251], v142 offset:19712\n"
    "v_add_u32 v141, 0x10000, v116\n"
    "ds_read_b64_tr_b16 a[68:69], v141 offset:17408\n"
    "ds_read_b64_tr_b16 a[140:141], v141 offset:26624\n"
    "ds_read_b64_tr_b16 a[212:213], v141 offset:35840\n"
    "ds_read_b64_tr_b16 a[70:71], v141 offset:19712\n"
    "ds_read_b64_tr_b16 a[142:143], v141 offset:28928\n"
    "ds_read_b64_tr_b16 a[214:215], v141 offset:38144\n"
    "s_waitcnt lgkmcnt(0)\n"
    "s_barrier\n"
    "v_mfma_f32_16x16x32_bf16 v[12:15], v[56:59], a[64:67], v[12:15]\n"
    "v_mfma_f32_16x16x32_bf16 v[16:19], v[56:59], a[136:139], v[16:19]\n"
    "v_mfma_f32_16x16x32_bf16 v[20:23], v[56:59], a[208:211], v[20:23]\n"
    "v_mfma_f32_16x16x32_bf16 v[24:27], v[56:59], a[248:251], v[24:27]\n"
    "v_mfma_f32_16x16x32_bf16 v[12:15], v[56:59], a[68:71], v[12:15]\n"
    "v_mfma_f32_16x16x32_bf16 v[16:19], v[56:59], a[140:143], v[16:19]\n"
    "v_mfma_f32_16x16x32_bf16 v[20:23], v[56:59], a[212:215], v[20:23]\n"
    "v_mov_b32 v28, 0\n"
    "v_mov_b32 v29, 0\n"
    "v_mov_b32 v30, 0\n"
    "v_mov_b32 v31, 0\n"
    "v_mov_b32 v215, 0\n"
    "v_mov_b32 v218, 0\n"
    "v_mov_b32 v219, 0\n"
    "v_add_u32 v73, 0x14000, v3\n"
    "ds_write_b128 v73, v[28:31] offset:4096\n"
    "ds_write_b128 v73, v[28:31] offset:8192\n"
    "ds_write_b128 v73, v[28:31] offset:20480\n"
    "ds_write_b128 v73, v[28:31] offset:24576\n"
    "v_mov_b32 v153, 0xf149f2ca\n"
    "v_add_u32 v103, 7, v152\n"
    "v_cmp_gt_u32 vcc, 8, v103\n"
    "s_nop 1\n"
    "v_cndmask_b32 v44, v153, v28, vcc\n"
    "v_cmp_le_i32 vcc, 9, v152\n"
    "s_nop 1\n"
    "v_cndmask_b32 v68, v153, v28, vcc\n"
    "v_add_u32 v103, 8, v152\n"
    "v_cmp_gt_u32 vcc, 8, v103\n"
    "s_nop 1\n"
    "v_cndmask_b32 v45, v153, v28, vcc\n"
    "v_cmp_le_i32 vcc, 8, v152\n"
    "s_nop 1\n"
    "v_cndmask_b32 v69, v153, v28, vcc\n"
    "v_add_u32 v103, 9, v152\n"
    "v_cmp_gt_u32 vcc, 8, v103\n"
    "s_nop 1\n"
    "v_cndmask_b32 v46, v153, v28, vcc\n"
    "v_cmp_le_i32 vcc, 7, v152\n"
    "s_nop 1\n"
    "v_cndmask_b32 v70, v153, v28, vcc\n"
    "v_add_u32 v103, 10, v152\n"
    "v_cmp_gt_u32 vcc, 8, v103\n"
    "s_nop 1\n"
    "v_cndmask_b32 v47, v153, v28, vcc\n"
    "v_cmp_le_i32 vcc, 6, v152\n"
    "s_nop 1\n"
    "v_cndmask_b32 v71, v153, v28, vcc\n"
    "v_mov_b32 v199, 0x3f803f80\n"
    "v_cvt_pk_bf16_f32 v31, v153, v153\n"
    "ds_write_b128 v73, v[28:31]\n"
    "ds_write_b128 v73, v[28:31] offset:16384\n"
    "s_mov_b32 s86, 0xcccccccc\n"
    "s_mov_b32 s87, 0xcccccccc\n"
    "v_cvt_pk_bf16_f32 v13, 0, v12\n"
    "v_sub_f32 v12, v12, v13\n"
    "v_cvt_pk_bf16_f32 v14, 0, v12\n"
    "v_sub_f32 v12, v12, v14\n"
    "v_cvt_pk_bf16_f32 v13, v13, v14\n"
    "v_cvt_pk_bf16_f32 v14, v12, 0\n"
    "s_mov_b64 vcc, 0xffff\n"
    "v_cndmask_b32 v60, 0, v13, vcc\n"
    "v_cndmask_b32 v61, 0, v14, vcc\n"
    "v_cvt_pk_bf16_f32 v17, 0, v16\n"
    "v_sub_f32 v16, v16, v17\n"
    "v_cvt_pk_bf16_f32 v18, 0, v16\n"
    "v_sub_f32 v16, v16, v18\n"
    "v_cvt_pk_bf16_f32 v17, v17, v18\n"
    "v_cvt_pk_bf16_f32 v18, v16, 0\n"
    "s_mov_b64 vcc, 0xffff\n"
    "v_cndmask_b32 v62, 0, v17, vcc\n"
    "v_cndmask_b32 v63, 0, v18, vcc\n"
    "v_cvt_pk_bf16_f32 v21, 0, v20\n"
    "v_sub_f32 v20, v20, v21\n"
    "v_cvt_pk_bf16_f32 v22, 0, v20\n"
    "v_sub_f32 v20, v20, v22\n"
    "v_cvt_pk_bf16_f32 v21, v21, v22\n"
    "v_cvt_pk_bf16_f32 v22, v20, 0\n"
    "s_mov_b64 vcc, 0xffff\n"
    "v_cndmask_b32 v64, 0, v21, vcc\n"
    "v_cndmask_b32 v65, 0, v22, vcc\n"
    "v_cvt_pk_bf16_f32 v25, 0, v24\n"
    "v_sub_f32 v24, v24, v25\n"
    "v_cvt_pk_bf16_f32 v26, 0, v24\n"
    "v_sub_f32 v24, v24, v26\n"
    "v_cvt_pk_bf16_f32 v25, v25, v26\n"
    "v_cvt_pk_bf16_f32 v26, v24, 0\n"
    "s_mov_b64 vcc, 0xffff\n"
    "v_cndmask_b32 v66, 0, v25, vcc\n"
    "v_cndmask_b32 v67, 0, v26, vcc\n"
    "v_xor_b32 v49, 0x2000, v0\n"
    "v_xor_b32 v50, 0x4000, v0\n"
    "v_xor_b32 v51, 0x6000, v0\n"
    "v_and_b32 v52, 0xffffbfff, v0\n"
    "v_xor_b32 v53, 0x2000, v52\n"
    "s_mov_b64 s[44:45], %1\n"
    "s_and_b32 s45, s45, 0xffff\n"
    "s_mov_b32 s46, 0x0\n"
    "s_mov_b32 s47, 0x20000\n"
    "v_mov_b32 v158, 0\n"
    "s_waitcnt vmcnt(0)\n"
    "v_add_u32 v76, 1, v76\n"
    "v_add_u32 v77, 1, v77\n"
    "s_mov_b32 s52, 1048576\n"
    ".Lago_a%=:\n"
    "v_cmp_ne_u64 vcc, v[76:77], v[78:79]\n"
    "s_cbranch_vccz .Lago_x%=\n"
    "global_load_dwordx2 v[78:79], v72, %1 sc1\n"
    "s_sub_u32 s52, s52, 1\n"
    "s_waitcnt vmcnt(0)\n"
    "s_cbranch_scc0 .Lago_a%=\n"
    ".Lago_x%=:\n"
    "v_mbcnt_lo_u32_b32 v76, -1, 0\n"
    "v_mbcnt_hi_u32_b32 v76, -1, v76\n"
    "v_min_u32 v72, 5, v76\n"
    "v_lshlrev_b32 v72, 7, v72\n"
    "v_mov_b32 v11, 0x26c00\n"
    "ds_read_b128 v[76:79], v11\n"
    "s_waitcnt lgkmcnt(0)\n"
    "v_readfirstlane_b32 s49, v77\n"
    "v_add_u32 v76, v76, v72\n"
    "s_branch .Lachk_%=\n"
    ".Lago_%=:\n"
    "v_add_u32 v11, 32, v11\n"
    "s_waitcnt lgkmcnt(3)\n"
    "v_mfma_f32_16x16x32_bf16 v[24:27], a[216:219], v[80:83], 0\n"
    "ds_read_b128 v[76:79], v11\n"
    "v_mul_f32 v20, v20, v200\n"
    "v_mul_f32 v21, v21, v201\n"
    "v_mul_f32 v22, v22, v202\n"
    "s_waitcnt lgkmcnt(3)\n"
    "v_mfma_f32_16x16x32_bf16 v[24:27], a[220:223], v[84:87], v[24:27]\n"
    "ds_read_b32 v74, v11 offset:24\n"
    "v_mul_f32 v23, v23, v203\n"
    "v_cvt_pk_bf16_f32 v34, v20, v21\n"
    "v_cvt_pk_bf16_f32 v35, v22, v23\n"
    "s_waitcnt lgkmcnt(3)\n"
    "v_mfma_f32_16x16x32_bf16 v[24:27], a[224:227], v[88:91], v[24:27]\n"
    "v_rcp_f32 v40, v186\n"
    "v_mfma_f32_16x16x32_bf16 v[180:183], v[32:35], v[204:207], 0\n"
    "ds_write2st64_b64 v73, v[34:35], v[38:39] offset0:8 offset1:16\n"
    "v_mul_f32 v184, v184, v40\n"
    "v_mul_f32 v185, v185, v40\n"
    "s_waitcnt lgkmcnt(3)\n"
    "v_mfma_f32_16x16x32_bf16 v[24:27], a[228:231], v[92:95], v[24:27]\n"
    "s_xor_b32 s58, s58, 0x8000\n"
    "s_xor_b32 s59, s59, 0x8000\n"
    "v_cvt_pk_bf16_f32 v184, v184, v185\n"
    "s_waitcnt lgkmcnt(2)\n"
    "v_add_u32 v76, v76, v72\n"
    "v_mfma_f32_16x16x32_bf16 v[12:15], a[0:3], v[80:83], 0\n"
    "v_readfirstlane_b32 s49, v77\n"
    "v_mul_f32 v180, v180, v40\n"
    "v_mul_f32 v181, v181, v40\n"
    "v_mfma_f32_16x16x32_bf16 v[16:19], a[72:75], v[80:83], 0\n"
    "s_waitcnt vmcnt(2)\n"
    "s_barrier\n"
    "ds_read_b128 v[112:115], v50\n"
    "v_mul_f32 v182, v182, v40\n"
    "v_mul_f32 v183, v183, v40\n"
    "v_mfma_f32_16x16x32_bf16 v[12:15], a[4:7], v[84:87], v[12:15]\n"
    "ds_read2st64_b32 v[160:161], v7 offset1:1\n"
    "v_cvt_pk_bf16_f32 v204, v180, v181\n"
    "v_cvt_pk_bf16_f32 v206, v180, v181\n"
    "v_mfma_f32_16x16x32_bf16 v[16:19], a[76:79], v[84:87], v[16:19]\n"
    "ds_read2st64_b32 v[162:163], v7 offset0:2 offset1:3\n"
    "v_cvt_pk_bf16_f32 v205, v182, v183\n"
    "v_cvt_pk_bf16_f32 v207, v182, v183\n"
    "v_mfma_f32_16x16x32_bf16 v[12:15], a[8:11], v[88:91], v[12:15]\n"
    "ds_read2st64_b32 v[164:165], v7 offset0:4 offset1:5\n"
    "v_permlane16_swap_b32 v204, v206\n"
    "v_mfma_f32_16x16x32_bf16 v[16:19], a[80:83], v[88:91], v[16:19]\n"
    "ds_read2st64_b32 v[166:167], v7 offset0:6 offset1:7\n"
    "v_permlane16_swap_b32 v205, v207\n"
    "v_mfma_f32_16x16x32_bf16 v[12:15], a[12:15], v[92:95], v[12:15]\n"
    "ds_read_b32 v168, v7 offset:2048\n"
    "v_mov_b32_dpp v204, v184 quad_perm:[0,0,0,0] row_mask:0x2 bank_mask:0xf\n"
    "v_mov_b32_dpp v206, v184 quad_perm:[2,2,2,2] row_mask:0x2 bank_mask:0xf\n"
    "v_mfma_f32_16x16x32_bf16 v[16:19], a[84:87], v[92:95], v[16:19]\n"
    "ds_read_b128 v[116:119], v50 offset:1024\n"
    "v_mov_b32_dpp v205, v184 quad_perm:[1,1,1,1] row_mask:0x2 bank_mask:0xf\n"
    "v_mov_b32_dpp v207, v184 quad_perm:[3,3,3,3] row_mask:0x2 bank_mask:0xf\n"
    "s_waitcnt lgkmcnt(6)\n"
    "v_mfma_f32_16x16x32_bf16 v[24:27], a[232:235], v[112:115], v[24:27]\n"
    "buffer_store_dwordx4 v[204:207], v1, s[40:43], s62 offen sc1\n"
    "v_add_u32 v73, v74, v3\n"
    "s_waitcnt lgkmcnt(5)\n"
    "v_add_f32 v160, v160, v161\n"
    "v_mfma_f32_16x16x32_bf16 v[12:15], a[32:35], v[112:115], v[12:15]\n"
    "ds_read_b128 v[120:123], v50 offset:2048\n"
    "s_waitcnt lgkmcnt(5)\n"
    "v_add_f32 v162, v162, v163\n"
    "s_waitcnt lgkmcnt(4)\n"
    "v_add_f32 v164, v164, v165\n"
    "v_mfma_f32_16x16x32_bf16 v[16:19], a[104:107], v[112:115], v[16:19]\n"
    "ds_read_b128 v[124:127], v50 offset:3072\n"
    "s_waitcnt lgkmcnt(4)\n"
    "v_add_f32 v166, v166, v167\n"
    "v_add_f32 v160, v160, v162\n"
    ".Latop_%=:\n"
    "s_waitcnt lgkmcnt(2)\n"
    "v_mfma_f32_16x16x32_bf16 v[24:27], a[236:239], v[116:119], v[24:27]\n"
    "ds_read_b128 v[144:147], v52 offset:4096\n"
    "v_add_f32 v164, v164, v166\n"
    "v_add_f32 v160, v160, v168\n"
    "v_mfma_f32_16x16x32_bf16 v[12:15], a[36:39], v[116:119], v[12:15]\n"
    "ds_read_b128 v[96:99], v49\n"
    "v_add_f32 v168, v160, v164\n"
    "v_add_f32 v160, v160, v164\n"
    "v_mfma_f32_16x16x32_bf16 v[16:19], a[108:111], v[116:119], v[16:19]\n"
    "ds_read_b128 v[100:103], v49 offset:1024\n"
    "v_permlane32_swap_b32 v160, v168\n"
    "s_waitcnt lgkmcnt(4)\n"
    "v_mfma_f32_16x16x32_bf16 v[24:27], a[240:243], v[120:123], v[24:27]\n"
    "ds_read_b128 v[104:107], v49 offset:2048\n"
    "v_add_f32 v161, v160, v168\n"
    "v_add_f32 v168, v168, v160\n"
    "v_mfma_f32_16x16x32_bf16 v[12:15], a[40:43], v[120:123], v[12:15]\n"
    "ds_read_b128 v[108:111], v49 offset:3072\n"
    "v_permlane16_swap_b32 v161, v168\n"
    "v_mfma_f32_16x16x32_bf16 v[16:19], a[112:115], v[120:123], v[16:19]\n"
    "ds_read_b128 v[128:131], v51\n"
    "v_mul_f32 v160, 0xbae38e39, v161\n"
    "v_mul_f32 v168, 0x3ae38e39, v168\n"
    "s_waitcnt lgkmcnt(6)\n"
    "v_mfma_f32_16x16x32_bf16 v[24:27], a[244:247], v[124:127], v[24:27]\n"
    "ds_read_b128 v[132:135], v51 offset:1024\n"
    "s_waitcnt vmcnt(2)\n"
    "v_cmp_eq_u32 vcc, 0, v158\n"
    "v_mfma_f32_16x16x32_bf16 v[12:15], a[44:47], v[124:127], v[12:15]\n"
    "ds_read_b128 v[136:139], v51 offset:2048\n"
    "s_cmp_eq_u64 vcc, 0\n"
    "s_cselect_b32 s46, 0, 0x20000\n"
    "s_cselect_b32 s94, s42, 0\n"
    "v_mfma_f32_16x16x32_bf16 v[16:19], a[116:119], v[124:127], v[16:19]\n"
    "buffer_load_dword v55, v76, s[44:47], 0 offen sc1\n"
    "s_cselect_b32 s90, s96, 0\n"
    "v_cvt_pk_bf16_f32 v216, v160, v160\n"
    "s_waitcnt lgkmcnt(7)\n"
    "v_mfma_f32_16x16x32_bf16 v[24:27], a[248:251], v[144:147], v[24:27]\n"
    "ds_read_b128 v[140:143], v51 offset:3072\n"
    "s_mov_b32 m0, s58\n"
    "v_cvt_pk_bf16_f32 v217, v160, v160\n"
    "v_mfma_f32_16x16x32_bf16 v[12:15], a[64:67], v[144:147], v[12:15]\n"
    "buffer_load_dwordx4 v2, s[92:95], s49 offen sc1 lds\n"
    "v_fma_f32 v168, -v160, v160, v168\n"
    "v_max_f32 v168, 0, v168\n"
    "v_mfma_f32_16x16x32_bf16 v[24:27], v[66:69], v[216:219], v[24:27]\n"
    "ds_read_b32 v158, v11 offset:32\n"
    "v_cvt_pk_bf16_f32 v168, 0, v168\n"
    "v_add_f32 v168, 0x3727c5ac, v168\n"
    "v_mfma_f32_16x16x32_bf16 v[16:19], a[136:139], v[144:147], v[16:19]\n"
    "ds_read_b128 v[148:151], v53 offset:4096\n"
    "v_cvt_pk_bf16_f32 v168, 0, v168\n"
    "v_sqrt_f32 v168, v168\n"
    "s_waitcnt lgkmcnt(9)\n"
    "v_mfma_f32_16x16x32_bf16 v[12:15], a[16:19], v[96:99], v[12:15]\n"
    "buffer_load_dwordx4 v2, s[92:95], s49 offen offset:1024 sc1 lds\n"
    "v_cvt_pk_bf16_f32 v168, 0, v168\n"
    "v_mfma_f32_16x16x32_bf16 v[12:15], v[60:63], v[216:219], v[12:15]\n"
    "ds_read_b128 v[28:31], v73\n"
    "v_rcp_f32 v168, v168\n"
    "s_nop 0\n"
    "v_mul_f32 v24, v24, v168\n"
    "v_mfma_f32_16x16x32_bf16 v[16:19], a[88:91], v[96:99], v[16:19]\n"
    "ds_read_b64 v[32:33], v73 offset:4096\n"
    "v_mul_f32 v25, v25, v168\n"
    "v_mul_f32 v26, v26, v168\n"
    "v_mul_f32 v27, v27, v168\n"
    "v_mfma_f32_16x16x32_bf16 v[16:19], v[62:65], v[216:219], v[16:19]\n"
    "ds_write_b128 v9, v[24:27]\n"
    "s_waitcnt lgkmcnt(4)\n"
    "v_add_u32 v158, v158, v72\n"
    "v_mfma_f32_16x16x32_bf16 v[12:15], a[20:23], v[100:103], v[12:15]\n"
    "buffer_load_dwordx4 v2, s[92:95], s49 offen offset:2048 sc1 lds\n"
    "v_mfma_f32_16x16x32_bf16 v[16:19], a[92:95], v[100:103], v[16:19]\n"
    "ds_write_b32 v7, v168 offset:2304\n"
    "v_readfirstlane_b32 s62, v78\n"
    "v_mfma_f32_16x16x32_bf16 v[12:15], a[24:27], v[104:107], v[12:15]\n"
    "ds_read_b128 v[200:203], v8\n"
    "v_mfma_f32_16x16x32_bf16 v[16:19], a[96:99], v[104:107], v[16:19]\n"
    "buffer_load_dwordx4 v2, s[92:95], s49 offen offset:3072 sc1 lds\n"
    "v_xor_b32 v7, 0x8000, v7\n"
    "v_xor_b32 v0, 0x8000, v0\n"
    "v_mfma_f32_16x16x32_bf16 v[12:15], a[28:31], v[108:111], v[12:15]\n"
    "ds_read_b64 v[36:37], v73 offset:8192\n"
    "v_xor_b32 v8, 0x8000, v8\n"
    "v_xor_b32 v49, 0x8000, v49\n"
    "v_mfma_f32_16x16x32_bf16 v[16:19], a[100:103], v[108:111], v[16:19]\n"
    "s_waitcnt lgkmcnt(0)\n"
    "s_barrier\n"
    "ds_read2st64_b64 v[180:183], v4 offset1:2\n"
    "s_bitset1_b32 m0, 12\n"
    "v_xor_b32 v50, 0x8000, v50\n"
    "v_mfma_f32_16x16x32_bf16 v[12:15], a[48:51], v[128:131], v[12:15]\n"
    "buffer_load_dwordx4 v3, s[88:91], s49 offen sc1 lds\n"
    "v_xor_b32 v51, 0x8000, v51\n"
    "v_xor_b32 v52, 0x8000, v52\n"
    "v_mfma_f32_16x16x32_bf16 v[16:19], a[120:123], v[128:131], v[16:19]\n"
    "ds_read2st64_b64 v[184:187], v5 offset1:2\n"
    "s_mov_b32 m0, s59\n"
    "s_mov_b32 s55, s49\n"
    "s_bitset1_b32 s55, 15\n"
    "v_mfma_f32_16x16x32_bf16 v[12:15], a[52:55], v[132:135], v[12:15]\n"
    "ds_read2_b32 v[188:189], v6 offset1:4\n"
    "v_xor_b32 v53, 0x8000, v53\n"
    "v_mfma_f32_16x16x32_bf16 v[16:19], a[124:127], v[132:135], v[16:19]\n"
    "s_mov_b64 exec, s[60:61]\n"
    "buffer_load_dwordx4 v3, s[92:95], s55 offen offset:2048 sc1 lds\n"
    "s_mov_b64 exec, -1\n"
    "v_mfma_f32_16x16x32_bf16 v[12:15], a[56:59], v[136:139], v[12:15]\n"
    "ds_read2_b32 v[192:193], v10 offset1:4\n"
    "v_mfma_f32_16x16x32_bf16 v[16:19], a[128:131], v[136:139], v[16:19]\n"
    "ds_read2_b32 v[190:191], v6 offset0:8 offset1:12\n"
    "s_waitcnt lgkmcnt(4)\n"
    "v_add_f32 v180, v180, v182\n"
    "v_add_f32 v181, v181, v183\n"
    "v_mfma_f32_16x16x32_bf16 v[12:15], a[60:63], v[140:143], v[12:15]\n"
    "ds_read2_b32 v[194:195], v10 offset0:8 offset1:12\n"
    "v_cvt_pk_bf16_f32 v198, v180, v181\n"
    "s_waitcnt lgkmcnt(4)\n"
    "v_add_f32 v184, v184, v186\n"
    "v_mfma_f32_16x16x32_bf16 v[16:19], a[132:135], v[140:143], v[16:19]\n"
    "s_cmp_eq_u32 s94, 0\n"
    "s_cbranch_scc1 .Lak10_%=\n"
    ".Lak10_x%=:\n"
    "v_add_f32 v185, v185, v187\n"
    "v_cvt_pk_bf16_f32 v214, v184, v185\n"
    "v_mfma_f32_16x16x32_bf16 v[12:15], a[68:71], v[148:151], v[12:15]\n"
    "v_mfma_f32_16x16x32_bf16 v[16:19], a[140:143], v[148:151], v[16:19]\n"
    "s_waitcnt lgkmcnt(2)\n"
    "v_add_f32 v188, v188, v192\n"
    "v_add_f32 v189, v189, v193\n"
    "s_waitcnt lgkmcnt(0)\n"
    "v_add_f32 v190, v190, v194\n"
    "v_mfma_f32_16x16x32_bf16 v[20:23], v[216:219], v[64:67], 0\n"
    "v_mul_f32 v12, v12, v168\n"
    "v_mul_f32 v13, v13, v168\n"
    "v_mul_f32 v14, v14, v168\n"
    "v_mfma_f32_16x16x32_bf16 v[20:23], v[80:83], a[144:147], v[20:23]\n"
    "v_mov_b32 v54, 1\n"
    "v_mul_f32 v15, v15, v168\n"
    "v_cvt_pk_bf16_f32 v196, v12, v13\n"
    "v_mfma_f32_16x16x32_bf16 v[20:23], v[84:87], a[148:151], v[20:23]\n"
    "s_waitcnt vmcnt(7)\n"
    "v_cvt_pk_bf16_f32 v197, v14, v15\n"
    "v_mul_f32 v16, v16, v168\n"
    "v_mul_f32 v17, v17, v168\n"
    "v_mfma_f32_16x16x32_bf16 v[204:207], v[28:31], v[196:199], v[68:71]\n"
    "s_mov_b64 exec, s[56:57]\n"
    "ds_add_rtn_u32 v54, v11, v54 offset:16\n"
    "s_mov_b64 exec, -1\n"
    "v_mul_f32 v18, v18, v168\n"
    "v_mfma_f32_16x16x32_bf16 v[20:23], v[88:91], a[152:155], v[20:23]\n"
    "v_mul_f32 v19, v19, v168\n"
    "v_cvt_pk_bf16_f32 v212, v16, v17\n"
    "v_cvt_pk_bf16_f32 v213, v18, v19\n"
    "v_mfma_f32_16x16x32_bf16 v[20:23], v[92:95], a[156:159], v[20:23]\n"
    "v_add_f32 v191, v191, v195\n"
    "v_cvt_pk_bf16_f32 v38, v188, v189\n"
    "v_cvt_pk_bf16_f32 v39, v190, v191\n"
    "v_mfma_f32_16x16x32_bf16 v[208:211], v[212:215], v[196:199], v[44:47]\n"
    "ds_write_b128 v73, v[212:215]\n"
    "v_maximum3_f32 v40, v204, v205, v206\n"
    "v_cndmask_b32 v38, v38, v56, s[86:87]\n"
    "v_mfma_f32_16x16x32_bf16 v[20:23], v[96:99], a[160:163], v[20:23]\n"
    "global_load_dword v158, v158, %1 sc1\n"
    "v_cndmask_b32 v39, v39, v56, s[86:87]\n"
    "v_mfma_f32_16x16x32_bf16 v[20:23], v[100:103], a[164:167], v[20:23]\n"
    "s_waitcnt lgkmcnt(1)\n"
    "v_readfirstlane_b32 s50, v54\n"
    "v_maximum3_f32 v40, v40, v207, v208\n"
    "v_maximum3_f32 v40, v40, v209, v210\n"
    "v_max_f32 v41, v40, v211\n"
    "v_mfma_f32_16x16x32_bf16 v[20:23], v[104:107], a[168:171], v[20:23]\n"
    "s_cmp_eq_u32 s50, 3\n"
    "s_cselect_b32 s46, 0x20000, 0\n"
    "v_max_f32 v40, v40, v211\n"
    "v_mfma_f32_16x16x32_bf16 v[20:23], v[108:111], a[172:175], v[20:23]\n"
    "buffer_store_dword v54, v79, s[44:47], 0 offen sc1\n"
    "v_permlane16_swap_b32 v41, v40\n"
    "v_max_f32 v42, v40, v41\n"
    "v_max_f32 v40, v40, v41\n"
    "v_mfma_f32_16x16x32_bf16 v[20:23], v[112:115], a[176:179], v[20:23]\n"
    "s_nop 0\n"
    "v_permlane32_swap_b32 v42, v40\n"
    "v_max_f32 v40, v40, v42\n"
    "v_mul_f32 v40, 0xbeae1a93, v40\n"
    "v_fmamk_f32 v204, v204, 0x3eae1a93, v40\n"
    "v_mfma_f32_16x16x32_bf16 v[20:23], v[116:119], a[180:183], v[20:23]\n"
    "v_fmamk_f32 v205, v205, 0x3eae1a93, v40\n"
    "v_exp_f32 v204, v204\n"
    "v_exp_f32 v205, v205\n"
    "v_mfma_f32_16x16x32_bf16 v[20:23], v[120:123], a[184:187], v[20:23]\n"
    "v_fmamk_f32 v206, v206, 0x3eae1a93, v40\n"
    "v_fmamk_f32 v207, v207, 0x3eae1a93, v40\n"
    "v_exp_f32 v206, v206\n"
    "v_mfma_f32_16x16x32_bf16 v[20:23], v[124:127], a[188:191], v[20:23]\n"
    "v_exp_f32 v207, v207\n"
    "v_fmamk_f32 v208, v208, 0x3eae1a93, v40\n"
    "v_mfma_f32_16x16x32_bf16 v[20:23], v[128:131], a[192:195], v[20:23]\n"
    "v_fmamk_f32 v209, v209, 0x3eae1a93, v40\n"
    "v_exp_f32 v208, v208\n"
    "v_exp_f32 v209, v209\n"
    "v_mfma_f32_16x16x32_bf16 v[20:23], v[132:135], a[196:199], v[20:23]\n"
    "v_fmamk_f32 v210, v210, 0x3eae1a93, v40\n"
    "v_fmamk_f32 v211, v211, 0x3eae1a93, v40\n"
    "v_exp_f32 v210, v210\n"
    "v_mfma_f32_16x16x32_bf16 v[20:23], v[136:139], a[200:203], v[20:23]\n"
    "s_waitcnt vmcnt(4)\n"
    "ds_read_b128 v[80:83], v0\n"
    "v_exp_f32 v211, v211\n"
    "v_cvt_pk_bf16_f32 v204, v204, v205\n"
    "v_mfma_f32_16x16x32_bf16 v[20:23], v[140:143], a[204:207], v[20:23]\n"
    "ds_read_b128 v[84:87], v0 offset:1024\n"
    "v_cvt_pk_bf16_f32 v205, v206, v207\n"
    "v_cvt_pk_bf16_f32 v206, v208, v209\n"
    "v_cvt_pk_bf16_f32 v207, v210, v211\n"
    "v_mfma_f32_16x16x32_bf16 v[20:23], v[144:147], a[208:211], v[20:23]\n"
    "ds_read_b128 v[88:91], v0 offset:2048\n"
    "s_mov_b32 s56, 1\n"
    "s_cmp_lg_u32 s94, 0\n"
    "v_mfma_f32_16x16x32_bf16 v[20:23], v[148:151], a[212:215], v[20:23]\n"
    "ds_read_b128 v[92:95], v0 offset:3072\n"
    "v_mfma_f32_16x16x32_bf16 v[184:187], v[36:39], v[204:207], 0\n"
    "s_cbranch_scc1 .Lago_%=\n"
    "ds_read_b32 v79, v11 offset:44\n"
    "s_nop 2\n"
    "v_mul_f32 v20, v20, v200\n"
    "v_mul_f32 v21, v21, v201\n"
    "v_mul_f32 v22, v22, v202\n"
    "v_mul_f32 v23, v23, v203\n"
    "v_cvt_pk_bf16_f32 v34, v20, v21\n"
    "v_cvt_pk_bf16_f32 v35, v22, v23\n"
    "v_rcp_f32 v40, v186\n"
    "s_nop 0\n"
    "v_mfma_f32_16x16x32_bf16 v[180:183], v[32:35], v[204:207], 0\n"
    "ds_write2st64_b64 v73, v[34:35], v[38:39] offset0:8 offset1:16\n"
    "v_mul_f32 v184, v184, v40\n"
    "v_mul_f32 v185, v185, v40\n"
    "v_cvt_pk_bf16_f32 v184, v184, v185\n"
    "s_nop 4\n"
    "v_mul_f32 v180, v180, v40\n"
    "v_mul_f32 v181, v181, v40\n"
    "v_mul_f32 v182, v182, v40\n"
    "v_mul_f32 v183, v183, v40\n"
    "v_cvt_pk_bf16_f32 v204, v180, v181\n"
    "v_cvt_pk_bf16_f32 v206, v180, v181\n"
    "v_cvt_pk_bf16_f32 v205, v182, v183\n"
    "v_cvt_pk_bf16_f32 v207, v182, v183\n"
    "v_permlane16_swap_b32 v204, v206\n"
    "s_nop 0\n"
    "v_permlane16_swap_b32 v205, v207\n"
    "v_mov_b32_dpp v204, v184 quad_perm:[0,0,0,0] row_mask:0x2 bank_mask:0xf\n"
    "v_mov_b32_dpp v206, v184 quad_perm:[2,2,2,2] row_mask:0x2 bank_mask:0xf\n"
    "v_mov_b32_dpp v205, v184 quad_perm:[1,1,1,1] row_mask:0x2 bank_mask:0xf\n"
    "v_mov_b32_dpp v207, v184 quad_perm:[3,3,3,3] row_mask:0x2 bank_mask:0xf\n"
    "buffer_store_dwordx4 v[204:207], v1, s[40:43], s62 offen sc1\n"
    "s_waitcnt vmcnt(7) lgkmcnt(0)\n"
    "v_cmp_eq_u32 vcc, 0, v55\n"
    "s_cmp_eq_u64 vcc, 0\n"
    "s_cbranch_scc1 .Lald_%=\n"
    "global_load_dword v55, v76, %1 sc1\n"
    "s_waitcnt vmcnt(0)\n"
    "v_mov_b32 v54, 1\n"
    "s_mov_b64 exec, s[56:57]\n"
    "ds_add_rtn_u32 v54, v11, v54 offset:48\n"
    "s_mov_b64 exec, -1\n"
    "s_waitcnt lgkmcnt(0)\n"
    "v_readfirstlane_b32 s50, v54\n"
    "s_cmp_eq_u32 s50, 3\n"
    "s_cselect_b32 s46, 0x20000, 0\n"
    "buffer_store_dword v54, v79, s[44:47], 0 offen sc1\n"
    "s_mov_b32 s56, 0\n"
    "v_cmp_eq_u32 vcc, 0, v55\n"
    "s_cmp_eq_u64 vcc, 0\n"
    "s_cbranch_scc1 .Lald_%=\n"
    ".Lachk_%=:\n"
    "v_readfirstlane_b32 s50, v76\n"
    "s_cmp_lt_u32 s50, 200\n"
    "s_cbranch_scc1 .Laend_%=\n"
    ".Lawait_%=:\n"
    "global_load_dword v55, v76, %1 sc1\n"
    "s_mov_b32 s52, 0\n"
    ".Lap11_%=:\n"
    "s_waitcnt vmcnt(0)\n"
    "v_cmp_eq_u32 vcc, 0, v55\n"
    "s_cmp_eq_u64 vcc, 0\n"
    "s_cbranch_scc1 .Lap11_x%=\n"
    "s_add_u32 s52, s52, 1\n"
    "s_sleep 2\n"
    "global_load_dword v55, v76, %1 sc1\n"
    "s_cmp_lt_u32 s52, s53\n"
    "s_cbranch_scc1 .Lap11_%=\n"
    "s_mov_b32 s53, 0\n"
    "v_mov_b32 v78, 0\n"
    "v_mov_b32 v79, 1\n"
    "global_store_dword v78, v79, %1 sc1\n"
    "s_waitcnt vmcnt(0)\n"
    ".Lap11_x%=:\n"
    ".Lald_%=:\n"
    "s_mov_b32 s94, s42\n"
    "s_mov_b32 s90, s96\n"
    "s_mov_b32 m0, s58\n"
    "s_nop 0\n"
    "buffer_load_dwordx4 v2, s[92:95], s49 offen sc1 lds\n"
    "buffer_load_dwordx4 v2, s[92:95], s49 offen offset:1024 sc1 lds\n"
    "buffer_load_dwordx4 v2, s[92:95], s49 offen offset:2048 sc1 lds\n"
    "buffer_load_dwordx4 v2, s[92:95], s49 offen offset:3072 sc1 lds\n"
    "s_bitset1_b32 m0, 12\n"
    "s_nop 0\n"
    "buffer_load_dwordx4 v3, s[88:91], s49 offen sc1 lds\n"
    "s_mov_b32 m0, s59\n"
    "s_mov_b32 s55, s49\n"
    "s_bitset1_b32 s55, 15\n"
    "s_mov_b64 exec, s[60:61]\n"
    "buffer_load_dwordx4 v3, s[92:95], s55 offen offset:2048 sc1 lds\n"
    "s_mov_b64 exec, -1\n"
    "s_waitcnt vmcnt(6)\n"
    "v_mov_b32 v54, 1\n"
    "s_mov_b64 exec, s[56:57]\n"
    "ds_add_rtn_u32 v54, v11, v54 offset:48\n"
    "s_mov_b64 exec, -1\n"
    "s_waitcnt lgkmcnt(0)\n"
    "v_readfirstlane_b32 s50, v54\n"
    "s_cmp_eq_u32 s50, 3\n"
    "s_cselect_b32 s46, 0x20000, 0\n"
    "buffer_store_dword v54, v79, s[44:47], 0 offen sc1\n"
    "s_mov_b32 s56, 0\n"
    "s_waitcnt vmcnt(3)\n"
    "ds_read_b128 v[80:83], v0\n"
    "ds_read_b128 v[84:87], v0 offset:1024\n"
    "ds_read_b128 v[88:91], v0 offset:2048\n"
    "ds_read_b128 v[92:95], v0 offset:3072\n"
    "s_waitcnt vmcnt(1)\n"
    "v_add_u32 v11, 32, v11\n"
    "s_waitcnt lgkmcnt(3)\n"
    "v_mfma_f32_16x16x32_bf16 v[24:27], a[216:219], v[80:83], 0\n"
    "ds_read_b128 v[76:79], v11\n"
    "s_waitcnt lgkmcnt(3)\n"
    "v_mfma_f32_16x16x32_bf16 v[24:27], a[220:223], v[84:87], v[24:27]\n"
    "ds_read_b32 v74, v11 offset:24\n"
    "s_waitcnt lgkmcnt(3)\n"
    "v_mfma_f32_16x16x32_bf16 v[24:27], a[224:227], v[88:91], v[24:27]\n"
    "s_waitcnt lgkmcnt(2)\n"
    "v_mfma_f32_16x16x32_bf16 v[24:27], a[228:231], v[92:95], v[24:27]\n"
    "s_xor_b32 s58, s58, 0x8000\n"
    "s_xor_b32 s59, s59, 0x8000\n"
    "s_waitcnt lgkmcnt(1)\n"
    "v_add_u32 v76, v76, v72\n"
    "v_mfma_f32_16x16x32_bf16 v[12:15], a[0:3], v[80:83], 0\n"
    "v_readfirstlane_b32 s49, v77\n"
    "v_mfma_f32_16x16x32_bf16 v[16:19], a[72:75], v[80:83], 0\n"
    "s_barrier\n"
    "ds_read_b128 v[112:115], v50\n"
    "v_mfma_f32_16x16x32_bf16 v[12:15], a[4:7], v[84:87], v[12:15]\n"
    "ds_read2st64_b32 v[160:161], v7 offset1:1\n"
    "v_mfma_f32_16x16x32_bf16 v[16:19], a[76:79], v[84:87], v[16:19]\n"
    "ds_read2st64_b32 v[162:163], v7 offset0:2 offset1:3\n"
    "v_mfma_f32_16x16x32_bf16 v[12:15], a[8:11], v[88:91], v[12:15]\n"
    "ds_read2st64_b32 v[164:165], v7 offset0:4 offset1:5\n"
    "v_mfma_f32_16x16x32_bf16 v[16:19], a[80:83], v[88:91], v[16:19]\n"
    "ds_read2st64_b32 v[166:167], v7 offset0:6 offset1:7\n"
    "v_mfma_f32_16x16x32_bf16 v[12:15], a[12:15], v[92:95], v[12:15]\n"
    "ds_read_b32 v168, v7 offset:2048\n"
    "v_mfma_f32_16x16x32_bf16 v[16:19], a[84:87], v[92:95], v[16:19]\n"
    "ds_read_b128 v[116:119], v50 offset:1024\n"
    "s_waitcnt lgkmcnt(6)\n"
    "v_mfma_f32_16x16x32_bf16 v[24:27], a[232:235], v[112:115], v[24:27]\n"
    "v_add_u32 v73, v74, v3\n"
    "s_waitcnt lgkmcnt(5)\n"
    "v_add_f32 v160, v160, v161\n"
    "v_mfma_f32_16x16x32_bf16 v[12:15], a[32:35], v[112:115], v[12:15]\n"
    "ds_read_b128 v[120:123], v50 offset:2048\n"
    "s_waitcnt lgkmcnt(5)\n"
    "v_add_f32 v162, v162, v163\n"
    "s_waitcnt lgkmcnt(4)\n"
    "v_add_f32 v164, v164, v165\n"
    "v_mfma_f32_16x16x32_bf16 v[16:19], a[104:107], v[112:115], v[16:19]\n"
    "ds_read_b128 v[124:127], v50 offset:3072\n"
    "s_waitcnt lgkmcnt(4)\n"
    "v_add_f32 v166, v166, v167\n"
    "v_add_f32 v160, v160, v162\n"
    "s_waitcnt lgkmcnt(0)\n"
    "s_branch .Latop_%=\n"
    ".Lak10_%=:\n"
    "s_waitcnt vmcnt(6)\n"
    "v_cmp_eq_u32 vcc, 0, v55\n"
    "s_cmp_eq_u64 vcc, 0\n"
    "s_cselect_b32 s46, 0, 0x20000\n"
    "s_cselect_b32 s94, s42, 0\n"
    "s_cselect_b32 s90, s96, 0\n"
    "buffer_load_dword v55, v76, s[44:47], 0 offen sc1\n"
    "s_mov_b32 m0, s58\n"
    "s_nop 0\n"
    "buffer_load_dwordx4 v2, s[92:95], s49 offen sc1 lds\n"
    "buffer_load_dwordx4 v2, s[92:95], s49 offen offset:1024 sc1 lds\n"
    "buffer_load_dwordx4 v2, s[92:95], s49 offen offset:2048 sc1 lds\n"
    "buffer_load_dwordx4 v2, s[92:95], s49 offen offset:3072 sc1 lds\n"
    "s_bitset1_b32 m0, 12\n"
    "s_nop 0\n"
    "buffer_load_dwordx4 v3, s[88:91], s49 offen sc1 lds\n"
    "s_mov_b32 m0, s59\n"
    "s_mov_b32 s55, s49\n"
    "s_bitset1_b32 s55, 15\n"
    "s_mov_b64 exec, s[60:61]\n"
    "buffer_load_dwordx4 v3, s[92:95], s55 offen offset:2048 sc1 lds\n"
    "s_mov_b64 exec, -1\n"
    "s_branch .Lak10_x%=\n"
    ".Lapol_%=:\n"
    "v_mov_b32 v76, 64\n"
    "global_load_dword v55, v76, %1 sc1\n"
    "s_mov_b32 s52, 0\n"
    ".Lap12_%=:\n"
    "s_waitcnt vmcnt(0)\n"
    "v_cmp_eq_u32 vcc, 0, v55\n"
    "s_cmp_eq_u64 vcc, 0\n"
    "s_cbranch_scc1 .Lap12_x%=\n"
    "s_add_u32 s52, s52, 1\n"
    "s_sleep 2\n"
    "global_load_dword v55, v76, %1 sc1\n"
    "s_cmp_lt_u32 s52, s53\n"
    "s_cbranch_scc1 .Lap12_%=\n"
    "s_mov_b32 s53, 0\n"
    "v_mov_b32 v78, 0\n"
    "v_mov_b32 v79, 1\n"
    "global_store_dword v78, v79, %1 sc1\n"
    "s_waitcnt vmcnt(0)\n"
    ".Lap12_x%=:\n"
    "v_readfirstlane_b32 s50, v55\n"
    "v_mbcnt_lo_u32_b32 v76, -1, 0\n"
    "v_mbcnt_hi_u32_b32 v76, -1, v76\n"
    "s_lshl_b32 s51, %2, 6\n"
    "v_add_u32 v76, s51, v76\n"
    "v_min_u32 v76, 129, v76\n"
    "v_lshlrev_b32 v76, 5, v76\n"
    "v_add_u32 v76, 0x26c00, v76\n"
    "ds_read2_b32 v[78:79], v76 offset0:1 offset1:2\n"
    "s_and_b32 s50, s50, 1\n"
    "s_waitcnt lgkmcnt(0)\n"
    "v_and_b32 v77, s50, v78\n"
    "v_lshlrev_b32 v77, 13, v77\n"
    "v_and_b32 v78, -2, v78\n"
    "v_xor_b32 v78, v77, v78\n"
    "v_and_b32 v77, s50, v79\n"
    "v_lshlrev_b32 v77, 13, v77\n"
    "v_and_b32 v79, -2, v79\n"
    "v_xor_b32 v79, v77, v79\n"
    "ds_write2_b32 v76, v78, v79 offset0:1 offset1:2\n"
    "s_waitcnt lgkmcnt(0)\n"
    "s_barrier\n"
    "ds_read_b32 v77, v11 offset:4\n"
    "v_add_u32 v76, %4, v72\n"
    "s_waitcnt lgkmcnt(0)\n"
    "v_readfirstlane_b32 s49, v77\n"
    "s_branch .Lawait_%=\n"
    ".Laend_%=:\n"
    "s_cmp_eq_u32 s50, 192\n"
    "s_cbranch_scc0 .Lapol_%=\n"
    "s_waitcnt vmcnt(0) lgkmcnt(0)\n"
    "s_endpgm\n"
    ".p2align 6\n"
    ".Lrole4_%=:\n"
    "s_mov_b64 s[40:41], %0\n"
    "s_and_b32 s41, s41, 0xffff\n"
    "s_mov_b32 s42, 0xc000000\n"
    "s_mov_b32 s43, 0x20000\n"
    "s_mov_b64 s[92:93], %0\n"
    "s_and_b32 s93, s93, 0xffff\n"
    "s_mov_b32 s94, 0xc000000\n"
    "s_mov_b32 s95, 0x20000\n"
    "s_mov_b32 s53, 131072\n"
    "s_mov_b64 s[56:57], 0\n"
    "v_mbcnt_lo_u32_b32 v20, -1, 0\n"
    "v_mbcnt_hi_u32_b32 v20, -1, v20\n"
    "s_lshl_b32 s50, %2, 8\n"
    "s_add_u32 s50, s50, 147456\n"
    "v_lshl_add_u32 v20, v20, 2, s50\n"
    "ds_read_b32 v0, v20\n"
    "ds_read_b32 v1, v20 offset:1024\n"
    "ds_read_b32 v2, v20 offset:2048\n"
    "ds_read_b32 v3, v20 offset:3072\n"
    "ds_read_b32 v4, v20 offset:4096\n"
    "ds_read_b32 v5, v20 offset:5120\n"
    "ds_read_b32 v8, v20 offset:6144\n"
    "ds_read_b32 v9, v20 offset:7168\n"
    "ds_read_b32 v10, v20 offset:8192\n"
    "s_waitcnt lgkmcnt(0)\n"
    "s_barrier\n"
    "s_lshl_b32 s54, %2, 10\n"
    "s_lshl_b32 s58, %2, 13\n"
    "s_add_u32 s58, s58, 0\n"
    "s_add_u32 s88, s40, 32768\n"
    "s_mov_b32 s89, s41\n"
    "s_cmp_lt_u32 %2, 2\n"
    "s_cselect_b32 s96, s42, 0\n"
    "s_mov_b32 s91, s43\n"
    "s_mul_i32 s59, %2, 576\n"
    "s_add_u32 s59, s59, 3072\n"
    "s_bfm_b64 s[60:61], 36, 0\n"
    "s_mov_b32 s86, 0x3f803f80\n"
    "s_mov_b64 s[44:45], %8\n"
    "s_and_b32 s45, s45, 0xffff\n"
    "s_mov_b32 s46, 0xa2000\n"
    "s_mov_b32 s47, 0x20000\n"
    "s_add_u32 m0, s54, 0\n"
    "s_add_u32 s55, %6, 0\n"
    "buffer_load_dwordx4 v8, s[44:47], s55 offen lds\n"
    "s_add_u32 m0, s54, 36864\n"
    "s_add_u32 s55, %6, 64\n"
    "buffer_load_dwordx4 v8, s[44:47], s55 offen lds\n"
    "s_add_u32 m0, s54, 73728\n"
    "s_add_u32 s55, %6, 128\n"
    "buffer_load_dwordx4 v8, s[44:47], s55 offen lds\n"
    "s_add_u32 m0, s54, 4096\n"
    "s_add_u32 s55, %6, 73728\n"
    "buffer_load_dwordx4 v8, s[44:47], s55 offen lds\n"
    "s_add_u32 m0, s54, 40960\n"
    "s_add_u32 s55, %6, 73792\n"
    "buffer_load_dwordx4 v8, s[44:47], s55 offen lds\n"
    "s_add_u32 m0, s54, 77824\n"
    "s_add_u32 s55, %6, 73856\n"
    "buffer_load_dwordx4 v8, s[44:47], s55 offen lds\n"
    "s_add_u32 m0, s54, 8192\n"
    "s_add_u32 s55, %6, 147456\n"
    "buffer_load_dwordx4 v8, s[44:47], s55 offen lds\n"
    "s_add_u32 m0, s54, 45056\n"
    "s_add_u32 s55, %6, 147520\n"
    "buffer_load_dwordx4 v8, s[44:47], s55 offen lds\n"
    "s_add_u32 m0, s54, 81920\n"
    "s_add_u32 s55, %6, 147584\n"
    "buffer_load_dwordx4 v8, s[44:47], s55 offen lds\n"
    "s_add_u32 m0, s54, 12288\n"
    "s_add_u32 s55, %6, 221184\n"
    "buffer_load_dwordx4 v8, s[44:47], s55 offen lds\n"
    "s_add_u32 m0, s54, 49152\n"
    "s_add_u32 s55, %6, 221248\n"
    "buffer_load_dwordx4 v8, s[44:47], s55 offen lds\n"
    "s_add_u32 m0, s54, 86016\n"
    "s_add_u32 s55, %6, 221312\n"
    "buffer_load_dwordx4 v8, s[44:47], s55 offen lds\n"
    "s_add_u32 m0, s54, 16384\n"
    "s_add_u32 s55, %6, 294912\n"
    "buffer_load_dwordx4 v8, s[44:47], s55 offen lds\n"
    "s_add_u32 m0, s54, 53248\n"
    "s_add_u32 s55, %6, 294976\n"
    "buffer_load_dwordx4 v8, s[44:47], s55 offen lds\n"
    "s_add_u32 m0, s54, 90112\n"
    "s_add_u32 s55, %6, 295040\n"
    "buffer_load_dwordx4 v8, s[44:47], s55 offen lds\n"
    "s_add_u32 m0, s54, 20480\n"
    "s_add_u32 s55, %6, 368640\n"
    "buffer_load_dwordx4 v8, s[44:47], s55 offen lds\n"
    "s_add_u32 m0, s54, 57344\n"
    "s_add_u32 s55, %6, 368704\n"
    "buffer_load_dwordx4 v8, s[44:47], s55 offen lds\n"
    "s_add_u32 m0, s54, 94208\n"
    "s_add_u32 s55, %6, 368768\n"
    "buffer_load_dwordx4 v8, s[44:47], s55 offen lds\n"
    "s_add_u32 m0, s54, 24576\n"
    "s_add_u32 s55, %6, 442368\n"
    "buffer_load_dwordx4 v8, s[44:47], s55 offen lds\n"
    "s_add_u32 m0, s54, 61440\n"
    "s_add_u32 s55, %6, 442432\n"
    "buffer_load_dwordx4 v8, s[44:47], s55 offen lds\n"
    "s_add_u32 m0, s54, 98304\n"
    "s_add_u32 s55, %6, 442496\n"
    "buffer_load_dwordx4 v8, s[44:47], s55 offen lds\n"
    "s_add_u32 m0, s54, 28672\n"
    "s_add_u32 s55, %6, 516096\n"
    "buffer_load_dwordx4 v8, s[44:47], s55 offen lds\n"
    "s_add_u32 m0, s54, 65536\n"
    "s_add_u32 s55, %6, 516160\n"
    "buffer_load_dwordx4 v8, s[44:47], s55 offen lds\n"
    "s_add_u32 m0, s54, 102400\n"
    "s_add_u32 s55, %6, 516224\n"
    "buffer_load_dwordx4 v8, s[44:47], s55 offen lds\n"
    "s_add_u32 m0, s54, 32768\n"
    "s_add_u32 s55, %6, 589824\n"
    "buffer_load_dwordx4 v8, s[44:47], s55 offen lds\n"
    "s_add_u32 m0, s54, 69632\n"
    "s_add_u32 s55, %6, 589888\n"
    "buffer_load_dwordx4 v8, s[44:47], s55 offen lds\n"
    "s_add_u32 m0, s54, 106496\n"
    "s_add_u32 s55, %6, 589952\n"
    "buffer_load_dwordx4 v8, s[44:47], s55 offen lds\n"
    "v_mov_b32 v7, 0x20400\n"
    "global_load_dwordx2 v[20:21], v7, %1 sc1\n"
    "v_mbcnt_lo_u32_b32 v6, -1, 0\n"
    "v_min_u32 v6, 7, v6\n"
    "v_lshlrev_b32 v6, 7, v6\n"
    "v_add_u32 v6, 0x20000, v6\n"
    "global_load_dwordx2 v[22:23], v6, %1 sc1\n"
    "s_waitcnt vmcnt(2) lgkmcnt(0)\n"
    "s_barrier\n"
    "ds_read_b64_tr_b16 a[0:1], v9\n"
    "ds_read_b64_tr_b16 a[2:3], v9 offset:256\n"
    "ds_read_b64_tr_b16 a[4:5], v9 offset:8\n"
    "ds_read_b64_tr_b16 a[6:7], v9 offset:264\n"
    "ds_read_b64_tr_b16 a[8:9], v9 offset:2304\n"
    "ds_read_b64_tr_b16 a[10:11], v9 offset:2560\n"
    "ds_read_b64_tr_b16 a[12:13], v9 offset:2312\n"
    "ds_read_b64_tr_b16 a[14:15], v9 offset:2568\n"
    "ds_read_b64_tr_b16 a[16:17], v9 offset:4608\n"
    "ds_read_b64_tr_b16 a[18:19], v9 offset:4864\n"
    "ds_read_b64_tr_b16 a[20:21], v9 offset:4616\n"
    "ds_read_b64_tr_b16 a[22:23], v9 offset:4872\n"
    "ds_read_b64_tr_b16 a[24:25], v9 offset:6912\n"
    "ds_read_b64_tr_b16 a[26:27], v9 offset:7168\n"
    "ds_read_b64_tr_b16 a[28:29], v9 offset:6920\n"
    "ds_read_b64_tr_b16 a[30:31], v9 offset:7176\n"
    "ds_read_b64_tr_b16 a[32:33], v9 offset:9216\n"
    "ds_read_b64_tr_b16 a[34:35], v9 offset:9472\n"
    "ds_read_b64_tr_b16 a[36:37], v9 offset:9224\n"
    "ds_read_b64_tr_b16 a[38:39], v9 offset:9480\n"
    "ds_read_b64_tr_b16 a[40:41], v9 offset:11520\n"
    "ds_read_b64_tr_b16 a[42:43], v9 offset:11776\n"
    "ds_read_b64_tr_b16 a[44:45], v9 offset:11528\n"
    "ds_read_b64_tr_b16 a[46:47], v9 offset:11784\n"
    "ds_read_b64_tr_b16 a[48:49], v9 offset:13824\n"
    "ds_read_b64_tr_b16 a[50:51], v9 offset:14080\n"
    "ds_read_b64_tr_b16 a[52:53], v9 offset:13832\n"
    "ds_read_b64_tr_b16 a[54:55], v9 offset:14088\n"
    "ds_read_b64_tr_b16 a[56:57], v9 offset:16128\n"
    "ds_read_b64_tr_b16 a[58:59], v9 offset:16384\n"
    "ds_read_b64_tr_b16 a[60:61], v9 offset:16136\n"
    "ds_read_b64_tr_b16 a[62:63], v9 offset:16392\n"
    "ds_read_b64_tr_b16 a[64:65], v9 offset:18432\n"
    "ds_read_b64_tr_b16 a[66:67], v9 offset:18688\n"
    "ds_read_b64_tr_b16 a[68:69], v9 offset:18440\n"
    "ds_read_b64_tr_b16 a[70:71], v9 offset:18696\n"
    "ds_read_b64_tr_b16 a[72:73], v9 offset:20736\n"
    "ds_read_b64_tr_b16 a[74:75], v9 offset:20992\n"
    "ds_read_b64_tr_b16 a[76:77], v9 offset:20744\n"
    "ds_read_b64_tr_b16 a[78:79], v9 offset:21000\n"
    "ds_read_b64_tr_b16 a[80:81], v9 offset:23040\n"
    "ds_read_b64_tr_b16 a[82:83], v9 offset:23296\n"
    "ds_read_b64_tr_b16 a[84:85], v9 offset:23048\n"
    "ds_read_b64_tr_b16 a[86:87], v9 offset:23304\n"
    "ds_read_b64_tr_b16 a[88:89], v9 offset:25344\n"
    "ds_read_b64_tr_b16 a[90:91], v9 offset:25600\n"
    "ds_read_b64_tr_b16 a[92:93], v9 offset:25352\n"
    "ds_read_b64_tr_b16 a[94:95], v9 offset:25608\n"
    "ds_read_b64_tr_b16 a[96:97], v9 offset:27648\n"
    "ds_read_b64_tr_b16 a[98:99], v9 offset:27904\n"
    "ds_read_b64_tr_b16 a[100:101], v9 offset:27656\n"
    "ds_read_b64_tr_b16 a[102:103], v9 offset:27912\n"
    "ds_read_b64_tr_b16 a[104:105], v9 offset:29952\n"
    "ds_read_b64_tr_b16 a[106:107], v9 offset:30208\n"
    "ds_read_b64_tr_b16 a[108:109], v9 offset:29960\n"
    "ds_read_b64_tr_b16 a[110:111], v9 offset:30216\n"
    "ds_read_b64_tr_b16 a[112:113], v9 offset:32256\n"
    "ds_read_b64_tr_b16 a[114:115], v9 offset:32512\n"
    "ds_read_b64_tr_b16 a[116:117], v9 offset:32264\n"
    "ds_read_b64_tr_b16 a[118:119], v9 offset:32520\n"
    "ds_read_b64_tr_b16 a[120:121], v9 offset:34560\n"
    "ds_read_b64_tr_b16 a[122:123], v9 offset:34816\n"
    "ds_read_b64_tr_b16 a[124:125], v9 offset:34568\n"
    "ds_read_b64_tr_b16 a[126:127], v9 offset:34824\n"
    "ds_read_b64_tr_b16 a[128:129], v10\n"
    "ds_read_b64_tr_b16 a[130:131], v10 offset:2304\n"
    "ds_read_b64_tr_b16 a[132:133], v10 offset:8\n"
    "ds_read_b64_tr_b16 a[134:135], v10 offset:2312\n"
    "ds_read_b64_tr_b16 a[136:137], v10 offset:18432\n"
    "ds_read_b64_tr_b16 a[138:139], v10 offset:20736\n"
    "ds_read_b64_tr_b16 a[140:141], v10 offset:18440\n"
    "ds_read_b64_tr_b16 a[142:143], v10 offset:20744\n"
    "s_waitcnt lgkmcnt(0)\n"
    "s_barrier\n"
    "s_mov_b64 s[44:45], %1\n"
    "s_and_b32 s45, s45, 0xffff\n"
    "s_mov_b32 s46, 0x0\n"
    "s_mov_b32 s47, 0x20000\n"
    "s_waitcnt vmcnt(0)\n"
    "v_add_u32 v20, 1, v20\n"
    "v_add_u32 v21, 1, v21\n"
    "s_mov_b32 s52, 1048576\n"
    ".Lbgo_a%=:\n"
    "v_cmp_ne_u64 vcc, v[20:21], v[22:23]\n"
    "s_cbranch_vccz .Lbgo_x%=\n"
    "global_load_dwordx2 v[22:23], v6, %1 sc1\n"
    "s_sub_u32 s52, s52, 1\n"
    "s_waitcnt vmcnt(0)\n"
    "s_cbranch_scc0 .Lbgo_a%=\n"
    ".Lbgo_x%=:\n"
    "v_mbcnt_lo_u32_b32 v20, -1, 0\n"
    "v_mbcnt_hi_u32_b32 v20, -1, v20\n"
    "v_min_u32 v6, 7, v20\n"
    "v_lshlrev_b32 v6, 7, v6\n"
    "v_mov_b32 v7, 0x26c00\n"
    "ds_read_b128 v[20:23], v7\n"
    "s_waitcnt lgkmcnt(0)\n"
    "v_readfirstlane_b32 s49, v21\n"
    "v_add_u32 v20, v20, v6\n"
    "v_subrev_u32 v7, 32, v7\n"
    "s_add_u32 s49, s49, 1966080\n"
    "s_branch .Lbld_%=\n"
    ".Lbgo_%=:\n"
    "v_add_u32 v7, 32, v7\n"
    "ds_read_b128 v[20:23], v7\n"
    "v_xor_b32 v0, 0x8000, v0\n"
    "v_xor_b32 v4, 0x8000, v4\n"
    "v_add_u32 v104, 8192, v4\n"
    "s_waitcnt lgkmcnt(0)\n"
    "v_add_u32 v20, v20, v6\n"
    "v_readfirstlane_b32 s62, v22\n"
    "v_readfirstlane_b32 s49, v21\n"
    "s_xor_b32 s58, s58, 0x8000\n"
    "global_load_dword v105, v20, %1 sc1\n"
    "s_waitcnt vmcnt(3)\n"
    ".Lbtop_%=:\n"
    "s_barrier\n"
    "ds_read_b128 v[24:27], v0\n"
    "ds_read_b128 v[28:31], v0 offset:1024\n"
    "ds_read_b128 v[32:35], v0 offset:2048\n"
    "ds_read_b128 v[36:39], v0 offset:3072\n"
    "ds_read_b128 v[40:43], v0 offset:8192\n"
    "ds_read_b128 v[44:47], v0 offset:9216\n"
    "v_lshlrev_b32 v8, 16, v100\n"
    "v_and_b32 v9, 0xffff0000, v100\n"
    "v_lshlrev_b32 v10, 16, v101\n"
    "v_and_b32 v11, 0xffff0000, v101\n"
    "v_lshlrev_b32 v12, 16, v102\n"
    "v_and_b32 v13, 0xffff0000, v102\n"
    "v_lshlrev_b32 v14, 16, v103\n"
    "v_and_b32 v15, 0xffff0000, v103\n"
    "s_waitcnt lgkmcnt(5)\n"
    "v_mfma_f32_16x16x32_bf16 v[8:11], a[0:3], v[24:27], v[8:11]\n"
    "ds_read_b128 v[48:51], v0 offset:10240\n"
    "v_mfma_f32_16x16x32_bf16 v[12:15], a[4:7], v[24:27], v[12:15]\n"
    "ds_read_b128 v[52:55], v0 offset:11264\n"
    "s_waitcnt lgkmcnt(6)\n"
    "v_mfma_f32_16x16x32_bf16 v[8:11], a[8:11], v[28:31], v[8:11]\n"
    "ds_read_b128 v[56:59], v0 offset:16384\n"
    "v_mfma_f32_16x16x32_bf16 v[12:15], a[12:15], v[28:31], v[12:15]\n"
    "ds_read_b128 v[60:63], v0 offset:17408\n"
    "s_waitcnt lgkmcnt(7)\n"
    "v_mfma_f32_16x16x32_bf16 v[8:11], a[16:19], v[32:35], v[8:11]\n"
    "ds_read_b128 v[64:67], v0 offset:18432\n"
    "v_mfma_f32_16x16x32_bf16 v[12:15], a[20:23], v[32:35], v[12:15]\n"
    "ds_read_b128 v[68:71], v0 offset:19456\n"
    "s_waitcnt lgkmcnt(8)\n"
    "v_mfma_f32_16x16x32_bf16 v[8:11], a[24:27], v[36:39], v[8:11]\n"
    "ds_read_b128 v[72:75], v0 offset:24576\n"
    "v_mfma_f32_16x16x32_bf16 v[12:15], a[28:31], v[36:39], v[12:15]\n"
    "ds_read_b128 v[76:79], v0 offset:25600\n"
    "s_waitcnt lgkmcnt(9)\n"
    "v_mfma_f32_16x16x32_bf16 v[8:11], a[32:35], v[40:43], v[8:11]\n"
    "ds_read_b128 v[80:83], v0 offset:26624\n"
    "v_mfma_f32_16x16x32_bf16 v[12:15], a[36:39], v[40:43], v[12:15]\n"
    "ds_read_b128 v[84:87], v0 offset:27648\n"
    "s_waitcnt lgkmcnt(10)\n"
    "v_mfma_f32_16x16x32_bf16 v[8:11], a[40:43], v[44:47], v[8:11]\n"
    "ds_read2_b32 v[88:89], v4 offset1:16\n"
    "v_mov_b32 v22, 1\n"
    "v_mfma_f32_16x16x32_bf16 v[12:15], a[44:47], v[44:47], v[12:15]\n"
    "ds_read2_b32 v[90:91], v4 offset0:32 offset1:48\n"
    "s_waitcnt vmcnt(1)\n"
    "s_waitcnt lgkmcnt(11)\n"
    "v_mfma_f32_16x16x32_bf16 v[8:11], a[48:51], v[48:51], v[8:11]\n"
    "s_mov_b64 exec, s[56:57]\n"
    "ds_add_rtn_u32 v22, v7, v22 offset:16\n"
    "s_mov_b64 exec, -1\n"
    "v_mfma_f32_16x16x32_bf16 v[12:15], a[52:55], v[48:51], v[12:15]\n"
    "s_waitcnt vmcnt(0)\n"
    "v_cmp_eq_u32 vcc, 0, v105\n"
    "s_waitcnt lgkmcnt(10)\n"
    "v_mfma_f32_16x16x32_bf16 v[8:11], a[56:59], v[52:55], v[8:11]\n"
    "s_cmp_eq_u64 vcc, 0\n"
    "s_cselect_b32 s46, 0, 0x20000\n"
    "s_cselect_b32 s94, s42, 0\n"
    "v_mfma_f32_16x16x32_bf16 v[12:15], a[60:63], v[52:55], v[12:15]\n"
    "buffer_load_dword v105, v20, s[44:47], 0 offen sc1\n"
    "s_cselect_b32 s90, s96, 0\n"
    "s_waitcnt lgkmcnt(9)\n"
    "v_mfma_f32_16x16x32_bf16 v[8:11], a[64:67], v[56:59], v[8:11]\n"
    "ds_read2_b32 v[92:93], v104 offset1:16\n"
    "v_mfma_f32_16x16x32_bf16 v[12:15], a[68:71], v[56:59], v[12:15]\n"
    "s_waitcnt lgkmcnt(1)\n"
    "v_readfirstlane_b32 s50, v22\n"
    "v_mfma_f32_16x16x32_bf16 v[8:11], a[72:75], v[60:63], v[8:11]\n"
    "s_cmp_eq_u32 s50, 3\n"
    "s_cselect_b32 s46, 0x20000, 0\n"
    "v_mfma_f32_16x16x32_bf16 v[12:15], a[76:79], v[60:63], v[12:15]\n"
    "buffer_store_dword v22, v23, s[44:47], 0 offen sc1\n"
    "s_mov_b32 m0, s58\n"
    "v_mfma_f32_16x16x32_bf16 v[8:11], a[80:83], v[64:67], v[8:11]\n"
    "buffer_load_dwordx4 v2, s[92:95], s49 offen sc1 lds\n"
    "v_mfma_f32_16x16x32_bf16 v[12:15], a[84:87], v[64:67], v[12:15]\n"
    "ds_read2_b32 v[94:95], v104 offset0:32 offset1:48\n"
    "v_mfma_f32_16x16x32_bf16 v[8:11], a[88:91], v[68:71], v[8:11]\n"
    "v_mfma_f32_16x16x32_bf16 v[12:15], a[92:95], v[68:71], v[12:15]\n"
    "buffer_load_dwordx4 v2, s[92:95], s49 offen offset:1024 sc1 lds\n"
    "v_mfma_f32_16x16x32_bf16 v[8:11], a[96:99], v[72:75], v[8:11]\n"
    "v_mfma_f32_16x16x32_bf16 v[12:15], a[100:103], v[72:75], v[12:15]\n"
    "v_mfma_f32_16x16x32_bf16 v[8:11], a[104:107], v[76:79], v[8:11]\n"
    "buffer_load_dwordx4 v2, s[92:95], s49 offen offset:2048 sc1 lds\n"
    "v_mfma_f32_16x16x32_bf16 v[12:15], a[108:111], v[76:79], v[12:15]\n"
    "v_mfma_f32_16x16x32_bf16 v[8:11], a[112:115], v[80:83], v[8:11]\n"
    "v_mfma_f32_16x16x32_bf16 v[12:15], a[116:119], v[80:83], v[12:15]\n"
    "buffer_load_dwordx4 v2, s[92:95], s49 offen offset:3072 sc1 lds\n"
    "v_mfma_f32_16x16x32_bf16 v[8:11], a[120:123], v[84:87], v[8:11]\n"
    "v_mfma_f32_16x16x32_bf16 v[12:15], a[124:127], v[84:87], v[12:15]\n"
    "s_bitset1_b32 m0, 12\n"
    "v_mfma_f32_16x16x32_bf16 v[8:11], a[128:131], v[88:91], v[8:11]\n"
    "buffer_load_dwordx4 v1, s[88:91], s49 offen sc1 lds\n"
    "v_mfma_f32_16x16x32_bf16 v[12:15], a[132:135], v[88:91], v[12:15]\n"
    "s_waitcnt lgkmcnt(0)\n"
    "v_mfma_f32_16x16x32_bf16 v[8:11], a[136:139], v[92:95], v[8:11]\n"
    "s_sub_u32 s55, s49, 0x1000000\n"
    "v_mfma_f32_16x16x32_bf16 v[12:15], a[140:143], v[92:95], v[12:15]\n"
    "buffer_load_dwordx4 v[100:103], v3, s[92:95], s55 offen sc1\n"
    "s_nop 3\n"
    "v_cvt_pk_bf16_f32 v16, v8, v9\n"
    "v_cvt_pk_bf16_f32 v17, v10, v11\n"
    "s_nop 1\n"
    "v_cvt_pk_bf16_f32 v18, v12, v13\n"
    "v_cvt_pk_bf16_f32 v19, v14, v15\n"
    "buffer_store_dwordx4 v[16:19], v3, s[40:43], s62 offen sc1\n"
    "v_dot2_f32_bf16 v8, v16, s86, 0\n"
    "v_dot2_f32_bf16 v9, v16, v16, 0\n"
    "v_dot2_f32_bf16 v8, v17, s86, v8\n"
    "v_dot2_f32_bf16 v9, v17, v17, v9\n"
    "v_dot2_f32_bf16 v8, v18, s86, v8\n"
    "v_dot2_f32_bf16 v9, v18, v18, v9\n"
    "v_dot2_f32_bf16 v8, v19, s86, v8\n"
    "v_dot2_f32_bf16 v9, v19, v19, v9\n"
    "s_nop 2\n"
    "v_permlane32_swap_b32 v8, v9\n"
    "v_add_f32 v10, v8, v9\n"
    "v_add_f32 v8, v8, v9\n"
    "s_nop 1\n"
    "v_permlane16_swap_b32 v10, v8\n"
    "v_add_f32 v8, v8, v10\n"
    "buffer_store_dword v8, v5, s[40:43], s62 offen sc1\n"
    "s_mov_b32 s56, 1\n"
    "s_cmp_lg_u32 s94, 0\n"
    "s_cbranch_scc1 .Lbgo_%=\n"
    "ds_read_b32 v23, v7 offset:44\n"
    "s_waitcnt vmcnt(8) lgkmcnt(0)\n"
    "v_cmp_eq_u32 vcc, 0, v105\n"
    "s_cmp_eq_u64 vcc, 0\n"
    "s_cbranch_scc1 .Lbld_%=\n"
    "global_load_dword v105, v20, %1 sc1\n"
    "s_waitcnt vmcnt(0)\n"
    "v_mov_b32 v22, 1\n"
    "s_mov_b64 exec, s[56:57]\n"
    "ds_add_rtn_u32 v22, v7, v22 offset:48\n"
    "s_mov_b64 exec, -1\n"
    "s_waitcnt lgkmcnt(0)\n"
    "v_readfirstlane_b32 s50, v22\n"
    "s_cmp_eq_u32 s50, 3\n"
    "s_cselect_b32 s46, 0x20000, 0\n"
    "buffer_store_dword v22, v23, s[44:47], 0 offen sc1\n"
    "s_mov_b32 s56, 0\n"
    "v_cmp_eq_u32 vcc, 0, v105\n"
    "s_cmp_eq_u64 vcc, 0\n"
    "s_cbranch_scc1 .Lbld_%=\n"
    ".Lbchk_%=:\n"
    "v_readfirstlane_b32 s50, v20\n"
    "s_cmp_lt_u32 s50, 200\n"
    "s_cbranch_scc1 .Lbend_%=\n"
    ".Lbwait_%=:\n"
    "global_load_dword v105, v20, %1 sc1\n"
    "s_mov_b32 s52, 0\n"
    ".Lbp1_%=:\n"
    "s_waitcnt vmcnt(0)\n"
    "v_cmp_eq_u32 vcc, 0, v105\n"
    "s_cmp_eq_u64 vcc, 0\n"
    "s_cbranch_scc1 .Lbp1_x%=\n"
    "s_add_u32 s52, s52, 1\n"
    "s_sleep 2\n"
    "global_load_dword v105, v20, %1 sc1\n"
    "s_cmp_lt_u32 s52, s53\n"
    "s_cbranch_scc1 .Lbp1_%=\n"
    "s_mov_b32 s53, 0\n"
    "v_mov_b32 v22, 0\n"
    "v_mov_b32 v23, 1\n"
    "global_store_dword v22, v23, %1 sc1\n"
    "s_waitcnt vmcnt(0)\n"
    ".Lbp1_x%=:\n"
    ".Lbld_%=:\n"
    "s_mov_b32 s94, s42\n"
    "s_mov_b32 s90, s96\n"
    "s_mov_b32 m0, s58\n"
    "s_nop 0\n"
    "buffer_load_dwordx4 v2, s[92:95], s49 offen sc1 lds\n"
    "buffer_load_dwordx4 v2, s[92:95], s49 offen offset:1024 sc1 lds\n"
    "buffer_load_dwordx4 v2, s[92:95], s49 offen offset:2048 sc1 lds\n"
    "buffer_load_dwordx4 v2, s[92:95], s49 offen offset:3072 sc1 lds\n"
    "s_bitset1_b32 m0, 12\n"
    "s_nop 0\n"
    "buffer_load_dwordx4 v1, s[88:91], s49 offen sc1 lds\n"
    "s_sub_u32 s55, s49, 0x1000000\n"
    "buffer_load_dwordx4 v[100:103], v3, s[92:95], s55 offen sc1\n"
    "s_waitcnt vmcnt(6)\n"
    "v_mov_b32 v22, 1\n"
    "s_mov_b64 exec, s[56:57]\n"
    "ds_add_rtn_u32 v22, v7, v22 offset:48\n"
    "s_mov_b64 exec, -1\n"
    "s_waitcnt lgkmcnt(0)\n"
    "v_readfirstlane_b32 s50, v22\n"
    "s_cmp_eq_u32 s50, 3\n"
    "s_cselect_b32 s46, 0x20000, 0\n"
    "buffer_store_dword v22, v23, s[44:47], 0 offen sc1\n"
    "s_mov_b32 s56, 0\n"
    "v_add_u32 v7, 32, v7\n"
    "ds_read_b128 v[20:23], v7\n"
    "v_xor_b32 v0, 0x8000, v0\n"
    "v_xor_b32 v4, 0x8000, v4\n"
    "v_add_u32 v104, 8192, v4\n"
    "s_waitcnt lgkmcnt(0)\n"
    "v_add_u32 v20, v20, v6\n"
    "v_readfirstlane_b32 s62, v22\n"
    "v_readfirstlane_b32 s49, v21\n"
    "s_xor_b32 s58, s58, 0x8000\n"
    "s_waitcnt vmcnt(1)\n"
    "global_load_dword v105, v20, %1 sc1\n"
    "s_branch .Lbtop_%=\n"
    ".Lbpol_%=:\n"
    "v_mov_b32 v20, 64\n"
    "global_load_dword v105, v20, %1 sc1\n"
    "s_mov_b32 s52, 0\n"
    ".Lbp2_%=:\n"
    "s_waitcnt vmcnt(0)\n"
    "v_cmp_eq_u32 vcc, 0, v105\n"
    "s_cmp_eq_u64 vcc, 0\n"
    "s_cbranch_scc1 .Lbp2_x%=\n"
    "s_add_u32 s52, s52, 1\n"
    "s_sleep 2\n"
    "global_load_dword v105, v20, %1 sc1\n"
    "s_cmp_lt_u32 s52, s53\n"
    "s_cbranch_scc1 .Lbp2_%=\n"
    "s_mov_b32 s53, 0\n"
    "v_mov_b32 v22, 0\n"
    "v_mov_b32 v23, 1\n"
    "global_store_dword v22, v23, %1 sc1\n"
    "s_waitcnt vmcnt(0)\n"
    ".Lbp2_x%=:\n"
    "v_readfirstlane_b32 s50, v105\n"
    "v_mbcnt_lo_u32_b32 v20, -1, 0\n"
    "v_mbcnt_hi_u32_b32 v20, -1, v20\n"
    "s_lshl_b32 s51, %2, 6\n"
    "v_add_u32 v20, s51, v20\n"
    "v_min_u32 v20, 129, v20\n"
    "v_lshlrev_b32 v20, 5, v20\n"
    "v_add_u32 v20, 0x26c00, v20\n"
    "ds_read2_b32 v[22:23], v20 offset0:1 offset1:2\n"
    "s_and_b32 s50, s50, 1\n"
    "s_waitcnt lgkmcnt(0)\n"
    "v_and_b32 v21, s50, v22\n"
    "v_lshlrev_b32 v21, 13, v21\n"
    "v_and_b32 v22, -2, v22\n"
    "v_xor_b32 v22, v21, v22\n"
    "v_and_b32 v21, s50, v23\n"
    "v_lshlrev_b32 v21, 13, v21\n"
    "v_and_b32 v23, -2, v23\n"
    "v_xor_b32 v23, v21, v23\n"
    "ds_write2_b32 v20, v22, v23 offset0:1 offset1:2\n"
    "s_waitcnt lgkmcnt(0)\n"
    "s_barrier\n"
    "ds_read_b32 v21, v7 offset:4\n"
    "v_add_u32 v20, %4, v6\n"
    "s_waitcnt lgkmcnt(0)\n"
    "v_readfirstlane_b32 s49, v21\n"
    "s_branch .Lbwait_%=\n"
    ".Lbend_%=:\n"
    "s_cmp_eq_u32 s50, 192\n"
    "s_cbranch_scc0 .Lbpol_%=\n"
    "s_waitcnt vmcnt(0) lgkmcnt(0)\n"
    ".Lrole5_%=:\n"
    ".Lfin:\n"
        :: "s"(wd), "s"(ws), "s"(wave), "s"(rid), "s"(P.h), "s"(P.p), "s"(P.woff), "s"(layer), "s"(w0), "s"(Wk), "s"(Wv), "s"(Wo),
           "s"(mem0), "s"(P.role) : CLOBBERS);
}

extern "C" void kernel_launch(void* const* d_in, const int* in_sizes, int n_in, void* d_out, int out_size, void* d_ws,
                              size_t ws_size, hipStream_t stream) {
    mega_kernel<<<256, 256, 0, stream>>>((const u16*)d_in[0], (const u16*)d_in[1], (const u16*)d_in[2], (const u16*)d_in[4],
                                         (const u16*)d_in[5], (const u16*)d_in[6], (const u16*)d_in[7], (const u16*)d_in[8],
                                         (u16*)d_out, (char*)d_ws);
}
```

```cpp
#define FC 30
#include <hip/hip_runtime.h>
#include <hip/hip_bf16.h>
#include <stdint.h>

typedef unsigned short u16;
typedef unsigned u32;
typedef unsigned long long u64;
using bf16x8 = __attribute__((ext_vector_type(8))) short;
using bf16x4 = __attribute__((ext_vector_type(4))) short;
using f32x4 = __attribute__((ext_vector_type(4))) float;
using f32x2 = __attribute__((ext_vector_type(2))) float;
using v4u = __attribute__((ext_vector_type(4))) unsigned;
using v2u = __attribute__((ext_vector_type(2))) unsigned;
typedef __bf16 bfv2 __attribute__((ext_vector_type(2)));
#define GAS __attribute__((address_space(1)))
typedef GAS unsigned gu32;
typedef GAS u64 gu64;
#define RLX_AGENT __ATOMIC_RELAXED, __HIP_MEMORY_SCOPE_AGENT
typedef __amdgpu_buffer_rsrc_t rsrc_t;

constexpr int S_ = 2048, D_ = 576, NSEG = 64, SEG = 32, NB = 4096, NQA = 1728;

constexpr u32 OFF_ABORT = 0;
constexpr u32 OFF_POL = 64;
constexpr u32 OFF_RDY = 128;
constexpr u32 OFF_NEVER = 192;
constexpr u32 OFF_FP = 32768;
constexpr u32 OFF_COB = OFF_FP + 32768;
constexpr u32 OFF_FKV = OFF_COB + 32768;
constexpr u32 OFF_GO = 131072;
constexpr u32 OFF_EP = OFF_GO + 1024;
constexpr u32 SYNC_BYTES = OFF_EP + 128;
constexpr u32 MB2 = 2u << 20;
constexpr u32 OFF_XA = 0;
constexpr u32 OFF_XB = MB2;
constexpr u32 XB = S_ * D_ * 2;
constexpr u32 OFF_TA = 4 * MB2;
constexpr u32 OFF_TB = 8 * MB2;
constexpr u32 OFF_OA = 12 * MB2;
constexpr u32 OFF_PB = 16 * MB2;
constexpr u32 HB_ = 2 * 64 * 32 * 8192;
constexpr u32 OFF_QX = 32 * MB2;
constexpr u32 OFF_KX = OFF_QX + HB_;
constexpr u32 OFF_VX = OFF_KX + HB_;
constexpr u32 OFF_OX = OFF_VX + HB_;
constexpr u32 WS_BYTES = OFF_OX + HB_;
constexpr u32 L_TAB = 147456, L_IT = L_TAB + 11264, L_EX = 138240, L_W = 32064;
constexpr u32 O_SUM = 32768 + 2048;

#define STAMP(i, k) do {} while (0)
#define PH(k) do {} while (0)
__device__ __forceinline__ float bf2f(u16 v) { return __uint_as_float(((u32)v) << 16); }
__device__ __forceinline__ float bflo(u32 v) { return __uint_as_float(v << 16); }
__device__ __forceinline__ float bfhi(u32 v) { return __uint_as_float(v & 0xffff0000u); }
__device__ __forceinline__ u32 pack2(float a, float b) { bfv2 v = {(__bf16)a, (__bf16)b}; return __builtin_bit_cast(u32, v); }
__device__ __forceinline__ float rbf(float f) { return (float)(__bf16)f; }
__device__ __forceinline__ v4u ld16(rsrc_t r, u32 off) { return __builtin_amdgcn_raw_buffer_load_b128(r, off, 0, 16); }
__device__ __forceinline__ v2u ld8(rsrc_t r, u32 off) { return __builtin_amdgcn_raw_buffer_load_b64(r, off, 0, 16); }
__device__ __forceinline__ void st16(rsrc_t r, u32 off, v4u v) { __builtin_amdgcn_raw_buffer_store_b128(v, r, off, 0, 16); }
__device__ __forceinline__ void st8(rsrc_t r, u32 off, v2u v) { __builtin_amdgcn_raw_buffer_store_b64(v, r, off, 0, 16); }
__device__ __forceinline__ void st4(rsrc_t r, u32 off, u32 v) { __builtin_amdgcn_raw_buffer_store_b32(v, r, off, 0, 16); }
#define DRAIN() asm volatile("s_waitcnt vmcnt(0)" ::: "memory")
#define TOUCH(p) asm volatile("global_load_dword v255, %0, off" :: "v"(p) : "v255")
#define CBAR() asm volatile("" ::: "memory")
__device__ __forceinline__ u32 ldw(char* ws, u32 off) { return __hip_atomic_load((gu32*)(ws + off), RLX_AGENT); }
__device__ __forceinline__ void setw(char* ws, u32 off) { __hip_atomic_store((gu32*)(ws + off), 1u, RLX_AGENT); }
struct Fl { u32 a, b; __device__ __forceinline__ u32 operator()(u32 i) const { return a + (i >> 6 & 1) * (b - a) + (i & 32) * 32 + (i & 31) * 4; } };
__device__ __forceinline__ Fl f_row(u32 g, u32 m, u32 o) {
    const u32 c = g == 1 ? FC >> 1 & 1 : (FC ^ g >> 1) & 1;
    return Fl{(1 + 2 * g + m) * 2048 + o, 16385 + (c << 13) + ((g == 1 ? c == (FC & 1) : g >> 1) * 2 + m) * 2048 + o};
}
__device__ __forceinline__ Fl f_two(u32 b, u32 u, u32 o) { b += (u >> 2) * 16384 + (u & 3) * 2048 + o + ((FC >> (1 + (b >> 15)) & 1) << 13) + 1; return Fl{b, b ^ 8192}; }
__device__ __forceinline__ u32 fix(u32 x, u32 pol) { return (x & ~1u) ^ (x & pol & 1) << 13; }
constexpr u32 SPIN_CAP = 1u << 20;
#define SLP_W 2
#define SLP_C 8
__device__ __forceinline__ void until(u64 t) { while (__builtin_amdgcn_s_memrealtime() < t) __builtin_amdgcn_s_sleep(8); }
#define TDEL 0
__device__ __forceinline__ u64 succ(char* ws) {
    const u64 e = __hip_atomic_load((gu64*)(ws + OFF_EP), RLX_AGENT);
    return (u64)((u32)(e >> 32) + 1) << 32 | ((u32)e + 1);
}
__device__ __forceinline__ void wait_go(char* ws) {
    const u64 e = succ(ws);
    for (u32 n = 0; n < SPIN_CAP && __builtin_amdgcn_ballot_w64(__hip_atomic_load((gu64*)(ws + OFF_GO + (threadIdx.x & 7) * 128), RLX_AGENT) != e); ++n) __builtin_amdgcn_s_sleep(2);
    CBAR();
}
__device__ __forceinline__ bool spin_fail(char* ws, u32& spins) {
    ++spins;
    if ((spins & 63u) == 0u) {
        if (ldw(ws, OFF_ABORT)) return true;
        if (spins > SPIN_CAP) { __hip_atomic_store((gu32*)(ws + OFF_ABORT), 1u, RLX_AGENT); return true; }
    }
    return false;
}
__device__ __forceinline__ void wait_set(char* ws, u32 off) {
    u32 spins = 0;
    while (__builtin_amdgcn_ballot_w64(ldw(ws, off) == 0)) {
        if (spin_fail(ws, spins)) break;
        __builtin_amdgcn_s_sleep(SLP_W);
    }
    CBAR();
}
constexpr u32 OOB = 0x80000000u;
#define DC 5
__device__ __forceinline__ u32 rows(u32 b, u32 i, u32 m) { return b + (i << 16) + (i < 16 ? m << 13 : (((i >> 5 ^ m ^ DC >> ((b == OFF_TB) * 2 + (i >> 6))) & 1) << 13) + 1); }
__device__ __forceinline__ u32 part(u32 i, u32 m) { return (i << 18) + (((i >> 3 ^ m ^ DC >> (4 + (i >> 6))) & 1) << 13) + 1; }
__device__ __forceinline__ u32 cgo(u32 cg) { return (cg >> 5) * 16384 + (cg & 31) * 256; }
__device__ __forceinline__ u32 sumo(u32 l) {
    const u32 lane = threadIdx.x & 63, b = l * 128 + (lane >> 5) * 64 + (lane & 15) * 4;
    return lane & 16 ? OOB : O_SUM + b / 576 * 1024 + b % 576;
}
__device__ __forceinline__ u32 sums(v4u x) {
    float s = 0.f, q = 0.f;
    __builtin_amdgcn_sched_barrier(0);
#pragma unroll
    for (int e = 0; e < 4; ++e) {
        const u32 z = x[e];
        const bfv2 y = __builtin_bit_cast(bfv2, z);
        s = __builtin_amdgcn_fdot2_f32_bf16(y, __builtin_bit_cast(bfv2, 0x3f803f80u), s, false);
        q = __builtin_amdgcn_fdot2_f32_bf16(y, y, q, false);
    }
    auto a = __builtin_amdgcn_permlane32_swap(__float_as_uint(s), __float_as_uint(q), false, false);
    const u32 r = __float_as_uint(__uint_as_float(a[0]) + __uint_as_float(a[1]));
    a = __builtin_amdgcn_permlane16_swap(r, r, false, false);
    return __float_as_uint(__uint_as_float(a[0]) + __uint_as_float(a[1]));
}

template <int N, bool FIN>
__device__ __forceinline__ void t_item(int rid, u32 i, u32 j, u32 m, u32 pol, u32 w, rsrc_t ro, char* ws, rsrc_t rs) {
    const u32 lane = threadIdx.x & 63, s = i & 63, ta = fix(rows(OFF_TA, 64 + s, m), pol);
    v4u x[N], p[N][4];
    u32 v[N];
    wait_set(ws, fix(f_row(2, m, j * 128)(i), pol));
#pragma unroll
    for (int u = 0; u < N; ++u) { v[u] = cgo(12 * j + 4 * (w + u) + (lane >> 4)) + (lane & 15) * 16; x[u] = ld16(rs, fix(rows(OFF_TB, i, m), pol) + v[u]); }
    wait_set(ws, fix(f_two(OFF_FP, m * 3 + (j >> 1), (lane & 3) * 128)(i), pol));
    STAMP(i, 0);
#pragma unroll
    for (int u = 0; u < N; ++u)
#pragma unroll
        for (int k = 0; k < 4; ++k) p[u][k] = __builtin_amdgcn_raw_buffer_load_b128(rs, v[u], OFF_PB + (k << 16) + fix(part(i, m), pol), 16);
#pragma unroll
    for (int u = 0; u < N; ++u) {
#pragma unroll
        for (int e = 0; e < 4; ++e) {
            f32x2 a = {bflo(x[u][e]), bfhi(x[u][e])};
#pragma unroll
            for (int k = 0; k < 4; ++k) a += f32x2{bflo(p[u][k][e]), bfhi(p[u][k][e])};
            x[u][e] = pack2(a[0], a[1]);
        }
        if (FIN) __builtin_amdgcn_raw_buffer_store_b128(x[u], ro, (s * 32 + 2 * (lane & 15) + m) * 1152 + (12 * j + 4 * (w + u) + (lane >> 4)) * 16, 0, 0);
        else {
            st16(rs, ta + v[u], x[u]);
            __builtin_amdgcn_raw_buffer_store_b32(sums(x[u]), rs, sumo(3 * j + w), ta, 16);
        }
    }
    STAMP(i, 1);
}
__device__ __forceinline__ bool t_last(u32* cnt) {
    DRAIN();
    u32 o = 0;
    if ((threadIdx.x & 63) == 0) o = __hip_atomic_fetch_add(cnt, 1u, __ATOMIC_RELAXED, __HIP_MEMORY_SCOPE_WORKGROUP);
    return __builtin_amdgcn_readfirstlane(o) == 2;
}
__device__ __forceinline__ void role_t(u64 t_in, int rid, u32 j, u32 m, const u16* x0, u16* out, char* ws, rsrc_t rs, char* smem) {
    const u32 lane = threadIdx.x & 63, w = __builtin_amdgcn_readfirstlane(threadIdx.x >> 6);
    u32* cnt = (u32*)smem + 64;
    cnt[threadIdx.x] = 0;
    __syncthreads();
    const rsrc_t ro = __builtin_amdgcn_make_buffer_rsrc(out, 0, XB, 0x00020000);
    if (w < 3) {
        const rsrc_t rx = __builtin_amdgcn_make_buffer_rsrc((void*)x0, 0, XB, 0x00020000);
        const u32 cg = 12 * j + 4 * w + (lane >> 4), vx = (2 * (lane & 15) + m) * 1152 + cg * 16, v = cgo(cg) + (lane & 15) * 16;
        u32 pol = 0;
#pragma unroll 1
        for (u32 s0 = 0; s0 < 64; s0 += s0 < 4 ? 2 : 10) {
            const u32 n = s0 < 4 ? 2 : 10;
            if (s0 == 14) { wait_set(ws, OFF_POL); pol = ldw(ws, OFF_POL); }
            v4u x[10];
#pragma unroll
            for (u32 u = 0; u < 10; ++u) x[u] = __builtin_amdgcn_raw_buffer_load_b128(rx, u < n ? vx + (s0 + u) * 36864 : OOB, 0, 0);
#pragma unroll
            for (u32 u = 0; u < 10; ++u) if (u < n) {
                const u32 ta = __builtin_amdgcn_readfirstlane(fix(rows(OFF_TA, s0 + u, m), pol));
                st16(rs, v + ta, x[u]);
                __builtin_amdgcn_raw_buffer_store_b32(sums(x[u]), rs, sumo(3 * j + w), ta, 16);
            }
            if (s0 == 0) { wait_go(ws); __builtin_amdgcn_s_barrier(); }
            if (t_last(cnt + s0) && lane < n) setw(ws, f_row(0, m, j * 128)(s0 + lane));
        }
    }
    else __builtin_amdgcn_s_barrier();
    CBAR();
    wait_set(ws, OFF_POL);
    const u32 pol = __builtin_amdgcn_readfirstlane(ldw(ws, OFF_POL));
    if (w == 3) {
        for (u32 i = 64; i < 127; ++i) t_item<3, true>(rid, i, j, m, pol, 0, ro, ws, rs);
        return;
    }
    for (u32 i = 0; i < 64; ++i) {
        t_item<1, false>(rid, i, j, m, pol, w, ro, ws, rs);
        if (t_last(cnt + 64 + i) && lane == 0) setw(ws, fix(f_row(0, m, j * 128)(64 + i), pol));
    }
    t_item<1, true>(rid, 127, j, m, pol, w, ro, ws, rs);
}

#define CLOBBERS "memory", "vcc", "scc", \
    "v0", "v1", "v2", "v3", "v4", "v5", "v6", "v7", "v8", "v9", "v10", "v11", "v12", "v13", "v14", "v15", "v16", "v17", "v18", "v19", "v20", "v21", "v22", "v23", \
    "v24", "v25", "v26", "v27", "v28", "v29", "v30", "v31", "v32", "v33", "v34", "v35", "v36", "v37", "v38", "v39", "v40", "v41", "v42", "v43", "v44", "v45", "v46", "v47", \
    "v48", "v49", "v50", "v51", "v52", "v53", "v54", "v55", "v56", "v57", "v58", "v59", "v60", "v61", "v62", "v63", "v64", "v65", "v66", "v67", "v68", "v69", "v70", "v71", \
    "v72", "v73", "v74", "v75", "v76", "v77", "v78", "v79", "v80", "v81", "v82", "v83", "v84", "v85", "v86", "v87", "v88", "v89", "v90", "v91", "v92", "v93", "v94", "v95", \
    "v96", "v97", "v98", "v99", "v100", "v101", "v102", "v103", "v104", "v105", "v106", "v107", "v108", "v109", "v110", "v111", "v112", "v113", "v114", "v115", "v116", "v117", "v118", "v119", \
    "v120", "v121", "v122", "v123", "v124", "v125", "v126", "v127", "v128", "v129", "v130", "v131", "v132", "v133", "v134", "v135", "v136", "v137", "v138", "v139", "v140", "v141", "v142", "v143", \
    "v144", "v145", "v146", "v147", "v148", "v149", "v150", "v151", "v152", "v153", "v154", "v155", "v156", "v157", "v158", "v159", "v160", "v161", "v162", "v163", "v164", "v165", "v166", "v167", \
    "v168", "v169", "v170", "v171", "v172", "v173", "v174", "v175", "v176", "v177", "v178", "v179", "v180", "v181", "v182", "v183", "v184", "v185", "v186", "v187", "v188", "v189", "v190", "v191", \
    "v192", "v193", "v194", "v195", "v196", "v197", "v198", "v199", "v200", "v201", "v202", "v203", "v204", "v205", "v206", "v207", "v208", "v209", "v210", "v211", "v212", "v213", "v214", "v215", \
    "v216", "v217", "v218", "v219", "v220", "v221", "v222", "v223", "v224", "v225", "v226", "v227", "v228", "v229", "v230", "v231", "v232", "v233", "v234", "v235", "v236", "v237", "v238", "v239", \
    "v240", "v241", "v242", "v243", "v244", "v245", "v246", "v247", "v248", "v249", "v250", "v251", "v252", "v253", "v254", "v255", "a0", "a1", "a2", "a3", "a4", "a5", "a6", "a7", \
    "a8", "a9", "a10", "a11", "a12", "a13", "a14", "a15", "a16", "a17", "a18", "a19", "a20", "a21", "a22", "a23", "a24", "a25", "a26", "a27", "a28", "a29", "a30", "a31", \
    "a32", "a33", "a34", "a35", "a36", "a37", "a38", "a39", "a40", "a41", "a42", "a43", "a44", "a45", "a46", "a47", "a48", "a49", "a50", "a51", "a52", "a53", "a54", "a55", \
    "a56", "a57", "a58", "a59", "a60", "a61", "a62", "a63", "a64", "a65", "a66", "a67", "a68", "a69", "a70", "a71", "a72", "a73", "a74", "a75", "a76", "a77", "a78", "a79", \
    "a80", "a81", "a82", "a83", "a84", "a85", "a86", "a87", "a88", "a89", "a90", "a91", "a92", "a93", "a94", "a95", "a96", "a97", "a98", "a99", "a100", "a101", "a102", "a103", \
    "a104", "a105", "a106", "a107", "a108", "a109", "a110", "a111", "a112", "a113", "a114", "a115", "a116", "a117", "a118", "a119", "a120", "a121", "a122", "a123", "a124", "a125", "a126", "a127", \
    "a128", "a129", "a130", "a131", "a132", "a133", "a134", "a135", "a136", "a137", "a138", "a139", "a140", "a141", "a142", "a143", "a144", "a145", "a146", "a147", "a148", "a149", "a150", "a151", \
    "a152", "a153", "a154", "a155", "a156", "a157", "a158", "a159", "a160", "a161", "a162", "a163", "a164", "a165", "a166", "a167", "a168", "a169", "a170", "a171", "a172", "a173", "a174", "a175", \
    "a176", "a177", "a178", "a179", "a180", "a181", "a182", "a183", "a184", "a185", "a186", "a187", "a188", "a189", "a190", "a191", "a192", "a193", "a194", "a195", "a196", "a197", "a198", "a199", \
    "a200", "a201", "a202", "a203", "a204", "a205", "a206", "a207", "a208", "a209", "a210", "a211", "a212", "a213", "a214", "a215", "a216", "a217", "a218", "a219", "a220", "a221", "a222", "a223", \
    "a224", "a225", "a226", "a227", "a228", "a229", "a230", "a231", "a232", "a233", "a234", "a235", "a236", "a237", "a238", "a239", "a240", "a241", "a242", "a243", "a244", "a245", "a246", "a247", \
    "a248", "a249", "a250", "a251", "a252", "a253", "a254", "a255", "s38", "s39", "s40", "s41", "s42", "s43", "s44", "s45", "s46", "s47", "s48", "s49", "s50", "s51", "s52", "s53", \
    "s54", "s55", "s56", "s57", "s58", "s59", "s60", "s61", "s62", "s63", "s64", "s65", "s66", "s67", "s68", "s69", "s70", "s71", "s72", "s73", "s74", "s75", "s76", "s77", \
    "s78", "s79", "s80", "s81", "s82", "s83", "s84", "s85", "s86", "s87", "s88", "s89", "s90", "s91", "s92", "s93", "s94", "s95", "s96", "s97", "s98", "s99", "s100", "s101"
#define LANES() const int tid = threadIdx.x, lane = tid & 63, c = lane & 15, g = lane >> 4, q = c >> 2, p4 = c & 3; \
    const int wave = __builtin_amdgcn_readfirstlane(tid >> 6); u32* tab = (u32*)(smem + L_TAB) + tid; (void)q; (void)p4; (void)g
struct Par { u32 role, h, p, woff; };
__device__ __forceinline__ u32 slot(u32 i) { return ((i & 63) << 19) + (((i >> 2 ^ i >> 6) & 1) << 13) + 1; }
template <class FS, class FD>
__device__ __forceinline__ u32 items(u32 K, u32 mk, Fl fin, Fl fout, FS src, FD dst, char* smem) {
    const u32 n = threadIdx.x - 1;
    auto idx = [&](u32 k) { const u32 t = k - K; return t < 2 * (64 - K) ? ((t & 1) ? K : 64) + (t >> 1) : k & 127; };
    if (threadIdx.x < 130) {
        v4u* row = (v4u*)(smem + L_IT) + 2 * threadIdx.x;
        row[0] = v4u{n + 1 == mk ? OFF_NEVER + 4 : n + 1 < 128 ? fin(idx(n + 1)) : OFF_NEVER, src(idx(n + 1)), n + 1 ? dst(idx(n)) : dst(3) & ~1u, n ? fout(idx(n - 1)) : 1536u};
        row[1] = v4u{0u, 0u, 0x14000 + (idx(n) >> 6) * 16384, idx(n)};
    }
    return fin(mk ? 64 : 0);
}
#define KA 15
static_assert(KA <= 16);
#define KB 14
static_assert(KB <= 16);
#define KH 13
#define KW 12

__device__ __forceinline__ Par role_h(u32 m, u32 h, char* smem) {
    LANES();
    tab[0] = 0x10000 + wave * 8192 + lane * 16;
    tab[256] = tid * 16;
    tab[2 * 256] = cgo(16 * wave) + lane * 16;
    tab[3 * 256] = 0x10000 + 5120 + lane * 4;
    tab[4 * 256] = (8 * wave + g) * 8192 + (c ^ ((g | (wave & 1) << 2) << 1)) * 16;
    tab[5 * 256] = (4 * g + q) * 256 + ((2 * wave) ^ (q | (g & 1) << 2)) * 32 + p4 * 8;
    tab[6 * 256] = tab[5 * 256] ^ 32;
    const u32 f = items(KH, 0, f_row(2, m, 0), f_two(OFF_FKV, h >> 2, (h & 3) * 256 + m * 128), [&](u32 i) { return rows(OFF_TB, i, m); }, [&](u32 i) { return OFF_QX + h * 16384 + m * 4096 + slot(i); }, smem);
    return Par{1, f, 0, h * 256};
}

__device__ __forceinline__ Par role_w(u32 m, u32 kb, u32 nb, char* smem) {
    LANES();
    tab[0] = wave * 8192 + lane * 16;
    tab[256] = wave * 32768 + lane * 16;
    tab[512] = cgo(24 * nb + 6 * wave + g) + c * 16;
    tab[768] = (256 * wave + 4 * g + (c >> 3)) * 1152 + ((lane & 7) ^ ((g & 1) << 2)) * 16;
#pragma unroll
    for (int t = 0; t < 3; ++t) {
        const int col = wave * 96 + (t < 2 ? p4 * 16 + t * 8 : 64 + p4 * 8);
        tab[(4 + t) * 256] = (col >> 7) * 2048 + (q & 1) * 1024 + (2 * g + (q >> 1)) * 128 + ((col & 127) ^ ((g & 1) << 6));
    }
    tab[7 * 256] = (g & 1) ? OOB : cgo(24 * nb + 6 * wave + 4 + (g >> 1)) + c * 16;
    const u32 f = items(KW, 0, f_two(OFF_COB, m * 4 + kb, 0), f_two(OFF_FP, m * 3 + nb, kb * 128), [&](u32 i) { return OFF_OX + kb * 8 * 16384 + m * 4096 + slot(i); },
          [&](u32 i) { return OFF_PB + kb * 65536 + part(i, m); }, smem);
    return Par{2, f, 0, kb * 1024 * 1152 + nb * 384};
}

__device__ __forceinline__ Par role_a2(u32 m, u32 j, char* smem) {
    LANES();
    const int w = wave < 3 ? wave : 2;
    tab[0] = 0x8000 + lane * 16;
    tab[256] = tid * 16;
    tab[512] = cgo(16 * wave) + lane * 16;
    tab[768] = wave < 3 ? cgo(12 * j + 4 * wave + g) + c * 16 : OOB;
    tab[4 * 256] = 0x9000 + g * 256 + c * 4;
    tab[5 * 256] = wave < 3 ? sumo(3 * j + wave) : OOB;
    tab[6 * 256] = (16 * wave + (lane >> 2)) * 1152 + (lane & 3) * 16;
    tab[7 * 256] = w * 36864 + p4 * 16 + (20 * (g >> 1) + 8 * (g & 1) + q) * 64;
    tab[8 * 256] = w * 36864 + p4 * 16 + (72 * g + 16 + q) * 64;
    const u32 f = items(KB, KB, f_row(1, m, 0), f_row(2, m, j * 128), [&](u32 i) { return rows(OFF_OA, i, m); }, [&](u32 i) { return rows(OFF_TB, i, m); }, smem);
    return Par{4, f, 0, j * 192};
}

__device__ __forceinline__ Par role_a1(u32 m, u32 hg, char* smem) {
    LANES();
    const int hd = 4 * hg + wave;
    tab -= 768;
    tab[0] = wave * 8192 + lane * 16;
    tab[256] = !(g & 1) ? cgo(2 * hd + (g >> 1)) + c * 16 : g == 1 && !p4 ? 32768 + hd * 64 + c * 4 : OOB;
    tab[2 * 256] = cgo(16 * wave) + lane * 16;
    tab[3 * 256] = tid * 16;
    tab[4 * 256] = L_EX + ((wave >> 1) * 16 + c) * 16 + (wave & 1) * 8;
    tab[5 * 256] = g ? L_EX + 4096 : tab[4 * 256] + 512;
    tab[6 * 256] = L_EX + 2048 + ((wave >> 1) * 16 + 4 * g) * 16 + (2 * (wave & 1) + (c & 1)) * 4;
    tab[7 * 256] = 5120 + lane * 4;
    tab[8 * 256] = 7424 + g * 16;
    tab[9 * 256] = 4 * g - c;
    tab[10 * 256] = (4 * g + q) * 144 + 36 * wave + 4 * (wave & 1) + p4 * 8;
    tab[11 * 256] = (wave < 2 ? p4 >> 1 : 2) * 9216 + (4 * g + q) * 144 + 72 * (p4 & 1) + 32;
    if (tid < 2) *(v4u*)(smem + L_EX + 4096 + tid * 1024) = v4u{0u, 0u, 0u, 0u};
    const u32 f = items(KA, KA, f_row(0, m, 0), f_row(1, m, hg * 128), [&](u32 i) { return rows(OFF_TA, i, m); }, [&](u32 i) { return rows(OFF_OA, i, m); }, smem);
    return Par{3, f, 0, hg * 144};
}

#define SB() __builtin_amdgcn_sched_barrier(0)
__device__ __forceinline__ u32 now32() { return (u32)__builtin_amdgcn_s_memtime(); }
__device__ __forceinline__ Par role_chain(u32 h, u32 p, char* smem) {
    LANES();
    const int ro = (q & 1) + 4 * (q >> 1) + 8 * (g >> 1) + 16 * (g & 1), rr = (q & 1) | (g & 1) << 1 | (q >> 1) << 2;
    tab[0] = lane * 16;
    tab[256] = L_W + (wave * 15 + (wave == 3 ? 2 : 0)) * 2048 + lane * 16;
    tab[2 * 256] = ((g & 1) + 4 * (g >> 1) + 8 * wave) * 8192 + (c ^ ((g | (wave & 1) << 2) << 1)) * 16;
    tab[3 * 256] = (rr + 8 * (g >> 1)) * 256 + ((2 * wave) ^ rr) * 32 + p4 * 8;
    tab[4 * 256] = tab[3 * 256] ^ 32;
    tab[5 * 256] = (2 * wave + (lane >> 5)) * 1152 + ((lane & 31) ^ ((lane >> 5 | (wave & 2)) << 2)) * 16;
    tab[6 * 256] = ((q & 1) + 4 * (g >> 1) + 16 * (g & 1)) * 512 + ((2 * wave + (p4 >> 1)) ^ ((q & 1) | (g & 2))) * 64 + (q >> 1) * 16 + (p4 & 1) * 8;
    tab[7 * 256] = (32 * wave + (lane >> 2)) * 1152 + (lane & 3) * 16;
    tab[8 * 256] = ro * 64 + (wave & 1) * 32 + p4 * 8;
    return Par{0, h, p, 0};
}

#define MAP 4
#define DED 3
#define YX 0
constexpr int RW = MAP == 4 ? (YX & 2 ? 94 : 93) : 90, RA1 = 24, RA2 = MAP >= 2 ? 184 : 56, RTT = 58;
#define PFO 3
#define WDEL1 700
#define PDEL 1000
#define ODEL 600
#define NSW 0
#define CDEL 1200
#define WDEL 700
#define NPOL 6
static_assert(OFF_GO == 131072u);
static_assert(OFF_EP == 132096u);
static_assert(OFF_POL == 64u);
static_assert(OFF_RDY == 128u);
static_assert(OFF_COB == 65536u);
static_assert(OFF_FKV == 98304u);
static_assert(OFF_XA == 0u);
static_assert(OFF_XB == 2097152u);
static_assert(OFF_TA == 8388608u);
static_assert(OFF_OA == 25165824u);
static_assert(OFF_NEVER == 192u);
static_assert(L_IT == 158720u);
static_assert(OFF_QX == 67108864u);
static_assert(OFF_KX == 100663296u);
static_assert(OFF_VX == 134217728u);
static_assert(OFF_OX == 167772160u);
static_assert(WS_BYTES == 201326592u);
static_assert(L_TAB == 147456u);
static_assert(L_EX == 138240u);
static_assert(O_SUM == 34816u);
static_assert(L_W == 32064);
__device__ __forceinline__ void measure_pol(char* ws, rsrc_t rs, char* smem) {
    const int wave = __builtin_amdgcn_readfirstlane(threadIdx.x >> 6);
    u32 best[2] = {~0u, ~0u};
    for (int n = 0; n < NPOL; ++n)
#pragma unroll
        for (int i = 0; i < 2; ++i) {
            SB();
            const u32 t0 = now32();
            SB();
            st4(rs, (wave & 1) * MB2 + (508 + 2 * i + (wave >> 1)) * 4096 + n * 128, t0);
            DRAIN();
            const u32 t = now32() - t0;
            best[i] = t < best[i] ? t : best[i];
        }
    int* d = (int*)smem;
    if ((threadIdx.x & 63) == 0) d[wave] = (int)(best[0] - best[1]);
    wait_go(ws);
    __syncthreads();
    if (threadIdx.x == 0) __hip_atomic_store((gu32*)(ws + OFF_POL), 4u | (d[0] + d[2] >= 0) | (d[1] + d[3] >= 0) << 1, RLX_AGENT);
}

__global__ __launch_bounds__(256, 1) void mega_kernel(const u16* __restrict__ x0, const u16* __restrict__ wqkv, const u16* __restrict__ woA,
                                                     const u16* __restrict__ mem0, const u16* __restrict__ Wq, const u16* __restrict__ Wk,
                                                     const u16* __restrict__ Wv, const u16* __restrict__ Wo, u16* out, char* ws) {
    __shared__ __attribute__((aligned(16))) char smem[163840];
    char* wd = (char*)(((uintptr_t)ws + SYNC_BYTES + MB2 - 1) & ~(uintptr_t)(MB2 - 1));
    const rsrc_t rs = __builtin_amdgcn_make_buffer_rsrc(wd, 0, WS_BYTES, 0x00020000);
    const u64 t_in = __builtin_amdgcn_s_memrealtime();
    {
        const char* pc; u32 n;
        asm volatile("s_getpc_b64 %0\n.Lbeg:\ns_mov_b32 %1, .Lfin-.Lbeg" : "=s"(pc), "=s"(n));
        const u32 o = ((blockIdx.x >> 3 & 3) * 256 + threadIdx.x) * 128;
        if (o < n) TOUCH(pc + o);
    }
    u32 rid = (blockIdx.x & 7) * 32 + (blockIdx.x >> 3);
    rid = (rid & 143) | (rid & 48) << 1 | (rid & 64) >> 2 ^ (DED & 16);
    if (rid & 16) {
        const u32 y = (rid >> 6) ^ YX, sl = blockIdx.x >> 3, m = sl & 1, e = sl >> 1;
        auto rw = [](u32 i) { const u32 v = 12 + (i + 3) % 12; return v / 6 * 32 + v % 6 + 26; };
        auto rh = [](u32 h) { return (h >> 3) * 32 + 16 + (h & 7); };
        if (y < 2) rid = e < 10 ? m * 128 + rh(y * 10 + e) : e < 12 ? m * 128 + rw((2 * y + e - 10) * 3) : y * 128 + (e < 13 ? 62 + m : (e - 12) * 32 + 24 + m);
        else rid = m * 128 + (y == 2 ? (e < 8 ? 24 + e : e < 16 - NSW ? rw((e & 3) * 3 + (e >> 2) - 1) : rh(16 + e)) : e < 12 - NSW ? rh(20 + e) : e < 12 ? rw((e & 3) * 3 + 2) : 46 + e);
    }
    asm volatile("" : "+s"(rid));
    STAMP(159, 0);
    const u32 xq = (rid >> 5) & 3, t = rid & 31, layer = rid >> 7;
    const int u = t - 24;
    const int role = t < 16 ? 0 : t < 24 ? 1 : xq == 0 ? 3 : u < 2 ? 4 : xq == 1 ? 5 : 2;
    const int ix = role == 3 ? u : role == 4 ? 2 * xq - 2 + u : role == 5 ? u - 2 : (xq * 6 + u - 5) % 12;
    if (role == 5) { role_t(t_in, rid, ix, layer, x0, out, ws, rs, smem); asm volatile("s_endpgm" ::: "memory"); }
    if (role == 3) for (u32 k = (layer * 8 + ix) * 256 + threadIdx.x; k < 15552; k += 4096) TOUCH((const char*)wqkv + k * 128);
    if (role == 0 && layer) {
        const u32 i = t * 256 + threadIdx.x;
        if ((DED & 2) && i < 3888) TOUCH((const char*)wqkv + (xq * 3888 + i) * 128);
        if (xq && i < 1728) TOUCH((const char*)woA + (i / 3) * 1152 + (xq - 1) * 384 + (i % 3) * 128);
        for (const u16* w : {Wq, Wk, Wv}) if ((PFO & 2) || w != Wq)
            for (u32 k = i; k < 9216; k += 4096) TOUCH((const char*)w + (k >> 4) * 8192 + xq * 2048 + (k & 15) * 128);
        until(t_in + ODEL);
        if (PFO & 1) for (u32 k = i; k < 9216; k += 4096) TOUCH((const char*)Wo + (xq * 9216 + k) * 128);
        until(t_in + PDEL);
        for (u32 k = 0; k < 18; ++k) st16(rs, (k % 3 * 3 >> 1) * MB2 + k / 3 * 262144 + (xq * 4096 + i) * 16, v4u{~0u, ~0u, ~0u, ~0u});
        DRAIN();
        wait_go(ws);
        __syncthreads();
        if (threadIdx.x == 0) __hip_atomic_fetch_add((gu32*)(ws + OFF_RDY), 1u, RLX_AGENT);
    }
    if (role == 2 && layer && ix < 8) {
        const u64 e = succ(ws);
        const rsrc_t rc = __builtin_amdgcn_make_buffer_rsrc(ws, 0, OFF_GO, 0x00020000);
        for (u32 k = 0; k < 4; ++k) st16(rc, ix * 16384 + k * 4096 + threadIdx.x * 16, v4u{0u, 0u, 0u, 0u});
        DRAIN();
        __syncthreads();
        if (threadIdx.x == 0) __hip_atomic_store((gu64*)(ws + OFF_GO + ix * 128), e, RLX_AGENT);
    }
    if (rid == RW) measure_pol(ws, rs, smem);
    until(t_in + (role == 2 ? (layer ? WDEL1 : WDEL) : role == 0 && layer ? CDEL : 0));
    Par P{5, 0, 0, 0};
    const u16* w0 = role == 2 ? Wo : role == 3 ? wqkv : role == 4 ? woA : Wq;
    if (role == 0) P = role_chain(xq * 8 + (t >> 1), t & 1, smem);
    else if (role == 1) P = role_h(layer, xq * 8 + t - 16, smem);
    else if (role == 2) P = role_w(layer, ix / 3, ix % 3, smem);
    else if (role == 3) P = role_a1(layer, ix, smem);
    else if (role == 4) P = role_a2(layer ^ (MAP >= 2), ix, smem);
    if (role == 0) P.woff = (u32)t_in;
    const int wave = __builtin_amdgcn_readfirstlane(threadIdx.x >> 6);
    asm volatile(
    "s_cmp_eq_u32 %13, 1\n"
    "s_cbranch_scc1 .Lrole1_%=\n"
    "s_cmp_eq_u32 %13, 2\n"
    "s_cbranch_scc1 .Lrole2_%=\n"
    "s_cmp_eq_u32 %13, 3\n"
    "s_cbranch_scc1 .Lrole3_%=\n"
    "s_cmp_eq_u32 %13, 4\n"
    "s_cbranch_scc1 .Lrole4_%=\n"
    "s_cmp_eq_u32 %13, 5\n"
    "s_cbranch_scc1 .Lrole5_%=\n"
    "s_mov_b64 s[40:41], %0\n"
    "s_and_b32 s41, s41, 0xffff\n"
    "s_mov_b32 s42, 0xc000000\n"
    "s_mov_b32 s43, 0x20000\n"
    "s_mov_b32 s57, 131072\n"
    "v_mbcnt_lo_u32_b32 v10, -1, 0\n"
    "v_mbcnt_hi_u32_b32 v10, -1, v10\n"
    "s_lshl_b32 s73, %2, 8\n"
    "s_add_u32 s73, s73, 147456\n"
    "v_lshl_add_u32 v10, v10, 2, s73\n"
    "ds_read_b32 v0, v10\n"
    "ds_read_b32 v2, v10 offset:1024\n"
    "ds_read_b32 v1, v10 offset:2048\n"
    "ds_read_b32 v3, v10 offset:3072\n"
    "ds_read_b32 v4, v10 offset:4096\n"
    "ds_read_b32 v5, v10 offset:5120\n"
    "ds_read_b32 v6, v10 offset:6144\n"
    "ds_read_b32 v8, v10 offset:7168\n"
    "ds_read_b32 v9, v10 offset:8192\n"
    "s_waitcnt lgkmcnt(0)\n"
    "s_barrier\n"
    "s_xor_b32 s73, %2, 0\n"
    "s_lshl_b32 s73, s73, 12\n"
    "v_add_u32 v96, s73, v3\n"
    "v_add_u32 v97, s73, v4\n"
    "s_xor_b32 s73, %2, 1\n"
    "s_lshl_b32 s73, s73, 12\n"
    "v_add_u32 v98, s73, v3\n"
    "v_add_u32 v99, s73, v4\n"
    "s_xor_b32 s73, %2, 2\n"
    "s_lshl_b32 s73, s73, 12\n"
    "v_add_u32 v100, s73, v3\n"
    "v_add_u32 v101, s73, v4\n"
    "s_xor_b32 s73, %2, 3\n"
    "s_lshl_b32 s73, s73, 12\n"
    "v_add_u32 v102, s73, v3\n"
    "v_add_u32 v103, s73, v4\n"
    "s_lshl_b32 s60, %4, 8\n"
    "s_lshl_b32 s61, %2, 10\n"
    "s_mul_i32 s63, %4, 147456\n"
    "s_mul_i32 s62, %5, 576\n"
    "s_add_u32 s63, s63, s62\n"
    "s_mov_b64 s[44:45], %11\n"
    "s_and_b32 s45, s45, 0xffff\n"
    "s_mov_b32 s46, 0x480000\n"
    "s_mov_b32 s47, 0x20000\n"
    "s_lshl_b32 s73, s61, 1\n"
    "s_add_u32 m0, s73, 0\n"
    "s_add_u32 s62, s63, 512\n"
    "buffer_load_dwordx4 v8, s[44:47], s62 offen lds\n"
    "s_lshl_b32 s73, s61, 1\n"
    "s_add_u32 m0, s73, 1024\n"
    "s_add_u32 s62, s63, 18944\n"
    "buffer_load_dwordx4 v8, s[44:47], s62 offen lds\n"
    "s_mov_b64 vcc, 0\n"
    "s_mov_b32 vcc_lo, %7\n"
    "s_cbranch_vccz .Lsl0_%=\n"
    "s_sleep 6\n"
    ".Lsl0_%=:\n"
    "s_mov_b64 s[44:45], %8\n"
    "s_and_b32 s45, s45, 0xffff\n"
    "s_mov_b32 s46, 0x480000\n"
    "s_mov_b32 s47, 0x20000\n"
    "s_add_u32 m0, s61, 16384\n"
    "s_add_u32 s62, s60, 262144\n"
    "buffer_load_dwordx4 v1, s[44:47], s62 offen lds\n"
    "s_add_u32 m0, s61, 20480\n"
    "s_add_u32 s62, s60, 1310720\n"
    "buffer_load_dwordx4 v1, s[44:47], s62 offen lds\n"
    "s_add_u32 m0, s61, 24576\n"
    "s_add_u32 s62, s60, 2359296\n"
    "buffer_load_dwordx4 v1, s[44:47], s62 offen lds\n"
    "s_add_u32 m0, s61, 28672\n"
    "s_add_u32 s62, s60, 3407872\n"
    "buffer_load_dwordx4 v1, s[44:47], s62 offen lds\n"
    "s_cbranch_vccz .Lsl1_%=\n"
    "s_sleep 6\n"
    ".Lsl1_%=:\n"
    "s_add_u32 m0, s61, 32768\n"
    "s_add_u32 s62, s60, 786432\n"
    "buffer_load_dwordx4 v1, s[44:47], s62 offen lds\n"
    "s_add_u32 m0, s61, 36864\n"
    "s_add_u32 s62, s60, 1835008\n"
    "buffer_load_dwordx4 v1, s[44:47], s62 offen lds\n"
    "s_add_u32 m0, s61, 40960\n"
    "s_add_u32 s62, s60, 2883584\n"
    "buffer_load_dwordx4 v1, s[44:47], s62 offen lds\n"
    "s_add_u32 m0, s61, 45056\n"
    "s_add_u32 s62, s60, 3932160\n"
    "buffer_load_dwordx4 v1, s[44:47], s62 offen lds\n"
    "s_cbranch_vccz .Lsl2_%=\n"
    "s_sleep 6\n"
    ".Lsl2_%=:\n"
    "s_add_u32 m0, s61, 49152\n"
    "s_add_u32 s62, s60, 278528\n"
    "buffer_load_dwordx4 v1, s[44:47], s62 offen lds\n"
    "s_add_u32 m0, s61, 53248\n"
    "s_add_u32 s62, s60, 1327104\n"
    "buffer_load_dwordx4 v1, s[44:47], s62 offen lds\n"
    "s_add_u32 m0, s61, 57344\n"
    "s_add_u32 s62, s60, 2375680\n"
    "buffer_load_dwordx4 v1, s[44:47], s62 offen lds\n"
    "s_add_u32 m0, s61, 61440\n"
    "s_add_u32 s62, s60, 3424256\n"
    "buffer_load_dwordx4 v1, s[44:47], s62 offen lds\n"
    "s_cbranch_vccz .Lsl3_%=\n"
    "s_sleep 6\n"
    ".Lsl3_%=:\n"
    "s_add_u32 m0, s61, 65536\n"
    "s_add_u32 s62, s60, 802816\n"
    "buffer_load_dwordx4 v1, s[44:47], s62 offen lds\n"
    "s_add_u32 m0, s61, 69632\n"
    "s_add_u32 s62, s60, 1851392\n"
    "buffer_load_dwordx4 v1, s[44:47], s62 offen lds\n"
    "s_add_u32 m0, s61, 73728\n"
    "s_add_u32 s62, s60, 2899968\n"
    "buffer_load_dwordx4 v1, s[44:47], s62 offen lds\n"
    "s_add_u32 m0, s61, 77824\n"
    "s_add_u32 s62, s60, 3948544\n"
    "buffer_load_dwordx4 v1, s[44:47], s62 offen lds\n"
    "s_cbranch_vccz .Lsl4_%=\n"
    "s_sleep 6\n"
    ".Lsl4_%=:\n"
    "s_add_u32 m0, s61, 81920\n"
    "s_add_u32 s62, s60, 4194304\n"
    "buffer_load_dwordx4 v1, s[44:47], s62 offen lds\n"
    "s_add_u32 m0, s61, 86016\n"
    "s_add_u32 s62, s60, 4210688\n"
    "buffer_load_dwordx4 v1, s[44:47], s62 offen lds\n"
    "s_add_u32 m0, s61, 90112\n"
    "s_add_u32 s62, s60, 4456448\n"
    "buffer_load_dwordx4 v1, s[44:47], s62 offen lds\n"
    "s_add_u32 m0, s61, 94208\n"
    "s_add_u32 s62, s60, 4472832\n"
    "buffer_load_dwordx4 v1, s[44:47], s62 offen lds\n"
    "s_cbranch_vccz .Lsl5_%=\n"
    "s_sleep 6\n"
    ".Lsl5_%=:\n"
    "s_mov_b64 s[44:45], %9\n"
    "s_and_b32 s45, s45, 0xffff\n"
    "s_mov_b32 s46, 0x480000\n"
    "s_mov_b32 s47, 0x20000\n"
    "s_add_u32 m0, s61, 98304\n"
    "s_add_u32 s62, s60, 0\n"
    "buffer_load_dwordx4 v1, s[44:47], s62 offen lds\n"
    "s_add_u32 m0, s61, 102400\n"
    "s_add_u32 s62, s60, 1048576\n"
    "buffer_load_dwordx4 v1, s[44:47], s62 offen lds\n"
    "s_add_u32 m0, s61, 106496\n"
    "s_add_u32 s62, s60, 2097152\n"
    "buffer_load_dwordx4 v1, s[44:47], s62 offen lds\n"
    "s_add_u32 m0, s61, 110592\n"
    "s_add_u32 s62, s60, 3145728\n"
    "buffer_load_dwordx4 v1, s[44:47], s62 offen lds\n"
    "s_cbranch_vccz .Lsl6_%=\n"
    "s_sleep 6\n"
    ".Lsl6_%=:\n"
    "s_add_u32 m0, s61, 114688\n"
    "s_add_u32 s62, s60, 524288\n"
    "buffer_load_dwordx4 v1, s[44:47], s62 offen lds\n"
    "s_add_u32 m0, s61, 118784\n"
    "s_add_u32 s62, s60, 1572864\n"
    "buffer_load_dwordx4 v1, s[44:47], s62 offen lds\n"
    "s_add_u32 m0, s61, 122880\n"
    "s_add_u32 s62, s60, 2621440\n"
    "buffer_load_dwordx4 v1, s[44:47], s62 offen lds\n"
    "s_add_u32 m0, s61, 126976\n"
    "s_add_u32 s62, s60, 3670016\n"
    "buffer_load_dwordx4 v1, s[44:47], s62 offen lds\n"
    "s_cbranch_vccz .Lsl7_%=\n"
    "s_sleep 6\n"
    ".Lsl7_%=:\n"
    "s_add_u32 m0, s61, 131072\n"
    "s_add_u32 s62, s60, 16384\n"
    "buffer_load_dwordx4 v1, s[44:47], s62 offen lds\n"
    "s_add_u32 m0, s61, 135168\n"
    "s_add_u32 s62, s60, 1064960\n"
    "buffer_load_dwordx4 v1, s[44:47], s62 offen lds\n"
    "s_add_u32 m0, s61, 139264\n"
    "s_add_u32 s62, s60, 2113536\n"
    "buffer_load_dwordx4 v1, s[44:47], s62 offen lds\n"
    "s_add_u32 m0, s61, 143360\n"
    "s_add_u32 s62, s60, 3162112\n"
    "buffer_load_dwordx4 v1, s[44:47], s62 offen lds\n"
    "s_cbranch_vccz .Lsl8_%=\n"
    "s_sleep 6\n"
    ".Lsl8_%=:\n"
    "s_add_u32 m0, s61, 147456\n"
    "s_add_u32 s62, s60, 540672\n"
    "buffer_load_dwordx4 v1, s[44:47], s62 offen lds\n"
    "s_add_u32 m0, s61, 151552\n"
    "s_add_u32 s62, s60, 1589248\n"
    "buffer_load_dwordx4 v1, s[44:47], s62 offen lds\n"
    "s_add_u32 m0, s61, 155648\n"
    "s_add_u32 s62, s60, 2637824\n"
    "buffer_load_dwordx4 v1, s[44:47], s62 offen lds\n"
    "s_add_u32 m0, s61, 159744\n"
    "s_add_u32 s62, s60, 3686400\n"
    "buffer_load_dwordx4 v1, s[44:47], s62 offen lds\n"
    "s_waitcnt vmcnt(32) lgkmcnt(0)\n"
    "s_barrier\n"
    "ds_read_b64_tr_b16 v[36:37], v96 offset:16384\n"
    "ds_read_b64_tr_b16 v[68:69], v98 offset:16384\n"
    "ds_read_b64_tr_b16 v[148:149], v100 offset:16384\n"
    "ds_read_b64_tr_b16 v[180:181], v102 offset:16384\n"
    "ds_read_b64_tr_b16 v[44:45], v97 offset:16384\n"
    "ds_read_b64_tr_b16 v[76:77], v99 offset:16384\n"
    "ds_read_b64_tr_b16 v[156:157], v101 offset:16384\n"
    "ds_read_b64_tr_b16 v[188:189], v103 offset:16384\n"
    "s_waitcnt vmcnt(28) lgkmcnt(0)\n"
    "s_barrier\n"
    "s_cbranch_vccz .Lsl9_%=\n"
    "s_sleep 6\n"
    ".Lsl9_%=:\n"
    "s_add_u32 m0, s61, 16384\n"
    "s_add_u32 s62, s60, 262144\n"
    "buffer_load_dwordx4 v1, s[44:47], s62 offen lds\n"
    "s_add_u32 m0, s61, 20480\n"
    "s_add_u32 s62, s60, 1310720\n"
    "buffer_load_dwordx4 v1, s[44:47], s62 offen lds\n"
    "s_add_u32 m0, s61, 24576\n"
    "s_add_u32 s62, s60, 2359296\n"
    "buffer_load_dwordx4 v1, s[44:47], s62 offen lds\n"
    "s_add_u32 m0, s61, 28672\n"
    "s_add_u32 s62, s60, 3407872\n"
    "buffer_load_dwordx4 v1, s[44:47], s62 offen lds\n"
    "ds_read_b64_tr_b16 v[52:53], v96 offset:32768\n"
    "ds_read_b64_tr_b16 v[84:85], v98 offset:32768\n"
    "ds_read_b64_tr_b16 v[164:165], v100 offset:32768\n"
    "ds_read_b64_tr_b16 v[196:197], v102 offset:32768\n"
    "ds_read_b64_tr_b16 v[60:61], v97 offset:32768\n"
    "ds_read_b64_tr_b16 v[140:141], v99 offset:32768\n"
    "ds_read_b64_tr_b16 v[172:173], v101 offset:32768\n"
    "ds_read_b64_tr_b16 v[204:205], v103 offset:32768\n"
    "s_waitcnt vmcnt(28) lgkmcnt(0)\n"
    "s_barrier\n"
    "s_cbranch_vccz .Lsl10_%=\n"
    "s_sleep 6\n"
    ".Lsl10_%=:\n"
    "s_add_u32 m0, s61, 32768\n"
    "s_add_u32 s62, s60, 786432\n"
    "buffer_load_dwordx4 v1, s[44:47], s62 offen lds\n"
    "s_add_u32 m0, s61, 36864\n"
    "s_add_u32 s62, s60, 1835008\n"
    "buffer_load_dwordx4 v1, s[44:47], s62 offen lds\n"
    "s_add_u32 m0, s61, 40960\n"
    "s_add_u32 s62, s60, 2883584\n"
    "buffer_load_dwordx4 v1, s[44:47], s62 offen lds\n"
    "s_add_u32 m0, s61, 45056\n"
    "s_add_u32 s62, s60, 3932160\n"
    "buffer_load_dwordx4 v1, s[44:47], s62 offen lds\n"
    "ds_read_b64_tr_b16 v[38:39], v96 offset:49152\n"
    "ds_read_b64_tr_b16 v[70:71], v98 offset:49152\n"
    "ds_read_b64_tr_b16 v[150:151], v100 offset:49152\n"
    "ds_read_b64_tr_b16 v[182:183], v102 offset:49152\n"
    "ds_read_b64_tr_b16 v[46:47], v97 offset:49152\n"
    "ds_read_b64_tr_b16 v[78:79], v99 offset:49152\n"
    "ds_read_b64_tr_b16 v[158:159], v101 offset:49152\n"
    "ds_read_b64_tr_b16 v[190:191], v103 offset:49152\n"
    "s_waitcnt vmcnt(28) lgkmcnt(0)\n"
    "s_barrier\n"
    "s_cbranch_vccz .Lsl11_%=\n"
    "s_sleep 6\n"
    ".Lsl11_%=:\n"
    "s_add_u32 m0, s61, 49152\n"
    "s_add_u32 s62, s60, 278528\n"
    "buffer_load_dwordx4 v1, s[44:47], s62 offen lds\n"
    "s_add_u32 m0, s61, 53248\n"
    "s_add_u32 s62, s60, 1327104\n"
    "buffer_load_dwordx4 v1, s[44:47], s62 offen lds\n"
    "s_add_u32 m0, s61, 57344\n"
    "s_add_u32 s62, s60, 2375680\n"
    "buffer_load_dwordx4 v1, s[44:47], s62 offen lds\n"
    "s_add_u32 m0, s61, 61440\n"
    "s_add_u32 s62, s60, 3424256\n"
    "buffer_load_dwordx4 v1, s[44:47], s62 offen lds\n"
    "v_add_u32 v127, 0x10000, v96\n"
    "ds_read_b64_tr_b16 v[54:55], v127\n"
    "v_add_u32 v126, 0x10000, v98\n"
    "ds_read_b64_tr_b16 v[86:87], v126\n"
    "v_add_u32 v125, 0x10000, v100\n"
    "ds_read_b64_tr_b16 v[166:167], v125\n"
    "v_add_u32 v124, 0x10000, v102\n"
    "ds_read_b64_tr_b16 v[198:199], v124\n"
    "v_add_u32 v123, 0x10000, v97\n"
    "ds_read_b64_tr_b16 v[62:63], v123\n"
    "v_add_u32 v122, 0x10000, v99\n"
    "ds_read_b64_tr_b16 v[142:143], v122\n"
    "v_add_u32 v121, 0x10000, v101\n"
    "ds_read_b64_tr_b16 v[174:175], v121\n"
    "v_add_u32 v120, 0x10000, v103\n"
    "ds_read_b64_tr_b16 v[206:207], v120\n"
    "s_waitcnt vmcnt(28) lgkmcnt(0)\n"
    "s_barrier\n"
    "s_cbranch_vccz .Lsl12_%=\n"
    "s_sleep 6\n"
    ".Lsl12_%=:\n"
    "s_add_u32 m0, s61, 65536\n"
    "s_add_u32 s62, s60, 802816\n"
    "buffer_load_dwordx4 v1, s[44:47], s62 offen lds\n"
    "s_add_u32 m0, s61, 69632\n"
    "s_add_u32 s62, s60, 1851392\n"
    "buffer_load_dwordx4 v1, s[44:47], s62 offen lds\n"
    "s_add_u32 m0, s61, 73728\n"
    "s_add_u32 s62, s60, 2899968\n"
    "buffer_load_dwordx4 v1, s[44:47], s62 offen lds\n"
    "s_add_u32 m0, s61, 77824\n"
    "s_add_u32 s62, s60, 3948544\n"
    "buffer_load_dwordx4 v1, s[44:47], s62 offen lds\n"
    "v_add_u32 v119, 0x10000, v3\n"
    "ds_read_b64_tr_b16 v[208:209], v119 offset:16384\n"
    "ds_read_b64_tr_b16 v[210:211], v119 offset:20480\n"
    "ds_read_b64_tr_b16 v[212:213], v119 offset:24576\n"
    "ds_read_b64_tr_b16 v[214:215], v119 offset:28672\n"
    "v_add_u32 v118, 0x10000, v4\n"
    "ds_read_b64_tr_b16 v[216:217], v118 offset:16384\n"
    "ds_read_b64_tr_b16 v[218:219], v118 offset:20480\n"
    "ds_read_b64_tr_b16 v[220:221], v118 offset:24576\n"
    "ds_read_b64_tr_b16 v[222:223], v118 offset:28672\n"
    "s_waitcnt vmcnt(28) lgkmcnt(0)\n"
    "s_barrier\n"
    "s_cbranch_vccz .Lsl13_%=\n"
    "s_sleep 6\n"
    ".Lsl13_%=:\n"
    "s_add_u32 m0, s61, 81920\n"
    "s_add_u32 s62, s60, 4194304\n"
    "buffer_load_dwordx4 v1, s[44:47], s62 offen lds\n"
    "s_add_u32 m0, s61, 86016\n"
    "s_add_u32 s62, s60, 4210688\n"
    "buffer_load_dwordx4 v1, s[44:47], s62 offen lds\n"
    "s_add_u32 m0, s61, 90112\n"
    "s_add_u32 s62, s60, 4456448\n"
    "buffer_load_dwordx4 v1, s[44:47], s62 offen lds\n"
    "s_add_u32 m0, s61, 94208\n"
    "s_add_u32 s62, s60, 4472832\n"
    "buffer_load_dwordx4 v1, s[44:47], s62 offen lds\n"
    "ds_read_b64_tr_b16 v[224:225], v127 offset:32768\n"
    "ds_read_b64_tr_b16 a[0:1], v126 offset:32768\n"
    "ds_read_b64_tr_b16 a[32:33], v125 offset:32768\n"
    "ds_read_b64_tr_b16 a[64:65], v124 offset:32768\n"
    "ds_read_b64_tr_b16 v[232:233], v123 offset:32768\n"
    "ds_read_b64_tr_b16 a[8:9], v122 offset:32768\n"
    "ds_read_b64_tr_b16 a[40:41], v121 offset:32768\n"
    "ds_read_b64_tr_b16 a[72:73], v120 offset:32768\n"
    "s_waitcnt vmcnt(28) lgkmcnt(0)\n"
    "s_barrier\n"
    "s_cbranch_vccz .Lsl14_%=\n"
    "s_sleep 6\n"
    ".Lsl14_%=:\n"
    "s_mov_b64 s[44:45], %10\n"
    "s_and_b32 s45, s45, 0xffff\n"
    "s_mov_b32 s46, 0x480000\n"
    "s_mov_b32 s47, 0x20000\n"
    "s_add_u32 m0, s61, 98304\n"
    "s_add_u32 s62, s60, 0\n"
    "buffer_load_dwordx4 v1, s[44:47], s62 offen lds\n"
    "s_add_u32 m0, s61, 102400\n"
    "s_add_u32 s62, s60, 1048576\n"
    "buffer_load_dwordx4 v1, s[44:47], s62 offen lds\n"
    "s_add_u32 m0, s61, 106496\n"
    "s_add_u32 s62, s60, 2097152\n"
    "buffer_load_dwordx4 v1, s[44:47], s62 offen lds\n"
    "s_add_u32 m0, s61, 110592\n"
    "s_add_u32 s62, s60, 3145728\n"
    "buffer_load_dwordx4 v1, s[44:47], s62 offen lds\n"
    "ds_read_b64_tr_b16 v[240:241], v127 offset:49152\n"
    "ds_read_b64_tr_b16 a[16:17], v126 offset:49152\n"
    "ds_read_b64_tr_b16 a[48:49], v125 offset:49152\n"
    "ds_read_b64_tr_b16 a[80:81], v124 offset:49152\n"
    "ds_read_b64_tr_b16 v[248:249], v123 offset:49152\n"
    "ds_read_b64_tr_b16 a[24:25], v122 offset:49152\n"
    "ds_read_b64_tr_b16 a[56:57], v121 offset:49152\n"
    "ds_read_b64_tr_b16 a[88:89], v120 offset:49152\n"
    "s_waitcnt vmcnt(28) lgkmcnt(0)\n"
    "s_barrier\n"
    "s_cbranch_vccz .Lsl15_%=\n"
    "s_sleep 6\n"
    ".Lsl15_%=:\n"
    "s_add_u32 m0, s61, 114688\n"
    "s_add_u32 s62, s60, 524288\n"
    "buffer_load_dwordx4 v1, s[44:47], s62 offen lds\n"
    "s_add_u32 m0, s61, 118784\n"
    "s_add_u32 s62, s60, 1572864\n"
    "buffer_load_dwordx4 v1, s[44:47], s62 offen lds\n"
    "s_add_u32 m0, s61, 122880\n"
    "s_add_u32 s62, s60, 2621440\n"
    "buffer_load_dwordx4 v1, s[44:47], s62 offen lds\n"
    "s_add_u32 m0, s61, 126976\n"
    "s_add_u32 s62, s60, 3670016\n"
    "buffer_load_dwordx4 v1, s[44:47], s62 offen lds\n"
    "v_add_u32 v117, 0x20000, v96\n"
    "ds_read_b64_tr_b16 v[226:227], v117\n"
    "v_add_u32 v116, 0x20000, v98\n"
    "ds_read_b64_tr_b16 a[2:3], v116\n"
    "v_add_u32 v115, 0x20000, v100\n"
    "ds_read_b64_tr_b16 a[34:35], v115\n"
    "v_add_u32 v114, 0x20000, v102\n"
    "ds_read_b64_tr_b16 a[66:67], v114\n"
    "v_add_u32 v113, 0x20000, v97\n"
    "ds_read_b64_tr_b16 v[234:235], v113\n"
    "v_add_u32 v112, 0x20000, v99\n"
    "ds_read_b64_tr_b16 a[10:11], v112\n"
    "v_add_u32 v111, 0x20000, v101\n"
    "ds_read_b64_tr_b16 a[42:43], v111\n"
    "v_add_u32 v110, 0x20000, v103\n"
    "ds_read_b64_tr_b16 a[74:75], v110\n"
    "s_waitcnt vmcnt(28) lgkmcnt(0)\n"
    "s_barrier\n"
    "s_cbranch_vccz .Lsl16_%=\n"
    "s_sleep 6\n"
    ".Lsl16_%=:\n"
    "s_add_u32 m0, s61, 131072\n"
    "s_add_u32 s62, s60, 16384\n"
    "buffer_load_dwordx4 v1, s[44:47], s62 offen lds\n"
    "s_add_u32 m0, s61, 135168\n"
    "s_add_u32 s62, s60, 1064960\n"
    "buffer_load_dwordx4 v1, s[44:47], s62 offen lds\n"
    "s_add_u32 m0, s61, 139264\n"
    "s_add_u32 s62, s60, 2113536\n"
    "buffer_load_dwordx4 v1, s[44:47], s62 offen lds\n"
    "s_add_u32 m0, s61, 143360\n"
    "s_add_u32 s62, s60, 3162112\n"
    "buffer_load_dwordx4 v1, s[44:47], s62 offen lds\n"
    "ds_read_b64_tr_b16 v[242:243], v117 offset:16384\n"
    "ds_read_b64_tr_b16 a[18:19], v116 offset:16384\n"
    "ds_read_b64_tr_b16 a[50:51], v115 offset:16384\n"
    "ds_read_b64_tr_b16 a[82:83], v114 offset:16384\n"
    "ds_read_b64_tr_b16 v[250:251], v113 offset:16384\n"
    "ds_read_b64_tr_b16 a[26:27], v112 offset:16384\n"
    "ds_read_b64_tr_b16 a[58:59], v111 offset:16384\n"
    "ds_read_b64_tr_b16 a[90:91], v110 offset:16384\n"
    "s_waitcnt vmcnt(28) lgkmcnt(0)\n"
    "s_barrier\n"
    "s_cbranch_vccz .Lsl17_%=\n"
    "s_sleep 6\n"
    ".Lsl17_%=:\n"
    "s_add_u32 m0, s61, 147456\n"
    "s_add_u32 s62, s60, 540672\n"
    "buffer_load_dwordx4 v1, s[44:47], s62 offen lds\n"
    "s_add_u32 m0, s61, 151552\n"
    "s_add_u32 s62, s60, 1589248\n"
    "buffer_load_dwordx4 v1, s[44:47], s62 offen lds\n"
    "s_add_u32 m0, s61, 155648\n"
    "s_add_u32 s62, s60, 2637824\n"
    "buffer_load_dwordx4 v1, s[44:47], s62 offen lds\n"
    "s_add_u32 m0, s61, 159744\n"
    "s_add_u32 s62, s60, 3686400\n"
    "buffer_load_dwordx4 v1, s[44:47], s62 offen lds\n"
    "ds_read_b64_tr_b16 v[228:229], v96 offset:16384\n"
    "ds_read_b64_tr_b16 a[4:5], v98 offset:16384\n"
    "ds_read_b64_tr_b16 a[36:37], v100 offset:16384\n"
    "ds_read_b64_tr_b16 a[68:69], v102 offset:16384\n"
    "ds_read_b64_tr_b16 v[236:237], v97 offset:16384\n"
    "ds_read_b64_tr_b16 a[12:13], v99 offset:16384\n"
    "ds_read_b64_tr_b16 a[44:45], v101 offset:16384\n"
    "ds_read_b64_tr_b16 a[76:77], v103 offset:16384\n"
    "s_waitcnt vmcnt(28) lgkmcnt(0)\n"
    "s_barrier\n"
    "s_cbranch_vccz .Lsl18_%=\n"
    "s_sleep 6\n"
    ".Lsl18_%=:\n"
    "s_add_u32 m0, s61, 16384\n"
    "s_add_u32 s62, s60, 262144\n"
    "buffer_load_dwordx4 v1, s[44:47], s62 offen lds\n"
    "s_add_u32 m0, s61, 20480\n"
    "s_add_u32 s62, s60, 1310720\n"
    "buffer_load_dwordx4 v1, s[44:47], s62 offen lds\n"
    "s_add_u32 m0, s61, 24576\n"
    "s_add_u32 s62, s60, 2359296\n"
    "buffer_load_dwordx4 v1, s[44:47], s62 offen lds\n"
    "s_add_u32 m0, s61, 28672\n"
    "s_add_u32 s62, s60, 3407872\n"
    "buffer_load_dwordx4 v1, s[44:47], s62 offen lds\n"
    "ds_read_b64_tr_b16 v[244:245], v96 offset:32768\n"
    "ds_read_b64_tr_b16 a[20:21], v98 offset:32768\n"
    "ds_read_b64_tr_b16 a[52:53], v100 offset:32768\n"
    "ds_read_b64_tr_b16 a[84:85], v102 offset:32768\n"
    "ds_read_b64_tr_b16 v[252:253], v97 offset:32768\n"
    "ds_read_b64_tr_b16 a[28:29], v99 offset:32768\n"
    "ds_read_b64_tr_b16 a[60:61], v101 offset:32768\n"
    "ds_read_b64_tr_b16 a[92:93], v103 offset:32768\n"
    "s_waitcnt vmcnt(28) lgkmcnt(0)\n"
    "s_barrier\n"
    "s_cbranch_vccz .Lsl19_%=\n"
    "s_sleep 6\n"
    ".Lsl19_%=:\n"
    "s_add_u32 m0, s61, 32768\n"
    "s_add_u32 s62, s60, 786432\n"
    "buffer_load_dwordx4 v1, s[44:47], s62 offen lds\n"
    "s_add_u32 m0, s61, 36864\n"
    "s_add_u32 s62, s60, 1835008\n"
    "buffer_load_dwordx4 v1, s[44:47], s62 offen lds\n"
    "s_add_u32 m0, s61, 40960\n"
    "s_add_u32 s62, s60, 2883584\n"
    "buffer_load_dwordx4 v1, s[44:47], s62 offen lds\n"
    "s_add_u32 m0, s61, 45056\n"
    "s_add_u32 s62, s60, 3932160\n"
    "buffer_load_dwordx4 v1, s[44:47], s62 offen lds\n"
    "ds_read_b64_tr_b16 v[230:231], v96 offset:49152\n"
    "ds_read_b64_tr_b16 a[6:7], v98 offset:49152\n"
    "ds_read_b64_tr_b16 a[38:39], v100 offset:49152\n"
    "ds_read_b64_tr_b16 a[70:71], v102 offset:49152\n"
    "ds_read_b64_tr_b16 v[238:239], v97 offset:49152\n"
    "ds_read_b64_tr_b16 a[14:15], v99 offset:49152\n"
    "ds_read_b64_tr_b16 a[46:47], v101 offset:49152\n"
    "ds_read_b64_tr_b16 a[78:79], v103 offset:49152\n"
    "s_waitcnt vmcnt(28) lgkmcnt(0)\n"
    "s_barrier\n"
    "s_cbranch_vccz .Lsl20_%=\n"
    "s_sleep 6\n"
    ".Lsl20_%=:\n"
    "s_add_u32 m0, s61, 49152\n"
    "s_add_u32 s62, s60, 278528\n"
    "buffer_load_dwordx4 v1, s[44:47], s62 offen lds\n"
    "s_add_u32 m0, s61, 53248\n"
    "s_add_u32 s62, s60, 1327104\n"
    "buffer_load_dwordx4 v1, s[44:47], s62 offen lds\n"
    "s_add_u32 m0, s61, 57344\n"
    "s_add_u32 s62, s60, 2375680\n"
    "buffer_load_dwordx4 v1, s[44:47], s62 offen lds\n"
    "s_add_u32 m0, s61, 61440\n"
    "s_add_u32 s62, s60, 3424256\n"
    "buffer_load_dwordx4 v1, s[44:47], s62 offen lds\n"
    "ds_read_b64_tr_b16 v[246:247], v127\n"
    "ds_read_b64_tr_b16 a[22:23], v126\n"
    "ds_read_b64_tr_b16 a[54:55], v125\n"
    "ds_read_b64_tr_b16 a[86:87], v124\n"
    "ds_read_b64_tr_b16 v[254:255], v123\n"
    "ds_read_b64_tr_b16 a[30:31], v122\n"
    "ds_read_b64_tr_b16 a[62:63], v121\n"
    "ds_read_b64_tr_b16 a[94:95], v120\n"
    "s_waitcnt vmcnt(28) lgkmcnt(0)\n"
    "s_barrier\n"
    "s_cbranch_vccz .Lsl21_%=\n"
    "s_sleep 6\n"
    ".Lsl21_%=:\n"
    "s_add_u32 m0, s61, 65536\n"
    "s_add_u32 s62, s60, 802816\n"
    "buffer_load_dwordx4 v1, s[44:47], s62 offen lds\n"
    "s_add_u32 m0, s61, 69632\n"
    "s_add_u32 s62, s60, 1851392\n"
    "buffer_load_dwordx4 v1, s[44:47], s62 offen lds\n"
    "s_add_u32 m0, s61, 73728\n"
    "s_add_u32 s62, s60, 2899968\n"
    "buffer_load_dwordx4 v1, s[44:47], s62 offen lds\n"
    "s_add_u32 m0, s61, 77824\n"
    "s_add_u32 s62, s60, 3948544\n"
    "buffer_load_dwordx4 v1, s[44:47], s62 offen lds\n"
    "ds_read_b64_tr_b16 a[96:97], v119 offset:16384\n"
    "ds_read_b64_tr_b16 a[98:99], v119 offset:20480\n"
    "ds_read_b64_tr_b16 a[100:101], v119 offset:24576\n"
    "ds_read_b64_tr_b16 a[102:103], v119 offset:28672\n"
    "ds_read_b64_tr_b16 a[104:105], v118 offset:16384\n"
    "ds_read_b64_tr_b16 a[106:107], v118 offset:20480\n"
    "ds_read_b64_tr_b16 a[108:109], v118 offset:24576\n"
    "ds_read_b64_tr_b16 a[110:111], v118 offset:28672\n"
    "s_waitcnt vmcnt(28) lgkmcnt(0)\n"
    "s_barrier\n"
    "s_cbranch_vccz .Lsl22_%=\n"
    "s_sleep 6\n"
    ".Lsl22_%=:\n"
    "s_add_u32 m0, s61, 81920\n"
    "s_add_u32 s62, s60, 4194304\n"
    "buffer_load_dwordx4 v1, s[44:47], s62 offen lds\n"
    "s_add_u32 m0, s61, 86016\n"
    "s_add_u32 s62, s60, 4210688\n"
    "buffer_load_dwordx4 v1, s[44:47], s62 offen lds\n"
    "s_add_u32 m0, s61, 90112\n"
    "s_add_u32 s62, s60, 4456448\n"
    "buffer_load_dwordx4 v1, s[44:47], s62 offen lds\n"
    "s_add_u32 m0, s61, 94208\n"
    "s_add_u32 s62, s60, 4472832\n"
    "buffer_load_dwordx4 v1, s[44:47], s62 offen lds\n"
    "ds_read_b64_tr_b16 a[112:113], v127 offset:32768\n"
    "ds_read_b64_tr_b16 a[144:145], v126 offset:32768\n"
    "ds_read_b64_tr_b16 a[176:177], v125 offset:32768\n"
    "ds_read_b64_tr_b16 a[208:209], v124 offset:32768\n"
    "ds_read_b64_tr_b16 a[120:121], v123 offset:32768\n"
    "ds_read_b64_tr_b16 a[152:153], v122 offset:32768\n"
    "ds_read_b64_tr_b16 a[184:185], v121 offset:32768\n"
    "ds_read_b64_tr_b16 a[216:217], v120 offset:32768\n"
    "s_waitcnt vmcnt(28) lgkmcnt(0)\n"
    "s_barrier\n"
    "s_cbranch_vccz .Lsl23_%=\n"
    "s_sleep 6\n"
    ".Lsl23_%=:\n"
    "s_mov_b64 s[44:45], %8\n"
    "s_and_b32 s45, s45, 0xffff\n"
    "s_mov_b32 s46, 0x480000\n"
    "s_mov_b32 s47, 0x20000\n"
    "s_add_u32 m0, s61, 98304\n"
    "s_add_u32 s62, s60, 0\n"
    "buffer_load_dwordx4 v1, s[44:47], s62 offen lds\n"
    "s_add_u32 m0, s61, 102400\n"
    "s_add_u32 s62, s60, 1048576\n"
    "buffer_load_dwordx4 v1, s[44:47], s62 offen lds\n"
    "s_add_u32 m0, s61, 106496\n"
    "s_add_u32 s62, s60, 2097152\n"
    "buffer_load_dwordx4 v1, s[44:47], s62 offen lds\n"
    "s_add_u32 m0, s61, 110592\n"
    "s_add_u32 s62, s60, 3145728\n"
    "buffer_load_dwordx4 v1, s[44:47], s62 offen lds\n"
    "ds_read_b64_tr_b16 a[128:129], v127 offset:49152\n"
    "ds_read_b64_tr_b16 a[160:161], v126 offset:49152\n"
    "ds_read_b64_tr_b16 a[192:193], v125 offset:49152\n"
    "ds_read_b64_tr_b16 a[224:225], v124 offset:49152\n"
    "ds_read_b64_tr_b16 a[136:137], v123 offset:49152\n"
    "ds_read_b64_tr_b16 a[168:169], v122 offset:49152\n"
    "ds_read_b64_tr_b16 a[200:201], v121 offset:49152\n"
    "ds_read_b64_tr_b16 a[232:233], v120 offset:49152\n"
    "s_waitcnt vmcnt(28) lgkmcnt(0)\n"
    "s_barrier\n"
    "s_cbranch_vccz .Lsl24_%=\n"
    "s_sleep 6\n"
    ".Lsl24_%=:\n"
    "s_add_u32 m0, s61, 114688\n"
    "s_add_u32 s62, s60, 524288\n"
    "buffer_load_dwordx4 v1, s[44:47], s62 offen lds\n"
    "s_add_u32 m0, s61, 118784\n"
    "s_add_u32 s62, s60, 1572864\n"
    "buffer_load_dwordx4 v1, s[44:47], s62 offen lds\n"
    "s_add_u32 m0, s61, 122880\n"
    "s_add_u32 s62, s60, 2621440\n"
    "buffer_load_dwordx4 v1, s[44:47], s62 offen lds\n"
    "s_add_u32 m0, s61, 126976\n"
    "s_add_u32 s62, s60, 3670016\n"
    "buffer_load_dwordx4 v1, s[44:47], s62 offen lds\n"
    "ds_read_b64_tr_b16 a[114:115], v117\n"
    "ds_read_b64_tr_b16 a[146:147], v116\n"
    "ds_read_b64_tr_b16 a[178:179], v115\n"
    "ds_read_b64_tr_b16 a[210:211], v114\n"
    "ds_read_b64_tr_b16 a[122:123], v113\n"
    "ds_read_b64_tr_b16 a[154:155], v112\n"
    "ds_read_b64_tr_b16 a[186:187], v111\n"
    "ds_read_b64_tr_b16 a[218:219], v110\n"
    "s_waitcnt vmcnt(28) lgkmcnt(0)\n"
    "s_barrier\n"
    "s_cbranch_vccz .Lsl25_%=\n"
    "s_sleep 6\n"
    ".Lsl25_%=:\n"
    "s_add_u32 m0, s61, 131072\n"
    "s_add_u32 s62, s60, 16384\n"
    "buffer_load_dwordx4 v1, s[44:47], s62 offen lds\n"
    "s_add_u32 m0, s61, 135168\n"
    "s_add_u32 s62, s60, 1064960\n"
    "buffer_load_dwordx4 v1, s[44:47], s62 offen lds\n"
    "s_add_u32 m0, s61, 139264\n"
    "s_add_u32 s62, s60, 2113536\n"
    "buffer_load_dwordx4 v1, s[44:47], s62 offen lds\n"
    "s_add_u32 m0, s61, 143360\n"
    "s_add_u32 s62, s60, 3162112\n"
    "buffer_load_dwordx4 v1, s[44:47], s62 offen lds\n"
    "ds_read_b64_tr_b16 a[130:131], v117 offset:16384\n"
    "ds_read_b64_tr_b16 a[162:163], v116 offset:16384\n"
    "ds_read_b64_tr_b16 a[194:195], v115 offset:16384\n"
    "ds_read_b64_tr_b16 a[226:227], v114 offset:16384\n"
    "ds_read_b64_tr_b16 a[138:139], v113 offset:16384\n"
    "ds_read_b64_tr_b16 a[170:171], v112 offset:16384\n"
    "ds_read_b64_tr_b16 a[202:203], v111 offset:16384\n"
    "ds_read_b64_tr_b16 a[234:235], v110 offset:16384\n"
    "s_waitcnt vmcnt(28) lgkmcnt(0)\n"
    "s_barrier\n"
    "s_cbranch_vccz .Lsl26_%=\n"
    "s_sleep 6\n"
    ".Lsl26_%=:\n"
    "s_add_u32 m0, s61, 147456\n"
    "s_add_u32 s62, s60, 540672\n"
    "buffer_load_dwordx4 v1, s[44:47], s62 offen lds\n"
    "s_add_u32 m0, s61, 151552\n"
    "s_add_u32 s62, s60, 1589248\n"
    "buffer_load_dwordx4 v1, s[44:47], s62 offen lds\n"
    "s_add_u32 m0, s61, 155648\n"
    "s_add_u32 s62, s60, 2637824\n"
    "buffer_load_dwordx4 v1, s[44:47], s62 offen lds\n"
    "s_add_u32 m0, s61, 159744\n"
    "s_add_u32 s62, s60, 3686400\n"
    "buffer_load_dwordx4 v1, s[44:47], s62 offen lds\n"
    "ds_read_b64_tr_b16 a[116:117], v96 offset:16384\n"
    "ds_read_b64_tr_b16 a[148:149], v98 offset:16384\n"
    "ds_read_b64_tr_b16 a[180:181], v100 offset:16384\n"
    "ds_read_b64_tr_b16 a[212:213], v102 offset:16384\n"
    "ds_read_b64_tr_b16 a[124:125], v97 offset:16384\n"
    "ds_read_b64_tr_b16 a[156:157], v99 offset:16384\n"
    "ds_read_b64_tr_b16 a[188:189], v101 offset:16384\n"
    "ds_read_b64_tr_b16 a[220:221], v103 offset:16384\n"
    "s_waitcnt vmcnt(28) lgkmcnt(0)\n"
    "s_barrier\n"
    "s_cbranch_vccz .Lsl27_%=\n"
    "s_sleep 6\n"
    ".Lsl27_%=:\n"
    "s_mov_b64 s[44:45], %11\n"
    "s_and_b32 s45, s45, 0xffff\n"
    "s_mov_b32 s46, 0x480000\n"
    "s_mov_b32 s47, 0x20000\n"
    "s_add_u32 m0, s61, 16384\n"
    "s_add_u32 s62, s63, 0\n"
    "buffer_load_dwordx4 v5, s[44:47], s62 offen lds\n"
    "s_add_u32 m0, s61, 20480\n"
    "s_add_u32 s62, s63, 9216\n"
    "buffer_load_dwordx4 v5, s[44:47], s62 offen lds\n"
    "s_add_u32 m0, s61, 24576\n"
    "s_add_u32 s62, s63, 18432\n"
    "buffer_load_dwordx4 v5, s[44:47], s62 offen lds\n"
    "s_add_u32 m0, s61, 28672\n"
    "s_add_u32 s62, s63, 27648\n"
    "buffer_load_dwordx4 v5, s[44:47], s62 offen lds\n"
    "ds_read_b64_tr_b16 a[132:133], v96 offset:32768\n"
    "ds_read_b64_tr_b16 a[164:165], v98 offset:32768\n"
    "ds_read_b64_tr_b16 a[196:197], v100 offset:32768\n"
    "ds_read_b64_tr_b16 a[228:229], v102 offset:32768\n"
    "ds_read_b64_tr_b16 a[140:141], v97 offset:32768\n"
    "ds_read_b64_tr_b16 a[172:173], v99 offset:32768\n"
    "ds_read_b64_tr_b16 a[204:205], v101 offset:32768\n"
    "ds_read_b64_tr_b16 a[236:237], v103 offset:32768\n"
    "s_waitcnt vmcnt(28) lgkmcnt(0)\n"
    "s_barrier\n"
    "s_cbranch_vccz .Lsl28_%=\n"
    "s_sleep 6\n"
    ".Lsl28_%=:\n"
    "s_add_u32 m0, s61, 32768\n"
    "s_add_u32 s62, s63, 36864\n"
    "buffer_load_dwordx4 v5, s[44:47], s62 offen lds\n"
    "s_add_u32 m0, s61, 36864\n"
    "s_add_u32 s62, s63, 46080\n"
    "buffer_load_dwordx4 v5, s[44:47], s62 offen lds\n"
    "s_add_u32 m0, s61, 40960\n"
    "s_add_u32 s62, s63, 55296\n"
    "buffer_load_dwordx4 v5, s[44:47], s62 offen lds\n"
    "s_add_u32 m0, s61, 45056\n"
    "s_add_u32 s62, s63, 64512\n"
    "buffer_load_dwordx4 v5, s[44:47], s62 offen lds\n"
    "ds_read_b64_tr_b16 a[118:119], v96 offset:49152\n"
    "ds_read_b64_tr_b16 a[150:151], v98 offset:49152\n"
    "ds_read_b64_tr_b16 a[182:183], v100 offset:49152\n"
    "ds_read_b64_tr_b16 a[214:215], v102 offset:49152\n"
    "ds_read_b64_tr_b16 a[126:127], v97 offset:49152\n"
    "ds_read_b64_tr_b16 a[158:159], v99 offset:49152\n"
    "ds_read_b64_tr_b16 a[190:191], v101 offset:49152\n"
    "ds_read_b64_tr_b16 a[222:223], v103 offset:49152\n"
    "s_waitcnt vmcnt(28) lgkmcnt(0)\n"
    "s_barrier\n"
    "s_cbranch_vccz .Lsl29_%=\n"
    "s_sleep 6\n"
    ".Lsl29_%=:\n"
    "s_add_u32 m0, s61, 49152\n"
    "s_add_u32 s62, s63, 73728\n"
    "buffer_load_dwordx4 v5, s[44:47], s62 offen lds\n"
    "s_add_u32 m0, s61, 53248\n"
    "s_add_u32 s62, s63, 82944\n"
    "buffer_load_dwordx4 v5, s[44:47], s62 offen lds\n"
    "s_add_u32 m0, s61, 57344\n"
    "s_add_u32 s62, s63, 92160\n"
    "buffer_load_dwordx4 v5, s[44:47], s62 offen lds\n"
    "s_add_u32 m0, s61, 61440\n"
    "s_add_u32 s62, s63, 101376\n"
    "buffer_load_dwordx4 v5, s[44:47], s62 offen lds\n"
    "ds_read_b64_tr_b16 a[134:135], v127\n"
    "ds_read_b64_tr_b16 a[166:167], v126\n"
    "ds_read_b64_tr_b16 a[198:199], v125\n"
    "ds_read_b64_tr_b16 a[230:231], v124\n"
    "ds_read_b64_tr_b16 a[142:143], v123\n"
    "ds_read_b64_tr_b16 a[174:175], v122\n"
    "ds_read_b64_tr_b16 a[206:207], v121\n"
    "ds_read_b64_tr_b16 a[238:239], v120\n"
    "s_waitcnt vmcnt(28) lgkmcnt(0)\n"
    "s_barrier\n"
    "s_cbranch_vccz .Lsl30_%=\n"
    "s_sleep 6\n"
    ".Lsl30_%=:\n"
    "s_add_u32 m0, s61, 65536\n"
    "s_add_u32 s62, s63, 110592\n"
    "buffer_load_dwordx4 v5, s[44:47], s62 offen lds\n"
    "s_add_u32 m0, s61, 69632\n"
    "s_add_u32 s62, s63, 119808\n"
    "buffer_load_dwordx4 v5, s[44:47], s62 offen lds\n"
    "s_add_u32 m0, s61, 73728\n"
    "s_add_u32 s62, s63, 129024\n"
    "buffer_load_dwordx4 v5, s[44:47], s62 offen lds\n"
    "s_add_u32 m0, s61, 77824\n"
    "s_add_u32 s62, s63, 138240\n"
    "buffer_load_dwordx4 v5, s[44:47], s62 offen lds\n"
    "ds_read_b64_tr_b16 a[240:241], v119 offset:16384\n"
    "ds_read_b64_tr_b16 a[242:243], v119 offset:20480\n"
    "ds_read_b64_tr_b16 a[244:245], v119 offset:24576\n"
    "ds_read_b64_tr_b16 a[246:247], v119 offset:28672\n"
    "ds_read_b64_tr_b16 a[248:249], v118 offset:16384\n"
    "ds_read_b64_tr_b16 a[250:251], v118 offset:20480\n"
    "ds_read_b64_tr_b16 a[252:253], v118 offset:24576\n"
    "ds_read_b64_tr_b16 a[254:255], v118 offset:28672\n"
    "s_waitcnt vmcnt(28) lgkmcnt(0)\n"
    "s_barrier\n"
    "ds_read_b64_tr_b16 v[32:33], v127 offset:32768\n"
    "ds_read_b64_tr_b16 v[64:65], v126 offset:32768\n"
    "ds_read_b64_tr_b16 v[144:145], v125 offset:32768\n"
    "ds_read_b64_tr_b16 v[176:177], v124 offset:32768\n"
    "ds_read_b64_tr_b16 v[40:41], v123 offset:32768\n"
    "ds_read_b64_tr_b16 v[72:73], v122 offset:32768\n"
    "ds_read_b64_tr_b16 v[152:153], v121 offset:32768\n"
    "ds_read_b64_tr_b16 v[184:185], v120 offset:32768\n"
    "s_waitcnt vmcnt(24) lgkmcnt(0)\n"
    "s_barrier\n"
    "ds_read_b64_tr_b16 v[48:49], v127 offset:49152\n"
    "ds_read_b64_tr_b16 v[80:81], v126 offset:49152\n"
    "ds_read_b64_tr_b16 v[160:161], v125 offset:49152\n"
    "ds_read_b64_tr_b16 v[192:193], v124 offset:49152\n"
    "ds_read_b64_tr_b16 v[56:57], v123 offset:49152\n"
    "ds_read_b64_tr_b16 v[136:137], v122 offset:49152\n"
    "ds_read_b64_tr_b16 v[168:169], v121 offset:49152\n"
    "ds_read_b64_tr_b16 v[200:201], v120 offset:49152\n"
    "s_waitcnt vmcnt(20) lgkmcnt(0)\n"
    "s_barrier\n"
    "ds_read_b64_tr_b16 v[34:35], v117\n"
    "ds_read_b64_tr_b16 v[66:67], v116\n"
    "ds_read_b64_tr_b16 v[146:147], v115\n"
    "ds_read_b64_tr_b16 v[178:179], v114\n"
    "ds_read_b64_tr_b16 v[42:43], v113\n"
    "ds_read_b64_tr_b16 v[74:75], v112\n"
    "ds_read_b64_tr_b16 v[154:155], v111\n"
    "ds_read_b64_tr_b16 v[186:187], v110\n"
    "s_waitcnt vmcnt(16) lgkmcnt(0)\n"
    "s_barrier\n"
    "ds_read_b64_tr_b16 v[50:51], v117 offset:16384\n"
    "ds_read_b64_tr_b16 v[82:83], v116 offset:16384\n"
    "ds_read_b64_tr_b16 v[162:163], v115 offset:16384\n"
    "ds_read_b64_tr_b16 v[194:195], v114 offset:16384\n"
    "ds_read_b64_tr_b16 v[58:59], v113 offset:16384\n"
    "ds_read_b64_tr_b16 v[138:139], v112 offset:16384\n"
    "ds_read_b64_tr_b16 v[170:171], v111 offset:16384\n"
    "ds_read_b64_tr_b16 v[202:203], v110 offset:16384\n"
    "s_waitcnt vmcnt(12) lgkmcnt(0)\n"
    "s_barrier\n"
    "ds_read_b64_tr_b16 v[88:89], v6 offset:16384\n"
    "ds_read_b64_tr_b16 v[90:91], v6 offset:17408\n"
    "ds_read_b64_tr_b16 v[92:93], v6 offset:20480\n"
    "ds_read_b64_tr_b16 v[94:95], v6 offset:21504\n"
    "ds_read_b64_tr_b16 v[96:97], v6 offset:16416\n"
    "ds_read_b64_tr_b16 v[98:99], v6 offset:17440\n"
    "ds_read_b64_tr_b16 v[100:101], v6 offset:20512\n"
    "ds_read_b64_tr_b16 v[102:103], v6 offset:21536\n"
    "s_waitcnt vmcnt(8) lgkmcnt(0)\n"
    "s_barrier\n"
    "ds_read_b64_tr_b16 v[104:105], v6 offset:32768\n"
    "ds_read_b64_tr_b16 v[106:107], v6 offset:33792\n"
    "ds_read_b64_tr_b16 v[108:109], v6 offset:36864\n"
    "ds_read_b64_tr_b16 v[110:111], v6 offset:37888\n"
    "ds_read_b64_tr_b16 v[112:113], v6 offset:32800\n"
    "ds_read_b64_tr_b16 v[114:115], v6 offset:33824\n"
    "ds_read_b64_tr_b16 v[116:117], v6 offset:36896\n"
    "ds_read_b64_tr_b16 v[118:119], v6 offset:37920\n"
    "s_waitcnt vmcnt(4) lgkmcnt(0)\n"
    "s_barrier\n"
    "ds_read_b64_tr_b16 v[120:121], v6 offset:49152\n"
    "ds_read_b64_tr_b16 v[122:123], v6 offset:50176\n"
    "ds_read_b64_tr_b16 v[124:125], v6 offset:53248\n"
    "ds_read_b64_tr_b16 v[126:127], v6 offset:54272\n"
    "ds_read_b64_tr_b16 v[128:129], v6 offset:49184\n"
    "ds_read_b64_tr_b16 v[130:131], v6 offset:50208\n"
    "ds_read_b64_tr_b16 v[132:133], v6 offset:53280\n"
    "ds_read_b64_tr_b16 v[134:135], v6 offset:54304\n"
    "s_waitcnt vmcnt(0) lgkmcnt(0)\n"
    "s_barrier\n"
    "v_add_u32 v31, 0x10000, v6\n"
    "ds_read_b64_tr_b16 v[12:13], v31\n"
    "ds_read_b64_tr_b16 v[14:15], v31 offset:1024\n"
    "ds_read_b64_tr_b16 v[16:17], v31 offset:4096\n"
    "ds_read_b64_tr_b16 v[18:19], v31 offset:5120\n"
    "ds_read_b64_tr_b16 v[20:21], v31 offset:32\n"
    "ds_read_b64_tr_b16 v[22:23], v31 offset:1056\n"
    "ds_read_b64_tr_b16 v[24:25], v31 offset:4128\n"
    "ds_read_b64_tr_b16 v[26:27], v31 offset:5152\n"
    "s_waitcnt lgkmcnt(0)\n"
    "s_barrier\n"
    "s_xor_b32 s73, %2, 0\n"
    "s_lshl_b32 s73, s73, 12\n"
    "v_add_u32 v6, s73, v2\n"
    "ds_write_b128 v6, v[88:91] offset:14336\n"
    "ds_write_b128 v6, v[92:95] offset:15360\n"
    "ds_write_b128 v6, v[96:99] offset:16384\n"
    "ds_write_b128 v6, v[100:103] offset:17408\n"
    "s_xor_b32 s73, %2, 1\n"
    "s_lshl_b32 s73, s73, 12\n"
    "v_add_u32 v6, s73, v2\n"
    "ds_write_b128 v6, v[104:107] offset:14336\n"
    "ds_write_b128 v6, v[108:111] offset:15360\n"
    "ds_write_b128 v6, v[112:115] offset:16384\n"
    "ds_write_b128 v6, v[116:119] offset:17408\n"
    "s_xor_b32 s73, %2, 2\n"
    "s_lshl_b32 s73, s73, 12\n"
    "v_add_u32 v6, s73, v2\n"
    "ds_write_b128 v6, v[120:123] offset:14336\n"
    "ds_write_b128 v6, v[124:127] offset:15360\n"
    "ds_write_b128 v6, v[128:131] offset:16384\n"
    "ds_write_b128 v6, v[132:135] offset:17408\n"
    "s_xor_b32 s73, %2, 3\n"
    "s_lshl_b32 s73, s73, 12\n"
    "v_add_u32 v6, s73, v2\n"
    "ds_write_b128 v6, v[12:15] offset:14336\n"
    "ds_write_b128 v6, v[16:19] offset:15360\n"
    "ds_write_b128 v6, v[20:23] offset:16384\n"
    "ds_write_b128 v6, v[24:27] offset:17408\n"
    "ds_read_b64_tr_b16 v[88:89], v9\n"
    "ds_read_b64_tr_b16 v[90:91], v9 offset:128\n"
    "ds_read_b64_tr_b16 v[92:93], v9 offset:2048\n"
    "ds_read_b64_tr_b16 v[94:95], v9 offset:2176\n"
    "ds_read_b64_tr_b16 v[96:97], v9 offset:4096\n"
    "ds_read_b64_tr_b16 v[98:99], v9 offset:4224\n"
    "ds_read_b64_tr_b16 v[100:101], v9 offset:6144\n"
    "ds_read_b64_tr_b16 v[102:103], v9 offset:6272\n"
    "ds_write_b128 v2, v[32:35]\n"
    "ds_write_b128 v2, v[36:39] offset:1024\n"
    "ds_write_b128 v2, v[40:43] offset:2048\n"
    "ds_write_b128 v2, v[44:47] offset:3072\n"
    "ds_write_b128 v2, v[48:51] offset:4096\n"
    "ds_write_b128 v2, v[52:55] offset:5120\n"
    "ds_write_b128 v2, v[56:59] offset:6144\n"
    "ds_write_b128 v2, v[60:63] offset:7168\n"
    "ds_write_b128 v2, v[64:67] offset:8192\n"
    "ds_write_b128 v2, v[68:71] offset:9216\n"
    "ds_write_b128 v2, v[72:75] offset:10240\n"
    "ds_write_b128 v2, v[76:79] offset:11264\n"
    "ds_write_b128 v2, v[80:83] offset:12288\n"
    "ds_write_b128 v2, v[84:87] offset:13312\n"
    "v_lshrrev_b32 v112, 4, v0\n"
    "v_and_b32 v113, 15, v112\n"
    "v_lshrrev_b32 v114, 4, v112\n"
    "v_mov_b32 v11, 0\n"
    "v_lshrrev_b32 v115, 1, v113\n"
    "v_bfi_b32 v111, 1, v113, v115\n"
    "v_and_b32 v115, 1, v115\n"
    "v_lshl_add_u32 v115, v114, 1, v115\n"
    "v_mul_u32_u24 v117, 1152, v111\n"
    "v_lshl_add_u32 v117, v115, 4, v117\n"
    "v_and_b32 v115, 1, v114\n"
    "v_lshrrev_b32 v116, 1, v114\n"
    "v_mov_b32 v120, 0\n"
    "v_mov_b32 v121, 0\n"
    "v_mov_b32 v122, 0\n"
    "v_mov_b32 v123, 0\n"
    "ds_write_b128 v11, v[120:123] offset:31872\n"
    "v_mov_b32 v124, 0x3f803f80\n"
    "v_mov_b32 v125, 0x3f803f80\n"
    "v_mov_b32 v126, 0x3f803f80\n"
    "v_mov_b32 v127, 0x3f803f80\n"
    "ds_write_b128 v11, v[124:127] offset:31888\n"
    "v_mov_b32 v128, 0x3f003f00\n"
    "v_mov_b32 v129, 0x3f003f00\n"
    "v_mov_b32 v130, 0x3f003f00\n"
    "v_mov_b32 v131, 0x3f003f00\n"
    "ds_write_b128 v11, v[128:131] offset:31904\n"
    "v_mul_u32_u24 v118, 57, v112\n"
    "v_lshrrev_b32 v118, 9, v118\n"
    "v_mul_u32_u24 v118, 9, v118\n"
    "v_cmp_eq_u32 vcc, v118, v112\n"
    "v_lshlrev_b32 v118, 1, v112\n"
    "s_nop 0\n"
    "v_cndmask_b32 v120, v11, v124, vcc\n"
    "ds_write_b16 v118, v120 offset:31936\n"
    "s_waitcnt lgkmcnt(0)\n"
    "s_cmp_lt_u32 %2, 2\n"
    "s_cbranch_scc1 .Lsk_%=\n"
    "ds_write_b128 v2, v[88:91] offset:30720\n"
    "ds_write_b128 v2, v[92:95] offset:31744\n"
    "ds_write_b128 v2, v[96:99] offset:32768\n"
    "ds_write_b128 v2, v[100:103] offset:33792\n"
    ".Lsk_%=:\n"
    "s_cmp_eq_u32 %2, 3\n"
    "s_cbranch_scc1 .Lsm_%=\n"
    "s_mov_b64 s[44:45], %12\n"
    "s_and_b32 s45, s45, 0xffff\n"
    "s_mov_b32 s46, 0x2400\n"
    "s_mov_b32 s47, 0x20000\n"
    "s_mul_i32 s62, %2, 384\n"
    "s_mul_i32 s61, %2, 3072\n"
    "s_add_u32 m0, s61, 20480\n"
    "s_add_u32 s73, s62, 0\n"
    "buffer_load_dwordx4 v117, s[44:47], s73 offen lds\n"
    "s_add_u32 m0, s61, 21504\n"
    "s_add_u32 s73, s62, 128\n"
    "buffer_load_dwordx4 v117, s[44:47], s73 offen lds\n"
    "s_add_u32 m0, s61, 22528\n"
    "s_add_u32 s73, s62, 256\n"
    "buffer_load_dwordx4 v117, s[44:47], s73 offen lds\n"
    ".Lsm_%=:\n"
    "v_mov_b32 v130, 0x20400\n"
    "global_load_dwordx2 v[132:133], v130, %1 sc1\n"
    "v_mbcnt_lo_u32_b32 v131, -1, 0\n"
    "v_min_u32 v131, 7, v131\n"
    "v_lshlrev_b32 v131, 7, v131\n"
    "v_add_u32 v131, 0x20000, v131\n"
    "global_load_dwordx2 v[134:135], v131, %1 sc1\n"
    "s_waitcnt vmcnt(0) lgkmcnt(0)\n"
    "s_barrier\n"
    "v_mov_b32 v1, 0x4444\n"
    "v_mov_b32 v118, 0xeeee\n"
    "v_and_b32 v4, 2, v113\n"
    "v_cmp_eq_u32 vcc, 0, v4\n"
    "s_nop 1\n"
    "v_cndmask_b32 v1, v118, v1, vcc\n"
    "v_xor_b32 v118, 7, v111\n"
    "v_cndmask_b32 v4, v118, v111, vcc\n"
    "s_lshl_b32 s73, %2, 10\n"
    "v_add_u32 v5, s73, v0\n"
    "v_and_b32 v119, 7, v113\n"
    "v_lshrrev_b32 v120, 3, v113\n"
    "v_mul_u32_u24 v3, 544, v114\n"
    "v_lshl_add_u32 v3, v113, 2, v3\n"
    "s_lshl_b32 s73, %2, 6\n"
    "s_add_u32 s73, s73, 20480\n"
    "v_add_u32 v3, s73, v3\n"
    "v_mul_u32_u24 v4, 272, v4\n"
    "v_lshl_add_u32 v4, v114, 4, v4\n"
    "v_and_b32 v6, 13, v113\n"
    "v_lshl_add_u32 v6, v116, 4, v6\n"
    "v_lshl_add_u32 v6, v115, 1, v6\n"
    "s_lshl_b32 s73, %2, 9\n"
    "v_lshl_add_u32 v6, v6, 4, s73\n"
    "s_waitcnt vmcnt(0)\n"
    "v_add_u32 v132, 1, v132\n"
    "v_add_u32 v133, 1, v133\n"
    "s_mov_b32 s56, 1048576\n"
    ".Lcgo_a%=:\n"
    "v_cmp_ne_u64 vcc, v[132:133], v[134:135]\n"
    "s_cbranch_vccz .Lcgo_x%=\n"
    "global_load_dwordx2 v[134:135], v131, %1 sc1\n"
    "s_sub_u32 s56, s56, 1\n"
    "s_waitcnt vmcnt(0)\n"
    "s_cbranch_scc0 .Lcgo_a%=\n"
    ".Lcgo_x%=:\n"
    "s_mov_b32 s56, 0\n"
    "v_mov_b32 v134, 64\n"
    ".Lpoll1_%=:\n"
    "global_load_dword v135, v134, %1 sc1\n"
    "s_waitcnt vmcnt(0)\n"
    "v_readfirstlane_b32 s73, v135\n"
    "s_cmp_ge_u32 s73, 4\n"
    "s_cbranch_scc1 .Lpoll1_x%=\n"
    "s_add_u32 s56, s56, 1\n"
    "s_cmp_lt_u32 s56, 3\n"
    "s_cbranch_scc1 .Lpoll1_%=\n"
    "s_sleep 8\n"
    "s_cmp_lt_u32 s56, s57\n"
    "s_cbranch_scc1 .Lpoll1_%=\n"
    "s_mov_b32 s57, 0\n"
    "v_mov_b32 v135, 1\n"
    "global_store_dword v11, v135, %1 sc1\n"
    ".Lpoll1_x%=:\n"
    "s_xor_b32 s69, s73, %7\n"
    "s_lshr_b32 s51, s73, 1\n"
    "s_xor_b32 s51, s51, %7\n"
    "s_and_b32 s51, s51, 1\n"
    "s_lshl_b32 s51, s51, 13\n"
    "s_add_u32 s68, s51, 0x200000\n"
    "s_and_b32 s60, s69, 1\n"
    "s_lshl_b32 s60, s60, 13\n"
    "v_lshl_add_u32 v8, v116, 2, v115\n"
    "v_lshlrev_b32 v8, 12, v8\n"
    "v_lshl_add_u32 v8, v119, 4, v8\n"
    "v_lshl_add_u32 v7, v120, 15, v8\n"
    "v_lshl_add_u32 v8, v120, 30, v8\n"
    "s_mul_i32 s73, %5, 294912\n"
    "s_lshl_b32 s51, %4, 7\n"
    "s_add_u32 s73, s73, s51\n"
    "s_add_u32 s73, s73, s60\n"
    "s_lshl_b32 s51, %2, 16\n"
    "s_add_u32 s51, s51, s73\n"
    "v_add_u32 v7, s51, v7\n"
    "s_lshl3_add_u32 s51, %2, s73\n"
    "s_add_u32 s51, s51, 262128\n"
    "s_cmp_lt_u32 %2, 2\n"
    "s_cselect_b32 s51, 0x80000000, s51\n"
    "v_add_u32 v8, s51, v8\n"
    "s_lshl_b32 s61, %4, 1\n"
    "s_add_u32 s61, s61, %5\n"
    "s_lshl4_add_u32 s44, s61, 1024\n"
    "s_mul_i32 s61, %5, 36\n"
    "s_lshr_b32 s73, %4, 3\n"
    "s_add_u32 s73, s73, s61\n"
    "s_add_u32 s73, s73, 32\n"
    "s_add_u32 s61, s61, %4\n"
    "s_and_b32 s51, %4, 7\n"
    "s_add_u32 s51, s51, %2\n"
    "s_cmp_eq_u32 s51, 1\n"
    "s_cselect_b32 s71, s73, -1\n"
    "s_cmp_eq_u32 %2, 0\n"
    "s_cselect_b32 s71, s61, s71\n"
    "s_max_i32 s61, s71, 0\n"
    "s_lshr_b32 s73, s61, 1\n"
    "s_lshl_b32 s73, s73, 14\n"
    "s_and_b32 s51, s61, 1\n"
    "s_lshl_b32 s51, s51, 12\n"
    "s_add_u32 s65, s73, s51\n"
    "s_add_u32 s65, s65, s60\n"
    "s_lshr_b32 s94, s61, 5\n"
    "s_and_b32 s73, s61, 31\n"
    "s_lshl_b32 s73, s73, 7\n"
    "v_lshl_add_u32 v9, v114, 2, s73\n"
    "v_lshl_add_u32 v9, v119, 4, v9\n"
    "v_lshl_add_u32 v9, v120, 22, v9\n"
    "s_lshl_b32 s73, %2, 11\n"
    "v_add_u32 v10, s73, v0\n"
    "s_cmp_ge_u32 %2, 2\n"
    "s_cbranch_scc0 .Lnw3_%=\n"
    "v_lshl_add_u32 v9, v113, 1, %5\n"
    "v_lshlrev_b32 v118, 3, v114\n"
    "v_sub_u32 v9, v9, v118\n"
    "v_cmp_le_i32 s[76:77], 0, v9\n"
    "v_cmp_le_i32 s[78:79], 2, v9\n"
    "v_cmp_le_i32 s[80:81], 4, v9\n"
    "v_cmp_le_i32 s[82:83], 6, v9\n"
    "v_cmp_le_i32 s[84:85], 1, v9\n"
    "v_cmp_le_i32 s[86:87], 3, v9\n"
    "v_cmp_le_i32 s[88:89], 5, v9\n"
    "v_cmp_le_i32 s[90:91], 7, v9\n"
    ".Lnw3_%=:\n"
    "s_and_b32 s73, %4, 16\n"
    "s_lshl_b32 s92, s73, 10\n"
    "s_and_b32 s73, %4, 12\n"
    "s_lshl_b32 s73, s73, 9\n"
    "s_add_u32 s92, s92, s73\n"
    "s_and_b32 s73, %4, 3\n"
    "s_lshl_b32 s73, s73, 8\n"
    "s_add_u32 s92, s92, s73\n"
    "s_add_u32 s92, s92, 106496\n"
    "s_xor_b32 s92, s92, s60\n"
    "s_lshr_b32 s51, %4, 3\n"
    "s_lshl3_add_u32 s73, %5, s51\n"
    "s_and_b32 s51, %4, 7\n"
    "s_lshl4_add_u32 s73, s73, s51\n"
    "s_lshl_b32 s73, s73, 7\n"
    "s_add_u32 s93, s73, 73728\n"
    "s_xor_b32 s93, s93, s60\n"
    "s_lshl_b32 s73, %5, 12\n"
    "s_lshl_b32 s51, %4, 14\n"
    "s_add_u32 s73, s73, s51\n"
    "s_add_u32 s75, s73, 0x9fff000\n"
    "s_brev_b32 s45, 1\n"
    "s_mov_b32 s46, 0x3f803f80\n"
    "s_mov_b32 s47, s46\n"
    "s_mov_b32 s48, 0\n"
    "s_mov_b32 s54, 56\n"
    "s_mov_b32 s55, 80\n"
    "s_mov_b32 s96, 736\n"
    "s_mov_b32 s56, 0\n"
    "v_mov_b32 v134, 128\n"
    ".Lpoll2_%=:\n"
    "global_load_dword v135, v134, %1 sc1\n"
    "s_waitcnt vmcnt(0)\n"
    "v_readfirstlane_b32 s73, v135\n"
    "s_cmp_ge_u32 s73, 64\n"
    "s_cbranch_scc1 .Lpoll2_x%=\n"
    "s_add_u32 s56, s56, 1\n"
    "s_cmp_lt_u32 s56, 3\n"
    "s_cbranch_scc1 .Lpoll2_%=\n"
    "s_sleep 8\n"
    "s_cmp_lt_u32 s56, s57\n"
    "s_cbranch_scc1 .Lpoll2_%=\n"
    "s_mov_b32 s57, 0\n"
    "v_mov_b32 v135, 1\n"
    "global_store_dword v11, v135, %1 sc1\n"
    ".Lpoll2_x%=:\n"
    "ds_read_b128 v[32:35], v2\n"
    "ds_read_b128 v[36:39], v2 offset:1024\n"
    "ds_read_b128 v[40:43], v2 offset:2048\n"
    "ds_read_b128 v[44:47], v2 offset:3072\n"
    "ds_read_b128 v[48:51], v2 offset:4096\n"
    "ds_read_b128 v[52:55], v2 offset:5120\n"
    "ds_read_b128 v[56:59], v2 offset:6144\n"
    "ds_read_b128 v[60:63], v2 offset:7168\n"
    "ds_read_b128 v[64:67], v2 offset:8192\n"
    "ds_read_b128 v[68:71], v2 offset:9216\n"
    "ds_read_b128 v[72:75], v2 offset:10240\n"
    "ds_read_b128 v[76:79], v2 offset:11264\n"
    "ds_read_b128 v[80:83], v2 offset:12288\n"
    "ds_read_b128 v[84:87], v2 offset:13312\n"
    "v_mov_b64 v[124:125], 0\n"
    "v_mov_b64 v[126:127], 0\n"
    "v_mov_b64 v[128:129], 0\n"
    "v_mov_b64 v[130:131], 0\n"
    "s_add_u32 s73, s48, 0\n"
    "s_and_b32 s73, s73, 63\n"
    "s_lshr_b32 s51, s73, 2\n"
    "s_xor_b32 s51, s51, s69\n"
    "s_and_b32 s51, s51, 1\n"
    "s_lshl_b32 s51, s51, 13\n"
    "s_lshl_b32 s73, s73, 5\n"
    "s_add_u32 s73, s73, %4\n"
    "s_lshl_b32 s73, s73, 14\n"
    "s_add_u32 s73, s73, s51\n"
    "s_add_u32 s60, s73, 0x6000000\n"
    "s_add_u32 s61, s73, 0x6001000\n"
    "s_add_u32 s62, s73, 0x8000000\n"
    "s_add_u32 s63, s73, 0x8001000\n"
    "s_lshl_b32 s51, %5, 12\n"
    "s_add_u32 s73, s73, s51\n"
    "s_add_u32 s64, s73, 0x4000000\n"
    "s_mul_i32 s73, s48, 3\n"
    "s_add_u32 s73, s73, 2\n"
    "s_and_b32 s51, s73, 1\n"
    "s_lshl_b32 s51, s51, 12\n"
    "s_lshr_b32 s73, s73, 1\n"
    "s_lshl_b32 s73, s73, 14\n"
    "s_add_u32 s73, s73, s51\n"
    "s_add_u32 s58, s73, s68\n"
    "s_waitcnt lgkmcnt(0)\n"
    "s_mov_b32 s56, 0\n"
    "v_mov_b32 v134, s92\n"
    ".Lpoll3_%=:\n"
    "global_load_dword v132, v134, %1 sc1\n"
    "global_load_dword v133, v134, %1 offset:128 sc1\n"
    "s_waitcnt vmcnt(0)\n"
    "v_and_b32 v133, v132, v133\n"
    "s_nop 0\n"
    "v_readfirstlane_b32 s73, v133\n"
    "s_cmp_ge_u32 s73, 1\n"
    "s_cbranch_scc1 .Lpoll3_x%=\n"
    "s_add_u32 s56, s56, 1\n"
    "s_cmp_lt_u32 s56, 3\n"
    "s_cbranch_scc1 .Lpoll3_%=\n"
    "s_sleep 8\n"
    "s_cmp_lt_u32 s56, s57\n"
    "s_cbranch_scc1 .Lpoll3_%=\n"
    "s_mov_b32 s57, 0\n"
    "v_mov_b32 v135, 1\n"
    "global_store_dword v11, v135, %1 sc1\n"
    ".Lpoll3_x%=:\n"
    "buffer_load_dwordx4 v[12:15], v5, s[40:43], s60 offen sc1\n"
    "buffer_load_dwordx4 v[16:19], v5, s[40:43], s61 offen sc1\n"
    "buffer_load_dwordx4 v[20:23], v5, s[40:43], s62 offen sc1\n"
    "buffer_load_dwordx4 v[24:27], v5, s[40:43], s63 offen sc1\n"
    "buffer_load_dwordx4 v[28:31], v5, s[40:43], s64 offen sc1\n"
    "v_xor_b32 v132, 0x800, v10\n"
    "v_xor_b32 v133, 0x1000, v10\n"
    "v_xor_b32 v134, 0x1800, v10\n"
    "v_lshrrev_b32 v135, 2, v5\n"
    "s_movk_i32 s73, 0x180\n"
    "v_lshlrev_b32 v120, 1, v0\n"
    "v_bfi_b32 v120, s73, v120, v0\n"
    "v_lshrrev_b32 v122, 2, v0\n"
    "v_bfi_b32 v120, 64, v122, v120\n"
    "v_lshlrev_b32 v121, 1, v135\n"
    "v_bfi_b32 v121, s73, v121, v135\n"
    "v_lshrrev_b32 v122, 2, v135\n"
    "v_bfi_b32 v121, 64, v122, v121\n"
    "ds_read_b128 v[88:91], v10 offset:20480\n"
    "ds_read_b128 v[92:95], v10 offset:21504\n"
    "ds_read_b32 v96, v135 offset:28672\n"
    "s_add_u32 s73, s48, 1\n"
    "s_and_b32 s73, s73, 63\n"
    "s_lshr_b32 s51, s73, 2\n"
    "s_xor_b32 s51, s51, s69\n"
    "s_and_b32 s51, s51, 1\n"
    "s_lshl_b32 s51, s51, 13\n"
    "s_lshl_b32 s73, s73, 5\n"
    "s_add_u32 s73, s73, %4\n"
    "s_lshl_b32 s73, s73, 14\n"
    "s_add_u32 s73, s73, s51\n"
    "s_add_u32 s60, s73, 0x6000000\n"
    "s_add_u32 s61, s73, 0x6001000\n"
    "s_add_u32 s62, s73, 0x8000000\n"
    "s_add_u32 s63, s73, 0x8001000\n"
    "s_lshl_b32 s51, %5, 12\n"
    "s_add_u32 s73, s73, s51\n"
    "s_add_u32 s64, s73, 0x4000000\n"
    "s_add_u32 s73, s48, 1\n"
    "s_and_b32 s51, s73, 32\n"
    "s_and_b32 s73, s73, 31\n"
    "s_lshl3_add_u32 s73, s51, s73\n"
    "s_lshl2_add_u32 s73, s73, s92\n"
    "s_waitcnt lgkmcnt(2)\n"
    "v_min_u32 v88, -2, v88\n"
    "v_min_u32 v89, -2, v89\n"
    "v_min_u32 v90, -2, v90\n"
    "v_min_u32 v91, -2, v91\n"
    "s_waitcnt lgkmcnt(1)\n"
    "v_min_u32 v92, -2, v92\n"
    "v_min_u32 v93, -2, v93\n"
    "v_min_u32 v94, -2, v94\n"
    "v_min_u32 v95, -2, v95\n"
    "s_waitcnt lgkmcnt(0)\n"
    "v_min_u32 v96, -2, v96\n"
    "s_waitcnt vmcnt(4)\n"
    "ds_write_b128 v5, v[12:15]\n"
    "s_waitcnt vmcnt(3)\n"
    "ds_write_b128 v5, v[16:19] offset:4096\n"
    "s_waitcnt vmcnt(0)\n"
    "ds_write_b128 v5, v[28:31] offset:16384\n"
    "v_swap_b32 v22, v24\n"
    "v_swap_b32 v23, v25\n"
    "ds_write_b128 v5, v[20:23] offset:8192\n"
    "ds_write_b128 v5, v[24:27] offset:12288\n"
    "v_mov_b32 v14, s73\n"
    "global_load_dword v12, v14, %1 sc1\n"
    "global_load_dword v13, v14, %1 offset:128 sc1\n"
    "global_load_dword v14, v14, %1 sc1\n"
    "v_mov_b64 v[16:17], 0\n"
    "v_mov_b64 v[18:19], 0\n"
    "v_mov_b64 v[20:21], 0\n"
    "v_mov_b64 v[22:23], 0\n"
    "v_mov_b64 v[24:25], 0\n"
    "v_mov_b64 v[26:27], 0\n"
    "v_mov_b64 v[28:29], 0\n"
    "v_mov_b64 v[30:31], 0\n"
    ".Lstep_%=:\n"
    "s_waitcnt vmcnt(5)\n"
    "v_smfmac_f32_16x16x64_bf16 v[16:19], v[88:91], v[32:39], v1\n"
    "ds_write_b128 v10, v[88:91] offset:20480\n"
    "v_smfmac_f32_16x16x64_bf16 v[20:23], v[88:91], v[40:47], v1\n"
    "s_waitcnt vmcnt(4)\n"
    "ds_write_b128 v10, v[92:95] offset:21504\n"
    "v_max3_u32 v100, v88, v89, v90\n"
    "v_smfmac_f32_16x16x64_bf16 v[16:19], v[92:95], v[48:55], v1\n"
    "s_waitcnt vmcnt(3)\n"
    "ds_write_b32 v135, v96 offset:28672\n"
    "v_max3_u32 v100, v100, v91, v92\n"
    "v_smfmac_f32_16x16x64_bf16 v[20:23], v[92:95], v[56:63], v1\n"
    "v_max3_u32 v100, v100, v93, v94\n"
    "v_smfmac_f32_16x16x64_bf16 v[24:27], v[88:91], v[224:231], v1\n"
    "v_max3_u32 v100, v100, v95, v96\n"
    "v_smfmac_f32_16x16x64_bf16 v[28:31], v[88:91], v[232:239], v1\n"
    "v_cmp_eq_u32 vcc, -1, v100\n"
    "v_smfmac_f32_16x16x64_bf16 v[24:27], v[92:95], v[240:247], v1\n"
    "s_cbranch_vccnz .Lgf_%=\n"
    ".Lgx_%=:\n"
    "s_waitcnt lgkmcnt(0)\n"
    "s_barrier\n"
    "s_memrealtime s[52:53]\n"
    "ds_read_b128 v[96:99], v132 offset:20480\n"
    "v_smfmac_f32_16x16x64_bf16 v[28:31], v[92:95], v[248:255], v1\n"
    "ds_read_b128 v[100:103], v132 offset:21504\n"
    "v_smfmac_f32_16x16x64_bf16 v[124:127], v[88:91], a[112:119], v1\n"
    "ds_read_b128 v[104:107], v133 offset:20480\n"
    "v_smfmac_f32_16x16x64_bf16 v[128:131], v[88:91], a[120:127], v1\n"
    "ds_read_b128 v[108:111], v133 offset:21504\n"
    "v_smfmac_f32_16x16x64_bf16 v[124:127], v[92:95], a[128:135], v1\n"
    "ds_read_b128 v[112:115], v134 offset:20480\n"
    "v_smfmac_f32_16x16x64_bf16 v[128:131], v[92:95], a[136:143], v1\n"
    "ds_read_b128 v[116:119], v134 offset:21504\n"
    "s_waitcnt lgkmcnt(5)\n"
    "v_smfmac_f32_16x16x64_bf16 v[16:19], v[96:99], v[64:71], v1\n"
    "ds_read_b128 v[120:123], v0 offset:28672\n"
    "v_smfmac_f32_16x16x64_bf16 v[20:23], v[96:99], v[72:79], v1\n"
    "s_waitcnt lgkmcnt(5)\n"
    "v_smfmac_f32_16x16x64_bf16 v[16:19], v[100:103], v[80:87], v1\n"
    "v_cmp_eq_u32 vcc, s44, v5\n"
    "v_smfmac_f32_16x16x64_bf16 v[20:23], v[100:103], v[136:143], v1\n"
    "v_mov_b32 v79, s45\n"
    "s_waitcnt lgkmcnt(4)\n"
    "v_smfmac_f32_16x16x64_bf16 v[16:19], v[104:107], v[144:151], v1\n"
    "v_cndmask_b32 v79, v79, v0, vcc\n"
    "v_smfmac_f32_16x16x64_bf16 v[20:23], v[104:107], v[152:159], v1\n"
    "s_lshr_b32 s73, s96, 3\n"
    "s_waitcnt lgkmcnt(3)\n"
    "v_smfmac_f32_16x16x64_bf16 v[16:19], v[108:111], v[160:167], v1\n"
    "s_waitcnt lgkmcnt(0)\n"
    "s_add_u32 s73, s73, s52\n"
    "v_and_b32 v78, -2, s73\n"
    "v_smfmac_f32_16x16x64_bf16 v[20:23], v[108:111], v[168:175], v1\n"
    "buffer_store_dword v78, v79, s[40:43], s58 offen offset:3072 sc1\n"
    "s_waitcnt vmcnt(2)\n"
    "v_smfmac_f32_16x16x64_bf16 v[16:19], v[112:115], v[176:183], v1\n"
    "ds_read_b128 v[40:43], v0\n"
    "v_smfmac_f32_16x16x64_bf16 v[20:23], v[112:115], v[184:191], v1\n"
    "ds_read_b128 v[44:47], v0 offset:4096\n"
    "v_and_b32 v12, v12, v13\n"
    "v_smfmac_f32_16x16x64_bf16 v[16:19], v[116:119], v[192:199], v1\n"
    "ds_read_b128 v[48:51], v0 offset:1024\n"
    "v_add_u32 v13, -1, v12\n"
    "v_smfmac_f32_16x16x64_bf16 v[20:23], v[116:119], v[200:207], v1\n"
    "ds_read_b128 v[52:55], v0 offset:5120\n"
    "v_and_or_b32 v15, v13, s45, v5\n"
    "v_smfmac_f32_16x16x64_bf16 v[16:19], v[120:123], v[208:215], v1\n"
    "ds_read_b128 v[56:59], v0 offset:2048\n"
    "v_readfirstlane_b32 s59, v12\n"
    "v_smfmac_f32_16x16x64_bf16 v[20:23], v[120:123], v[216:223], v1\n"
    "ds_read_b128 v[60:63], v0 offset:6144\n"
    "v_smfmac_f32_16x16x64_bf16 v[24:27], v[96:99], a[0:7], v1\n"
    "ds_read_b128 v[64:67], v0 offset:3072\n"
    "v_add_f32 v16, v16, v18\n"
    "v_smfmac_f32_16x16x64_bf16 v[28:31], v[96:99], a[8:15], v1\n"
    "ds_read_b128 v[68:71], v0 offset:7168\n"
    "v_add_f32 v17, v17, v19\n"
    "v_smfmac_f32_16x16x64_bf16 v[24:27], v[100:103], a[16:23], v1\n"
    "ds_read_b128 v[88:91], v2 offset:14336\n"
    "v_add_f32 v20, v20, v22\n"
    "v_smfmac_f32_16x16x64_bf16 v[28:31], v[100:103], a[24:31], v1\n"
    "ds_read_b128 v[92:95], v2 offset:15360\n"
    "v_add_f32 v21, v21, v23\n"
    "v_smfmac_f32_16x16x64_bf16 v[24:27], v[104:107], a[32:39], v1\n"
    "v_cvt_pk_bf16_f32 v76, v16, v20\n"
    "v_smfmac_f32_16x16x64_bf16 v[28:31], v[104:107], a[40:47], v1\n"
    "v_cvt_pk_bf16_f32 v77, v17, v21\n"
    "v_smfmac_f32_16x16x64_bf16 v[24:27], v[108:111], a[48:55], v1\n"
    "ds_write_b32 v3, v76 offset:9216\n"
    "v_smfmac_f32_16x16x64_bf16 v[28:31], v[108:111], a[56:63], v1\n"
    "ds_write_b32 v3, v77 offset:9488\n"
    "v_smfmac_f32_16x16x64_bf16 v[24:27], v[112:115], a[64:71], v1\n"
    "buffer_load_dwordx4 v[16:19], v15, s[40:43], s61 offen sc1\n"
    "v_smfmac_f32_16x16x64_bf16 v[28:31], v[112:115], a[72:79], v1\n"
    "v_smfmac_f32_16x16x64_bf16 v[24:27], v[116:119], a[80:87], v1\n"
    "v_smfmac_f32_16x16x64_bf16 v[28:31], v[116:119], a[88:95], v1\n"
    "s_waitcnt lgkmcnt(0)\n"
    "s_barrier\n"
    "ds_read_b128 v[32:35], v4 offset:29696\n"
    "v_smfmac_f32_16x16x64_bf16 v[24:27], v[120:123], a[96:103], v1\n"
    "ds_read_b128 v[36:39], v4 offset:29760\n"
    "v_smfmac_f32_16x16x64_bf16 v[28:31], v[120:123], a[104:111], v1\n"
    "ds_read_b128 v[132:135], v4 offset:29824\n"
    "v_smfmac_f32_16x16x64_bf16 v[124:127], v[96:99], a[144:151], v1\n"
    "ds_read_b128 v[72:75], v4 offset:29888\n"
    "v_add_f32 v24, v24, v26\n"
    "v_add_f32 v25, v25, v27\n"
    "v_smfmac_f32_16x16x64_bf16 v[128:131], v[96:99], a[152:159], v1\n"
    "buffer_load_dwordx4 v[20:23], v15, s[40:43], s62 offen sc1\n"
    "v_add_f32 v28, v28, v30\n"
    "v_add_f32 v29, v29, v31\n"
    "v_smfmac_f32_16x16x64_bf16 v[124:127], v[100:103], a[160:167], v1\n"
    "v_cvt_pk_bf16_f32 v76, v24, v28\n"
    "v_cvt_pk_bf16_f32 v77, v25, v29\n"
    "s_waitcnt lgkmcnt(3)\n"
    "v_mfma_f32_16x16x32_bf16 v[80:83], v[40:43], v[32:35], 0\n"
    "ds_write2_b32 v3, v76, v77 offset1:68\n"
    "v_mfma_f32_16x16x32_bf16 v[84:87], v[44:47], v[32:35], 0\n"
    "s_add_u32 s73, s48, 1\n"
    "s_and_b32 s51, s73, 32\n"
    "s_and_b32 s73, s73, 31\n"
    "s_waitcnt lgkmcnt(3)\n"
    "v_mfma_f32_16x16x32_bf16 v[80:83], v[48:51], v[36:39], v[80:83]\n"
    "buffer_load_dwordx4 v[24:27], v15, s[40:43], s63 offen sc1\n"
    "v_mfma_f32_16x16x32_bf16 v[84:87], v[52:55], v[36:39], v[84:87]\n"
    "s_lshl3_add_u32 s73, s51, s73\n"
    "s_lshl2_add_u32 s73, s73, s92\n"
    "s_waitcnt lgkmcnt(2)\n"
    "v_mfma_f32_16x16x32_bf16 v[80:83], v[56:59], v[132:135], v[80:83]\n"
    "s_mov_b32 vcc_lo, 0xcccccccc\n"
    "s_mov_b32 vcc_hi, 0xcccccccc\n"
    "v_mfma_f32_16x16x32_bf16 v[84:87], v[60:63], v[132:135], v[84:87]\n"
    "s_waitcnt lgkmcnt(0)\n"
    "s_barrier\n"
    "ds_read_b128 v[40:43], v4 offset:20480\n"
    "v_mfma_f32_16x16x32_bf16 v[80:83], v[64:67], v[72:75], v[80:83]\n"
    "ds_read_b128 v[44:47], v4 offset:20544\n"
    "v_mfma_f32_16x16x32_bf16 v[84:87], v[68:71], v[72:75], v[84:87]\n"
    "ds_read_b128 v[48:51], v4 offset:20608\n"
    "v_smfmac_f32_16x16x64_bf16 v[128:131], v[100:103], a[168:175], v1\n"
    "ds_read_b128 v[52:55], v4 offset:20672\n"
    "v_smfmac_f32_16x16x64_bf16 v[124:127], v[104:107], a[176:183], v1\n"
    "ds_read_b128 v[56:59], v5 offset:8192\n"
    "v_cndmask_b32 v80, v80, v84, vcc\n"
    "v_smfmac_f32_16x16x64_bf16 v[128:131], v[104:107], a[184:191], v1\n"
    "ds_read_b128 v[60:63], v5 offset:12288\n"
    "v_cndmask_b32 v81, v81, v85, vcc\n"
    "s_waitcnt lgkmcnt(5)\n"
    "v_mfma_f32_16x16x32_bf16 v[64:67], v[40:43], v[32:35], 0\n"
    "v_cndmask_b32 v82, v82, v86, vcc\n"
    "v_cndmask_b32 v83, v83, v87, vcc\n"
    "s_waitcnt lgkmcnt(4)\n"
    "v_mfma_f32_16x16x32_bf16 v[64:67], v[44:47], v[36:39], v[64:67]\n"
    "ds_read_b128 v[96:99], v2 offset:16384\n"
    "v_maximum3_f32 v78, v80, v81, v82\n"
    "v_maximum3_f32 v78, v78, v83, v83\n"
    "s_waitcnt lgkmcnt(4)\n"
    "v_mfma_f32_16x16x32_bf16 v[64:67], v[48:51], v[132:135], v[64:67]\n"
    "ds_read_b128 v[100:103], v2 offset:17408\n"
    "v_max_f32_dpp v78, v78, v78 row_mirror row_mask:0xf bank_mask:0xf\n"
    "s_waitcnt lgkmcnt(4)\n"
    "v_mfma_f32_16x16x32_bf16 v[64:67], v[52:55], v[72:75], v[64:67]\n"
    "buffer_load_dwordx4 v[28:31], v15, s[40:43], s64 offen sc1\n"
    "v_mov_b32 v77, v78\n"
    "v_smfmac_f32_16x16x64_bf16 v[124:127], v[108:111], a[192:199], v1\n"
    "ds_read_b128 v[40:43], v2 offset:18432\n"
    "v_permlane32_swap_b32 v77, v78\n"
    "v_smfmac_f32_16x16x64_bf16 v[128:131], v[108:111], a[200:207], v1\n"
    "ds_read_b128 v[44:47], v2 offset:19456\n"
    "v_maximum3_f32 v78, v78, v77, v64\n"
    "v_maximum3_f32 v78, v78, v65, v66\n"
    "v_max_f32 v77, v78, v67\n"
    "v_max_f32 v78, v78, v67\n"
    "v_smfmac_f32_16x16x64_bf16 v[124:127], v[112:115], a[208:215], v1\n"
    "ds_read_b128 v[48:51], v2 offset:22528\n"
    "v_permlane16_swap_b32 v77, v78\n"
    "v_max_f32 v78, v78, v77\n"
    "v_mul_f32 v78, 0xbe0293ee, v78\n"
    "v_smfmac_f32_16x16x64_bf16 v[128:131], v[112:115], a[216:223], v1\n"
    "ds_read_b128 v[52:55], v2 offset:23552\n"
    "v_fmamk_f32 v80, v80, 0x3e0293ee, v78\n"
    "v_fmamk_f32 v81, v81, 0x3e0293ee, v78\n"
    "v_exp_f32 v80, v80\n"
    "v_exp_f32 v81, v81\n"
    "v_smfmac_f32_16x16x64_bf16 v[124:127], v[116:119], a[224:231], v1\n"
    "ds_read_b128 v[32:35], v2 offset:24576\n"
    "v_fmamk_f32 v82, v82, 0x3e0293ee, v78\n"
    "v_fmamk_f32 v83, v83, 0x3e0293ee, v78\n"
    "v_exp_f32 v82, v82\n"
    "v_exp_f32 v83, v83\n"
    "v_cvt_pk_bf16_f32 v80, v80, v81\n"
    "v_smfmac_f32_16x16x64_bf16 v[128:131], v[116:119], a[232:239], v1\n"
    "ds_read_b128 v[36:39], v2 offset:25600\n"
    "v_fmamk_f32 v64, v64, 0x3e0293ee, v78\n"
    "v_fmamk_f32 v65, v65, 0x3e0293ee, v78\n"
    "v_exp_f32 v64, v64\n"
    "v_exp_f32 v65, v65\n"
    "v_cvt_pk_bf16_f32 v81, v82, v83\n"
    "v_smfmac_f32_16x16x64_bf16 v[124:127], v[120:123], a[240:247], v1\n"
    "ds_read_b128 v[104:107], v2 offset:26624\n"
    "v_cvt_pk_bf16_f32 v66, v64, v65\n"
    "v_mov_b32_dpp v82, v80 row_mirror row_mask:0xf bank_mask:0xf\n"
    "v_smfmac_f32_16x16x64_bf16 v[128:131], v[120:123], a[248:255], v1\n"
    "ds_read_b128 v[108:111], v2 offset:27648\n"
    "v_mov_b32_dpp v83, v81 row_mirror row_mask:0xf bank_mask:0xf\n"
    "v_cvt_pk_bf16_f32 v67, v64, v65\n"
    "s_waitcnt lgkmcnt(11)\n"
    "v_mfma_f32_16x16x32_bf16 v[132:135], v[56:59], v[80:83], 0\n"
    "ds_read_b128 v[112:115], v2 offset:20480\n"
    "v_cvt_pk_bf16_f32 v124, v124, v125\n"
    "v_cvt_pk_bf16_f32 v125, v126, v127\n"
    "s_waitcnt lgkmcnt(11)\n"
    "v_mfma_f32_16x16x32_bf16 v[120:123], v[60:63], v[80:83], 0\n"
    "ds_read_b128 v[116:119], v2 offset:21504\n"
    "v_cvt_pk_bf16_f32 v126, v128, v129\n"
    "v_cvt_pk_bf16_f32 v127, v130, v131\n"
    "v_mfma_f32_16x16x16_bf16 v[132:135], v[124:125], v[66:67], v[132:135]\n"
    "buffer_load_dwordx4 v[12:15], v15, s[40:43], s60 offen sc1\n"
    "ds_read_b128 v[68:71], v2 offset:28672\n"
    "s_nop 0\n"
    "v_mfma_f32_16x16x16_bf16 v[120:123], v[126:127], v[66:67], v[120:123]\n"
    "ds_read_b128 v[72:75], v2 offset:29696\n"
    "buffer_load_dword v79, v0, s[40:43], s58 offen offset:3072 sc1\n"
    "v_mov_b64 v[56:57], s[46:47]\n"
    "v_mov_b64 v[58:59], s[46:47]\n"
    "v_xor_b32 v76, 0x200, v6\n"
    "v_cvt_pk_bf16_f32 v84, v132, v133\n"
    "v_cvt_pk_bf16_f32 v85, v134, v135\n"
    "v_xor_b32 v77, 0x400, v6\n"
    "s_not_b64 exec, vcc\n"
    "v_cvt_pk_bf16_f32 v86, v120, v121\n"
    "v_cvt_pk_bf16_f32 v87, v122, v123\n"
    "ds_write_b128 v6, v[84:87] offset:26880\n"
    "s_mov_b64 exec, -1\n"
    "s_cmp_eq_u32 s48, 63\n"
    "v_mfma_f32_16x16x32_bf16 v[128:131], v[80:83], v[56:59], 0\n"
    "v_mov_b32 v67, 0\n"
    "v_mov_b64 v[132:133], 0\n"
    "v_mov_b64 v[134:135], 0\n"
    "v_mov_b32_dpp v84, v84 quad_perm:[0,1,0,1] row_mask:0xf bank_mask:0xf\n"
    "v_mov_b32_dpp v85, v85 quad_perm:[0,1,0,1] row_mask:0xf bank_mask:0xf\n"
    "v_mov_b32_dpp v86, v86 quad_perm:[0,1,0,1] row_mask:0xf bank_mask:0xf\n"
    "v_mov_b32_dpp v87, v87 quad_perm:[0,1,0,1] row_mask:0xf bank_mask:0xf\n"
    "v_mfma_f32_16x16x16_bf16 v[128:131], v[66:67], v[56:57], v[128:131]\n"
    "v_mov_b64 v[80:81], 0\n"
    "v_mov_b64 v[82:83], 0\n"
    "v_smfmac_f32_16x16x64_bf16 v[132:135], v[84:87], v[88:95], v1\n"
    "v_xor_b32 v78, 0x600, v6\n"
    "s_waitcnt lgkmcnt(13)\n"
    "v_smfmac_f32_16x16x64_bf16 v[80:83], v[84:87], v[96:103], v1\n"
    "s_waitcnt lgkmcnt(0)\n"
    "s_barrier\n"
    "s_cbranch_scc1 .Lend_%=\n"
    "ds_read_b128 v[56:59], v76 offset:26880\n"
    "ds_read_b128 v[60:63], v77 offset:26880\n"
    "ds_read_b128 v[120:123], v78 offset:26880\n"
    "ds_read_b128 v[88:91], v2 offset:30720\n"
    "v_rcp_f32 v128, v128\n"
    "v_rcp_f32 v129, v129\n"
    "s_waitcnt lgkmcnt(3)\n"
    "v_smfmac_f32_16x16x64_bf16 v[132:135], v[56:59], v[40:47], v1\n"
    "ds_read_b128 v[40:43], v0 offset:26880\n"
    "v_mov_b32 v78, s73\n"
    "s_waitcnt lgkmcnt(3)\n"
    "v_smfmac_f32_16x16x64_bf16 v[132:135], v[60:63], v[48:55], v1\n"
    "ds_read_b128 v[92:95], v2 offset:31744\n"
    "s_waitcnt lgkmcnt(3)\n"
    "v_smfmac_f32_16x16x64_bf16 v[132:135], v[120:123], v[104:111], v1\n"
    "ds_read_b128 v[96:99], v2 offset:32768\n"
    "v_mov_b64 v[64:65], 0\n"
    "v_smfmac_f32_16x16x64_bf16 v[80:83], v[56:59], v[112:119], v1\n"
    "ds_read_b128 v[100:103], v2 offset:33792\n"
    "v_mov_b64 v[66:67], 0\n"
    "v_smfmac_f32_16x16x64_bf16 v[80:83], v[60:63], v[32:39], v1\n"
    "ds_read_b128 v[44:47], v0 offset:27904\n"
    "v_mul_f32 v132, v132, v128\n"
    "v_mul_f32 v133, v133, v129\n"
    "v_smfmac_f32_16x16x64_bf16 v[80:83], v[120:123], v[68:75], v1\n"
    "global_load_dword v76, v78, %1 sc1\n"
    "v_mul_f32 v134, v134, v128\n"
    "v_mul_f32 v135, v135, v129\n"
    "s_waitcnt lgkmcnt(3)\n"
    "v_smfmac_f32_16x16x64_bf16 v[64:67], v[40:43], v[88:95], v1\n"
    "global_load_dword v77, v78, %1 offset:128 sc1\n"
    "v_cvt_pk_bf16_f32 v132, v132, v133\n"
    "v_cvt_pk_bf16_f32 v133, v134, v135\n"
    "v_bfi_b32 v132, 1, s48, v132\n"
    "s_waitcnt lgkmcnt(0)\n"
    "v_smfmac_f32_16x16x64_bf16 v[64:67], v[44:47], v[96:103], v1\n"
    "v_pk_mul_f32 v[80:81], v[80:81], v[128:129]\n"
    "v_pk_mul_f32 v[82:83], v[82:83], v[128:129]\n"
    "v_cvt_pk_bf16_f32 v134, v80, v81\n"
    "v_cvt_pk_bf16_f32 v135, v82, v83\n"
    "v_bfi_b32 v134, 1, s48, v134\n"
    "buffer_store_dwordx4 v[132:135], v7, s[40:43], 0 offen sc1\n"
    "s_nop 2\n"
    "v_pk_add_f32 v[64:65], v[64:65], v[66:67]\n"
    "v_pk_mul_f32 v[64:65], v[64:65], v[128:129]\n"
    "v_cvt_pk_bf16_f32 v66, v64, v65\n"
    "v_bfi_b32 v64, 1, s48, v66\n"
    "s_nop 0\n"
    "v_mov_b32_dpp v65, v66 row_ror:8 row_mask:0xf bank_mask:0xf\n"
    "buffer_store_dwordx2 v[64:65], v8, s[40:43], 0 offen sc1\n"
    "s_memrealtime s[38:39]\n"
    "ds_write_b128 v5, v[124:127] offset:22784\n"
    "s_cmp_ge_u32 %2, 2\n"
    "s_cbranch_scc1 .Lw3_%=\n"
    "v_and_b32 v104, 0x70, v0\n"
    "ds_read_b128 v[112:115], v104 offset:31936\n"
    "s_movk_i32 s73, 0x60\n"
    "s_movk_i32 s51, 0x80\n"
    "v_lshrrev_b32 v104, 1, v0\n"
    "v_bfi_b32 v104, s73, v104, v0\n"
    "v_lshlrev_b32 v105, 2, v0\n"
    "v_bfi_b32 v104, s51, v105, v104\n"
    "s_waitcnt lgkmcnt(0)\n"
    "s_barrier\n"
    "s_waitcnt vmcnt(4)\n"
    "s_mov_b32 s56, 0\n"
    ".Lr4_a%=:\n"
    "v_subrev_u32 v78, s52, v79\n"
    "v_cmp_eq_u32 vcc, -1, v79\n"
    "s_cbranch_vccnz .Lr4_%=\n"
    ".Lr4_x%=:\n"
    "v_max_i32_dpp v78, v78, v78 row_ror:8 row_mask:0xf bank_mask:0xf\n"
    "s_nop 1\n"
    "v_max_i32_dpp v78, v78, v78 row_ror:4 row_mask:0xf bank_mask:0xf\n"
    "s_nop 1\n"
    "v_max_i32_dpp v78, v78, v78 row_ror:2 row_mask:0xf bank_mask:0xf\n"
    "s_nop 1\n"
    "v_max_i32_dpp v78, v78, v78 row_ror:1 row_mask:0xf bank_mask:0xf\n"
    "s_nop 0\n"
    "v_readlane_b32 s95, v78, 0\n"
    "v_readlane_b32 s73, v78, 16\n"
    "v_readlane_b32 s51, v78, 32\n"
    "v_readlane_b32 s53, v78, 48\n"
    "s_max_i32 s95, s95, s73\n"
    "s_max_i32 s51, s51, s53\n"
    "s_max_i32 s95, s95, s51\n"
    "s_min_i32 s95, s95, 1024\n"
    "s_add_u32 s95, s95, s52\n"
    "s_add_u32 s95, s95, s55\n"
    "s_cmp_lt_i32 s71, 0\n"
    "s_cbranch_scc1 .Lnored_%=\n"
    "s_mov_b32 s56, 0\n"
    "s_sub_u32 s53, s95, s54\n"
    "s_waitcnt lgkmcnt(0)\n"
    ".Lgl5_%=:\n"
    "s_memrealtime s[50:51]\n"
    "s_waitcnt lgkmcnt(0)\n"
    "s_sub_u32 s50, s50, s53\n"
    "s_cmp_lt_i32 s50, 0\n"
    "s_cbranch_scc1 .Lgl5_%=\n"
    "buffer_load_dwordx4 v[88:91], v104, s[40:43], s65 offen sc1\n"
    "buffer_load_dwordx4 v[92:95], v104, s[40:43], s65 offen offset:1024 sc1\n"
    "buffer_load_dwordx4 v[96:99], v104, s[40:43], s65 offen offset:2048 sc1\n"
    "buffer_load_dwordx4 v[100:103], v104, s[40:43], s65 offen offset:3072 sc1\n"
    "ds_read_b128 v[32:35], v2\n"
    "ds_read_b128 v[36:39], v2 offset:1024\n"
    "ds_read_b128 v[40:43], v2 offset:2048\n"
    "ds_read_b128 v[44:47], v2 offset:3072\n"
    "ds_read_b128 v[48:51], v2 offset:4096\n"
    "ds_read_b128 v[52:55], v2 offset:5120\n"
    "ds_read_b128 v[56:59], v2 offset:6144\n"
    "ds_read_b128 v[60:63], v2 offset:7168\n"
    "ds_read_b128 v[64:67], v2 offset:8192\n"
    "ds_read_b128 v[68:71], v2 offset:9216\n"
    "ds_read_b128 v[80:83], v2 offset:12288\n"
    "ds_read_b128 v[84:87], v2 offset:13312\n"
    "s_cmp_eq_u32 %2, 0\n"
    "s_cselect_b64 exec, 1, 0\n"
    "v_and_b32 v106, -2, s95\n"
    "ds_write_b32 v11, v106 offset:31876\n"
    "s_mov_b64 exec, -1\n"
    "v_xor_b32 v132, 0x800, v10\n"
    "v_xor_b32 v133, 0x1000, v10\n"
    "v_xor_b32 v134, 0x1800, v10\n"
    "v_lshrrev_b32 v135, 2, v5\n"
    "s_movk_i32 s73, 0x180\n"
    "v_lshlrev_b32 v120, 1, v0\n"
    "v_bfi_b32 v120, s73, v120, v0\n"
    "v_lshrrev_b32 v122, 2, v0\n"
    "v_bfi_b32 v120, 64, v122, v120\n"
    "v_lshlrev_b32 v121, 1, v135\n"
    "v_bfi_b32 v121, s73, v121, v135\n"
    "v_lshrrev_b32 v122, 2, v135\n"
    "v_bfi_b32 v121, 64, v122, v121\n"
    "s_mul_i32 s73, s48, 3\n"
    "s_add_u32 s73, s73, s94\n"
    "s_and_b32 s51, s73, 1\n"
    "s_lshl_b32 s51, s51, 12\n"
    "s_lshr_b32 s73, s73, 1\n"
    "s_lshl_b32 s73, s73, 14\n"
    "s_add_u32 s73, s73, s51\n"
    "s_add_u32 s66, s73, s68\n"
    "s_lshr_b32 s67, %2, 1\n"
    "s_mul_i32 s73, s48, 3\n"
    "s_add_u32 s73, s73, s67\n"
    "s_and_b32 s51, s73, 1\n"
    "s_lshl_b32 s51, s51, 12\n"
    "s_lshr_b32 s73, s73, 1\n"
    "s_lshl_b32 s73, s73, 14\n"
    "s_add_u32 s73, s73, s51\n"
    "s_add_u32 s67, s73, s68\n"
    "s_and_b32 s73, %2, 1\n"
    "s_lshl_b32 s73, s73, 11\n"
    "s_add_u32 s67, s67, s73\n"
    "s_mov_b32 s72, s58\n"
    "s_mul_i32 s73, s48, 3\n"
    "s_add_u32 s73, s73, 5\n"
    "s_and_b32 s51, s73, 1\n"
    "s_lshl_b32 s51, s51, 12\n"
    "s_lshr_b32 s73, s73, 1\n"
    "s_lshl_b32 s73, s73, 14\n"
    "s_add_u32 s73, s73, s51\n"
    "s_add_u32 s58, s73, s68\n"
    "s_waitcnt vmcnt(6)\n"
    "v_and_b32 v77, v76, v77\n"
    "s_nop 0\n"
    "v_readfirstlane_b32 s70, v77\n"
    "ds_read_b128 v[72:75], v2 offset:10240\n"
    "ds_read_b128 v[76:79], v2 offset:11264\n"
    "v_mov_b64 v[124:125], 0\n"
    "v_mov_b64 v[126:127], 0\n"
    "v_mov_b64 v[128:129], 0\n"
    "v_mov_b64 v[130:131], 0\n"
    "s_sub_u32 s73, s38, s52\n"
    "s_lshl_b32 s73, s73, 3\n"
    "s_sub_u32 s73, s73, s96\n"
    "s_ashr_i32 s73, s73, 2\n"
    "s_max_i32 s73, s73, -16\n"
    "s_min_i32 s73, s73, 16\n"
    "s_add_u32 s96, s96, s73\n"
    "s_waitcnt lgkmcnt(0)\n"
    ".Lred_%=:\n"
    "s_waitcnt vmcnt(3)\n"
    "v_mfma_f32_16x16x32_bf16 v[108:111], v[88:91], v[112:115], 0\n"
    "v_bitop3_b32 v116, v88, v90, s48 bitop3:0x7e\n"
    "s_waitcnt vmcnt(2)\n"
    "v_mfma_f32_16x16x32_bf16 v[108:111], v[92:95], v[112:115], v[108:111]\n"
    "v_bitop3_b32 v117, v92, v94, s48 bitop3:0x7e\n"
    "s_waitcnt vmcnt(1)\n"
    "v_mfma_f32_16x16x32_bf16 v[108:111], v[96:99], v[112:115], v[108:111]\n"
    "v_bitop3_b32 v118, v96, v98, s48 bitop3:0x7e\n"
    "v_or3_b32 v116, v116, v117, v118\n"
    "s_waitcnt vmcnt(0)\n"
    "v_mfma_f32_16x16x32_bf16 v[108:111], v[100:103], v[112:115], v[108:111]\n"
    "v_bitop3_b32 v119, v100, v102, s48 bitop3:0x7e\n"
    "v_or_b32 v116, v116, v119\n"
    "v_and_b32 v116, 1, v116\n"
    "v_cmp_ne_u32 vcc, 0, v116\n"
    "s_cbranch_vccnz .Lrf_%=\n"
    ".Lrx_%=:\n"
    "s_nop 3\n"
    "v_pk_add_f32 v[108:109], v[108:109], v[110:111]\n"
    "v_cvt_pk_bf16_f32 v108, v108, v109\n"
    "v_min_u32 v108, -2, v108\n"
    "buffer_store_dword v108, v9, s[40:43], s66 offen sc1\n"
    ".Lpub_%=:\n"
    "s_cmp_eq_u32 s59, 0\n"
    "s_cbranch_scc1 .Lr6_%=\n"
    ".Lr6_x%=:\n"
    "s_add_u32 s73, s48, 2\n"
    "s_and_b32 s73, s73, 63\n"
    "s_lshr_b32 s51, s73, 2\n"
    "s_xor_b32 s51, s51, s69\n"
    "s_and_b32 s51, s51, 1\n"
    "s_lshl_b32 s51, s51, 13\n"
    "s_lshl_b32 s73, s73, 5\n"
    "s_add_u32 s73, s73, %4\n"
    "s_lshl_b32 s73, s73, 14\n"
    "s_add_u32 s73, s73, s51\n"
    "s_add_u32 s60, s73, 0x6000000\n"
    "s_add_u32 s61, s73, 0x6001000\n"
    "s_add_u32 s62, s73, 0x8000000\n"
    "s_add_u32 s63, s73, 0x8001000\n"
    "s_lshl_b32 s51, %5, 12\n"
    "s_add_u32 s73, s73, s51\n"
    "s_add_u32 s64, s73, 0x4000000\n"
    "s_add_u32 s73, s48, 2\n"
    "s_and_b32 s51, s73, 32\n"
    "s_and_b32 s73, s73, 31\n"
    "s_lshl3_add_u32 s73, s51, s73\n"
    "s_lshl2_add_u32 s73, s73, s92\n"
    "v_mov_b32 v101, s73\n"
    ".Lg_%=:\n"
    "s_waitcnt lgkmcnt(0)\n"
    "s_barrier\n"
    "ds_read_b32 v97, v11 offset:31876\n"
    "s_mov_b32 s56, 0\n"
    "s_waitcnt lgkmcnt(0)\n"
    "v_readfirstlane_b32 s95, v97\n"
    "s_and_b32 s73, s95, 1\n"
    "s_lshl_b32 s73, s73, 22\n"
    "s_or_b32 s67, s67, s73\n"
    "s_or_b32 s72, s72, s73\n"
    "s_sub_u32 s53, s95, 0\n"
    "s_waitcnt lgkmcnt(0)\n"
    ".Lgl7_%=:\n"
    "s_memrealtime s[50:51]\n"
    "s_waitcnt lgkmcnt(0)\n"
    "s_sub_u32 s50, s50, s53\n"
    "s_cmp_lt_i32 s50, 0\n"
    "s_cbranch_scc1 .Lgl7_%=\n"
    "buffer_load_dwordx4 v[88:91], v120, s[40:43], s67 offen sc1\n"
    "buffer_load_dwordx4 v[92:95], v120, s[40:43], s67 offen offset:1024 sc1\n"
    "buffer_load_dword v96, v121, s[40:43], s72 offen sc1\n"
    "s_waitcnt vmcnt(5)\n"
    "ds_write_b128 v5, v[12:15]\n"
    "ds_write_b128 v5, v[16:19] offset:4096\n"
    "ds_write_b128 v5, v[28:31] offset:16384\n"
    "v_swap_b32 v22, v24\n"
    "v_swap_b32 v23, v25\n"
    "ds_write_b128 v5, v[20:23] offset:8192\n"
    "ds_write_b128 v5, v[24:27] offset:12288\n"
    "v_mov_b64 v[16:17], 0\n"
    "v_mov_b64 v[18:19], 0\n"
    "v_mov_b64 v[20:21], 0\n"
    "v_mov_b64 v[22:23], 0\n"
    "v_mov_b64 v[24:25], 0\n"
    "v_mov_b64 v[26:27], 0\n"
    "v_mov_b64 v[28:29], 0\n"
    "v_mov_b64 v[30:31], 0\n"
    "v_mov_b32 v14, v101\n"
    "global_load_dword v12, v14, %1 sc1\n"
    "global_load_dword v13, v14, %1 offset:128 sc1\n"
    "s_cmp_ge_u32 %2, 2\n"
    "s_cbranch_scc1 .Lcob_%=\n"
    ".Lcod_%=:\n"
    "global_load_dword v14, v14, %1 sc1\n"
    ".Lcox_%=:\n"
    "s_add_u32 s48, s48, 1\n"
    "s_branch .Lstep_%=\n"
    ".Lcob_%=:\n"
    "s_add_u32 s73, s48, 0\n"
    "s_and_b32 s51, s73, 32\n"
    "s_and_b32 s73, s73, 31\n"
    "s_lshl3_add_u32 s73, s51, s73\n"
    "s_lshl2_add_u32 s73, s73, s93\n"
    "v_mov_b32 v100, s73\n"
    "v_mov_b32 v101, 1\n"
    "s_waitcnt vmcnt(5)\n"
    "s_mov_b64 exec, 1\n"
    "ds_add_rtn_u32 v101, v11, v101 offset:31872\n"
    "s_mov_b64 exec, -1\n"
    "s_waitcnt lgkmcnt(0)\n"
    "v_readfirstlane_b32 s73, v101\n"
    "v_mov_b32 v101, 1\n"
    "s_bitcmp1_b32 s73, 0\n"
    "s_cbranch_scc0 .Lcod_%=\n"
    "s_mov_b64 exec, 1\n"
    "global_store_dword v100, v101, %1 sc1\n"
    "s_mov_b64 exec, -1\n"
    "s_branch .Lcox_%=\n"
    ".Lgf_%=:\n"
    "s_bitset1_b32 s67, 22\n"
    "s_bitset1_b32 s72, 22\n"
    "s_waitcnt lgkmcnt(0)\n"
    "buffer_load_dwordx4 v[88:91], v120, s[40:43], s67 offen sc1\n"
    "buffer_load_dwordx4 v[92:95], v120, s[40:43], s67 offen offset:1024 sc1\n"
    "buffer_load_dword v96, v121, s[40:43], s72 offen sc1\n"
    "s_waitcnt vmcnt(0)\n"
    "v_mov_b64 v[16:17], 0\n"
    "v_mov_b64 v[18:19], 0\n"
    "v_mov_b64 v[20:21], 0\n"
    "v_mov_b64 v[22:23], 0\n"
    "v_mov_b64 v[24:25], 0\n"
    "v_mov_b64 v[26:27], 0\n"
    "v_mov_b64 v[28:29], 0\n"
    "v_mov_b64 v[30:31], 0\n"
    "v_mov_b64 v[124:125], 0\n"
    "v_mov_b64 v[126:127], 0\n"
    "v_mov_b64 v[128:129], 0\n"
    "v_mov_b64 v[130:131], 0\n"
    "s_add_u32 s56, s56, 1\n"
    "s_cmp_lt_u32 s56, s57\n"
    "s_cbranch_scc1 .Lstep_%=\n"
    "s_mov_b32 s57, 0\n"
    "v_mov_b32 v135, 1\n"
    "global_store_dword v11, v135, %1 sc1\n"
    "s_branch .Lgx_%=\n"
    ".Lrf_%=:\n"
    "s_memrealtime s[50:51]\n"
    "s_waitcnt lgkmcnt(0)\n"
    "s_add_u32 s95, s50, 62\n"
    "v_or_b32 v106, 1, s95\n"
    "ds_write_b32 v11, v106 offset:31876\n"
    "buffer_load_dwordx4 v[88:91], v104, s[40:43], s65 offen sc1\n"
    "buffer_load_dwordx4 v[92:95], v104, s[40:43], s65 offen offset:1024 sc1\n"
    "buffer_load_dwordx4 v[96:99], v104, s[40:43], s65 offen offset:2048 sc1\n"
    "buffer_load_dwordx4 v[100:103], v104, s[40:43], s65 offen offset:3072 sc1\n"
    "s_add_u32 s56, s56, 1\n"
    "s_cmp_lt_u32 s56, s57\n"
    "s_cbranch_scc1 .Lred_%=\n"
    "s_mov_b32 s57, 0\n"
    "v_mov_b32 v135, 1\n"
    "global_store_dword v11, v135, %1 sc1\n"
    "s_branch .Lrx_%=\n"
    ".Lnored_%=:\n"
    "s_waitcnt vmcnt(0)\n"
    "ds_read_b128 v[32:35], v2\n"
    "ds_read_b128 v[36:39], v2 offset:1024\n"
    "ds_read_b128 v[40:43], v2 offset:2048\n"
    "ds_read_b128 v[44:47], v2 offset:3072\n"
    "ds_read_b128 v[48:51], v2 offset:4096\n"
    "ds_read_b128 v[52:55], v2 offset:5120\n"
    "ds_read_b128 v[56:59], v2 offset:6144\n"
    "ds_read_b128 v[60:63], v2 offset:7168\n"
    "ds_read_b128 v[64:67], v2 offset:8192\n"
    "ds_read_b128 v[68:71], v2 offset:9216\n"
    "ds_read_b128 v[80:83], v2 offset:12288\n"
    "ds_read_b128 v[84:87], v2 offset:13312\n"
    "s_cmp_eq_u32 %2, 0\n"
    "s_cselect_b64 exec, 1, 0\n"
    "v_and_b32 v106, -2, s95\n"
    "ds_write_b32 v11, v106 offset:31876\n"
    "s_mov_b64 exec, -1\n"
    "v_xor_b32 v132, 0x800, v10\n"
    "v_xor_b32 v133, 0x1000, v10\n"
    "v_xor_b32 v134, 0x1800, v10\n"
    "v_lshrrev_b32 v135, 2, v5\n"
    "s_movk_i32 s73, 0x180\n"
    "v_lshlrev_b32 v120, 1, v0\n"
    "v_bfi_b32 v120, s73, v120, v0\n"
    "v_lshrrev_b32 v122, 2, v0\n"
    "v_bfi_b32 v120, 64, v122, v120\n"
    "v_lshlrev_b32 v121, 1, v135\n"
    "v_bfi_b32 v121, s73, v121, v135\n"
    "v_lshrrev_b32 v122, 2, v135\n"
    "v_bfi_b32 v121, 64, v122, v121\n"
    "s_mul_i32 s73, s48, 3\n"
    "s_add_u32 s73, s73, s94\n"
    "s_and_b32 s51, s73, 1\n"
    "s_lshl_b32 s51, s51, 12\n"
    "s_lshr_b32 s73, s73, 1\n"
    "s_lshl_b32 s73, s73, 14\n"
    "s_add_u32 s73, s73, s51\n"
    "s_add_u32 s66, s73, s68\n"
    "s_lshr_b32 s67, %2, 1\n"
    "s_mul_i32 s73, s48, 3\n"
    "s_add_u32 s73, s73, s67\n"
    "s_and_b32 s51, s73, 1\n"
    "s_lshl_b32 s51, s51, 12\n"
    "s_lshr_b32 s73, s73, 1\n"
    "s_lshl_b32 s73, s73, 14\n"
    "s_add_u32 s73, s73, s51\n"
    "s_add_u32 s67, s73, s68\n"
    "s_and_b32 s73, %2, 1\n"
    "s_lshl_b32 s73, s73, 11\n"
    "s_add_u32 s67, s67, s73\n"
    "s_mov_b32 s72, s58\n"
    "s_mul_i32 s73, s48, 3\n"
    "s_add_u32 s73, s73, 5\n"
    "s_and_b32 s51, s73, 1\n"
    "s_lshl_b32 s51, s51, 12\n"
    "s_lshr_b32 s73, s73, 1\n"
    "s_lshl_b32 s73, s73, 14\n"
    "s_add_u32 s73, s73, s51\n"
    "s_add_u32 s58, s73, s68\n"
    "s_waitcnt vmcnt(6)\n"
    "v_and_b32 v77, v76, v77\n"
    "s_nop 0\n"
    "v_readfirstlane_b32 s70, v77\n"
    "ds_read_b128 v[72:75], v2 offset:10240\n"
    "ds_read_b128 v[76:79], v2 offset:11264\n"
    "v_mov_b64 v[124:125], 0\n"
    "v_mov_b64 v[126:127], 0\n"
    "v_mov_b64 v[128:129], 0\n"
    "v_mov_b64 v[130:131], 0\n"
    "s_sub_u32 s73, s38, s52\n"
    "s_lshl_b32 s73, s73, 3\n"
    "s_sub_u32 s73, s73, s96\n"
    "s_ashr_i32 s73, s73, 2\n"
    "s_max_i32 s73, s73, -16\n"
    "s_min_i32 s73, s73, 16\n"
    "s_add_u32 s96, s96, s73\n"
    "s_waitcnt lgkmcnt(0)\n"
    "s_branch .Lpub_%=\n"
    ".Lw3_%=:\n"
    "ds_read_b128 v[32:35], v0 offset:16384\n"
    "ds_read_b128 v[48:51], v0\n"
    "ds_read_b128 v[88:91], v0 offset:4096\n"
    "ds_read_b128 v[104:107], v4 offset:20480\n"
    "ds_read_b128 v[36:39], v0 offset:17408\n"
    "ds_read_b128 v[52:55], v0 offset:1024\n"
    "ds_read_b128 v[92:95], v0 offset:5120\n"
    "ds_read_b128 v[108:111], v4 offset:20544\n"
    "ds_read_b128 v[40:43], v0 offset:18432\n"
    "ds_read_b128 v[56:59], v0 offset:2048\n"
    "ds_read_b128 v[96:99], v0 offset:6144\n"
    "ds_read_b128 v[112:115], v4 offset:20608\n"
    "s_waitcnt lgkmcnt(10)\n"
    "v_mfma_f32_16x16x32_bf16 v[120:123], v[48:51], v[32:35], 0\n"
    "s_barrier\n"
    "ds_read_b128 v[44:47], v0 offset:19456\n"
    "ds_read_b128 v[60:63], v0 offset:3072\n"
    "ds_read_b128 v[100:103], v0 offset:7168\n"
    "ds_read_b128 v[116:119], v4 offset:20672\n"
    "s_waitcnt lgkmcnt(13)\n"
    "v_mfma_f32_16x16x32_bf16 v[124:127], v[88:91], v[32:35], 0\n"
    "s_waitcnt lgkmcnt(12)\n"
    "v_mfma_f32_16x16x32_bf16 v[128:131], v[104:107], v[32:35], 0\n"
    "s_waitcnt lgkmcnt(10)\n"
    "v_mfma_f32_16x16x32_bf16 v[120:123], v[52:55], v[36:39], v[120:123]\n"
    "s_waitcnt lgkmcnt(9)\n"
    "v_mfma_f32_16x16x32_bf16 v[124:127], v[92:95], v[36:39], v[124:127]\n"
    "ds_read_b128 v[64:67], v10 offset:4096\n"
    "s_waitcnt lgkmcnt(9)\n"
    "v_mfma_f32_16x16x32_bf16 v[128:131], v[108:111], v[36:39], v[128:131]\n"
    "ds_read_b128 v[68:71], v10 offset:8192\n"
    "s_waitcnt lgkmcnt(8)\n"
    "v_mfma_f32_16x16x32_bf16 v[120:123], v[56:59], v[40:43], v[120:123]\n"
    "ds_read_b128 v[72:75], v10 offset:5120\n"
    "s_waitcnt lgkmcnt(8)\n"
    "v_mfma_f32_16x16x32_bf16 v[124:127], v[96:99], v[40:43], v[124:127]\n"
    "ds_read_b128 v[80:83], v10 offset:9216\n"
    "s_waitcnt lgkmcnt(8)\n"
    "v_mfma_f32_16x16x32_bf16 v[128:131], v[112:115], v[40:43], v[128:131]\n"
    "ds_read_b128 v[84:87], v11 offset:31888\n"
    "s_waitcnt lgkmcnt(7)\n"
    "v_mfma_f32_16x16x32_bf16 v[120:123], v[60:63], v[44:47], v[120:123]\n"
    "ds_read_b64 v[134:135], v11 offset:31904\n"
    "s_waitcnt lgkmcnt(7)\n"
    "v_mfma_f32_16x16x32_bf16 v[124:127], v[100:103], v[44:47], v[124:127]\n"
    "ds_read_b128 v[88:91], v10 offset:18688\n"
    "s_waitcnt lgkmcnt(7)\n"
    "v_mfma_f32_16x16x32_bf16 v[128:131], v[116:119], v[44:47], v[128:131]\n"
    "ds_read_b128 v[92:95], v10 offset:19712\n"
    "v_mov_b32 v132, 0xd01502f9\n"
    "ds_read_b128 v[32:35], v2\n"
    "ds_read_b128 v[36:39], v2 offset:1024\n"
    "ds_read_b128 v[40:43], v2 offset:2048\n"
    "ds_read_b128 v[44:47], v2 offset:3072\n"
    "v_cndmask_b32 v120, v132, v120, s[76:77]\n"
    "v_cndmask_b32 v121, v132, v121, s[78:79]\n"
    "v_cndmask_b32 v122, v132, v122, s[80:81]\n"
    "v_cndmask_b32 v123, v132, v123, s[82:83]\n"
    "v_cndmask_b32 v124, v132, v124, s[84:85]\n"
    "v_cndmask_b32 v125, v132, v125, s[86:87]\n"
    "v_cndmask_b32 v126, v132, v126, s[88:89]\n"
    "v_cndmask_b32 v127, v132, v127, s[90:91]\n"
    "v_maximum3_f32 v133, v128, v129, v120\n"
    "v_maximum3_f32 v133, v133, v121, v122\n"
    "v_maximum3_f32 v133, v133, v123, v124\n"
    "v_maximum3_f32 v133, v133, v125, v126\n"
    "v_maximum3_f32 v133, v133, v127, v127\n"
    "v_mov_b32 v132, v133\n"
    "ds_read_b128 v[48:51], v2 offset:4096\n"
    "ds_read_b128 v[52:55], v2 offset:5120\n"
    "v_permlane16_swap_b32 v132, v133\n"
    "v_maximum3_f32 v133, v133, v132, v132\n"
    "v_mov_b32 v132, v133\n"
    "ds_read_b128 v[56:59], v2 offset:6144\n"
    "ds_read_b128 v[60:63], v2 offset:7168\n"
    "v_permlane32_swap_b32 v132, v133\n"
    "v_maximum3_f32 v133, v133, v132, v132\n"
    "v_mul_f32 v133, 0xbe0293ee, v133\n"
    "v_fmamk_f32 v120, v120, 0x3e0293ee, v133\n"
    "v_fmamk_f32 v121, v121, 0x3e0293ee, v133\n"
    "v_fmamk_f32 v122, v122, 0x3e0293ee, v133\n"
    "v_fmamk_f32 v123, v123, 0x3e0293ee, v133\n"
    "v_fmamk_f32 v124, v124, 0x3e0293ee, v133\n"
    "v_fmamk_f32 v125, v125, 0x3e0293ee, v133\n"
    "v_fmamk_f32 v126, v126, 0x3e0293ee, v133\n"
    "v_fmamk_f32 v127, v127, 0x3e0293ee, v133\n"
    "v_fmamk_f32 v128, v128, 0x3e0293ee, v133\n"
    "v_fmamk_f32 v129, v129, 0x3e0293ee, v133\n"
    "v_exp_f32 v120, v120\n"
    "v_exp_f32 v121, v121\n"
    "v_exp_f32 v122, v122\n"
    "v_exp_f32 v123, v123\n"
    "v_exp_f32 v124, v124\n"
    "v_exp_f32 v125, v125\n"
    "v_exp_f32 v126, v126\n"
    "v_exp_f32 v127, v127\n"
    "v_exp_f32 v128, v128\n"
    "v_exp_f32 v129, v129\n"
    "v_cvt_pk_bf16_f32 v120, v120, v121\n"
    "v_cvt_pk_bf16_f32 v121, v122, v123\n"
    "v_cvt_pk_bf16_f32 v122, v124, v125\n"
    "v_cvt_pk_bf16_f32 v123, v126, v127\n"
    "v_cvt_pk_bf16_f32 v124, v128, v129\n"
    "v_cvt_pk_bf16_f32 v125, v128, v129\n"
    "s_waitcnt lgkmcnt(11)\n"
    "v_mfma_f32_16x16x32_bf16 v[100:103], v[84:87], v[120:123], 0\n"
    "v_mfma_f32_16x16x32_bf16 v[104:107], v[64:67], v[120:123], 0\n"
    "v_mfma_f32_16x16x32_bf16 v[108:111], v[68:71], v[120:123], 0\n"
    "s_waitcnt lgkmcnt(10)\n"
    "v_mfma_f32_16x16x16_bf16 v[100:103], v[134:135], v[124:125], v[100:103]\n"
    "v_mfma_f32_16x16x32_bf16 v[112:115], v[72:75], v[120:123], 0\n"
    "v_mfma_f32_16x16x32_bf16 v[116:119], v[80:83], v[120:123], 0\n"
    "s_waitcnt lgkmcnt(9)\n"
    "v_mfma_f32_16x16x16_bf16 v[104:107], v[88:89], v[124:125], v[104:107]\n"
    "ds_read_b128 v[64:67], v2 offset:8192\n"
    "v_mfma_f32_16x16x16_bf16 v[108:111], v[90:91], v[124:125], v[108:111]\n"
    "ds_read_b128 v[68:71], v2 offset:9216\n"
    "v_rcp_f32 v132, v100\n"
    "s_waitcnt lgkmcnt(10)\n"
    "v_mfma_f32_16x16x16_bf16 v[112:115], v[92:93], v[124:125], v[112:115]\n"
    "ds_read_b128 v[80:83], v2 offset:12288\n"
    "v_mfma_f32_16x16x16_bf16 v[116:119], v[94:95], v[124:125], v[116:119]\n"
    "ds_read_b128 v[84:87], v2 offset:13312\n"
    "v_pk_mul_f32 v[104:105], v[104:105], v[132:133] op_sel_hi:[1,0]\n"
    "v_pk_mul_f32 v[106:107], v[106:107], v[132:133] op_sel_hi:[1,0]\n"
    "v_pk_mul_f32 v[108:109], v[108:109], v[132:133] op_sel_hi:[1,0]\n"
    "v_pk_mul_f32 v[110:111], v[110:111], v[132:133] op_sel_hi:[1,0]\n"
    "v_cvt_pk_bf16_f32 v104, v104, v108\n"
    "v_cvt_pk_bf16_f32 v105, v105, v109\n"
    "v_cvt_pk_bf16_f32 v106, v106, v110\n"
    "v_cvt_pk_bf16_f32 v107, v107, v111\n"
    "v_pk_mul_f32 v[112:113], v[112:113], v[132:133] op_sel_hi:[1,0]\n"
    "v_pk_mul_f32 v[114:115], v[114:115], v[132:133] op_sel_hi:[1,0]\n"
    "v_pk_mul_f32 v[116:117], v[116:117], v[132:133] op_sel_hi:[1,0]\n"
    "v_pk_mul_f32 v[118:119], v[118:119], v[132:133] op_sel_hi:[1,0]\n"
    "v_cvt_pk_bf16_f32 v112, v112, v116\n"
    "v_cvt_pk_bf16_f32 v113, v113, v117\n"
    "v_cvt_pk_bf16_f32 v114, v114, v118\n"
    "v_cvt_pk_bf16_f32 v115, v115, v119\n"
    "s_waitcnt vmcnt(2)\n"
    "s_cmp_eq_u32 s59, 0\n"
    "s_cbranch_scc1 .Lr8_%=\n"
    ".Lr8_x%=:\n"
    "s_lshr_b32 s51, s48, 2\n"
    "s_xor_b32 s51, s51, s69\n"
    "s_and_b32 s51, s51, 1\n"
    "s_lshl_b32 s51, s51, 13\n"
    "s_lshl_b32 s73, s48, 19\n"
    "s_add_u32 s73, s73, s75\n"
    "s_add_u32 s73, s73, s51\n"
    "buffer_store_dwordx4 v[104:107], v10, s[40:43], s73 offen sc1\n"
    "buffer_store_dwordx4 v[112:115], v10, s[40:43], s73 offen offset:1024 sc1\n"
    "ds_read_b128 v[72:75], v2 offset:10240\n"
    "ds_read_b128 v[76:79], v2 offset:11264\n"
    "v_mov_b64 v[124:125], 0\n"
    "v_mov_b64 v[126:127], 0\n"
    "v_mov_b64 v[128:129], 0\n"
    "v_mov_b64 v[130:131], 0\n"
    "v_xor_b32 v132, 0x800, v10\n"
    "v_xor_b32 v133, 0x1000, v10\n"
    "v_xor_b32 v134, 0x1800, v10\n"
    "v_lshrrev_b32 v135, 2, v5\n"
    "s_movk_i32 s73, 0x180\n"
    "v_lshlrev_b32 v120, 1, v0\n"
    "v_bfi_b32 v120, s73, v120, v0\n"
    "v_lshrrev_b32 v122, 2, v0\n"
    "v_bfi_b32 v120, 64, v122, v120\n"
    "v_lshlrev_b32 v121, 1, v135\n"
    "v_bfi_b32 v121, s73, v121, v135\n"
    "v_lshrrev_b32 v122, 2, v135\n"
    "v_bfi_b32 v121, 64, v122, v121\n"
    "s_lshr_b32 s67, %2, 1\n"
    "s_mul_i32 s73, s48, 3\n"
    "s_add_u32 s73, s73, s67\n"
    "s_and_b32 s51, s73, 1\n"
    "s_lshl_b32 s51, s51, 12\n"
    "s_lshr_b32 s73, s73, 1\n"
    "s_lshl_b32 s73, s73, 14\n"
    "s_add_u32 s73, s73, s51\n"
    "s_add_u32 s67, s73, s68\n"
    "s_and_b32 s73, %2, 1\n"
    "s_lshl_b32 s73, s73, 11\n"
    "s_add_u32 s67, s67, s73\n"
    "s_mov_b32 s72, s58\n"
    "s_mul_i32 s73, s48, 3\n"
    "s_add_u32 s73, s73, 5\n"
    "s_and_b32 s51, s73, 1\n"
    "s_lshl_b32 s51, s51, 12\n"
    "s_lshr_b32 s73, s73, 1\n"
    "s_lshl_b32 s73, s73, 14\n"
    "s_add_u32 s73, s73, s51\n"
    "s_add_u32 s58, s73, s68\n"
    "s_cmp_eq_u32 s59, 0\n"
    "s_cbranch_scc1 .Lr9_%=\n"
    ".Lr9_x%=:\n"
    "s_add_u32 s73, s48, 2\n"
    "s_and_b32 s73, s73, 63\n"
    "s_lshr_b32 s51, s73, 2\n"
    "s_xor_b32 s51, s51, s69\n"
    "s_and_b32 s51, s51, 1\n"
    "s_lshl_b32 s51, s51, 13\n"
    "s_lshl_b32 s73, s73, 5\n"
    "s_add_u32 s73, s73, %4\n"
    "s_lshl_b32 s73, s73, 14\n"
    "s_add_u32 s73, s73, s51\n"
    "s_add_u32 s60, s73, 0x6000000\n"
    "s_add_u32 s61, s73, 0x6001000\n"
    "s_add_u32 s62, s73, 0x8000000\n"
    "s_add_u32 s63, s73, 0x8001000\n"
    "s_lshl_b32 s51, %5, 12\n"
    "s_add_u32 s73, s73, s51\n"
    "s_add_u32 s64, s73, 0x4000000\n"
    "s_add_u32 s73, s48, 2\n"
    "s_and_b32 s51, s73, 32\n"
    "s_and_b32 s73, s73, 31\n"
    "s_lshl3_add_u32 s73, s51, s73\n"
    "s_lshl2_add_u32 s73, s73, s92\n"
    "v_mov_b32 v101, s73\n"
    "s_branch .Lg_%=\n"
    ".Lr4_%=:\n"
    "s_sleep 2\n"
    "buffer_load_dword v79, v0, s[40:43], s58 offen offset:3072 sc1\n"
    "s_waitcnt vmcnt(0)\n"
    "s_add_u32 s56, s56, 1\n"
    "s_cmp_lt_u32 s56, s57\n"
    "s_cbranch_scc1 .Lr4_a%=\n"
    "s_mov_b32 s57, 0\n"
    "v_mov_b32 v135, 1\n"
    "global_store_dword v11, v135, %1 sc1\n"
    "s_branch .Lr4_x%=\n"
    ".Lr6_%=:\n"
    "s_cmp_lg_u32 s70, 0\n"
    "s_cbranch_scc1 .Lr6_y%=\n"
    "s_waitcnt lgkmcnt(0)\n"
    "s_add_u32 s73, s48, 1\n"
    "s_and_b32 s51, s73, 32\n"
    "s_and_b32 s73, s73, 31\n"
    "s_lshl3_add_u32 s73, s51, s73\n"
    "s_lshl2_add_u32 s73, s73, s92\n"
    "s_mov_b32 s53, s73\n"
    "s_mov_b32 s56, 0\n"
    "v_mov_b32 v134, s53\n"
    ".Lpoll10_%=:\n"
    "global_load_dword v132, v134, %1 sc1\n"
    "global_load_dword v133, v134, %1 offset:128 sc1\n"
    "s_waitcnt vmcnt(0)\n"
    "v_and_b32 v133, v132, v133\n"
    "s_nop 0\n"
    "v_readfirstlane_b32 s73, v133\n"
    "s_cmp_ge_u32 s73, 1\n"
    "s_cbranch_scc1 .Lpoll10_x%=\n"
    "s_add_u32 s56, s56, 1\n"
    "s_cmp_lt_u32 s56, 3\n"
    "s_cbranch_scc1 .Lpoll10_%=\n"
    "s_sleep 8\n"
    "s_cmp_lt_u32 s56, s57\n"
    "s_cbranch_scc1 .Lpoll10_%=\n"
    "s_mov_b32 s57, 0\n"
    "v_mov_b32 v135, 1\n"
    "global_store_dword v11, v135, %1 sc1\n"
    ".Lpoll10_x%=:\n"
    "v_xor_b32 v132, 0x800, v10\n"
    "v_xor_b32 v133, 0x1000, v10\n"
    "v_xor_b32 v134, 0x1800, v10\n"
    "v_lshrrev_b32 v135, 2, v5\n"
    "s_movk_i32 s73, 0x180\n"
    "v_lshlrev_b32 v120, 1, v0\n"
    "v_bfi_b32 v120, s73, v120, v0\n"
    "v_lshrrev_b32 v122, 2, v0\n"
    "v_bfi_b32 v120, 64, v122, v120\n"
    "v_lshlrev_b32 v121, 1, v135\n"
    "v_bfi_b32 v121, s73, v121, v135\n"
    "v_lshrrev_b32 v122, 2, v135\n"
    "v_bfi_b32 v121, 64, v122, v121\n"
    ".Lr6_y%=:\n"
    "buffer_load_dwordx4 v[12:15], v5, s[40:43], s60 offen sc1\n"
    "buffer_load_dwordx4 v[16:19], v5, s[40:43], s61 offen sc1\n"
    "buffer_load_dwordx4 v[20:23], v5, s[40:43], s62 offen sc1\n"
    "buffer_load_dwordx4 v[24:27], v5, s[40:43], s63 offen sc1\n"
    "buffer_load_dwordx4 v[28:31], v5, s[40:43], s64 offen sc1\n"
    "global_load_dword v116, v11, %1 sc1\n"
    "global_load_dword v117, v11, %1 sc1\n"
    "s_branch .Lr6_x%=\n"
    ".Lr8_%=:\n"
    "v_and_b32 v77, v76, v77\n"
    "s_nop 0\n"
    "v_readfirstlane_b32 s59, v77\n"
    "s_cmp_eq_u32 s59, 0\n"
    "s_cbranch_scc1 .Lr8_x%=\n"
    "buffer_load_dwordx4 v[12:15], v5, s[40:43], s60 offen sc1\n"
    "buffer_load_dwordx4 v[16:19], v5, s[40:43], s61 offen sc1\n"
    "buffer_load_dwordx4 v[20:23], v5, s[40:43], s62 offen sc1\n"
    "buffer_load_dwordx4 v[24:27], v5, s[40:43], s63 offen sc1\n"
    "buffer_load_dwordx4 v[28:31], v5, s[40:43], s64 offen sc1\n"
    "s_branch .Lr8_x%=\n"
    ".Lr9_%=:\n"
    "s_waitcnt lgkmcnt(0)\n"
    "s_add_u32 s73, s48, 1\n"
    "s_and_b32 s51, s73, 32\n"
    "s_and_b32 s73, s73, 31\n"
    "s_lshl3_add_u32 s73, s51, s73\n"
    "s_lshl2_add_u32 s73, s73, s92\n"
    "s_mov_b32 s53, s73\n"
    "s_mov_b32 s56, 0\n"
    "v_mov_b32 v134, s53\n"
    ".Lpoll11_%=:\n"
    "global_load_dword v132, v134, %1 sc1\n"
    "global_load_dword v133, v134, %1 offset:128 sc1\n"
    "s_waitcnt vmcnt(0)\n"
    "v_and_b32 v133, v132, v133\n"
    "s_nop 0\n"
    "v_readfirstlane_b32 s73, v133\n"
    "s_cmp_ge_u32 s73, 1\n"
    "s_cbranch_scc1 .Lpoll11_x%=\n"
    "s_add_u32 s56, s56, 1\n"
    "s_cmp_lt_u32 s56, 3\n"
    "s_cbranch_scc1 .Lpoll11_%=\n"
    "s_sleep 8\n"
    "s_cmp_lt_u32 s56, s57\n"
    "s_cbranch_scc1 .Lpoll11_%=\n"
    "s_mov_b32 s57, 0\n"
    "v_mov_b32 v135, 1\n"
    "global_store_dword v11, v135, %1 sc1\n"
    ".Lpoll11_x%=:\n"
    "v_xor_b32 v132, 0x800, v10\n"
    "v_xor_b32 v133, 0x1000, v10\n"
    "v_xor_b32 v134, 0x1800, v10\n"
    "v_lshrrev_b32 v135, 2, v5\n"
    "s_movk_i32 s73, 0x180\n"
    "v_lshlrev_b32 v120, 1, v0\n"
    "v_bfi_b32 v120, s73, v120, v0\n"
    "v_lshrrev_b32 v122, 2, v0\n"
    "v_bfi_b32 v120, 64, v122, v120\n"
    "v_lshlrev_b32 v121, 1, v135\n"
    "v_bfi_b32 v121, s73, v121, v135\n"
    "v_lshrrev_b32 v122, 2, v135\n"
    "v_bfi_b32 v121, 64, v122, v121\n"
    ".Lr9_y%=:\n"
    "buffer_load_dwordx4 v[12:15], v5, s[40:43], s60 offen sc1\n"
    "buffer_load_dwordx4 v[16:19], v5, s[40:43], s61 offen sc1\n"
    "buffer_load_dwordx4 v[20:23], v5, s[40:43], s62 offen sc1\n"
    "buffer_load_dwordx4 v[24:27], v5, s[40:43], s63 offen sc1\n"
    "buffer_load_dwordx4 v[28:31], v5, s[40:43], s64 offen sc1\n"
    "global_load_dword v116, v11, %1 sc1\n"
    "global_load_dword v117, v11, %1 sc1\n"
    "s_branch .Lr9_x%=\n"
    ".Lend_%=:\n"
    "ds_write_b128 v5, v[124:127] offset:22784\n"
    "s_waitcnt vmcnt(0)\n"
    "s_cmp_ge_u32 %2, 2\n"
    "s_cbranch_scc1 .Lla_%=\n"
    "s_waitcnt lgkmcnt(0)\n"
    "s_barrier\n"
    "s_branch .Lout_%=\n"
    ".Lla_%=:\n"
    "ds_read_b128 v[32:35], v0 offset:16384\n"
    "ds_read_b128 v[48:51], v0\n"
    "ds_read_b128 v[88:91], v0 offset:4096\n"
    "ds_read_b128 v[104:107], v4 offset:20480\n"
    "ds_read_b128 v[36:39], v0 offset:17408\n"
    "ds_read_b128 v[52:55], v0 offset:1024\n"
    "ds_read_b128 v[92:95], v0 offset:5120\n"
    "ds_read_b128 v[108:111], v4 offset:20544\n"
    "ds_read_b128 v[40:43], v0 offset:18432\n"
    "ds_read_b128 v[56:59], v0 offset:2048\n"
    "ds_read_b128 v[96:99], v0 offset:6144\n"
    "ds_read_b128 v[112:115], v4 offset:20608\n"
    "s_waitcnt lgkmcnt(10)\n"
    "v_mfma_f32_16x16x32_bf16 v[120:123], v[48:51], v[32:35], 0\n"
    "s_barrier\n"
    "ds_read_b128 v[44:47], v0 offset:19456\n"
    "ds_read_b128 v[60:63], v0 offset:3072\n"
    "ds_read_b128 v[100:103], v0 offset:7168\n"
    "ds_read_b128 v[116:119], v4 offset:20672\n"
    "s_waitcnt lgkmcnt(13)\n"
    "v_mfma_f32_16x16x32_bf16 v[124:127], v[88:91], v[32:35], 0\n"
    "s_waitcnt lgkmcnt(12)\n"
    "v_mfma_f32_16x16x32_bf16 v[128:131], v[104:107], v[32:35], 0\n"
    "s_waitcnt lgkmcnt(10)\n"
    "v_mfma_f32_16x16x32_bf16 v[120:123], v[52:55], v[36:39], v[120:123]\n"
    "s_waitcnt lgkmcnt(9)\n"
    "v_mfma_f32_16x16x32_bf16 v[124:127], v[92:95], v[36:39], v[124:127]\n"
    "ds_read_b128 v[64:67], v10 offset:4096\n"
    "s_waitcnt lgkmcnt(9)\n"
    "v_mfma_f32_16x16x32_bf16 v[128:131], v[108:111], v[36:39], v[128:131]\n"
    "ds_read_b128 v[68:71], v10 offset:8192\n"
    "s_waitcnt lgkmcnt(8)\n"
    "v_mfma_f32_16x16x32_bf16 v[120:123], v[56:59], v[40:43], v[120:123]\n"
    "ds_read_b128 v[72:75], v10 offset:5120\n"
    "s_waitcnt lgkmcnt(8)\n"
    "v_mfma_f32_16x16x32_bf16 v[124:127], v[96:99], v[40:43], v[124:127]\n"
    "ds_read_b128 v[80:83], v10 offset:9216\n"
    "s_waitcnt lgkmcnt(8)\n"
    "v_mfma_f32_16x16x32_bf16 v[128:131], v[112:115], v[40:43], v[128:131]\n"
    "ds_read_b128 v[84:87], v11 offset:31888\n"
    "s_waitcnt lgkmcnt(7)\n"
    "v_mfma_f32_16x16x32_bf16 v[120:123], v[60:63], v[44:47], v[120:123]\n"
    "ds_read_b64 v[134:135], v11 offset:31904\n"
    "s_waitcnt lgkmcnt(7)\n"
    "v_mfma_f32_16x16x32_bf16 v[124:127], v[100:103], v[44:47], v[124:127]\n"
    "ds_read_b128 v[88:91], v10 offset:18688\n"
    "s_waitcnt lgkmcnt(7)\n"
    "v_mfma_f32_16x16x32_bf16 v[128:131], v[116:119], v[44:47], v[128:131]\n"
    "ds_read_b128 v[92:95], v10 offset:19712\n"
    "v_mov_b32 v132, 0xd01502f9\n"
    "v_cndmask_b32 v120, v132, v120, s[76:77]\n"
    "v_cndmask_b32 v121, v132, v121, s[78:79]\n"
    "v_cndmask_b32 v122, v132, v122, s[80:81]\n"
    "v_cndmask_b32 v123, v132, v123, s[82:83]\n"
    "v_cndmask_b32 v124, v132, v124, s[84:85]\n"
    "v_cndmask_b32 v125, v132, v125, s[86:87]\n"
    "v_cndmask_b32 v126, v132, v126, s[88:89]\n"
    "v_cndmask_b32 v127, v132, v127, s[90:91]\n"
    "v_maximum3_f32 v133, v128, v129, v120\n"
    "v_maximum3_f32 v133, v133, v121, v122\n"
    "v_maximum3_f32 v133, v133, v123, v124\n"
    "v_maximum3_f32 v133, v133, v125, v126\n"
    "v_maximum3_f32 v133, v133, v127, v127\n"
    "v_mov_b32 v132, v133\n"
    "s_nop 1\n"
    "v_permlane16_swap_b32 v132, v133\n"
    "v_maximum3_f32 v133, v133, v132, v132\n"
    "v_mov_b32 v132, v133\n"
    "s_nop 1\n"
    "v_permlane32_swap_b32 v132, v133\n"
    "v_maximum3_f32 v133, v133, v132, v132\n"
    "v_mul_f32 v133, 0xbe0293ee, v133\n"
    "v_fmamk_f32 v120, v120, 0x3e0293ee, v133\n"
    "v_fmamk_f32 v121, v121, 0x3e0293ee, v133\n"
    "v_fmamk_f32 v122, v122, 0x3e0293ee, v133\n"
    "v_fmamk_f32 v123, v123, 0x3e0293ee, v133\n"
    "v_fmamk_f32 v124, v124, 0x3e0293ee, v133\n"
    "v_fmamk_f32 v125, v125, 0x3e0293ee, v133\n"
    "v_fmamk_f32 v126, v126, 0x3e0293ee, v133\n"
    "v_fmamk_f32 v127, v127, 0x3e0293ee, v133\n"
    "v_fmamk_f32 v128, v128, 0x3e0293ee, v133\n"
    "v_fmamk_f32 v129, v129, 0x3e0293ee, v133\n"
    "v_exp_f32 v120, v120\n"
    "v_exp_f32 v121, v121\n"
    "v_exp_f32 v122, v122\n"
    "v_exp_f32 v123, v123\n"
    "v_exp_f32 v124, v124\n"
    "v_exp_f32 v125, v125\n"
    "v_exp_f32 v126, v126\n"
    "v_exp_f32 v127, v127\n"
    "v_exp_f32 v128, v128\n"
    "v_exp_f32 v129, v129\n"
    "v_cvt_pk_bf16_f32 v120, v120, v121\n"
    "v_cvt_pk_bf16_f32 v121, v122, v123\n"
    "v_cvt_pk_bf16_f32 v122, v124, v125\n"
    "v_cvt_pk_bf16_f32 v123, v126, v127\n"
    "v_cvt_pk_bf16_f32 v124, v128, v129\n"
    "v_cvt_pk_bf16_f32 v125, v128, v129\n"
    "s_waitcnt lgkmcnt(3)\n"
    "v_mfma_f32_16x16x32_bf16 v[100:103], v[84:87], v[120:123], 0\n"
    "v_mfma_f32_16x16x32_bf16 v[104:107], v[64:67], v[120:123], 0\n"
    "v_mfma_f32_16x16x32_bf16 v[108:111], v[68:71], v[120:123], 0\n"
    "s_waitcnt lgkmcnt(2)\n"
    "v_mfma_f32_16x16x16_bf16 v[100:103], v[134:135], v[124:125], v[100:103]\n"
    "v_mfma_f32_16x16x32_bf16 v[112:115], v[72:75], v[120:123], 0\n"
    "v_mfma_f32_16x16x32_bf16 v[116:119], v[80:83], v[120:123], 0\n"
    "s_waitcnt lgkmcnt(1)\n"
    "v_mfma_f32_16x16x16_bf16 v[104:107], v[88:89], v[124:125], v[104:107]\n"
    "v_mfma_f32_16x16x16_bf16 v[108:111], v[90:91], v[124:125], v[108:111]\n"
    "v_rcp_f32 v132, v100\n"
    "s_waitcnt lgkmcnt(0)\n"
    "v_mfma_f32_16x16x16_bf16 v[112:115], v[92:93], v[124:125], v[112:115]\n"
    "v_mfma_f32_16x16x16_bf16 v[116:119], v[94:95], v[124:125], v[116:119]\n"
    "s_nop 1\n"
    "v_pk_mul_f32 v[104:105], v[104:105], v[132:133] op_sel_hi:[1,0]\n"
    "v_pk_mul_f32 v[106:107], v[106:107], v[132:133] op_sel_hi:[1,0]\n"
    "v_pk_mul_f32 v[108:109], v[108:109], v[132:133] op_sel_hi:[1,0]\n"
    "v_pk_mul_f32 v[110:111], v[110:111], v[132:133] op_sel_hi:[1,0]\n"
    "v_cvt_pk_bf16_f32 v104, v104, v108\n"
    "v_cvt_pk_bf16_f32 v105, v105, v109\n"
    "v_cvt_pk_bf16_f32 v106, v106, v110\n"
    "v_cvt_pk_bf16_f32 v107, v107, v111\n"
    "v_pk_mul_f32 v[112:113], v[112:113], v[132:133] op_sel_hi:[1,0]\n"
    "v_pk_mul_f32 v[114:115], v[114:115], v[132:133] op_sel_hi:[1,0]\n"
    "v_pk_mul_f32 v[116:117], v[116:117], v[132:133] op_sel_hi:[1,0]\n"
    "v_pk_mul_f32 v[118:119], v[118:119], v[132:133] op_sel_hi:[1,0]\n"
    "v_cvt_pk_bf16_f32 v112, v112, v116\n"
    "v_cvt_pk_bf16_f32 v113, v113, v117\n"
    "v_cvt_pk_bf16_f32 v114, v114, v118\n"
    "v_cvt_pk_bf16_f32 v115, v115, v119\n"
    "s_lshr_b32 s51, s48, 2\n"
    "s_xor_b32 s51, s51, s69\n"
    "s_and_b32 s51, s51, 1\n"
    "s_lshl_b32 s51, s51, 13\n"
    "s_lshl_b32 s73, s48, 19\n"
    "s_add_u32 s73, s73, s75\n"
    "s_add_u32 s73, s73, s51\n"
    "buffer_store_dwordx4 v[104:107], v10, s[40:43], s73 offen sc1\n"
    "buffer_store_dwordx4 v[112:115], v10, s[40:43], s73 offen offset:1024 sc1\n"
    "s_add_u32 s73, s48, 0\n"
    "s_and_b32 s51, s73, 32\n"
    "s_and_b32 s73, s73, 31\n"
    "s_lshl3_add_u32 s73, s51, s73\n"
    "s_lshl2_add_u32 s73, s73, s93\n"
    "v_mov_b32 v100, s73\n"
    "v_mov_b32 v101, 1\n"
    "s_waitcnt vmcnt(0)\n"
    "s_mov_b64 exec, 1\n"
    "ds_add_rtn_u32 v101, v11, v101 offset:31872\n"
    "s_mov_b64 exec, -1\n"
    "s_waitcnt lgkmcnt(0)\n"
    "v_readfirstlane_b32 s73, v101\n"
    "v_mov_b32 v101, 1\n"
    "s_bitcmp1_b32 s73, 0\n"
    "s_cbranch_scc0 .Lout_%=\n"
    "s_mov_b64 exec, 1\n"
    "global_store_dword v100, v101, %1 sc1\n"
    "s_mov_b64 exec, -1\n"
    "s_cmp_eq_u32 %3, 128\n"
    "s_cbranch_scc0 .Lout_%=\n"
    "v_mov_b32 v130, 0x20400\n"
    "global_load_dwordx2 v[132:133], v130, %1 sc1\n"
    "v_mbcnt_lo_u32_b32 v131, -1, 0\n"
    "v_min_u32 v131, 7, v131\n"
    "v_lshlrev_b32 v131, 7, v131\n"
    "v_add_u32 v131, 0x20000, v131\n"
    "global_load_dwordx2 v[134:135], v131, %1 sc1\n"
    "s_waitcnt vmcnt(0)\n"
    "v_add_u32 v132, 1, v132\n"
    "v_add_u32 v133, 1, v133\n"
    "global_store_dwordx2 v130, v[132:133], %1 sc1\n"
    ".Lout_%=:\n"
    "s_waitcnt vmcnt(0)\n"
    "s_endpgm\n"
    ".p2align 6\n"
    ".Lrole1_%=:\n"
    "s_mov_b64 s[40:41], %0\n"
    "s_and_b32 s41, s41, 0xffff\n"
    "s_mov_b32 s42, 0xc000000\n"
    "s_mov_b32 s43, 0x20000\n"
    "s_mov_b64 s[92:93], %0\n"
    "s_and_b32 s93, s93, 0xffff\n"
    "s_mov_b32 s94, 0xc000000\n"
    "s_mov_b32 s95, 0x20000\n"
    "s_mov_b32 s53, 131072\n"
    "s_mov_b64 s[56:57], 0\n"
    "v_mbcnt_lo_u32_b32 v7, -1, 0\n"
    "v_mbcnt_hi_u32_b32 v7, -1, v7\n"
    "s_lshl_b32 s50, %2, 8\n"
    "s_add_u32 s50, s50, 147456\n"
    "v_lshl_add_u32 v7, v7, 2, s50\n"
    "ds_read_b32 v1, v7\n"
    "ds_read_b32 v2, v7 offset:1024\n"
    "ds_read_b32 v3, v7 offset:2048\n"
    "ds_read_b32 v5, v7 offset:3072\n"
    "ds_read_b32 v48, v7 offset:4096\n"
    "ds_read_b32 v49, v7 offset:5120\n"
    "ds_read_b32 v50, v7 offset:6144\n"
    "s_waitcnt lgkmcnt(0)\n"
    "s_barrier\n"
    "s_movk_i32 s86, 255\n"
    "s_lshl_b32 s54, %2, 10\n"
    "s_lshl_b32 s58, %2, 13\n"
    "s_add_u32 s58, s58, 65536\n"
    "s_add_u32 s88, s40, 32768\n"
    "s_mov_b32 s89, s41\n"
    "s_cmp_lt_u32 %2, 2\n"
    "s_cselect_b32 s96, s42, 0\n"
    "s_mov_b32 s91, s43\n"
    "s_mul_i32 s59, %2, 576\n"
    "s_add_u32 s59, s59, 68608\n"
    "s_bfm_b64 s[60:61], 36, 0\n"
    "v_mov_b32 v72, 0x3f803f80\n"
    "v_mov_b32 v73, 0x3f803f80\n"
    "v_mov_b32 v74, 0x3f803f80\n"
    "v_mov_b32 v75, 0x3f803f80\n"
    "s_mov_b64 s[44:45], %8\n"
    "s_and_b32 s45, s45, 0xffff\n"
    "s_mov_b32 s46, 0x480000\n"
    "s_mov_b32 s47, 0x20000\n"
    "s_add_u32 m0, s54, 0\n"
    "s_add_u32 s55, %6, 0\n"
    "buffer_load_dwordx4 v48, s[44:47], s55 offen lds\n"
    "s_add_u32 m0, s54, 4096\n"
    "s_add_u32 s55, %6, 1048576\n"
    "buffer_load_dwordx4 v48, s[44:47], s55 offen lds\n"
    "s_add_u32 m0, s54, 8192\n"
    "s_add_u32 s55, %6, 2097152\n"
    "buffer_load_dwordx4 v48, s[44:47], s55 offen lds\n"
    "s_add_u32 m0, s54, 12288\n"
    "s_add_u32 s55, %6, 3145728\n"
    "buffer_load_dwordx4 v48, s[44:47], s55 offen lds\n"
    "s_add_u32 m0, s54, 16384\n"
    "s_add_u32 s55, %6, 32768\n"
    "buffer_load_dwordx4 v48, s[44:47], s55 offen lds\n"
    "s_add_u32 m0, s54, 20480\n"
    "s_add_u32 s55, %6, 1081344\n"
    "buffer_load_dwordx4 v48, s[44:47], s55 offen lds\n"
    "s_add_u32 m0, s54, 24576\n"
    "s_add_u32 s55, %6, 2129920\n"
    "buffer_load_dwordx4 v48, s[44:47], s55 offen lds\n"
    "s_add_u32 m0, s54, 28672\n"
    "s_add_u32 s55, %6, 3178496\n"
    "buffer_load_dwordx4 v48, s[44:47], s55 offen lds\n"
    "s_add_u32 m0, s54, 32768\n"
    "s_add_u32 s55, %6, 262144\n"
    "buffer_load_dwordx4 v48, s[44:47], s55 offen lds\n"
    "s_add_u32 m0, s54, 36864\n"
    "s_add_u32 s55, %6, 1310720\n"
    "buffer_load_dwordx4 v48, s[44:47], s55 offen lds\n"
    "s_add_u32 m0, s54, 40960\n"
    "s_add_u32 s55, %6, 2359296\n"
    "buffer_load_dwordx4 v48, s[44:47], s55 offen lds\n"
    "s_add_u32 m0, s54, 45056\n"
    "s_add_u32 s55, %6, 3407872\n"
    "buffer_load_dwordx4 v48, s[44:47], s55 offen lds\n"
    "s_add_u32 m0, s54, 49152\n"
    "s_add_u32 s55, %6, 294912\n"
    "buffer_load_dwordx4 v48, s[44:47], s55 offen lds\n"
    "s_add_u32 m0, s54, 53248\n"
    "s_add_u32 s55, %6, 1343488\n"
    "buffer_load_dwordx4 v48, s[44:47], s55 offen lds\n"
    "s_add_u32 m0, s54, 57344\n"
    "s_add_u32 s55, %6, 2392064\n"
    "buffer_load_dwordx4 v48, s[44:47], s55 offen lds\n"
    "s_add_u32 m0, s54, 61440\n"
    "s_add_u32 s55, %6, 3440640\n"
    "buffer_load_dwordx4 v48, s[44:47], s55 offen lds\n"
    "s_add_u32 m0, s54, 65536\n"
    "s_add_u32 s55, %6, 524288\n"
    "buffer_load_dwordx4 v48, s[44:47], s55 offen lds\n"
    "s_add_u32 m0, s54, 69632\n"
    "s_add_u32 s55, %6, 1572864\n"
    "buffer_load_dwordx4 v48, s[44:47], s55 offen lds\n"
    "s_add_u32 m0, s54, 73728\n"
    "s_add_u32 s55, %6, 2621440\n"
    "buffer_load_dwordx4 v48, s[44:47], s55 offen lds\n"
    "s_add_u32 m0, s54, 77824\n"
    "s_add_u32 s55, %6, 3670016\n"
    "buffer_load_dwordx4 v48, s[44:47], s55 offen lds\n"
    "s_add_u32 m0, s54, 81920\n"
    "s_add_u32 s55, %6, 557056\n"
    "buffer_load_dwordx4 v48, s[44:47], s55 offen lds\n"
    "s_add_u32 m0, s54, 86016\n"
    "s_add_u32 s55, %6, 1605632\n"
    "buffer_load_dwordx4 v48, s[44:47], s55 offen lds\n"
    "s_add_u32 m0, s54, 90112\n"
    "s_add_u32 s55, %6, 2654208\n"
    "buffer_load_dwordx4 v48, s[44:47], s55 offen lds\n"
    "s_add_u32 m0, s54, 94208\n"
    "s_add_u32 s55, %6, 3702784\n"
    "buffer_load_dwordx4 v48, s[44:47], s55 offen lds\n"
    "s_add_u32 m0, s54, 98304\n"
    "s_add_u32 s55, %6, 786432\n"
    "buffer_load_dwordx4 v48, s[44:47], s55 offen lds\n"
    "s_add_u32 m0, s54, 102400\n"
    "s_add_u32 s55, %6, 1835008\n"
    "buffer_load_dwordx4 v48, s[44:47], s55 offen lds\n"
    "s_add_u32 m0, s54, 106496\n"
    "s_add_u32 s55, %6, 2883584\n"
    "buffer_load_dwordx4 v48, s[44:47], s55 offen lds\n"
    "s_add_u32 m0, s54, 110592\n"
    "s_add_u32 s55, %6, 3932160\n"
    "buffer_load_dwordx4 v48, s[44:47], s55 offen lds\n"
    "s_add_u32 m0, s54, 114688\n"
    "s_add_u32 s55, %6, 819200\n"
    "buffer_load_dwordx4 v48, s[44:47], s55 offen lds\n"
    "s_add_u32 m0, s54, 118784\n"
    "s_add_u32 s55, %6, 1867776\n"
    "buffer_load_dwordx4 v48, s[44:47], s55 offen lds\n"
    "s_add_u32 m0, s54, 122880\n"
    "s_add_u32 s55, %6, 2916352\n"
    "buffer_load_dwordx4 v48, s[44:47], s55 offen lds\n"
    "s_add_u32 m0, s54, 126976\n"
    "s_add_u32 s55, %6, 3964928\n"
    "buffer_load_dwordx4 v48, s[44:47], s55 offen lds\n"
    "s_add_u32 m0, s54, 131072\n"
    "s_add_u32 s55, %6, 4194304\n"
    "buffer_load_dwordx4 v48, s[44:47], s55 offen lds\n"
    "s_add_u32 m0, s54, 135168\n"
    "s_add_u32 s55, %6, 4227072\n"
    "buffer_load_dwordx4 v48, s[44:47], s55 offen lds\n"
    "s_add_u32 m0, s54, 139264\n"
    "s_add_u32 s55, %6, 4456448\n"
    "buffer_load_dwordx4 v48, s[44:47], s55 offen lds\n"
    "s_add_u32 m0, s54, 143360\n"
    "s_add_u32 s55, %6, 4489216\n"
    "buffer_load_dwordx4 v48, s[44:47], s55 offen lds\n"
    "s_xor_b32 s50, %2, 0\n"
    "s_lshl_b32 s50, s50, 12\n"
    "v_add_u32 v32, s50, v49\n"
    "v_add_u32 v33, s50, v50\n"
    "s_xor_b32 s50, %2, 1\n"
    "s_lshl_b32 s50, s50, 12\n"
    "v_add_u32 v34, s50, v49\n"
    "v_add_u32 v35, s50, v50\n"
    "s_xor_b32 s50, %2, 2\n"
    "s_lshl_b32 s50, s50, 12\n"
    "v_add_u32 v36, s50, v49\n"
    "v_add_u32 v37, s50, v50\n"
    "s_xor_b32 s50, %2, 3\n"
    "s_lshl_b32 s50, s50, 12\n"
    "v_add_u32 v38, s50, v49\n"
    "v_add_u32 v39, s50, v50\n"
    "s_waitcnt vmcnt(32) lgkmcnt(0)\n"
    "s_barrier\n"
    "ds_read_b64_tr_b16 a[0:1], v32\n"
    "ds_read_b64_tr_b16 a[4:5], v33\n"
    "ds_read_b64_tr_b16 a[32:33], v34\n"
    "ds_read_b64_tr_b16 a[36:37], v35\n"
    "ds_read_b64_tr_b16 a[64:65], v36\n"
    "ds_read_b64_tr_b16 a[68:69], v37\n"
    "ds_read_b64_tr_b16 a[96:97], v38\n"
    "ds_read_b64_tr_b16 a[100:101], v39\n"
    "s_waitcnt vmcnt(28) lgkmcnt(0)\n"
    "s_barrier\n"
    "s_mov_b64 s[44:45], %9\n"
    "s_and_b32 s45, s45, 0xffff\n"
    "s_mov_b32 s46, 0x480000\n"
    "s_mov_b32 s47, 0x20000\n"
    "s_add_u32 m0, s54, 0\n"
    "s_add_u32 s55, %6, 0\n"
    "buffer_load_dwordx4 v48, s[44:47], s55 offen lds\n"
    "s_add_u32 m0, s54, 4096\n"
    "s_add_u32 s55, %6, 1048576\n"
    "buffer_load_dwordx4 v48, s[44:47], s55 offen lds\n"
    "s_add_u32 m0, s54, 8192\n"
    "s_add_u32 s55, %6, 2097152\n"
    "buffer_load_dwordx4 v48, s[44:47], s55 offen lds\n"
    "s_add_u32 m0, s54, 12288\n"
    "s_add_u32 s55, %6, 3145728\n"
    "buffer_load_dwordx4 v48, s[44:47], s55 offen lds\n"
    "ds_read_b64_tr_b16 a[2:3], v32 offset:16384\n"
    "ds_read_b64_tr_b16 a[6:7], v33 offset:16384\n"
    "ds_read_b64_tr_b16 a[34:35], v34 offset:16384\n"
    "ds_read_b64_tr_b16 a[38:39], v35 offset:16384\n"
    "ds_read_b64_tr_b16 a[66:67], v36 offset:16384\n"
    "ds_read_b64_tr_b16 a[70:71], v37 offset:16384\n"
    "ds_read_b64_tr_b16 a[98:99], v38 offset:16384\n"
    "ds_read_b64_tr_b16 a[102:103], v39 offset:16384\n"
    "s_waitcnt vmcnt(28) lgkmcnt(0)\n"
    "s_barrier\n"
    "s_add_u32 m0, s54, 16384\n"
    "s_add_u32 s55, %6, 32768\n"
    "buffer_load_dwordx4 v48, s[44:47], s55 offen lds\n"
    "s_add_u32 m0, s54, 20480\n"
    "s_add_u32 s55, %6, 1081344\n"
    "buffer_load_dwordx4 v48, s[44:47], s55 offen lds\n"
    "s_add_u32 m0, s54, 24576\n"
    "s_add_u32 s55, %6, 2129920\n"
    "buffer_load_dwordx4 v48, s[44:47], s55 offen lds\n"
    "s_add_u32 m0, s54, 28672\n"
    "s_add_u32 s55, %6, 3178496\n"
    "buffer_load_dwordx4 v48, s[44:47], s55 offen lds\n"
    "v_mfma_f32_16x16x32_bf16 v[8:11], a[0:3], v[72:75], 0\n"
    "v_mfma_f32_16x16x32_bf16 v[12:15], a[4:7], v[72:75], 0\n"
    "v_mfma_f32_16x16x32_bf16 v[8:11], a[32:35], v[72:75], v[8:11]\n"
    "v_mfma_f32_16x16x32_bf16 v[12:15], a[36:39], v[72:75], v[12:15]\n"
    "v_mfma_f32_16x16x32_bf16 v[8:11], a[64:67], v[72:75], v[8:11]\n"
    "v_mfma_f32_16x16x32_bf16 v[12:15], a[68:71], v[72:75], v[12:15]\n"
    "v_mfma_f32_16x16x32_bf16 v[8:11], a[96:99], v[72:75], v[8:11]\n"
    "v_mfma_f32_16x16x32_bf16 v[12:15], a[100:103], v[72:75], v[12:15]\n"
    "ds_read_b64_tr_b16 a[8:9], v32 offset:32768\n"
    "ds_read_b64_tr_b16 a[12:13], v33 offset:32768\n"
    "ds_read_b64_tr_b16 a[40:41], v34 offset:32768\n"
    "ds_read_b64_tr_b16 a[44:45], v35 offset:32768\n"
    "ds_read_b64_tr_b16 a[72:73], v36 offset:32768\n"
    "ds_read_b64_tr_b16 a[76:77], v37 offset:32768\n"
    "ds_read_b64_tr_b16 a[104:105], v38 offset:32768\n"
    "ds_read_b64_tr_b16 a[108:109], v39 offset:32768\n"
    "s_waitcnt vmcnt(28) lgkmcnt(0)\n"
    "s_barrier\n"
    "s_add_u32 m0, s54, 32768\n"
    "s_add_u32 s55, %6, 262144\n"
    "buffer_load_dwordx4 v48, s[44:47], s55 offen lds\n"
    "s_add_u32 m0, s54, 36864\n"
    "s_add_u32 s55, %6, 1310720\n"
    "buffer_load_dwordx4 v48, s[44:47], s55 offen lds\n"
    "s_add_u32 m0, s54, 40960\n"
    "s_add_u32 s55, %6, 2359296\n"
    "buffer_load_dwordx4 v48, s[44:47], s55 offen lds\n"
    "s_add_u32 m0, s54, 45056\n"
    "s_add_u32 s55, %6, 3407872\n"
    "buffer_load_dwordx4 v48, s[44:47], s55 offen lds\n"
    "ds_read_b64_tr_b16 a[10:11], v32 offset:49152\n"
    "ds_read_b64_tr_b16 a[14:15], v33 offset:49152\n"
    "ds_read_b64_tr_b16 a[42:43], v34 offset:49152\n"
    "ds_read_b64_tr_b16 a[46:47], v35 offset:49152\n"
    "ds_read_b64_tr_b16 a[74:75], v36 offset:49152\n"
    "ds_read_b64_tr_b16 a[78:79], v37 offset:49152\n"
    "ds_read_b64_tr_b16 a[106:107], v38 offset:49152\n"
    "ds_read_b64_tr_b16 a[110:111], v39 offset:49152\n"
    "s_waitcnt vmcnt(28) lgkmcnt(0)\n"
    "s_barrier\n"
    "s_add_u32 m0, s54, 49152\n"
    "s_add_u32 s55, %6, 294912\n"
    "buffer_load_dwordx4 v48, s[44:47], s55 offen lds\n"
    "s_add_u32 m0, s54, 53248\n"
    "s_add_u32 s55, %6, 1343488\n"
    "buffer_load_dwordx4 v48, s[44:47], s55 offen lds\n"
    "s_add_u32 m0, s54, 57344\n"
    "s_add_u32 s55, %6, 2392064\n"
    "buffer_load_dwordx4 v48, s[44:47], s55 offen lds\n"
    "s_add_u32 m0, s54, 61440\n"
    "s_add_u32 s55, %6, 3440640\n"
    "buffer_load_dwordx4 v48, s[44:47], s55 offen lds\n"
    "v_mfma_f32_16x16x32_bf16 v[8:11], a[8:11], v[72:75], v[8:11]\n"
    "v_mfma_f32_16x16x32_bf16 v[12:15], a[12:15], v[72:75], v[12:15]\n"
    "v_mfma_f32_16x16x32_bf16 v[8:11], a[40:43], v[72:75], v[8:11]\n"
    "v_mfma_f32_16x16x32_bf16 v[12:15], a[44:47], v[72:75], v[12:15]\n"
    "v_mfma_f32_16x16x32_bf16 v[8:11], a[72:75], v[72:75], v[8:11]\n"
    "v_mfma_f32_16x16x32_bf16 v[12:15], a[76:79], v[72:75], v[12:15]\n"
    "v_mfma_f32_16x16x32_bf16 v[8:11], a[104:107], v[72:75], v[8:11]\n"
    "v_mfma_f32_16x16x32_bf16 v[12:15], a[108:111], v[72:75], v[12:15]\n"
    "v_add_u32 v71, 0x10000, v32\n"
    "ds_read_b64_tr_b16 a[16:17], v71\n"
    "v_add_u32 v70, 0x10000, v33\n"
    "ds_read_b64_tr_b16 a[20:21], v70\n"
    "v_add_u32 v69, 0x10000, v34\n"
    "ds_read_b64_tr_b16 a[48:49], v69\n"
    "v_add_u32 v68, 0x10000, v35\n"
    "ds_read_b64_tr_b16 a[52:53], v68\n"
    "v_add_u32 v67, 0x10000, v36\n"
    "ds_read_b64_tr_b16 a[80:81], v67\n"
    "v_add_u32 v66, 0x10000, v37\n"
    "ds_read_b64_tr_b16 a[84:85], v66\n"
    "v_add_u32 v65, 0x10000, v38\n"
    "ds_read_b64_tr_b16 a[112:113], v65\n"
    "v_add_u32 v64, 0x10000, v39\n"
    "ds_read_b64_tr_b16 a[116:117], v64\n"
    "s_waitcnt vmcnt(28) lgkmcnt(0)\n"
    "s_barrier\n"
    "s_add_u32 m0, s54, 65536\n"
    "s_add_u32 s55, %6, 524288\n"
    "buffer_load_dwordx4 v48, s[44:47], s55 offen lds\n"
    "s_add_u32 m0, s54, 69632\n"
    "s_add_u32 s55, %6, 1572864\n"
    "buffer_load_dwordx4 v48, s[44:47], s55 offen lds\n"
    "s_add_u32 m0, s54, 73728\n"
    "s_add_u32 s55, %6, 2621440\n"
    "buffer_load_dwordx4 v48, s[44:47], s55 offen lds\n"
    "s_add_u32 m0, s54, 77824\n"
    "s_add_u32 s55, %6, 3670016\n"
    "buffer_load_dwordx4 v48, s[44:47], s55 offen lds\n"
    "ds_read_b64_tr_b16 a[18:19], v71 offset:16384\n"
    "ds_read_b64_tr_b16 a[22:23], v70 offset:16384\n"
    "ds_read_b64_tr_b16 a[50:51], v69 offset:16384\n"
    "ds_read_b64_tr_b16 a[54:55], v68 offset:16384\n"
    "ds_read_b64_tr_b16 a[82:83], v67 offset:16384\n"
    "ds_read_b64_tr_b16 a[86:87], v66 offset:16384\n"
    "ds_read_b64_tr_b16 a[114:115], v65 offset:16384\n"
    "ds_read_b64_tr_b16 a[118:119], v64 offset:16384\n"
    "s_waitcnt vmcnt(28) lgkmcnt(0)\n"
    "s_barrier\n"
    "s_add_u32 m0, s54, 81920\n"
    "s_add_u32 s55, %6, 557056\n"
    "buffer_load_dwordx4 v48, s[44:47], s55 offen lds\n"
    "s_add_u32 m0, s54, 86016\n"
    "s_add_u32 s55, %6, 1605632\n"
    "buffer_load_dwordx4 v48, s[44:47], s55 offen lds\n"
    "s_add_u32 m0, s54, 90112\n"
    "s_add_u32 s55, %6, 2654208\n"
    "buffer_load_dwordx4 v48, s[44:47], s55 offen lds\n"
    "s_add_u32 m0, s54, 94208\n"
    "s_add_u32 s55, %6, 3702784\n"
    "buffer_load_dwordx4 v48, s[44:47], s55 offen lds\n"
    "v_mfma_f32_16x16x32_bf16 v[8:11], a[16:19], v[72:75], v[8:11]\n"
    "v_mfma_f32_16x16x32_bf16 v[12:15], a[20:23], v[72:75], v[12:15]\n"
    "v_mfma_f32_16x16x32_bf16 v[8:11], a[48:51], v[72:75], v[8:11]\n"
    "v_mfma_f32_16x16x32_bf16 v[12:15], a[52:55], v[72:75], v[12:15]\n"
    "v_mfma_f32_16x16x32_bf16 v[8:11], a[80:83], v[72:75], v[8:11]\n"
    "v_mfma_f32_16x16x32_bf16 v[12:15], a[84:87], v[72:75], v[12:15]\n"
    "v_mfma_f32_16x16x32_bf16 v[8:11], a[112:115], v[72:75], v[8:11]\n"
    "v_mfma_f32_16x16x32_bf16 v[12:15], a[116:119], v[72:75], v[12:15]\n"
    "ds_read_b64_tr_b16 a[24:25], v71 offset:32768\n"
    "ds_read_b64_tr_b16 a[28:29], v70 offset:32768\n"
    "ds_read_b64_tr_b16 a[56:57], v69 offset:32768\n"
    "ds_read_b64_tr_b16 a[60:61], v68 offset:32768\n"
    "ds_read_b64_tr_b16 a[88:89], v67 offset:32768\n"
    "ds_read_b64_tr_b16 a[92:93], v66 offset:32768\n"
    "ds_read_b64_tr_b16 a[120:121], v65 offset:32768\n"
    "ds_read_b64_tr_b16 a[124:125], v64 offset:32768\n"
    "s_waitcnt vmcnt(28) lgkmcnt(0)\n"
    "s_barrier\n"
    "s_add_u32 m0, s54, 98304\n"
    "s_add_u32 s55, %6, 786432\n"
    "buffer_load_dwordx4 v48, s[44:47], s55 offen lds\n"
    "s_add_u32 m0, s54, 102400\n"
    "s_add_u32 s55, %6, 1835008\n"
    "buffer_load_dwordx4 v48, s[44:47], s55 offen lds\n"
    "s_add_u32 m0, s54, 106496\n"
    "s_add_u32 s55, %6, 2883584\n"
    "buffer_load_dwordx4 v48, s[44:47], s55 offen lds\n"
    "s_add_u32 m0, s54, 110592\n"
    "s_add_u32 s55, %6, 3932160\n"
    "buffer_load_dwordx4 v48, s[44:47], s55 offen lds\n"
    "ds_read_b64_tr_b16 a[26:27], v71 offset:49152\n"
    "ds_read_b64_tr_b16 a[30:31], v70 offset:49152\n"
    "ds_read_b64_tr_b16 a[58:59], v69 offset:49152\n"
    "ds_read_b64_tr_b16 a[62:63], v68 offset:49152\n"
    "ds_read_b64_tr_b16 a[90:91], v67 offset:49152\n"
    "ds_read_b64_tr_b16 a[94:95], v66 offset:49152\n"
    "ds_read_b64_tr_b16 a[122:123], v65 offset:49152\n"
    "ds_read_b64_tr_b16 a[126:127], v64 offset:49152\n"
    "s_waitcnt vmcnt(28) lgkmcnt(0)\n"
    "s_barrier\n"
    "s_add_u32 m0, s54, 114688\n"
    "s_add_u32 s55, %6, 819200\n"
    "buffer_load_dwordx4 v48, s[44:47], s55 offen lds\n"
    "s_add_u32 m0, s54, 118784\n"
    "s_add_u32 s55, %6, 1867776\n"
    "buffer_load_dwordx4 v48, s[44:47], s55 offen lds\n"
    "s_add_u32 m0, s54, 122880\n"
    "s_add_u32 s55, %6, 2916352\n"
    "buffer_load_dwordx4 v48, s[44:47], s55 offen lds\n"
    "s_add_u32 m0, s54, 126976\n"
    "s_add_u32 s55, %6, 3964928\n"
    "buffer_load_dwordx4 v48, s[44:47], s55 offen lds\n"
    "v_mfma_f32_16x16x32_bf16 v[8:11], a[24:27], v[72:75], v[8:11]\n"
    "v_mfma_f32_16x16x32_bf16 v[12:15], a[28:31], v[72:75], v[12:15]\n"
    "v_mfma_f32_16x16x32_bf16 v[8:11], a[56:59], v[72:75], v[8:11]\n"
    "v_mfma_f32_16x16x32_bf16 v[12:15], a[60:63], v[72:75], v[12:15]\n"
    "v_mfma_f32_16x16x32_bf16 v[8:11], a[88:91], v[72:75], v[8:11]\n"
    "v_mfma_f32_16x16x32_bf16 v[12:15], a[92:95], v[72:75], v[12:15]\n"
    "v_mfma_f32_16x16x32_bf16 v[8:11], a[120:123], v[72:75], v[8:11]\n"
    "v_mfma_f32_16x16x32_bf16 v[12:15], a[124:127], v[72:75], v[12:15]\n"
    "v_add_u32 v63, 0x20000, v49\n"
    "ds_read_b64_tr_b16 a[128:129], v63\n"
    "v_add_u32 v62, 0x20000, v50\n"
    "ds_read_b64_tr_b16 a[132:133], v62\n"
    "ds_read_b64_tr_b16 a[130:131], v63 offset:4096\n"
    "ds_read_b64_tr_b16 a[134:135], v62 offset:4096\n"
    "ds_read_b64_tr_b16 a[136:137], v63 offset:8192\n"
    "ds_read_b64_tr_b16 a[140:141], v62 offset:8192\n"
    "ds_read_b64_tr_b16 a[138:139], v63 offset:12288\n"
    "ds_read_b64_tr_b16 a[142:143], v62 offset:12288\n"
    "s_waitcnt vmcnt(28) lgkmcnt(0)\n"
    "s_barrier\n"
    "s_add_u32 m0, s54, 131072\n"
    "s_add_u32 s55, %6, 4194304\n"
    "buffer_load_dwordx4 v48, s[44:47], s55 offen lds\n"
    "s_add_u32 m0, s54, 135168\n"
    "s_add_u32 s55, %6, 4227072\n"
    "buffer_load_dwordx4 v48, s[44:47], s55 offen lds\n"
    "s_add_u32 m0, s54, 139264\n"
    "s_add_u32 s55, %6, 4456448\n"
    "buffer_load_dwordx4 v48, s[44:47], s55 offen lds\n"
    "s_add_u32 m0, s54, 143360\n"
    "s_add_u32 s55, %6, 4489216\n"
    "buffer_load_dwordx4 v48, s[44:47], s55 offen lds\n"
    "v_mfma_f32_16x16x32_bf16 v[8:11], a[128:131], v[72:75], v[8:11]\n"
    "v_mfma_f32_16x16x32_bf16 v[12:15], a[132:135], v[72:75], v[12:15]\n"
    "v_mfma_f32_16x16x32_bf16 v[8:11], a[136:139], v[72:75], v[8:11]\n"
    "v_mfma_f32_16x16x32_bf16 v[12:15], a[140:143], v[72:75], v[12:15]\n"
    "ds_read_b64_tr_b16 a[144:145], v32\n"
    "ds_read_b64_tr_b16 a[148:149], v33\n"
    "ds_read_b64_tr_b16 a[176:177], v34\n"
    "ds_read_b64_tr_b16 a[180:181], v35\n"
    "ds_read_b64_tr_b16 a[208:209], v36\n"
    "ds_read_b64_tr_b16 a[212:213], v37\n"
    "ds_read_b64_tr_b16 a[240:241], v38\n"
    "ds_read_b64_tr_b16 a[244:245], v39\n"
    "s_waitcnt vmcnt(28) lgkmcnt(0)\n"
    "s_barrier\n"
    "s_mov_b64 s[44:45], %10\n"
    "s_and_b32 s45, s45, 0xffff\n"
    "s_mov_b32 s46, 0x480000\n"
    "s_mov_b32 s47, 0x20000\n"
    "s_add_u32 m0, s54, 0\n"
    "s_add_u32 s55, %6, 0\n"
    "buffer_load_dwordx4 v48, s[44:47], s55 offen lds\n"
    "s_add_u32 m0, s54, 4096\n"
    "s_add_u32 s55, %6, 1048576\n"
    "buffer_load_dwordx4 v48, s[44:47], s55 offen lds\n"
    "s_add_u32 m0, s54, 8192\n"
    "s_add_u32 s55, %6, 2097152\n"
    "buffer_load_dwordx4 v48, s[44:47], s55 offen lds\n"
    "s_add_u32 m0, s54, 12288\n"
    "s_add_u32 s55, %6, 3145728\n"
    "buffer_load_dwordx4 v48, s[44:47], s55 offen lds\n"
    "ds_read_b64_tr_b16 a[146:147], v32 offset:16384\n"
    "ds_read_b64_tr_b16 a[150:151], v33 offset:16384\n"
    "ds_read_b64_tr_b16 a[178:179], v34 offset:16384\n"
    "ds_read_b64_tr_b16 a[182:183], v35 offset:16384\n"
    "ds_read_b64_tr_b16 a[210:211], v36 offset:16384\n"
    "ds_read_b64_tr_b16 a[214:215], v37 offset:16384\n"
    "ds_read_b64_tr_b16 a[242:243], v38 offset:16384\n"
    "ds_read_b64_tr_b16 a[246:247], v39 offset:16384\n"
    "s_waitcnt vmcnt(28) lgkmcnt(0)\n"
    "s_barrier\n"
    "s_add_u32 m0, s54, 16384\n"
    "s_add_u32 s55, %6, 32768\n"
    "buffer_load_dwordx4 v48, s[44:47], s55 offen lds\n"
    "s_add_u32 m0, s54, 20480\n"
    "s_add_u32 s55, %6, 1081344\n"
    "buffer_load_dwordx4 v48, s[44:47], s55 offen lds\n"
    "s_add_u32 m0, s54, 24576\n"
    "s_add_u32 s55, %6, 2129920\n"
    "buffer_load_dwordx4 v48, s[44:47], s55 offen lds\n"
    "s_add_u32 m0, s54, 28672\n"
    "s_add_u32 s55, %6, 3178496\n"
    "buffer_load_dwordx4 v48, s[44:47], s55 offen lds\n"
    "v_mfma_f32_16x16x32_bf16 v[16:19], a[144:147], v[72:75], 0\n"
    "v_mfma_f32_16x16x32_bf16 v[20:23], a[148:151], v[72:75], 0\n"
    "v_mfma_f32_16x16x32_bf16 v[16:19], a[176:179], v[72:75], v[16:19]\n"
    "v_mfma_f32_16x16x32_bf16 v[20:23], a[180:183], v[72:75], v[20:23]\n"
    "v_mfma_f32_16x16x32_bf16 v[16:19], a[208:211], v[72:75], v[16:19]\n"
    "v_mfma_f32_16x16x32_bf16 v[20:23], a[212:215], v[72:75], v[20:23]\n"
    "v_mfma_f32_16x16x32_bf16 v[16:19], a[240:243], v[72:75], v[16:19]\n"
    "v_mfma_f32_16x16x32_bf16 v[20:23], a[244:247], v[72:75], v[20:23]\n"
    "ds_read_b64_tr_b16 a[152:153], v32 offset:32768\n"
    "ds_read_b64_tr_b16 a[156:157], v33 offset:32768\n"
    "ds_read_b64_tr_b16 a[184:185], v34 offset:32768\n"
    "ds_read_b64_tr_b16 a[188:189], v35 offset:32768\n"
    "ds_read_b64_tr_b16 a[216:217], v36 offset:32768\n"
    "ds_read_b64_tr_b16 a[220:221], v37 offset:32768\n"
    "ds_read_b64_tr_b16 a[248:249], v38 offset:32768\n"
    "ds_read_b64_tr_b16 a[252:253], v39 offset:32768\n"
    "s_waitcnt vmcnt(28) lgkmcnt(0)\n"
    "s_barrier\n"
    "s_add_u32 m0, s54, 32768\n"
    "s_add_u32 s55, %6, 262144\n"
    "buffer_load_dwordx4 v48, s[44:47], s55 offen lds\n"
    "s_add_u32 m0, s54, 36864\n"
    "s_add_u32 s55, %6, 1310720\n"
    "buffer_load_dwordx4 v48, s[44:47], s55 offen lds\n"
    "s_add_u32 m0, s54, 40960\n"
    "s_add_u32 s55, %6, 2359296\n"
    "buffer_load_dwordx4 v48, s[44:47], s55 offen lds\n"
    "s_add_u32 m0, s54, 45056\n"
    "s_add_u32 s55, %6, 3407872\n"
    "buffer_load_dwordx4 v48, s[44:47], s55 offen lds\n"
    "ds_read_b64_tr_b16 a[154:155], v32 offset:49152\n"
    "ds_read_b64_tr_b16 a[158:159], v33 offset:49152\n"
    "ds_read_b64_tr_b16 a[186:187], v34 offset:49152\n"
    "ds_read_b64_tr_b16 a[190:191], v35 offset:49152\n"
    "ds_read_b64_tr_b16 a[218:219], v36 offset:49152\n"
    "ds_read_b64_tr_b16 a[222:223], v37 offset:49152\n"
    "ds_read_b64_tr_b16 a[250:251], v38 offset:49152\n"
    "ds_read_b64_tr_b16 a[254:255], v39 offset:49152\n"
    "s_waitcnt vmcnt(28) lgkmcnt(0)\n"
    "s_barrier\n"
    "s_add_u32 m0, s54, 49152\n"
    "s_add_u32 s55, %6, 294912\n"
    "buffer_load_dwordx4 v48, s[44:47], s55 offen lds\n"
    "s_add_u32 m0, s54, 53248\n"
    "s_add_u32 s55, %6, 1343488\n"
    "buffer_load_dwordx4 v48, s[44:47], s55 offen lds\n"
    "s_add_u32 m0, s54, 57344\n"
    "s_add_u32 s55, %6, 2392064\n"
    "buffer_load_dwordx4 v48, s[44:47], s55 offen lds\n"
    "s_add_u32 m0, s54, 61440\n"
    "s_add_u32 s55, %6, 3440640\n"
    "buffer_load_dwordx4 v48, s[44:47], s55 offen lds\n"
    "v_mfma_f32_16x16x32_bf16 v[16:19], a[152:155], v[72:75], v[16:19]\n"
    "v_mfma_f32_16x16x32_bf16 v[20:23], a[156:159], v[72:75], v[20:23]\n"
    "v_mfma_f32_16x16x32_bf16 v[16:19], a[184:187], v[72:75], v[16:19]\n"
    "v_mfma_f32_16x16x32_bf16 v[20:23], a[188:191], v[72:75], v[20:23]\n"
    "v_mfma_f32_16x16x32_bf16 v[16:19], a[216:219], v[72:75], v[16:19]\n"
    "v_mfma_f32_16x16x32_bf16 v[20:23], a[220:223], v[72:75], v[20:23]\n"
    "v_mfma_f32_16x16x32_bf16 v[16:19], a[248:251], v[72:75], v[16:19]\n"
    "v_mfma_f32_16x16x32_bf16 v[20:23], a[252:255], v[72:75], v[20:23]\n"
    "ds_read_b64_tr_b16 a[160:161], v71\n"
    "ds_read_b64_tr_b16 a[164:165], v70\n"
    "ds_read_b64_tr_b16 a[192:193], v69\n"
    "ds_read_b64_tr_b16 a[196:197], v68\n"
    "ds_read_b64_tr_b16 a[224:225], v67\n"
    "ds_read_b64_tr_b16 a[228:229], v66\n"
    "ds_read_b64_tr_b16 v[84:85], v65\n"
    "ds_read_b64_tr_b16 v[88:89], v64\n"
    "s_waitcnt vmcnt(28) lgkmcnt(0)\n"
    "s_barrier\n"
    "s_add_u32 m0, s54, 65536\n"
    "s_add_u32 s55, %6, 524288\n"
    "buffer_load_dwordx4 v48, s[44:47], s55 offen lds\n"
    "s_add_u32 m0, s54, 69632\n"
    "s_add_u32 s55, %6, 1572864\n"
    "buffer_load_dwordx4 v48, s[44:47], s55 offen lds\n"
    "s_add_u32 m0, s54, 73728\n"
    "s_add_u32 s55, %6, 2621440\n"
    "buffer_load_dwordx4 v48, s[44:47], s55 offen lds\n"
    "s_add_u32 m0, s54, 77824\n"
    "s_add_u32 s55, %6, 3670016\n"
    "buffer_load_dwordx4 v48, s[44:47], s55 offen lds\n"
    "ds_read_b64_tr_b16 a[162:163], v71 offset:16384\n"
    "ds_read_b64_tr_b16 a[166:167], v70 offset:16384\n"
    "ds_read_b64_tr_b16 a[194:195], v69 offset:16384\n"
    "ds_read_b64_tr_b16 a[198:199], v68 offset:16384\n"
    "ds_read_b64_tr_b16 a[226:227], v67 offset:16384\n"
    "ds_read_b64_tr_b16 a[230:231], v66 offset:16384\n"
    "ds_read_b64_tr_b16 v[86:87], v65 offset:16384\n"
    "ds_read_b64_tr_b16 v[90:91], v64 offset:16384\n"
    "s_waitcnt vmcnt(28) lgkmcnt(0)\n"
    "s_barrier\n"
    "s_add_u32 m0, s54, 81920\n"
    "s_add_u32 s55, %6, 557056\n"
    "buffer_load_dwordx4 v48, s[44:47], s55 offen lds\n"
    "s_add_u32 m0, s54, 86016\n"
    "s_add_u32 s55, %6, 1605632\n"
    "buffer_load_dwordx4 v48, s[44:47], s55 offen lds\n"
    "s_add_u32 m0, s54, 90112\n"
    "s_add_u32 s55, %6, 2654208\n"
    "buffer_load_dwordx4 v48, s[44:47], s55 offen lds\n"
    "s_add_u32 m0, s54, 94208\n"
    "s_add_u32 s55, %6, 3702784\n"
    "buffer_load_dwordx4 v48, s[44:47], s55 offen lds\n"
    "v_mfma_f32_16x16x32_bf16 v[16:19], a[160:163], v[72:75], v[16:19]\n"
    "v_mfma_f32_16x16x32_bf16 v[20:23], a[164:167], v[72:75], v[20:23]\n"
    "v_mfma_f32_16x16x32_bf16 v[16:19], a[192:195], v[72:75], v[16:19]\n"
    "v_mfma_f32_16x16x32_bf16 v[20:23], a[196:199], v[72:75], v[20:23]\n"
    "v_mfma_f32_16x16x32_bf16 v[16:19], a[224:227], v[72:75], v[16:19]\n"
    "v_mfma_f32_16x16x32_bf16 v[20:23], a[228:231], v[72:75], v[20:23]\n"
    "v_mfma_f32_16x16x32_bf16 v[16:19], v[84:87], v[72:75], v[16:19]\n"
    "v_mfma_f32_16x16x32_bf16 v[20:23], v[88:91], v[72:75], v[20:23]\n"
    "ds_read_b64_tr_b16 a[168:169], v71 offset:32768\n"
    "ds_read_b64_tr_b16 a[172:173], v70 offset:32768\n"
    "ds_read_b64_tr_b16 a[200:201], v69 offset:32768\n"
    "ds_read_b64_tr_b16 a[204:205], v68 offset:32768\n"
    "ds_read_b64_tr_b16 a[232:233], v67 offset:32768\n"
    "ds_read_b64_tr_b16 a[236:237], v66 offset:32768\n"
    "ds_read_b64_tr_b16 v[92:93], v65 offset:32768\n"
    "ds_read_b64_tr_b16 v[96:97], v64 offset:32768\n"
    "s_waitcnt vmcnt(28) lgkmcnt(0)\n"
    "s_barrier\n"
    "s_add_u32 m0, s54, 98304\n"
    "s_add_u32 s55, %6, 786432\n"
    "buffer_load_dwordx4 v48, s[44:47], s55 offen lds\n"
    "s_add_u32 m0, s54, 102400\n"
    "s_add_u32 s55, %6, 1835008\n"
    "buffer_load_dwordx4 v48, s[44:47], s55 offen lds\n"
    "s_add_u32 m0, s54, 106496\n"
    "s_add_u32 s55, %6, 2883584\n"
    "buffer_load_dwordx4 v48, s[44:47], s55 offen lds\n"
    "s_add_u32 m0, s54, 110592\n"
    "s_add_u32 s55, %6, 3932160\n"
    "buffer_load_dwordx4 v48, s[44:47], s55 offen lds\n"
    "ds_read_b64_tr_b16 a[170:171], v71 offset:49152\n"
    "ds_read_b64_tr_b16 a[174:175], v70 offset:49152\n"
    "ds_read_b64_tr_b16 a[202:203], v69 offset:49152\n"
    "ds_read_b64_tr_b16 a[206:207], v68 offset:49152\n"
    "ds_read_b64_tr_b16 a[234:235], v67 offset:49152\n"
    "ds_read_b64_tr_b16 a[238:239], v66 offset:49152\n"
    "ds_read_b64_tr_b16 v[94:95], v65 offset:49152\n"
    "ds_read_b64_tr_b16 v[98:99], v64 offset:49152\n"
    "s_waitcnt vmcnt(28) lgkmcnt(0)\n"
    "s_barrier\n"
    "s_add_u32 m0, s54, 114688\n"
    "s_add_u32 s55, %6, 819200\n"
    "buffer_load_dwordx4 v48, s[44:47], s55 offen lds\n"
    "s_add_u32 m0, s54, 118784\n"
    "s_add_u32 s55, %6, 1867776\n"
    "buffer_load_dwordx4 v48, s[44:47], s55 offen lds\n"
    "s_add_u32 m0, s54, 122880\n"
    "s_add_u32 s55, %6, 2916352\n"
    "buffer_load_dwordx4 v48, s[44:47], s55 offen lds\n"
    "s_add_u32 m0, s54, 126976\n"
    "s_add_u32 s55, %6, 3964928\n"
    "buffer_load_dwordx4 v48, s[44:47], s55 offen lds\n"
    "v_mfma_f32_16x16x32_bf16 v[16:19], a[168:171], v[72:75], v[16:19]\n"
    "v_mfma_f32_16x16x32_bf16 v[20:23], a[172:175], v[72:75], v[20:23]\n"
    "v_mfma_f32_16x16x32_bf16 v[16:19], a[200:203], v[72:75], v[16:19]\n"
    "v_mfma_f32_16x16x32_bf16 v[20:23], a[204:207], v[72:75], v[20:23]\n"
    "v_mfma_f32_16x16x32_bf16 v[16:19], a[232:235], v[72:75], v[16:19]\n"
    "v_mfma_f32_16x16x32_bf16 v[20:23], a[236:239], v[72:75], v[20:23]\n"
    "v_mfma_f32_16x16x32_bf16 v[16:19], v[92:95], v[72:75], v[16:19]\n"
    "v_mfma_f32_16x16x32_bf16 v[20:23], v[96:99], v[72:75], v[20:23]\n"
    "ds_read_b64_tr_b16 v[100:101], v63\n"
    "ds_read_b64_tr_b16 v[104:105], v62\n"
    "ds_read_b64_tr_b16 v[102:103], v63 offset:4096\n"
    "ds_read_b64_tr_b16 v[106:107], v62 offset:4096\n"
    "ds_read_b64_tr_b16 v[108:109], v63 offset:8192\n"
    "ds_read_b64_tr_b16 v[112:113], v62 offset:8192\n"
    "ds_read_b64_tr_b16 v[110:111], v63 offset:12288\n"
    "ds_read_b64_tr_b16 v[114:115], v62 offset:12288\n"
    "s_waitcnt vmcnt(28) lgkmcnt(0)\n"
    "s_barrier\n"
    "s_add_u32 m0, s54, 131072\n"
    "s_add_u32 s55, %6, 4194304\n"
    "buffer_load_dwordx4 v48, s[44:47], s55 offen lds\n"
    "s_add_u32 m0, s54, 135168\n"
    "s_add_u32 s55, %6, 4227072\n"
    "buffer_load_dwordx4 v48, s[44:47], s55 offen lds\n"
    "s_add_u32 m0, s54, 139264\n"
    "s_add_u32 s55, %6, 4456448\n"
    "buffer_load_dwordx4 v48, s[44:47], s55 offen lds\n"
    "s_add_u32 m0, s54, 143360\n"
    "s_add_u32 s55, %6, 4489216\n"
    "buffer_load_dwordx4 v48, s[44:47], s55 offen lds\n"
    "v_mov_b32 v7, 0x20400\n"
    "global_load_dwordx2 v[80:81], v7, %1 sc1\n"
    "v_mbcnt_lo_u32_b32 v4, -1, 0\n"
    "v_min_u32 v4, 7, v4\n"
    "v_lshlrev_b32 v4, 7, v4\n"
    "v_add_u32 v4, 0x20000, v4\n"
    "global_load_dwordx2 v[82:83], v4, %1 sc1\n"
    "v_mfma_f32_16x16x32_bf16 v[16:19], v[100:103], v[72:75], v[16:19]\n"
    "v_mfma_f32_16x16x32_bf16 v[20:23], v[104:107], v[72:75], v[20:23]\n"
    "v_mfma_f32_16x16x32_bf16 v[16:19], v[108:111], v[72:75], v[16:19]\n"
    "v_mfma_f32_16x16x32_bf16 v[20:23], v[112:115], v[72:75], v[20:23]\n"
    "ds_read_b64_tr_b16 v[116:117], v32\n"
    "ds_read_b64_tr_b16 v[120:121], v33\n"
    "ds_read_b64_tr_b16 v[148:149], v34\n"
    "ds_read_b64_tr_b16 v[152:153], v35\n"
    "ds_read_b64_tr_b16 v[180:181], v36\n"
    "ds_read_b64_tr_b16 v[184:185], v37\n"
    "ds_read_b64_tr_b16 v[212:213], v38\n"
    "ds_read_b64_tr_b16 v[216:217], v39\n"
    "s_waitcnt vmcnt(30) lgkmcnt(0)\n"
    "s_barrier\n"
    "ds_read_b64_tr_b16 v[118:119], v32 offset:16384\n"
    "ds_read_b64_tr_b16 v[122:123], v33 offset:16384\n"
    "ds_read_b64_tr_b16 v[150:151], v34 offset:16384\n"
    "ds_read_b64_tr_b16 v[154:155], v35 offset:16384\n"
    "ds_read_b64_tr_b16 v[182:183], v36 offset:16384\n"
    "ds_read_b64_tr_b16 v[186:187], v37 offset:16384\n"
    "ds_read_b64_tr_b16 v[214:215], v38 offset:16384\n"
    "ds_read_b64_tr_b16 v[218:219], v39 offset:16384\n"
    "s_waitcnt vmcnt(26) lgkmcnt(0)\n"
    "s_barrier\n"
    "v_mfma_f32_16x16x32_bf16 v[24:27], v[72:75], v[116:119], 0\n"
    "v_mfma_f32_16x16x32_bf16 v[28:31], v[72:75], v[120:123], 0\n"
    "v_mfma_f32_16x16x32_bf16 v[24:27], v[72:75], v[148:151], v[24:27]\n"
    "v_mfma_f32_16x16x32_bf16 v[28:31], v[72:75], v[152:155], v[28:31]\n"
    "v_mfma_f32_16x16x32_bf16 v[24:27], v[72:75], v[180:183], v[24:27]\n"
    "v_mfma_f32_16x16x32_bf16 v[28:31], v[72:75], v[184:187], v[28:31]\n"
    "v_mfma_f32_16x16x32_bf16 v[24:27], v[72:75], v[212:215], v[24:27]\n"
    "v_mfma_f32_16x16x32_bf16 v[28:31], v[72:75], v[216:219], v[28:31]\n"
    "ds_read_b64_tr_b16 v[124:125], v32 offset:32768\n"
    "ds_read_b64_tr_b16 v[128:129], v33 offset:32768\n"
    "ds_read_b64_tr_b16 v[156:157], v34 offset:32768\n"
    "ds_read_b64_tr_b16 v[160:161], v35 offset:32768\n"
    "ds_read_b64_tr_b16 v[188:189], v36 offset:32768\n"
    "ds_read_b64_tr_b16 v[192:193], v37 offset:32768\n"
    "ds_read_b64_tr_b16 v[220:221], v38 offset:32768\n"
    "ds_read_b64_tr_b16 v[224:225], v39 offset:32768\n"
    "s_waitcnt vmcnt(22) lgkmcnt(0)\n"
    "s_barrier\n"
    "ds_read_b64_tr_b16 v[126:127], v32 offset:49152\n"
    "ds_read_b64_tr_b16 v[130:131], v33 offset:49152\n"
    "ds_read_b64_tr_b16 v[158:159], v34 offset:49152\n"
    "ds_read_b64_tr_b16 v[162:163], v35 offset:49152\n"
    "ds_read_b64_tr_b16 v[190:191], v36 offset:49152\n"
    "ds_read_b64_tr_b16 v[194:195], v37 offset:49152\n"
    "ds_read_b64_tr_b16 v[222:223], v38 offset:49152\n"
    "ds_read_b64_tr_b16 v[226:227], v39 offset:49152\n"
    "s_waitcnt vmcnt(18) lgkmcnt(0)\n"
    "s_barrier\n"
    "v_mfma_f32_16x16x32_bf16 v[24:27], v[72:75], v[124:127], v[24:27]\n"
    "v_mfma_f32_16x16x32_bf16 v[28:31], v[72:75], v[128:131], v[28:31]\n"
    "v_mfma_f32_16x16x32_bf16 v[24:27], v[72:75], v[156:159], v[24:27]\n"
    "v_mfma_f32_16x16x32_bf16 v[28:31], v[72:75], v[160:163], v[28:31]\n"
    "v_mfma_f32_16x16x32_bf16 v[24:27], v[72:75], v[188:191], v[24:27]\n"
    "v_mfma_f32_16x16x32_bf16 v[28:31], v[72:75], v[192:195], v[28:31]\n"
    "v_mfma_f32_16x16x32_bf16 v[24:27], v[72:75], v[220:223], v[24:27]\n"
    "v_mfma_f32_16x16x32_bf16 v[28:31], v[72:75], v[224:227], v[28:31]\n"
    "ds_read_b64_tr_b16 v[132:133], v71\n"
    "ds_read_b64_tr_b16 v[136:137], v70\n"
    "ds_read_b64_tr_b16 v[164:165], v69\n"
    "ds_read_b64_tr_b16 v[168:169], v68\n"
    "ds_read_b64_tr_b16 v[196:197], v67\n"
    "ds_read_b64_tr_b16 v[200:201], v66\n"
    "ds_read_b64_tr_b16 v[228:229], v65\n"
    "ds_read_b64_tr_b16 v[232:233], v64\n"
    "s_waitcnt vmcnt(14) lgkmcnt(0)\n"
    "s_barrier\n"
    "ds_read_b64_tr_b16 v[134:135], v71 offset:16384\n"
    "ds_read_b64_tr_b16 v[138:139], v70 offset:16384\n"
    "ds_read_b64_tr_b16 v[166:167], v69 offset:16384\n"
    "ds_read_b64_tr_b16 v[170:171], v68 offset:16384\n"
    "ds_read_b64_tr_b16 v[198:199], v67 offset:16384\n"
    "ds_read_b64_tr_b16 v[202:203], v66 offset:16384\n"
    "ds_read_b64_tr_b16 v[230:231], v65 offset:16384\n"
    "ds_read_b64_tr_b16 v[234:235], v64 offset:16384\n"
    "s_waitcnt vmcnt(10) lgkmcnt(0)\n"
    "s_barrier\n"
    "v_mfma_f32_16x16x32_bf16 v[24:27], v[72:75], v[132:135], v[24:27]\n"
    "v_mfma_f32_16x16x32_bf16 v[28:31], v[72:75], v[136:139], v[28:31]\n"
    "v_mfma_f32_16x16x32_bf16 v[24:27], v[72:75], v[164:167], v[24:27]\n"
    "v_mfma_f32_16x16x32_bf16 v[28:31], v[72:75], v[168:171], v[28:31]\n"
    "v_mfma_f32_16x16x32_bf16 v[24:27], v[72:75], v[196:199], v[24:27]\n"
    "v_mfma_f32_16x16x32_bf16 v[28:31], v[72:75], v[200:203], v[28:31]\n"
    "v_mfma_f32_16x16x32_bf16 v[24:27], v[72:75], v[228:231], v[24:27]\n"
    "v_mfma_f32_16x16x32_bf16 v[28:31], v[72:75], v[232:235], v[28:31]\n"
    "ds_read_b64_tr_b16 v[140:141], v71 offset:32768\n"
    "ds_read_b64_tr_b16 v[144:145], v70 offset:32768\n"
    "ds_read_b64_tr_b16 v[172:173], v69 offset:32768\n"
    "ds_read_b64_tr_b16 v[176:177], v68 offset:32768\n"
    "ds_read_b64_tr_b16 v[204:205], v67 offset:32768\n"
    "ds_read_b64_tr_b16 v[208:209], v66 offset:32768\n"
    "ds_read_b64_tr_b16 v[236:237], v65 offset:32768\n"
    "ds_read_b64_tr_b16 v[240:241], v64 offset:32768\n"
    "s_waitcnt vmcnt(6) lgkmcnt(0)\n"
    "s_barrier\n"
    "ds_read_b64_tr_b16 v[142:143], v71 offset:49152\n"
    "ds_read_b64_tr_b16 v[146:147], v70 offset:49152\n"
    "ds_read_b64_tr_b16 v[174:175], v69 offset:49152\n"
    "ds_read_b64_tr_b16 v[178:179], v68 offset:49152\n"
    "ds_read_b64_tr_b16 v[206:207], v67 offset:49152\n"
    "ds_read_b64_tr_b16 v[210:211], v66 offset:49152\n"
    "ds_read_b64_tr_b16 v[238:239], v65 offset:49152\n"
    "ds_read_b64_tr_b16 v[242:243], v64 offset:49152\n"
    "s_waitcnt vmcnt(2) lgkmcnt(0)\n"
    "s_barrier\n"
    "v_mfma_f32_16x16x32_bf16 v[24:27], v[72:75], v[140:143], v[24:27]\n"
    "v_mfma_f32_16x16x32_bf16 v[28:31], v[72:75], v[144:147], v[28:31]\n"
    "v_mfma_f32_16x16x32_bf16 v[24:27], v[72:75], v[172:175], v[24:27]\n"
    "v_mfma_f32_16x16x32_bf16 v[28:31], v[72:75], v[176:179], v[28:31]\n"
    "v_mfma_f32_16x16x32_bf16 v[24:27], v[72:75], v[204:207], v[24:27]\n"
    "v_mfma_f32_16x16x32_bf16 v[28:31], v[72:75], v[208:211], v[28:31]\n"
    "v_mfma_f32_16x16x32_bf16 v[24:27], v[72:75], v[236:239], v[24:27]\n"
    "v_mfma_f32_16x16x32_bf16 v[28:31], v[72:75], v[240:243], v[28:31]\n"
    "ds_read_b64_tr_b16 v[244:245], v63\n"
    "ds_read_b64_tr_b16 v[248:249], v62\n"
    "ds_read_b64_tr_b16 v[246:247], v63 offset:4096\n"
    "ds_read_b64_tr_b16 v[250:251], v62 offset:4096\n"
    "ds_read_b64_tr_b16 v[252:253], v63 offset:8192\n"
    "ds_read_b64_tr_b16 v[76:77], v62 offset:8192\n"
    "ds_read_b64_tr_b16 v[254:255], v63 offset:12288\n"
    "ds_read_b64_tr_b16 v[78:79], v62 offset:12288\n"
    "s_waitcnt lgkmcnt(0)\n"
    "s_barrier\n"
    "v_mfma_f32_16x16x32_bf16 v[24:27], v[72:75], v[244:247], v[24:27]\n"
    "v_mfma_f32_16x16x32_bf16 v[28:31], v[72:75], v[248:251], v[28:31]\n"
    "v_mfma_f32_16x16x32_bf16 v[24:27], v[72:75], v[252:255], v[24:27]\n"
    "v_mfma_f32_16x16x32_bf16 v[28:31], v[72:75], v[76:79], v[28:31]\n"
    "ds_write_b128 v2, v[8:11] offset:8192\n"
    "ds_write_b128 v2, v[12:15] offset:12288\n"
    "ds_write_b128 v2, v[16:19] offset:16384\n"
    "ds_write_b128 v2, v[20:23] offset:20480\n"
    "s_nop 0\n"
    "v_mov_b32 v74, v24\n"
    "s_nop 2\n"
    "v_mov_b32 v75, v28\n"
    "s_mov_b64 s[44:45], %1\n"
    "s_and_b32 s45, s45, 0xffff\n"
    "s_mov_b32 s46, 0x0\n"
    "s_mov_b32 s47, 0x20000\n"
    "v_mov_b32 v6, 0\n"
    "s_waitcnt vmcnt(0)\n"
    "v_add_u32 v80, 1, v80\n"
    "v_add_u32 v81, 1, v81\n"
    "s_mov_b32 s52, 1048576\n"
    ".Lhgo_a%=:\n"
    "v_cmp_ne_u64 vcc, v[80:81], v[82:83]\n"
    "s_cbranch_vccz .Lhgo_x%=\n"
    "global_load_dwordx2 v[82:83], v4, %1 sc1\n"
    "s_sub_u32 s52, s52, 1\n"
    "s_waitcnt vmcnt(0)\n"
    "s_cbranch_scc0 .Lhgo_a%=\n"
    ".Lhgo_x%=:\n"
    "v_mbcnt_lo_u32_b32 v80, -1, 0\n"
    "v_mbcnt_hi_u32_b32 v80, -1, v80\n"
    "v_min_u32 v4, 5, v80\n"
    "v_lshlrev_b32 v4, 7, v4\n"
    "v_mov_b32 v7, 0x26c00\n"
    "ds_read_b128 v[80:83], v7\n"
    "s_waitcnt lgkmcnt(0)\n"
    "v_readfirstlane_b32 s49, v81\n"
    "v_add_u32 v80, v80, v4\n"
    "s_branch .Lhchk_%=\n"
    ".Lhgo_%=:\n"
    "s_waitcnt lgkmcnt(1)\n"
    "v_mfma_f32_16x16x32_bf16 v[8:11], a[0:3], v[48:51], 0\n"
    "s_waitcnt vmcnt(5)\n"
    "ds_read_b128 v[56:59], v1 offset:2048\n"
    "v_mfma_f32_16x16x32_bf16 v[12:15], a[4:7], v[48:51], 0\n"
    "ds_read_b128 v[80:83], v7 offset:32\n"
    "v_add_u32 v7, 32, v7\n"
    "v_mfma_f32_16x16x32_bf16 v[16:19], a[144:147], v[48:51], 0\n"
    "ds_read_b128 v[60:63], v1 offset:3072\n"
    "v_mul_f32 v24, v24, v68\n"
    "v_mfma_f32_16x16x32_bf16 v[20:23], a[148:151], v[48:51], 0\n"
    "s_waitcnt lgkmcnt(3)\n"
    "v_mul_f32 v25, v25, v69\n"
    "v_cvt_pk_bf16_f32 v24, v24, v25\n"
    "v_mfma_f32_16x16x32_bf16 v[8:11], a[8:11], v[52:55], v[8:11]\n"
    "v_mul_f32 v26, v26, v70\n"
    "v_mul_f32 v27, v27, v71\n"
    "v_mfma_f32_16x16x32_bf16 v[12:15], a[12:15], v[52:55], v[12:15]\n"
    "v_cvt_pk_bf16_f32 v25, v26, v27\n"
    "v_mul_f32 v28, v28, v68\n"
    "v_mfma_f32_16x16x32_bf16 v[16:19], a[152:155], v[52:55], v[16:19]\n"
    "v_mul_f32 v29, v29, v69\n"
    "v_cvt_pk_bf16_f32 v26, v28, v29\n"
    "v_mfma_f32_16x16x32_bf16 v[20:23], a[156:159], v[52:55], v[20:23]\n"
    "s_waitcnt lgkmcnt(1)\n"
    "v_mul_f32 v30, v30, v70\n"
    "v_mul_f32 v31, v31, v71\n"
    "v_mfma_f32_16x16x32_bf16 v[8:11], a[16:19], v[56:59], v[8:11]\n"
    "v_cvt_pk_bf16_f32 v27, v30, v31\n"
    "v_readfirstlane_b32 s49, v81\n"
    "v_mfma_f32_16x16x32_bf16 v[12:15], a[20:23], v[56:59], v[12:15]\n"
    "buffer_store_dwordx4 v[24:27], v2, s[40:43], s64 offen sc1\n"
    "v_add_u32 v80, v80, v4\n"
    "v_mfma_f32_16x16x32_bf16 v[16:19], a[160:163], v[56:59], v[16:19]\n"
    "v_readfirstlane_b32 s62, v82\n"
    "v_xor_b32 v0, 0x2000, v1\n"
    "v_mfma_f32_16x16x32_bf16 v[20:23], a[164:167], v[56:59], v[20:23]\n"
    "s_waitcnt vmcnt(4)\n"
    ".Lhtop_%=:\n"
    "v_mfma_f32_16x16x32_bf16 v[24:27], v[48:51], v[116:119], 0\n"
    "s_barrier\n"
    "ds_read_b128 v[32:35], v0\n"
    "v_mfma_f32_16x16x32_bf16 v[28:31], v[48:51], v[120:123], 0\n"
    "ds_read2st64_b32 v[64:65], v5 offset1:1\n"
    "s_xor_b32 s58, s58, 0x8000\n"
    "v_mfma_f32_16x16x32_bf16 v[24:27], v[52:55], v[124:127], v[24:27]\n"
    "ds_read2st64_b32 v[66:67], v5 offset0:2 offset1:3\n"
    "s_xor_b32 s59, s59, 0x8000\n"
    "v_mfma_f32_16x16x32_bf16 v[28:31], v[52:55], v[128:131], v[28:31]\n"
    "ds_read2st64_b32 v[68:69], v5 offset0:4 offset1:5\n"
    "s_add_u32 s63, s62, 0x2000000\n"
    "v_mfma_f32_16x16x32_bf16 v[24:27], v[56:59], v[132:135], v[24:27]\n"
    "ds_read2st64_b32 v[70:71], v5 offset0:6 offset1:7\n"
    "s_add_u32 s64, s62, 0x4000000\n"
    "v_mfma_f32_16x16x32_bf16 v[28:31], v[56:59], v[136:139], v[28:31]\n"
    "ds_read_b32 v72, v5 offset:2048\n"
    "s_waitcnt lgkmcnt(6)\n"
    "v_mfma_f32_16x16x32_bf16 v[8:11], a[24:27], v[60:63], v[8:11]\n"
    "ds_read_b128 v[36:39], v0 offset:1024\n"
    "v_lshrrev_b32 v48, 2, v5\n"
    "v_mfma_f32_16x16x32_bf16 v[12:15], a[28:31], v[60:63], v[12:15]\n"
    "ds_read_b128 v[40:43], v0 offset:2048\n"
    "v_and_b32 v48, 48, v48\n"
    "v_mfma_f32_16x16x32_bf16 v[16:19], a[168:171], v[60:63], v[16:19]\n"
    "ds_read_b128 v[44:47], v0 offset:3072\n"
    "v_bfi_b32 v48, s86, v48, v5\n"
    "v_mfma_f32_16x16x32_bf16 v[20:23], a[172:175], v[60:63], v[20:23]\n"
    "s_waitcnt lgkmcnt(3)\n"
    "v_add_f32 v64, v64, v65\n"
    "v_add_f32 v66, v66, v67\n"
    "v_mfma_f32_16x16x32_bf16 v[24:27], v[60:63], v[140:143], v[24:27]\n"
    "v_add_f32 v68, v68, v69\n"
    "v_add_f32 v70, v70, v71\n"
    "v_mfma_f32_16x16x32_bf16 v[28:31], v[60:63], v[144:147], v[28:31]\n"
    "v_add_f32 v64, v64, v66\n"
    "v_add_f32 v68, v68, v70\n"
    "v_mfma_f32_16x16x32_bf16 v[8:11], a[32:35], v[32:35], v[8:11]\n"
    "s_waitcnt vmcnt(3)\n"
    "v_cmp_eq_u32 vcc, 0, v6\n"
    "v_add_f32 v64, v64, v72\n"
    "v_mfma_f32_16x16x32_bf16 v[12:15], a[36:39], v[32:35], v[12:15]\n"
    "s_cmp_eq_u64 vcc, 0\n"
    "s_cselect_b32 s46, 0, 0x20000\n"
    "s_cselect_b32 s94, s42, 0\n"
    "v_mfma_f32_16x16x32_bf16 v[16:19], a[176:179], v[32:35], v[16:19]\n"
    "buffer_load_dword v81, v80, s[44:47], 0 offen sc1\n"
    "s_cselect_b32 s90, s96, 0\n"
    "v_mfma_f32_16x16x32_bf16 v[20:23], a[180:183], v[32:35], v[20:23]\n"
    "s_mov_b32 m0, s58\n"
    "v_xor_b32 v0, 0x4000, v1\n"
    "v_add_f32 v72, v64, v68\n"
    "v_mfma_f32_16x16x32_bf16 v[24:27], v[32:35], v[148:151], v[24:27]\n"
    "buffer_load_dwordx4 v3, s[92:95], s49 offen sc1 lds\n"
    "s_waitcnt lgkmcnt(2)\n"
    "v_mfma_f32_16x16x32_bf16 v[28:31], v[32:35], v[152:155], v[28:31]\n"
    "ds_read_b128 v[32:35], v0\n"
    "v_add_f32 v64, v64, v68\n"
    "v_mfma_f32_16x16x32_bf16 v[8:11], a[40:43], v[36:39], v[8:11]\n"
    "ds_read_b32 v6, v7 offset:32\n"
    "v_mfma_f32_16x16x32_bf16 v[12:15], a[44:47], v[36:39], v[12:15]\n"
    "v_permlane32_swap_b32 v64, v72\n"
    "v_mfma_f32_16x16x32_bf16 v[16:19], a[184:187], v[36:39], v[16:19]\n"
    "v_add_f32 v65, v64, v72\n"
    "v_add_f32 v72, v72, v64\n"
    "v_mfma_f32_16x16x32_bf16 v[20:23], a[188:191], v[36:39], v[20:23]\n"
    "s_nop 0\n"
    "v_permlane16_swap_b32 v65, v72\n"
    "v_mfma_f32_16x16x32_bf16 v[24:27], v[36:39], v[156:159], v[24:27]\n"
    "s_waitcnt lgkmcnt(3)\n"
    "v_mul_f32 v64, 0xbae38e39, v65\n"
    "v_mul_f32 v72, 0x3ae38e39, v72\n"
    "v_mfma_f32_16x16x32_bf16 v[28:31], v[36:39], v[160:163], v[28:31]\n"
    "ds_read_b128 v[36:39], v0 offset:1024\n"
    "v_cvt_pk_bf16_f32 v73, 0, v64\n"
    "v_mfma_f32_16x16x32_bf16 v[8:11], a[48:51], v[40:43], v[8:11]\n"
    "s_waitcnt lgkmcnt(1)\n"
    "v_add_u32 v6, v6, v4\n"
    "v_fma_f32 v72, -v64, v64, v72\n"
    "v_mfma_f32_16x16x32_bf16 v[12:15], a[52:55], v[40:43], v[12:15]\n"
    "buffer_load_dwordx4 v3, s[92:95], s49 offen offset:1024 sc1 lds\n"
    "v_max_f32 v72, 0, v72\n"
    "v_mfma_f32_16x16x32_bf16 v[16:19], a[192:195], v[40:43], v[16:19]\n"
    "v_cvt_pk_bf16_f32 v72, 0, v72\n"
    "v_add_f32 v72, 0x3727c5ac, v72\n"
    "v_mfma_f32_16x16x32_bf16 v[20:23], a[196:199], v[40:43], v[20:23]\n"
    "ds_read_b128 v[52:55], v2 offset:12288\n"
    "v_cvt_pk_bf16_f32 v72, 0, v72\n"
    "v_mfma_f32_16x16x32_bf16 v[24:27], v[40:43], v[164:167], v[24:27]\n"
    "v_sqrt_f32 v72, v72\n"
    "v_mfma_f32_16x16x32_bf16 v[28:31], v[40:43], v[168:171], v[28:31]\n"
    "ds_read_b128 v[40:43], v0 offset:2048\n"
    "v_cvt_pk_bf16_f32 v72, 0, v72\n"
    "v_mfma_f32_16x16x32_bf16 v[8:11], a[56:59], v[44:47], v[8:11]\n"
    "v_rcp_f32 v72, v72\n"
    "v_mfma_f32_16x16x32_bf16 v[12:15], a[60:63], v[44:47], v[12:15]\n"
    "ds_write2st64_b32 v5, v73, v72 offset0:9 offset1:10\n"
    "v_mfma_f32_16x16x32_bf16 v[16:19], a[200:203], v[44:47], v[16:19]\n"
    "buffer_load_dwordx4 v3, s[92:95], s49 offen offset:2048 sc1 lds\n"
    "v_mfma_f32_16x16x32_bf16 v[20:23], a[204:207], v[44:47], v[20:23]\n"
    "ds_read_b128 v[64:67], v48 offset:2304\n"
    "v_mfma_f32_16x16x32_bf16 v[24:27], v[44:47], v[172:175], v[24:27]\n"
    "v_mov_b32 v82, 1\n"
    "v_mfma_f32_16x16x32_bf16 v[28:31], v[44:47], v[176:179], v[28:31]\n"
    "ds_read_b128 v[44:47], v0 offset:3072\n"
    "s_waitcnt vmcnt(4)\n"
    "v_mfma_f32_16x16x32_bf16 v[8:11], a[64:67], v[32:35], v[8:11]\n"
    "s_mov_b64 exec, s[56:57]\n"
    "ds_add_rtn_u32 v82, v7, v82 offset:16\n"
    "s_mov_b64 exec, -1\n"
    "v_mfma_f32_16x16x32_bf16 v[12:15], a[68:71], v[32:35], v[12:15]\n"
    "ds_read_b128 v[68:71], v48 offset:2560\n"
    "v_mfma_f32_16x16x32_bf16 v[16:19], a[208:211], v[32:35], v[16:19]\n"
    "ds_read_b128 v[56:59], v2 offset:16384\n"
    "v_mfma_f32_16x16x32_bf16 v[20:23], a[212:215], v[32:35], v[20:23]\n"
    "buffer_load_dwordx4 v3, s[92:95], s49 offen offset:3072 sc1 lds\n"
    "v_xor_b32 v0, 0x6000, v1\n"
    "v_mfma_f32_16x16x32_bf16 v[24:27], v[32:35], v[180:183], v[24:27]\n"
    "ds_read_b128 v[60:63], v2 offset:20480\n"
    "s_waitcnt lgkmcnt(8)\n"
    "v_mfma_f32_16x16x32_bf16 v[28:31], v[32:35], v[184:187], v[28:31]\n"
    "ds_read_b128 v[32:35], v0\n"
    "v_mfma_f32_16x16x32_bf16 v[8:11], a[72:75], v[36:39], v[8:11]\n"
    "s_waitcnt lgkmcnt(4)\n"
    "v_readfirstlane_b32 s50, v82\n"
    "v_mfma_f32_16x16x32_bf16 v[12:15], a[76:79], v[36:39], v[12:15]\n"
    "s_cmp_eq_u32 s50, 3\n"
    "s_cselect_b32 s46, 0x20000, 0\n"
    "v_mfma_f32_16x16x32_bf16 v[16:19], a[216:219], v[36:39], v[16:19]\n"
    "buffer_store_dword v82, v83, s[44:47], 0 offen sc1\n"
    "v_mfma_f32_16x16x32_bf16 v[20:23], a[220:223], v[36:39], v[20:23]\n"
    "ds_read_b128 v[48:51], v2 offset:8192\n"
    "s_bitset1_b32 m0, 12\n"
    "v_mfma_f32_16x16x32_bf16 v[24:27], v[36:39], v[188:191], v[24:27]\n"
    "buffer_load_dwordx4 v2, s[88:91], s49 offen sc1 lds\n"
    "v_mfma_f32_16x16x32_bf16 v[28:31], v[36:39], v[192:195], v[28:31]\n"
    "ds_read_b128 v[36:39], v0 offset:1024\n"
    "s_mov_b32 m0, s59\n"
    "v_mfma_f32_16x16x32_bf16 v[8:11], a[80:83], v[40:43], v[8:11]\n"
    "s_mov_b32 s55, s49\n"
    "v_mfma_f32_16x16x32_bf16 v[12:15], a[84:87], v[40:43], v[12:15]\n"
    "s_bitset1_b32 s55, 15\n"
    "v_mfma_f32_16x16x32_bf16 v[16:19], a[224:227], v[40:43], v[16:19]\n"
    "s_waitcnt lgkmcnt(1)\n"
    "v_fmac_f32 v24, v64, v74\n"
    "v_mfma_f32_16x16x32_bf16 v[20:23], a[228:231], v[40:43], v[20:23]\n"
    "v_fmac_f32 v28, v64, v75\n"
    "v_mfma_f32_16x16x32_bf16 v[24:27], v[40:43], v[196:199], v[24:27]\n"
    "v_fmac_f32 v8, v48, v73\n"
    "v_mfma_f32_16x16x32_bf16 v[28:31], v[40:43], v[200:203], v[28:31]\n"
    "ds_read_b128 v[40:43], v0 offset:2048\n"
    "v_mfma_f32_16x16x32_bf16 v[8:11], a[88:91], v[44:47], v[8:11]\n"
    "s_mov_b64 exec, s[60:61]\n"
    "buffer_load_dwordx4 v2, s[92:95], s55 offen offset:2048 sc1 lds\n"
    "s_mov_b64 exec, -1\n"
    "v_mfma_f32_16x16x32_bf16 v[12:15], a[92:95], v[44:47], v[12:15]\n"
    "v_fmac_f32 v25, v65, v74\n"
    "v_mfma_f32_16x16x32_bf16 v[16:19], a[232:235], v[44:47], v[16:19]\n"
    "s_cmp_eq_u32 s94, 0\n"
    "s_cbranch_scc1 .Lhk1_%=\n"
    ".Lhk1_x%=:\n"
    "v_mfma_f32_16x16x32_bf16 v[20:23], a[236:239], v[44:47], v[20:23]\n"
    "v_fmac_f32 v29, v65, v75\n"
    "v_fmac_f32 v30, v66, v75\n"
    "v_mfma_f32_16x16x32_bf16 v[24:27], v[44:47], v[204:207], v[24:27]\n"
    "v_fmac_f32 v9, v49, v73\n"
    "v_fmac_f32 v10, v50, v73\n"
    "v_mfma_f32_16x16x32_bf16 v[28:31], v[44:47], v[208:211], v[28:31]\n"
    "ds_read_b128 v[44:47], v0 offset:3072\n"
    "v_fmac_f32 v12, v52, v73\n"
    "v_mfma_f32_16x16x32_bf16 v[8:11], a[96:99], v[32:35], v[8:11]\n"
    "v_fmac_f32 v16, v56, v73\n"
    "v_fmac_f32 v17, v57, v73\n"
    "v_mfma_f32_16x16x32_bf16 v[12:15], a[100:103], v[32:35], v[12:15]\n"
    "v_fmac_f32 v26, v66, v74\n"
    "v_fmac_f32 v27, v67, v74\n"
    "v_mfma_f32_16x16x32_bf16 v[16:19], a[240:243], v[32:35], v[16:19]\n"
    "v_fmac_f32 v31, v67, v75\n"
    "v_mfma_f32_16x16x32_bf16 v[20:23], a[244:247], v[32:35], v[20:23]\n"
    "v_and_b32 v0, 0xffff9fff, v1\n"
    "v_fmac_f32 v11, v51, v73\n"
    "v_mfma_f32_16x16x32_bf16 v[24:27], v[32:35], v[212:215], v[24:27]\n"
    "s_waitcnt lgkmcnt(2)\n"
    "v_fmac_f32 v13, v53, v73\n"
    "v_fmac_f32 v14, v54, v73\n"
    "v_mfma_f32_16x16x32_bf16 v[28:31], v[32:35], v[216:219], v[28:31]\n"
    "ds_read_b128 v[32:35], v0 offset:4096\n"
    "v_fmac_f32 v15, v55, v73\n"
    "v_mfma_f32_16x16x32_bf16 v[8:11], a[104:107], v[36:39], v[8:11]\n"
    "ds_read_b128 v[64:67], v0 offset:12288\n"
    "v_fmac_f32 v18, v58, v73\n"
    "v_mfma_f32_16x16x32_bf16 v[12:15], a[108:111], v[36:39], v[12:15]\n"
    "s_waitcnt lgkmcnt(2)\n"
    "v_fmac_f32 v19, v59, v73\n"
    "v_fmac_f32 v20, v60, v73\n"
    "v_mfma_f32_16x16x32_bf16 v[8:11], a[112:115], v[40:43], v[8:11]\n"
    "v_fmac_f32 v21, v61, v73\n"
    "v_fmac_f32 v22, v62, v73\n"
    "v_mfma_f32_16x16x32_bf16 v[12:15], a[116:119], v[40:43], v[12:15]\n"
    "v_fmac_f32 v23, v63, v73\n"
    "v_xor_b32 v1, 0x8000, v1\n"
    "v_mfma_f32_16x16x32_bf16 v[8:11], a[120:123], v[44:47], v[8:11]\n"
    "v_xor_b32 v5, 0x8000, v5\n"
    "v_mfma_f32_16x16x32_bf16 v[12:15], a[124:127], v[44:47], v[12:15]\n"
    "s_waitcnt lgkmcnt(1)\n"
    "v_mfma_f32_16x16x32_bf16 v[8:11], a[128:131], v[32:35], v[8:11]\n"
    "v_mfma_f32_16x16x32_bf16 v[12:15], a[132:135], v[32:35], v[12:15]\n"
    "s_waitcnt lgkmcnt(0)\n"
    "v_mfma_f32_16x16x32_bf16 v[8:11], a[136:139], v[64:67], v[8:11]\n"
    "s_cmp_eq_u32 s94, 0\n"
    "s_cbranch_scc1 .Lhk2_%=\n"
    ".Lhk2_x%=:\n"
    "v_mfma_f32_16x16x32_bf16 v[12:15], a[140:143], v[64:67], v[12:15]\n"
    "v_mfma_f32_16x16x32_bf16 v[16:19], a[248:251], v[36:39], v[16:19]\n"
    "global_load_dword v6, v6, %1 sc1\n"
    "v_mfma_f32_16x16x32_bf16 v[20:23], a[252:255], v[36:39], v[20:23]\n"
    "v_mul_f32 v8, v8, v72\n"
    "v_mul_f32 v12, v12, v72\n"
    "v_mfma_f32_16x16x32_bf16 v[16:19], v[84:87], v[40:43], v[16:19]\n"
    "v_cvt_pk_bf16_f32 v8, v8, v12\n"
    "v_mul_f32 v9, v9, v72\n"
    "v_mfma_f32_16x16x32_bf16 v[20:23], v[88:91], v[40:43], v[20:23]\n"
    "v_mul_f32 v13, v13, v72\n"
    "v_cvt_pk_bf16_f32 v9, v9, v13\n"
    "v_mfma_f32_16x16x32_bf16 v[16:19], v[92:95], v[44:47], v[16:19]\n"
    "v_mul_f32 v10, v10, v72\n"
    "v_mul_f32 v14, v14, v72\n"
    "v_mfma_f32_16x16x32_bf16 v[20:23], v[96:99], v[44:47], v[20:23]\n"
    "v_cvt_pk_bf16_f32 v10, v10, v14\n"
    "v_mul_f32 v11, v11, v72\n"
    "v_mfma_f32_16x16x32_bf16 v[16:19], v[100:103], v[32:35], v[16:19]\n"
    "v_mul_f32 v15, v15, v72\n"
    "v_cvt_pk_bf16_f32 v11, v11, v15\n"
    "v_mfma_f32_16x16x32_bf16 v[20:23], v[104:107], v[32:35], v[20:23]\n"
    "buffer_store_dwordx4 v[8:11], v2, s[40:43], s62 offen sc1\n"
    "v_mfma_f32_16x16x32_bf16 v[16:19], v[108:111], v[64:67], v[16:19]\n"
    "s_waitcnt vmcnt(5)\n"
    "ds_read_b128 v[48:51], v1\n"
    "v_mfma_f32_16x16x32_bf16 v[20:23], v[112:115], v[64:67], v[20:23]\n"
    "ds_read_b128 v[52:55], v1 offset:1024\n"
    "v_mfma_f32_16x16x32_bf16 v[24:27], v[36:39], v[220:223], v[24:27]\n"
    "v_mfma_f32_16x16x32_bf16 v[28:31], v[36:39], v[224:227], v[28:31]\n"
    "v_mul_f32 v16, v16, v72\n"
    "v_mul_f32 v20, v20, v72\n"
    "v_mfma_f32_16x16x32_bf16 v[24:27], v[40:43], v[228:231], v[24:27]\n"
    "v_cvt_pk_bf16_f32 v16, v16, v20\n"
    "v_mul_f32 v17, v17, v72\n"
    "v_mfma_f32_16x16x32_bf16 v[28:31], v[40:43], v[232:235], v[28:31]\n"
    "v_mul_f32 v21, v21, v72\n"
    "v_cvt_pk_bf16_f32 v17, v17, v21\n"
    "v_mfma_f32_16x16x32_bf16 v[24:27], v[44:47], v[236:239], v[24:27]\n"
    "v_mul_f32 v18, v18, v72\n"
    "v_mul_f32 v22, v22, v72\n"
    "v_mfma_f32_16x16x32_bf16 v[28:31], v[44:47], v[240:243], v[28:31]\n"
    "v_cvt_pk_bf16_f32 v18, v18, v22\n"
    "v_mul_f32 v19, v19, v72\n"
    "v_mfma_f32_16x16x32_bf16 v[24:27], v[32:35], v[244:247], v[24:27]\n"
    "v_mul_f32 v23, v23, v72\n"
    "v_cvt_pk_bf16_f32 v19, v19, v23\n"
    "v_mfma_f32_16x16x32_bf16 v[28:31], v[32:35], v[248:251], v[28:31]\n"
    "buffer_store_dwordx4 v[16:19], v2, s[40:43], s63 offen sc1\n"
    "s_mov_b32 s56, 1\n"
    "v_mfma_f32_16x16x32_bf16 v[24:27], v[64:67], v[252:255], v[24:27]\n"
    "s_cmp_lg_u32 s94, 0\n"
    "v_mfma_f32_16x16x32_bf16 v[28:31], v[64:67], v[76:79], v[28:31]\n"
    "s_cbranch_scc1 .Lhgo_%=\n"
    "ds_read_b32 v83, v7 offset:44\n"
    "s_nop 2\n"
    "v_mul_f32 v24, v24, v68\n"
    "v_mul_f32 v25, v25, v69\n"
    "v_cvt_pk_bf16_f32 v24, v24, v25\n"
    "v_mul_f32 v26, v26, v70\n"
    "v_mul_f32 v27, v27, v71\n"
    "v_cvt_pk_bf16_f32 v25, v26, v27\n"
    "v_mul_f32 v28, v28, v68\n"
    "v_mul_f32 v29, v29, v69\n"
    "v_cvt_pk_bf16_f32 v26, v28, v29\n"
    "v_mul_f32 v30, v30, v70\n"
    "v_mul_f32 v31, v31, v71\n"
    "v_cvt_pk_bf16_f32 v27, v30, v31\n"
    "buffer_store_dwordx4 v[24:27], v2, s[40:43], s64 offen sc1\n"
    "s_waitcnt vmcnt(9) lgkmcnt(0)\n"
    "v_cmp_eq_u32 vcc, 0, v81\n"
    "s_cmp_eq_u64 vcc, 0\n"
    "s_cbranch_scc1 .Lhld_%=\n"
    "global_load_dword v81, v80, %1 sc1\n"
    "s_waitcnt vmcnt(0)\n"
    "v_mov_b32 v82, 1\n"
    "s_mov_b64 exec, s[56:57]\n"
    "ds_add_rtn_u32 v82, v7, v82 offset:48\n"
    "s_mov_b64 exec, -1\n"
    "s_waitcnt lgkmcnt(0)\n"
    "v_readfirstlane_b32 s50, v82\n"
    "s_cmp_eq_u32 s50, 3\n"
    "s_cselect_b32 s46, 0x20000, 0\n"
    "buffer_store_dword v82, v83, s[44:47], 0 offen sc1\n"
    "s_mov_b32 s56, 0\n"
    "v_cmp_eq_u32 vcc, 0, v81\n"
    "s_cmp_eq_u64 vcc, 0\n"
    "s_cbranch_scc1 .Lhld_%=\n"
    ".Lhchk_%=:\n"
    "v_readfirstlane_b32 s50, v80\n"
    "s_cmp_lt_u32 s50, 200\n"
    "s_cbranch_scc1 .Lhend_%=\n"
    ".Lhwait_%=:\n"
    "global_load_dword v81, v80, %1 sc1\n"
    "s_mov_b32 s52, 0\n"
    ".Lhp3_%=:\n"
    "s_waitcnt vmcnt(0)\n"
    "v_cmp_eq_u32 vcc, 0, v81\n"
    "s_cmp_eq_u64 vcc, 0\n"
    "s_cbranch_scc1 .Lhp3_x%=\n"
    "s_add_u32 s52, s52, 1\n"
    "s_sleep 2\n"
    "global_load_dword v81, v80, %1 sc1\n"
    "s_cmp_lt_u32 s52, s53\n"
    "s_cbranch_scc1 .Lhp3_%=\n"
    "s_mov_b32 s53, 0\n"
    "v_mov_b32 v82, 0\n"
    "v_mov_b32 v83, 1\n"
    "global_store_dword v82, v83, %1 sc1\n"
    "s_waitcnt vmcnt(0)\n"
    ".Lhp3_x%=:\n"
    ".Lhld_%=:\n"
    "s_mov_b32 s94, s42\n"
    "s_mov_b32 s90, s96\n"
    "s_mov_b32 m0, s58\n"
    "s_nop 0\n"
    "buffer_load_dwordx4 v3, s[92:95], s49 offen sc1 lds\n"
    "buffer_load_dwordx4 v3, s[92:95], s49 offen offset:1024 sc1 lds\n"
    "buffer_load_dwordx4 v3, s[92:95], s49 offen offset:2048 sc1 lds\n"
    "buffer_load_dwordx4 v3, s[92:95], s49 offen offset:3072 sc1 lds\n"
    "s_bitset1_b32 m0, 12\n"
    "s_nop 0\n"
    "buffer_load_dwordx4 v2, s[88:91], s49 offen sc1 lds\n"
    "s_mov_b32 m0, s59\n"
    "s_mov_b32 s55, s49\n"
    "s_bitset1_b32 s55, 15\n"
    "s_mov_b64 exec, s[60:61]\n"
    "buffer_load_dwordx4 v2, s[92:95], s55 offen offset:2048 sc1 lds\n"
    "s_mov_b64 exec, -1\n"
    "s_waitcnt vmcnt(6)\n"
    "v_mov_b32 v82, 1\n"
    "s_mov_b64 exec, s[56:57]\n"
    "ds_add_rtn_u32 v82, v7, v82 offset:48\n"
    "s_mov_b64 exec, -1\n"
    "s_waitcnt lgkmcnt(0)\n"
    "v_readfirstlane_b32 s50, v82\n"
    "s_cmp_eq_u32 s50, 3\n"
    "s_cselect_b32 s46, 0x20000, 0\n"
    "buffer_store_dword v82, v83, s[44:47], 0 offen sc1\n"
    "s_mov_b32 s56, 0\n"
    "s_waitcnt vmcnt(5)\n"
    "ds_read_b128 v[48:51], v1\n"
    "ds_read_b128 v[52:55], v1 offset:1024\n"
    "s_waitcnt lgkmcnt(1)\n"
    "v_mfma_f32_16x16x32_bf16 v[8:11], a[0:3], v[48:51], 0\n"
    "s_waitcnt vmcnt(1)\n"
    "ds_read_b128 v[56:59], v1 offset:2048\n"
    "v_mfma_f32_16x16x32_bf16 v[12:15], a[4:7], v[48:51], 0\n"
    "ds_read_b128 v[80:83], v7 offset:32\n"
    "v_add_u32 v7, 32, v7\n"
    "v_mfma_f32_16x16x32_bf16 v[16:19], a[144:147], v[48:51], 0\n"
    "ds_read_b128 v[60:63], v1 offset:3072\n"
    "v_mfma_f32_16x16x32_bf16 v[20:23], a[148:151], v[48:51], 0\n"
    "s_waitcnt lgkmcnt(3)\n"
    "v_mfma_f32_16x16x32_bf16 v[8:11], a[8:11], v[52:55], v[8:11]\n"
    "v_mfma_f32_16x16x32_bf16 v[12:15], a[12:15], v[52:55], v[12:15]\n"
    "v_mfma_f32_16x16x32_bf16 v[16:19], a[152:155], v[52:55], v[16:19]\n"
    "v_mfma_f32_16x16x32_bf16 v[20:23], a[156:159], v[52:55], v[20:23]\n"
    "s_waitcnt lgkmcnt(1)\n"
    "v_mfma_f32_16x16x32_bf16 v[8:11], a[16:19], v[56:59], v[8:11]\n"
    "v_readfirstlane_b32 s49, v81\n"
    "v_mfma_f32_16x16x32_bf16 v[12:15], a[20:23], v[56:59], v[12:15]\n"
    "v_add_u32 v80, v80, v4\n"
    "v_mfma_f32_16x16x32_bf16 v[16:19], a[160:163], v[56:59], v[16:19]\n"
    "v_readfirstlane_b32 s62, v82\n"
    "v_xor_b32 v0, 0x2000, v1\n"
    "v_mfma_f32_16x16x32_bf16 v[20:23], a[164:167], v[56:59], v[20:23]\n"
    "s_branch .Lhtop_%=\n"
    ".Lhk1_%=:\n"
    "s_waitcnt vmcnt(6)\n"
    "v_cmp_eq_u32 vcc, 0, v81\n"
    "s_cmp_eq_u64 vcc, 0\n"
    "s_cselect_b32 s46, 0, 0x20000\n"
    "s_cselect_b32 s94, s42, 0\n"
    "s_cselect_b32 s90, s96, 0\n"
    "buffer_load_dword v81, v80, s[44:47], 0 offen sc1\n"
    "s_mov_b32 m0, s58\n"
    "s_nop 0\n"
    "buffer_load_dwordx4 v3, s[92:95], s49 offen sc1 lds\n"
    "buffer_load_dwordx4 v3, s[92:95], s49 offen offset:1024 sc1 lds\n"
    "buffer_load_dwordx4 v3, s[92:95], s49 offen offset:2048 sc1 lds\n"
    "buffer_load_dwordx4 v3, s[92:95], s49 offen offset:3072 sc1 lds\n"
    "s_bitset1_b32 m0, 12\n"
    "s_nop 0\n"
    "buffer_load_dwordx4 v2, s[88:91], s49 offen sc1 lds\n"
    "s_mov_b32 m0, s59\n"
    "s_mov_b32 s55, s49\n"
    "s_bitset1_b32 s55, 15\n"
    "s_mov_b64 exec, s[60:61]\n"
    "buffer_load_dwordx4 v2, s[92:95], s55 offen offset:2048 sc1 lds\n"
    "s_mov_b64 exec, -1\n"
    "s_branch .Lhk1_x%=\n"
    ".Lhk2_%=:\n"
    "s_waitcnt vmcnt(6)\n"
    "v_cmp_eq_u32 vcc, 0, v81\n"
    "s_cmp_eq_u64 vcc, 0\n"
    "s_cselect_b32 s46, 0, 0x20000\n"
    "s_cselect_b32 s94, s42, 0\n"
    "s_cselect_b32 s90, s96, 0\n"
    "buffer_load_dword v81, v80, s[44:47], 0 offen sc1\n"
    "s_mov_b32 m0, s58\n"
    "s_nop 0\n"
    "buffer_load_dwordx4 v3, s[92:95], s49 offen sc1 lds\n"
    "buffer_load_dwordx4 v3, s[92:95], s49 offen offset:1024 sc1 lds\n"
    "buffer_load_dwordx4 v3, s[92:95], s49 offen offset:2048 sc1 lds\n"
    "buffer_load_dwordx4 v3, s[92:95], s49 offen offset:3072 sc1 lds\n"
    "s_bitset1_b32 m0, 12\n"
    "s_nop 0\n"
    "buffer_load_dwordx4 v2, s[88:91], s49 offen sc1 lds\n"
    "s_mov_b32 m0, s59\n"
    "s_mov_b32 s55, s49\n"
    "s_bitset1_b32 s55, 15\n"
    "s_mov_b64 exec, s[60:61]\n"
    "buffer_load_dwordx4 v2, s[92:95], s55 offen offset:2048 sc1 lds\n"
    "s_mov_b64 exec, -1\n"
    "s_branch .Lhk2_x%=\n"
    ".Lhpol_%=:\n"
    "v_mov_b32 v80, 64\n"
    "global_load_dword v81, v80, %1 sc1\n"
    "s_mov_b32 s52, 0\n"
    ".Lhp4_%=:\n"
    "s_waitcnt vmcnt(0)\n"
    "v_cmp_eq_u32 vcc, 0, v81\n"
    "s_cmp_eq_u64 vcc, 0\n"
    "s_cbranch_scc1 .Lhp4_x%=\n"
    "s_add_u32 s52, s52, 1\n"
    "s_sleep 2\n"
    "global_load_dword v81, v80, %1 sc1\n"
    "s_cmp_lt_u32 s52, s53\n"
    "s_cbranch_scc1 .Lhp4_%=\n"
    "s_mov_b32 s53, 0\n"
    "v_mov_b32 v82, 0\n"
    "v_mov_b32 v83, 1\n"
    "global_store_dword v82, v83, %1 sc1\n"
    "s_waitcnt vmcnt(0)\n"
    ".Lhp4_x%=:\n"
    "v_readfirstlane_b32 s50, v81\n"
    "v_mbcnt_lo_u32_b32 v80, -1, 0\n"
    "v_mbcnt_hi_u32_b32 v80, -1, v80\n"
    "s_lshl_b32 s51, %2, 6\n"
    "v_add_u32 v80, s51, v80\n"
    "v_min_u32 v80, 129, v80\n"
    "v_lshlrev_b32 v80, 5, v80\n"
    "v_add_u32 v80, 0x26c00, v80\n"
    "s_and_b32 s50, s50, 1\n"
    "ds_read_b64 v[82:83], v80\n"
    "s_waitcnt lgkmcnt(0)\n"
    "v_and_b32 v81, s50, v82\n"
    "v_lshlrev_b32 v81, 13, v81\n"
    "v_and_b32 v82, -2, v82\n"
    "v_xor_b32 v82, v81, v82\n"
    "v_and_b32 v81, s50, v83\n"
    "v_lshlrev_b32 v81, 13, v81\n"
    "v_and_b32 v83, -2, v83\n"
    "v_xor_b32 v83, v81, v83\n"
    "ds_write_b64 v80, v[82:83]\n"
    "ds_read_b64 v[82:83], v80 offset:8\n"
    "s_waitcnt lgkmcnt(0)\n"
    "v_and_b32 v81, s50, v82\n"
    "v_lshlrev_b32 v81, 13, v81\n"
    "v_and_b32 v82, -2, v82\n"
    "v_xor_b32 v82, v81, v82\n"
    "v_and_b32 v81, s50, v83\n"
    "v_lshlrev_b32 v81, 13, v81\n"
    "v_and_b32 v83, -2, v83\n"
    "v_xor_b32 v83, v81, v83\n"
    "ds_write_b64 v80, v[82:83] offset:8\n"
    "s_waitcnt lgkmcnt(0)\n"
    "s_barrier\n"
    "s_and_b32 s50, s50, %4\n"
    "s_lshl_b32 s50, s50, 13\n"
    "s_and_b32 s51, %4, -2\n"
    "s_xor_b32 s50, s50, s51\n"
    "ds_read_b32 v81, v7 offset:4\n"
    "v_add_u32 v80, s50, v4\n"
    "s_waitcnt lgkmcnt(0)\n"
    "v_readfirstlane_b32 s49, v81\n"
    "s_branch .Lhwait_%=\n"
    ".Lhend_%=:\n"
    "s_cmp_eq_u32 s50, 192\n"
    "s_cbranch_scc0 .Lhpol_%=\n"
    "s_waitcnt vmcnt(0) lgkmcnt(0)\n"
    "s_endpgm\n"
    ".p2align 6\n"
    ".Lrole2_%=:\n"
    "s_mov_b64 s[40:41], %0\n"
    "s_and_b32 s41, s41, 0xffff\n"
    "s_mov_b32 s42, 0xc000000\n"
    "s_mov_b32 s43, 0x20000\n"
    "s_mov_b64 s[92:93], %0\n"
    "s_and_b32 s93, s93, 0xffff\n"
    "s_mov_b32 s94, 0xc000000\n"
    "s_mov_b32 s95, 0x20000\n"
    "s_mov_b32 s53, 131072\n"
    "s_mov_b64 s[56:57], 0\n"
    "v_mbcnt_lo_u32_b32 v7, -1, 0\n"
    "v_mbcnt_hi_u32_b32 v7, -1, v7\n"
    "s_lshl_b32 s50, %2, 8\n"
    "s_add_u32 s50, s50, 147456\n"
    "v_lshl_add_u32 v7, v7, 2, s50\n"
    "ds_read_b32 v1, v7\n"
    "ds_read_b32 v4, v7 offset:1024\n"
    "ds_read_b32 v2, v7 offset:2048\n"
    "ds_read_b32 v8, v7 offset:3072\n"
    "ds_read_b32 v9, v7 offset:4096\n"
    "ds_read_b32 v10, v7 offset:5120\n"
    "ds_read_b32 v11, v7 offset:6144\n"
    "ds_read_b32 v3, v7 offset:7168\n"
    "s_waitcnt lgkmcnt(0)\n"
    "s_barrier\n"
    "s_mul_i32 s54, %2, 6144\n"
    "v_add_u32 v5, 16384, v4\n"
    "s_lshl_b32 s58, %2, 13\n"
    "s_add_u32 s58, s58, 0\n"
    "s_xor_b32 s50, %2, 0\n"
    "s_mul_i32 s50, s50, 6144\n"
    "v_add_u32 v32, s50, v9\n"
    "v_add_u32 v33, s50, v10\n"
    "v_add_u32 v34, s50, v11\n"
    "s_xor_b32 s50, %2, 1\n"
    "s_mul_i32 s50, s50, 6144\n"
    "v_add_u32 v35, s50, v9\n"
    "v_add_u32 v36, s50, v10\n"
    "v_add_u32 v37, s50, v11\n"
    "s_xor_b32 s50, %2, 2\n"
    "s_mul_i32 s50, s50, 6144\n"
    "v_add_u32 v38, s50, v9\n"
    "v_add_u32 v39, s50, v10\n"
    "v_add_u32 v40, s50, v11\n"
    "s_xor_b32 s50, %2, 3\n"
    "s_mul_i32 s50, s50, 6144\n"
    "v_add_u32 v41, s50, v9\n"
    "v_add_u32 v42, s50, v10\n"
    "v_add_u32 v43, s50, v11\n"
    "s_mov_b64 s[44:45], %8\n"
    "s_and_b32 s45, s45, 0xffff\n"
    "s_mov_b32 s46, 0x480000\n"
    "s_mov_b32 s47, 0x20000\n"
    "s_add_u32 m0, s54, 0\n"
    "s_add_u32 s55, %6, 0\n"
    "buffer_load_dwordx4 v8, s[44:47], s55 offen lds\n"
    "s_add_u32 m0, s54, 1024\n"
    "s_add_u32 s55, %6, 18432\n"
    "buffer_load_dwordx4 v8, s[44:47], s55 offen lds\n"
    "s_add_u32 m0, s54, 2048\n"
    "s_add_u32 s55, %6, 128\n"
    "buffer_load_dwordx4 v8, s[44:47], s55 offen lds\n"
    "s_add_u32 m0, s54, 3072\n"
    "s_add_u32 s55, %6, 18560\n"
    "buffer_load_dwordx4 v8, s[44:47], s55 offen lds\n"
    "s_add_u32 m0, s54, 4096\n"
    "s_add_u32 s55, %6, 256\n"
    "buffer_load_dwordx4 v8, s[44:47], s55 offen lds\n"
    "s_add_u32 m0, s54, 5120\n"
    "s_add_u32 s55, %6, 18688\n"
    "buffer_load_dwordx4 v8, s[44:47], s55 offen lds\n"
    "s_add_u32 m0, s54, 24576\n"
    "s_add_u32 s55, %6, 2304\n"
    "buffer_load_dwordx4 v8, s[44:47], s55 offen lds\n"
    "s_add_u32 m0, s54, 25600\n"
    "s_add_u32 s55, %6, 20736\n"
    "buffer_load_dwordx4 v8, s[44:47], s55 offen lds\n"
    "s_add_u32 m0, s54, 26624\n"
    "s_add_u32 s55, %6, 2432\n"
    "buffer_load_dwordx4 v8, s[44:47], s55 offen lds\n"
    "s_add_u32 m0, s54, 27648\n"
    "s_add_u32 s55, %6, 20864\n"
    "buffer_load_dwordx4 v8, s[44:47], s55 offen lds\n"
    "s_add_u32 m0, s54, 28672\n"
    "s_add_u32 s55, %6, 2560\n"
    "buffer_load_dwordx4 v8, s[44:47], s55 offen lds\n"
    "s_add_u32 m0, s54, 29696\n"
    "s_add_u32 s55, %6, 20992\n"
    "buffer_load_dwordx4 v8, s[44:47], s55 offen lds\n"
    "s_add_u32 m0, s54, 49152\n"
    "s_add_u32 s55, %6, 36864\n"
    "buffer_load_dwordx4 v8, s[44:47], s55 offen lds\n"
    "s_add_u32 m0, s54, 50176\n"
    "s_add_u32 s55, %6, 55296\n"
    "buffer_load_dwordx4 v8, s[44:47], s55 offen lds\n"
    "s_add_u32 m0, s54, 51200\n"
    "s_add_u32 s55, %6, 36992\n"
    "buffer_load_dwordx4 v8, s[44:47], s55 offen lds\n"
    "s_add_u32 m0, s54, 52224\n"
    "s_add_u32 s55, %6, 55424\n"
    "buffer_load_dwordx4 v8, s[44:47], s55 offen lds\n"
    "s_add_u32 m0, s54, 53248\n"
    "s_add_u32 s55, %6, 37120\n"
    "buffer_load_dwordx4 v8, s[44:47], s55 offen lds\n"
    "s_add_u32 m0, s54, 54272\n"
    "s_add_u32 s55, %6, 55552\n"
    "buffer_load_dwordx4 v8, s[44:47], s55 offen lds\n"
    "s_add_u32 m0, s54, 73728\n"
    "s_add_u32 s55, %6, 39168\n"
    "buffer_load_dwordx4 v8, s[44:47], s55 offen lds\n"
    "s_add_u32 m0, s54, 74752\n"
    "s_add_u32 s55, %6, 57600\n"
    "buffer_load_dwordx4 v8, s[44:47], s55 offen lds\n"
    "s_add_u32 m0, s54, 75776\n"
    "s_add_u32 s55, %6, 39296\n"
    "buffer_load_dwordx4 v8, s[44:47], s55 offen lds\n"
    "s_add_u32 m0, s54, 76800\n"
    "s_add_u32 s55, %6, 57728\n"
    "buffer_load_dwordx4 v8, s[44:47], s55 offen lds\n"
    "s_add_u32 m0, s54, 77824\n"
    "s_add_u32 s55, %6, 39424\n"
    "buffer_load_dwordx4 v8, s[44:47], s55 offen lds\n"
    "s_add_u32 m0, s54, 78848\n"
    "s_add_u32 s55, %6, 57856\n"
    "buffer_load_dwordx4 v8, s[44:47], s55 offen lds\n"
    "s_add_u32 m0, s54, 98304\n"
    "s_add_u32 s55, %6, 73728\n"
    "buffer_load_dwordx4 v8, s[44:47], s55 offen lds\n"
    "s_add_u32 m0, s54, 99328\n"
    "s_add_u32 s55, %6, 92160\n"
    "buffer_load_dwordx4 v8, s[44:47], s55 offen lds\n"
    "s_add_u32 m0, s54, 100352\n"
    "s_add_u32 s55, %6, 73856\n"
    "buffer_load_dwordx4 v8, s[44:47], s55 offen lds\n"
    "s_add_u32 m0, s54, 101376\n"
    "s_add_u32 s55, %6, 92288\n"
    "buffer_load_dwordx4 v8, s[44:47], s55 offen lds\n"
    "s_add_u32 m0, s54, 102400\n"
    "s_add_u32 s55, %6, 73984\n"
    "buffer_load_dwordx4 v8, s[44:47], s55 offen lds\n"
    "s_add_u32 m0, s54, 103424\n"
    "s_add_u32 s55, %6, 92416\n"
    "buffer_load_dwordx4 v8, s[44:47], s55 offen lds\n"
    "s_add_u32 m0, s54, 122880\n"
    "s_add_u32 s55, %6, 76032\n"
    "buffer_load_dwordx4 v8, s[44:47], s55 offen lds\n"
    "s_add_u32 m0, s54, 123904\n"
    "s_add_u32 s55, %6, 94464\n"
    "buffer_load_dwordx4 v8, s[44:47], s55 offen lds\n"
    "s_add_u32 m0, s54, 124928\n"
    "s_add_u32 s55, %6, 76160\n"
    "buffer_load_dwordx4 v8, s[44:47], s55 offen lds\n"
    "s_add_u32 m0, s54, 125952\n"
    "s_add_u32 s55, %6, 94592\n"
    "buffer_load_dwordx4 v8, s[44:47], s55 offen lds\n"
    "s_add_u32 m0, s54, 126976\n"
    "s_add_u32 s55, %6, 76288\n"
    "buffer_load_dwordx4 v8, s[44:47], s55 offen lds\n"
    "s_add_u32 m0, s54, 128000\n"
    "s_add_u32 s55, %6, 94720\n"
    "buffer_load_dwordx4 v8, s[44:47], s55 offen lds\n"
    "s_waitcnt vmcnt(30) lgkmcnt(0)\n"
    "s_barrier\n"
    "ds_read_b64_tr_b16 a[0:1], v32\n"
    "ds_read_b64_tr_b16 a[4:5], v33\n"
    "ds_read_b64_tr_b16 a[8:9], v34\n"
    "ds_read_b64_tr_b16 a[96:97], v35\n"
    "ds_read_b64_tr_b16 a[100:101], v36\n"
    "ds_read_b64_tr_b16 a[104:105], v37\n"
    "ds_read_b64_tr_b16 a[192:193], v38\n"
    "ds_read_b64_tr_b16 a[196:197], v39\n"
    "ds_read_b64_tr_b16 a[200:201], v40\n"
    "ds_read_b64_tr_b16 v[160:161], v41\n"
    "ds_read_b64_tr_b16 v[164:165], v42\n"
    "ds_read_b64_tr_b16 v[168:169], v43\n"
    "s_waitcnt vmcnt(24) lgkmcnt(0)\n"
    "s_barrier\n"
    "s_add_u32 m0, s54, 0\n"
    "s_add_u32 s55, %6, 110592\n"
    "buffer_load_dwordx4 v8, s[44:47], s55 offen lds\n"
    "s_add_u32 m0, s54, 1024\n"
    "s_add_u32 s55, %6, 129024\n"
    "buffer_load_dwordx4 v8, s[44:47], s55 offen lds\n"
    "s_add_u32 m0, s54, 2048\n"
    "s_add_u32 s55, %6, 110720\n"
    "buffer_load_dwordx4 v8, s[44:47], s55 offen lds\n"
    "s_add_u32 m0, s54, 3072\n"
    "s_add_u32 s55, %6, 129152\n"
    "buffer_load_dwordx4 v8, s[44:47], s55 offen lds\n"
    "s_add_u32 m0, s54, 4096\n"
    "s_add_u32 s55, %6, 110848\n"
    "buffer_load_dwordx4 v8, s[44:47], s55 offen lds\n"
    "s_add_u32 m0, s54, 5120\n"
    "s_add_u32 s55, %6, 129280\n"
    "buffer_load_dwordx4 v8, s[44:47], s55 offen lds\n"
    "ds_read_b64_tr_b16 a[2:3], v32 offset:24576\n"
    "ds_read_b64_tr_b16 a[6:7], v33 offset:24576\n"
    "ds_read_b64_tr_b16 a[10:11], v34 offset:24576\n"
    "ds_read_b64_tr_b16 a[98:99], v35 offset:24576\n"
    "ds_read_b64_tr_b16 a[102:103], v36 offset:24576\n"
    "ds_read_b64_tr_b16 a[106:107], v37 offset:24576\n"
    "ds_read_b64_tr_b16 a[194:195], v38 offset:24576\n"
    "ds_read_b64_tr_b16 a[198:199], v39 offset:24576\n"
    "ds_read_b64_tr_b16 a[202:203], v40 offset:24576\n"
    "ds_read_b64_tr_b16 v[162:163], v41 offset:24576\n"
    "ds_read_b64_tr_b16 v[166:167], v42 offset:24576\n"
    "ds_read_b64_tr_b16 v[170:171], v43 offset:24576\n"
    "s_waitcnt vmcnt(24) lgkmcnt(0)\n"
    "s_barrier\n"
    "s_add_u32 m0, s54, 24576\n"
    "s_add_u32 s55, %6, 112896\n"
    "buffer_load_dwordx4 v8, s[44:47], s55 offen lds\n"
    "s_add_u32 m0, s54, 25600\n"
    "s_add_u32 s55, %6, 131328\n"
    "buffer_load_dwordx4 v8, s[44:47], s55 offen lds\n"
    "s_add_u32 m0, s54, 26624\n"
    "s_add_u32 s55, %6, 113024\n"
    "buffer_load_dwordx4 v8, s[44:47], s55 offen lds\n"
    "s_add_u32 m0, s54, 27648\n"
    "s_add_u32 s55, %6, 131456\n"
    "buffer_load_dwordx4 v8, s[44:47], s55 offen lds\n"
    "s_add_u32 m0, s54, 28672\n"
    "s_add_u32 s55, %6, 113152\n"
    "buffer_load_dwordx4 v8, s[44:47], s55 offen lds\n"
    "s_add_u32 m0, s54, 29696\n"
    "s_add_u32 s55, %6, 131584\n"
    "buffer_load_dwordx4 v8, s[44:47], s55 offen lds\n"
    "ds_read_b64_tr_b16 a[12:13], v32 offset:49152\n"
    "ds_read_b64_tr_b16 a[16:17], v33 offset:49152\n"
    "ds_read_b64_tr_b16 a[20:21], v34 offset:49152\n"
    "ds_read_b64_tr_b16 a[108:109], v35 offset:49152\n"
    "ds_read_b64_tr_b16 a[112:113], v36 offset:49152\n"
    "ds_read_b64_tr_b16 a[116:117], v37 offset:49152\n"
    "ds_read_b64_tr_b16 a[204:205], v38 offset:49152\n"
    "ds_read_b64_tr_b16 a[208:209], v39 offset:49152\n"
    "ds_read_b64_tr_b16 a[212:213], v40 offset:49152\n"
    "ds_read_b64_tr_b16 v[172:173], v41 offset:49152\n"
    "ds_read_b64_tr_b16 v[176:177], v42 offset:49152\n"
    "ds_read_b64_tr_b16 v[180:181], v43 offset:49152\n"
    "s_waitcnt vmcnt(24) lgkmcnt(0)\n"
    "s_barrier\n"
    "s_add_u32 m0, s54, 49152\n"
    "s_add_u32 s55, %6, 147456\n"
    "buffer_load_dwordx4 v8, s[44:47], s55 offen lds\n"
    "s_add_u32 m0, s54, 50176\n"
    "s_add_u32 s55, %6, 165888\n"
    "buffer_load_dwordx4 v8, s[44:47], s55 offen lds\n"
    "s_add_u32 m0, s54, 51200\n"
    "s_add_u32 s55, %6, 147584\n"
    "buffer_load_dwordx4 v8, s[44:47], s55 offen lds\n"
    "s_add_u32 m0, s54, 52224\n"
    "s_add_u32 s55, %6, 166016\n"
    "buffer_load_dwordx4 v8, s[44:47], s55 offen lds\n"
    "s_add_u32 m0, s54, 53248\n"
    "s_add_u32 s55, %6, 147712\n"
    "buffer_load_dwordx4 v8, s[44:47], s55 offen lds\n"
    "s_add_u32 m0, s54, 54272\n"
    "s_add_u32 s55, %6, 166144\n"
    "buffer_load_dwordx4 v8, s[44:47], s55 offen lds\n"
    "v_add_u32 v71, 0x10000, v32\n"
    "ds_read_b64_tr_b16 a[14:15], v71 offset:8192\n"
    "v_add_u32 v70, 0x10000, v33\n"
    "ds_read_b64_tr_b16 a[18:19], v70 offset:8192\n"
    "v_add_u32 v69, 0x10000, v34\n"
    "ds_read_b64_tr_b16 a[22:23], v69 offset:8192\n"
    "v_add_u32 v68, 0x10000, v35\n"
    "ds_read_b64_tr_b16 a[110:111], v68 offset:8192\n"
    "v_add_u32 v67, 0x10000, v36\n"
    "ds_read_b64_tr_b16 a[114:115], v67 offset:8192\n"
    "v_add_u32 v66, 0x10000, v37\n"
    "ds_read_b64_tr_b16 a[118:119], v66 offset:8192\n"
    "v_add_u32 v65, 0x10000, v38\n"
    "ds_read_b64_tr_b16 a[206:207], v65 offset:8192\n"
    "v_add_u32 v64, 0x10000, v39\n"
    "ds_read_b64_tr_b16 a[210:211], v64 offset:8192\n"
    "v_add_u32 v63, 0x10000, v40\n"
    "ds_read_b64_tr_b16 a[214:215], v63 offset:8192\n"
    "v_add_u32 v62, 0x10000, v41\n"
    "ds_read_b64_tr_b16 v[174:175], v62 offset:8192\n"
    "v_add_u32 v61, 0x10000, v42\n"
    "ds_read_b64_tr_b16 v[178:179], v61 offset:8192\n"
    "v_add_u32 v60, 0x10000, v43\n"
    "ds_read_b64_tr_b16 v[182:183], v60 offset:8192\n"
    "s_waitcnt vmcnt(24) lgkmcnt(0)\n"
    "s_barrier\n"
    "s_add_u32 m0, s54, 73728\n"
    "s_add_u32 s55, %6, 149760\n"
    "buffer_load_dwordx4 v8, s[44:47], s55 offen lds\n"
    "s_add_u32 m0, s54, 74752\n"
    "s_add_u32 s55, %6, 168192\n"
    "buffer_load_dwordx4 v8, s[44:47], s55 offen lds\n"
    "s_add_u32 m0, s54, 75776\n"
    "s_add_u32 s55, %6, 149888\n"
    "buffer_load_dwordx4 v8, s[44:47], s55 offen lds\n"
    "s_add_u32 m0, s54, 76800\n"
    "s_add_u32 s55, %6, 168320\n"
    "buffer_load_dwordx4 v8, s[44:47], s55 offen lds\n"
    "s_add_u32 m0, s54, 77824\n"
    "s_add_u32 s55, %6, 150016\n"
    "buffer_load_dwordx4 v8, s[44:47], s55 offen lds\n"
    "s_add_u32 m0, s54, 78848\n"
    "s_add_u32 s55, %6, 168448\n"
    "buffer_load_dwordx4 v8, s[44:47], s55 offen lds\n"
    "ds_read_b64_tr_b16 a[24:25], v71 offset:32768\n"
    "ds_read_b64_tr_b16 a[28:29], v70 offset:32768\n"
    "ds_read_b64_tr_b16 a[32:33], v69 offset:32768\n"
    "ds_read_b64_tr_b16 a[120:121], v68 offset:32768\n"
    "ds_read_b64_tr_b16 a[124:125], v67 offset:32768\n"
    "ds_read_b64_tr_b16 a[128:129], v66 offset:32768\n"
    "ds_read_b64_tr_b16 a[216:217], v65 offset:32768\n"
    "ds_read_b64_tr_b16 a[220:221], v64 offset:32768\n"
    "ds_read_b64_tr_b16 a[224:225], v63 offset:32768\n"
    "ds_read_b64_tr_b16 v[184:185], v62 offset:32768\n"
    "ds_read_b64_tr_b16 v[188:189], v61 offset:32768\n"
    "ds_read_b64_tr_b16 v[192:193], v60 offset:32768\n"
    "s_waitcnt vmcnt(24) lgkmcnt(0)\n"
    "s_barrier\n"
    "s_add_u32 m0, s54, 98304\n"
    "s_add_u32 s55, %6, 184320\n"
    "buffer_load_dwordx4 v8, s[44:47], s55 offen lds\n"
    "s_add_u32 m0, s54, 99328\n"
    "s_add_u32 s55, %6, 202752\n"
    "buffer_load_dwordx4 v8, s[44:47], s55 offen lds\n"
    "s_add_u32 m0, s54, 100352\n"
    "s_add_u32 s55, %6, 184448\n"
    "buffer_load_dwordx4 v8, s[44:47], s55 offen lds\n"
    "s_add_u32 m0, s54, 101376\n"
    "s_add_u32 s55, %6, 202880\n"
    "buffer_load_dwordx4 v8, s[44:47], s55 offen lds\n"
    "s_add_u32 m0, s54, 102400\n"
    "s_add_u32 s55, %6, 184576\n"
    "buffer_load_dwordx4 v8, s[44:47], s55 offen lds\n"
    "s_add_u32 m0, s54, 103424\n"
    "s_add_u32 s55, %6, 203008\n"
    "buffer_load_dwordx4 v8, s[44:47], s55 offen lds\n"
    "ds_read_b64_tr_b16 a[26:27], v71 offset:57344\n"
    "ds_read_b64_tr_b16 a[30:31], v70 offset:57344\n"
    "ds_read_b64_tr_b16 a[34:35], v69 offset:57344\n"
    "ds_read_b64_tr_b16 a[122:123], v68 offset:57344\n"
    "ds_read_b64_tr_b16 a[126:127], v67 offset:57344\n"
    "ds_read_b64_tr_b16 a[130:131], v66 offset:57344\n"
    "ds_read_b64_tr_b16 a[218:219], v65 offset:57344\n"
    "ds_read_b64_tr_b16 a[222:223], v64 offset:57344\n"
    "ds_read_b64_tr_b16 a[226:227], v63 offset:57344\n"
    "ds_read_b64_tr_b16 v[186:187], v62 offset:57344\n"
    "ds_read_b64_tr_b16 v[190:191], v61 offset:57344\n"
    "ds_read_b64_tr_b16 v[194:195], v60 offset:57344\n"
    "s_waitcnt vmcnt(24) lgkmcnt(0)\n"
    "s_barrier\n"
    "s_add_u32 m0, s54, 122880\n"
    "s_add_u32 s55, %6, 186624\n"
    "buffer_load_dwordx4 v8, s[44:47], s55 offen lds\n"
    "s_add_u32 m0, s54, 123904\n"
    "s_add_u32 s55, %6, 205056\n"
    "buffer_load_dwordx4 v8, s[44:47], s55 offen lds\n"
    "s_add_u32 m0, s54, 124928\n"
    "s_add_u32 s55, %6, 186752\n"
    "buffer_load_dwordx4 v8, s[44:47], s55 offen lds\n"
    "s_add_u32 m0, s54, 125952\n"
    "s_add_u32 s55, %6, 205184\n"
    "buffer_load_dwordx4 v8, s[44:47], s55 offen lds\n"
    "s_add_u32 m0, s54, 126976\n"
    "s_add_u32 s55, %6, 186880\n"
    "buffer_load_dwordx4 v8, s[44:47], s55 offen lds\n"
    "s_add_u32 m0, s54, 128000\n"
    "s_add_u32 s55, %6, 205312\n"
    "buffer_load_dwordx4 v8, s[44:47], s55 offen lds\n"
    "ds_read_b64_tr_b16 a[36:37], v32\n"
    "ds_read_b64_tr_b16 a[40:41], v33\n"
    "ds_read_b64_tr_b16 a[44:45], v34\n"
    "ds_read_b64_tr_b16 a[132:133], v35\n"
    "ds_read_b64_tr_b16 a[136:137], v36\n"
    "ds_read_b64_tr_b16 a[140:141], v37\n"
    "ds_read_b64_tr_b16 a[228:229], v38\n"
    "ds_read_b64_tr_b16 a[232:233], v39\n"
    "ds_read_b64_tr_b16 a[236:237], v40\n"
    "ds_read_b64_tr_b16 v[196:197], v41\n"
    "ds_read_b64_tr_b16 v[200:201], v42\n"
    "ds_read_b64_tr_b16 v[204:205], v43\n"
    "s_waitcnt vmcnt(24) lgkmcnt(0)\n"
    "s_barrier\n"
    "s_add_u32 m0, s54, 0\n"
    "s_add_u32 s55, %6, 221184\n"
    "buffer_load_dwordx4 v8, s[44:47], s55 offen lds\n"
    "s_add_u32 m0, s54, 1024\n"
    "s_add_u32 s55, %6, 239616\n"
    "buffer_load_dwordx4 v8, s[44:47], s55 offen lds\n"
    "s_add_u32 m0, s54, 2048\n"
    "s_add_u32 s55, %6, 221312\n"
    "buffer_load_dwordx4 v8, s[44:47], s55 offen lds\n"
    "s_add_u32 m0, s54, 3072\n"
    "s_add_u32 s55, %6, 239744\n"
    "buffer_load_dwordx4 v8, s[44:47], s55 offen lds\n"
    "s_add_u32 m0, s54, 4096\n"
    "s_add_u32 s55, %6, 221440\n"
    "buffer_load_dwordx4 v8, s[44:47], s55 offen lds\n"
    "s_add_u32 m0, s54, 5120\n"
    "s_add_u32 s55, %6, 239872\n"
    "buffer_load_dwordx4 v8, s[44:47], s55 offen lds\n"
    "ds_read_b64_tr_b16 a[38:39], v32 offset:24576\n"
    "ds_read_b64_tr_b16 a[42:43], v33 offset:24576\n"
    "ds_read_b64_tr_b16 a[46:47], v34 offset:24576\n"
    "ds_read_b64_tr_b16 a[134:135], v35 offset:24576\n"
    "ds_read_b64_tr_b16 a[138:139], v36 offset:24576\n"
    "ds_read_b64_tr_b16 a[142:143], v37 offset:24576\n"
    "ds_read_b64_tr_b16 a[230:231], v38 offset:24576\n"
    "ds_read_b64_tr_b16 a[234:235], v39 offset:24576\n"
    "ds_read_b64_tr_b16 a[238:239], v40 offset:24576\n"
    "ds_read_b64_tr_b16 v[198:199], v41 offset:24576\n"
    "ds_read_b64_tr_b16 v[202:203], v42 offset:24576\n"
    "ds_read_b64_tr_b16 v[206:207], v43 offset:24576\n"
    "s_waitcnt vmcnt(24) lgkmcnt(0)\n"
    "s_barrier\n"
    "s_add_u32 m0, s54, 24576\n"
    "s_add_u32 s55, %6, 223488\n"
    "buffer_load_dwordx4 v8, s[44:47], s55 offen lds\n"
    "s_add_u32 m0, s54, 25600\n"
    "s_add_u32 s55, %6, 241920\n"
    "buffer_load_dwordx4 v8, s[44:47], s55 offen lds\n"
    "s_add_u32 m0, s54, 26624\n"
    "s_add_u32 s55, %6, 223616\n"
    "buffer_load_dwordx4 v8, s[44:47], s55 offen lds\n"
    "s_add_u32 m0, s54, 27648\n"
    "s_add_u32 s55, %6, 242048\n"
    "buffer_load_dwordx4 v8, s[44:47], s55 offen lds\n"
    "s_add_u32 m0, s54, 28672\n"
    "s_add_u32 s55, %6, 223744\n"
    "buffer_load_dwordx4 v8, s[44:47], s55 offen lds\n"
    "s_add_u32 m0, s54, 29696\n"
    "s_add_u32 s55, %6, 242176\n"
    "buffer_load_dwordx4 v8, s[44:47], s55 offen lds\n"
    "ds_read_b64_tr_b16 a[48:49], v32 offset:49152\n"
    "ds_read_b64_tr_b16 a[52:53], v33 offset:49152\n"
    "ds_read_b64_tr_b16 a[56:57], v34 offset:49152\n"
    "ds_read_b64_tr_b16 a[144:145], v35 offset:49152\n"
    "ds_read_b64_tr_b16 a[148:149], v36 offset:49152\n"
    "ds_read_b64_tr_b16 a[152:153], v37 offset:49152\n"
    "ds_read_b64_tr_b16 a[240:241], v38 offset:49152\n"
    "ds_read_b64_tr_b16 a[244:245], v39 offset:49152\n"
    "ds_read_b64_tr_b16 a[248:249], v40 offset:49152\n"
    "ds_read_b64_tr_b16 v[208:209], v41 offset:49152\n"
    "ds_read_b64_tr_b16 v[212:213], v42 offset:49152\n"
    "ds_read_b64_tr_b16 v[216:217], v43 offset:49152\n"
    "s_waitcnt vmcnt(24) lgkmcnt(0)\n"
    "s_barrier\n"
    "s_add_u32 m0, s54, 49152\n"
    "s_add_u32 s55, %6, 258048\n"
    "buffer_load_dwordx4 v8, s[44:47], s55 offen lds\n"
    "s_add_u32 m0, s54, 50176\n"
    "s_add_u32 s55, %6, 276480\n"
    "buffer_load_dwordx4 v8, s[44:47], s55 offen lds\n"
    "s_add_u32 m0, s54, 51200\n"
    "s_add_u32 s55, %6, 258176\n"
    "buffer_load_dwordx4 v8, s[44:47], s55 offen lds\n"
    "s_add_u32 m0, s54, 52224\n"
    "s_add_u32 s55, %6, 276608\n"
    "buffer_load_dwordx4 v8, s[44:47], s55 offen lds\n"
    "s_add_u32 m0, s54, 53248\n"
    "s_add_u32 s55, %6, 258304\n"
    "buffer_load_dwordx4 v8, s[44:47], s55 offen lds\n"
    "s_add_u32 m0, s54, 54272\n"
    "s_add_u32 s55, %6, 276736\n"
    "buffer_load_dwordx4 v8, s[44:47], s55 offen lds\n"
    "ds_read_b64_tr_b16 a[50:51], v71 offset:8192\n"
    "ds_read_b64_tr_b16 a[54:55], v70 offset:8192\n"
    "ds_read_b64_tr_b16 a[58:59], v69 offset:8192\n"
    "ds_read_b64_tr_b16 a[146:147], v68 offset:8192\n"
    "ds_read_b64_tr_b16 a[150:151], v67 offset:8192\n"
    "ds_read_b64_tr_b16 a[154:155], v66 offset:8192\n"
    "ds_read_b64_tr_b16 a[242:243], v65 offset:8192\n"
    "ds_read_b64_tr_b16 a[246:247], v64 offset:8192\n"
    "ds_read_b64_tr_b16 a[250:251], v63 offset:8192\n"
    "ds_read_b64_tr_b16 v[210:211], v62 offset:8192\n"
    "ds_read_b64_tr_b16 v[214:215], v61 offset:8192\n"
    "ds_read_b64_tr_b16 v[218:219], v60 offset:8192\n"
    "s_waitcnt vmcnt(24) lgkmcnt(0)\n"
    "s_barrier\n"
    "s_add_u32 m0, s54, 73728\n"
    "s_add_u32 s55, %6, 260352\n"
    "buffer_load_dwordx4 v8, s[44:47], s55 offen lds\n"
    "s_add_u32 m0, s54, 74752\n"
    "s_add_u32 s55, %6, 278784\n"
    "buffer_load_dwordx4 v8, s[44:47], s55 offen lds\n"
    "s_add_u32 m0, s54, 75776\n"
    "s_add_u32 s55, %6, 260480\n"
    "buffer_load_dwordx4 v8, s[44:47], s55 offen lds\n"
    "s_add_u32 m0, s54, 76800\n"
    "s_add_u32 s55, %6, 278912\n"
    "buffer_load_dwordx4 v8, s[44:47], s55 offen lds\n"
    "s_add_u32 m0, s54, 77824\n"
    "s_add_u32 s55, %6, 260608\n"
    "buffer_load_dwordx4 v8, s[44:47], s55 offen lds\n"
    "s_add_u32 m0, s54, 78848\n"
    "s_add_u32 s55, %6, 279040\n"
    "buffer_load_dwordx4 v8, s[44:47], s55 offen lds\n"
    "v_mov_b32 v7, 0x20400\n"
    "global_load_dwordx2 v[20:21], v7, %1 sc1\n"
    "v_mbcnt_lo_u32_b32 v6, -1, 0\n"
    "v_min_u32 v6, 7, v6\n"
    "v_lshlrev_b32 v6, 7, v6\n"
    "v_add_u32 v6, 0x20000, v6\n"
    "global_load_dwordx2 v[22:23], v6, %1 sc1\n"
    "ds_read_b64_tr_b16 a[60:61], v71 offset:32768\n"
    "ds_read_b64_tr_b16 a[64:65], v70 offset:32768\n"
    "ds_read_b64_tr_b16 a[68:69], v69 offset:32768\n"
    "ds_read_b64_tr_b16 a[156:157], v68 offset:32768\n"
    "ds_read_b64_tr_b16 a[160:161], v67 offset:32768\n"
    "ds_read_b64_tr_b16 a[164:165], v66 offset:32768\n"
    "ds_read_b64_tr_b16 a[252:253], v65 offset:32768\n"
    "ds_read_b64_tr_b16 v[128:129], v64 offset:32768\n"
    "ds_read_b64_tr_b16 v[132:133], v63 offset:32768\n"
    "ds_read_b64_tr_b16 v[220:221], v62 offset:32768\n"
    "ds_read_b64_tr_b16 v[224:225], v61 offset:32768\n"
    "ds_read_b64_tr_b16 v[228:229], v60 offset:32768\n"
    "s_waitcnt vmcnt(26) lgkmcnt(0)\n"
    "s_barrier\n"
    "ds_read_b64_tr_b16 a[62:63], v71 offset:57344\n"
    "ds_read_b64_tr_b16 a[66:67], v70 offset:57344\n"
    "ds_read_b64_tr_b16 a[70:71], v69 offset:57344\n"
    "ds_read_b64_tr_b16 a[158:159], v68 offset:57344\n"
    "ds_read_b64_tr_b16 a[162:163], v67 offset:57344\n"
    "ds_read_b64_tr_b16 a[166:167], v66 offset:57344\n"
    "ds_read_b64_tr_b16 a[254:255], v65 offset:57344\n"
    "ds_read_b64_tr_b16 v[130:131], v64 offset:57344\n"
    "ds_read_b64_tr_b16 v[134:135], v63 offset:57344\n"
    "ds_read_b64_tr_b16 v[222:223], v62 offset:57344\n"
    "ds_read_b64_tr_b16 v[226:227], v61 offset:57344\n"
    "ds_read_b64_tr_b16 v[230:231], v60 offset:57344\n"
    "s_waitcnt vmcnt(20) lgkmcnt(0)\n"
    "s_barrier\n"
    "ds_read_b64_tr_b16 a[72:73], v32\n"
    "ds_read_b64_tr_b16 a[76:77], v33\n"
    "ds_read_b64_tr_b16 a[80:81], v34\n"
    "ds_read_b64_tr_b16 a[168:169], v35\n"
    "ds_read_b64_tr_b16 a[172:173], v36\n"
    "ds_read_b64_tr_b16 a[176:177], v37\n"
    "ds_read_b64_tr_b16 v[136:137], v38\n"
    "ds_read_b64_tr_b16 v[140:141], v39\n"
    "ds_read_b64_tr_b16 v[144:145], v40\n"
    "ds_read_b64_tr_b16 v[232:233], v41\n"
    "ds_read_b64_tr_b16 v[236:237], v42\n"
    "ds_read_b64_tr_b16 v[240:241], v43\n"
    "s_waitcnt vmcnt(14) lgkmcnt(0)\n"
    "s_barrier\n"
    "ds_read_b64_tr_b16 a[74:75], v32 offset:24576\n"
    "ds_read_b64_tr_b16 a[78:79], v33 offset:24576\n"
    "ds_read_b64_tr_b16 a[82:83], v34 offset:24576\n"
    "ds_read_b64_tr_b16 a[170:171], v35 offset:24576\n"
    "ds_read_b64_tr_b16 a[174:175], v36 offset:24576\n"
    "ds_read_b64_tr_b16 a[178:179], v37 offset:24576\n"
    "ds_read_b64_tr_b16 v[138:139], v38 offset:24576\n"
    "ds_read_b64_tr_b16 v[142:143], v39 offset:24576\n"
    "ds_read_b64_tr_b16 v[146:147], v40 offset:24576\n"
    "ds_read_b64_tr_b16 v[234:235], v41 offset:24576\n"
    "ds_read_b64_tr_b16 v[238:239], v42 offset:24576\n"
    "ds_read_b64_tr_b16 v[242:243], v43 offset:24576\n"
    "s_waitcnt vmcnt(8) lgkmcnt(0)\n"
    "s_barrier\n"
    "ds_read_b64_tr_b16 a[84:85], v32 offset:49152\n"
    "ds_read_b64_tr_b16 a[88:89], v33 offset:49152\n"
    "ds_read_b64_tr_b16 a[92:93], v34 offset:49152\n"
    "ds_read_b64_tr_b16 a[180:181], v35 offset:49152\n"
    "ds_read_b64_tr_b16 a[184:185], v36 offset:49152\n"
    "ds_read_b64_tr_b16 a[188:189], v37 offset:49152\n"
    "ds_read_b64_tr_b16 v[148:149], v38 offset:49152\n"
    "ds_read_b64_tr_b16 v[152:153], v39 offset:49152\n"
    "ds_read_b64_tr_b16 v[156:157], v40 offset:49152\n"
    "ds_read_b64_tr_b16 v[244:245], v41 offset:49152\n"
    "ds_read_b64_tr_b16 v[248:249], v42 offset:49152\n"
    "ds_read_b64_tr_b16 v[252:253], v43 offset:49152\n"
    "s_waitcnt vmcnt(2) lgkmcnt(0)\n"
    "s_barrier\n"
    "ds_read_b64_tr_b16 a[86:87], v71 offset:8192\n"
    "ds_read_b64_tr_b16 a[90:91], v70 offset:8192\n"
    "ds_read_b64_tr_b16 a[94:95], v69 offset:8192\n"
    "ds_read_b64_tr_b16 a[182:183], v68 offset:8192\n"
    "ds_read_b64_tr_b16 a[186:187], v67 offset:8192\n"
    "ds_read_b64_tr_b16 a[190:191], v66 offset:8192\n"
    "ds_read_b64_tr_b16 v[150:151], v65 offset:8192\n"
    "ds_read_b64_tr_b16 v[154:155], v64 offset:8192\n"
    "ds_read_b64_tr_b16 v[158:159], v63 offset:8192\n"
    "ds_read_b64_tr_b16 v[246:247], v62 offset:8192\n"
    "ds_read_b64_tr_b16 v[250:251], v61 offset:8192\n"
    "ds_read_b64_tr_b16 v[254:255], v60 offset:8192\n"
    "s_waitcnt lgkmcnt(0)\n"
    "s_barrier\n"
    "s_mov_b64 s[44:45], %1\n"
    "s_and_b32 s45, s45, 0xffff\n"
    "s_mov_b32 s46, 0x0\n"
    "s_mov_b32 s47, 0x20000\n"
    "v_mov_b32 v97, 0\n"
    "s_waitcnt vmcnt(0)\n"
    "v_add_u32 v20, 1, v20\n"
    "v_add_u32 v21, 1, v21\n"
    "s_mov_b32 s52, 1048576\n"
    ".Lwgo_a%=:\n"
    "v_cmp_ne_u64 vcc, v[20:21], v[22:23]\n"
    "s_cbranch_vccz .Lwgo_x%=\n"
    "global_load_dwordx2 v[22:23], v6, %1 sc1\n"
    "s_sub_u32 s52, s52, 1\n"
    "s_waitcnt vmcnt(0)\n"
    "s_cbranch_scc0 .Lwgo_a%=\n"
    ".Lwgo_x%=:\n"
    "v_mbcnt_lo_u32_b32 v20, -1, 0\n"
    "v_mbcnt_hi_u32_b32 v20, -1, v20\n"
    "v_min_u32 v6, 7, v20\n"
    "v_lshlrev_b32 v6, 7, v6\n"
    "v_mov_b32 v7, 0x26c00\n"
    "ds_read_b128 v[20:23], v7\n"
    "s_waitcnt lgkmcnt(0)\n"
    "v_readfirstlane_b32 s49, v21\n"
    "v_add_u32 v20, v20, v6\n"
    "s_branch .Lwchk_%=\n"
    ".Lwgo_%=:\n"
    "s_waitcnt lgkmcnt(3)\n"
    "v_mfma_f32_16x16x32_bf16 v[8:11], a[0:3], v[64:67], 0\n"
    "v_add_u32 v7, 32, v7\n"
    "v_mfma_f32_16x16x32_bf16 v[12:15], a[4:7], v[64:67], 0\n"
    "ds_read_b128 v[20:23], v7\n"
    "s_waitcnt lgkmcnt(3)\n"
    "v_mfma_f32_16x16x32_bf16 v[8:11], a[12:15], v[68:71], v[8:11]\n"
    "v_cvt_pk_bf16_f32 v28, v16, v17\n"
    "v_cvt_pk_bf16_f32 v30, v16, v17\n"
    "v_mfma_f32_16x16x32_bf16 v[12:15], a[16:19], v[68:71], v[12:15]\n"
    "v_cvt_pk_bf16_f32 v29, v18, v19\n"
    "v_cvt_pk_bf16_f32 v31, v18, v19\n"
    "s_waitcnt lgkmcnt(2)\n"
    "v_mfma_f32_16x16x32_bf16 v[8:11], a[24:27], v[72:75], v[8:11]\n"
    "s_waitcnt vmcnt(4)\n"
    "ds_read_b128 v[80:83], v1 offset:4096\n"
    "v_permlane16_swap_b32 v28, v30\n"
    "v_mfma_f32_16x16x32_bf16 v[12:15], a[28:31], v[72:75], v[12:15]\n"
    "ds_read_b128 v[84:87], v1 offset:5120\n"
    "v_permlane16_swap_b32 v29, v31\n"
    "s_waitcnt lgkmcnt(3)\n"
    "v_mfma_f32_16x16x32_bf16 v[8:11], a[36:39], v[76:79], v[8:11]\n"
    "buffer_store_dwordx4 v[28:31], v3, s[40:43], s62 offen sc1\n"
    "v_mfma_f32_16x16x32_bf16 v[12:15], a[40:43], v[76:79], v[12:15]\n"
    "s_waitcnt vmcnt(3)\n"
    "ds_read_b128 v[88:91], v1 offset:6144\n"
    "s_waitcnt lgkmcnt(3)\n"
    "v_readfirstlane_b32 s49, v21\n"
    "v_mfma_f32_16x16x32_bf16 v[16:19], a[8:11], v[64:67], 0\n"
    "ds_read_b128 v[92:95], v1 offset:7168\n"
    "v_readfirstlane_b32 s62, v22\n"
    "v_mfma_f32_16x16x32_bf16 v[16:19], a[20:23], v[68:71], v[16:19]\n"
    "v_add_u32 v20, v20, v6\n"
    ".Lwtop_%=:\n"
    "v_mfma_f32_16x16x32_bf16 v[16:19], a[32:35], v[72:75], v[16:19]\n"
    "v_xor_b32 v0, 0x2000, v1\n"
    "v_mfma_f32_16x16x32_bf16 v[16:19], a[44:47], v[76:79], v[16:19]\n"
    "s_xor_b32 s58, s58, 0x8000\n"
    "s_waitcnt lgkmcnt(3)\n"
    "v_mfma_f32_16x16x32_bf16 v[8:11], a[48:51], v[80:83], v[8:11]\n"
    "s_barrier\n"
    "ds_read_b128 v[32:35], v0\n"
    "v_mfma_f32_16x16x32_bf16 v[12:15], a[52:55], v[80:83], v[12:15]\n"
    "ds_read_b128 v[36:39], v0 offset:1024\n"
    "v_mfma_f32_16x16x32_bf16 v[16:19], a[56:59], v[80:83], v[16:19]\n"
    "ds_read_b128 v[40:43], v0 offset:2048\n"
    "s_waitcnt lgkmcnt(5)\n"
    "v_mfma_f32_16x16x32_bf16 v[8:11], a[60:63], v[84:87], v[8:11]\n"
    "ds_read_b128 v[44:47], v0 offset:3072\n"
    "v_mfma_f32_16x16x32_bf16 v[12:15], a[64:67], v[84:87], v[12:15]\n"
    "ds_read_b128 v[48:51], v0 offset:4096\n"
    "v_mfma_f32_16x16x32_bf16 v[16:19], a[68:71], v[84:87], v[16:19]\n"
    "ds_read_b128 v[52:55], v0 offset:5120\n"
    "s_waitcnt lgkmcnt(7)\n"
    "v_mfma_f32_16x16x32_bf16 v[8:11], a[72:75], v[88:91], v[8:11]\n"
    "ds_read_b128 v[56:59], v0 offset:6144\n"
    "v_mfma_f32_16x16x32_bf16 v[12:15], a[76:79], v[88:91], v[12:15]\n"
    "ds_read_b128 v[60:63], v0 offset:7168\n"
    "v_mfma_f32_16x16x32_bf16 v[16:19], a[80:83], v[88:91], v[16:19]\n"
    "s_waitcnt vmcnt(2)\n"
    "v_cmp_eq_u32 vcc, 0, v97\n"
    "s_waitcnt lgkmcnt(8)\n"
    "v_mfma_f32_16x16x32_bf16 v[8:11], a[84:87], v[92:95], v[8:11]\n"
    "s_cmp_eq_u64 vcc, 0\n"
    "s_cselect_b32 s46, 0, 0x20000\n"
    "s_cselect_b32 s94, s42, 0\n"
    "v_mfma_f32_16x16x32_bf16 v[12:15], a[88:91], v[92:95], v[12:15]\n"
    "buffer_load_dword v96, v20, s[44:47], 0 offen sc1\n"
    "v_mfma_f32_16x16x32_bf16 v[16:19], a[92:95], v[92:95], v[16:19]\n"
    "s_mov_b32 m0, s58\n"
    "s_waitcnt lgkmcnt(7)\n"
    "v_mfma_f32_16x16x32_bf16 v[8:11], a[96:99], v[32:35], v[8:11]\n"
    "buffer_load_dwordx4 v4, s[92:95], s49 offen sc1 lds\n"
    "v_mfma_f32_16x16x32_bf16 v[12:15], a[100:103], v[32:35], v[12:15]\n"
    "ds_read_b32 v97, v7 offset:32\n"
    "v_xor_b32 v0, 0x4000, v1\n"
    "v_mfma_f32_16x16x32_bf16 v[16:19], a[104:107], v[32:35], v[16:19]\n"
    "ds_read_b128 v[32:35], v0\n"
    "s_waitcnt lgkmcnt(8)\n"
    "v_mfma_f32_16x16x32_bf16 v[8:11], a[108:111], v[36:39], v[8:11]\n"
    "v_mfma_f32_16x16x32_bf16 v[12:15], a[112:115], v[36:39], v[12:15]\n"
    "buffer_load_dwordx4 v4, s[92:95], s49 offen offset:1024 sc1 lds\n"
    "v_mfma_f32_16x16x32_bf16 v[16:19], a[116:119], v[36:39], v[16:19]\n"
    "ds_read_b128 v[36:39], v0 offset:1024\n"
    "s_waitcnt lgkmcnt(8)\n"
    "v_mfma_f32_16x16x32_bf16 v[8:11], a[120:123], v[40:43], v[8:11]\n"
    "s_waitcnt lgkmcnt(2)\n"
    "v_add_u32 v97, v97, v6\n"
    "v_mfma_f32_16x16x32_bf16 v[12:15], a[124:127], v[40:43], v[12:15]\n"
    "v_mfma_f32_16x16x32_bf16 v[16:19], a[128:131], v[40:43], v[16:19]\n"
    "ds_read_b128 v[40:43], v0 offset:2048\n"
    "v_mfma_f32_16x16x32_bf16 v[8:11], a[132:135], v[44:47], v[8:11]\n"
    "buffer_load_dwordx4 v4, s[92:95], s49 offen offset:2048 sc1 lds\n"
    "v_mfma_f32_16x16x32_bf16 v[12:15], a[136:139], v[44:47], v[12:15]\n"
    "v_mfma_f32_16x16x32_bf16 v[16:19], a[140:143], v[44:47], v[16:19]\n"
    "ds_read_b128 v[44:47], v0 offset:3072\n"
    "v_mfma_f32_16x16x32_bf16 v[8:11], a[144:147], v[48:51], v[8:11]\n"
    "v_mfma_f32_16x16x32_bf16 v[12:15], a[148:151], v[48:51], v[12:15]\n"
    "buffer_load_dwordx4 v4, s[92:95], s49 offen offset:3072 sc1 lds\n"
    "v_mfma_f32_16x16x32_bf16 v[16:19], a[152:155], v[48:51], v[16:19]\n"
    "ds_read_b128 v[48:51], v0 offset:4096\n"
    "v_mfma_f32_16x16x32_bf16 v[8:11], a[156:159], v[52:55], v[8:11]\n"
    "v_mfma_f32_16x16x32_bf16 v[12:15], a[160:163], v[52:55], v[12:15]\n"
    "v_mov_b32 v22, 1\n"
    "v_mfma_f32_16x16x32_bf16 v[16:19], a[164:167], v[52:55], v[16:19]\n"
    "ds_read_b128 v[52:55], v0 offset:5120\n"
    "s_waitcnt vmcnt(5)\n"
    "v_mfma_f32_16x16x32_bf16 v[8:11], a[168:171], v[56:59], v[8:11]\n"
    "s_mov_b64 exec, s[56:57]\n"
    "ds_add_rtn_u32 v22, v7, v22 offset:16\n"
    "s_mov_b64 exec, -1\n"
    "v_mfma_f32_16x16x32_bf16 v[12:15], a[172:175], v[56:59], v[12:15]\n"
    "v_mfma_f32_16x16x32_bf16 v[16:19], a[176:179], v[56:59], v[16:19]\n"
    "ds_read_b128 v[56:59], v0 offset:6144\n"
    "s_bitset1_b32 m0, 12\n"
    "v_mfma_f32_16x16x32_bf16 v[8:11], a[180:183], v[60:63], v[8:11]\n"
    "buffer_load_dwordx4 v5, s[92:95], s49 offen sc1 lds\n"
    "v_mfma_f32_16x16x32_bf16 v[12:15], a[184:187], v[60:63], v[12:15]\n"
    "v_mfma_f32_16x16x32_bf16 v[16:19], a[188:191], v[60:63], v[16:19]\n"
    "ds_read_b128 v[60:63], v0 offset:7168\n"
    "s_waitcnt lgkmcnt(7)\n"
    "v_mfma_f32_16x16x32_bf16 v[8:11], a[192:195], v[32:35], v[8:11]\n"
    "s_waitcnt lgkmcnt(2)\n"
    "v_readfirstlane_b32 s50, v22\n"
    "v_mfma_f32_16x16x32_bf16 v[12:15], a[196:199], v[32:35], v[12:15]\n"
    "s_cmp_eq_u32 s50, 3\n"
    "s_cselect_b32 s46, 0x20000, 0\n"
    "v_xor_b32 v0, 0x6000, v1\n"
    "v_mfma_f32_16x16x32_bf16 v[16:19], a[200:203], v[32:35], v[16:19]\n"
    "ds_read_b128 v[32:35], v0\n"
    "v_mfma_f32_16x16x32_bf16 v[8:11], a[204:207], v[36:39], v[8:11]\n"
    "buffer_store_dword v22, v23, s[44:47], 0 offen sc1\n"
    "v_mfma_f32_16x16x32_bf16 v[12:15], a[208:211], v[36:39], v[12:15]\n"
    "buffer_load_dwordx4 v5, s[92:95], s49 offen offset:1024 sc1 lds\n"
    "v_mfma_f32_16x16x32_bf16 v[16:19], a[212:215], v[36:39], v[16:19]\n"
    "ds_read_b128 v[36:39], v0 offset:1024\n"
    "v_mfma_f32_16x16x32_bf16 v[8:11], a[216:219], v[40:43], v[8:11]\n"
    "v_mfma_f32_16x16x32_bf16 v[12:15], a[220:223], v[40:43], v[12:15]\n"
    "v_mfma_f32_16x16x32_bf16 v[16:19], a[224:227], v[40:43], v[16:19]\n"
    "ds_read_b128 v[40:43], v0 offset:2048\n"
    "v_mfma_f32_16x16x32_bf16 v[8:11], a[228:231], v[44:47], v[8:11]\n"
    "buffer_load_dwordx4 v5, s[92:95], s49 offen offset:2048 sc1 lds\n"
    "v_mfma_f32_16x16x32_bf16 v[12:15], a[232:235], v[44:47], v[12:15]\n"
    "v_mfma_f32_16x16x32_bf16 v[16:19], a[236:239], v[44:47], v[16:19]\n"
    "ds_read_b128 v[44:47], v0 offset:3072\n"
    "v_mfma_f32_16x16x32_bf16 v[8:11], a[240:243], v[48:51], v[8:11]\n"
    "v_mfma_f32_16x16x32_bf16 v[12:15], a[244:247], v[48:51], v[12:15]\n"
    "buffer_load_dwordx4 v5, s[92:95], s49 offen offset:3072 sc1 lds\n"
    "v_mfma_f32_16x16x32_bf16 v[16:19], a[248:251], v[48:51], v[16:19]\n"
    "ds_read_b128 v[48:51], v0 offset:4096\n"
    "v_mfma_f32_16x16x32_bf16 v[8:11], a[252:255], v[52:55], v[8:11]\n"
    "s_cmp_eq_u32 s94, 0\n"
    "s_cbranch_scc1 .Lwk1_%=\n"
    ".Lwk1_x%=:\n"
    "v_mfma_f32_16x16x32_bf16 v[12:15], v[128:131], v[52:55], v[12:15]\n"
    "v_mfma_f32_16x16x32_bf16 v[16:19], v[132:135], v[52:55], v[16:19]\n"
    "ds_read_b128 v[52:55], v0 offset:5120\n"
    "s_waitcnt lgkmcnt(7)\n"
    "v_mfma_f32_16x16x32_bf16 v[8:11], v[136:139], v[56:59], v[8:11]\n"
    "v_mfma_f32_16x16x32_bf16 v[12:15], v[140:143], v[56:59], v[12:15]\n"
    "v_mfma_f32_16x16x32_bf16 v[16:19], v[144:147], v[56:59], v[16:19]\n"
    "ds_read_b128 v[56:59], v0 offset:6144\n"
    "s_waitcnt lgkmcnt(7)\n"
    "v_mfma_f32_16x16x32_bf16 v[8:11], v[148:151], v[60:63], v[8:11]\n"
    "v_mfma_f32_16x16x32_bf16 v[12:15], v[152:155], v[60:63], v[12:15]\n"
    "v_mfma_f32_16x16x32_bf16 v[16:19], v[156:159], v[60:63], v[16:19]\n"
    "ds_read_b128 v[60:63], v0 offset:7168\n"
    "s_waitcnt lgkmcnt(7)\n"
    "v_mfma_f32_16x16x32_bf16 v[8:11], v[160:163], v[32:35], v[8:11]\n"
    "v_mfma_f32_16x16x32_bf16 v[12:15], v[164:167], v[32:35], v[12:15]\n"
    "v_mfma_f32_16x16x32_bf16 v[16:19], v[168:171], v[32:35], v[16:19]\n"
    "s_waitcnt lgkmcnt(6)\n"
    "v_mfma_f32_16x16x32_bf16 v[8:11], v[172:175], v[36:39], v[8:11]\n"
    "v_mfma_f32_16x16x32_bf16 v[12:15], v[176:179], v[36:39], v[12:15]\n"
    "v_mfma_f32_16x16x32_bf16 v[16:19], v[180:183], v[36:39], v[16:19]\n"
    "s_waitcnt lgkmcnt(5)\n"
    "v_mfma_f32_16x16x32_bf16 v[8:11], v[184:187], v[40:43], v[8:11]\n"
    "s_cmp_eq_u32 s94, 0\n"
    "s_cbranch_scc1 .Lwk2_%=\n"
    ".Lwk2_x%=:\n"
    "v_mfma_f32_16x16x32_bf16 v[12:15], v[188:191], v[40:43], v[12:15]\n"
    "v_mfma_f32_16x16x32_bf16 v[16:19], v[192:195], v[40:43], v[16:19]\n"
    "s_waitcnt lgkmcnt(4)\n"
    "v_mfma_f32_16x16x32_bf16 v[8:11], v[196:199], v[44:47], v[8:11]\n"
    "v_xor_b32 v1, 0x8000, v1\n"
    "v_mfma_f32_16x16x32_bf16 v[12:15], v[200:203], v[44:47], v[12:15]\n"
    "global_load_dword v97, v97, %1 sc1\n"
    "s_waitcnt lgkmcnt(3)\n"
    "v_mfma_f32_16x16x32_bf16 v[8:11], v[208:211], v[48:51], v[8:11]\n"
    "v_mfma_f32_16x16x32_bf16 v[12:15], v[212:215], v[48:51], v[12:15]\n"
    "s_waitcnt lgkmcnt(2)\n"
    "v_mfma_f32_16x16x32_bf16 v[8:11], v[220:223], v[52:55], v[8:11]\n"
    "v_mfma_f32_16x16x32_bf16 v[12:15], v[224:227], v[52:55], v[12:15]\n"
    "s_waitcnt lgkmcnt(1)\n"
    "v_mfma_f32_16x16x32_bf16 v[8:11], v[232:235], v[56:59], v[8:11]\n"
    "s_waitcnt vmcnt(5)\n"
    "ds_read_b128 v[64:67], v1\n"
    "v_mfma_f32_16x16x32_bf16 v[12:15], v[236:239], v[56:59], v[12:15]\n"
    "ds_read_b128 v[68:71], v1 offset:1024\n"
    "s_waitcnt lgkmcnt(2)\n"
    "v_mfma_f32_16x16x32_bf16 v[8:11], v[244:247], v[60:63], v[8:11]\n"
    "ds_read_b128 v[72:75], v1 offset:2048\n"
    "v_mfma_f32_16x16x32_bf16 v[12:15], v[248:251], v[60:63], v[12:15]\n"
    "ds_read_b128 v[76:79], v1 offset:3072\n"
    "v_mfma_f32_16x16x32_bf16 v[16:19], v[204:207], v[44:47], v[16:19]\n"
    "v_mfma_f32_16x16x32_bf16 v[16:19], v[216:219], v[48:51], v[16:19]\n"
    "s_mov_b32 s56, 1\n"
    "v_cvt_pk_bf16_f32 v24, v8, v9\n"
    "v_cvt_pk_bf16_f32 v25, v10, v11\n"
    "v_mfma_f32_16x16x32_bf16 v[16:19], v[228:231], v[52:55], v[16:19]\n"
    "s_cmp_lg_u32 s94, 0\n"
    "v_cvt_pk_bf16_f32 v26, v12, v13\n"
    "v_cvt_pk_bf16_f32 v27, v14, v15\n"
    "v_mfma_f32_16x16x32_bf16 v[16:19], v[240:243], v[56:59], v[16:19]\n"
    "buffer_store_dwordx4 v[24:27], v2, s[40:43], s62 offen sc1\n"
    "v_mfma_f32_16x16x32_bf16 v[16:19], v[252:255], v[60:63], v[16:19]\n"
    "s_cbranch_scc1 .Lwgo_%=\n"
    "ds_read_b32 v23, v7 offset:44\n"
    "s_nop 6\n"
    "v_cvt_pk_bf16_f32 v28, v16, v17\n"
    "v_cvt_pk_bf16_f32 v30, v16, v17\n"
    "v_cvt_pk_bf16_f32 v29, v18, v19\n"
    "v_cvt_pk_bf16_f32 v31, v18, v19\n"
    "v_permlane16_swap_b32 v28, v30\n"
    "s_nop 0\n"
    "v_permlane16_swap_b32 v29, v31\n"
    "buffer_store_dwordx4 v[28:31], v3, s[40:43], s62 offen sc1\n"
    "s_waitcnt vmcnt(10) lgkmcnt(0)\n"
    "v_cmp_eq_u32 vcc, 0, v96\n"
    "s_cmp_eq_u64 vcc, 0\n"
    "s_cbranch_scc1 .Lwld_%=\n"
    "global_load_dword v96, v20, %1 sc1\n"
    "s_waitcnt vmcnt(0)\n"
    "v_mov_b32 v22, 1\n"
    "s_mov_b64 exec, s[56:57]\n"
    "ds_add_rtn_u32 v22, v7, v22 offset:48\n"
    "s_mov_b64 exec, -1\n"
    "s_waitcnt lgkmcnt(0)\n"
    "v_readfirstlane_b32 s50, v22\n"
    "s_cmp_eq_u32 s50, 3\n"
    "s_cselect_b32 s46, 0x20000, 0\n"
    "buffer_store_dword v22, v23, s[44:47], 0 offen sc1\n"
    "s_mov_b32 s56, 0\n"
    "v_cmp_eq_u32 vcc, 0, v96\n"
    "s_cmp_eq_u64 vcc, 0\n"
    "s_cbranch_scc1 .Lwld_%=\n"
    ".Lwchk_%=:\n"
    "v_readfirstlane_b32 s50, v20\n"
    "s_cmp_lt_u32 s50, 200\n"
    "s_cbranch_scc1 .Lwend_%=\n"
    ".Lwwait_%=:\n"
    "global_load_dword v96, v20, %1 sc1\n"
    "s_mov_b32 s52, 0\n"
    ".Lwp3_%=:\n"
    "s_waitcnt vmcnt(0)\n"
    "v_cmp_eq_u32 vcc, 0, v96\n"
    "s_cmp_eq_u64 vcc, 0\n"
    "s_cbranch_scc1 .Lwp3_x%=\n"
    "s_add_u32 s52, s52, 1\n"
    "s_sleep 2\n"
    "global_load_dword v96, v20, %1 sc1\n"
    "s_cmp_lt_u32 s52, s53\n"
    "s_cbranch_scc1 .Lwp3_%=\n"
    "s_mov_b32 s53, 0\n"
    "v_mov_b32 v22, 0\n"
    "v_mov_b32 v23, 1\n"
    "global_store_dword v22, v23, %1 sc1\n"
    "s_waitcnt vmcnt(0)\n"
    ".Lwp3_x%=:\n"
    ".Lwld_%=:\n"
    "s_mov_b32 s94, s42\n"
    "s_mov_b32 m0, s58\n"
    "s_nop 0\n"
    "buffer_load_dwordx4 v4, s[92:95], s49 offen sc1 lds\n"
    "buffer_load_dwordx4 v4, s[92:95], s49 offen offset:1024 sc1 lds\n"
    "buffer_load_dwordx4 v4, s[92:95], s49 offen offset:2048 sc1 lds\n"
    "buffer_load_dwordx4 v4, s[92:95], s49 offen offset:3072 sc1 lds\n"
    "s_bitset1_b32 m0, 12\n"
    "s_nop 0\n"
    "buffer_load_dwordx4 v5, s[92:95], s49 offen sc1 lds\n"
    "buffer_load_dwordx4 v5, s[92:95], s49 offen offset:1024 sc1 lds\n"
    "buffer_load_dwordx4 v5, s[92:95], s49 offen offset:2048 sc1 lds\n"
    "buffer_load_dwordx4 v5, s[92:95], s49 offen offset:3072 sc1 lds\n"
    "s_waitcnt vmcnt(8)\n"
    "v_mov_b32 v22, 1\n"
    "s_mov_b64 exec, s[56:57]\n"
    "ds_add_rtn_u32 v22, v7, v22 offset:48\n"
    "s_mov_b64 exec, -1\n"
    "s_waitcnt lgkmcnt(0)\n"
    "v_readfirstlane_b32 s50, v22\n"
    "s_cmp_eq_u32 s50, 3\n"
    "s_cselect_b32 s46, 0x20000, 0\n"
    "buffer_store_dword v22, v23, s[44:47], 0 offen sc1\n"
    "s_mov_b32 s56, 0\n"
    "s_waitcnt vmcnt(5)\n"
    "ds_read_b128 v[64:67], v1\n"
    "ds_read_b128 v[68:71], v1 offset:1024\n"
    "ds_read_b128 v[72:75], v1 offset:2048\n"
    "ds_read_b128 v[76:79], v1 offset:3072\n"
    "s_waitcnt lgkmcnt(3)\n"
    "v_mfma_f32_16x16x32_bf16 v[8:11], a[0:3], v[64:67], 0\n"
    "v_add_u32 v7, 32, v7\n"
    "v_mfma_f32_16x16x32_bf16 v[12:15], a[4:7], v[64:67], 0\n"
    "ds_read_b128 v[20:23], v7\n"
    "s_waitcnt lgkmcnt(3)\n"
    "v_mfma_f32_16x16x32_bf16 v[8:11], a[12:15], v[68:71], v[8:11]\n"
    "v_mfma_f32_16x16x32_bf16 v[12:15], a[16:19], v[68:71], v[12:15]\n"
    "s_waitcnt lgkmcnt(2)\n"
    "v_mfma_f32_16x16x32_bf16 v[8:11], a[24:27], v[72:75], v[8:11]\n"
    "s_waitcnt vmcnt(1)\n"
    "ds_read_b128 v[80:83], v1 offset:4096\n"
    "v_mfma_f32_16x16x32_bf16 v[12:15], a[28:31], v[72:75], v[12:15]\n"
    "ds_read_b128 v[84:87], v1 offset:5120\n"
    "s_waitcnt lgkmcnt(3)\n"
    "v_mfma_f32_16x16x32_bf16 v[8:11], a[36:39], v[76:79], v[8:11]\n"
    "v_mfma_f32_16x16x32_bf16 v[12:15], a[40:43], v[76:79], v[12:15]\n"
    "ds_read_b128 v[88:91], v1 offset:6144\n"
    "s_waitcnt lgkmcnt(3)\n"
    "v_readfirstlane_b32 s49, v21\n"
    "v_mfma_f32_16x16x32_bf16 v[16:19], a[8:11], v[64:67], 0\n"
    "ds_read_b128 v[92:95], v1 offset:7168\n"
    "v_readfirstlane_b32 s62, v22\n"
    "v_mfma_f32_16x16x32_bf16 v[16:19], a[20:23], v[68:71], v[16:19]\n"
    "v_add_u32 v20, v20, v6\n"
    "s_branch .Lwtop_%=\n"
    ".Lwk1_%=:\n"
    "s_waitcnt vmcnt(8)\n"
    "v_cmp_eq_u32 vcc, 0, v96\n"
    "s_cmp_eq_u64 vcc, 0\n"
    "s_cselect_b32 s46, 0, 0x20000\n"
    "s_cselect_b32 s94, s42, 0\n"
    "buffer_load_dword v96, v20, s[44:47], 0 offen sc1\n"
    "s_mov_b32 m0, s58\n"
    "s_nop 0\n"
    "buffer_load_dwordx4 v4, s[92:95], s49 offen sc1 lds\n"
    "buffer_load_dwordx4 v4, s[92:95], s49 offen offset:1024 sc1 lds\n"
    "buffer_load_dwordx4 v4, s[92:95], s49 offen offset:2048 sc1 lds\n"
    "buffer_load_dwordx4 v4, s[92:95], s49 offen offset:3072 sc1 lds\n"
    "s_bitset1_b32 m0, 12\n"
    "s_nop 0\n"
    "buffer_load_dwordx4 v5, s[92:95], s49 offen sc1 lds\n"
    "buffer_load_dwordx4 v5, s[92:95], s49 offen offset:1024 sc1 lds\n"
    "buffer_load_dwordx4 v5, s[92:95], s49 offen offset:2048 sc1 lds\n"
    "buffer_load_dwordx4 v5, s[92:95], s49 offen offset:3072 sc1 lds\n"
    "s_branch .Lwk1_x%=\n"
    ".Lwk2_%=:\n"
    "s_waitcnt vmcnt(8)\n"
    "v_cmp_eq_u32 vcc, 0, v96\n"
    "s_cmp_eq_u64 vcc, 0\n"
    "s_cselect_b32 s46, 0, 0x20000\n"
    "s_cselect_b32 s94, s42, 0\n"
    "buffer_load_dword v96, v20, s[44:47], 0 offen sc1\n"
    "s_mov_b32 m0, s58\n"
    "s_nop 0\n"
    "buffer_load_dwordx4 v4, s[92:95], s49 offen sc1 lds\n"
    "buffer_load_dwordx4 v4, s[92:95], s49 offen offset:1024 sc1 lds\n"
    "buffer_load_dwordx4 v4, s[92:95], s49 offen offset:2048 sc1 lds\n"
    "buffer_load_dwordx4 v4, s[92:95], s49 offen offset:3072 sc1 lds\n"
    "s_bitset1_b32 m0, 12\n"
    "s_nop 0\n"
    "buffer_load_dwordx4 v5, s[92:95], s49 offen sc1 lds\n"
    "buffer_load_dwordx4 v5, s[92:95], s49 offen offset:1024 sc1 lds\n"
    "buffer_load_dwordx4 v5, s[92:95], s49 offen offset:2048 sc1 lds\n"
    "buffer_load_dwordx4 v5, s[92:95], s49 offen offset:3072 sc1 lds\n"
    "s_branch .Lwk2_x%=\n"
    ".Lwpol_%=:\n"
    "v_mov_b32 v20, 64\n"
    "global_load_dword v96, v20, %1 sc1\n"
    "s_mov_b32 s52, 0\n"
    ".Lwp4_%=:\n"
    "s_waitcnt vmcnt(0)\n"
    "v_cmp_eq_u32 vcc, 0, v96\n"
    "s_cmp_eq_u64 vcc, 0\n"
    "s_cbranch_scc1 .Lwp4_x%=\n"
    "s_add_u32 s52, s52, 1\n"
    "s_sleep 2\n"
    "global_load_dword v96, v20, %1 sc1\n"
    "s_cmp_lt_u32 s52, s53\n"
    "s_cbranch_scc1 .Lwp4_%=\n"
    "s_mov_b32 s53, 0\n"
    "v_mov_b32 v22, 0\n"
    "v_mov_b32 v23, 1\n"
    "global_store_dword v22, v23, %1 sc1\n"
    "s_waitcnt vmcnt(0)\n"
    ".Lwp4_x%=:\n"
    "v_readfirstlane_b32 s50, v96\n"
    "v_mbcnt_lo_u32_b32 v20, -1, 0\n"
    "v_mbcnt_hi_u32_b32 v20, -1, v20\n"
    "s_lshl_b32 s51, %2, 6\n"
    "v_add_u32 v20, s51, v20\n"
    "v_min_u32 v20, 129, v20\n"
    "v_lshlrev_b32 v20, 5, v20\n"
    "v_add_u32 v20, 0x26c00, v20\n"
    "s_and_b32 s50, s50, 1\n"
    "ds_read_b64 v[22:23], v20\n"
    "s_waitcnt lgkmcnt(0)\n"
    "v_and_b32 v21, s50, v22\n"
    "v_lshlrev_b32 v21, 13, v21\n"
    "v_and_b32 v22, -2, v22\n"
    "v_xor_b32 v22, v21, v22\n"
    "v_and_b32 v21, s50, v23\n"
    "v_lshlrev_b32 v21, 13, v21\n"
    "v_and_b32 v23, -2, v23\n"
    "v_xor_b32 v23, v21, v23\n"
    "ds_write_b64 v20, v[22:23]\n"
    "ds_read_b64 v[22:23], v20 offset:8\n"
    "s_waitcnt lgkmcnt(0)\n"
    "v_and_b32 v21, s50, v22\n"
    "v_lshlrev_b32 v21, 13, v21\n"
    "v_and_b32 v22, -2, v22\n"
    "v_xor_b32 v22, v21, v22\n"
    "v_and_b32 v21, s50, v23\n"
    "v_lshlrev_b32 v21, 13, v21\n"
    "v_and_b32 v23, -2, v23\n"
    "v_xor_b32 v23, v21, v23\n"
    "ds_write_b64 v20, v[22:23] offset:8\n"
    "s_waitcnt lgkmcnt(0)\n"
    "s_barrier\n"
    "s_and_b32 s50, s50, %4\n"
    "s_lshl_b32 s50, s50, 13\n"
    "s_and_b32 s51, %4, -2\n"
    "s_xor_b32 s50, s50, s51\n"
    "ds_read_b32 v21, v7 offset:4\n"
    "v_add_u32 v20, s50, v6\n"
    "s_waitcnt lgkmcnt(0)\n"
    "v_readfirstlane_b32 s49, v21\n"
    "s_branch .Lwwait_%=\n"
    ".Lwend_%=:\n"
    "s_cmp_eq_u32 s50, 192\n"
    "s_cbranch_scc0 .Lwpol_%=\n"
    "s_waitcnt vmcnt(0) lgkmcnt(0)\n"
    "s_endpgm\n"
    ".p2align 6\n"
    ".Lrole3_%=:\n"
    "s_mov_b64 s[40:41], %0\n"
    "s_and_b32 s41, s41, 0xffff\n"
    "s_mov_b32 s42, 0xc000000\n"
    "s_mov_b32 s43, 0x20000\n"
    "s_mov_b64 s[92:93], %0\n"
    "s_and_b32 s93, s93, 0xffff\n"
    "s_mov_b32 s94, 0xc000000\n"
    "s_mov_b32 s95, 0x20000\n"
    "s_mov_b32 s53, 131072\n"
    "s_mov_b64 s[56:57], 0\n"
    "v_mbcnt_lo_u32_b32 v100, -1, 0\n"
    "v_mbcnt_hi_u32_b32 v100, -1, v100\n"
    "s_lshl_b32 s50, %2, 8\n"
    "s_add_u32 s50, s50, 144384\n"
    "v_lshl_add_u32 v100, v100, 2, s50\n"
    "ds_read_b32 v0, v100\n"
    "ds_read_b32 v1, v100 offset:1024\n"
    "ds_read_b32 v2, v100 offset:2048\n"
    "ds_read_b32 v3, v100 offset:3072\n"
    "ds_read_b32 v4, v100 offset:4096\n"
    "ds_read_b32 v5, v100 offset:5120\n"
    "ds_read_b32 v6, v100 offset:6144\n"
    "ds_read_b32 v7, v100 offset:7168\n"
    "ds_read_b32 v8, v100 offset:8192\n"
    "ds_read_b32 v152, v100 offset:9216\n"
    "ds_read_b32 v98, v100 offset:10240\n"
    "ds_read_b32 v99, v100 offset:11264\n"
    "s_waitcnt lgkmcnt(0)\n"
    "s_barrier\n"
    "v_mbcnt_lo_u32_b32 v100, -1, 0\n"
    "v_mbcnt_hi_u32_b32 v100, -1, v100\n"
    "v_add_u32 v101, 0, v100\n"
    "v_mul_u32_u24 v102, 7282, v101\n"
    "v_lshrrev_b32 v102, 16, v102\n"
    "v_and_b32 v103, 12, v102\n"
    "v_and_b32 v104, 48, v102\n"
    "v_mul_i32_i24 v104, 24192, v104\n"
    "v_mul_u32_u24 v103, 3456, v103\n"
    "v_mul_u32_u24 v102, 3312, v102\n"
    "v_add3_u32 v102, v102, v103, v104\n"
    "v_lshl_add_u32 v80, v101, 4, v102\n"
    "v_add_u32 v101, 64, v100\n"
    "v_mul_u32_u24 v102, 7282, v101\n"
    "v_lshrrev_b32 v102, 16, v102\n"
    "v_and_b32 v103, 12, v102\n"
    "v_and_b32 v104, 48, v102\n"
    "v_mul_i32_i24 v104, 24192, v104\n"
    "v_mul_u32_u24 v103, 3456, v103\n"
    "v_mul_u32_u24 v102, 3312, v102\n"
    "v_add3_u32 v102, v102, v103, v104\n"
    "v_lshl_add_u32 v81, v101, 4, v102\n"
    "v_add_u32 v101, 128, v100\n"
    "v_mul_u32_u24 v102, 7282, v101\n"
    "v_lshrrev_b32 v102, 16, v102\n"
    "v_and_b32 v103, 12, v102\n"
    "v_and_b32 v104, 48, v102\n"
    "v_mul_i32_i24 v104, 24192, v104\n"
    "v_mul_u32_u24 v103, 3456, v103\n"
    "v_mul_u32_u24 v102, 3312, v102\n"
    "v_add3_u32 v102, v102, v103, v104\n"
    "v_lshl_add_u32 v82, v101, 4, v102\n"
    "v_add_u32 v101, 192, v100\n"
    "v_mul_u32_u24 v102, 7282, v101\n"
    "v_lshrrev_b32 v102, 16, v102\n"
    "v_and_b32 v103, 12, v102\n"
    "v_and_b32 v104, 48, v102\n"
    "v_mul_i32_i24 v104, 24192, v104\n"
    "v_mul_u32_u24 v103, 3456, v103\n"
    "v_mul_u32_u24 v102, 3312, v102\n"
    "v_add3_u32 v102, v102, v103, v104\n"
    "v_lshl_add_u32 v83, v101, 4, v102\n"
    "v_add_u32 v101, 256, v100\n"
    "v_mul_u32_u24 v102, 7282, v101\n"
    "v_lshrrev_b32 v102, 16, v102\n"
    "v_and_b32 v103, 12, v102\n"
    "v_and_b32 v104, 48, v102\n"
    "v_mul_i32_i24 v104, 24192, v104\n"
    "v_mul_u32_u24 v103, 3456, v103\n"
    "v_mul_u32_u24 v102, 3312, v102\n"
    "v_add3_u32 v102, v102, v103, v104\n"
    "v_lshl_add_u32 v84, v101, 4, v102\n"
    "v_add_u32 v101, 320, v100\n"
    "v_mul_u32_u24 v102, 7282, v101\n"
    "v_lshrrev_b32 v102, 16, v102\n"
    "v_and_b32 v103, 12, v102\n"
    "v_and_b32 v104, 48, v102\n"
    "v_mul_i32_i24 v104, 24192, v104\n"
    "v_mul_u32_u24 v103, 3456, v103\n"
    "v_mul_u32_u24 v102, 3312, v102\n"
    "v_add3_u32 v102, v102, v103, v104\n"
    "v_lshl_add_u32 v85, v101, 4, v102\n"
    "v_add_u32 v101, 384, v100\n"
    "v_mul_u32_u24 v102, 7282, v101\n"
    "v_lshrrev_b32 v102, 16, v102\n"
    "v_and_b32 v103, 12, v102\n"
    "v_and_b32 v104, 48, v102\n"
    "v_mul_i32_i24 v104, 24192, v104\n"
    "v_mul_u32_u24 v103, 3456, v103\n"
    "v_mul_u32_u24 v102, 3312, v102\n"
    "v_add3_u32 v102, v102, v103, v104\n"
    "v_lshl_add_u32 v86, v101, 4, v102\n"
    "v_add_u32 v101, 448, v100\n"
    "v_mul_u32_u24 v102, 7282, v101\n"
    "v_lshrrev_b32 v102, 16, v102\n"
    "v_and_b32 v103, 12, v102\n"
    "v_and_b32 v104, 48, v102\n"
    "v_mul_i32_i24 v104, 24192, v104\n"
    "v_mul_u32_u24 v103, 3456, v103\n"
    "v_mul_u32_u24 v102, 3312, v102\n"
    "v_add3_u32 v102, v102, v103, v104\n"
    "v_lshl_add_u32 v87, v101, 4, v102\n"
    "v_add_u32 v101, 512, v100\n"
    "v_mul_u32_u24 v102, 7282, v101\n"
    "v_lshrrev_b32 v102, 16, v102\n"
    "v_and_b32 v103, 12, v102\n"
    "v_and_b32 v104, 48, v102\n"
    "v_mul_i32_i24 v104, 24192, v104\n"
    "v_mul_u32_u24 v103, 3456, v103\n"
    "v_mul_u32_u24 v102, 3312, v102\n"
    "v_add3_u32 v102, v102, v103, v104\n"
    "v_lshl_add_u32 v88, v101, 4, v102\n"
    "s_min_u32 s50, %2, 2\n"
    "s_mul_i32 s54, s50, 9216\n"
    "s_mul_i32 s50, s50, 1152\n"
    "s_add_u32 s51, s50, %6\n"
    "s_mov_b64 s[44:45], %8\n"
    "s_and_b32 s45, s45, 0xffff\n"
    "s_mov_b32 s46, 0x1e6000\n"
    "s_mov_b32 s47, 0x20000\n"
    "v_add_u32 v101, 0, v100\n"
    "v_mul_u32_u24 v102, 7282, v101\n"
    "v_lshrrev_b32 v102, 16, v102\n"
    "v_and_b32 v103, 12, v102\n"
    "v_and_b32 v104, 16, v102\n"
    "v_mul_i32_i24 v104, -2592, v104\n"
    "v_mul_u32_u24 v103, 3456, v103\n"
    "v_mul_u32_u24 v102, 3312, v102\n"
    "v_add3_u32 v102, v102, v103, v104\n"
    "v_lshl_add_u32 v89, v101, 4, v102\n"
    "v_add_u32 v101, 64, v100\n"
    "v_mul_u32_u24 v102, 7282, v101\n"
    "v_lshrrev_b32 v102, 16, v102\n"
    "v_and_b32 v103, 12, v102\n"
    "v_and_b32 v104, 16, v102\n"
    "v_mul_i32_i24 v104, -2592, v104\n"
    "v_mul_u32_u24 v103, 3456, v103\n"
    "v_mul_u32_u24 v102, 3312, v102\n"
    "v_add3_u32 v102, v102, v103, v104\n"
    "v_lshl_add_u32 v90, v101, 4, v102\n"
    "v_add_u32 v101, 128, v100\n"
    "v_mul_u32_u24 v102, 7282, v101\n"
    "v_lshrrev_b32 v102, 16, v102\n"
    "v_and_b32 v103, 12, v102\n"
    "v_and_b32 v104, 16, v102\n"
    "v_mul_i32_i24 v104, -2592, v104\n"
    "v_mul_u32_u24 v103, 3456, v103\n"
    "v_mul_u32_u24 v102, 3312, v102\n"
    "v_add3_u32 v102, v102, v103, v104\n"
    "v_lshl_add_u32 v91, v101, 4, v102\n"
    "v_add_u32 v101, 192, v100\n"
    "v_mul_u32_u24 v102, 7282, v101\n"
    "v_lshrrev_b32 v102, 16, v102\n"
    "v_and_b32 v103, 12, v102\n"
    "v_and_b32 v104, 16, v102\n"
    "v_mul_i32_i24 v104, -2592, v104\n"
    "v_mul_u32_u24 v103, 3456, v103\n"
    "v_mul_u32_u24 v102, 3312, v102\n"
    "v_add3_u32 v102, v102, v103, v104\n"
    "v_lshl_add_u32 v92, v101, 4, v102\n"
    "v_add_u32 v101, 256, v100\n"
    "v_mul_u32_u24 v102, 7282, v101\n"
    "v_lshrrev_b32 v102, 16, v102\n"
    "v_and_b32 v103, 12, v102\n"
    "v_and_b32 v104, 16, v102\n"
    "v_mul_i32_i24 v104, -2592, v104\n"
    "v_mul_u32_u24 v103, 3456, v103\n"
    "v_mul_u32_u24 v102, 3312, v102\n"
    "v_add3_u32 v102, v102, v103, v104\n"
    "v_lshl_add_u32 v93, v101, 4, v102\n"
    "v_add_u32 v101, 320, v100\n"
    "v_mul_u32_u24 v102, 7282, v101\n"
    "v_lshrrev_b32 v102, 16, v102\n"
    "v_and_b32 v103, 12, v102\n"
    "v_and_b32 v104, 16, v102\n"
    "v_mul_i32_i24 v104, -2592, v104\n"
    "v_mul_u32_u24 v103, 3456, v103\n"
    "v_mul_u32_u24 v102, 3312, v102\n"
    "v_add3_u32 v102, v102, v103, v104\n"
    "v_lshl_add_u32 v94, v101, 4, v102\n"
    "v_add_u32 v101, 384, v100\n"
    "v_mul_u32_u24 v102, 7282, v101\n"
    "v_lshrrev_b32 v102, 16, v102\n"
    "v_and_b32 v103, 12, v102\n"
    "v_and_b32 v104, 16, v102\n"
    "v_mul_i32_i24 v104, -2592, v104\n"
    "v_mul_u32_u24 v103, 3456, v103\n"
    "v_mul_u32_u24 v102, 3312, v102\n"
    "v_add3_u32 v102, v102, v103, v104\n"
    "v_lshl_add_u32 v95, v101, 4, v102\n"
    "v_add_u32 v101, 448, v100\n"
    "v_mul_u32_u24 v102, 7282, v101\n"
    "v_lshrrev_b32 v102, 16, v102\n"
    "v_and_b32 v103, 12, v102\n"
    "v_and_b32 v104, 16, v102\n"
    "v_mul_i32_i24 v104, -2592, v104\n"
    "v_mul_u32_u24 v103, 3456, v103\n"
    "v_mul_u32_u24 v102, 3312, v102\n"
    "v_add3_u32 v102, v102, v103, v104\n"
    "v_lshl_add_u32 v96, v101, 4, v102\n"
    "v_add_u32 v101, 512, v100\n"
    "v_mul_u32_u24 v102, 7282, v101\n"
    "v_lshrrev_b32 v102, 16, v102\n"
    "v_and_b32 v103, 12, v102\n"
    "v_and_b32 v104, 16, v102\n"
    "v_mul_i32_i24 v104, -2592, v104\n"
    "v_mul_u32_u24 v103, 3456, v103\n"
    "v_mul_u32_u24 v102, 3312, v102\n"
    "v_add3_u32 v102, v102, v103, v104\n"
    "v_lshl_add_u32 v97, v101, 4, v102\n"
    "v_add_u32 v9, 0x21c00, v3\n"
    "v_add_u32 v10, 1024, v6\n"
    "s_lshl_b32 s58, %2, 13\n"
    "s_add_u32 s58, s58, 0\n"
    "s_add_u32 s88, s40, 32768\n"
    "s_mov_b32 s89, s41\n"
    "s_cmp_lt_u32 %2, 2\n"
    "s_cselect_b32 s96, s42, 0\n"
    "s_mov_b32 s91, s43\n"
    "s_mul_i32 s59, %2, 576\n"
    "s_add_u32 s59, s59, 3072\n"
    "s_bfm_b64 s[60:61], 36, 0\n"
    "s_xor_b32 s50, %2, 0\n"
    "s_mul_i32 s50, s50, 2304\n"
    "v_add_u32 v106, s50, v98\n"
    "v_add_u32 v107, s50, v99\n"
    "s_xor_b32 s50, %2, 1\n"
    "s_mul_i32 s50, s50, 2304\n"
    "v_add_u32 v108, s50, v98\n"
    "v_add_u32 v109, s50, v99\n"
    "s_xor_b32 s50, %2, 2\n"
    "s_mul_i32 s50, s50, 2304\n"
    "v_add_u32 v110, s50, v98\n"
    "v_add_u32 v111, s50, v99\n"
    "s_xor_b32 s50, %2, 3\n"
    "s_mul_i32 s50, s50, 2304\n"
    "v_add_u32 v112, s50, v98\n"
    "v_add_u32 v113, s50, v99\n"
    "s_xor_b32 s50, %2, 4\n"
    "s_and_b32 s50, s50, 1\n"
    "s_lshl_b32 s50, s50, 1\n"
    "s_mul_i32 s50, s50, 2304\n"
    "v_add_u32 v114, s50, v98\n"
    "v_add_u32 v115, s50, v99\n"
    "s_xor_b32 s50, %2, 5\n"
    "s_and_b32 s50, s50, 1\n"
    "s_lshl_b32 s50, s50, 1\n"
    "s_mul_i32 s50, s50, 2304\n"
    "v_add_u32 v116, s50, v98\n"
    "v_add_u32 v117, s50, v99\n"
    "v_mov_b32 v56, 0x3f803f80\n"
    "v_mov_b32 v57, 0x3f803f80\n"
    "v_mov_b32 v58, 0x3f803f80\n"
    "v_mov_b32 v59, 0x3f803f80\n"
    "s_cmp_eq_u32 %2, 3\n"
    "s_cbranch_scc1 .Lac1_%=\n"
    "s_add_u32 s55, s51, 0\n"
    "s_add_u32 m0, s54, 0\n"
    "s_nop 0\n"
    "buffer_load_dwordx4 v80, s[44:47], s55 offen lds\n"
    "s_add_u32 m0, s54, 1024\n"
    "s_nop 0\n"
    "buffer_load_dwordx4 v81, s[44:47], s55 offen lds\n"
    "s_add_u32 m0, s54, 2048\n"
    "s_nop 0\n"
    "buffer_load_dwordx4 v82, s[44:47], s55 offen lds\n"
    "s_add_u32 m0, s54, 3072\n"
    "s_nop 0\n"
    "buffer_load_dwordx4 v83, s[44:47], s55 offen lds\n"
    "s_add_u32 m0, s54, 4096\n"
    "s_nop 0\n"
    "buffer_load_dwordx4 v84, s[44:47], s55 offen lds\n"
    "s_add_u32 m0, s54, 5120\n"
    "s_nop 0\n"
    "buffer_load_dwordx4 v85, s[44:47], s55 offen lds\n"
    "s_add_u32 m0, s54, 6144\n"
    "s_nop 0\n"
    "buffer_load_dwordx4 v86, s[44:47], s55 offen lds\n"
    "s_add_u32 m0, s54, 7168\n"
    "s_nop 0\n"
    "buffer_load_dwordx4 v87, s[44:47], s55 offen lds\n"
    "s_add_u32 m0, s54, 8192\n"
    "s_nop 0\n"
    "buffer_load_dwordx4 v88, s[44:47], s55 offen lds\n"
    ".Lac1_%=:\n"
    "s_cmp_eq_u32 %2, 3\n"
    "s_cbranch_scc1 .Lac2_%=\n"
    "s_add_u32 s55, s51, 13824\n"
    "s_add_u32 m0, s54, 27648\n"
    "s_nop 0\n"
    "buffer_load_dwordx4 v80, s[44:47], s55 offen lds\n"
    "s_add_u32 m0, s54, 28672\n"
    "s_nop 0\n"
    "buffer_load_dwordx4 v81, s[44:47], s55 offen lds\n"
    "s_add_u32 m0, s54, 29696\n"
    "s_nop 0\n"
    "buffer_load_dwordx4 v82, s[44:47], s55 offen lds\n"
    "s_add_u32 m0, s54, 30720\n"
    "s_nop 0\n"
    "buffer_load_dwordx4 v83, s[44:47], s55 offen lds\n"
    "s_add_u32 m0, s54, 31744\n"
    "s_nop 0\n"
    "buffer_load_dwordx4 v84, s[44:47], s55 offen lds\n"
    "s_add_u32 m0, s54, 32768\n"
    "s_nop 0\n"
    "buffer_load_dwordx4 v85, s[44:47], s55 offen lds\n"
    "s_add_u32 m0, s54, 33792\n"
    "s_nop 0\n"
    "buffer_load_dwordx4 v86, s[44:47], s55 offen lds\n"
    "s_add_u32 m0, s54, 34816\n"
    "s_nop 0\n"
    "buffer_load_dwordx4 v87, s[44:47], s55 offen lds\n"
    "s_add_u32 m0, s54, 35840\n"
    "s_nop 0\n"
    "buffer_load_dwordx4 v88, s[44:47], s55 offen lds\n"
    ".Lac2_%=:\n"
    "s_cmp_eq_u32 %2, 3\n"
    "s_cbranch_scc1 .Lac3_%=\n"
    "s_add_u32 s55, s51, 110592\n"
    "s_add_u32 m0, s54, 55296\n"
    "s_nop 0\n"
    "buffer_load_dwordx4 v80, s[44:47], s55 offen lds\n"
    "s_add_u32 m0, s54, 56320\n"
    "s_nop 0\n"
    "buffer_load_dwordx4 v81, s[44:47], s55 offen lds\n"
    "s_add_u32 m0, s54, 57344\n"
    "s_nop 0\n"
    "buffer_load_dwordx4 v82, s[44:47], s55 offen lds\n"
    "s_add_u32 m0, s54, 58368\n"
    "s_nop 0\n"
    "buffer_load_dwordx4 v83, s[44:47], s55 offen lds\n"
    "s_add_u32 m0, s54, 59392\n"
    "s_nop 0\n"
    "buffer_load_dwordx4 v84, s[44:47], s55 offen lds\n"
    "s_add_u32 m0, s54, 60416\n"
    "s_nop 0\n"
    "buffer_load_dwordx4 v85, s[44:47], s55 offen lds\n"
    "s_add_u32 m0, s54, 61440\n"
    "s_nop 0\n"
    "buffer_load_dwordx4 v86, s[44:47], s55 offen lds\n"
    "s_add_u32 m0, s54, 62464\n"
    "s_nop 0\n"
    "buffer_load_dwordx4 v87, s[44:47], s55 offen lds\n"
    "s_add_u32 m0, s54, 63488\n"
    "s_nop 0\n"
    "buffer_load_dwordx4 v88, s[44:47], s55 offen lds\n"
    ".Lac3_%=:\n"
    "s_cmp_eq_u32 %2, 3\n"
    "s_cbranch_scc1 .Lac4_%=\n"
    "s_add_u32 s55, s51, 124416\n"
    "s_add_u32 m0, s54, 82944\n"
    "s_nop 0\n"
    "buffer_load_dwordx4 v80, s[44:47], s55 offen lds\n"
    "s_add_u32 m0, s54, 83968\n"
    "s_nop 0\n"
    "buffer_load_dwordx4 v81, s[44:47], s55 offen lds\n"
    "s_add_u32 m0, s54, 84992\n"
    "s_nop 0\n"
    "buffer_load_dwordx4 v82, s[44:47], s55 offen lds\n"
    "s_add_u32 m0, s54, 86016\n"
    "s_nop 0\n"
    "buffer_load_dwordx4 v83, s[44:47], s55 offen lds\n"
    "s_add_u32 m0, s54, 87040\n"
    "s_nop 0\n"
    "buffer_load_dwordx4 v84, s[44:47], s55 offen lds\n"
    "s_add_u32 m0, s54, 88064\n"
    "s_nop 0\n"
    "buffer_load_dwordx4 v85, s[44:47], s55 offen lds\n"
    "s_add_u32 m0, s54, 89088\n"
    "s_nop 0\n"
    "buffer_load_dwordx4 v86, s[44:47], s55 offen lds\n"
    "s_add_u32 m0, s54, 90112\n"
    "s_nop 0\n"
    "buffer_load_dwordx4 v87, s[44:47], s55 offen lds\n"
    "s_add_u32 m0, s54, 91136\n"
    "s_nop 0\n"
    "buffer_load_dwordx4 v88, s[44:47], s55 offen lds\n"
    ".Lac4_%=:\n"
    "s_cmp_eq_u32 %2, 3\n"
    "s_cbranch_scc1 .Lac5_%=\n"
    "s_add_u32 s55, s51, 221184\n"
    "s_add_u32 m0, s54, 110592\n"
    "s_nop 0\n"
    "buffer_load_dwordx4 v80, s[44:47], s55 offen lds\n"
    "s_add_u32 m0, s54, 111616\n"
    "s_nop 0\n"
    "buffer_load_dwordx4 v81, s[44:47], s55 offen lds\n"
    "s_add_u32 m0, s54, 112640\n"
    "s_nop 0\n"
    "buffer_load_dwordx4 v82, s[44:47], s55 offen lds\n"
    "s_add_u32 m0, s54, 113664\n"
    "s_nop 0\n"
    "buffer_load_dwordx4 v83, s[44:47], s55 offen lds\n"
    "s_add_u32 m0, s54, 114688\n"
    "s_nop 0\n"
    "buffer_load_dwordx4 v84, s[44:47], s55 offen lds\n"
    "s_add_u32 m0, s54, 115712\n"
    "s_nop 0\n"
    "buffer_load_dwordx4 v85, s[44:47], s55 offen lds\n"
    "s_add_u32 m0, s54, 116736\n"
    "s_nop 0\n"
    "buffer_load_dwordx4 v86, s[44:47], s55 offen lds\n"
    "s_add_u32 m0, s54, 117760\n"
    "s_nop 0\n"
    "buffer_load_dwordx4 v87, s[44:47], s55 offen lds\n"
    "s_add_u32 m0, s54, 118784\n"
    "s_nop 0\n"
    "buffer_load_dwordx4 v88, s[44:47], s55 offen lds\n"
    ".Lac5_%=:\n"
    "s_waitcnt vmcnt(36) lgkmcnt(0)\n"
    "s_barrier\n"
    "ds_read_b64_tr_b16 a[0:1], v106\n"
    "ds_read_b64_tr_b16 a[72:73], v106 offset:9216\n"
    "ds_read_b64_tr_b16 a[144:145], v106 offset:18432\n"
    "ds_read_b64_tr_b16 a[216:217], v107\n"
    "ds_read_b64_tr_b16 a[16:17], v108\n"
    "ds_read_b64_tr_b16 a[88:89], v108 offset:9216\n"
    "ds_read_b64_tr_b16 a[160:161], v108 offset:18432\n"
    "ds_read_b64_tr_b16 a[32:33], v110\n"
    "ds_read_b64_tr_b16 a[104:105], v110 offset:9216\n"
    "ds_read_b64_tr_b16 a[176:177], v110 offset:18432\n"
    "ds_read_b64_tr_b16 a[232:233], v111\n"
    "ds_read_b64_tr_b16 a[48:49], v112\n"
    "ds_read_b64_tr_b16 a[120:121], v112 offset:9216\n"
    "ds_read_b64_tr_b16 a[192:193], v112 offset:18432\n"
    "s_waitcnt vmcnt(27) lgkmcnt(0)\n"
    "s_barrier\n"
    "s_cmp_eq_u32 %2, 3\n"
    "s_cbranch_scc1 .Lac6_%=\n"
    "s_add_u32 s55, s51, 235008\n"
    "s_add_u32 m0, s54, 0\n"
    "s_nop 0\n"
    "buffer_load_dwordx4 v80, s[44:47], s55 offen lds\n"
    "s_add_u32 m0, s54, 1024\n"
    "s_nop 0\n"
    "buffer_load_dwordx4 v81, s[44:47], s55 offen lds\n"
    "s_add_u32 m0, s54, 2048\n"
    "s_nop 0\n"
    "buffer_load_dwordx4 v82, s[44:47], s55 offen lds\n"
    "s_add_u32 m0, s54, 3072\n"
    "s_nop 0\n"
    "buffer_load_dwordx4 v83, s[44:47], s55 offen lds\n"
    "s_add_u32 m0, s54, 4096\n"
    "s_nop 0\n"
    "buffer_load_dwordx4 v84, s[44:47], s55 offen lds\n"
    "s_add_u32 m0, s54, 5120\n"
    "s_nop 0\n"
    "buffer_load_dwordx4 v85, s[44:47], s55 offen lds\n"
    "s_add_u32 m0, s54, 6144\n"
    "s_nop 0\n"
    "buffer_load_dwordx4 v86, s[44:47], s55 offen lds\n"
    "s_add_u32 m0, s54, 7168\n"
    "s_nop 0\n"
    "buffer_load_dwordx4 v87, s[44:47], s55 offen lds\n"
    "s_add_u32 m0, s54, 8192\n"
    "s_nop 0\n"
    "buffer_load_dwordx4 v88, s[44:47], s55 offen lds\n"
    ".Lac6_%=:\n"
    "ds_read_b64_tr_b16 a[2:3], v106 offset:27648\n"
    "ds_read_b64_tr_b16 a[74:75], v106 offset:36864\n"
    "ds_read_b64_tr_b16 a[146:147], v106 offset:46080\n"
    "ds_read_b64_tr_b16 a[218:219], v107 offset:27648\n"
    "ds_read_b64_tr_b16 a[18:19], v108 offset:27648\n"
    "ds_read_b64_tr_b16 a[90:91], v108 offset:36864\n"
    "ds_read_b64_tr_b16 a[162:163], v108 offset:46080\n"
    "ds_read_b64_tr_b16 a[34:35], v110 offset:27648\n"
    "ds_read_b64_tr_b16 a[106:107], v110 offset:36864\n"
    "ds_read_b64_tr_b16 a[178:179], v110 offset:46080\n"
    "ds_read_b64_tr_b16 a[234:235], v111 offset:27648\n"
    "ds_read_b64_tr_b16 a[50:51], v112 offset:27648\n"
    "ds_read_b64_tr_b16 a[122:123], v112 offset:36864\n"
    "ds_read_b64_tr_b16 a[194:195], v112 offset:46080\n"
    "s_waitcnt vmcnt(27) lgkmcnt(0)\n"
    "s_barrier\n"
    "s_cmp_eq_u32 %2, 3\n"
    "s_cbranch_scc1 .Lac7_%=\n"
    "s_add_u32 s55, s51, 331776\n"
    "s_add_u32 m0, s54, 27648\n"
    "s_nop 0\n"
    "buffer_load_dwordx4 v80, s[44:47], s55 offen lds\n"
    "s_add_u32 m0, s54, 28672\n"
    "s_nop 0\n"
    "buffer_load_dwordx4 v81, s[44:47], s55 offen lds\n"
    "s_add_u32 m0, s54, 29696\n"
    "s_nop 0\n"
    "buffer_load_dwordx4 v82, s[44:47], s55 offen lds\n"
    "s_add_u32 m0, s54, 30720\n"
    "s_nop 0\n"
    "buffer_load_dwordx4 v83, s[44:47], s55 offen lds\n"
    "s_add_u32 m0, s54, 31744\n"
    "s_nop 0\n"
    "buffer_load_dwordx4 v84, s[44:47], s55 offen lds\n"
    "s_add_u32 m0, s54, 32768\n"
    "s_nop 0\n"
    "buffer_load_dwordx4 v85, s[44:47], s55 offen lds\n"
    "s_add_u32 m0, s54, 33792\n"
    "s_nop 0\n"
    "buffer_load_dwordx4 v86, s[44:47], s55 offen lds\n"
    "s_add_u32 m0, s54, 34816\n"
    "s_nop 0\n"
    "buffer_load_dwordx4 v87, s[44:47], s55 offen lds\n"
    "s_add_u32 m0, s54, 35840\n"
    "s_nop 0\n"
    "buffer_load_dwordx4 v88, s[44:47], s55 offen lds\n"
    ".Lac7_%=:\n"
    "v_mfma_f32_16x16x32_bf16 v[12:15], v[56:59], a[0:3], 0\n"
    "v_mfma_f32_16x16x32_bf16 v[16:19], v[56:59], a[72:75], 0\n"
    "v_mfma_f32_16x16x32_bf16 v[20:23], v[56:59], a[144:147], 0\n"
    "v_mfma_f32_16x16x32_bf16 v[24:27], v[56:59], a[216:219], 0\n"
    "v_mfma_f32_16x16x32_bf16 v[12:15], v[56:59], a[16:19], v[12:15]\n"
    "v_mfma_f32_16x16x32_bf16 v[16:19], v[56:59], a[88:91], v[16:19]\n"
    "v_mfma_f32_16x16x32_bf16 v[20:23], v[56:59], a[160:163], v[20:23]\n"
    "v_mfma_f32_16x16x32_bf16 v[12:15], v[56:59], a[32:35], v[12:15]\n"
    "v_mfma_f32_16x16x32_bf16 v[16:19], v[56:59], a[104:107], v[16:19]\n"
    "v_mfma_f32_16x16x32_bf16 v[20:23], v[56:59], a[176:179], v[20:23]\n"
    "v_mfma_f32_16x16x32_bf16 v[24:27], v[56:59], a[232:235], v[24:27]\n"
    "v_mfma_f32_16x16x32_bf16 v[12:15], v[56:59], a[48:51], v[12:15]\n"
    "v_mfma_f32_16x16x32_bf16 v[16:19], v[56:59], a[120:123], v[16:19]\n"
    "v_mfma_f32_16x16x32_bf16 v[20:23], v[56:59], a[192:195], v[20:23]\n"
    "ds_read_b64_tr_b16 a[4:5], v106 offset:55296\n"
    "ds_read_b64_tr_b16 a[76:77], v106 offset:64512\n"
    "v_add_u32 v149, 0x10000, v106\n"
    "ds_read_b64_tr_b16 a[148:149], v149 offset:8192\n"
    "ds_read_b64_tr_b16 a[220:221], v107 offset:55296\n"
    "ds_read_b64_tr_b16 a[20:21], v108 offset:55296\n"
    "ds_read_b64_tr_b16 a[92:93], v108 offset:64512\n"
    "v_add_u32 v148, 0x10000, v108\n"
    "ds_read_b64_tr_b16 a[164:165], v148 offset:8192\n"
    "ds_read_b64_tr_b16 a[36:37], v110 offset:55296\n"
    "ds_read_b64_tr_b16 a[108:109], v110 offset:64512\n"
    "v_add_u32 v147, 0x10000, v110\n"
    "ds_read_b64_tr_b16 a[180:181], v147 offset:8192\n"
    "ds_read_b64_tr_b16 a[236:237], v111 offset:55296\n"
    "ds_read_b64_tr_b16 a[52:53], v112 offset:55296\n"
    "ds_read_b64_tr_b16 a[124:125], v112 offset:64512\n"
    "v_add_u32 v146, 0x10000, v112\n"
    "ds_read_b64_tr_b16 a[196:197], v146 offset:8192\n"
    "s_waitcnt vmcnt(27) lgkmcnt(0)\n"
    "s_barrier\n"
    "s_cmp_eq_u32 %2, 3\n"
    "s_cbranch_scc1 .Lac8_%=\n"
    "s_add_u32 s55, s51, 345600\n"
    "s_add_u32 m0, s54, 55296\n"
    "s_nop 0\n"
    "buffer_load_dwordx4 v80, s[44:47], s55 offen lds\n"
    "s_add_u32 m0, s54, 56320\n"
    "s_nop 0\n"
    "buffer_load_dwordx4 v81, s[44:47], s55 offen lds\n"
    "s_add_u32 m0, s54, 57344\n"
    "s_nop 0\n"
    "buffer_load_dwordx4 v82, s[44:47], s55 offen lds\n"
    "s_add_u32 m0, s54, 58368\n"
    "s_nop 0\n"
    "buffer_load_dwordx4 v83, s[44:47], s55 offen lds\n"
    "s_add_u32 m0, s54, 59392\n"
    "s_nop 0\n"
    "buffer_load_dwordx4 v84, s[44:47], s55 offen lds\n"
    "s_add_u32 m0, s54, 60416\n"
    "s_nop 0\n"
    "buffer_load_dwordx4 v85, s[44:47], s55 offen lds\n"
    "s_add_u32 m0, s54, 61440\n"
    "s_nop 0\n"
    "buffer_load_dwordx4 v86, s[44:47], s55 offen lds\n"
    "s_add_u32 m0, s54, 62464\n"
    "s_nop 0\n"
    "buffer_load_dwordx4 v87, s[44:47], s55 offen lds\n"
    "s_add_u32 m0, s54, 63488\n"
    "s_nop 0\n"
    "buffer_load_dwordx4 v88, s[44:47], s55 offen lds\n"
    ".Lac8_%=:\n"
    "ds_read_b64_tr_b16 a[6:7], v149 offset:17408\n"
    "ds_read_b64_tr_b16 a[78:79], v149 offset:26624\n"
    "ds_read_b64_tr_b16 a[150:151], v149 offset:35840\n"
    "v_add_u32 v145, 0x10000, v107\n"
    "ds_read_b64_tr_b16 a[222:223], v145 offset:17408\n"
    "ds_read_b64_tr_b16 a[22:23], v148 offset:17408\n"
    "ds_read_b64_tr_b16 a[94:95], v148 offset:26624\n"
    "ds_read_b64_tr_b16 a[166:167], v148 offset:35840\n"
    "ds_read_b64_tr_b16 a[38:39], v147 offset:17408\n"
    "ds_read_b64_tr_b16 a[110:111], v147 offset:26624\n"
    "ds_read_b64_tr_b16 a[182:183], v147 offset:35840\n"
    "v_add_u32 v144, 0x10000, v111\n"
    "ds_read_b64_tr_b16 a[238:239], v144 offset:17408\n"
    "ds_read_b64_tr_b16 a[54:55], v146 offset:17408\n"
    "ds_read_b64_tr_b16 a[126:127], v146 offset:26624\n"
    "ds_read_b64_tr_b16 a[198:199], v146 offset:35840\n"
    "s_waitcnt vmcnt(27) lgkmcnt(0)\n"
    "s_barrier\n"
    "s_cmp_eq_u32 %2, 3\n"
    "s_cbranch_scc1 .Lac9_%=\n"
    "s_add_u32 s55, s51, 1769472\n"
    "s_add_u32 m0, s54, 82944\n"
    "s_nop 0\n"
    "buffer_load_dwordx4 v89, s[44:47], s55 offen lds\n"
    "s_add_u32 m0, s54, 83968\n"
    "s_nop 0\n"
    "buffer_load_dwordx4 v90, s[44:47], s55 offen lds\n"
    "s_add_u32 m0, s54, 84992\n"
    "s_nop 0\n"
    "buffer_load_dwordx4 v91, s[44:47], s55 offen lds\n"
    "s_add_u32 m0, s54, 86016\n"
    "s_nop 0\n"
    "buffer_load_dwordx4 v92, s[44:47], s55 offen lds\n"
    "s_add_u32 m0, s54, 87040\n"
    "s_nop 0\n"
    "buffer_load_dwordx4 v93, s[44:47], s55 offen lds\n"
    "s_add_u32 m0, s54, 88064\n"
    "s_nop 0\n"
    "buffer_load_dwordx4 v94, s[44:47], s55 offen lds\n"
    "s_add_u32 m0, s54, 89088\n"
    "s_nop 0\n"
    "buffer_load_dwordx4 v95, s[44:47], s55 offen lds\n"
    "s_add_u32 m0, s54, 90112\n"
    "s_nop 0\n"
    "buffer_load_dwordx4 v96, s[44:47], s55 offen lds\n"
    "s_add_u32 m0, s54, 91136\n"
    "s_nop 0\n"
    "buffer_load_dwordx4 v97, s[44:47], s55 offen lds\n"
    ".Lac9_%=:\n"
    "v_mov_b32 v11, 0x20400\n"
    "global_load_dwordx2 v[76:77], v11, %1 sc1\n"
    "v_mbcnt_lo_u32_b32 v72, -1, 0\n"
    "v_min_u32 v72, 7, v72\n"
    "v_lshlrev_b32 v72, 7, v72\n"
    "v_add_u32 v72, 0x20000, v72\n"
    "global_load_dwordx2 v[78:79], v72, %1 sc1\n"
    "v_mfma_f32_16x16x32_bf16 v[12:15], v[56:59], a[4:7], v[12:15]\n"
    "v_mfma_f32_16x16x32_bf16 v[16:19], v[56:59], a[76:79], v[16:19]\n"
    "v_mfma_f32_16x16x32_bf16 v[20:23], v[56:59], a[148:151], v[20:23]\n"
    "v_mfma_f32_16x16x32_bf16 v[24:27], v[56:59], a[220:223], v[24:27]\n"
    "v_mfma_f32_16x16x32_bf16 v[12:15], v[56:59], a[20:23], v[12:15]\n"
    "v_mfma_f32_16x16x32_bf16 v[16:19], v[56:59], a[92:95], v[16:19]\n"
    "v_mfma_f32_16x16x32_bf16 v[20:23], v[56:59], a[164:167], v[20:23]\n"
    "v_mfma_f32_16x16x32_bf16 v[12:15], v[56:59], a[36:39], v[12:15]\n"
    "v_mfma_f32_16x16x32_bf16 v[16:19], v[56:59], a[108:111], v[16:19]\n"
    "v_mfma_f32_16x16x32_bf16 v[20:23], v[56:59], a[180:183], v[20:23]\n"
    "v_mfma_f32_16x16x32_bf16 v[24:27], v[56:59], a[236:239], v[24:27]\n"
    "v_mfma_f32_16x16x32_bf16 v[12:15], v[56:59], a[52:55], v[12:15]\n"
    "v_mfma_f32_16x16x32_bf16 v[16:19], v[56:59], a[124:127], v[16:19]\n"
    "v_mfma_f32_16x16x32_bf16 v[20:23], v[56:59], a[196:199], v[20:23]\n"
    "ds_read_b64_tr_b16 a[8:9], v149 offset:45056\n"
    "ds_read_b64_tr_b16 a[80:81], v149 offset:54272\n"
    "ds_read_b64_tr_b16 a[152:153], v149 offset:63488\n"
    "ds_read_b64_tr_b16 a[224:225], v145 offset:45056\n"
    "ds_read_b64_tr_b16 a[24:25], v148 offset:45056\n"
    "ds_read_b64_tr_b16 a[96:97], v148 offset:54272\n"
    "ds_read_b64_tr_b16 a[168:169], v148 offset:63488\n"
    "ds_read_b64_tr_b16 a[40:41], v147 offset:45056\n"
    "ds_read_b64_tr_b16 a[112:113], v147 offset:54272\n"
    "ds_read_b64_tr_b16 a[184:185], v147 offset:63488\n"
    "ds_read_b64_tr_b16 a[240:241], v144 offset:45056\n"
    "ds_read_b64_tr_b16 a[56:57], v146 offset:45056\n"
    "ds_read_b64_tr_b16 a[128:129], v146 offset:54272\n"
    "ds_read_b64_tr_b16 a[200:201], v146 offset:63488\n"
    "s_waitcnt vmcnt(29) lgkmcnt(0)\n"
    "s_barrier\n"
    "ds_read_b64_tr_b16 a[10:11], v106\n"
    "ds_read_b64_tr_b16 a[82:83], v106 offset:9216\n"
    "ds_read_b64_tr_b16 a[154:155], v106 offset:18432\n"
    "ds_read_b64_tr_b16 a[226:227], v107\n"
    "ds_read_b64_tr_b16 a[26:27], v108\n"
    "ds_read_b64_tr_b16 a[98:99], v108 offset:9216\n"
    "ds_read_b64_tr_b16 a[170:171], v108 offset:18432\n"
    "ds_read_b64_tr_b16 a[42:43], v110\n"
    "ds_read_b64_tr_b16 a[114:115], v110 offset:9216\n"
    "ds_read_b64_tr_b16 a[186:187], v110 offset:18432\n"
    "ds_read_b64_tr_b16 a[242:243], v111\n"
    "ds_read_b64_tr_b16 a[58:59], v112\n"
    "ds_read_b64_tr_b16 a[130:131], v112 offset:9216\n"
    "ds_read_b64_tr_b16 a[202:203], v112 offset:18432\n"
    "s_waitcnt vmcnt(20) lgkmcnt(0)\n"
    "s_barrier\n"
    "v_mfma_f32_16x16x32_bf16 v[12:15], v[56:59], a[8:11], v[12:15]\n"
    "v_mfma_f32_16x16x32_bf16 v[16:19], v[56:59], a[80:83], v[16:19]\n"
    "v_mfma_f32_16x16x32_bf16 v[20:23], v[56:59], a[152:155], v[20:23]\n"
    "v_mfma_f32_16x16x32_bf16 v[24:27], v[56:59], a[224:227], v[24:27]\n"
    "v_mfma_f32_16x16x32_bf16 v[12:15], v[56:59], a[24:27], v[12:15]\n"
    "v_mfma_f32_16x16x32_bf16 v[16:19], v[56:59], a[96:99], v[16:19]\n"
    "v_mfma_f32_16x16x32_bf16 v[20:23], v[56:59], a[168:171], v[20:23]\n"
    "v_mfma_f32_16x16x32_bf16 v[12:15], v[56:59], a[40:43], v[12:15]\n"
    "v_mfma_f32_16x16x32_bf16 v[16:19], v[56:59], a[112:115], v[16:19]\n"
    "v_mfma_f32_16x16x32_bf16 v[20:23], v[56:59], a[184:187], v[20:23]\n"
    "v_mfma_f32_16x16x32_bf16 v[24:27], v[56:59], a[240:243], v[24:27]\n"
    "v_mfma_f32_16x16x32_bf16 v[12:15], v[56:59], a[56:59], v[12:15]\n"
    "v_mfma_f32_16x16x32_bf16 v[16:19], v[56:59], a[128:131], v[16:19]\n"
    "v_mfma_f32_16x16x32_bf16 v[20:23], v[56:59], a[200:203], v[20:23]\n"
    "ds_read_b64_tr_b16 a[12:13], v106 offset:27648\n"
    "ds_read_b64_tr_b16 a[84:85], v106 offset:36864\n"
    "ds_read_b64_tr_b16 a[156:157], v106 offset:46080\n"
    "ds_read_b64_tr_b16 a[228:229], v107 offset:27648\n"
    "ds_read_b64_tr_b16 a[28:29], v108 offset:27648\n"
    "ds_read_b64_tr_b16 a[100:101], v108 offset:36864\n"
    "ds_read_b64_tr_b16 a[172:173], v108 offset:46080\n"
    "ds_read_b64_tr_b16 a[44:45], v110 offset:27648\n"
    "ds_read_b64_tr_b16 a[116:117], v110 offset:36864\n"
    "ds_read_b64_tr_b16 a[188:189], v110 offset:46080\n"
    "ds_read_b64_tr_b16 a[244:245], v111 offset:27648\n"
    "ds_read_b64_tr_b16 a[60:61], v112 offset:27648\n"
    "ds_read_b64_tr_b16 a[132:133], v112 offset:36864\n"
    "ds_read_b64_tr_b16 a[204:205], v112 offset:46080\n"
    "s_waitcnt vmcnt(11) lgkmcnt(0)\n"
    "s_barrier\n"
    "ds_read_b64_tr_b16 a[14:15], v106 offset:55296\n"
    "ds_read_b64_tr_b16 a[86:87], v106 offset:64512\n"
    "ds_read_b64_tr_b16 a[158:159], v149 offset:8192\n"
    "ds_read_b64_tr_b16 a[230:231], v107 offset:55296\n"
    "ds_read_b64_tr_b16 a[30:31], v108 offset:55296\n"
    "ds_read_b64_tr_b16 a[102:103], v108 offset:64512\n"
    "ds_read_b64_tr_b16 a[174:175], v148 offset:8192\n"
    "ds_read_b64_tr_b16 a[46:47], v110 offset:55296\n"
    "ds_read_b64_tr_b16 a[118:119], v110 offset:64512\n"
    "ds_read_b64_tr_b16 a[190:191], v147 offset:8192\n"
    "ds_read_b64_tr_b16 a[246:247], v111 offset:55296\n"
    "ds_read_b64_tr_b16 a[62:63], v112 offset:55296\n"
    "ds_read_b64_tr_b16 a[134:135], v112 offset:64512\n"
    "ds_read_b64_tr_b16 a[206:207], v146 offset:8192\n"
    "s_waitcnt vmcnt(2) lgkmcnt(0)\n"
    "s_barrier\n"
    "v_mfma_f32_16x16x32_bf16 v[12:15], v[56:59], a[12:15], v[12:15]\n"
    "v_mfma_f32_16x16x32_bf16 v[16:19], v[56:59], a[84:87], v[16:19]\n"
    "v_mfma_f32_16x16x32_bf16 v[20:23], v[56:59], a[156:159], v[20:23]\n"
    "v_mfma_f32_16x16x32_bf16 v[24:27], v[56:59], a[228:231], v[24:27]\n"
    "v_mfma_f32_16x16x32_bf16 v[12:15], v[56:59], a[28:31], v[12:15]\n"
    "v_mfma_f32_16x16x32_bf16 v[16:19], v[56:59], a[100:103], v[16:19]\n"
    "v_mfma_f32_16x16x32_bf16 v[20:23], v[56:59], a[172:175], v[20:23]\n"
    "v_mfma_f32_16x16x32_bf16 v[12:15], v[56:59], a[44:47], v[12:15]\n"
    "v_mfma_f32_16x16x32_bf16 v[16:19], v[56:59], a[116:119], v[16:19]\n"
    "v_mfma_f32_16x16x32_bf16 v[20:23], v[56:59], a[188:191], v[20:23]\n"
    "v_mfma_f32_16x16x32_bf16 v[24:27], v[56:59], a[244:247], v[24:27]\n"
    "v_mfma_f32_16x16x32_bf16 v[12:15], v[56:59], a[60:63], v[12:15]\n"
    "v_mfma_f32_16x16x32_bf16 v[16:19], v[56:59], a[132:135], v[16:19]\n"
    "v_mfma_f32_16x16x32_bf16 v[20:23], v[56:59], a[204:207], v[20:23]\n"
    "v_add_u32 v143, 0x10000, v114\n"
    "ds_read_b64_tr_b16 a[64:65], v143 offset:17408\n"
    "ds_read_b64_tr_b16 a[136:137], v143 offset:26624\n"
    "ds_read_b64_tr_b16 a[208:209], v143 offset:35840\n"
    "v_add_u32 v142, 0x10000, v115\n"
    "ds_read_b64_tr_b16 a[248:249], v142 offset:17408\n"
    "ds_read_b64_tr_b16 a[66:67], v143 offset:19712\n"
    "ds_read_b64_tr_b16 a[138:139], v143 offset:28928\n"
    "ds_read_b64_tr_b16 a[210:211], v143 offset:38144\n"
    "ds_read_b64_tr_b16 a[250:251], v142 offset:19712\n"
    "v_add_u32 v141, 0x10000, v116\n"
    "ds_read_b64_tr_b16 a[68:69], v141 offset:17408\n"
    "ds_read_b64_tr_b16 a[140:141], v141 offset:26624\n"
    "ds_read_b64_tr_b16 a[212:213], v141 offset:35840\n"
    "ds_read_b64_tr_b16 a[70:71], v141 offset:19712\n"
    "ds_read_b64_tr_b16 a[142:143], v141 offset:28928\n"
    "ds_read_b64_tr_b16 a[214:215], v141 offset:38144\n"
    "s_waitcnt lgkmcnt(0)\n"
    "s_barrier\n"
    "v_mfma_f32_16x16x32_bf16 v[12:15], v[56:59], a[64:67], v[12:15]\n"
    "v_mfma_f32_16x16x32_bf16 v[16:19], v[56:59], a[136:139], v[16:19]\n"
    "v_mfma_f32_16x16x32_bf16 v[20:23], v[56:59], a[208:211], v[20:23]\n"
    "v_mfma_f32_16x16x32_bf16 v[24:27], v[56:59], a[248:251], v[24:27]\n"
    "v_mfma_f32_16x16x32_bf16 v[12:15], v[56:59], a[68:71], v[12:15]\n"
    "v_mfma_f32_16x16x32_bf16 v[16:19], v[56:59], a[140:143], v[16:19]\n"
    "v_mfma_f32_16x16x32_bf16 v[20:23], v[56:59], a[212:215], v[20:23]\n"
    "v_mov_b32 v28, 0\n"
    "v_mov_b32 v29, 0\n"
    "v_mov_b32 v30, 0\n"
    "v_mov_b32 v31, 0\n"
    "v_mov_b32 v215, 0\n"
    "v_mov_b32 v218, 0\n"
    "v_mov_b32 v219, 0\n"
    "v_add_u32 v73, 0x14000, v3\n"
    "ds_write_b128 v73, v[28:31] offset:4096\n"
    "ds_write_b128 v73, v[28:31] offset:8192\n"
    "ds_write_b128 v73, v[28:31] offset:20480\n"
    "ds_write_b128 v73, v[28:31] offset:24576\n"
    "v_mov_b32 v153, 0xf149f2ca\n"
    "v_add_u32 v103, 7, v152\n"
    "v_cmp_gt_u32 vcc, 8, v103\n"
    "s_nop 1\n"
    "v_cndmask_b32 v44, v153, v28, vcc\n"
    "v_cmp_le_i32 vcc, 9, v152\n"
    "s_nop 1\n"
    "v_cndmask_b32 v68, v153, v28, vcc\n"
    "v_add_u32 v103, 8, v152\n"
    "v_cmp_gt_u32 vcc, 8, v103\n"
    "s_nop 1\n"
    "v_cndmask_b32 v45, v153, v28, vcc\n"
    "v_cmp_le_i32 vcc, 8, v152\n"
    "s_nop 1\n"
    "v_cndmask_b32 v69, v153, v28, vcc\n"
    "v_add_u32 v103, 9, v152\n"
    "v_cmp_gt_u32 vcc, 8, v103\n"
    "s_nop 1\n"
    "v_cndmask_b32 v46, v153, v28, vcc\n"
    "v_cmp_le_i32 vcc, 7, v152\n"
    "s_nop 1\n"
    "v_cndmask_b32 v70, v153, v28, vcc\n"
    "v_add_u32 v103, 10, v152\n"
    "v_cmp_gt_u32 vcc, 8, v103\n"
    "s_nop 1\n"
    "v_cndmask_b32 v47, v153, v28, vcc\n"
    "v_cmp_le_i32 vcc, 6, v152\n"
    "s_nop 1\n"
    "v_cndmask_b32 v71, v153, v28, vcc\n"
    "v_mov_b32 v199, 0x3f803f80\n"
    "v_cvt_pk_bf16_f32 v31, v153, v153\n"
    "ds_write_b128 v73, v[28:31]\n"
    "ds_write_b128 v73, v[28:31] offset:16384\n"
    "s_mov_b32 s86, 0xcccccccc\n"
    "s_mov_b32 s87, 0xcccccccc\n"
    "v_cvt_pk_bf16_f32 v13, 0, v12\n"
    "v_sub_f32 v12, v12, v13\n"
    "v_cvt_pk_bf16_f32 v14, 0, v12\n"
    "v_sub_f32 v12, v12, v14\n"
    "v_cvt_pk_bf16_f32 v13, v13, v14\n"
    "v_cvt_pk_bf16_f32 v14, v12, 0\n"
    "s_mov_b64 vcc, 0xffff\n"
    "v_cndmask_b32 v60, 0, v13, vcc\n"
    "v_cndmask_b32 v61, 0, v14, vcc\n"
    "v_cvt_pk_bf16_f32 v17, 0, v16\n"
    "v_sub_f32 v16, v16, v17\n"
    "v_cvt_pk_bf16_f32 v18, 0, v16\n"
    "v_sub_f32 v16, v16, v18\n"
    "v_cvt_pk_bf16_f32 v17, v17, v18\n"
    "v_cvt_pk_bf16_f32 v18, v16, 0\n"
    "s_mov_b64 vcc, 0xffff\n"
    "v_cndmask_b32 v62, 0, v17, vcc\n"
    "v_cndmask_b32 v63, 0, v18, vcc\n"
    "v_cvt_pk_bf16_f32 v21, 0, v20\n"
    "v_sub_f32 v20, v20, v21\n"
    "v_cvt_pk_bf16_f32 v22, 0, v20\n"
    "v_sub_f32 v20, v20, v22\n"
    "v_cvt_pk_bf16_f32 v21, v21, v22\n"
    "v_cvt_pk_bf16_f32 v22, v20, 0\n"
    "s_mov_b64 vcc, 0xffff\n"
    "v_cndmask_b32 v64, 0, v21, vcc\n"
    "v_cndmask_b32 v65, 0, v22, vcc\n"
    "v_cvt_pk_bf16_f32 v25, 0, v24\n"
    "v_sub_f32 v24, v24, v25\n"
    "v_cvt_pk_bf16_f32 v26, 0, v24\n"
    "v_sub_f32 v24, v24, v26\n"
    "v_cvt_pk_bf16_f32 v25, v25, v26\n"
    "v_cvt_pk_bf16_f32 v26, v24, 0\n"
    "s_mov_b64 vcc, 0xffff\n"
    "v_cndmask_b32 v66, 0, v25, vcc\n"
    "v_cndmask_b32 v67, 0, v26, vcc\n"
    "v_xor_b32 v49, 0x2000, v0\n"
    "v_xor_b32 v50, 0x4000, v0\n"
    "v_xor_b32 v51, 0x6000, v0\n"
    "v_and_b32 v52, 0xffffbfff, v0\n"
    "v_xor_b32 v53, 0x2000, v52\n"
    "s_mov_b64 s[44:45], %1\n"
    "s_and_b32 s45, s45, 0xffff\n"
    "s_mov_b32 s46, 0x0\n"
    "s_mov_b32 s47, 0x20000\n"
    "v_mov_b32 v158, 0\n"
    "s_waitcnt vmcnt(0)\n"
    "v_add_u32 v76, 1, v76\n"
    "v_add_u32 v77, 1, v77\n"
    "s_mov_b32 s52, 1048576\n"
    ".Lago_a%=:\n"
    "v_cmp_ne_u64 vcc, v[76:77], v[78:79]\n"
    "s_cbranch_vccz .Lago_x%=\n"
    "global_load_dwordx2 v[78:79], v72, %1 sc1\n"
    "s_sub_u32 s52, s52, 1\n"
    "s_waitcnt vmcnt(0)\n"
    "s_cbranch_scc0 .Lago_a%=\n"
    ".Lago_x%=:\n"
    "v_mbcnt_lo_u32_b32 v76, -1, 0\n"
    "v_mbcnt_hi_u32_b32 v76, -1, v76\n"
    "v_min_u32 v72, 5, v76\n"
    "v_lshlrev_b32 v72, 7, v72\n"
    "v_mov_b32 v11, 0x26c00\n"
    "ds_read_b128 v[76:79], v11\n"
    "s_waitcnt lgkmcnt(0)\n"
    "v_readfirstlane_b32 s49, v77\n"
    "v_add_u32 v76, v76, v72\n"
    "s_branch .Lachk_%=\n"
    ".Lago_%=:\n"
    "v_add_u32 v11, 32, v11\n"
    "s_waitcnt lgkmcnt(3)\n"
    "v_mfma_f32_16x16x32_bf16 v[24:27], a[216:219], v[80:83], 0\n"
    "ds_read_b128 v[76:79], v11\n"
    "v_mul_f32 v20, v20, v200\n"
    "v_mul_f32 v21, v21, v201\n"
    "v_mul_f32 v22, v22, v202\n"
    "s_waitcnt lgkmcnt(3)\n"
    "v_mfma_f32_16x16x32_bf16 v[24:27], a[220:223], v[84:87], v[24:27]\n"
    "ds_read_b32 v74, v11 offset:24\n"
    "v_mul_f32 v23, v23, v203\n"
    "v_cvt_pk_bf16_f32 v34, v20, v21\n"
    "v_cvt_pk_bf16_f32 v35, v22, v23\n"
    "s_waitcnt lgkmcnt(3)\n"
    "v_mfma_f32_16x16x32_bf16 v[24:27], a[224:227], v[88:91], v[24:27]\n"
    "v_rcp_f32 v40, v186\n"
    "v_mfma_f32_16x16x32_bf16 v[180:183], v[32:35], v[204:207], 0\n"
    "ds_write2st64_b64 v73, v[34:35], v[38:39] offset0:8 offset1:16\n"
    "v_mul_f32 v184, v184, v40\n"
    "v_mul_f32 v185, v185, v40\n"
    "s_waitcnt lgkmcnt(3)\n"
    "v_mfma_f32_16x16x32_bf16 v[24:27], a[228:231], v[92:95], v[24:27]\n"
    "s_xor_b32 s58, s58, 0x8000\n"
    "s_xor_b32 s59, s59, 0x8000\n"
    "v_cvt_pk_bf16_f32 v184, v184, v185\n"
    "s_waitcnt lgkmcnt(2)\n"
    "v_add_u32 v76, v76, v72\n"
    "v_mfma_f32_16x16x32_bf16 v[12:15], a[0:3], v[80:83], 0\n"
    "v_readfirstlane_b32 s49, v77\n"
    "v_mul_f32 v180, v180, v40\n"
    "v_mul_f32 v181, v181, v40\n"
    "v_mfma_f32_16x16x32_bf16 v[16:19], a[72:75], v[80:83], 0\n"
    "s_waitcnt vmcnt(2)\n"
    "s_barrier\n"
    "ds_read_b128 v[112:115], v50\n"
    "v_mul_f32 v182, v182, v40\n"
    "v_mul_f32 v183, v183, v40\n"
    "v_mfma_f32_16x16x32_bf16 v[12:15], a[4:7], v[84:87], v[12:15]\n"
    "ds_read2st64_b32 v[160:161], v7 offset1:1\n"
    "v_cvt_pk_bf16_f32 v204, v180, v181\n"
    "v_cvt_pk_bf16_f32 v206, v180, v181\n"
    "v_mfma_f32_16x16x32_bf16 v[16:19], a[76:79], v[84:87], v[16:19]\n"
    "ds_read2st64_b32 v[162:163], v7 offset0:2 offset1:3\n"
    "v_cvt_pk_bf16_f32 v205, v182, v183\n"
    "v_cvt_pk_bf16_f32 v207, v182, v183\n"
    "v_mfma_f32_16x16x32_bf16 v[12:15], a[8:11], v[88:91], v[12:15]\n"
    "ds_read2st64_b32 v[164:165], v7 offset0:4 offset1:5\n"
    "v_permlane16_swap_b32 v204, v206\n"
    "v_mfma_f32_16x16x32_bf16 v[16:19], a[80:83], v[88:91], v[16:19]\n"
    "ds_read2st64_b32 v[166:167], v7 offset0:6 offset1:7\n"
    "v_permlane16_swap_b32 v205, v207\n"
    "v_mfma_f32_16x16x32_bf16 v[12:15], a[12:15], v[92:95], v[12:15]\n"
    "ds_read_b32 v168, v7 offset:2048\n"
    "v_mov_b32_dpp v204, v184 quad_perm:[0,0,0,0] row_mask:0x2 bank_mask:0xf\n"
    "v_mov_b32_dpp v206, v184 quad_perm:[2,2,2,2] row_mask:0x2 bank_mask:0xf\n"
    "v_mfma_f32_16x16x32_bf16 v[16:19], a[84:87], v[92:95], v[16:19]\n"
    "ds_read_b128 v[116:119], v50 offset:1024\n"
    "v_mov_b32_dpp v205, v184 quad_perm:[1,1,1,1] row_mask:0x2 bank_mask:0xf\n"
    "v_mov_b32_dpp v207, v184 quad_perm:[3,3,3,3] row_mask:0x2 bank_mask:0xf\n"
    "s_waitcnt lgkmcnt(6)\n"
    "v_mfma_f32_16x16x32_bf16 v[24:27], a[232:235], v[112:115], v[24:27]\n"
    "buffer_store_dwordx4 v[204:207], v1, s[40:43], s62 offen sc1\n"
    "v_add_u32 v73, v74, v3\n"
    "s_waitcnt lgkmcnt(5)\n"
    "v_add_f32 v160, v160, v161\n"
    "v_mfma_f32_16x16x32_bf16 v[12:15], a[32:35], v[112:115], v[12:15]\n"
    "ds_read_b128 v[120:123], v50 offset:2048\n"
    "s_waitcnt lgkmcnt(5)\n"
    "v_add_f32 v162, v162, v163\n"
    "s_waitcnt lgkmcnt(4)\n"
    "v_add_f32 v164, v164, v165\n"
    "v_mfma_f32_16x16x32_bf16 v[16:19], a[104:107], v[112:115], v[16:19]\n"
    "ds_read_b128 v[124:127], v50 offset:3072\n"
    "s_waitcnt lgkmcnt(4)\n"
    "v_add_f32 v166, v166, v167\n"
    "v_add_f32 v160, v160, v162\n"
    ".Latop_%=:\n"
    "s_waitcnt lgkmcnt(2)\n"
    "v_mfma_f32_16x16x32_bf16 v[24:27], a[236:239], v[116:119], v[24:27]\n"
    "ds_read_b128 v[144:147], v52 offset:4096\n"
    "v_add_f32 v164, v164, v166\n"
    "v_add_f32 v160, v160, v168\n"
    "v_mfma_f32_16x16x32_bf16 v[12:15], a[36:39], v[116:119], v[12:15]\n"
    "ds_read_b128 v[96:99], v49\n"
    "v_add_f32 v168, v160, v164\n"
    "v_add_f32 v160, v160, v164\n"
    "v_mfma_f32_16x16x32_bf16 v[16:19], a[108:111], v[116:119], v[16:19]\n"
    "ds_read_b128 v[100:103], v49 offset:1024\n"
    "v_permlane32_swap_b32 v160, v168\n"
    "s_waitcnt lgkmcnt(4)\n"
    "v_mfma_f32_16x16x32_bf16 v[24:27], a[240:243], v[120:123], v[24:27]\n"
    "ds_read_b128 v[104:107], v49 offset:2048\n"
    "v_add_f32 v161, v160, v168\n"
    "v_add_f32 v168, v168, v160\n"
    "v_mfma_f32_16x16x32_bf16 v[12:15], a[40:43], v[120:123], v[12:15]\n"
    "ds_read_b128 v[108:111], v49 offset:3072\n"
    "v_permlane16_swap_b32 v161, v168\n"
    "v_mfma_f32_16x16x32_bf16 v[16:19], a[112:115], v[120:123], v[16:19]\n"
    "ds_read_b128 v[128:131], v51\n"
    "v_mul_f32 v160, 0xbae38e39, v161\n"
    "v_mul_f32 v168, 0x3ae38e39, v168\n"
    "s_waitcnt lgkmcnt(6)\n"
    "v_mfma_f32_16x16x32_bf16 v[24:27], a[244:247], v[124:127], v[24:27]\n"
    "ds_read_b128 v[132:135], v51 offset:1024\n"
    "s_waitcnt vmcnt(2)\n"
    "v_cmp_eq_u32 vcc, 0, v158\n"
    "v_mfma_f32_16x16x32_bf16 v[12:15], a[44:47], v[124:127], v[12:15]\n"
    "ds_read_b128 v[136:139], v51 offset:2048\n"
    "s_cmp_eq_u64 vcc, 0\n"
    "s_cselect_b32 s46, 0, 0x20000\n"
    "s_cselect_b32 s94, s42, 0\n"
    "v_mfma_f32_16x16x32_bf16 v[16:19], a[116:119], v[124:127], v[16:19]\n"
    "buffer_load_dword v55, v76, s[44:47], 0 offen sc1\n"
    "s_cselect_b32 s90, s96, 0\n"
    "v_cvt_pk_bf16_f32 v216, v160, v160\n"
    "s_waitcnt lgkmcnt(7)\n"
    "v_mfma_f32_16x16x32_bf16 v[24:27], a[248:251], v[144:147], v[24:27]\n"
    "ds_read_b128 v[140:143], v51 offset:3072\n"
    "s_mov_b32 m0, s58\n"
    "v_cvt_pk_bf16_f32 v217, v160, v160\n"
    "v_mfma_f32_16x16x32_bf16 v[12:15], a[64:67], v[144:147], v[12:15]\n"
    "buffer_load_dwordx4 v2, s[92:95], s49 offen sc1 lds\n"
    "v_fma_f32 v168, -v160, v160, v168\n"
    "v_max_f32 v168, 0, v168\n"
    "v_mfma_f32_16x16x32_bf16 v[24:27], v[66:69], v[216:219], v[24:27]\n"
    "ds_read_b32 v158, v11 offset:32\n"
    "v_cvt_pk_bf16_f32 v168, 0, v168\n"
    "v_add_f32 v168, 0x3727c5ac, v168\n"
    "v_mfma_f32_16x16x32_bf16 v[16:19], a[136:139], v[144:147], v[16:19]\n"
    "ds_read_b128 v[148:151], v53 offset:4096\n"
    "v_cvt_pk_bf16_f32 v168, 0, v168\n"
    "v_sqrt_f32 v168, v168\n"
    "s_waitcnt lgkmcnt(9)\n"
    "v_mfma_f32_16x16x32_bf16 v[12:15], a[16:19], v[96:99], v[12:15]\n"
    "buffer_load_dwordx4 v2, s[92:95], s49 offen offset:1024 sc1 lds\n"
    "v_cvt_pk_bf16_f32 v168, 0, v168\n"
    "v_mfma_f32_16x16x32_bf16 v[12:15], v[60:63], v[216:219], v[12:15]\n"
    "ds_read_b128 v[28:31], v73\n"
    "v_rcp_f32 v168, v168\n"
    "s_nop 0\n"
    "v_mul_f32 v24, v24, v168\n"
    "v_mfma_f32_16x16x32_bf16 v[16:19], a[88:91], v[96:99], v[16:19]\n"
    "ds_read_b64 v[32:33], v73 offset:4096\n"
    "v_mul_f32 v25, v25, v168\n"
    "v_mul_f32 v26, v26, v168\n"
    "v_mul_f32 v27, v27, v168\n"
    "v_mfma_f32_16x16x32_bf16 v[16:19], v[62:65], v[216:219], v[16:19]\n"
    "ds_write_b128 v9, v[24:27]\n"
    "s_waitcnt lgkmcnt(4)\n"
    "v_add_u32 v158, v158, v72\n"
    "v_mfma_f32_16x16x32_bf16 v[12:15], a[20:23], v[100:103], v[12:15]\n"
    "buffer_load_dwordx4 v2, s[92:95], s49 offen offset:2048 sc1 lds\n"
    "v_mfma_f32_16x16x32_bf16 v[16:19], a[92:95], v[100:103], v[16:19]\n"
    "ds_write_b32 v7, v168 offset:2304\n"
    "v_readfirstlane_b32 s62, v78\n"
    "v_mfma_f32_16x16x32_bf16 v[12:15], a[24:27], v[104:107], v[12:15]\n"
    "ds_read_b128 v[200:203], v8\n"
    "v_mfma_f32_16x16x32_bf16 v[16:19], a[96:99], v[104:107], v[16:19]\n"
    "buffer_load_dwordx4 v2, s[92:95], s49 offen offset:3072 sc1 lds\n"
    "v_xor_b32 v7, 0x8000, v7\n"
    "v_xor_b32 v0, 0x8000, v0\n"
    "v_mfma_f32_16x16x32_bf16 v[12:15], a[28:31], v[108:111], v[12:15]\n"
    "ds_read_b64 v[36:37], v73 offset:8192\n"
    "v_xor_b32 v8, 0x8000, v8\n"
    "v_xor_b32 v49, 0x8000, v49\n"
    "v_mfma_f32_16x16x32_bf16 v[16:19], a[100:103], v[108:111], v[16:19]\n"
    "s_waitcnt lgkmcnt(0)\n"
    "s_barrier\n"
    "ds_read2st64_b64 v[180:183], v4 offset1:2\n"
    "s_bitset1_b32 m0, 12\n"
    "v_xor_b32 v50, 0x8000, v50\n"
    "v_mfma_f32_16x16x32_bf16 v[12:15], a[48:51], v[128:131], v[12:15]\n"
    "buffer_load_dwordx4 v3, s[88:91], s49 offen sc1 lds\n"
    "v_xor_b32 v51, 0x8000, v51\n"
    "v_xor_b32 v52, 0x8000, v52\n"
    "v_mfma_f32_16x16x32_bf16 v[16:19], a[120:123], v[128:131], v[16:19]\n"
    "ds_read2st64_b64 v[184:187], v5 offset1:2\n"
    "s_mov_b32 m0, s59\n"
    "s_mov_b32 s55, s49\n"
    "s_bitset1_b32 s55, 15\n"
    "v_mfma_f32_16x16x32_bf16 v[12:15], a[52:55], v[132:135], v[12:15]\n"
    "ds_read2_b32 v[188:189], v6 offset1:4\n"
    "v_xor_b32 v53, 0x8000, v53\n"
    "v_mfma_f32_16x16x32_bf16 v[16:19], a[124:127], v[132:135], v[16:19]\n"
    "s_mov_b64 exec, s[60:61]\n"
    "buffer_load_dwordx4 v3, s[92:95], s55 offen offset:2048 sc1 lds\n"
    "s_mov_b64 exec, -1\n"
    "v_mfma_f32_16x16x32_bf16 v[12:15], a[56:59], v[136:139], v[12:15]\n"
    "ds_read2_b32 v[192:193], v10 offset1:4\n"
    "v_mfma_f32_16x16x32_bf16 v[16:19], a[128:131], v[136:139], v[16:19]\n"
    "ds_read2_b32 v[190:191], v6 offset0:8 offset1:12\n"
    "s_waitcnt lgkmcnt(4)\n"
    "v_add_f32 v180, v180, v182\n"
    "v_add_f32 v181, v181, v183\n"
    "v_mfma_f32_16x16x32_bf16 v[12:15], a[60:63], v[140:143], v[12:15]\n"
    "ds_read2_b32 v[194:195], v10 offset0:8 offset1:12\n"
    "v_cvt_pk_bf16_f32 v198, v180, v181\n"
    "s_waitcnt lgkmcnt(4)\n"
    "v_add_f32 v184, v184, v186\n"
    "v_mfma_f32_16x16x32_bf16 v[16:19], a[132:135], v[140:143], v[16:19]\n"
    "s_cmp_eq_u32 s94, 0\n"
    "s_cbranch_scc1 .Lak10_%=\n"
    ".Lak10_x%=:\n"
    "v_add_f32 v185, v185, v187\n"
    "v_cvt_pk_bf16_f32 v214, v184, v185\n"
    "v_mfma_f32_16x16x32_bf16 v[12:15], a[68:71], v[148:151], v[12:15]\n"
    "v_mfma_f32_16x16x32_bf16 v[16:19], a[140:143], v[148:151], v[16:19]\n"
    "s_waitcnt lgkmcnt(2)\n"
    "v_add_f32 v188, v188, v192\n"
    "v_add_f32 v189, v189, v193\n"
    "s_waitcnt lgkmcnt(0)\n"
    "v_add_f32 v190, v190, v194\n"
    "v_mfma_f32_16x16x32_bf16 v[20:23], v[216:219], v[64:67], 0\n"
    "v_mul_f32 v12, v12, v168\n"
    "v_mul_f32 v13, v13, v168\n"
    "v_mul_f32 v14, v14, v168\n"
    "v_mfma_f32_16x16x32_bf16 v[20:23], v[80:83], a[144:147], v[20:23]\n"
    "v_mov_b32 v54, 1\n"
    "v_mul_f32 v15, v15, v168\n"
    "v_cvt_pk_bf16_f32 v196, v12, v13\n"
    "v_mfma_f32_16x16x32_bf16 v[20:23], v[84:87], a[148:151], v[20:23]\n"
    "s_waitcnt vmcnt(7)\n"
    "v_cvt_pk_bf16_f32 v197, v14, v15\n"
    "v_mul_f32 v16, v16, v168\n"
    "v_mul_f32 v17, v17, v168\n"
    "v_mfma_f32_16x16x32_bf16 v[204:207], v[28:31], v[196:199], v[68:71]\n"
    "s_mov_b64 exec, s[56:57]\n"
    "ds_add_rtn_u32 v54, v11, v54 offset:16\n"
    "s_mov_b64 exec, -1\n"
    "v_mul_f32 v18, v18, v168\n"
    "v_mfma_f32_16x16x32_bf16 v[20:23], v[88:91], a[152:155], v[20:23]\n"
    "v_mul_f32 v19, v19, v168\n"
    "v_cvt_pk_bf16_f32 v212, v16, v17\n"
    "v_cvt_pk_bf16_f32 v213, v18, v19\n"
    "v_mfma_f32_16x16x32_bf16 v[20:23], v[92:95], a[156:159], v[20:23]\n"
    "v_add_f32 v191, v191, v195\n"
    "v_cvt_pk_bf16_f32 v38, v188, v189\n"
    "v_cvt_pk_bf16_f32 v39, v190, v191\n"
    "v_mfma_f32_16x16x32_bf16 v[208:211], v[212:215], v[196:199], v[44:47]\n"
    "ds_write_b128 v73, v[212:215]\n"
    "v_maximum3_f32 v40, v204, v205, v206\n"
    "v_cndmask_b32 v38, v38, v56, s[86:87]\n"
    "v_mfma_f32_16x16x32_bf16 v[20:23], v[96:99], a[160:163], v[20:23]\n"
    "global_load_dword v158, v158, %1 sc1\n"
    "v_cndmask_b32 v39, v39, v56, s[86:87]\n"
    "v_mfma_f32_16x16x32_bf16 v[20:23], v[100:103], a[164:167], v[20:23]\n"
    "s_waitcnt lgkmcnt(1)\n"
    "v_readfirstlane_b32 s50, v54\n"
    "v_maximum3_f32 v40, v40, v207, v208\n"
    "v_maximum3_f32 v40, v40, v209, v210\n"
    "v_max_f32 v41, v40, v211\n"
    "v_mfma_f32_16x16x32_bf16 v[20:23], v[104:107], a[168:171], v[20:23]\n"
    "s_cmp_eq_u32 s50, 3\n"
    "s_cselect_b32 s46, 0x20000, 0\n"
    "v_max_f32 v40, v40, v211\n"
    "v_mfma_f32_16x16x32_bf16 v[20:23], v[108:111], a[172:175], v[20:23]\n"
    "buffer_store_dword v54, v79, s[44:47], 0 offen sc1\n"
    "v_permlane16_swap_b32 v41, v40\n"
    "v_max_f32 v42, v40, v41\n"
    "v_max_f32 v40, v40, v41\n"
    "v_mfma_f32_16x16x32_bf16 v[20:23], v[112:115], a[176:179], v[20:23]\n"
    "s_nop 0\n"
    "v_permlane32_swap_b32 v42, v40\n"
    "v_max_f32 v40, v40, v42\n"
    "v_mul_f32 v40, 0xbeae1a93, v40\n"
    "v_fmamk_f32 v204, v204, 0x3eae1a93, v40\n"
    "v_mfma_f32_16x16x32_bf16 v[20:23], v[116:119], a[180:183], v[20:23]\n"
    "v_fmamk_f32 v205, v205, 0x3eae1a93, v40\n"
    "v_exp_f32 v204, v204\n"
    "v_exp_f32 v205, v205\n"
    "v_mfma_f32_16x16x32_bf16 v[20:23], v[120:123], a[184:187], v[20:23]\n"
    "v_fmamk_f32 v206, v206, 0x3eae1a93, v40\n"
    "v_fmamk_f32 v207, v207, 0x3eae1a93, v40\n"
    "v_exp_f32 v206, v206\n"
    "v_mfma_f32_16x16x32_bf16 v[20:23], v[124:127], a[188:191], v[20:23]\n"
    "v_exp_f32 v207, v207\n"
    "v_fmamk_f32 v208, v208, 0x3eae1a93, v40\n"
    "v_mfma_f32_16x16x32_bf16 v[20:23], v[128:131], a[192:195], v[20:23]\n"
    "v_fmamk_f32 v209, v209, 0x3eae1a93, v40\n"
    "v_exp_f32 v208, v208\n"
    "v_exp_f32 v209, v209\n"
    "v_mfma_f32_16x16x32_bf16 v[20:23], v[132:135], a[196:199], v[20:23]\n"
    "v_fmamk_f32 v210, v210, 0x3eae1a93, v40\n"
    "v_fmamk_f32 v211, v211, 0x3eae1a93, v40\n"
    "v_exp_f32 v210, v210\n"
    "v_mfma_f32_16x16x32_bf16 v[20:23], v[136:139], a[200:203], v[20:23]\n"
    "s_waitcnt vmcnt(4)\n"
    "ds_read_b128 v[80:83], v0\n"
    "v_exp_f32 v211, v211\n"
    "v_cvt_pk_bf16_f32 v204, v204, v205\n"
    "v_mfma_f32_16x16x32_bf16 v[20:23], v[140:143], a[204:207], v[20:23]\n"
    "ds_read_b128 v[84:87], v0 offset:1024\n"
    "v_cvt_pk_bf16_f32 v205, v206, v207\n"
    "v_cvt_pk_bf16_f32 v206, v208, v209\n"
    "v_cvt_pk_bf16_f32 v207, v210, v211\n"
    "v_mfma_f32_16x16x32_bf16 v[20:23], v[144:147], a[208:211], v[20:23]\n"
    "ds_read_b128 v[88:91], v0 offset:2048\n"
    "s_mov_b32 s56, 1\n"
    "s_cmp_lg_u32 s94, 0\n"
    "v_mfma_f32_16x16x32_bf16 v[20:23], v[148:151], a[212:215], v[20:23]\n"
    "ds_read_b128 v[92:95], v0 offset:3072\n"
    "v_mfma_f32_16x16x32_bf16 v[184:187], v[36:39], v[204:207], 0\n"
    "s_cbranch_scc1 .Lago_%=\n"
    "ds_read_b32 v79, v11 offset:44\n"
    "s_nop 2\n"
    "v_mul_f32 v20, v20, v200\n"
    "v_mul_f32 v21, v21, v201\n"
    "v_mul_f32 v22, v22, v202\n"
    "v_mul_f32 v23, v23, v203\n"
    "v_cvt_pk_bf16_f32 v34, v20, v21\n"
    "v_cvt_pk_bf16_f32 v35, v22, v23\n"
    "v_rcp_f32 v40, v186\n"
    "s_nop 0\n"
    "v_mfma_f32_16x16x32_bf16 v[180:183], v[32:35], v[204:207], 0\n"
    "ds_write2st64_b64 v73, v[34:35], v[38:39] offset0:8 offset1:16\n"
    "v_mul_f32 v184, v184, v40\n"
    "v_mul_f32 v185, v185, v40\n"
    "v_cvt_pk_bf16_f32 v184, v184, v185\n"
    "s_nop 4\n"
    "v_mul_f32 v180, v180, v40\n"
    "v_mul_f32 v181, v181, v40\n"
    "v_mul_f32 v182, v182, v40\n"
    "v_mul_f32 v183, v183, v40\n"
    "v_cvt_pk_bf16_f32 v204, v180, v181\n"
    "v_cvt_pk_bf16_f32 v206, v180, v181\n"
    "v_cvt_pk_bf16_f32 v205, v182, v183\n"
    "v_cvt_pk_bf16_f32 v207, v182, v183\n"
    "v_permlane16_swap_b32 v204, v206\n"
    "s_nop 0\n"
    "v_permlane16_swap_b32 v205, v207\n"
    "v_mov_b32_dpp v204, v184 quad_perm:[0,0,0,0] row_mask:0x2 bank_mask:0xf\n"
    "v_mov_b32_dpp v206, v184 quad_perm:[2,2,2,2] row_mask:0x2 bank_mask:0xf\n"
    "v_mov_b32_dpp v205, v184 quad_perm:[1,1,1,1] row_mask:0x2 bank_mask:0xf\n"
    "v_mov_b32_dpp v207, v184 quad_perm:[3,3,3,3] row_mask:0x2 bank_mask:0xf\n"
    "buffer_store_dwordx4 v[204:207], v1, s[40:43], s62 offen sc1\n"
    "s_waitcnt vmcnt(7) lgkmcnt(0)\n"
    "v_cmp_eq_u32 vcc, 0, v55\n"
    "s_cmp_eq_u64 vcc, 0\n"
    "s_cbranch_scc1 .Lald_%=\n"
    "global_load_dword v55, v76, %1 sc1\n"
    "s_waitcnt vmcnt(0)\n"
    "v_mov_b32 v54, 1\n"
    "s_mov_b64 exec, s[56:57]\n"
    "ds_add_rtn_u32 v54, v11, v54 offset:48\n"
    "s_mov_b64 exec, -1\n"
    "s_waitcnt lgkmcnt(0)\n"
    "v_readfirstlane_b32 s50, v54\n"
    "s_cmp_eq_u32 s50, 3\n"
    "s_cselect_b32 s46, 0x20000, 0\n"
    "buffer_store_dword v54, v79, s[44:47], 0 offen sc1\n"
    "s_mov_b32 s56, 0\n"
    "v_cmp_eq_u32 vcc, 0, v55\n"
    "s_cmp_eq_u64 vcc, 0\n"
    "s_cbranch_scc1 .Lald_%=\n"
    ".Lachk_%=:\n"
    "v_readfirstlane_b32 s50, v76\n"
    "s_cmp_lt_u32 s50, 200\n"
    "s_cbranch_scc1 .Laend_%=\n"
    ".Lawait_%=:\n"
    "global_load_dword v55, v76, %1 sc1\n"
    "s_mov_b32 s52, 0\n"
    ".Lap11_%=:\n"
    "s_waitcnt vmcnt(0)\n"
    "v_cmp_eq_u32 vcc, 0, v55\n"
    "s_cmp_eq_u64 vcc, 0\n"
    "s_cbranch_scc1 .Lap11_x%=\n"
    "s_add_u32 s52, s52, 1\n"
    "s_sleep 2\n"
    "global_load_dword v55, v76, %1 sc1\n"
    "s_cmp_lt_u32 s52, s53\n"
    "s_cbranch_scc1 .Lap11_%=\n"
    "s_mov_b32 s53, 0\n"
    "v_mov_b32 v78, 0\n"
    "v_mov_b32 v79, 1\n"
    "global_store_dword v78, v79, %1 sc1\n"
    "s_waitcnt vmcnt(0)\n"
    ".Lap11_x%=:\n"
    ".Lald_%=:\n"
    "s_mov_b32 s94, s42\n"
    "s_mov_b32 s90, s96\n"
    "s_mov_b32 m0, s58\n"
    "s_nop 0\n"
    "buffer_load_dwordx4 v2, s[92:95], s49 offen sc1 lds\n"
    "buffer_load_dwordx4 v2, s[92:95], s49 offen offset:1024 sc1 lds\n"
    "buffer_load_dwordx4 v2, s[92:95], s49 offen offset:2048 sc1 lds\n"
    "buffer_load_dwordx4 v2, s[92:95], s49 offen offset:3072 sc1 lds\n"
    "s_bitset1_b32 m0, 12\n"
    "s_nop 0\n"
    "buffer_load_dwordx4 v3, s[88:91], s49 offen sc1 lds\n"
    "s_mov_b32 m0, s59\n"
    "s_mov_b32 s55, s49\n"
    "s_bitset1_b32 s55, 15\n"
    "s_mov_b64 exec, s[60:61]\n"
    "buffer_load_dwordx4 v3, s[92:95], s55 offen offset:2048 sc1 lds\n"
    "s_mov_b64 exec, -1\n"
    "s_waitcnt vmcnt(6)\n"
    "v_mov_b32 v54, 1\n"
    "s_mov_b64 exec, s[56:57]\n"
    "ds_add_rtn_u32 v54, v11, v54 offset:48\n"
    "s_mov_b64 exec, -1\n"
    "s_waitcnt lgkmcnt(0)\n"
    "v_readfirstlane_b32 s50, v54\n"
    "s_cmp_eq_u32 s50, 3\n"
    "s_cselect_b32 s46, 0x20000, 0\n"
    "buffer_store_dword v54, v79, s[44:47], 0 offen sc1\n"
    "s_mov_b32 s56, 0\n"
    "s_waitcnt vmcnt(3)\n"
    "ds_read_b128 v[80:83], v0\n"
    "ds_read_b128 v[84:87], v0 offset:1024\n"
    "ds_read_b128 v[88:91], v0 offset:2048\n"
    "ds_read_b128 v[92:95], v0 offset:3072\n"
    "s_waitcnt vmcnt(1)\n"
    "v_add_u32 v11, 32, v11\n"
    "s_waitcnt lgkmcnt(3)\n"
    "v_mfma_f32_16x16x32_bf16 v[24:27], a[216:219], v[80:83], 0\n"
    "ds_read_b128 v[76:79], v11\n"
    "s_waitcnt lgkmcnt(3)\n"
    "v_mfma_f32_16x16x32_bf16 v[24:27], a[220:223], v[84:87], v[24:27]\n"
    "ds_read_b32 v74, v11 offset:24\n"
    "s_waitcnt lgkmcnt(3)\n"
    "v_mfma_f32_16x16x32_bf16 v[24:27], a[224:227], v[88:91], v[24:27]\n"
    "s_waitcnt lgkmcnt(2)\n"
    "v_mfma_f32_16x16x32_bf16 v[24:27], a[228:231], v[92:95], v[24:27]\n"
    "s_xor_b32 s58, s58, 0x8000\n"
    "s_xor_b32 s59, s59, 0x8000\n"
    "s_waitcnt lgkmcnt(1)\n"
    "v_add_u32 v76, v76, v72\n"
    "v_mfma_f32_16x16x32_bf16 v[12:15], a[0:3], v[80:83], 0\n"
    "v_readfirstlane_b32 s49, v77\n"
    "v_mfma_f32_16x16x32_bf16 v[16:19], a[72:75], v[80:83], 0\n"
    "s_barrier\n"
    "ds_read_b128 v[112:115], v50\n"
    "v_mfma_f32_16x16x32_bf16 v[12:15], a[4:7], v[84:87], v[12:15]\n"
    "ds_read2st64_b32 v[160:161], v7 offset1:1\n"
    "v_mfma_f32_16x16x32_bf16 v[16:19], a[76:79], v[84:87], v[16:19]\n"
    "ds_read2st64_b32 v[162:163], v7 offset0:2 offset1:3\n"
    "v_mfma_f32_16x16x32_bf16 v[12:15], a[8:11], v[88:91], v[12:15]\n"
    "ds_read2st64_b32 v[164:165], v7 offset0:4 offset1:5\n"
    "v_mfma_f32_16x16x32_bf16 v[16:19], a[80:83], v[88:91], v[16:19]\n"
    "ds_read2st64_b32 v[166:167], v7 offset0:6 offset1:7\n"
    "v_mfma_f32_16x16x32_bf16 v[12:15], a[12:15], v[92:95], v[12:15]\n"
    "ds_read_b32 v168, v7 offset:2048\n"
    "v_mfma_f32_16x16x32_bf16 v[16:19], a[84:87], v[92:95], v[16:19]\n"
    "ds_read_b128 v[116:119], v50 offset:1024\n"
    "s_waitcnt lgkmcnt(6)\n"
    "v_mfma_f32_16x16x32_bf16 v[24:27], a[232:235], v[112:115], v[24:27]\n"
    "v_add_u32 v73, v74, v3\n"
    "s_waitcnt lgkmcnt(5)\n"
    "v_add_f32 v160, v160, v161\n"
    "v_mfma_f32_16x16x32_bf16 v[12:15], a[32:35], v[112:115], v[12:15]\n"
    "ds_read_b128 v[120:123], v50 offset:2048\n"
    "s_waitcnt lgkmcnt(5)\n"
    "v_add_f32 v162, v162, v163\n"
    "s_waitcnt lgkmcnt(4)\n"
    "v_add_f32 v164, v164, v165\n"
    "v_mfma_f32_16x16x32_bf16 v[16:19], a[104:107], v[112:115], v[16:19]\n"
    "ds_read_b128 v[124:127], v50 offset:3072\n"
    "s_waitcnt lgkmcnt(4)\n"
    "v_add_f32 v166, v166, v167\n"
    "v_add_f32 v160, v160, v162\n"
    "s_waitcnt lgkmcnt(0)\n"
    "s_branch .Latop_%=\n"
    ".Lak10_%=:\n"
    "s_waitcnt vmcnt(6)\n"
    "v_cmp_eq_u32 vcc, 0, v55\n"
    "s_cmp_eq_u64 vcc, 0\n"
    "s_cselect_b32 s46, 0, 0x20000\n"
    "s_cselect_b32 s94, s42, 0\n"
    "s_cselect_b32 s90, s96, 0\n"
    "buffer_load_dword v55, v76, s[44:47], 0 offen sc1\n"
    "s_mov_b32 m0, s58\n"
    "s_nop 0\n"
    "buffer_load_dwordx4 v2, s[92:95], s49 offen sc1 lds\n"
    "buffer_load_dwordx4 v2, s[92:95], s49 offen offset:1024 sc1 lds\n"
    "buffer_load_dwordx4 v2, s[92:95], s49 offen offset:2048 sc1 lds\n"
    "buffer_load_dwordx4 v2, s[92:95], s49 offen offset:3072 sc1 lds\n"
    "s_bitset1_b32 m0, 12\n"
    "s_nop 0\n"
    "buffer_load_dwordx4 v3, s[88:91], s49 offen sc1 lds\n"
    "s_mov_b32 m0, s59\n"
    "s_mov_b32 s55, s49\n"
    "s_bitset1_b32 s55, 15\n"
    "s_mov_b64 exec, s[60:61]\n"
    "buffer_load_dwordx4 v3, s[92:95], s55 offen offset:2048 sc1 lds\n"
    "s_mov_b64 exec, -1\n"
    "s_branch .Lak10_x%=\n"
    ".Lapol_%=:\n"
    "v_mov_b32 v76, 64\n"
    "global_load_dword v55, v76, %1 sc1\n"
    "s_mov_b32 s52, 0\n"
    ".Lap12_%=:\n"
    "s_waitcnt vmcnt(0)\n"
    "v_cmp_eq_u32 vcc, 0, v55\n"
    "s_cmp_eq_u64 vcc, 0\n"
    "s_cbranch_scc1 .Lap12_x%=\n"
    "s_add_u32 s52, s52, 1\n"
    "s_sleep 2\n"
    "global_load_dword v55, v76, %1 sc1\n"
    "s_cmp_lt_u32 s52, s53\n"
    "s_cbranch_scc1 .Lap12_%=\n"
    "s_mov_b32 s53, 0\n"
    "v_mov_b32 v78, 0\n"
    "v_mov_b32 v79, 1\n"
    "global_store_dword v78, v79, %1 sc1\n"
    "s_waitcnt vmcnt(0)\n"
    ".Lap12_x%=:\n"
    "v_readfirstlane_b32 s50, v55\n"
    "v_mbcnt_lo_u32_b32 v76, -1, 0\n"
    "v_mbcnt_hi_u32_b32 v76, -1, v76\n"
    "s_lshl_b32 s51, %2, 6\n"
    "v_add_u32 v76, s51, v76\n"
    "v_min_u32 v76, 129, v76\n"
    "v_lshlrev_b32 v76, 5, v76\n"
    "v_add_u32 v76, 0x26c00, v76\n"
    "s_and_b32 s50, s50, 1\n"
    "ds_read_b64 v[78:79], v76\n"
    "s_waitcnt lgkmcnt(0)\n"
    "v_and_b32 v77, s50, v78\n"
    "v_lshlrev_b32 v77, 13, v77\n"
    "v_and_b32 v78, -2, v78\n"
    "v_xor_b32 v78, v77, v78\n"
    "v_and_b32 v77, s50, v79\n"
    "v_lshlrev_b32 v77, 13, v77\n"
    "v_and_b32 v79, -2, v79\n"
    "v_xor_b32 v79, v77, v79\n"
    "ds_write_b64 v76, v[78:79]\n"
    "ds_read_b64 v[78:79], v76 offset:8\n"
    "s_waitcnt lgkmcnt(0)\n"
    "v_and_b32 v77, s50, v78\n"
    "v_lshlrev_b32 v77, 13, v77\n"
    "v_and_b32 v78, -2, v78\n"
    "v_xor_b32 v78, v77, v78\n"
    "v_and_b32 v77, s50, v79\n"
    "v_lshlrev_b32 v77, 13, v77\n"
    "v_and_b32 v79, -2, v79\n"
    "v_xor_b32 v79, v77, v79\n"
    "ds_write_b64 v76, v[78:79] offset:8\n"
    "s_waitcnt lgkmcnt(0)\n"
    "s_barrier\n"
    "s_and_b32 s50, s50, %4\n"
    "s_lshl_b32 s50, s50, 13\n"
    "s_and_b32 s51, %4, -2\n"
    "s_xor_b32 s50, s50, s51\n"
    "ds_read_b32 v77, v11 offset:4\n"
    "v_add_u32 v76, s50, v72\n"
    "s_waitcnt lgkmcnt(0)\n"
    "v_readfirstlane_b32 s49, v77\n"
    "s_branch .Lawait_%=\n"
    ".Laend_%=:\n"
    "s_cmp_eq_u32 s50, 192\n"
    "s_cbranch_scc0 .Lapol_%=\n"
    "s_waitcnt vmcnt(0) lgkmcnt(0)\n"
    "s_endpgm\n"
    ".p2align 6\n"
    ".Lrole4_%=:\n"
    "s_mov_b64 s[40:41], %0\n"
    "s_and_b32 s41, s41, 0xffff\n"
    "s_mov_b32 s42, 0xc000000\n"
    "s_mov_b32 s43, 0x20000\n"
    "s_mov_b64 s[92:93], %0\n"
    "s_and_b32 s93, s93, 0xffff\n"
    "s_mov_b32 s94, 0xc000000\n"
    "s_mov_b32 s95, 0x20000\n"
    "s_mov_b32 s53, 131072\n"
    "s_mov_b64 s[56:57], 0\n"
    "v_mbcnt_lo_u32_b32 v20, -1, 0\n"
    "v_mbcnt_hi_u32_b32 v20, -1, v20\n"
    "s_lshl_b32 s50, %2, 8\n"
    "s_add_u32 s50, s50, 147456\n"
    "v_lshl_add_u32 v20, v20, 2, s50\n"
    "ds_read_b32 v0, v20\n"
    "ds_read_b32 v1, v20 offset:1024\n"
    "ds_read_b32 v2, v20 offset:2048\n"
    "ds_read_b32 v3, v20 offset:3072\n"
    "ds_read_b32 v4, v20 offset:4096\n"
    "ds_read_b32 v5, v20 offset:5120\n"
    "ds_read_b32 v8, v20 offset:6144\n"
    "ds_read_b32 v9, v20 offset:7168\n"
    "ds_read_b32 v10, v20 offset:8192\n"
    "s_waitcnt lgkmcnt(0)\n"
    "s_barrier\n"
    "s_lshl_b32 s54, %2, 10\n"
    "s_lshl_b32 s58, %2, 13\n"
    "s_add_u32 s58, s58, 0\n"
    "s_add_u32 s88, s40, 32768\n"
    "s_mov_b32 s89, s41\n"
    "s_cmp_lt_u32 %2, 2\n"
    "s_cselect_b32 s96, s42, 0\n"
    "s_mov_b32 s91, s43\n"
    "s_mul_i32 s59, %2, 576\n"
    "s_add_u32 s59, s59, 3072\n"
    "s_bfm_b64 s[60:61], 36, 0\n"
    "s_mov_b32 s86, 0x3f803f80\n"
    "s_mov_b64 s[44:45], %8\n"
    "s_and_b32 s45, s45, 0xffff\n"
    "s_mov_b32 s46, 0xa2000\n"
    "s_mov_b32 s47, 0x20000\n"
    "s_add_u32 m0, s54, 0\n"
    "s_add_u32 s55, %6, 0\n"
    "buffer_load_dwordx4 v8, s[44:47], s55 offen lds\n"
    "s_add_u32 m0, s54, 36864\n"
    "s_add_u32 s55, %6, 64\n"
    "buffer_load_dwordx4 v8, s[44:47], s55 offen lds\n"
    "s_add_u32 m0, s54, 73728\n"
    "s_add_u32 s55, %6, 128\n"
    "buffer_load_dwordx4 v8, s[44:47], s55 offen lds\n"
    "s_add_u32 m0, s54, 4096\n"
    "s_add_u32 s55, %6, 73728\n"
    "buffer_load_dwordx4 v8, s[44:47], s55 offen lds\n"
    "s_add_u32 m0, s54, 40960\n"
    "s_add_u32 s55, %6, 73792\n"
    "buffer_load_dwordx4 v8, s[44:47], s55 offen lds\n"
    "s_add_u32 m0, s54, 77824\n"
    "s_add_u32 s55, %6, 73856\n"
    "buffer_load_dwordx4 v8, s[44:47], s55 offen lds\n"
    "s_add_u32 m0, s54, 8192\n"
    "s_add_u32 s55, %6, 147456\n"
    "buffer_load_dwordx4 v8, s[44:47], s55 offen lds\n"
    "s_add_u32 m0, s54, 45056\n"
    "s_add_u32 s55, %6, 147520\n"
    "buffer_load_dwordx4 v8, s[44:47], s55 offen lds\n"
    "s_add_u32 m0, s54, 81920\n"
    "s_add_u32 s55, %6, 147584\n"
    "buffer_load_dwordx4 v8, s[44:47], s55 offen lds\n"
    "s_add_u32 m0, s54, 12288\n"
    "s_add_u32 s55, %6, 221184\n"
    "buffer_load_dwordx4 v8, s[44:47], s55 offen lds\n"
    "s_add_u32 m0, s54, 49152\n"
    "s_add_u32 s55, %6, 221248\n"
    "buffer_load_dwordx4 v8, s[44:47], s55 offen lds\n"
    "s_add_u32 m0, s54, 86016\n"
    "s_add_u32 s55, %6, 221312\n"
    "buffer_load_dwordx4 v8, s[44:47], s55 offen lds\n"
    "s_add_u32 m0, s54, 16384\n"
    "s_add_u32 s55, %6, 294912\n"
    "buffer_load_dwordx4 v8, s[44:47], s55 offen lds\n"
    "s_add_u32 m0, s54, 53248\n"
    "s_add_u32 s55, %6, 294976\n"
    "buffer_load_dwordx4 v8, s[44:47], s55 offen lds\n"
    "s_add_u32 m0, s54, 90112\n"
    "s_add_u32 s55, %6, 295040\n"
    "buffer_load_dwordx4 v8, s[44:47], s55 offen lds\n"
    "s_add_u32 m0, s54, 20480\n"
    "s_add_u32 s55, %6, 368640\n"
    "buffer_load_dwordx4 v8, s[44:47], s55 offen lds\n"
    "s_add_u32 m0, s54, 57344\n"
    "s_add_u32 s55, %6, 368704\n"
    "buffer_load_dwordx4 v8, s[44:47], s55 offen lds\n"
    "s_add_u32 m0, s54, 94208\n"
    "s_add_u32 s55, %6, 368768\n"
    "buffer_load_dwordx4 v8, s[44:47], s55 offen lds\n"
    "s_add_u32 m0, s54, 24576\n"
    "s_add_u32 s55, %6, 442368\n"
    "buffer_load_dwordx4 v8, s[44:47], s55 offen lds\n"
    "s_add_u32 m0, s54, 61440\n"
    "s_add_u32 s55, %6, 442432\n"
    "buffer_load_dwordx4 v8, s[44:47], s55 offen lds\n"
    "s_add_u32 m0, s54, 98304\n"
    "s_add_u32 s55, %6, 442496\n"
    "buffer_load_dwordx4 v8, s[44:47], s55 offen lds\n"
    "s_add_u32 m0, s54, 28672\n"
    "s_add_u32 s55, %6, 516096\n"
    "buffer_load_dwordx4 v8, s[44:47], s55 offen lds\n"
    "s_add_u32 m0, s54, 65536\n"
    "s_add_u32 s55, %6, 516160\n"
    "buffer_load_dwordx4 v8, s[44:47], s55 offen lds\n"
    "s_add_u32 m0, s54, 102400\n"
    "s_add_u32 s55, %6, 516224\n"
    "buffer_load_dwordx4 v8, s[44:47], s55 offen lds\n"
    "s_add_u32 m0, s54, 32768\n"
    "s_add_u32 s55, %6, 589824\n"
    "buffer_load_dwordx4 v8, s[44:47], s55 offen lds\n"
    "s_add_u32 m0, s54, 69632\n"
    "s_add_u32 s55, %6, 589888\n"
    "buffer_load_dwordx4 v8, s[44:47], s55 offen lds\n"
    "s_add_u32 m0, s54, 106496\n"
    "s_add_u32 s55, %6, 589952\n"
    "buffer_load_dwordx4 v8, s[44:47], s55 offen lds\n"
    "v_mov_b32 v7, 0x20400\n"
    "global_load_dwordx2 v[20:21], v7, %1 sc1\n"
    "v_mbcnt_lo_u32_b32 v6, -1, 0\n"
    "v_min_u32 v6, 7, v6\n"
    "v_lshlrev_b32 v6, 7, v6\n"
    "v_add_u32 v6, 0x20000, v6\n"
    "global_load_dwordx2 v[22:23], v6, %1 sc1\n"
    "s_waitcnt vmcnt(2) lgkmcnt(0)\n"
    "s_barrier\n"
    "ds_read_b64_tr_b16 a[0:1], v9\n"
    "ds_read_b64_tr_b16 a[2:3], v9 offset:256\n"
    "ds_read_b64_tr_b16 a[4:5], v9 offset:8\n"
    "ds_read_b64_tr_b16 a[6:7], v9 offset:264\n"
    "ds_read_b64_tr_b16 a[8:9], v9 offset:2304\n"
    "ds_read_b64_tr_b16 a[10:11], v9 offset:2560\n"
    "ds_read_b64_tr_b16 a[12:13], v9 offset:2312\n"
    "ds_read_b64_tr_b16 a[14:15], v9 offset:2568\n"
    "ds_read_b64_tr_b16 a[16:17], v9 offset:4608\n"
    "ds_read_b64_tr_b16 a[18:19], v9 offset:4864\n"
    "ds_read_b64_tr_b16 a[20:21], v9 offset:4616\n"
    "ds_read_b64_tr_b16 a[22:23], v9 offset:4872\n"
    "ds_read_b64_tr_b16 a[24:25], v9 offset:6912\n"
    "ds_read_b64_tr_b16 a[26:27], v9 offset:7168\n"
    "ds_read_b64_tr_b16 a[28:29], v9 offset:6920\n"
    "ds_read_b64_tr_b16 a[30:31], v9 offset:7176\n"
    "ds_read_b64_tr_b16 a[32:33], v9 offset:9216\n"
    "ds_read_b64_tr_b16 a[34:35], v9 offset:9472\n"
    "ds_read_b64_tr_b16 a[36:37], v9 offset:9224\n"
    "ds_read_b64_tr_b16 a[38:39], v9 offset:9480\n"
    "ds_read_b64_tr_b16 a[40:41], v9 offset:11520\n"
    "ds_read_b64_tr_b16 a[42:43], v9 offset:11776\n"
    "ds_read_b64_tr_b16 a[44:45], v9 offset:11528\n"
    "ds_read_b64_tr_b16 a[46:47], v9 offset:11784\n"
    "ds_read_b64_tr_b16 a[48:49], v9 offset:13824\n"
    "ds_read_b64_tr_b16 a[50:51], v9 offset:14080\n"
    "ds_read_b64_tr_b16 a[52:53], v9 offset:13832\n"
    "ds_read_b64_tr_b16 a[54:55], v9 offset:14088\n"
    "ds_read_b64_tr_b16 a[56:57], v9 offset:16128\n"
    "ds_read_b64_tr_b16 a[58:59], v9 offset:16384\n"
    "ds_read_b64_tr_b16 a[60:61], v9 offset:16136\n"
    "ds_read_b64_tr_b16 a[62:63], v9 offset:16392\n"
    "ds_read_b64_tr_b16 a[64:65], v9 offset:18432\n"
    "ds_read_b64_tr_b16 a[66:67], v9 offset:18688\n"
    "ds_read_b64_tr_b16 a[68:69], v9 offset:18440\n"
    "ds_read_b64_tr_b16 a[70:71], v9 offset:18696\n"
    "ds_read_b64_tr_b16 a[72:73], v9 offset:20736\n"
    "ds_read_b64_tr_b16 a[74:75], v9 offset:20992\n"
    "ds_read_b64_tr_b16 a[76:77], v9 offset:20744\n"
    "ds_read_b64_tr_b16 a[78:79], v9 offset:21000\n"
    "ds_read_b64_tr_b16 a[80:81], v9 offset:23040\n"
    "ds_read_b64_tr_b16 a[82:83], v9 offset:23296\n"
    "ds_read_b64_tr_b16 a[84:85], v9 offset:23048\n"
    "ds_read_b64_tr_b16 a[86:87], v9 offset:23304\n"
    "ds_read_b64_tr_b16 a[88:89], v9 offset:25344\n"
    "ds_read_b64_tr_b16 a[90:91], v9 offset:25600\n"
    "ds_read_b64_tr_b16 a[92:93], v9 offset:25352\n"
    "ds_read_b64_tr_b16 a[94:95], v9 offset:25608\n"
    "ds_read_b64_tr_b16 a[96:97], v9 offset:27648\n"
    "ds_read_b64_tr_b16 a[98:99], v9 offset:27904\n"
    "ds_read_b64_tr_b16 a[100:101], v9 offset:27656\n"
    "ds_read_b64_tr_b16 a[102:103], v9 offset:27912\n"
    "ds_read_b64_tr_b16 a[104:105], v9 offset:29952\n"
    "ds_read_b64_tr_b16 a[106:107], v9 offset:30208\n"
    "ds_read_b64_tr_b16 a[108:109], v9 offset:29960\n"
    "ds_read_b64_tr_b16 a[110:111], v9 offset:30216\n"
    "ds_read_b64_tr_b16 a[112:113], v9 offset:32256\n"
    "ds_read_b64_tr_b16 a[114:115], v9 offset:32512\n"
    "ds_read_b64_tr_b16 a[116:117], v9 offset:32264\n"
    "ds_read_b64_tr_b16 a[118:119], v9 offset:32520\n"
    "ds_read_b64_tr_b16 a[120:121], v9 offset:34560\n"
    "ds_read_b64_tr_b16 a[122:123], v9 offset:34816\n"
    "ds_read_b64_tr_b16 a[124:125], v9 offset:34568\n"
    "ds_read_b64_tr_b16 a[126:127], v9 offset:34824\n"
    "ds_read_b64_tr_b16 a[128:129], v10\n"
    "ds_read_b64_tr_b16 a[130:131], v10 offset:2304\n"
    "ds_read_b64_tr_b16 a[132:133], v10 offset:8\n"
    "ds_read_b64_tr_b16 a[134:135], v10 offset:2312\n"
    "ds_read_b64_tr_b16 a[136:137], v10 offset:18432\n"
    "ds_read_b64_tr_b16 a[138:139], v10 offset:20736\n"
    "ds_read_b64_tr_b16 a[140:141], v10 offset:18440\n"
    "ds_read_b64_tr_b16 a[142:143], v10 offset:20744\n"
    "s_waitcnt lgkmcnt(0)\n"
    "s_barrier\n"
    "s_mov_b64 s[44:45], %1\n"
    "s_and_b32 s45, s45, 0xffff\n"
    "s_mov_b32 s46, 0x0\n"
    "s_mov_b32 s47, 0x20000\n"
    "s_waitcnt vmcnt(0)\n"
    "v_add_u32 v20, 1, v20\n"
    "v_add_u32 v21, 1, v21\n"
    "s_mov_b32 s52, 1048576\n"
    ".Lbgo_a%=:\n"
    "v_cmp_ne_u64 vcc, v[20:21], v[22:23]\n"
    "s_cbranch_vccz .Lbgo_x%=\n"
    "global_load_dwordx2 v[22:23], v6, %1 sc1\n"
    "s_sub_u32 s52, s52, 1\n"
    "s_waitcnt vmcnt(0)\n"
    "s_cbranch_scc0 .Lbgo_a%=\n"
    ".Lbgo_x%=:\n"
    "v_mbcnt_lo_u32_b32 v20, -1, 0\n"
    "v_mbcnt_hi_u32_b32 v20, -1, v20\n"
    "v_min_u32 v6, 7, v20\n"
    "v_lshlrev_b32 v6, 7, v6\n"
    "v_mov_b32 v7, 0x26c00\n"
    "ds_read_b128 v[20:23], v7\n"
    "s_waitcnt lgkmcnt(0)\n"
    "v_readfirstlane_b32 s49, v21\n"
    "v_add_u32 v20, v20, v6\n"
    "v_subrev_u32 v7, 32, v7\n"
    "s_add_u32 s49, s49, 1966080\n"
    "s_branch .Lbld_%=\n"
    ".Lbgo_%=:\n"
    "v_add_u32 v7, 32, v7\n"
    "ds_read_b128 v[20:23], v7\n"
    "v_xor_b32 v0, 0x8000, v0\n"
    "v_xor_b32 v4, 0x8000, v4\n"
    "v_add_u32 v104, 8192, v4\n"
    "s_waitcnt lgkmcnt(0)\n"
    "v_add_u32 v20, v20, v6\n"
    "v_readfirstlane_b32 s62, v22\n"
    "v_readfirstlane_b32 s49, v21\n"
    "s_xor_b32 s58, s58, 0x8000\n"
    "global_load_dword v105, v20, %1 sc1\n"
    "s_waitcnt vmcnt(3)\n"
    ".Lbtop_%=:\n"
    "s_barrier\n"
    "ds_read_b128 v[24:27], v0\n"
    "ds_read_b128 v[28:31], v0 offset:1024\n"
    "ds_read_b128 v[32:35], v0 offset:2048\n"
    "ds_read_b128 v[36:39], v0 offset:3072\n"
    "ds_read_b128 v[40:43], v0 offset:8192\n"
    "ds_read_b128 v[44:47], v0 offset:9216\n"
    "v_lshlrev_b32 v8, 16, v100\n"
    "v_and_b32 v9, 0xffff0000, v100\n"
    "v_lshlrev_b32 v10, 16, v101\n"
    "v_and_b32 v11, 0xffff0000, v101\n"
    "v_lshlrev_b32 v12, 16, v102\n"
    "v_and_b32 v13, 0xffff0000, v102\n"
    "v_lshlrev_b32 v14, 16, v103\n"
    "v_and_b32 v15, 0xffff0000, v103\n"
    "s_waitcnt lgkmcnt(5)\n"
    "v_mfma_f32_16x16x32_bf16 v[8:11], a[0:3], v[24:27], v[8:11]\n"
    "ds_read_b128 v[48:51], v0 offset:10240\n"
    "v_mfma_f32_16x16x32_bf16 v[12:15], a[4:7], v[24:27], v[12:15]\n"
    "ds_read_b128 v[52:55], v0 offset:11264\n"
    "s_waitcnt lgkmcnt(6)\n"
    "v_mfma_f32_16x16x32_bf16 v[8:11], a[8:11], v[28:31], v[8:11]\n"
    "ds_read_b128 v[56:59], v0 offset:16384\n"
    "v_mfma_f32_16x16x32_bf16 v[12:15], a[12:15], v[28:31], v[12:15]\n"
    "ds_read_b128 v[60:63], v0 offset:17408\n"
    "s_waitcnt lgkmcnt(7)\n"
    "v_mfma_f32_16x16x32_bf16 v[8:11], a[16:19], v[32:35], v[8:11]\n"
    "ds_read_b128 v[64:67], v0 offset:18432\n"
    "v_mfma_f32_16x16x32_bf16 v[12:15], a[20:23], v[32:35], v[12:15]\n"
    "ds_read_b128 v[68:71], v0 offset:19456\n"
    "s_waitcnt lgkmcnt(8)\n"
    "v_mfma_f32_16x16x32_bf16 v[8:11], a[24:27], v[36:39], v[8:11]\n"
    "ds_read_b128 v[72:75], v0 offset:24576\n"
    "v_mfma_f32_16x16x32_bf16 v[12:15], a[28:31], v[36:39], v[12:15]\n"
    "ds_read_b128 v[76:79], v0 offset:25600\n"
    "s_waitcnt lgkmcnt(9)\n"
    "v_mfma_f32_16x16x32_bf16 v[8:11], a[32:35], v[40:43], v[8:11]\n"
    "ds_read_b128 v[80:83], v0 offset:26624\n"
    "v_mfma_f32_16x16x32_bf16 v[12:15], a[36:39], v[40:43], v[12:15]\n"
    "ds_read_b128 v[84:87], v0 offset:27648\n"
    "s_waitcnt lgkmcnt(10)\n"
    "v_mfma_f32_16x16x32_bf16 v[8:11], a[40:43], v[44:47], v[8:11]\n"
    "ds_read2_b32 v[88:89], v4 offset1:16\n"
    "v_mov_b32 v22, 1\n"
    "v_mfma_f32_16x16x32_bf16 v[12:15], a[44:47], v[44:47], v[12:15]\n"
    "ds_read2_b32 v[90:91], v4 offset0:32 offset1:48\n"
    "s_waitcnt vmcnt(1)\n"
    "s_waitcnt lgkmcnt(11)\n"
    "v_mfma_f32_16x16x32_bf16 v[8:11], a[48:51], v[48:51], v[8:11]\n"
    "s_mov_b64 exec, s[56:57]\n"
    "ds_add_rtn_u32 v22, v7, v22 offset:16\n"
    "s_mov_b64 exec, -1\n"
    "v_mfma_f32_16x16x32_bf16 v[12:15], a[52:55], v[48:51], v[12:15]\n"
    "s_waitcnt vmcnt(0)\n"
    "v_cmp_eq_u32 vcc, 0, v105\n"
    "s_waitcnt lgkmcnt(10)\n"
    "v_mfma_f32_16x16x32_bf16 v[8:11], a[56:59], v[52:55], v[8:11]\n"
    "s_cmp_eq_u64 vcc, 0\n"
    "s_cselect_b32 s46, 0, 0x20000\n"
    "s_cselect_b32 s94, s42, 0\n"
    "v_mfma_f32_16x16x32_bf16 v[12:15], a[60:63], v[52:55], v[12:15]\n"
    "buffer_load_dword v105, v20, s[44:47], 0 offen sc1\n"
    "s_cselect_b32 s90, s96, 0\n"
    "s_waitcnt lgkmcnt(9)\n"
    "v_mfma_f32_16x16x32_bf16 v[8:11], a[64:67], v[56:59], v[8:11]\n"
    "ds_read2_b32 v[92:93], v104 offset1:16\n"
    "v_mfma_f32_16x16x32_bf16 v[12:15], a[68:71], v[56:59], v[12:15]\n"
    "s_waitcnt lgkmcnt(1)\n"
    "v_readfirstlane_b32 s50, v22\n"
    "v_mfma_f32_16x16x32_bf16 v[8:11], a[72:75], v[60:63], v[8:11]\n"
    "s_cmp_eq_u32 s50, 3\n"
    "s_cselect_b32 s46, 0x20000, 0\n"
    "v_mfma_f32_16x16x32_bf16 v[12:15], a[76:79], v[60:63], v[12:15]\n"
    "buffer_store_dword v22, v23, s[44:47], 0 offen sc1\n"
    "s_mov_b32 m0, s58\n"
    "v_mfma_f32_16x16x32_bf16 v[8:11], a[80:83], v[64:67], v[8:11]\n"
    "buffer_load_dwordx4 v2, s[92:95], s49 offen sc1 lds\n"
    "v_mfma_f32_16x16x32_bf16 v[12:15], a[84:87], v[64:67], v[12:15]\n"
    "ds_read2_b32 v[94:95], v104 offset0:32 offset1:48\n"
    "v_mfma_f32_16x16x32_bf16 v[8:11], a[88:91], v[68:71], v[8:11]\n"
    "v_mfma_f32_16x16x32_bf16 v[12:15], a[92:95], v[68:71], v[12:15]\n"
    "buffer_load_dwordx4 v2, s[92:95], s49 offen offset:1024 sc1 lds\n"
    "v_mfma_f32_16x16x32_bf16 v[8:11], a[96:99], v[72:75], v[8:11]\n"
    "v_mfma_f32_16x16x32_bf16 v[12:15], a[100:103], v[72:75], v[12:15]\n"
    "v_mfma_f32_16x16x32_bf16 v[8:11], a[104:107], v[76:79], v[8:11]\n"
    "buffer_load_dwordx4 v2, s[92:95], s49 offen offset:2048 sc1 lds\n"
    "v_mfma_f32_16x16x32_bf16 v[12:15], a[108:111], v[76:79], v[12:15]\n"
    "v_mfma_f32_16x16x32_bf16 v[8:11], a[112:115], v[80:83], v[8:11]\n"
    "v_mfma_f32_16x16x32_bf16 v[12:15], a[116:119], v[80:83], v[12:15]\n"
    "buffer_load_dwordx4 v2, s[92:95], s49 offen offset:3072 sc1 lds\n"
    "v_mfma_f32_16x16x32_bf16 v[8:11], a[120:123], v[84:87], v[8:11]\n"
    "v_mfma_f32_16x16x32_bf16 v[12:15], a[124:127], v[84:87], v[12:15]\n"
    "s_bitset1_b32 m0, 12\n"
    "v_mfma_f32_16x16x32_bf16 v[8:11], a[128:131], v[88:91], v[8:11]\n"
    "buffer_load_dwordx4 v1, s[88:91], s49 offen sc1 lds\n"
    "v_mfma_f32_16x16x32_bf16 v[12:15], a[132:135], v[88:91], v[12:15]\n"
    "s_waitcnt lgkmcnt(0)\n"
    "v_mfma_f32_16x16x32_bf16 v[8:11], a[136:139], v[92:95], v[8:11]\n"
    "s_sub_u32 s55, s49, 0x1000000\n"
    "v_mfma_f32_16x16x32_bf16 v[12:15], a[140:143], v[92:95], v[12:15]\n"
    "buffer_load_dwordx4 v[100:103], v3, s[92:95], s55 offen sc1\n"
    "s_nop 3\n"
    "v_cvt_pk_bf16_f32 v16, v8, v9\n"
    "v_cvt_pk_bf16_f32 v17, v10, v11\n"
    "s_nop 1\n"
    "v_cvt_pk_bf16_f32 v18, v12, v13\n"
    "v_cvt_pk_bf16_f32 v19, v14, v15\n"
    "buffer_store_dwordx4 v[16:19], v3, s[40:43], s62 offen sc1\n"
    "v_dot2_f32_bf16 v8, v16, s86, 0\n"
    "v_dot2_f32_bf16 v9, v16, v16, 0\n"
    "v_dot2_f32_bf16 v8, v17, s86, v8\n"
    "v_dot2_f32_bf16 v9, v17, v17, v9\n"
    "v_dot2_f32_bf16 v8, v18, s86, v8\n"
    "v_dot2_f32_bf16 v9, v18, v18, v9\n"
    "v_dot2_f32_bf16 v8, v19, s86, v8\n"
    "v_dot2_f32_bf16 v9, v19, v19, v9\n"
    "s_nop 2\n"
    "v_permlane32_swap_b32 v8, v9\n"
    "v_add_f32 v10, v8, v9\n"
    "v_add_f32 v8, v8, v9\n"
    "s_nop 1\n"
    "v_permlane16_swap_b32 v10, v8\n"
    "v_add_f32 v8, v8, v10\n"
    "buffer_store_dword v8, v5, s[40:43], s62 offen sc1\n"
    "s_mov_b32 s56, 1\n"
    "s_cmp_lg_u32 s94, 0\n"
    "s_cbranch_scc1 .Lbgo_%=\n"
    "ds_read_b32 v23, v7 offset:44\n"
    "s_waitcnt vmcnt(8) lgkmcnt(0)\n"
    "v_cmp_eq_u32 vcc, 0, v105\n"
    "s_cmp_eq_u64 vcc, 0\n"
    "s_cbranch_scc1 .Lbld_%=\n"
    "global_load_dword v105, v20, %1 sc1\n"
    "s_waitcnt vmcnt(0)\n"
    "v_mov_b32 v22, 1\n"
    "s_mov_b64 exec, s[56:57]\n"
    "ds_add_rtn_u32 v22, v7, v22 offset:48\n"
    "s_mov_b64 exec, -1\n"
    "s_waitcnt lgkmcnt(0)\n"
    "v_readfirstlane_b32 s50, v22\n"
    "s_cmp_eq_u32 s50, 3\n"
    "s_cselect_b32 s46, 0x20000, 0\n"
    "buffer_store_dword v22, v23, s[44:47], 0 offen sc1\n"
    "s_mov_b32 s56, 0\n"
    "v_cmp_eq_u32 vcc, 0, v105\n"
    "s_cmp_eq_u64 vcc, 0\n"
    "s_cbranch_scc1 .Lbld_%=\n"
    ".Lbchk_%=:\n"
    "v_readfirstlane_b32 s50, v20\n"
    "s_cmp_lt_u32 s50, 200\n"
    "s_cbranch_scc1 .Lbend_%=\n"
    ".Lbwait_%=:\n"
    "global_load_dword v105, v20, %1 sc1\n"
    "s_mov_b32 s52, 0\n"
    ".Lbp1_%=:\n"
    "s_waitcnt vmcnt(0)\n"
    "v_cmp_eq_u32 vcc, 0, v105\n"
    "s_cmp_eq_u64 vcc, 0\n"
    "s_cbranch_scc1 .Lbp1_x%=\n"
    "s_add_u32 s52, s52, 1\n"
    "s_sleep 2\n"
    "global_load_dword v105, v20, %1 sc1\n"
    "s_cmp_lt_u32 s52, s53\n"
    "s_cbranch_scc1 .Lbp1_%=\n"
    "s_mov_b32 s53, 0\n"
    "v_mov_b32 v22, 0\n"
    "v_mov_b32 v23, 1\n"
    "global_store_dword v22, v23, %1 sc1\n"
    "s_waitcnt vmcnt(0)\n"
    ".Lbp1_x%=:\n"
    ".Lbld_%=:\n"
    "s_mov_b32 s94, s42\n"
    "s_mov_b32 s90, s96\n"
    "s_mov_b32 m0, s58\n"
    "s_nop 0\n"
    "buffer_load_dwordx4 v2, s[92:95], s49 offen sc1 lds\n"
    "buffer_load_dwordx4 v2, s[92:95], s49 offen offset:1024 sc1 lds\n"
    "buffer_load_dwordx4 v2, s[92:95], s49 offen offset:2048 sc1 lds\n"
    "buffer_load_dwordx4 v2, s[92:95], s49 offen offset:3072 sc1 lds\n"
    "s_bitset1_b32 m0, 12\n"
    "s_nop 0\n"
    "buffer_load_dwordx4 v1, s[88:91], s49 offen sc1 lds\n"
    "s_sub_u32 s55, s49, 0x1000000\n"
    "buffer_load_dwordx4 v[100:103], v3, s[92:95], s55 offen sc1\n"
    "s_waitcnt vmcnt(6)\n"
    "v_mov_b32 v22, 1\n"
    "s_mov_b64 exec, s[56:57]\n"
    "ds_add_rtn_u32 v22, v7, v22 offset:48\n"
    "s_mov_b64 exec, -1\n"
    "s_waitcnt lgkmcnt(0)\n"
    "v_readfirstlane_b32 s50, v22\n"
    "s_cmp_eq_u32 s50, 3\n"
    "s_cselect_b32 s46, 0x20000, 0\n"
    "buffer_store_dword v22, v23, s[44:47], 0 offen sc1\n"
    "s_mov_b32 s56, 0\n"
    "v_add_u32 v7, 32, v7\n"
    "ds_read_b128 v[20:23], v7\n"
    "v_xor_b32 v0, 0x8000, v0\n"
    "v_xor_b32 v4, 0x8000, v4\n"
    "v_add_u32 v104, 8192, v4\n"
    "s_waitcnt lgkmcnt(0)\n"
    "v_add_u32 v20, v20, v6\n"
    "v_readfirstlane_b32 s62, v22\n"
    "v_readfirstlane_b32 s49, v21\n"
    "s_xor_b32 s58, s58, 0x8000\n"
    "s_waitcnt vmcnt(1)\n"
    "global_load_dword v105, v20, %1 sc1\n"
    "s_branch .Lbtop_%=\n"
    ".Lbpol_%=:\n"
    "v_mov_b32 v20, 64\n"
    "global_load_dword v105, v20, %1 sc1\n"
    "s_mov_b32 s52, 0\n"
    ".Lbp2_%=:\n"
    "s_waitcnt vmcnt(0)\n"
    "v_cmp_eq_u32 vcc, 0, v105\n"
    "s_cmp_eq_u64 vcc, 0\n"
    "s_cbranch_scc1 .Lbp2_x%=\n"
    "s_add_u32 s52, s52, 1\n"
    "s_sleep 2\n"
    "global_load_dword v105, v20, %1 sc1\n"
    "s_cmp_lt_u32 s52, s53\n"
    "s_cbranch_scc1 .Lbp2_%=\n"
    "s_mov_b32 s53, 0\n"
    "v_mov_b32 v22, 0\n"
    "v_mov_b32 v23, 1\n"
    "global_store_dword v22, v23, %1 sc1\n"
    "s_waitcnt vmcnt(0)\n"
    ".Lbp2_x%=:\n"
    "v_readfirstlane_b32 s50, v105\n"
    "v_mbcnt_lo_u32_b32 v20, -1, 0\n"
    "v_mbcnt_hi_u32_b32 v20, -1, v20\n"
    "s_lshl_b32 s51, %2, 6\n"
    "v_add_u32 v20, s51, v20\n"
    "v_min_u32 v20, 129, v20\n"
    "v_lshlrev_b32 v20, 5, v20\n"
    "v_add_u32 v20, 0x26c00, v20\n"
    "s_and_b32 s50, s50, 1\n"
    "ds_read_b64 v[22:23], v20\n"
    "s_waitcnt lgkmcnt(0)\n"
    "v_and_b32 v21, s50, v22\n"
    "v_lshlrev_b32 v21, 13, v21\n"
    "v_and_b32 v22, -2, v22\n"
    "v_xor_b32 v22, v21, v22\n"
    "v_and_b32 v21, s50, v23\n"
    "v_lshlrev_b32 v21, 13, v21\n"
    "v_and_b32 v23, -2, v23\n"
    "v_xor_b32 v23, v21, v23\n"
    "ds_write_b64 v20, v[22:23]\n"
    "ds_read_b64 v[22:23], v20 offset:8\n"
    "s_waitcnt lgkmcnt(0)\n"
    "v_and_b32 v21, s50, v22\n"
    "v_lshlrev_b32 v21, 13, v21\n"
    "v_and_b32 v22, -2, v22\n"
    "v_xor_b32 v22, v21, v22\n"
    "v_and_b32 v21, s50, v23\n"
    "v_lshlrev_b32 v21, 13, v21\n"
    "v_and_b32 v23, -2, v23\n"
    "v_xor_b32 v23, v21, v23\n"
    "ds_write_b64 v20, v[22:23] offset:8\n"
    "s_waitcnt lgkmcnt(0)\n"
    "s_barrier\n"
    "s_and_b32 s50, s50, %4\n"
    "s_lshl_b32 s50, s50, 13\n"
    "s_and_b32 s51, %4, -2\n"
    "s_xor_b32 s50, s50, s51\n"
    "ds_read_b32 v21, v7 offset:4\n"
    "v_add_u32 v20, s50, v6\n"
    "s_waitcnt lgkmcnt(0)\n"
    "v_readfirstlane_b32 s49, v21\n"
    "s_branch .Lbwait_%=\n"
    ".Lbend_%=:\n"
    "s_cmp_eq_u32 s50, 192\n"
    "s_cbranch_scc0 .Lbpol_%=\n"
    "s_waitcnt vmcnt(0) lgkmcnt(0)\n"
    ".Lrole5_%=:\n"
    ".Lfin:\n"
        :: "s"(wd), "s"(ws), "s"(wave), "s"(rid), "s"(P.h), "s"(P.p), "s"(P.woff), "s"(layer), "s"(w0), "s"(Wk), "s"(Wv), "s"(Wo),
           "s"(mem0), "s"(P.role) : CLOBBERS);
}

extern "C" void kernel_launch(void* const* d_in, const int* in_sizes, int n_in, void* d_out, int out_size, void* d_ws,
                              size_t ws_size, hipStream_t stream) {
    mega_kernel<<<256, 256, 0, stream>>>((const u16*)d_in[0], (const u16*)d_in[1], (const u16*)d_in[2], (const u16*)d_in[4],
                                         (const u16*)d_in[5], (const u16*)d_in[6], (const u16*)d_in[7], (const u16*)d_in[8],
                                         (u16*)d_out, (char*)d_ws);
}
```
